```python
import torch

def _softmax(x, axis=-1):
    return torch.softmax(x, dim=axis)
def _gelu(x):
    return torch.nn.functional.gelu(x, approximate="tanh")
def _silu(x):
    return torch.nn.functional.silu(x)
def _sigmoid(x):
    return torch.sigmoid(x)
def _layernorm(x, axis=-1, eps=1e-5):
    m = x.mean(dim=axis, keepdim=True)
    v = x.var(dim=axis, keepdim=True, unbiased=False)
    return (x - m) / torch.sqrt(v + eps)
def _scatter_add(tgt, idx, src, axis=0):
    return tgt.clone().index_add_(axis, idx, src)
def _segment_sum(src, seg, n):
    out = torch.zeros((n,) + src.shape[1:], dtype=src.dtype)
    return out.index_add_(0, seg, src)
def _pad(x, pw):
    flat = tuple(v for pair in reversed(pw) for v in pair)
    return torch.nn.functional.pad(x, flat)

kernel_name = 'standalone__gqa_softmax__near_ridge'

def setup_inputs():

    _w = (lambda a: (lambda t: t.to(torch.bfloat16) if t.is_floating_point() else t)(torch.as_tensor(a)))
    B, Hq, Hkv, Sq, Sk, D = 16, 32, 32, 848, 512, 192
    return {
        "q": _w((lambda *s: torch.randn(*s, dtype=torch.bfloat16) * 0.8)(B, Hq, Sq, D)),
        "k": _w((lambda *s: torch.randn(*s, dtype=torch.bfloat16) * 0.8)(B, Hkv, Sk, D)),
        "v": _w((lambda *s: torch.randn(*s, dtype=torch.bfloat16) * 0.05)(B, Hkv, Sk, D)),
    }

def reference(q, k, v):
    B, Hq, Sq, D = q.shape
    Hkv = k.shape[1]
    rep = Hq // Hkv
    k = (lambda x, n, axis: torch.repeat_interleave(x, n, dim=axis))(k, rep, axis=1)
    v = (lambda x, n, axis: torch.repeat_interleave(x, n, dim=axis))(v, rep, axis=1)
    scale = D ** -0.5
    scores = torch.einsum("bhqd,bhkd->bhqk", q, k) * scale
    p = _softmax(scores, axis=-1)
    return torch.einsum("bhqk,bhkd->bhqd", p, v)
```

```cpp
#include <hip/hip_runtime.h>
#include <hip/hip_bf16.h>

typedef __bf16   bf16x8 __attribute__((ext_vector_type(8)));
typedef __bf16   bf16x2 __attribute__((ext_vector_type(2)));
typedef float    f32x4  __attribute__((ext_vector_type(4)));
typedef float    f32x2  __attribute__((ext_vector_type(2)));
typedef unsigned u32x4  __attribute__((ext_vector_type(4)));
typedef unsigned u32x2  __attribute__((ext_vector_type(2)));
typedef unsigned short u16x8 __attribute__((ext_vector_type(8)));

constexpr int SQ = 848, SK = 512, DH = 192, ROWB = 384;
constexpr int LDS_BYTES = 163840;
constexpr float CSC = 0.07216878364870322f * 1.4426950408889634f;

#define DI __device__ __forceinline__
DI unsigned cvtpk(float lo, float hi) { f32x2 v = {lo, hi}; bf16x2 b = __builtin_convertvector(v, bf16x2); return __builtin_bit_cast(unsigned, b); }
DI float xmax(float x) {
  auto r = __builtin_amdgcn_permlane32_swap(__float_as_uint(x), __float_as_uint(x), false, false);
  x = fmaxf(__uint_as_float(r[0]), __uint_as_float(r[1]));
  auto q = __builtin_amdgcn_permlane16_swap(__float_as_uint(x), __float_as_uint(x), false, false);
  return fmaxf(__uint_as_float(q[0]), __uint_as_float(q[1]));
}
DI float xsum(float x) {
  auto r = __builtin_amdgcn_permlane32_swap(__float_as_uint(x), __float_as_uint(x), false, false);
  x = __uint_as_float(r[0]) + __uint_as_float(r[1]);
  auto q = __builtin_amdgcn_permlane16_swap(__float_as_uint(x), __float_as_uint(x), false, false);
  return __uint_as_float(q[0]) + __uint_as_float(q[1]);
}
DI f32x4 score_tile(const unsigned short* krow, const bf16x8 (&qf)[6]) {
  f32x4 acc = {0.f, 0.f, 0.f, 0.f};
#pragma unroll
  for (int s = 0; s < 6; ++s) acc = __builtin_amdgcn_mfma_f32_16x16x32_bf16(*(const bf16x8*)(krow + 32 * s), qf[s], acc, 0, 0, 0);
  return acc;
}
DI void exact_tile(const unsigned short* Q, const unsigned short* K, const unsigned short* V, unsigned short* O, int head, int row0) {
  const unsigned lane = __builtin_amdgcn_mbcnt_hi(~0u, __builtin_amdgcn_mbcnt_lo(~0u, 0u));
  const int c = lane & 15, g4 = lane >> 4;
  const int qrow = min(row0 + c, SQ - 1);
  bf16x8 qf[6];
#pragma unroll
  for (int s = 0; s < 6; ++s) qf[s] = *(const bf16x8*)(Q + ((size_t)head * SQ + qrow) * DH + 32 * s + 8 * g4);
  const unsigned short* Kh = K + ((size_t)head * SK + c) * DH + 8 * g4;
  float m = -__builtin_huge_valf();
#pragma unroll 1
  for (int kb = 0; kb < SK / 16; ++kb) {
    const f32x4 a = score_tile(Kh + (size_t)kb * 16 * DH, qf);
    m = fmaxf(fmaxf(m, fmaxf(a[0], a[1])), fmaxf(a[2], a[3]));
  }
  const float mc = -xmax(m) * CSC;
  float l = 0.f;
  f32x4 o[12];
#pragma unroll
  for (int dt = 0; dt < 12; ++dt) o[dt] = f32x4{0.f, 0.f, 0.f, 0.f};
#pragma unroll 1
  for (int j = 0; j < SK / 32; ++j) {
    u32x4 p;
#pragma unroll
    for (int kt = 0; kt < 2; ++kt) {
      f32x4 a = score_tile(Kh + (size_t)(32 * j + 16 * kt) * DH, qf);
#pragma unroll
      for (int r = 0; r < 4; ++r) { a[r] = __builtin_amdgcn_exp2f(fmaf(a[r], CSC, mc)); l += a[r]; }
      p[2 * kt] = cvtpk(a[0], a[1]); p[2 * kt + 1] = cvtpk(a[2], a[3]);
    }
    const unsigned short* Vj = V + ((size_t)head * SK + 32 * j + 4 * g4) * DH + c;
#pragma unroll
    for (int dt = 0; dt < 12; ++dt) {
      u16x8 vf;
#pragma unroll
      for (int i = 0; i < 8; ++i) vf[i] = Vj[(16 * (i >> 2) + (i & 3)) * DH + 16 * dt];
      o[dt] = __builtin_amdgcn_mfma_f32_16x16x32_bf16(__builtin_bit_cast(bf16x8, vf), __builtin_bit_cast(bf16x8, p), o[dt], 0, 0, 0);
    }
  }
  const float inv = 1.0f / xsum(l);
  if (row0 + c < SQ) {
#pragma unroll
    for (int dt = 0; dt < 12; ++dt)
      *(u32x2*)(O + ((size_t)head * SQ + qrow) * DH + 16 * dt + 4 * g4) = u32x2{cvtpk(o[dt][0] * inv, o[dt][1] * inv), cvtpk(o[dt][2] * inv, o[dt][3] * inv)};
  }
}

__global__ __launch_bounds__(256, 1)
void attn_fwd_g(const unsigned short* __restrict__ Qg, const unsigned short* __restrict__ Kg,
                const unsigned short* __restrict__ Vg, unsigned short* __restrict__ Og) {
  const int wid = __builtin_amdgcn_readfirstlane(threadIdx.x >> 6);
  const int wg = blockIdx.x;
  unsigned it[4];
  const unsigned xcd = wg & 7, p = wg >> 4, side = (wg >> 3) & 1, x = ((p < 8 ? 0x110f0e0d0c0a0908ull : 0x1a19181716141312ull) >> (8 * (p & 7))) & 255;
  it[0] = (8 * p + xcd) << 16 | (side ? x << 8 | (53 - x) : x);
  it[1] = (8 * (16 + 2 * p + side) + xcd) << 16 | 53;
  it[2] = (8 * (48 + p) + xcd) << 16 | (side ? (53 - x) << 8 | x : 53 - x);
  it[3] = 0;
  unsigned bad;
  asm volatile(
  "s_load_dwordx8 s[76:83], %1, 0\n"
  "v_mbcnt_lo_u32_b32 v0, -1, 0\n"
  "v_mbcnt_hi_u32_b32 v0, -1, v0\n"
  "L_PC_%=:\n"
  "s_getpc_b64 s[84:85]\n"
  "s_lshr_b32 s62, %7, 6\n"
  "s_add_u32 s62, s62, %2\n"
  "s_lshl_b32 s62, s62, 13\n"
  "s_add_u32 s84, s84, s62\n"
  "s_addc_u32 s85, s85, 0\n"
  "s_sub_i32 s86, L_END_%=-L_PC_%=, s62\n"
  "s_max_i32 s86, s86, 0\n"
  "s_mov_b32 s87, 0x20000\n"
  "s_mov_b32 m0, 159744\n"
  "v_lshlrev_b32 v1, 7, v0\n"
  "buffer_load_dword v1, s[84:87], 0 offen lds\n"
  "s_mov_b32 s52, %3\n"
  "s_mov_b32 s53, %4\n"
  "s_mov_b32 s54, %5\n"
  "s_mov_b32 s55, %6\n"
  "s_and_b32 s65, %2, 1\n"
  "s_mul_i32 s59, %2, 18432\n"
  "s_add_u32 s59, s59, 86016\n"
  "s_mov_b32 s42, 100663296\n"
  "s_mov_b32 s43, 0x20000\n"
  "s_mov_b32 s46, 166723584\n"
  "s_mov_b32 s47, 0x20000\n"
  "s_lshr_b32 s62, s52, 16\n"
  "s_lshl1_add_u32 s62, s62, s65\n"
  "s_mul_i32 s57, s62, 98304\n"
  "s_and_b32 s56, s52, 0xff\n"
  "s_cmp_lg_u32 s53, 0\n"
  "s_cselect_b32 s62, 1, 0\n"
  "s_sub_u32 s56, s56, s62\n"
  "s_lshr_b32 s62, s52, 16\n"
  "s_mul_i32 s62, s62, 53\n"
  "s_bfe_u32 s63, s52, 0x80008\n"
  "s_add_u32 s62, s62, s63\n"
  "s_mul_i32 s67, s62, 6144\n"
  "s_add_u32 s64, s67, 6144\n"
  "s_lshl_b32 s66, s65, 10\n"
  "s_add_u32 s66, s66, 49152\n"
  "v_lshrrev_b32 v2, 3, v0\n"
  "v_and_b32 v1, 7, v0\n"
  "v_xor_b32 v1, v1, v2\n"
  "v_mul_u32_u24 v101, 0x180, v2\n"
  "v_lshl_add_u32 v101, v1, 4, v101\n"
  "v_add_u32 v102, 0x800, v101\n"
  "s_mul_i32 s62, s65, 3072\n"
  "v_add_u32 v100, s62, v101\n"
  "s_cmp_gt_u32 %2, 1\n"
  "s_waitcnt lgkmcnt(0)\n"
  "s_cbranch_scc1 B_START_%=\n"
  "A_START_%=:\n"
  "s_mov_b64 s[40:41], s[78:79]\n"
  "s_add_u32 m0, s59, 0\n"
  "s_nop 0\n"
  "buffer_load_dwordx4 v101, s[40:43], s57 offen offset:0  lds\n"
  "buffer_load_dwordx4 v102, s[40:43], s57 offen offset:1024  lds\n"
  "s_add_u32 m0, s59, 1920\n"
  "s_nop 0\n"
  "buffer_load_dwordx4 v101, s[40:43], s57 offen offset:128  lds\n"
  "buffer_load_dwordx4 v102, s[40:43], s57 offen offset:1152  lds\n"
  "s_add_u32 m0, s59, 3840\n"
  "s_nop 0\n"
  "buffer_load_dwordx4 v101, s[40:43], s57 offen offset:256  lds\n"
  "buffer_load_dwordx4 v102, s[40:43], s57 offen offset:1280  lds\n"
  "s_add_u32 s57, s57, 6144\n"
  "s_add_u32 m0, s59, 5888\n"
  "s_nop 0\n"
  "buffer_load_dwordx4 v101, s[40:43], s57 offen offset:256  lds\n"
  "buffer_load_dwordx4 v102, s[40:43], s57 offen offset:1280  lds\n"
  "s_add_u32 m0, s59, 8064\n"
  "s_nop 0\n"
  "buffer_load_dwordx4 v101, s[40:43], s57 offen offset:128  lds\n"
  "buffer_load_dwordx4 v102, s[40:43], s57 offen offset:1152  lds\n"
  "s_add_u32 m0, s59, 10240\n"
  "s_nop 0\n"
  "buffer_load_dwordx4 v101, s[40:43], s57 offen offset:0  lds\n"
  "buffer_load_dwordx4 v102, s[40:43], s57 offen offset:1024  lds\n"
  "s_add_u32 s57, s57, 6144\n"
  "s_add_u32 m0, s59, 12288\n"
  "s_nop 0\n"
  "buffer_load_dwordx4 v101, s[40:43], s57 offen offset:0  lds\n"
  "buffer_load_dwordx4 v102, s[40:43], s57 offen offset:1024  lds\n"
  "s_add_u32 m0, s59, 14208\n"
  "s_nop 0\n"
  "buffer_load_dwordx4 v101, s[40:43], s57 offen offset:128  lds\n"
  "buffer_load_dwordx4 v102, s[40:43], s57 offen offset:1152  lds\n"
  "s_add_u32 m0, s59, 16128\n"
  "s_nop 0\n"
  "buffer_load_dwordx4 v101, s[40:43], s57 offen offset:256  lds\n"
  "buffer_load_dwordx4 v102, s[40:43], s57 offen offset:1280  lds\n"
  "s_add_u32 s57, s57, 6144\n"
  "v_and_b32 v2, 7, v0\n"
  "v_lshrrev_b32 v83, 4, v0\n"
  "v_xor_b32 v83, v83, v2\n"
  "v_lshlrev_b32 v83, 4, v83\n"
  "v_lshl_add_u32 v83, v2, 7, v83\n"
  "v_bfe_u32 v1, v0, 3, 1\n"
  "v_lshl_add_u32 v83, v1, 10, v83\n"
  "v_xor_b32 v84, 64, v83\n"
  "v_add_u32 v87, s59, v83\n"
  "v_add_u32 v88, s59, v84\n"
  "s_mov_b32 s66, 49152\n"
  "v_add_u32 v81, s66, v83\n"
  "v_add_u32 v82, s66, v84\n"
  "s_lshl_b32 s62, s65, 13\n"
  "v_lshlrev_b32 v80, 4, v0\n"
  "v_add_u32 v80, s62, v80\n"
  "s_mov_b32 s60, 0x3dd53b94\n"
  "s_mov_b32 s61, 0xc0800000\n"
  "s_waitcnt vmcnt(16)\n"
  "ds_read_b128 a[0:3], v87 offset:0\n"
  "ds_read_b128 a[4:7], v88 offset:0\n"
  "s_waitcnt vmcnt(14)\n"
  "ds_read_b128 a[8:11], v87 offset:2048\n"
  "ds_read_b128 a[12:15], v88 offset:2048\n"
  "s_add_u32 m0, s59, -256\n"
  "s_waitcnt lgkmcnt(2)\n"
  "buffer_load_dwordx4 v101, s[40:43], s57 offen offset:256  lds\n"
  "buffer_load_dwordx4 v102, s[40:43], s57 offen offset:1280  lds\n"
  "s_waitcnt vmcnt(14)\n"
  "ds_read_b128 a[16:19], v87 offset:4096\n"
  "ds_read_b128 a[20:23], v88 offset:4096\n"
  "s_add_u32 m0, s59, 1920\n"
  "s_waitcnt lgkmcnt(2)\n"
  "buffer_load_dwordx4 v101, s[40:43], s57 offen offset:128  lds\n"
  "buffer_load_dwordx4 v102, s[40:43], s57 offen offset:1152  lds\n"
  "s_waitcnt vmcnt(14)\n"
  "ds_read_b128 a[40:43], v87 offset:6144\n"
  "ds_read_b128 a[44:47], v88 offset:6144\n"
  "s_add_u32 m0, s59, 4096\n"
  "s_waitcnt lgkmcnt(2)\n"
  "buffer_load_dwordx4 v101, s[40:43], s57 offen offset:0  lds\n"
  "buffer_load_dwordx4 v102, s[40:43], s57 offen offset:1024  lds\n"
  "s_add_u32 s57, s57, 6144\n"
  "s_waitcnt vmcnt(14)\n"
  "ds_read_b128 a[32:35], v87 offset:8192\n"
  "ds_read_b128 a[36:39], v88 offset:8192\n"
  "s_add_u32 m0, s59, 6144\n"
  "s_waitcnt lgkmcnt(2)\n"
  "buffer_load_dwordx4 v101, s[40:43], s57 offen offset:0  lds\n"
  "buffer_load_dwordx4 v102, s[40:43], s57 offen offset:1024  lds\n"
  "s_waitcnt vmcnt(14)\n"
  "ds_read_b128 a[24:27], v87 offset:10240\n"
  "ds_read_b128 a[28:31], v88 offset:10240\n"
  "s_add_u32 m0, s59, 8064\n"
  "s_waitcnt lgkmcnt(2)\n"
  "buffer_load_dwordx4 v101, s[40:43], s57 offen offset:128  lds\n"
  "buffer_load_dwordx4 v102, s[40:43], s57 offen offset:1152  lds\n"
  "s_waitcnt vmcnt(14)\n"
  "ds_read_b128 a[48:51], v87 offset:12288\n"
  "ds_read_b128 a[52:55], v88 offset:12288\n"
  "s_add_u32 m0, s59, 9984\n"
  "s_waitcnt lgkmcnt(2)\n"
  "buffer_load_dwordx4 v101, s[40:43], s57 offen offset:256  lds\n"
  "buffer_load_dwordx4 v102, s[40:43], s57 offen offset:1280  lds\n"
  "s_add_u32 s57, s57, 6144\n"
  "s_waitcnt vmcnt(14)\n"
  "ds_read_b128 a[56:59], v87 offset:14336\n"
  "ds_read_b128 a[60:63], v88 offset:14336\n"
  "s_add_u32 m0, s59, 12032\n"
  "s_waitcnt lgkmcnt(2)\n"
  "buffer_load_dwordx4 v101, s[40:43], s57 offen offset:256  lds\n"
  "buffer_load_dwordx4 v102, s[40:43], s57 offen offset:1280  lds\n"
  "s_waitcnt vmcnt(14)\n"
  "ds_read_b128 a[64:67], v87 offset:16384\n"
  "ds_read_b128 a[68:71], v88 offset:16384\n"
  "s_add_u32 m0, s59, 14208\n"
  "s_waitcnt lgkmcnt(2)\n"
  "buffer_load_dwordx4 v101, s[40:43], s57 offen offset:128  lds\n"
  "buffer_load_dwordx4 v102, s[40:43], s57 offen offset:1152  lds\n"
  "s_waitcnt vmcnt(14)\n"
  "ds_read_b128 a[88:91], v87 offset:0\n"
  "ds_read_b128 a[92:95], v88 offset:0\n"
  "s_add_u32 m0, s59, 16384\n"
  "s_barrier\n"
  "ds_read_b128 v[32:35], v81 offset:0\n"
  "ds_read_b128 v[36:39], v82 offset:0\n"
  "ds_read_b128 v[40:43], v81 offset:2048\n"
  "ds_read_b128 v[44:47], v82 offset:2048\n"
  "ds_read_b128 v[48:51], v81 offset:4096\n"
  "ds_read_b128 v[52:55], v82 offset:4096\n"
  "s_waitcnt lgkmcnt(0)\n"
  "v_mfma_f32_16x16x32_bf16 v[0:3], a[0:3], v[32:35], 0\n"
  "s_add_u32 s66, s66, 6144\n"
  "s_cmp_eq_u32 s66, 86016\n"
  "s_cselect_b32 s66, 49152, s66\n"
  "buffer_load_dwordx4 v101, s[40:43], s57 offen offset:0  lds\n"
  "buffer_load_dwordx4 v102, s[40:43], s57 offen offset:1024  lds\n"
  "s_add_u32 s57, s57, 6144\n"
  "v_mfma_f32_16x16x32_bf16 v[0:3], a[4:7], v[36:39], v[0:3]\n"
  "s_waitcnt vmcnt(14)\n"
  "ds_read_b128 a[80:83], v87 offset:2048\n"
  "ds_read_b128 a[84:87], v88 offset:2048\n"
  "s_add_u32 m0, s59, 0\n"
  "v_mfma_f32_16x16x32_bf16 v[0:3], a[8:11], v[40:43], v[0:3]\n"
  "buffer_load_dwordx4 v101, s[40:43], s57 offen offset:0  lds\n"
  "buffer_load_dwordx4 v102, s[40:43], s57 offen offset:1024  lds\n"
  "v_mfma_f32_16x16x32_bf16 v[0:3], a[12:15], v[44:47], v[0:3]\n"
  "s_waitcnt vmcnt(14)\n"
  "ds_read_b128 a[72:75], v87 offset:4096\n"
  "ds_read_b128 a[76:79], v88 offset:4096\n"
  "s_add_u32 m0, s59, 1920\n"
  "v_mfma_f32_16x16x32_bf16 v[0:3], a[16:19], v[48:51], v[0:3]\n"
  "s_waitcnt lgkmcnt(2)\n"
  "buffer_load_dwordx4 v101, s[40:43], s57 offen offset:128  lds\n"
  "buffer_load_dwordx4 v102, s[40:43], s57 offen offset:1152  lds\n"
  "v_mfma_f32_16x16x32_bf16 v[0:3], a[20:23], v[52:55], v[0:3]\n"
  "s_waitcnt vmcnt(14)\n"
  "ds_read_b128 a[96:99], v87 offset:6144\n"
  "ds_read_b128 a[100:103], v88 offset:6144\n"
  "s_add_u32 m0, s59, 3840\n"
  "v_mfma_f32_16x16x32_bf16 v[4:7], a[44:47], v[52:55], 0\n"
  "s_waitcnt lgkmcnt(2)\n"
  "buffer_load_dwordx4 v101, s[40:43], s57 offen offset:256  lds\n"
  "buffer_load_dwordx4 v102, s[40:43], s57 offen offset:1280  lds\n"
  "s_add_u32 s57, s57, 6144\n"
  "v_mfma_f32_16x16x32_bf16 v[4:7], a[40:43], v[48:51], v[4:7]\n"
  "s_waitcnt vmcnt(14)\n"
  "ds_read_b128 a[104:107], v87 offset:8192\n"
  "ds_read_b128 a[108:111], v88 offset:8192\n"
  "s_add_u32 m0, s59, 5888\n"
  "v_mfma_f32_16x16x32_bf16 v[4:7], a[36:39], v[44:47], v[4:7]\n"
  "s_waitcnt lgkmcnt(2)\n"
  "buffer_load_dwordx4 v101, s[40:43], s57 offen offset:256  lds\n"
  "buffer_load_dwordx4 v102, s[40:43], s57 offen offset:1280  lds\n"
  "v_mfma_f32_16x16x32_bf16 v[4:7], a[32:35], v[40:43], v[4:7]\n"
  "s_waitcnt vmcnt(14)\n"
  "ds_read_b128 a[112:115], v87 offset:10240\n"
  "ds_read_b128 a[116:119], v88 offset:10240\n"
  "s_add_u32 m0, s59, 8064\n"
  "v_mfma_f32_16x16x32_bf16 v[4:7], a[28:31], v[36:39], v[4:7]\n"
  "s_waitcnt lgkmcnt(2)\n"
  "buffer_load_dwordx4 v101, s[40:43], s57 offen offset:128  lds\n"
  "buffer_load_dwordx4 v102, s[40:43], s57 offen offset:1152  lds\n"
  "v_mfma_f32_16x16x32_bf16 v[4:7], a[24:27], v[32:35], v[4:7]\n"
  "s_waitcnt vmcnt(14)\n"
  "ds_read_b128 a[136:139], v87 offset:12288\n"
  "ds_read_b128 a[140:143], v88 offset:12288\n"
  "s_add_u32 m0, s59, 10240\n"
  "v_add_u32 v81, s66, v83\n"
  "v_add_u32 v82, s66, v84\n"
  "v_mfma_f32_16x16x32_bf16 v[8:11], a[48:51], v[32:35], 0\n"
  "s_waitcnt lgkmcnt(2)\n"
  "buffer_load_dwordx4 v101, s[40:43], s57 offen offset:0  lds\n"
  "buffer_load_dwordx4 v102, s[40:43], s57 offen offset:1024  lds\n"
  "s_add_u32 s57, s57, 6144\n"
  "v_mfma_f32_16x16x32_bf16 v[8:11], a[52:55], v[36:39], v[8:11]\n"
  "s_waitcnt vmcnt(14)\n"
  "ds_read_b128 a[128:131], v87 offset:14336\n"
  "ds_read_b128 a[132:135], v88 offset:14336\n"
  "s_add_u32 m0, s59, 12288\n"
  "v_mfma_f32_16x16x32_bf16 v[8:11], a[56:59], v[40:43], v[8:11]\n"
  "s_waitcnt lgkmcnt(2)\n"
  "buffer_load_dwordx4 v101, s[40:43], s57 offen offset:0  lds\n"
  "buffer_load_dwordx4 v102, s[40:43], s57 offen offset:1024  lds\n"
  "v_mfma_f32_16x16x32_bf16 v[8:11], a[60:63], v[44:47], v[8:11]\n"
  "s_waitcnt vmcnt(14)\n"
  "ds_read_b128 a[120:123], v87 offset:16384\n"
  "ds_read_b128 a[124:127], v88 offset:16384\n"
  "s_add_u32 m0, s59, 14208\n"
  "v_mfma_f32_16x16x32_bf16 v[8:11], a[64:67], v[48:51], v[8:11]\n"
  "s_waitcnt lgkmcnt(2)\n"
  "buffer_load_dwordx4 v101, s[40:43], s57 offen offset:128  lds\n"
  "buffer_load_dwordx4 v102, s[40:43], s57 offen offset:1152  lds\n"
  "v_mfma_f32_16x16x32_bf16 v[8:11], a[68:71], v[52:55], v[8:11]\n"
  "s_waitcnt vmcnt(14)\n"
  "ds_read_b128 a[144:147], v87 offset:0\n"
  "ds_read_b128 a[148:151], v88 offset:0\n"
  "s_add_u32 m0, s59, 16128\n"
  "v_pk_fma_f32 v[0:1], v[0:1], s[60:61], s[60:61] op_sel:[0,0,1] op_sel_hi:[1,0,1]\n"
  "v_pk_fma_f32 v[2:3], v[2:3], s[60:61], s[60:61] op_sel:[0,0,1] op_sel_hi:[1,0,1]\n"
  "v_pk_fma_f32 v[4:5], v[4:5], s[60:61], s[60:61] op_sel:[0,0,1] op_sel_hi:[1,0,1]\n"
  "v_pk_fma_f32 v[6:7], v[6:7], s[60:61], s[60:61] op_sel:[0,0,1] op_sel_hi:[1,0,1]\n"
  "v_mfma_f32_16x16x32_bf16 v[12:15], a[92:95], v[52:55], 0\n"
  "s_waitcnt lgkmcnt(2)\n"
  "buffer_load_dwordx4 v101, s[40:43], s57 offen offset:256  lds\n"
  "buffer_load_dwordx4 v102, s[40:43], s57 offen offset:1280  lds\n"
  "s_add_u32 s57, s57, 6144\n"
  "v_exp_f32 v0, v0\n"
  "v_mfma_f32_16x16x32_bf16 v[12:15], a[88:91], v[48:51], v[12:15]\n"
  "s_waitcnt vmcnt(14)\n"
  "ds_read_b128 a[152:155], v87 offset:2048\n"
  "ds_read_b128 a[156:159], v88 offset:2048\n"
  "s_add_u32 m0, s59, -256\n"
  "v_exp_f32 v1, v1\n"
  "v_mfma_f32_16x16x32_bf16 v[12:15], a[84:87], v[44:47], v[12:15]\n"
  "s_waitcnt lgkmcnt(2)\n"
  "buffer_load_dwordx4 v101, s[40:43], s57 offen offset:256  lds\n"
  "buffer_load_dwordx4 v102, s[40:43], s57 offen offset:1280  lds\n"
  "v_exp_f32 v2, v2\n"
  "v_mfma_f32_16x16x32_bf16 v[12:15], a[80:83], v[40:43], v[12:15]\n"
  "s_waitcnt vmcnt(14)\n"
  "ds_read_b128 a[160:163], v87 offset:4096\n"
  "ds_read_b128 a[164:167], v88 offset:4096\n"
  "s_add_u32 m0, s59, 1920\n"
  "v_exp_f32 v3, v3\n"
  "v_mfma_f32_16x16x32_bf16 v[12:15], a[76:79], v[36:39], v[12:15]\n"
  "s_waitcnt lgkmcnt(2)\n"
  "buffer_load_dwordx4 v101, s[40:43], s57 offen offset:128  lds\n"
  "buffer_load_dwordx4 v102, s[40:43], s57 offen offset:1152  lds\n"
  "v_cvt_pk_bf16_f32 v0, v0, v1\n"
  "v_cvt_pk_bf16_f32 v1, v2, v3\n"
  "v_mfma_f32_16x16x32_bf16 v[12:15], a[72:75], v[32:35], v[12:15]\n"
  "s_waitcnt vmcnt(14)\n"
  "ds_read_b128 a[184:187], v87 offset:6144\n"
  "ds_read_b128 a[188:191], v88 offset:6144\n"
  "s_add_u32 m0, s59, 4096\n"
  "v_mfma_f32_16x16x32_bf16 v[16:19], a[96:99], v[32:35], 0\n"
  "s_waitcnt lgkmcnt(2)\n"
  "buffer_load_dwordx4 v101, s[40:43], s57 offen offset:0  lds\n"
  "buffer_load_dwordx4 v102, s[40:43], s57 offen offset:1024  lds\n"
  "s_add_u32 s57, s57, 6144\n"
  "v_exp_f32 v4, v4\n"
  "v_mfma_f32_16x16x32_bf16 v[16:19], a[100:103], v[36:39], v[16:19]\n"
  "s_waitcnt vmcnt(14)\n"
  "ds_read_b128 a[176:179], v87 offset:8192\n"
  "ds_read_b128 a[180:183], v88 offset:8192\n"
  "s_add_u32 m0, s59, 6144\n"
  "v_exp_f32 v5, v5\n"
  "v_mfma_f32_16x16x32_bf16 v[16:19], a[104:107], v[40:43], v[16:19]\n"
  "s_waitcnt lgkmcnt(2)\n"
  "buffer_load_dwordx4 v101, s[40:43], s57 offen offset:0  lds\n"
  "buffer_load_dwordx4 v102, s[40:43], s57 offen offset:1024  lds\n"
  "v_exp_f32 v6, v6\n"
  "v_mfma_f32_16x16x32_bf16 v[16:19], a[108:111], v[44:47], v[16:19]\n"
  "s_waitcnt vmcnt(14)\n"
  "ds_read_b128 a[168:171], v87 offset:10240\n"
  "ds_read_b128 a[172:175], v88 offset:10240\n"
  "s_add_u32 m0, s59, 8064\n"
  "v_exp_f32 v7, v7\n"
  "v_mfma_f32_16x16x32_bf16 v[16:19], a[112:115], v[48:51], v[16:19]\n"
  "s_waitcnt lgkmcnt(2)\n"
  "buffer_load_dwordx4 v101, s[40:43], s57 offen offset:128  lds\n"
  "buffer_load_dwordx4 v102, s[40:43], s57 offen offset:1152  lds\n"
  "v_cvt_pk_bf16_f32 v2, v4, v5\n"
  "v_cvt_pk_bf16_f32 v3, v6, v7\n"
  "v_mfma_f32_16x16x32_bf16 v[16:19], a[116:119], v[52:55], v[16:19]\n"
  "s_waitcnt vmcnt(14)\n"
  "ds_read_b128 a[192:195], v87 offset:12288\n"
  "ds_read_b128 a[196:199], v88 offset:12288\n"
  "s_add_u32 m0, s59, 9984\n"
  "ds_write_b128 v80, v[0:3] offset:0\n"
  "v_pk_fma_f32 v[8:9], v[8:9], s[60:61], s[60:61] op_sel:[0,0,1] op_sel_hi:[1,0,1]\n"
  "v_pk_fma_f32 v[10:11], v[10:11], s[60:61], s[60:61] op_sel:[0,0,1] op_sel_hi:[1,0,1]\n"
  "v_pk_fma_f32 v[12:13], v[12:13], s[60:61], s[60:61] op_sel:[0,0,1] op_sel_hi:[1,0,1]\n"
  "v_pk_fma_f32 v[14:15], v[14:15], s[60:61], s[60:61] op_sel:[0,0,1] op_sel_hi:[1,0,1]\n"
  "v_mfma_f32_16x16x32_bf16 v[20:23], a[140:143], v[52:55], 0\n"
  "s_waitcnt lgkmcnt(3)\n"
  "buffer_load_dwordx4 v101, s[40:43], s57 offen offset:256  lds\n"
  "buffer_load_dwordx4 v102, s[40:43], s57 offen offset:1280  lds\n"
  "s_add_u32 s57, s57, 6144\n"
  "v_exp_f32 v8, v8\n"
  "v_mfma_f32_16x16x32_bf16 v[20:23], a[136:139], v[48:51], v[20:23]\n"
  "s_waitcnt vmcnt(14)\n"
  "ds_read_b128 a[200:203], v87 offset:14336\n"
  "ds_read_b128 a[204:207], v88 offset:14336\n"
  "s_add_u32 m0, s59, 12032\n"
  "v_exp_f32 v9, v9\n"
  "v_mfma_f32_16x16x32_bf16 v[20:23], a[132:135], v[44:47], v[20:23]\n"
  "s_waitcnt lgkmcnt(3)\n"
  "buffer_load_dwordx4 v101, s[40:43], s57 offen offset:256  lds\n"
  "buffer_load_dwordx4 v102, s[40:43], s57 offen offset:1280  lds\n"
  "v_exp_f32 v10, v10\n"
  "v_mfma_f32_16x16x32_bf16 v[20:23], a[128:131], v[40:43], v[20:23]\n"
  "s_waitcnt vmcnt(14)\n"
  "ds_read_b128 a[208:211], v87 offset:16384\n"
  "ds_read_b128 a[212:215], v88 offset:16384\n"
  "s_add_u32 m0, s59, 14208\n"
  "v_exp_f32 v11, v11\n"
  "v_mfma_f32_16x16x32_bf16 v[20:23], a[124:127], v[36:39], v[20:23]\n"
  "s_waitcnt lgkmcnt(2)\n"
  "buffer_load_dwordx4 v101, s[40:43], s57 offen offset:128  lds\n"
  "buffer_load_dwordx4 v102, s[40:43], s57 offen offset:1152  lds\n"
  "v_cvt_pk_bf16_f32 v8, v8, v9\n"
  "v_cvt_pk_bf16_f32 v9, v10, v11\n"
  "v_mfma_f32_16x16x32_bf16 v[20:23], a[120:123], v[32:35], v[20:23]\n"
  "s_waitcnt vmcnt(14)\n"
  "ds_read_b128 a[232:235], v87 offset:0\n"
  "ds_read_b128 a[236:239], v88 offset:0\n"
  "s_add_u32 m0, s59, 16384\n"
  "ds_read_b128 v[56:59], v81 offset:0\n"
  "ds_read_b128 v[60:63], v82 offset:0\n"
  "v_mfma_f32_16x16x32_bf16 v[24:27], a[144:147], v[32:35], 0\n"
  "s_waitcnt lgkmcnt(4)\n"
  "buffer_load_dwordx4 v101, s[40:43], s57 offen offset:0  lds\n"
  "buffer_load_dwordx4 v102, s[40:43], s57 offen offset:1024  lds\n"
  "s_add_u32 s57, s57, 6144\n"
  "v_exp_f32 v12, v12\n"
  "v_mfma_f32_16x16x32_bf16 v[24:27], a[148:151], v[36:39], v[24:27]\n"
  "s_waitcnt vmcnt(14)\n"
  "ds_read_b128 a[224:227], v87 offset:2048\n"
  "ds_read_b128 a[228:231], v88 offset:2048\n"
  "s_add_u32 m0, s59, 0\n"
  "v_exp_f32 v13, v13\n"
  "v_mfma_f32_16x16x32_bf16 v[24:27], a[152:155], v[40:43], v[24:27]\n"
  "s_waitcnt lgkmcnt(4)\n"
  "buffer_load_dwordx4 v101, s[40:43], s57 offen offset:0  lds\n"
  "buffer_load_dwordx4 v102, s[40:43], s57 offen offset:1024  lds\n"
  "v_exp_f32 v14, v14\n"
  "v_mfma_f32_16x16x32_bf16 v[24:27], a[156:159], v[44:47], v[24:27]\n"
  "s_waitcnt vmcnt(14)\n"
  "ds_read_b128 a[216:219], v87 offset:4096\n"
  "ds_read_b128 a[220:223], v88 offset:4096\n"
  "s_add_u32 m0, s59, 1920\n"
  "v_exp_f32 v15, v15\n"
  "v_mfma_f32_16x16x32_bf16 v[24:27], a[160:163], v[48:51], v[24:27]\n"
  "s_waitcnt lgkmcnt(2)\n"
  "buffer_load_dwordx4 v101, s[40:43], s57 offen offset:128  lds\n"
  "buffer_load_dwordx4 v102, s[40:43], s57 offen offset:1152  lds\n"
  "v_cvt_pk_bf16_f32 v10, v12, v13\n"
  "v_cvt_pk_bf16_f32 v11, v14, v15\n"
  "v_mfma_f32_16x16x32_bf16 v[24:27], a[164:167], v[52:55], v[24:27]\n"
  "s_waitcnt vmcnt(14)\n"
  "ds_read_b128 a[240:243], v87 offset:6144\n"
  "ds_read_b128 a[244:247], v88 offset:6144\n"
  "s_add_u32 m0, s59, 3840\n"
  "ds_write_b128 v80, v[8:11] offset:1024\n"
  "v_pk_fma_f32 v[16:17], v[16:17], s[60:61], s[60:61] op_sel:[0,0,1] op_sel_hi:[1,0,1]\n"
  "v_pk_fma_f32 v[18:19], v[18:19], s[60:61], s[60:61] op_sel:[0,0,1] op_sel_hi:[1,0,1]\n"
  "v_pk_fma_f32 v[20:21], v[20:21], s[60:61], s[60:61] op_sel:[0,0,1] op_sel_hi:[1,0,1]\n"
  "v_pk_fma_f32 v[22:23], v[22:23], s[60:61], s[60:61] op_sel:[0,0,1] op_sel_hi:[1,0,1]\n"
  "v_mfma_f32_16x16x32_bf16 v[28:31], a[188:191], v[52:55], 0\n"
  "s_waitcnt lgkmcnt(3)\n"
  "buffer_load_dwordx4 v101, s[40:43], s57 offen offset:256  lds\n"
  "buffer_load_dwordx4 v102, s[40:43], s57 offen offset:1280  lds\n"
  "s_add_u32 s57, s57, 6144\n"
  "v_exp_f32 v16, v16\n"
  "v_mfma_f32_16x16x32_bf16 v[28:31], a[184:187], v[48:51], v[28:31]\n"
  "s_waitcnt vmcnt(14)\n"
  "ds_read_b128 a[248:251], v87 offset:8192\n"
  "ds_read_b128 a[252:255], v88 offset:8192\n"
  "s_add_u32 m0, s59, 5888\n"
  "v_exp_f32 v17, v17\n"
  "v_mfma_f32_16x16x32_bf16 v[28:31], a[180:183], v[44:47], v[28:31]\n"
  "s_waitcnt lgkmcnt(3)\n"
  "buffer_load_dwordx4 v101, s[40:43], s57 offen offset:256  lds\n"
  "buffer_load_dwordx4 v102, s[40:43], s57 offen offset:1280  lds\n"
  "v_exp_f32 v18, v18\n"
  "v_mfma_f32_16x16x32_bf16 v[28:31], a[176:179], v[40:43], v[28:31]\n"
  "s_waitcnt vmcnt(14)\n"
  "ds_read_b128 v[128:131], v87 offset:10240\n"
  "ds_read_b128 v[132:135], v88 offset:10240\n"
  "s_add_u32 m0, s59, 8064\n"
  "v_exp_f32 v19, v19\n"
  "v_mfma_f32_16x16x32_bf16 v[28:31], a[172:175], v[36:39], v[28:31]\n"
  "s_waitcnt lgkmcnt(2)\n"
  "buffer_load_dwordx4 v101, s[40:43], s57 offen offset:128  lds\n"
  "buffer_load_dwordx4 v102, s[40:43], s57 offen offset:1152  lds\n"
  "v_cvt_pk_bf16_f32 v16, v16, v17\n"
  "v_cvt_pk_bf16_f32 v17, v18, v19\n"
  "v_mfma_f32_16x16x32_bf16 v[28:31], a[168:171], v[32:35], v[28:31]\n"
  "s_waitcnt vmcnt(14)\n"
  "ds_read_b128 v[152:155], v87 offset:12288\n"
  "ds_read_b128 v[156:159], v88 offset:12288\n"
  "s_add_u32 m0, s59, 10240\n"
  "ds_read_b128 v[64:67], v81 offset:2048\n"
  "ds_read_b128 v[68:71], v82 offset:2048\n"
  "v_mfma_f32_16x16x32_bf16 v[0:3], a[192:195], v[32:35], 0\n"
  "s_waitcnt lgkmcnt(4)\n"
  "buffer_load_dwordx4 v101, s[40:43], s57 offen offset:0  lds\n"
  "buffer_load_dwordx4 v102, s[40:43], s57 offen offset:1024  lds\n"
  "s_add_u32 s57, s57, 6144\n"
  "v_exp_f32 v20, v20\n"
  "v_mfma_f32_16x16x32_bf16 v[0:3], a[196:199], v[36:39], v[0:3]\n"
  "s_waitcnt vmcnt(14)\n"
  "ds_read_b128 v[144:147], v87 offset:14336\n"
  "ds_read_b128 v[148:151], v88 offset:14336\n"
  "s_add_u32 m0, s59, 12288\n"
  "v_exp_f32 v21, v21\n"
  "v_mfma_f32_16x16x32_bf16 v[0:3], a[200:203], v[40:43], v[0:3]\n"
  "s_waitcnt lgkmcnt(4)\n"
  "buffer_load_dwordx4 v101, s[40:43], s57 offen offset:0  lds\n"
  "buffer_load_dwordx4 v102, s[40:43], s57 offen offset:1024  lds\n"
  "v_exp_f32 v22, v22\n"
  "v_mfma_f32_16x16x32_bf16 v[0:3], a[204:207], v[44:47], v[0:3]\n"
  "s_waitcnt vmcnt(14)\n"
  "ds_read_b128 v[136:139], v87 offset:16384\n"
  "ds_read_b128 v[140:143], v88 offset:16384\n"
  "s_add_u32 m0, s59, 14208\n"
  "v_exp_f32 v23, v23\n"
  "v_mfma_f32_16x16x32_bf16 v[0:3], a[208:211], v[48:51], v[0:3]\n"
  "s_waitcnt lgkmcnt(2)\n"
  "buffer_load_dwordx4 v101, s[40:43], s57 offen offset:128  lds\n"
  "buffer_load_dwordx4 v102, s[40:43], s57 offen offset:1152  lds\n"
  "v_cvt_pk_bf16_f32 v18, v20, v21\n"
  "v_cvt_pk_bf16_f32 v19, v22, v23\n"
  "v_mfma_f32_16x16x32_bf16 v[0:3], a[212:215], v[52:55], v[0:3]\n"
  "s_waitcnt vmcnt(14)\n"
  "ds_read_b128 v[160:163], v87 offset:0\n"
  "ds_read_b128 v[164:167], v88 offset:0\n"
  "s_add_u32 m0, s59, 16128\n"
  "ds_write_b128 v80, v[16:19] offset:2048\n"
  "v_pk_fma_f32 v[24:25], v[24:25], s[60:61], s[60:61] op_sel:[0,0,1] op_sel_hi:[1,0,1]\n"
  "v_pk_fma_f32 v[26:27], v[26:27], s[60:61], s[60:61] op_sel:[0,0,1] op_sel_hi:[1,0,1]\n"
  "v_pk_fma_f32 v[28:29], v[28:29], s[60:61], s[60:61] op_sel:[0,0,1] op_sel_hi:[1,0,1]\n"
  "v_pk_fma_f32 v[30:31], v[30:31], s[60:61], s[60:61] op_sel:[0,0,1] op_sel_hi:[1,0,1]\n"
  "v_mfma_f32_16x16x32_bf16 v[4:7], a[236:239], v[52:55], 0\n"
  "s_waitcnt lgkmcnt(3)\n"
  "buffer_load_dwordx4 v101, s[40:43], s57 offen offset:256  lds\n"
  "buffer_load_dwordx4 v102, s[40:43], s57 offen offset:1280  lds\n"
  "s_add_u32 s57, s57, 6144\n"
  "v_exp_f32 v24, v24\n"
  "v_mfma_f32_16x16x32_bf16 v[4:7], a[232:235], v[48:51], v[4:7]\n"
  "s_waitcnt vmcnt(14)\n"
  "ds_read_b128 v[168:171], v87 offset:2048\n"
  "ds_read_b128 v[172:175], v88 offset:2048\n"
  "s_add_u32 m0, s59, -256\n"
  "v_exp_f32 v25, v25\n"
  "v_mfma_f32_16x16x32_bf16 v[4:7], a[228:231], v[44:47], v[4:7]\n"
  "s_waitcnt lgkmcnt(3)\n"
  "buffer_load_dwordx4 v101, s[40:43], s57 offen offset:256  lds\n"
  "buffer_load_dwordx4 v102, s[40:43], s57 offen offset:1280  lds\n"
  "v_exp_f32 v26, v26\n"
  "v_mfma_f32_16x16x32_bf16 v[4:7], a[224:227], v[40:43], v[4:7]\n"
  "s_waitcnt vmcnt(14)\n"
  "ds_read_b128 v[176:179], v87 offset:4096\n"
  "ds_read_b128 v[180:183], v88 offset:4096\n"
  "s_add_u32 m0, s59, 1920\n"
  "v_exp_f32 v27, v27\n"
  "v_mfma_f32_16x16x32_bf16 v[4:7], a[220:223], v[36:39], v[4:7]\n"
  "s_waitcnt lgkmcnt(2)\n"
  "buffer_load_dwordx4 v101, s[40:43], s57 offen offset:128  lds\n"
  "buffer_load_dwordx4 v102, s[40:43], s57 offen offset:1152  lds\n"
  "v_cvt_pk_bf16_f32 v24, v24, v25\n"
  "v_cvt_pk_bf16_f32 v25, v26, v27\n"
  "v_mfma_f32_16x16x32_bf16 v[4:7], a[216:219], v[32:35], v[4:7]\n"
  "s_waitcnt vmcnt(14)\n"
  "ds_read_b128 v[200:203], v87 offset:6144\n"
  "ds_read_b128 v[204:207], v88 offset:6144\n"
  "s_add_u32 m0, s59, 4096\n"
  "ds_read_b128 v[72:75], v81 offset:4096\n"
  "ds_read_b128 v[76:79], v82 offset:4096\n"
  "v_mfma_f32_16x16x32_bf16 v[8:11], a[240:243], v[32:35], 0\n"
  "s_waitcnt lgkmcnt(4)\n"
  "buffer_load_dwordx4 v101, s[40:43], s57 offen offset:0  lds\n"
  "buffer_load_dwordx4 v102, s[40:43], s57 offen offset:1024  lds\n"
  "s_add_u32 s57, s57, 6144\n"
  "s_barrier\n"
  "v_exp_f32 v28, v28\n"
  "v_mfma_f32_16x16x32_bf16 v[8:11], a[244:247], v[36:39], v[8:11]\n"
  "s_waitcnt vmcnt(14)\n"
  "ds_read_b128 v[192:195], v87 offset:8192\n"
  "ds_read_b128 v[196:199], v88 offset:8192\n"
  "v_exp_f32 v29, v29\n"
  "v_mfma_f32_16x16x32_bf16 v[8:11], a[248:251], v[40:43], v[8:11]\n"
  "v_exp_f32 v30, v30\n"
  "v_mfma_f32_16x16x32_bf16 v[8:11], a[252:255], v[44:47], v[8:11]\n"
  "s_waitcnt vmcnt(12)\n"
  "ds_read_b128 v[184:187], v87 offset:10240\n"
  "ds_read_b128 v[188:191], v88 offset:10240\n"
  "v_exp_f32 v31, v31\n"
  "v_mfma_f32_16x16x32_bf16 v[8:11], v[128:131], v[48:51], v[8:11]\n"
  "v_cvt_pk_bf16_f32 v26, v28, v29\n"
  "v_cvt_pk_bf16_f32 v27, v30, v31\n"
  "v_mfma_f32_16x16x32_bf16 v[8:11], v[132:135], v[52:55], v[8:11]\n"
  "s_waitcnt vmcnt(10)\n"
  "ds_read_b128 v[208:211], v87 offset:12288\n"
  "ds_read_b128 v[212:215], v88 offset:12288\n"
  "ds_write_b128 v80, v[24:27] offset:3072\n"
  "v_pk_fma_f32 v[0:1], v[0:1], s[60:61], s[60:61] op_sel:[0,0,1] op_sel_hi:[1,0,1]\n"
  "v_pk_fma_f32 v[2:3], v[2:3], s[60:61], s[60:61] op_sel:[0,0,1] op_sel_hi:[1,0,1]\n"
  "v_pk_fma_f32 v[4:5], v[4:5], s[60:61], s[60:61] op_sel:[0,0,1] op_sel_hi:[1,0,1]\n"
  "v_pk_fma_f32 v[6:7], v[6:7], s[60:61], s[60:61] op_sel:[0,0,1] op_sel_hi:[1,0,1]\n"
  "v_mfma_f32_16x16x32_bf16 v[12:15], v[156:159], v[52:55], 0\n"
  "v_exp_f32 v0, v0\n"
  "v_mfma_f32_16x16x32_bf16 v[12:15], v[152:155], v[48:51], v[12:15]\n"
  "s_waitcnt vmcnt(8)\n"
  "ds_read_b128 v[216:219], v87 offset:14336\n"
  "ds_read_b128 v[220:223], v88 offset:14336\n"
  "v_exp_f32 v1, v1\n"
  "v_mfma_f32_16x16x32_bf16 v[12:15], v[148:151], v[44:47], v[12:15]\n"
  "v_exp_f32 v2, v2\n"
  "v_mfma_f32_16x16x32_bf16 v[12:15], v[144:147], v[40:43], v[12:15]\n"
  "s_waitcnt vmcnt(6)\n"
  "ds_read_b128 v[224:227], v87 offset:16384\n"
  "ds_read_b128 v[228:231], v88 offset:16384\n"
  "v_exp_f32 v3, v3\n"
  "v_mfma_f32_16x16x32_bf16 v[12:15], v[140:143], v[36:39], v[12:15]\n"
  "v_cvt_pk_bf16_f32 v0, v0, v1\n"
  "v_cvt_pk_bf16_f32 v1, v2, v3\n"
  "v_mfma_f32_16x16x32_bf16 v[12:15], v[136:139], v[32:35], v[12:15]\n"
  "s_waitcnt vmcnt(4)\n"
  "ds_read_b128 v[248:251], v87 offset:0\n"
  "ds_read_b128 v[252:255], v88 offset:0\n"
  "v_mfma_f32_16x16x32_bf16 v[16:19], v[160:163], v[32:35], 0\n"
  "v_exp_f32 v4, v4\n"
  "v_mfma_f32_16x16x32_bf16 v[16:19], v[164:167], v[36:39], v[16:19]\n"
  "s_waitcnt vmcnt(2)\n"
  "ds_read_b128 v[240:243], v87 offset:2048\n"
  "ds_read_b128 v[244:247], v88 offset:2048\n"
  "v_exp_f32 v5, v5\n"
  "v_mfma_f32_16x16x32_bf16 v[16:19], v[168:171], v[40:43], v[16:19]\n"
  "v_exp_f32 v6, v6\n"
  "v_mfma_f32_16x16x32_bf16 v[16:19], v[172:175], v[44:47], v[16:19]\n"
  "s_waitcnt vmcnt(0)\n"
  "ds_read_b128 v[232:235], v87 offset:4096\n"
  "ds_read_b128 v[236:239], v88 offset:4096\n"
  "v_exp_f32 v7, v7\n"
  "v_mfma_f32_16x16x32_bf16 v[16:19], v[176:179], v[48:51], v[16:19]\n"
  "v_cvt_pk_bf16_f32 v2, v4, v5\n"
  "v_cvt_pk_bf16_f32 v3, v6, v7\n"
  "v_mfma_f32_16x16x32_bf16 v[16:19], v[180:183], v[52:55], v[16:19]\n"
  "s_waitcnt lgkmcnt(15)\n"
  "ds_write_b128 v80, v[0:3] offset:4096\n"
  "v_pk_fma_f32 v[8:9], v[8:9], s[60:61], s[60:61] op_sel:[0,0,1] op_sel_hi:[1,0,1]\n"
  "v_pk_fma_f32 v[10:11], v[10:11], s[60:61], s[60:61] op_sel:[0,0,1] op_sel_hi:[1,0,1]\n"
  "v_pk_fma_f32 v[12:13], v[12:13], s[60:61], s[60:61] op_sel:[0,0,1] op_sel_hi:[1,0,1]\n"
  "v_pk_fma_f32 v[14:15], v[14:15], s[60:61], s[60:61] op_sel:[0,0,1] op_sel_hi:[1,0,1]\n"
  "v_mfma_f32_16x16x32_bf16 v[20:23], v[204:207], v[52:55], 0\n"
  "v_exp_f32 v8, v8\n"
  "v_mfma_f32_16x16x32_bf16 v[20:23], v[200:203], v[48:51], v[20:23]\n"
  "v_exp_f32 v9, v9\n"
  "v_mfma_f32_16x16x32_bf16 v[20:23], v[196:199], v[44:47], v[20:23]\n"
  "v_exp_f32 v10, v10\n"
  "v_mfma_f32_16x16x32_bf16 v[20:23], v[192:195], v[40:43], v[20:23]\n"
  "s_waitcnt lgkmcnt(14)\n"
  "v_exp_f32 v11, v11\n"
  "v_mfma_f32_16x16x32_bf16 v[20:23], v[188:191], v[36:39], v[20:23]\n"
  "v_cvt_pk_bf16_f32 v8, v8, v9\n"
  "v_cvt_pk_bf16_f32 v9, v10, v11\n"
  "v_mfma_f32_16x16x32_bf16 v[20:23], v[184:187], v[32:35], v[20:23]\n"
  "s_waitcnt lgkmcnt(12)\n"
  "v_mfma_f32_16x16x32_bf16 v[24:27], v[208:211], v[32:35], 0\n"
  "v_exp_f32 v12, v12\n"
  "v_mfma_f32_16x16x32_bf16 v[24:27], v[212:215], v[36:39], v[24:27]\n"
  "s_waitcnt lgkmcnt(9)\n"
  "v_exp_f32 v13, v13\n"
  "v_mfma_f32_16x16x32_bf16 v[24:27], v[216:219], v[40:43], v[24:27]\n"
  "v_exp_f32 v14, v14\n"
  "v_mfma_f32_16x16x32_bf16 v[24:27], v[220:223], v[44:47], v[24:27]\n"
  "s_waitcnt lgkmcnt(7)\n"
  "v_exp_f32 v15, v15\n"
  "v_mfma_f32_16x16x32_bf16 v[24:27], v[224:227], v[48:51], v[24:27]\n"
  "v_cvt_pk_bf16_f32 v10, v12, v13\n"
  "v_cvt_pk_bf16_f32 v11, v14, v15\n"
  "v_mfma_f32_16x16x32_bf16 v[24:27], v[228:231], v[52:55], v[24:27]\n"
  "s_waitcnt lgkmcnt(5)\n"
  "ds_write_b128 v80, v[8:11] offset:5120\n"
  "v_pk_fma_f32 v[16:17], v[16:17], s[60:61], s[60:61] op_sel:[0,0,1] op_sel_hi:[1,0,1]\n"
  "v_pk_fma_f32 v[18:19], v[18:19], s[60:61], s[60:61] op_sel:[0,0,1] op_sel_hi:[1,0,1]\n"
  "v_pk_fma_f32 v[20:21], v[20:21], s[60:61], s[60:61] op_sel:[0,0,1] op_sel_hi:[1,0,1]\n"
  "v_pk_fma_f32 v[22:23], v[22:23], s[60:61], s[60:61] op_sel:[0,0,1] op_sel_hi:[1,0,1]\n"
  "v_mfma_f32_16x16x32_bf16 v[28:31], v[252:255], v[52:55], 0\n"
  "v_exp_f32 v16, v16\n"
  "v_mfma_f32_16x16x32_bf16 v[28:31], v[248:251], v[48:51], v[28:31]\n"
  "s_waitcnt lgkmcnt(4)\n"
  "v_exp_f32 v17, v17\n"
  "v_mfma_f32_16x16x32_bf16 v[28:31], v[244:247], v[44:47], v[28:31]\n"
  "v_exp_f32 v18, v18\n"
  "v_mfma_f32_16x16x32_bf16 v[28:31], v[240:243], v[40:43], v[28:31]\n"
  "s_waitcnt lgkmcnt(2)\n"
  "s_sub_u32 s56, s56, 1\n"
  "v_exp_f32 v19, v19\n"
  "v_mfma_f32_16x16x32_bf16 v[28:31], v[236:239], v[36:39], v[28:31]\n"
  "v_cvt_pk_bf16_f32 v16, v16, v17\n"
  "v_cvt_pk_bf16_f32 v17, v18, v19\n"
  "v_mfma_f32_16x16x32_bf16 v[28:31], v[232:235], v[32:35], v[28:31]\n"
  "A_H1_%=:\n"
  "v_mfma_f32_16x16x32_bf16 v[0:3], a[0:3], v[56:59], 0\n"
  "s_add_u32 s66, s66, 6144\n"
  "v_exp_f32 v20, v20\n"
  "v_mfma_f32_16x16x32_bf16 v[0:3], a[4:7], v[60:63], v[0:3]\n"
  "s_cmp_eq_u32 s66, 86016\n"
  "v_exp_f32 v21, v21\n"
  "v_mfma_f32_16x16x32_bf16 v[0:3], a[8:11], v[64:67], v[0:3]\n"
  "s_cselect_b32 s66, 49152, s66\n"
  "v_exp_f32 v22, v22\n"
  "v_mfma_f32_16x16x32_bf16 v[0:3], a[12:15], v[68:71], v[0:3]\n"
  "v_exp_f32 v23, v23\n"
  "v_mfma_f32_16x16x32_bf16 v[0:3], a[16:19], v[72:75], v[0:3]\n"
  "v_cvt_pk_bf16_f32 v18, v20, v21\n"
  "v_cvt_pk_bf16_f32 v19, v22, v23\n"
  "v_mfma_f32_16x16x32_bf16 v[0:3], a[20:23], v[76:79], v[0:3]\n"
  "ds_write_b128 v80, v[16:19] offset:6144\n"
  "v_pk_fma_f32 v[24:25], v[24:25], s[60:61], s[60:61] op_sel:[0,0,1] op_sel_hi:[1,0,1]\n"
  "v_pk_fma_f32 v[26:27], v[26:27], s[60:61], s[60:61] op_sel:[0,0,1] op_sel_hi:[1,0,1]\n"
  "v_pk_fma_f32 v[28:29], v[28:29], s[60:61], s[60:61] op_sel:[0,0,1] op_sel_hi:[1,0,1]\n"
  "v_pk_fma_f32 v[30:31], v[30:31], s[60:61], s[60:61] op_sel:[0,0,1] op_sel_hi:[1,0,1]\n"
  "v_mfma_f32_16x16x32_bf16 v[4:7], a[44:47], v[76:79], 0\n"
  "v_exp_f32 v24, v24\n"
  "v_mfma_f32_16x16x32_bf16 v[4:7], a[40:43], v[72:75], v[4:7]\n"
  "v_exp_f32 v25, v25\n"
  "v_mfma_f32_16x16x32_bf16 v[4:7], a[36:39], v[68:71], v[4:7]\n"
  "v_exp_f32 v26, v26\n"
  "v_mfma_f32_16x16x32_bf16 v[4:7], a[32:35], v[64:67], v[4:7]\n"
  "v_exp_f32 v27, v27\n"
  "v_mfma_f32_16x16x32_bf16 v[4:7], a[28:31], v[60:63], v[4:7]\n"
  "v_cvt_pk_bf16_f32 v24, v24, v25\n"
  "v_cvt_pk_bf16_f32 v25, v26, v27\n"
  "v_mfma_f32_16x16x32_bf16 v[4:7], a[24:27], v[56:59], v[4:7]\n"
  "v_add_u32 v81, s66, v83\n"
  "v_add_u32 v82, s66, v84\n"
  "v_mfma_f32_16x16x32_bf16 v[8:11], a[48:51], v[56:59], 0\n"
  "v_exp_f32 v28, v28\n"
  "v_mfma_f32_16x16x32_bf16 v[8:11], a[52:55], v[60:63], v[8:11]\n"
  "v_exp_f32 v29, v29\n"
  "v_mfma_f32_16x16x32_bf16 v[8:11], a[56:59], v[64:67], v[8:11]\n"
  "v_exp_f32 v30, v30\n"
  "v_mfma_f32_16x16x32_bf16 v[8:11], a[60:63], v[68:71], v[8:11]\n"
  "v_exp_f32 v31, v31\n"
  "v_mfma_f32_16x16x32_bf16 v[8:11], a[64:67], v[72:75], v[8:11]\n"
  "v_cvt_pk_bf16_f32 v26, v28, v29\n"
  "v_cvt_pk_bf16_f32 v27, v30, v31\n"
  "v_mfma_f32_16x16x32_bf16 v[8:11], a[68:71], v[76:79], v[8:11]\n"
  "ds_write_b128 v80, v[24:27] offset:7168\n"
  "v_pk_fma_f32 v[0:1], v[0:1], s[60:61], s[60:61] op_sel:[0,0,1] op_sel_hi:[1,0,1]\n"
  "v_pk_fma_f32 v[2:3], v[2:3], s[60:61], s[60:61] op_sel:[0,0,1] op_sel_hi:[1,0,1]\n"
  "v_pk_fma_f32 v[4:5], v[4:5], s[60:61], s[60:61] op_sel:[0,0,1] op_sel_hi:[1,0,1]\n"
  "v_pk_fma_f32 v[6:7], v[6:7], s[60:61], s[60:61] op_sel:[0,0,1] op_sel_hi:[1,0,1]\n"
  "v_mfma_f32_16x16x32_bf16 v[12:15], a[92:95], v[76:79], 0\n"
  "v_exp_f32 v0, v0\n"
  "v_mfma_f32_16x16x32_bf16 v[12:15], a[88:91], v[72:75], v[12:15]\n"
  "v_exp_f32 v1, v1\n"
  "v_mfma_f32_16x16x32_bf16 v[12:15], a[84:87], v[68:71], v[12:15]\n"
  "v_exp_f32 v2, v2\n"
  "v_mfma_f32_16x16x32_bf16 v[12:15], a[80:83], v[64:67], v[12:15]\n"
  "v_exp_f32 v3, v3\n"
  "v_mfma_f32_16x16x32_bf16 v[12:15], a[76:79], v[60:63], v[12:15]\n"
  "v_cvt_pk_bf16_f32 v0, v0, v1\n"
  "v_cvt_pk_bf16_f32 v1, v2, v3\n"
  "v_mfma_f32_16x16x32_bf16 v[12:15], a[72:75], v[56:59], v[12:15]\n"
  "s_waitcnt lgkmcnt(0)\n"
  "s_barrier\n"
  "v_xor_b32 v80, 0x4000, v80\n"
  "v_mfma_f32_16x16x32_bf16 v[16:19], a[96:99], v[56:59], 0\n"
  "v_exp_f32 v4, v4\n"
  "v_mfma_f32_16x16x32_bf16 v[16:19], a[100:103], v[60:63], v[16:19]\n"
  "v_exp_f32 v5, v5\n"
  "v_mfma_f32_16x16x32_bf16 v[16:19], a[104:107], v[64:67], v[16:19]\n"
  "v_exp_f32 v6, v6\n"
  "v_mfma_f32_16x16x32_bf16 v[16:19], a[108:111], v[68:71], v[16:19]\n"
  "v_exp_f32 v7, v7\n"
  "v_mfma_f32_16x16x32_bf16 v[16:19], a[112:115], v[72:75], v[16:19]\n"
  "v_cvt_pk_bf16_f32 v2, v4, v5\n"
  "v_cvt_pk_bf16_f32 v3, v6, v7\n"
  "v_mfma_f32_16x16x32_bf16 v[16:19], a[116:119], v[76:79], v[16:19]\n"
  "ds_write_b128 v80, v[0:3] offset:0\n"
  "v_pk_fma_f32 v[8:9], v[8:9], s[60:61], s[60:61] op_sel:[0,0,1] op_sel_hi:[1,0,1]\n"
  "v_pk_fma_f32 v[10:11], v[10:11], s[60:61], s[60:61] op_sel:[0,0,1] op_sel_hi:[1,0,1]\n"
  "v_pk_fma_f32 v[12:13], v[12:13], s[60:61], s[60:61] op_sel:[0,0,1] op_sel_hi:[1,0,1]\n"
  "v_pk_fma_f32 v[14:15], v[14:15], s[60:61], s[60:61] op_sel:[0,0,1] op_sel_hi:[1,0,1]\n"
  "v_mfma_f32_16x16x32_bf16 v[20:23], a[140:143], v[76:79], 0\n"
  "v_exp_f32 v8, v8\n"
  "v_mfma_f32_16x16x32_bf16 v[20:23], a[136:139], v[72:75], v[20:23]\n"
  "v_exp_f32 v9, v9\n"
  "v_mfma_f32_16x16x32_bf16 v[20:23], a[132:135], v[68:71], v[20:23]\n"
  "v_exp_f32 v10, v10\n"
  "v_mfma_f32_16x16x32_bf16 v[20:23], a[128:131], v[64:67], v[20:23]\n"
  "v_exp_f32 v11, v11\n"
  "v_mfma_f32_16x16x32_bf16 v[20:23], a[124:127], v[60:63], v[20:23]\n"
  "v_cvt_pk_bf16_f32 v8, v8, v9\n"
  "v_cvt_pk_bf16_f32 v9, v10, v11\n"
  "v_mfma_f32_16x16x32_bf16 v[20:23], a[120:123], v[56:59], v[20:23]\n"
  "ds_read_b128 v[32:35], v81 offset:0\n"
  "ds_read_b128 v[36:39], v82 offset:0\n"
  "v_mfma_f32_16x16x32_bf16 v[24:27], a[144:147], v[56:59], 0\n"
  "v_exp_f32 v12, v12\n"
  "v_mfma_f32_16x16x32_bf16 v[24:27], a[148:151], v[60:63], v[24:27]\n"
  "v_exp_f32 v13, v13\n"
  "v_mfma_f32_16x16x32_bf16 v[24:27], a[152:155], v[64:67], v[24:27]\n"
  "v_exp_f32 v14, v14\n"
  "v_mfma_f32_16x16x32_bf16 v[24:27], a[156:159], v[68:71], v[24:27]\n"
  "v_exp_f32 v15, v15\n"
  "v_mfma_f32_16x16x32_bf16 v[24:27], a[160:163], v[72:75], v[24:27]\n"
  "v_cvt_pk_bf16_f32 v10, v12, v13\n"
  "v_cvt_pk_bf16_f32 v11, v14, v15\n"
  "v_mfma_f32_16x16x32_bf16 v[24:27], a[164:167], v[76:79], v[24:27]\n"
  "ds_write_b128 v80, v[8:11] offset:1024\n"
  "v_pk_fma_f32 v[16:17], v[16:17], s[60:61], s[60:61] op_sel:[0,0,1] op_sel_hi:[1,0,1]\n"
  "v_pk_fma_f32 v[18:19], v[18:19], s[60:61], s[60:61] op_sel:[0,0,1] op_sel_hi:[1,0,1]\n"
  "v_pk_fma_f32 v[20:21], v[20:21], s[60:61], s[60:61] op_sel:[0,0,1] op_sel_hi:[1,0,1]\n"
  "v_pk_fma_f32 v[22:23], v[22:23], s[60:61], s[60:61] op_sel:[0,0,1] op_sel_hi:[1,0,1]\n"
  "v_mfma_f32_16x16x32_bf16 v[28:31], a[188:191], v[76:79], 0\n"
  "v_exp_f32 v16, v16\n"
  "v_mfma_f32_16x16x32_bf16 v[28:31], a[184:187], v[72:75], v[28:31]\n"
  "v_exp_f32 v17, v17\n"
  "v_mfma_f32_16x16x32_bf16 v[28:31], a[180:183], v[68:71], v[28:31]\n"
  "v_exp_f32 v18, v18\n"
  "v_mfma_f32_16x16x32_bf16 v[28:31], a[176:179], v[64:67], v[28:31]\n"
  "v_exp_f32 v19, v19\n"
  "v_mfma_f32_16x16x32_bf16 v[28:31], a[172:175], v[60:63], v[28:31]\n"
  "v_cvt_pk_bf16_f32 v16, v16, v17\n"
  "v_cvt_pk_bf16_f32 v17, v18, v19\n"
  "v_mfma_f32_16x16x32_bf16 v[28:31], a[168:171], v[56:59], v[28:31]\n"
  "ds_read_b128 v[40:43], v81 offset:2048\n"
  "ds_read_b128 v[44:47], v82 offset:2048\n"
  "v_mfma_f32_16x16x32_bf16 v[0:3], a[192:195], v[56:59], 0\n"
  "v_exp_f32 v20, v20\n"
  "v_mfma_f32_16x16x32_bf16 v[0:3], a[196:199], v[60:63], v[0:3]\n"
  "v_exp_f32 v21, v21\n"
  "v_mfma_f32_16x16x32_bf16 v[0:3], a[200:203], v[64:67], v[0:3]\n"
  "v_exp_f32 v22, v22\n"
  "v_mfma_f32_16x16x32_bf16 v[0:3], a[204:207], v[68:71], v[0:3]\n"
  "v_exp_f32 v23, v23\n"
  "v_mfma_f32_16x16x32_bf16 v[0:3], a[208:211], v[72:75], v[0:3]\n"
  "v_cvt_pk_bf16_f32 v18, v20, v21\n"
  "v_cvt_pk_bf16_f32 v19, v22, v23\n"
  "v_mfma_f32_16x16x32_bf16 v[0:3], a[212:215], v[76:79], v[0:3]\n"
  "ds_write_b128 v80, v[16:19] offset:2048\n"
  "v_pk_fma_f32 v[24:25], v[24:25], s[60:61], s[60:61] op_sel:[0,0,1] op_sel_hi:[1,0,1]\n"
  "v_pk_fma_f32 v[26:27], v[26:27], s[60:61], s[60:61] op_sel:[0,0,1] op_sel_hi:[1,0,1]\n"
  "v_pk_fma_f32 v[28:29], v[28:29], s[60:61], s[60:61] op_sel:[0,0,1] op_sel_hi:[1,0,1]\n"
  "v_pk_fma_f32 v[30:31], v[30:31], s[60:61], s[60:61] op_sel:[0,0,1] op_sel_hi:[1,0,1]\n"
  "v_mfma_f32_16x16x32_bf16 v[4:7], a[236:239], v[76:79], 0\n"
  "v_exp_f32 v24, v24\n"
  "v_mfma_f32_16x16x32_bf16 v[4:7], a[232:235], v[72:75], v[4:7]\n"
  "v_exp_f32 v25, v25\n"
  "v_mfma_f32_16x16x32_bf16 v[4:7], a[228:231], v[68:71], v[4:7]\n"
  "v_exp_f32 v26, v26\n"
  "v_mfma_f32_16x16x32_bf16 v[4:7], a[224:227], v[64:67], v[4:7]\n"
  "v_exp_f32 v27, v27\n"
  "v_mfma_f32_16x16x32_bf16 v[4:7], a[220:223], v[60:63], v[4:7]\n"
  "v_cvt_pk_bf16_f32 v24, v24, v25\n"
  "v_cvt_pk_bf16_f32 v25, v26, v27\n"
  "v_mfma_f32_16x16x32_bf16 v[4:7], a[216:219], v[56:59], v[4:7]\n"
  "ds_read_b128 v[48:51], v81 offset:4096\n"
  "ds_read_b128 v[52:55], v82 offset:4096\n"
  "v_mfma_f32_16x16x32_bf16 v[8:11], a[240:243], v[56:59], 0\n"
  "v_exp_f32 v28, v28\n"
  "v_mfma_f32_16x16x32_bf16 v[8:11], a[244:247], v[60:63], v[8:11]\n"
  "v_exp_f32 v29, v29\n"
  "v_mfma_f32_16x16x32_bf16 v[8:11], a[248:251], v[64:67], v[8:11]\n"
  "v_exp_f32 v30, v30\n"
  "v_mfma_f32_16x16x32_bf16 v[8:11], a[252:255], v[68:71], v[8:11]\n"
  "v_exp_f32 v31, v31\n"
  "v_mfma_f32_16x16x32_bf16 v[8:11], v[128:131], v[72:75], v[8:11]\n"
  "v_cvt_pk_bf16_f32 v26, v28, v29\n"
  "v_cvt_pk_bf16_f32 v27, v30, v31\n"
  "v_mfma_f32_16x16x32_bf16 v[8:11], v[132:135], v[76:79], v[8:11]\n"
  "ds_write_b128 v80, v[24:27] offset:3072\n"
  "v_pk_fma_f32 v[0:1], v[0:1], s[60:61], s[60:61] op_sel:[0,0,1] op_sel_hi:[1,0,1]\n"
  "v_pk_fma_f32 v[2:3], v[2:3], s[60:61], s[60:61] op_sel:[0,0,1] op_sel_hi:[1,0,1]\n"
  "v_pk_fma_f32 v[4:5], v[4:5], s[60:61], s[60:61] op_sel:[0,0,1] op_sel_hi:[1,0,1]\n"
  "v_pk_fma_f32 v[6:7], v[6:7], s[60:61], s[60:61] op_sel:[0,0,1] op_sel_hi:[1,0,1]\n"
  "v_mfma_f32_16x16x32_bf16 v[12:15], v[156:159], v[76:79], 0\n"
  "v_exp_f32 v0, v0\n"
  "v_mfma_f32_16x16x32_bf16 v[12:15], v[152:155], v[72:75], v[12:15]\n"
  "v_exp_f32 v1, v1\n"
  "v_mfma_f32_16x16x32_bf16 v[12:15], v[148:151], v[68:71], v[12:15]\n"
  "v_exp_f32 v2, v2\n"
  "v_mfma_f32_16x16x32_bf16 v[12:15], v[144:147], v[64:67], v[12:15]\n"
  "v_exp_f32 v3, v3\n"
  "v_mfma_f32_16x16x32_bf16 v[12:15], v[140:143], v[60:63], v[12:15]\n"
  "v_cvt_pk_bf16_f32 v0, v0, v1\n"
  "v_cvt_pk_bf16_f32 v1, v2, v3\n"
  "v_mfma_f32_16x16x32_bf16 v[12:15], v[136:139], v[56:59], v[12:15]\n"
  "v_mfma_f32_16x16x32_bf16 v[16:19], v[160:163], v[56:59], 0\n"
  "v_exp_f32 v4, v4\n"
  "v_mfma_f32_16x16x32_bf16 v[16:19], v[164:167], v[60:63], v[16:19]\n"
  "v_exp_f32 v5, v5\n"
  "v_mfma_f32_16x16x32_bf16 v[16:19], v[168:171], v[64:67], v[16:19]\n"
  "v_exp_f32 v6, v6\n"
  "v_mfma_f32_16x16x32_bf16 v[16:19], v[172:175], v[68:71], v[16:19]\n"
  "v_exp_f32 v7, v7\n"
  "v_mfma_f32_16x16x32_bf16 v[16:19], v[176:179], v[72:75], v[16:19]\n"
  "v_cvt_pk_bf16_f32 v2, v4, v5\n"
  "v_cvt_pk_bf16_f32 v3, v6, v7\n"
  "v_mfma_f32_16x16x32_bf16 v[16:19], v[180:183], v[76:79], v[16:19]\n"
  "ds_write_b128 v80, v[0:3] offset:4096\n"
  "v_pk_fma_f32 v[8:9], v[8:9], s[60:61], s[60:61] op_sel:[0,0,1] op_sel_hi:[1,0,1]\n"
  "v_pk_fma_f32 v[10:11], v[10:11], s[60:61], s[60:61] op_sel:[0,0,1] op_sel_hi:[1,0,1]\n"
  "v_pk_fma_f32 v[12:13], v[12:13], s[60:61], s[60:61] op_sel:[0,0,1] op_sel_hi:[1,0,1]\n"
  "v_pk_fma_f32 v[14:15], v[14:15], s[60:61], s[60:61] op_sel:[0,0,1] op_sel_hi:[1,0,1]\n"
  "v_mfma_f32_16x16x32_bf16 v[20:23], v[204:207], v[76:79], 0\n"
  "v_exp_f32 v8, v8\n"
  "v_mfma_f32_16x16x32_bf16 v[20:23], v[200:203], v[72:75], v[20:23]\n"
  "v_exp_f32 v9, v9\n"
  "v_mfma_f32_16x16x32_bf16 v[20:23], v[196:199], v[68:71], v[20:23]\n"
  "v_exp_f32 v10, v10\n"
  "v_mfma_f32_16x16x32_bf16 v[20:23], v[192:195], v[64:67], v[20:23]\n"
  "v_exp_f32 v11, v11\n"
  "v_mfma_f32_16x16x32_bf16 v[20:23], v[188:191], v[60:63], v[20:23]\n"
  "v_cvt_pk_bf16_f32 v8, v8, v9\n"
  "v_cvt_pk_bf16_f32 v9, v10, v11\n"
  "v_mfma_f32_16x16x32_bf16 v[20:23], v[184:187], v[56:59], v[20:23]\n"
  "v_mfma_f32_16x16x32_bf16 v[24:27], v[208:211], v[56:59], 0\n"
  "v_exp_f32 v12, v12\n"
  "v_mfma_f32_16x16x32_bf16 v[24:27], v[212:215], v[60:63], v[24:27]\n"
  "v_exp_f32 v13, v13\n"
  "v_mfma_f32_16x16x32_bf16 v[24:27], v[216:219], v[64:67], v[24:27]\n"
  "v_exp_f32 v14, v14\n"
  "v_mfma_f32_16x16x32_bf16 v[24:27], v[220:223], v[68:71], v[24:27]\n"
  "v_exp_f32 v15, v15\n"
  "v_mfma_f32_16x16x32_bf16 v[24:27], v[224:227], v[72:75], v[24:27]\n"
  "v_cvt_pk_bf16_f32 v10, v12, v13\n"
  "v_cvt_pk_bf16_f32 v11, v14, v15\n"
  "v_mfma_f32_16x16x32_bf16 v[24:27], v[228:231], v[76:79], v[24:27]\n"
  "ds_write_b128 v80, v[8:11] offset:5120\n"
  "v_pk_fma_f32 v[16:17], v[16:17], s[60:61], s[60:61] op_sel:[0,0,1] op_sel_hi:[1,0,1]\n"
  "v_pk_fma_f32 v[18:19], v[18:19], s[60:61], s[60:61] op_sel:[0,0,1] op_sel_hi:[1,0,1]\n"
  "v_pk_fma_f32 v[20:21], v[20:21], s[60:61], s[60:61] op_sel:[0,0,1] op_sel_hi:[1,0,1]\n"
  "v_pk_fma_f32 v[22:23], v[22:23], s[60:61], s[60:61] op_sel:[0,0,1] op_sel_hi:[1,0,1]\n"
  "v_mfma_f32_16x16x32_bf16 v[28:31], v[252:255], v[76:79], 0\n"
  "v_exp_f32 v16, v16\n"
  "v_mfma_f32_16x16x32_bf16 v[28:31], v[248:251], v[72:75], v[28:31]\n"
  "v_exp_f32 v17, v17\n"
  "v_mfma_f32_16x16x32_bf16 v[28:31], v[244:247], v[68:71], v[28:31]\n"
  "v_exp_f32 v18, v18\n"
  "v_mfma_f32_16x16x32_bf16 v[28:31], v[240:243], v[64:67], v[28:31]\n"
  "s_sub_u32 s56, s56, 1\n"
  "v_exp_f32 v19, v19\n"
  "v_mfma_f32_16x16x32_bf16 v[28:31], v[236:239], v[60:63], v[28:31]\n"
  "s_cmp_eq_u32 s56, 0\n"
  "v_cvt_pk_bf16_f32 v16, v16, v17\n"
  "v_cvt_pk_bf16_f32 v17, v18, v19\n"
  "v_mfma_f32_16x16x32_bf16 v[28:31], v[232:235], v[56:59], v[28:31]\n"
  "s_waitcnt lgkmcnt(3)\n"
  "s_cbranch_scc1 A_X0_%=\n"
  "A_H0_%=:\n"
  "v_mfma_f32_16x16x32_bf16 v[0:3], a[0:3], v[32:35], 0\n"
  "s_add_u32 s66, s66, 6144\n"
  "v_exp_f32 v20, v20\n"
  "v_mfma_f32_16x16x32_bf16 v[0:3], a[4:7], v[36:39], v[0:3]\n"
  "s_cmp_eq_u32 s66, 86016\n"
  "v_exp_f32 v21, v21\n"
  "v_mfma_f32_16x16x32_bf16 v[0:3], a[8:11], v[40:43], v[0:3]\n"
  "s_cselect_b32 s66, 49152, s66\n"
  "v_exp_f32 v22, v22\n"
  "v_mfma_f32_16x16x32_bf16 v[0:3], a[12:15], v[44:47], v[0:3]\n"
  "v_exp_f32 v23, v23\n"
  "v_mfma_f32_16x16x32_bf16 v[0:3], a[16:19], v[48:51], v[0:3]\n"
  "v_cvt_pk_bf16_f32 v18, v20, v21\n"
  "v_cvt_pk_bf16_f32 v19, v22, v23\n"
  "v_mfma_f32_16x16x32_bf16 v[0:3], a[20:23], v[52:55], v[0:3]\n"
  "ds_write_b128 v80, v[16:19] offset:6144\n"
  "v_pk_fma_f32 v[24:25], v[24:25], s[60:61], s[60:61] op_sel:[0,0,1] op_sel_hi:[1,0,1]\n"
  "v_pk_fma_f32 v[26:27], v[26:27], s[60:61], s[60:61] op_sel:[0,0,1] op_sel_hi:[1,0,1]\n"
  "v_pk_fma_f32 v[28:29], v[28:29], s[60:61], s[60:61] op_sel:[0,0,1] op_sel_hi:[1,0,1]\n"
  "v_pk_fma_f32 v[30:31], v[30:31], s[60:61], s[60:61] op_sel:[0,0,1] op_sel_hi:[1,0,1]\n"
  "v_mfma_f32_16x16x32_bf16 v[4:7], a[44:47], v[52:55], 0\n"
  "v_exp_f32 v24, v24\n"
  "v_mfma_f32_16x16x32_bf16 v[4:7], a[40:43], v[48:51], v[4:7]\n"
  "v_exp_f32 v25, v25\n"
  "v_mfma_f32_16x16x32_bf16 v[4:7], a[36:39], v[44:47], v[4:7]\n"
  "v_exp_f32 v26, v26\n"
  "v_mfma_f32_16x16x32_bf16 v[4:7], a[32:35], v[40:43], v[4:7]\n"
  "v_exp_f32 v27, v27\n"
  "v_mfma_f32_16x16x32_bf16 v[4:7], a[28:31], v[36:39], v[4:7]\n"
  "v_cvt_pk_bf16_f32 v24, v24, v25\n"
  "v_cvt_pk_bf16_f32 v25, v26, v27\n"
  "v_mfma_f32_16x16x32_bf16 v[4:7], a[24:27], v[32:35], v[4:7]\n"
  "v_add_u32 v81, s66, v83\n"
  "v_add_u32 v82, s66, v84\n"
  "v_mfma_f32_16x16x32_bf16 v[8:11], a[48:51], v[32:35], 0\n"
  "v_exp_f32 v28, v28\n"
  "v_mfma_f32_16x16x32_bf16 v[8:11], a[52:55], v[36:39], v[8:11]\n"
  "v_exp_f32 v29, v29\n"
  "v_mfma_f32_16x16x32_bf16 v[8:11], a[56:59], v[40:43], v[8:11]\n"
  "v_exp_f32 v30, v30\n"
  "v_mfma_f32_16x16x32_bf16 v[8:11], a[60:63], v[44:47], v[8:11]\n"
  "v_exp_f32 v31, v31\n"
  "v_mfma_f32_16x16x32_bf16 v[8:11], a[64:67], v[48:51], v[8:11]\n"
  "v_cvt_pk_bf16_f32 v26, v28, v29\n"
  "v_cvt_pk_bf16_f32 v27, v30, v31\n"
  "v_mfma_f32_16x16x32_bf16 v[8:11], a[68:71], v[52:55], v[8:11]\n"
  "ds_write_b128 v80, v[24:27] offset:7168\n"
  "v_pk_fma_f32 v[0:1], v[0:1], s[60:61], s[60:61] op_sel:[0,0,1] op_sel_hi:[1,0,1]\n"
  "v_pk_fma_f32 v[2:3], v[2:3], s[60:61], s[60:61] op_sel:[0,0,1] op_sel_hi:[1,0,1]\n"
  "v_pk_fma_f32 v[4:5], v[4:5], s[60:61], s[60:61] op_sel:[0,0,1] op_sel_hi:[1,0,1]\n"
  "v_pk_fma_f32 v[6:7], v[6:7], s[60:61], s[60:61] op_sel:[0,0,1] op_sel_hi:[1,0,1]\n"
  "v_mfma_f32_16x16x32_bf16 v[12:15], a[92:95], v[52:55], 0\n"
  "v_exp_f32 v0, v0\n"
  "v_mfma_f32_16x16x32_bf16 v[12:15], a[88:91], v[48:51], v[12:15]\n"
  "v_exp_f32 v1, v1\n"
  "v_mfma_f32_16x16x32_bf16 v[12:15], a[84:87], v[44:47], v[12:15]\n"
  "v_exp_f32 v2, v2\n"
  "v_mfma_f32_16x16x32_bf16 v[12:15], a[80:83], v[40:43], v[12:15]\n"
  "v_exp_f32 v3, v3\n"
  "v_mfma_f32_16x16x32_bf16 v[12:15], a[76:79], v[36:39], v[12:15]\n"
  "v_cvt_pk_bf16_f32 v0, v0, v1\n"
  "v_cvt_pk_bf16_f32 v1, v2, v3\n"
  "v_mfma_f32_16x16x32_bf16 v[12:15], a[72:75], v[32:35], v[12:15]\n"
  "s_waitcnt lgkmcnt(0)\n"
  "s_barrier\n"
  "v_xor_b32 v80, 0x4000, v80\n"
  "v_mfma_f32_16x16x32_bf16 v[16:19], a[96:99], v[32:35], 0\n"
  "v_exp_f32 v4, v4\n"
  "v_mfma_f32_16x16x32_bf16 v[16:19], a[100:103], v[36:39], v[16:19]\n"
  "v_exp_f32 v5, v5\n"
  "v_mfma_f32_16x16x32_bf16 v[16:19], a[104:107], v[40:43], v[16:19]\n"
  "v_exp_f32 v6, v6\n"
  "v_mfma_f32_16x16x32_bf16 v[16:19], a[108:111], v[44:47], v[16:19]\n"
  "v_exp_f32 v7, v7\n"
  "v_mfma_f32_16x16x32_bf16 v[16:19], a[112:115], v[48:51], v[16:19]\n"
  "v_cvt_pk_bf16_f32 v2, v4, v5\n"
  "v_cvt_pk_bf16_f32 v3, v6, v7\n"
  "v_mfma_f32_16x16x32_bf16 v[16:19], a[116:119], v[52:55], v[16:19]\n"
  "ds_write_b128 v80, v[0:3] offset:0\n"
  "v_pk_fma_f32 v[8:9], v[8:9], s[60:61], s[60:61] op_sel:[0,0,1] op_sel_hi:[1,0,1]\n"
  "v_pk_fma_f32 v[10:11], v[10:11], s[60:61], s[60:61] op_sel:[0,0,1] op_sel_hi:[1,0,1]\n"
  "v_pk_fma_f32 v[12:13], v[12:13], s[60:61], s[60:61] op_sel:[0,0,1] op_sel_hi:[1,0,1]\n"
  "v_pk_fma_f32 v[14:15], v[14:15], s[60:61], s[60:61] op_sel:[0,0,1] op_sel_hi:[1,0,1]\n"
  "v_mfma_f32_16x16x32_bf16 v[20:23], a[140:143], v[52:55], 0\n"
  "v_exp_f32 v8, v8\n"
  "v_mfma_f32_16x16x32_bf16 v[20:23], a[136:139], v[48:51], v[20:23]\n"
  "v_exp_f32 v9, v9\n"
  "v_mfma_f32_16x16x32_bf16 v[20:23], a[132:135], v[44:47], v[20:23]\n"
  "v_exp_f32 v10, v10\n"
  "v_mfma_f32_16x16x32_bf16 v[20:23], a[128:131], v[40:43], v[20:23]\n"
  "v_exp_f32 v11, v11\n"
  "v_mfma_f32_16x16x32_bf16 v[20:23], a[124:127], v[36:39], v[20:23]\n"
  "v_cvt_pk_bf16_f32 v8, v8, v9\n"
  "v_cvt_pk_bf16_f32 v9, v10, v11\n"
  "v_mfma_f32_16x16x32_bf16 v[20:23], a[120:123], v[32:35], v[20:23]\n"
  "ds_read_b128 v[56:59], v81 offset:0\n"
  "ds_read_b128 v[60:63], v82 offset:0\n"
  "v_mfma_f32_16x16x32_bf16 v[24:27], a[144:147], v[32:35], 0\n"
  "v_exp_f32 v12, v12\n"
  "v_mfma_f32_16x16x32_bf16 v[24:27], a[148:151], v[36:39], v[24:27]\n"
  "v_exp_f32 v13, v13\n"
  "v_mfma_f32_16x16x32_bf16 v[24:27], a[152:155], v[40:43], v[24:27]\n"
  "v_exp_f32 v14, v14\n"
  "v_mfma_f32_16x16x32_bf16 v[24:27], a[156:159], v[44:47], v[24:27]\n"
  "v_exp_f32 v15, v15\n"
  "v_mfma_f32_16x16x32_bf16 v[24:27], a[160:163], v[48:51], v[24:27]\n"
  "v_cvt_pk_bf16_f32 v10, v12, v13\n"
  "v_cvt_pk_bf16_f32 v11, v14, v15\n"
  "v_mfma_f32_16x16x32_bf16 v[24:27], a[164:167], v[52:55], v[24:27]\n"
  "ds_write_b128 v80, v[8:11] offset:1024\n"
  "v_pk_fma_f32 v[16:17], v[16:17], s[60:61], s[60:61] op_sel:[0,0,1] op_sel_hi:[1,0,1]\n"
  "v_pk_fma_f32 v[18:19], v[18:19], s[60:61], s[60:61] op_sel:[0,0,1] op_sel_hi:[1,0,1]\n"
  "v_pk_fma_f32 v[20:21], v[20:21], s[60:61], s[60:61] op_sel:[0,0,1] op_sel_hi:[1,0,1]\n"
  "v_pk_fma_f32 v[22:23], v[22:23], s[60:61], s[60:61] op_sel:[0,0,1] op_sel_hi:[1,0,1]\n"
  "v_mfma_f32_16x16x32_bf16 v[28:31], a[188:191], v[52:55], 0\n"
  "v_exp_f32 v16, v16\n"
  "v_mfma_f32_16x16x32_bf16 v[28:31], a[184:187], v[48:51], v[28:31]\n"
  "v_exp_f32 v17, v17\n"
  "v_mfma_f32_16x16x32_bf16 v[28:31], a[180:183], v[44:47], v[28:31]\n"
  "v_exp_f32 v18, v18\n"
  "v_mfma_f32_16x16x32_bf16 v[28:31], a[176:179], v[40:43], v[28:31]\n"
  "v_exp_f32 v19, v19\n"
  "v_mfma_f32_16x16x32_bf16 v[28:31], a[172:175], v[36:39], v[28:31]\n"
  "v_cvt_pk_bf16_f32 v16, v16, v17\n"
  "v_cvt_pk_bf16_f32 v17, v18, v19\n"
  "v_mfma_f32_16x16x32_bf16 v[28:31], a[168:171], v[32:35], v[28:31]\n"
  "ds_read_b128 v[64:67], v81 offset:2048\n"
  "ds_read_b128 v[68:71], v82 offset:2048\n"
  "v_mfma_f32_16x16x32_bf16 v[0:3], a[192:195], v[32:35], 0\n"
  "v_exp_f32 v20, v20\n"
  "v_mfma_f32_16x16x32_bf16 v[0:3], a[196:199], v[36:39], v[0:3]\n"
  "v_exp_f32 v21, v21\n"
  "v_mfma_f32_16x16x32_bf16 v[0:3], a[200:203], v[40:43], v[0:3]\n"
  "v_exp_f32 v22, v22\n"
  "v_mfma_f32_16x16x32_bf16 v[0:3], a[204:207], v[44:47], v[0:3]\n"
  "v_exp_f32 v23, v23\n"
  "v_mfma_f32_16x16x32_bf16 v[0:3], a[208:211], v[48:51], v[0:3]\n"
  "v_cvt_pk_bf16_f32 v18, v20, v21\n"
  "v_cvt_pk_bf16_f32 v19, v22, v23\n"
  "v_mfma_f32_16x16x32_bf16 v[0:3], a[212:215], v[52:55], v[0:3]\n"
  "ds_write_b128 v80, v[16:19] offset:2048\n"
  "v_pk_fma_f32 v[24:25], v[24:25], s[60:61], s[60:61] op_sel:[0,0,1] op_sel_hi:[1,0,1]\n"
  "v_pk_fma_f32 v[26:27], v[26:27], s[60:61], s[60:61] op_sel:[0,0,1] op_sel_hi:[1,0,1]\n"
  "v_pk_fma_f32 v[28:29], v[28:29], s[60:61], s[60:61] op_sel:[0,0,1] op_sel_hi:[1,0,1]\n"
  "v_pk_fma_f32 v[30:31], v[30:31], s[60:61], s[60:61] op_sel:[0,0,1] op_sel_hi:[1,0,1]\n"
  "v_mfma_f32_16x16x32_bf16 v[4:7], a[236:239], v[52:55], 0\n"
  "v_exp_f32 v24, v24\n"
  "v_mfma_f32_16x16x32_bf16 v[4:7], a[232:235], v[48:51], v[4:7]\n"
  "v_exp_f32 v25, v25\n"
  "v_mfma_f32_16x16x32_bf16 v[4:7], a[228:231], v[44:47], v[4:7]\n"
  "v_exp_f32 v26, v26\n"
  "v_mfma_f32_16x16x32_bf16 v[4:7], a[224:227], v[40:43], v[4:7]\n"
  "v_exp_f32 v27, v27\n"
  "v_mfma_f32_16x16x32_bf16 v[4:7], a[220:223], v[36:39], v[4:7]\n"
  "v_cvt_pk_bf16_f32 v24, v24, v25\n"
  "v_cvt_pk_bf16_f32 v25, v26, v27\n"
  "v_mfma_f32_16x16x32_bf16 v[4:7], a[216:219], v[32:35], v[4:7]\n"
  "ds_read_b128 v[72:75], v81 offset:4096\n"
  "ds_read_b128 v[76:79], v82 offset:4096\n"
  "v_mfma_f32_16x16x32_bf16 v[8:11], a[240:243], v[32:35], 0\n"
  "v_exp_f32 v28, v28\n"
  "v_mfma_f32_16x16x32_bf16 v[8:11], a[244:247], v[36:39], v[8:11]\n"
  "v_exp_f32 v29, v29\n"
  "v_mfma_f32_16x16x32_bf16 v[8:11], a[248:251], v[40:43], v[8:11]\n"
  "v_exp_f32 v30, v30\n"
  "v_mfma_f32_16x16x32_bf16 v[8:11], a[252:255], v[44:47], v[8:11]\n"
  "v_exp_f32 v31, v31\n"
  "v_mfma_f32_16x16x32_bf16 v[8:11], v[128:131], v[48:51], v[8:11]\n"
  "v_cvt_pk_bf16_f32 v26, v28, v29\n"
  "v_cvt_pk_bf16_f32 v27, v30, v31\n"
  "v_mfma_f32_16x16x32_bf16 v[8:11], v[132:135], v[52:55], v[8:11]\n"
  "ds_write_b128 v80, v[24:27] offset:3072\n"
  "v_pk_fma_f32 v[0:1], v[0:1], s[60:61], s[60:61] op_sel:[0,0,1] op_sel_hi:[1,0,1]\n"
  "v_pk_fma_f32 v[2:3], v[2:3], s[60:61], s[60:61] op_sel:[0,0,1] op_sel_hi:[1,0,1]\n"
  "v_pk_fma_f32 v[4:5], v[4:5], s[60:61], s[60:61] op_sel:[0,0,1] op_sel_hi:[1,0,1]\n"
  "v_pk_fma_f32 v[6:7], v[6:7], s[60:61], s[60:61] op_sel:[0,0,1] op_sel_hi:[1,0,1]\n"
  "v_mfma_f32_16x16x32_bf16 v[12:15], v[156:159], v[52:55], 0\n"
  "v_exp_f32 v0, v0\n"
  "v_mfma_f32_16x16x32_bf16 v[12:15], v[152:155], v[48:51], v[12:15]\n"
  "v_exp_f32 v1, v1\n"
  "v_mfma_f32_16x16x32_bf16 v[12:15], v[148:151], v[44:47], v[12:15]\n"
  "v_exp_f32 v2, v2\n"
  "v_mfma_f32_16x16x32_bf16 v[12:15], v[144:147], v[40:43], v[12:15]\n"
  "v_exp_f32 v3, v3\n"
  "v_mfma_f32_16x16x32_bf16 v[12:15], v[140:143], v[36:39], v[12:15]\n"
  "v_cvt_pk_bf16_f32 v0, v0, v1\n"
  "v_cvt_pk_bf16_f32 v1, v2, v3\n"
  "v_mfma_f32_16x16x32_bf16 v[12:15], v[136:139], v[32:35], v[12:15]\n"
  "v_mfma_f32_16x16x32_bf16 v[16:19], v[160:163], v[32:35], 0\n"
  "v_exp_f32 v4, v4\n"
  "v_mfma_f32_16x16x32_bf16 v[16:19], v[164:167], v[36:39], v[16:19]\n"
  "v_exp_f32 v5, v5\n"
  "v_mfma_f32_16x16x32_bf16 v[16:19], v[168:171], v[40:43], v[16:19]\n"
  "v_exp_f32 v6, v6\n"
  "v_mfma_f32_16x16x32_bf16 v[16:19], v[172:175], v[44:47], v[16:19]\n"
  "v_exp_f32 v7, v7\n"
  "v_mfma_f32_16x16x32_bf16 v[16:19], v[176:179], v[48:51], v[16:19]\n"
  "v_cvt_pk_bf16_f32 v2, v4, v5\n"
  "v_cvt_pk_bf16_f32 v3, v6, v7\n"
  "v_mfma_f32_16x16x32_bf16 v[16:19], v[180:183], v[52:55], v[16:19]\n"
  "ds_write_b128 v80, v[0:3] offset:4096\n"
  "v_pk_fma_f32 v[8:9], v[8:9], s[60:61], s[60:61] op_sel:[0,0,1] op_sel_hi:[1,0,1]\n"
  "v_pk_fma_f32 v[10:11], v[10:11], s[60:61], s[60:61] op_sel:[0,0,1] op_sel_hi:[1,0,1]\n"
  "v_pk_fma_f32 v[12:13], v[12:13], s[60:61], s[60:61] op_sel:[0,0,1] op_sel_hi:[1,0,1]\n"
  "v_pk_fma_f32 v[14:15], v[14:15], s[60:61], s[60:61] op_sel:[0,0,1] op_sel_hi:[1,0,1]\n"
  "v_mfma_f32_16x16x32_bf16 v[20:23], v[204:207], v[52:55], 0\n"
  "v_exp_f32 v8, v8\n"
  "v_mfma_f32_16x16x32_bf16 v[20:23], v[200:203], v[48:51], v[20:23]\n"
  "v_exp_f32 v9, v9\n"
  "v_mfma_f32_16x16x32_bf16 v[20:23], v[196:199], v[44:47], v[20:23]\n"
  "v_exp_f32 v10, v10\n"
  "v_mfma_f32_16x16x32_bf16 v[20:23], v[192:195], v[40:43], v[20:23]\n"
  "v_exp_f32 v11, v11\n"
  "v_mfma_f32_16x16x32_bf16 v[20:23], v[188:191], v[36:39], v[20:23]\n"
  "v_cvt_pk_bf16_f32 v8, v8, v9\n"
  "v_cvt_pk_bf16_f32 v9, v10, v11\n"
  "v_mfma_f32_16x16x32_bf16 v[20:23], v[184:187], v[32:35], v[20:23]\n"
  "v_mfma_f32_16x16x32_bf16 v[24:27], v[208:211], v[32:35], 0\n"
  "v_exp_f32 v12, v12\n"
  "v_mfma_f32_16x16x32_bf16 v[24:27], v[212:215], v[36:39], v[24:27]\n"
  "v_exp_f32 v13, v13\n"
  "v_mfma_f32_16x16x32_bf16 v[24:27], v[216:219], v[40:43], v[24:27]\n"
  "v_exp_f32 v14, v14\n"
  "v_mfma_f32_16x16x32_bf16 v[24:27], v[220:223], v[44:47], v[24:27]\n"
  "v_exp_f32 v15, v15\n"
  "v_mfma_f32_16x16x32_bf16 v[24:27], v[224:227], v[48:51], v[24:27]\n"
  "v_cvt_pk_bf16_f32 v10, v12, v13\n"
  "v_cvt_pk_bf16_f32 v11, v14, v15\n"
  "v_mfma_f32_16x16x32_bf16 v[24:27], v[228:231], v[52:55], v[24:27]\n"
  "ds_write_b128 v80, v[8:11] offset:5120\n"
  "v_pk_fma_f32 v[16:17], v[16:17], s[60:61], s[60:61] op_sel:[0,0,1] op_sel_hi:[1,0,1]\n"
  "v_pk_fma_f32 v[18:19], v[18:19], s[60:61], s[60:61] op_sel:[0,0,1] op_sel_hi:[1,0,1]\n"
  "v_pk_fma_f32 v[20:21], v[20:21], s[60:61], s[60:61] op_sel:[0,0,1] op_sel_hi:[1,0,1]\n"
  "v_pk_fma_f32 v[22:23], v[22:23], s[60:61], s[60:61] op_sel:[0,0,1] op_sel_hi:[1,0,1]\n"
  "v_mfma_f32_16x16x32_bf16 v[28:31], v[252:255], v[52:55], 0\n"
  "v_exp_f32 v16, v16\n"
  "v_mfma_f32_16x16x32_bf16 v[28:31], v[248:251], v[48:51], v[28:31]\n"
  "v_exp_f32 v17, v17\n"
  "v_mfma_f32_16x16x32_bf16 v[28:31], v[244:247], v[44:47], v[28:31]\n"
  "v_exp_f32 v18, v18\n"
  "v_mfma_f32_16x16x32_bf16 v[28:31], v[240:243], v[40:43], v[28:31]\n"
  "s_sub_u32 s56, s56, 1\n"
  "v_exp_f32 v19, v19\n"
  "v_mfma_f32_16x16x32_bf16 v[28:31], v[236:239], v[36:39], v[28:31]\n"
  "s_cmp_eq_u32 s56, 0\n"
  "v_cvt_pk_bf16_f32 v16, v16, v17\n"
  "v_cvt_pk_bf16_f32 v17, v18, v19\n"
  "v_mfma_f32_16x16x32_bf16 v[28:31], v[232:235], v[32:35], v[28:31]\n"
  "s_waitcnt lgkmcnt(3)\n"
  "s_cbranch_scc0 A_H1_%=\n"
  "A_X1_%=:\n"
  "s_mov_b32 s52, s53\n"
  "s_mov_b32 s53, s54\n"
  "s_mov_b32 s54, s55\n"
  "s_cmp_eq_u32 s52, 0\n"
  "s_cbranch_scc1 A_LAST_%=\n"
  "s_lshr_b32 s62, s52, 16\n"
  "s_lshl1_add_u32 s62, s62, s65\n"
  "s_mul_i32 s57, s62, 98304\n"
  "s_and_b32 s56, s52, 0xff\n"
  "s_cmp_lg_u32 s53, 0\n"
  "s_cselect_b32 s62, 1, 0\n"
  "s_sub_u32 s56, s56, s62\n"
  "s_add_u32 s57, s57, 18432\n"
  "v_mfma_f32_16x16x32_bf16 v[0:3], a[0:3], v[56:59], 0\n"
  "s_add_u32 s66, s66, 6144\n"
  "s_cmp_eq_u32 s66, 86016\n"
  "s_cselect_b32 s66, 49152, s66\n"
  "v_exp_f32 v20, v20\n"
  "v_mfma_f32_16x16x32_bf16 v[0:3], a[4:7], v[60:63], v[0:3]\n"
  "v_exp_f32 v21, v21\n"
  "v_mfma_f32_16x16x32_bf16 v[0:3], a[8:11], v[64:67], v[0:3]\n"
  "ds_read_b128 a[0:3], v87 offset:0\n"
  "ds_read_b128 a[4:7], v88 offset:0\n"
  "v_exp_f32 v22, v22\n"
  "v_mfma_f32_16x16x32_bf16 v[0:3], a[12:15], v[68:71], v[0:3]\n"
  "v_exp_f32 v23, v23\n"
  "v_mfma_f32_16x16x32_bf16 v[0:3], a[16:19], v[72:75], v[0:3]\n"
  "v_cvt_pk_bf16_f32 v18, v20, v21\n"
  "v_cvt_pk_bf16_f32 v19, v22, v23\n"
  "v_mfma_f32_16x16x32_bf16 v[0:3], a[20:23], v[76:79], v[0:3]\n"
  "ds_read_b128 a[8:11], v87 offset:2048\n"
  "ds_read_b128 a[12:15], v88 offset:2048\n"
  "ds_write_b128 v80, v[16:19] offset:6144\n"
  "v_pk_fma_f32 v[24:25], v[24:25], s[60:61], s[60:61] op_sel:[0,0,1] op_sel_hi:[1,0,1]\n"
  "v_pk_fma_f32 v[26:27], v[26:27], s[60:61], s[60:61] op_sel:[0,0,1] op_sel_hi:[1,0,1]\n"
  "v_pk_fma_f32 v[28:29], v[28:29], s[60:61], s[60:61] op_sel:[0,0,1] op_sel_hi:[1,0,1]\n"
  "v_pk_fma_f32 v[30:31], v[30:31], s[60:61], s[60:61] op_sel:[0,0,1] op_sel_hi:[1,0,1]\n"
  "v_mfma_f32_16x16x32_bf16 v[4:7], a[44:47], v[76:79], 0\n"
  "v_exp_f32 v24, v24\n"
  "v_mfma_f32_16x16x32_bf16 v[4:7], a[40:43], v[72:75], v[4:7]\n"
  "s_add_u32 m0, s59, -256\n"
  "v_exp_f32 v25, v25\n"
  "v_mfma_f32_16x16x32_bf16 v[4:7], a[36:39], v[68:71], v[4:7]\n"
  "s_waitcnt lgkmcnt(3)\n"
  "buffer_load_dwordx4 v101, s[40:43], s57 offen offset:256 nt lds\n"
  "buffer_load_dwordx4 v102, s[40:43], s57 offen offset:1280 nt lds\n"
  "v_exp_f32 v26, v26\n"
  "v_mfma_f32_16x16x32_bf16 v[4:7], a[32:35], v[64:67], v[4:7]\n"
  "ds_read_b128 a[16:19], v87 offset:4096\n"
  "ds_read_b128 a[20:23], v88 offset:4096\n"
  "v_exp_f32 v27, v27\n"
  "v_mfma_f32_16x16x32_bf16 v[4:7], a[28:31], v[60:63], v[4:7]\n"
  "s_add_u32 m0, s59, 1920\n"
  "v_cvt_pk_bf16_f32 v24, v24, v25\n"
  "v_cvt_pk_bf16_f32 v25, v26, v27\n"
  "v_mfma_f32_16x16x32_bf16 v[4:7], a[24:27], v[56:59], v[4:7]\n"
  "s_waitcnt lgkmcnt(3)\n"
  "buffer_load_dwordx4 v101, s[40:43], s57 offen offset:128 nt lds\n"
  "buffer_load_dwordx4 v102, s[40:43], s57 offen offset:1152 nt lds\n"
  "v_add_u32 v81, s66, v83\n"
  "v_add_u32 v82, s66, v84\n"
  "v_mfma_f32_16x16x32_bf16 v[8:11], a[48:51], v[56:59], 0\n"
  "ds_read_b128 a[40:43], v87 offset:6144\n"
  "ds_read_b128 a[44:47], v88 offset:6144\n"
  "v_exp_f32 v28, v28\n"
  "v_mfma_f32_16x16x32_bf16 v[8:11], a[52:55], v[60:63], v[8:11]\n"
  "s_add_u32 m0, s59, 4096\n"
  "v_exp_f32 v29, v29\n"
  "v_mfma_f32_16x16x32_bf16 v[8:11], a[56:59], v[64:67], v[8:11]\n"
  "s_waitcnt lgkmcnt(2)\n"
  "buffer_load_dwordx4 v101, s[40:43], s57 offen offset:0 nt lds\n"
  "buffer_load_dwordx4 v102, s[40:43], s57 offen offset:1024 nt lds\n"
  "s_add_u32 s57, s57, 6144\n"
  "v_exp_f32 v30, v30\n"
  "v_mfma_f32_16x16x32_bf16 v[8:11], a[60:63], v[68:71], v[8:11]\n"
  "ds_read_b128 a[32:35], v87 offset:8192\n"
  "ds_read_b128 a[36:39], v88 offset:8192\n"
  "v_exp_f32 v31, v31\n"
  "v_mfma_f32_16x16x32_bf16 v[8:11], a[64:67], v[72:75], v[8:11]\n"
  "s_add_u32 m0, s59, 6144\n"
  "v_cvt_pk_bf16_f32 v26, v28, v29\n"
  "v_cvt_pk_bf16_f32 v27, v30, v31\n"
  "v_mfma_f32_16x16x32_bf16 v[8:11], a[68:71], v[76:79], v[8:11]\n"
  "s_waitcnt lgkmcnt(2)\n"
  "buffer_load_dwordx4 v101, s[40:43], s57 offen offset:0 nt lds\n"
  "buffer_load_dwordx4 v102, s[40:43], s57 offen offset:1024 nt lds\n"
  "ds_write_b128 v80, v[24:27] offset:7168\n"
  "v_pk_fma_f32 v[0:1], v[0:1], s[60:61], s[60:61] op_sel:[0,0,1] op_sel_hi:[1,0,1]\n"
  "v_pk_fma_f32 v[2:3], v[2:3], s[60:61], s[60:61] op_sel:[0,0,1] op_sel_hi:[1,0,1]\n"
  "v_pk_fma_f32 v[4:5], v[4:5], s[60:61], s[60:61] op_sel:[0,0,1] op_sel_hi:[1,0,1]\n"
  "v_pk_fma_f32 v[6:7], v[6:7], s[60:61], s[60:61] op_sel:[0,0,1] op_sel_hi:[1,0,1]\n"
  "v_mfma_f32_16x16x32_bf16 v[12:15], a[92:95], v[76:79], 0\n"
  "ds_read_b128 a[24:27], v87 offset:10240\n"
  "ds_read_b128 a[28:31], v88 offset:10240\n"
  "v_exp_f32 v0, v0\n"
  "v_mfma_f32_16x16x32_bf16 v[12:15], a[88:91], v[72:75], v[12:15]\n"
  "s_add_u32 m0, s59, 8064\n"
  "v_exp_f32 v1, v1\n"
  "v_mfma_f32_16x16x32_bf16 v[12:15], a[84:87], v[68:71], v[12:15]\n"
  "s_waitcnt lgkmcnt(3)\n"
  "buffer_load_dwordx4 v101, s[40:43], s57 offen offset:128 nt lds\n"
  "buffer_load_dwordx4 v102, s[40:43], s57 offen offset:1152 nt lds\n"
  "v_exp_f32 v2, v2\n"
  "v_mfma_f32_16x16x32_bf16 v[12:15], a[80:83], v[64:67], v[12:15]\n"
  "ds_read_b128 a[48:51], v87 offset:12288\n"
  "ds_read_b128 a[52:55], v88 offset:12288\n"
  "v_exp_f32 v3, v3\n"
  "v_mfma_f32_16x16x32_bf16 v[12:15], a[76:79], v[60:63], v[12:15]\n"
  "v_cvt_pk_bf16_f32 v0, v0, v1\n"
  "v_cvt_pk_bf16_f32 v1, v2, v3\n"
  "v_mfma_f32_16x16x32_bf16 v[12:15], a[72:75], v[56:59], v[12:15]\n"
  "s_add_u32 m0, s59, 9984\n"
  "s_waitcnt lgkmcnt(4)\n"
  "s_barrier\n"
  "v_xor_b32 v80, 0x4000, v80\n"
  "v_mfma_f32_16x16x32_bf16 v[16:19], a[96:99], v[56:59], 0\n"
  "s_waitcnt lgkmcnt(2)\n"
  "buffer_load_dwordx4 v101, s[40:43], s57 offen offset:256 nt lds\n"
  "buffer_load_dwordx4 v102, s[40:43], s57 offen offset:1280 nt lds\n"
  "s_add_u32 s57, s57, 6144\n"
  "v_exp_f32 v4, v4\n"
  "v_mfma_f32_16x16x32_bf16 v[16:19], a[100:103], v[60:63], v[16:19]\n"
  "ds_read_b128 a[56:59], v87 offset:14336\n"
  "ds_read_b128 a[60:63], v88 offset:14336\n"
  "v_exp_f32 v5, v5\n"
  "v_mfma_f32_16x16x32_bf16 v[16:19], a[104:107], v[64:67], v[16:19]\n"
  "s_add_u32 m0, s59, 12032\n"
  "v_exp_f32 v6, v6\n"
  "v_mfma_f32_16x16x32_bf16 v[16:19], a[108:111], v[68:71], v[16:19]\n"
  "s_waitcnt lgkmcnt(2)\n"
  "buffer_load_dwordx4 v101, s[40:43], s57 offen offset:256 nt lds\n"
  "buffer_load_dwordx4 v102, s[40:43], s57 offen offset:1280 nt lds\n"
  "v_exp_f32 v7, v7\n"
  "v_mfma_f32_16x16x32_bf16 v[16:19], a[112:115], v[72:75], v[16:19]\n"
  "ds_read_b128 a[64:67], v87 offset:16384\n"
  "ds_read_b128 a[68:71], v88 offset:16384\n"
  "v_cvt_pk_bf16_f32 v2, v4, v5\n"
  "v_cvt_pk_bf16_f32 v3, v6, v7\n"
  "v_mfma_f32_16x16x32_bf16 v[16:19], a[116:119], v[76:79], v[16:19]\n"
  "s_add_u32 m0, s59, 14208\n"
  "ds_write_b128 v80, v[0:3] offset:0\n"
  "v_pk_fma_f32 v[8:9], v[8:9], s[60:61], s[60:61] op_sel:[0,0,1] op_sel_hi:[1,0,1]\n"
  "v_pk_fma_f32 v[10:11], v[10:11], s[60:61], s[60:61] op_sel:[0,0,1] op_sel_hi:[1,0,1]\n"
  "v_pk_fma_f32 v[12:13], v[12:13], s[60:61], s[60:61] op_sel:[0,0,1] op_sel_hi:[1,0,1]\n"
  "v_pk_fma_f32 v[14:15], v[14:15], s[60:61], s[60:61] op_sel:[0,0,1] op_sel_hi:[1,0,1]\n"
  "v_mfma_f32_16x16x32_bf16 v[20:23], a[140:143], v[76:79], 0\n"
  "s_waitcnt lgkmcnt(3)\n"
  "buffer_load_dwordx4 v101, s[40:43], s57 offen offset:128 nt lds\n"
  "buffer_load_dwordx4 v102, s[40:43], s57 offen offset:1152 nt lds\n"
  "v_exp_f32 v8, v8\n"
  "v_mfma_f32_16x16x32_bf16 v[20:23], a[136:139], v[72:75], v[20:23]\n"
  "s_waitcnt vmcnt(14)\n"
  "ds_read_b128 a[88:91], v87 offset:0\n"
  "ds_read_b128 a[92:95], v88 offset:0\n"
  "v_exp_f32 v9, v9\n"
  "v_mfma_f32_16x16x32_bf16 v[20:23], a[132:135], v[68:71], v[20:23]\n"
  "s_add_u32 m0, s59, 16384\n"
  "v_exp_f32 v10, v10\n"
  "v_mfma_f32_16x16x32_bf16 v[20:23], a[128:131], v[64:67], v[20:23]\n"
  "s_waitcnt lgkmcnt(3)\n"
  "buffer_load_dwordx4 v101, s[40:43], s57 offen offset:0 nt lds\n"
  "buffer_load_dwordx4 v102, s[40:43], s57 offen offset:1024 nt lds\n"
  "s_add_u32 s57, s57, 6144\n"
  "v_exp_f32 v11, v11\n"
  "v_mfma_f32_16x16x32_bf16 v[20:23], a[124:127], v[60:63], v[20:23]\n"
  "s_waitcnt vmcnt(14)\n"
  "ds_read_b128 a[80:83], v87 offset:2048\n"
  "ds_read_b128 a[84:87], v88 offset:2048\n"
  "v_cvt_pk_bf16_f32 v8, v8, v9\n"
  "v_cvt_pk_bf16_f32 v9, v10, v11\n"
  "v_mfma_f32_16x16x32_bf16 v[20:23], a[120:123], v[56:59], v[20:23]\n"
  "s_add_u32 m0, s59, 0\n"
  "ds_read_b128 v[32:35], v81 offset:0\n"
  "ds_read_b128 v[36:39], v82 offset:0\n"
  "v_mfma_f32_16x16x32_bf16 v[24:27], a[144:147], v[56:59], 0\n"
  "s_waitcnt lgkmcnt(4)\n"
  "buffer_load_dwordx4 v101, s[40:43], s57 offen offset:0 nt lds\n"
  "buffer_load_dwordx4 v102, s[40:43], s57 offen offset:1024 nt lds\n"
  "v_exp_f32 v12, v12\n"
  "v_mfma_f32_16x16x32_bf16 v[24:27], a[148:151], v[60:63], v[24:27]\n"
  "s_waitcnt vmcnt(14)\n"
  "ds_read_b128 a[72:75], v87 offset:4096\n"
  "ds_read_b128 a[76:79], v88 offset:4096\n"
  "v_exp_f32 v13, v13\n"
  "v_mfma_f32_16x16x32_bf16 v[24:27], a[152:155], v[64:67], v[24:27]\n"
  "v_exp_f32 v14, v14\n"
  "v_mfma_f32_16x16x32_bf16 v[24:27], a[156:159], v[68:71], v[24:27]\n"
  "s_add_u32 m0, s59, 1920\n"
  "v_exp_f32 v15, v15\n"
  "v_mfma_f32_16x16x32_bf16 v[24:27], a[160:163], v[72:75], v[24:27]\n"
  "s_waitcnt lgkmcnt(4)\n"
  "buffer_load_dwordx4 v101, s[40:43], s57 offen offset:128 nt lds\n"
  "buffer_load_dwordx4 v102, s[40:43], s57 offen offset:1152 nt lds\n"
  "v_cvt_pk_bf16_f32 v10, v12, v13\n"
  "v_cvt_pk_bf16_f32 v11, v14, v15\n"
  "v_mfma_f32_16x16x32_bf16 v[24:27], a[164:167], v[76:79], v[24:27]\n"
  "s_waitcnt vmcnt(14)\n"
  "ds_read_b128 a[96:99], v87 offset:6144\n"
  "ds_read_b128 a[100:103], v88 offset:6144\n"
  "ds_write_b128 v80, v[8:11] offset:1024\n"
  "v_pk_fma_f32 v[16:17], v[16:17], s[60:61], s[60:61] op_sel:[0,0,1] op_sel_hi:[1,0,1]\n"
  "v_pk_fma_f32 v[18:19], v[18:19], s[60:61], s[60:61] op_sel:[0,0,1] op_sel_hi:[1,0,1]\n"
  "v_pk_fma_f32 v[20:21], v[20:21], s[60:61], s[60:61] op_sel:[0,0,1] op_sel_hi:[1,0,1]\n"
  "v_pk_fma_f32 v[22:23], v[22:23], s[60:61], s[60:61] op_sel:[0,0,1] op_sel_hi:[1,0,1]\n"
  "v_mfma_f32_16x16x32_bf16 v[28:31], a[188:191], v[76:79], 0\n"
  "s_add_u32 m0, s59, 3840\n"
  "v_exp_f32 v16, v16\n"
  "v_mfma_f32_16x16x32_bf16 v[28:31], a[184:187], v[72:75], v[28:31]\n"
  "s_waitcnt lgkmcnt(3)\n"
  "buffer_load_dwordx4 v101, s[40:43], s57 offen offset:256 nt lds\n"
  "buffer_load_dwordx4 v102, s[40:43], s57 offen offset:1280 nt lds\n"
  "s_add_u32 s57, s57, 6144\n"
  "v_exp_f32 v17, v17\n"
  "v_mfma_f32_16x16x32_bf16 v[28:31], a[180:183], v[68:71], v[28:31]\n"
  "s_waitcnt vmcnt(14)\n"
  "ds_read_b128 a[104:107], v87 offset:8192\n"
  "ds_read_b128 a[108:111], v88 offset:8192\n"
  "v_exp_f32 v18, v18\n"
  "v_mfma_f32_16x16x32_bf16 v[28:31], a[176:179], v[64:67], v[28:31]\n"
  "s_add_u32 m0, s59, 5888\n"
  "v_exp_f32 v19, v19\n"
  "v_mfma_f32_16x16x32_bf16 v[28:31], a[172:175], v[60:63], v[28:31]\n"
  "s_waitcnt lgkmcnt(3)\n"
  "buffer_load_dwordx4 v101, s[40:43], s57 offen offset:256 nt lds\n"
  "buffer_load_dwordx4 v102, s[40:43], s57 offen offset:1280 nt lds\n"
  "v_cvt_pk_bf16_f32 v16, v16, v17\n"
  "v_cvt_pk_bf16_f32 v17, v18, v19\n"
  "v_mfma_f32_16x16x32_bf16 v[28:31], a[168:171], v[56:59], v[28:31]\n"
  "s_waitcnt vmcnt(14)\n"
  "ds_read_b128 a[112:115], v87 offset:10240\n"
  "ds_read_b128 a[116:119], v88 offset:10240\n"
  "ds_read_b128 v[40:43], v81 offset:2048\n"
  "ds_read_b128 v[44:47], v82 offset:2048\n"
  "v_mfma_f32_16x16x32_bf16 v[0:3], a[192:195], v[56:59], 0\n"
  "s_add_u32 m0, s59, 8064\n"
  "v_exp_f32 v20, v20\n"
  "v_mfma_f32_16x16x32_bf16 v[0:3], a[196:199], v[60:63], v[0:3]\n"
  "s_waitcnt lgkmcnt(4)\n"
  "buffer_load_dwordx4 v101, s[40:43], s57 offen offset:128 nt lds\n"
  "buffer_load_dwordx4 v102, s[40:43], s57 offen offset:1152 nt lds\n"
  "v_exp_f32 v21, v21\n"
  "v_mfma_f32_16x16x32_bf16 v[0:3], a[200:203], v[64:67], v[0:3]\n"
  "s_waitcnt vmcnt(14)\n"
  "ds_read_b128 a[136:139], v87 offset:12288\n"
  "ds_read_b128 a[140:143], v88 offset:12288\n"
  "v_exp_f32 v22, v22\n"
  "v_mfma_f32_16x16x32_bf16 v[0:3], a[204:207], v[68:71], v[0:3]\n"
  "s_add_u32 m0, s59, 10240\n"
  "v_exp_f32 v23, v23\n"
  "v_mfma_f32_16x16x32_bf16 v[0:3], a[208:211], v[72:75], v[0:3]\n"
  "s_waitcnt lgkmcnt(4)\n"
  "buffer_load_dwordx4 v101, s[40:43], s57 offen offset:0 nt lds\n"
  "buffer_load_dwordx4 v102, s[40:43], s57 offen offset:1024 nt lds\n"
  "s_add_u32 s57, s57, 6144\n"
  "v_cvt_pk_bf16_f32 v18, v20, v21\n"
  "v_cvt_pk_bf16_f32 v19, v22, v23\n"
  "v_mfma_f32_16x16x32_bf16 v[0:3], a[212:215], v[76:79], v[0:3]\n"
  "s_waitcnt vmcnt(14)\n"
  "ds_read_b128 a[128:131], v87 offset:14336\n"
  "ds_read_b128 a[132:135], v88 offset:14336\n"
  "ds_write_b128 v80, v[16:19] offset:2048\n"
  "v_pk_fma_f32 v[24:25], v[24:25], s[60:61], s[60:61] op_sel:[0,0,1] op_sel_hi:[1,0,1]\n"
  "v_pk_fma_f32 v[26:27], v[26:27], s[60:61], s[60:61] op_sel:[0,0,1] op_sel_hi:[1,0,1]\n"
  "v_pk_fma_f32 v[28:29], v[28:29], s[60:61], s[60:61] op_sel:[0,0,1] op_sel_hi:[1,0,1]\n"
  "v_pk_fma_f32 v[30:31], v[30:31], s[60:61], s[60:61] op_sel:[0,0,1] op_sel_hi:[1,0,1]\n"
  "v_mfma_f32_16x16x32_bf16 v[4:7], a[236:239], v[76:79], 0\n"
  "v_exp_f32 v24, v24\n"
  "v_mfma_f32_16x16x32_bf16 v[4:7], a[232:235], v[72:75], v[4:7]\n"
  "s_add_u32 m0, s59, 12288\n"
  "v_exp_f32 v25, v25\n"
  "v_mfma_f32_16x16x32_bf16 v[4:7], a[228:231], v[68:71], v[4:7]\n"
  "s_waitcnt lgkmcnt(3)\n"
  "buffer_load_dwordx4 v101, s[40:43], s57 offen offset:0 nt lds\n"
  "buffer_load_dwordx4 v102, s[40:43], s57 offen offset:1024 nt lds\n"
  "v_exp_f32 v26, v26\n"
  "v_mfma_f32_16x16x32_bf16 v[4:7], a[224:227], v[64:67], v[4:7]\n"
  "s_waitcnt vmcnt(14)\n"
  "ds_read_b128 a[120:123], v87 offset:16384\n"
  "ds_read_b128 a[124:127], v88 offset:16384\n"
  "v_exp_f32 v27, v27\n"
  "v_mfma_f32_16x16x32_bf16 v[4:7], a[220:223], v[60:63], v[4:7]\n"
  "s_add_u32 m0, s59, 14208\n"
  "v_cvt_pk_bf16_f32 v24, v24, v25\n"
  "v_cvt_pk_bf16_f32 v25, v26, v27\n"
  "v_mfma_f32_16x16x32_bf16 v[4:7], a[216:219], v[56:59], v[4:7]\n"
  "s_waitcnt lgkmcnt(3)\n"
  "buffer_load_dwordx4 v101, s[40:43], s57 offen offset:128 nt lds\n"
  "buffer_load_dwordx4 v102, s[40:43], s57 offen offset:1152 nt lds\n"
  "ds_read_b128 v[48:51], v81 offset:4096\n"
  "ds_read_b128 v[52:55], v82 offset:4096\n"
  "v_mfma_f32_16x16x32_bf16 v[8:11], a[240:243], v[56:59], 0\n"
  "s_waitcnt vmcnt(14)\n"
  "ds_read_b128 a[144:147], v87 offset:0\n"
  "ds_read_b128 a[148:151], v88 offset:0\n"
  "v_exp_f32 v28, v28\n"
  "v_mfma_f32_16x16x32_bf16 v[8:11], a[244:247], v[60:63], v[8:11]\n"
  "s_add_u32 m0, s59, 16128\n"
  "v_exp_f32 v29, v29\n"
  "v_mfma_f32_16x16x32_bf16 v[8:11], a[248:251], v[64:67], v[8:11]\n"
  "s_waitcnt lgkmcnt(4)\n"
  "buffer_load_dwordx4 v101, s[40:43], s57 offen offset:256 nt lds\n"
  "buffer_load_dwordx4 v102, s[40:43], s57 offen offset:1280 nt lds\n"
  "s_add_u32 s57, s57, 6144\n"
  "v_exp_f32 v30, v30\n"
  "v_mfma_f32_16x16x32_bf16 v[8:11], a[252:255], v[68:71], v[8:11]\n"
  "s_waitcnt vmcnt(14)\n"
  "ds_read_b128 a[152:155], v87 offset:2048\n"
  "ds_read_b128 a[156:159], v88 offset:2048\n"
  "v_exp_f32 v31, v31\n"
  "v_mfma_f32_16x16x32_bf16 v[8:11], v[128:131], v[72:75], v[8:11]\n"
  "s_add_u32 m0, s59, -256\n"
  "v_cvt_pk_bf16_f32 v26, v28, v29\n"
  "v_cvt_pk_bf16_f32 v27, v30, v31\n"
  "v_mfma_f32_16x16x32_bf16 v[8:11], v[132:135], v[76:79], v[8:11]\n"
  "s_waitcnt lgkmcnt(2)\n"
  "buffer_load_dwordx4 v101, s[40:43], s57 offen offset:256 nt lds\n"
  "buffer_load_dwordx4 v102, s[40:43], s57 offen offset:1280 nt lds\n"
  "ds_write_b128 v80, v[24:27] offset:3072\n"
  "v_pk_fma_f32 v[0:1], v[0:1], s[60:61], s[60:61] op_sel:[0,0,1] op_sel_hi:[1,0,1]\n"
  "v_pk_fma_f32 v[2:3], v[2:3], s[60:61], s[60:61] op_sel:[0,0,1] op_sel_hi:[1,0,1]\n"
  "v_pk_fma_f32 v[4:5], v[4:5], s[60:61], s[60:61] op_sel:[0,0,1] op_sel_hi:[1,0,1]\n"
  "v_pk_fma_f32 v[6:7], v[6:7], s[60:61], s[60:61] op_sel:[0,0,1] op_sel_hi:[1,0,1]\n"
  "v_mfma_f32_16x16x32_bf16 v[12:15], v[156:159], v[76:79], 0\n"
  "s_waitcnt vmcnt(14)\n"
  "ds_read_b128 a[160:163], v87 offset:4096\n"
  "ds_read_b128 a[164:167], v88 offset:4096\n"
  "v_exp_f32 v0, v0\n"
  "v_mfma_f32_16x16x32_bf16 v[12:15], v[152:155], v[72:75], v[12:15]\n"
  "s_add_u32 m0, s59, 1920\n"
  "v_exp_f32 v1, v1\n"
  "v_mfma_f32_16x16x32_bf16 v[12:15], v[148:151], v[68:71], v[12:15]\n"
  "s_waitcnt lgkmcnt(3)\n"
  "buffer_load_dwordx4 v101, s[40:43], s57 offen offset:128 nt lds\n"
  "buffer_load_dwordx4 v102, s[40:43], s57 offen offset:1152 nt lds\n"
  "v_exp_f32 v2, v2\n"
  "v_mfma_f32_16x16x32_bf16 v[12:15], v[144:147], v[64:67], v[12:15]\n"
  "s_waitcnt vmcnt(14)\n"
  "ds_read_b128 a[184:187], v87 offset:6144\n"
  "ds_read_b128 a[188:191], v88 offset:6144\n"
  "v_exp_f32 v3, v3\n"
  "v_mfma_f32_16x16x32_bf16 v[12:15], v[140:143], v[60:63], v[12:15]\n"
  "v_cvt_pk_bf16_f32 v0, v0, v1\n"
  "v_cvt_pk_bf16_f32 v1, v2, v3\n"
  "v_mfma_f32_16x16x32_bf16 v[12:15], v[136:139], v[56:59], v[12:15]\n"
  "s_add_u32 m0, s59, 4096\n"
  "v_mfma_f32_16x16x32_bf16 v[16:19], v[160:163], v[56:59], 0\n"
  "s_waitcnt lgkmcnt(2)\n"
  "buffer_load_dwordx4 v101, s[40:43], s57 offen offset:0 nt lds\n"
  "buffer_load_dwordx4 v102, s[40:43], s57 offen offset:1024 nt lds\n"
  "s_add_u32 s57, s57, 6144\n"
  "v_exp_f32 v4, v4\n"
  "v_mfma_f32_16x16x32_bf16 v[16:19], v[164:167], v[60:63], v[16:19]\n"
  "s_waitcnt vmcnt(14)\n"
  "ds_read_b128 a[176:179], v87 offset:8192\n"
  "ds_read_b128 a[180:183], v88 offset:8192\n"
  "v_exp_f32 v5, v5\n"
  "v_mfma_f32_16x16x32_bf16 v[16:19], v[168:171], v[64:67], v[16:19]\n"
  "s_add_u32 m0, s59, 6144\n"
  "v_exp_f32 v6, v6\n"
  "v_mfma_f32_16x16x32_bf16 v[16:19], v[172:175], v[68:71], v[16:19]\n"
  "s_waitcnt lgkmcnt(2)\n"
  "buffer_load_dwordx4 v101, s[40:43], s57 offen offset:0 nt lds\n"
  "buffer_load_dwordx4 v102, s[40:43], s57 offen offset:1024 nt lds\n"
  "v_exp_f32 v7, v7\n"
  "v_mfma_f32_16x16x32_bf16 v[16:19], v[176:179], v[72:75], v[16:19]\n"
  "s_waitcnt vmcnt(14)\n"
  "ds_read_b128 a[168:171], v87 offset:10240\n"
  "ds_read_b128 a[172:175], v88 offset:10240\n"
  "v_cvt_pk_bf16_f32 v2, v4, v5\n"
  "v_cvt_pk_bf16_f32 v3, v6, v7\n"
  "v_mfma_f32_16x16x32_bf16 v[16:19], v[180:183], v[76:79], v[16:19]\n"
  "s_add_u32 m0, s59, 8064\n"
  "ds_write_b128 v80, v[0:3] offset:4096\n"
  "v_pk_fma_f32 v[8:9], v[8:9], s[60:61], s[60:61] op_sel:[0,0,1] op_sel_hi:[1,0,1]\n"
  "v_pk_fma_f32 v[10:11], v[10:11], s[60:61], s[60:61] op_sel:[0,0,1] op_sel_hi:[1,0,1]\n"
  "v_pk_fma_f32 v[12:13], v[12:13], s[60:61], s[60:61] op_sel:[0,0,1] op_sel_hi:[1,0,1]\n"
  "v_pk_fma_f32 v[14:15], v[14:15], s[60:61], s[60:61] op_sel:[0,0,1] op_sel_hi:[1,0,1]\n"
  "v_mfma_f32_16x16x32_bf16 v[20:23], v[204:207], v[76:79], 0\n"
  "s_waitcnt lgkmcnt(3)\n"
  "buffer_load_dwordx4 v101, s[40:43], s57 offen offset:128 nt lds\n"
  "buffer_load_dwordx4 v102, s[40:43], s57 offen offset:1152 nt lds\n"
  "v_exp_f32 v8, v8\n"
  "v_mfma_f32_16x16x32_bf16 v[20:23], v[200:203], v[72:75], v[20:23]\n"
  "s_waitcnt vmcnt(14)\n"
  "ds_read_b128 a[192:195], v87 offset:12288\n"
  "ds_read_b128 a[196:199], v88 offset:12288\n"
  "v_exp_f32 v9, v9\n"
  "v_mfma_f32_16x16x32_bf16 v[20:23], v[196:199], v[68:71], v[20:23]\n"
  "s_add_u32 m0, s59, 9984\n"
  "v_exp_f32 v10, v10\n"
  "v_mfma_f32_16x16x32_bf16 v[20:23], v[192:195], v[64:67], v[20:23]\n"
  "s_waitcnt lgkmcnt(3)\n"
  "buffer_load_dwordx4 v101, s[40:43], s57 offen offset:256 nt lds\n"
  "buffer_load_dwordx4 v102, s[40:43], s57 offen offset:1280 nt lds\n"
  "s_add_u32 s57, s57, 6144\n"
  "v_exp_f32 v11, v11\n"
  "v_mfma_f32_16x16x32_bf16 v[20:23], v[188:191], v[60:63], v[20:23]\n"
  "s_waitcnt vmcnt(14)\n"
  "ds_read_b128 a[200:203], v87 offset:14336\n"
  "ds_read_b128 a[204:207], v88 offset:14336\n"
  "v_cvt_pk_bf16_f32 v8, v8, v9\n"
  "v_cvt_pk_bf16_f32 v9, v10, v11\n"
  "v_mfma_f32_16x16x32_bf16 v[20:23], v[184:187], v[56:59], v[20:23]\n"
  "s_add_u32 m0, s59, 12032\n"
  "v_mfma_f32_16x16x32_bf16 v[24:27], v[208:211], v[56:59], 0\n"
  "s_waitcnt lgkmcnt(2)\n"
  "buffer_load_dwordx4 v101, s[40:43], s57 offen offset:256 nt lds\n"
  "buffer_load_dwordx4 v102, s[40:43], s57 offen offset:1280 nt lds\n"
  "v_exp_f32 v12, v12\n"
  "v_mfma_f32_16x16x32_bf16 v[24:27], v[212:215], v[60:63], v[24:27]\n"
  "s_waitcnt vmcnt(14)\n"
  "ds_read_b128 a[208:211], v87 offset:16384\n"
  "ds_read_b128 a[212:215], v88 offset:16384\n"
  "v_exp_f32 v13, v13\n"
  "v_mfma_f32_16x16x32_bf16 v[24:27], v[216:219], v[64:67], v[24:27]\n"
  "v_exp_f32 v14, v14\n"
  "v_mfma_f32_16x16x32_bf16 v[24:27], v[220:223], v[68:71], v[24:27]\n"
  "s_add_u32 m0, s59, 14208\n"
  "v_exp_f32 v15, v15\n"
  "v_mfma_f32_16x16x32_bf16 v[24:27], v[224:227], v[72:75], v[24:27]\n"
  "s_waitcnt lgkmcnt(2)\n"
  "buffer_load_dwordx4 v101, s[40:43], s57 offen offset:128 nt lds\n"
  "buffer_load_dwordx4 v102, s[40:43], s57 offen offset:1152 nt lds\n"
  "v_cvt_pk_bf16_f32 v10, v12, v13\n"
  "v_cvt_pk_bf16_f32 v11, v14, v15\n"
  "v_mfma_f32_16x16x32_bf16 v[24:27], v[228:231], v[76:79], v[24:27]\n"
  "s_waitcnt vmcnt(14)\n"
  "ds_read_b128 a[232:235], v87 offset:0\n"
  "ds_read_b128 a[236:239], v88 offset:0\n"
  "ds_write_b128 v80, v[8:11] offset:5120\n"
  "v_pk_fma_f32 v[16:17], v[16:17], s[60:61], s[60:61] op_sel:[0,0,1] op_sel_hi:[1,0,1]\n"
  "v_pk_fma_f32 v[18:19], v[18:19], s[60:61], s[60:61] op_sel:[0,0,1] op_sel_hi:[1,0,1]\n"
  "v_pk_fma_f32 v[20:21], v[20:21], s[60:61], s[60:61] op_sel:[0,0,1] op_sel_hi:[1,0,1]\n"
  "v_pk_fma_f32 v[22:23], v[22:23], s[60:61], s[60:61] op_sel:[0,0,1] op_sel_hi:[1,0,1]\n"
  "v_mfma_f32_16x16x32_bf16 v[28:31], v[252:255], v[76:79], 0\n"
  "s_add_u32 m0, s59, 16384\n"
  "v_exp_f32 v16, v16\n"
  "v_mfma_f32_16x16x32_bf16 v[28:31], v[248:251], v[72:75], v[28:31]\n"
  "s_waitcnt lgkmcnt(3)\n"
  "buffer_load_dwordx4 v101, s[40:43], s57 offen offset:0 nt lds\n"
  "buffer_load_dwordx4 v102, s[40:43], s57 offen offset:1024 nt lds\n"
  "s_add_u32 s57, s57, 6144\n"
  "v_exp_f32 v17, v17\n"
  "v_mfma_f32_16x16x32_bf16 v[28:31], v[244:247], v[68:71], v[28:31]\n"
  "s_waitcnt vmcnt(14)\n"
  "ds_read_b128 a[224:227], v87 offset:2048\n"
  "ds_read_b128 a[228:231], v88 offset:2048\n"
  "v_exp_f32 v18, v18\n"
  "v_mfma_f32_16x16x32_bf16 v[28:31], v[240:243], v[64:67], v[28:31]\n"
  "s_add_u32 m0, s59, 0\n"
  "v_exp_f32 v19, v19\n"
  "v_mfma_f32_16x16x32_bf16 v[28:31], v[236:239], v[60:63], v[28:31]\n"
  "s_waitcnt lgkmcnt(3)\n"
  "buffer_load_dwordx4 v101, s[40:43], s57 offen offset:0 nt lds\n"
  "buffer_load_dwordx4 v102, s[40:43], s57 offen offset:1024 nt lds\n"
  "v_cvt_pk_bf16_f32 v16, v16, v17\n"
  "v_cvt_pk_bf16_f32 v17, v18, v19\n"
  "v_mfma_f32_16x16x32_bf16 v[28:31], v[232:235], v[56:59], v[28:31]\n"
  "s_waitcnt vmcnt(14)\n"
  "ds_read_b128 a[216:219], v87 offset:4096\n"
  "ds_read_b128 a[220:223], v88 offset:4096\n"
  "v_mfma_f32_16x16x32_bf16 v[0:3], a[0:3], v[32:35], 0\n"
  "s_add_u32 s66, s66, 6144\n"
  "s_cmp_eq_u32 s66, 86016\n"
  "s_cselect_b32 s66, 49152, s66\n"
  "s_add_u32 m0, s59, 1920\n"
  "v_exp_f32 v20, v20\n"
  "v_mfma_f32_16x16x32_bf16 v[0:3], a[4:7], v[36:39], v[0:3]\n"
  "s_waitcnt lgkmcnt(2)\n"
  "buffer_load_dwordx4 v101, s[40:43], s57 offen offset:128 nt lds\n"
  "buffer_load_dwordx4 v102, s[40:43], s57 offen offset:1152 nt lds\n"
  "v_exp_f32 v21, v21\n"
  "v_mfma_f32_16x16x32_bf16 v[0:3], a[8:11], v[40:43], v[0:3]\n"
  "s_waitcnt vmcnt(14)\n"
  "ds_read_b128 a[240:243], v87 offset:6144\n"
  "ds_read_b128 a[244:247], v88 offset:6144\n"
  "v_exp_f32 v22, v22\n"
  "v_mfma_f32_16x16x32_bf16 v[0:3], a[12:15], v[44:47], v[0:3]\n"
  "s_add_u32 m0, s59, 3840\n"
  "v_exp_f32 v23, v23\n"
  "v_mfma_f32_16x16x32_bf16 v[0:3], a[16:19], v[48:51], v[0:3]\n"
  "s_waitcnt lgkmcnt(2)\n"
  "buffer_load_dwordx4 v101, s[40:43], s57 offen offset:256 nt lds\n"
  "buffer_load_dwordx4 v102, s[40:43], s57 offen offset:1280 nt lds\n"
  "s_add_u32 s57, s57, 6144\n"
  "v_cvt_pk_bf16_f32 v18, v20, v21\n"
  "v_cvt_pk_bf16_f32 v19, v22, v23\n"
  "v_mfma_f32_16x16x32_bf16 v[0:3], a[20:23], v[52:55], v[0:3]\n"
  "s_waitcnt vmcnt(14)\n"
  "ds_read_b128 a[248:251], v87 offset:8192\n"
  "ds_read_b128 a[252:255], v88 offset:8192\n"
  "ds_write_b128 v80, v[16:19] offset:6144\n"
  "v_pk_fma_f32 v[24:25], v[24:25], s[60:61], s[60:61] op_sel:[0,0,1] op_sel_hi:[1,0,1]\n"
  "v_pk_fma_f32 v[26:27], v[26:27], s[60:61], s[60:61] op_sel:[0,0,1] op_sel_hi:[1,0,1]\n"
  "v_pk_fma_f32 v[28:29], v[28:29], s[60:61], s[60:61] op_sel:[0,0,1] op_sel_hi:[1,0,1]\n"
  "v_pk_fma_f32 v[30:31], v[30:31], s[60:61], s[60:61] op_sel:[0,0,1] op_sel_hi:[1,0,1]\n"
  "v_mfma_f32_16x16x32_bf16 v[4:7], a[44:47], v[52:55], 0\n"
  "v_exp_f32 v24, v24\n"
  "v_mfma_f32_16x16x32_bf16 v[4:7], a[40:43], v[48:51], v[4:7]\n"
  "s_add_u32 m0, s59, 5888\n"
  "v_exp_f32 v25, v25\n"
  "v_mfma_f32_16x16x32_bf16 v[4:7], a[36:39], v[44:47], v[4:7]\n"
  "s_waitcnt lgkmcnt(3)\n"
  "buffer_load_dwordx4 v101, s[40:43], s57 offen offset:256 nt lds\n"
  "buffer_load_dwordx4 v102, s[40:43], s57 offen offset:1280 nt lds\n"
  "v_exp_f32 v26, v26\n"
  "v_mfma_f32_16x16x32_bf16 v[4:7], a[32:35], v[40:43], v[4:7]\n"
  "s_waitcnt vmcnt(14)\n"
  "ds_read_b128 v[128:131], v87 offset:10240\n"
  "ds_read_b128 v[132:135], v88 offset:10240\n"
  "v_exp_f32 v27, v27\n"
  "v_mfma_f32_16x16x32_bf16 v[4:7], a[28:31], v[36:39], v[4:7]\n"
  "s_add_u32 m0, s59, 8064\n"
  "v_cvt_pk_bf16_f32 v24, v24, v25\n"
  "v_cvt_pk_bf16_f32 v25, v26, v27\n"
  "v_mfma_f32_16x16x32_bf16 v[4:7], a[24:27], v[32:35], v[4:7]\n"
  "s_waitcnt lgkmcnt(3)\n"
  "buffer_load_dwordx4 v101, s[40:43], s57 offen offset:128 nt lds\n"
  "buffer_load_dwordx4 v102, s[40:43], s57 offen offset:1152 nt lds\n"
  "v_add_u32 v81, s66, v83\n"
  "v_add_u32 v82, s66, v84\n"
  "v_mfma_f32_16x16x32_bf16 v[8:11], a[48:51], v[32:35], 0\n"
  "s_waitcnt vmcnt(14)\n"
  "ds_read_b128 v[152:155], v87 offset:12288\n"
  "ds_read_b128 v[156:159], v88 offset:12288\n"
  "v_exp_f32 v28, v28\n"
  "v_mfma_f32_16x16x32_bf16 v[8:11], a[52:55], v[36:39], v[8:11]\n"
  "s_add_u32 m0, s59, 10240\n"
  "v_exp_f32 v29, v29\n"
  "v_mfma_f32_16x16x32_bf16 v[8:11], a[56:59], v[40:43], v[8:11]\n"
  "s_waitcnt lgkmcnt(2)\n"
  "buffer_load_dwordx4 v101, s[40:43], s57 offen offset:0 nt lds\n"
  "buffer_load_dwordx4 v102, s[40:43], s57 offen offset:1024 nt lds\n"
  "s_add_u32 s57, s57, 6144\n"
  "v_exp_f32 v30, v30\n"
  "v_mfma_f32_16x16x32_bf16 v[8:11], a[60:63], v[44:47], v[8:11]\n"
  "s_waitcnt vmcnt(14)\n"
  "ds_read_b128 v[144:147], v87 offset:14336\n"
  "ds_read_b128 v[148:151], v88 offset:14336\n"
  "v_exp_f32 v31, v31\n"
  "v_mfma_f32_16x16x32_bf16 v[8:11], a[64:67], v[48:51], v[8:11]\n"
  "s_add_u32 m0, s59, 12288\n"
  "v_cvt_pk_bf16_f32 v26, v28, v29\n"
  "v_cvt_pk_bf16_f32 v27, v30, v31\n"
  "v_mfma_f32_16x16x32_bf16 v[8:11], a[68:71], v[52:55], v[8:11]\n"
  "s_waitcnt lgkmcnt(2)\n"
  "buffer_load_dwordx4 v101, s[40:43], s57 offen offset:0 nt lds\n"
  "buffer_load_dwordx4 v102, s[40:43], s57 offen offset:1024 nt lds\n"
  "ds_write_b128 v80, v[24:27] offset:7168\n"
  "v_pk_fma_f32 v[0:1], v[0:1], s[60:61], s[60:61] op_sel:[0,0,1] op_sel_hi:[1,0,1]\n"
  "v_pk_fma_f32 v[2:3], v[2:3], s[60:61], s[60:61] op_sel:[0,0,1] op_sel_hi:[1,0,1]\n"
  "v_pk_fma_f32 v[4:5], v[4:5], s[60:61], s[60:61] op_sel:[0,0,1] op_sel_hi:[1,0,1]\n"
  "v_pk_fma_f32 v[6:7], v[6:7], s[60:61], s[60:61] op_sel:[0,0,1] op_sel_hi:[1,0,1]\n"
  "v_mfma_f32_16x16x32_bf16 v[12:15], a[92:95], v[52:55], 0\n"
  "s_waitcnt vmcnt(14)\n"
  "ds_read_b128 v[136:139], v87 offset:16384\n"
  "ds_read_b128 v[140:143], v88 offset:16384\n"
  "v_exp_f32 v0, v0\n"
  "v_mfma_f32_16x16x32_bf16 v[12:15], a[88:91], v[48:51], v[12:15]\n"
  "s_add_u32 m0, s59, 14208\n"
  "v_exp_f32 v1, v1\n"
  "v_mfma_f32_16x16x32_bf16 v[12:15], a[84:87], v[44:47], v[12:15]\n"
  "s_waitcnt lgkmcnt(3)\n"
  "buffer_load_dwordx4 v101, s[40:43], s57 offen offset:128 nt lds\n"
  "buffer_load_dwordx4 v102, s[40:43], s57 offen offset:1152 nt lds\n"
  "v_exp_f32 v2, v2\n"
  "v_mfma_f32_16x16x32_bf16 v[12:15], a[80:83], v[40:43], v[12:15]\n"
  "s_waitcnt vmcnt(14)\n"
  "ds_read_b128 v[160:163], v87 offset:0\n"
  "ds_read_b128 v[164:167], v88 offset:0\n"
  "v_exp_f32 v3, v3\n"
  "v_mfma_f32_16x16x32_bf16 v[12:15], a[76:79], v[36:39], v[12:15]\n"
  "v_cvt_pk_bf16_f32 v0, v0, v1\n"
  "v_cvt_pk_bf16_f32 v1, v2, v3\n"
  "v_mfma_f32_16x16x32_bf16 v[12:15], a[72:75], v[32:35], v[12:15]\n"
  "s_add_u32 m0, s59, 16128\n"
  "s_waitcnt lgkmcnt(4)\n"
  "s_barrier\n"
  "v_xor_b32 v80, 0x4000, v80\n"
  "v_mfma_f32_16x16x32_bf16 v[16:19], a[96:99], v[32:35], 0\n"
  "s_waitcnt lgkmcnt(2)\n"
  "buffer_load_dwordx4 v101, s[40:43], s57 offen offset:256 nt lds\n"
  "buffer_load_dwordx4 v102, s[40:43], s57 offen offset:1280 nt lds\n"
  "s_add_u32 s57, s57, 6144\n"
  "v_exp_f32 v4, v4\n"
  "v_mfma_f32_16x16x32_bf16 v[16:19], a[100:103], v[36:39], v[16:19]\n"
  "s_waitcnt vmcnt(14)\n"
  "ds_read_b128 v[168:171], v87 offset:2048\n"
  "ds_read_b128 v[172:175], v88 offset:2048\n"
  "v_exp_f32 v5, v5\n"
  "v_mfma_f32_16x16x32_bf16 v[16:19], a[104:107], v[40:43], v[16:19]\n"
  "s_add_u32 m0, s59, -256\n"
  "v_exp_f32 v6, v6\n"
  "v_mfma_f32_16x16x32_bf16 v[16:19], a[108:111], v[44:47], v[16:19]\n"
  "s_waitcnt lgkmcnt(2)\n"
  "buffer_load_dwordx4 v101, s[40:43], s57 offen offset:256 nt lds\n"
  "buffer_load_dwordx4 v102, s[40:43], s57 offen offset:1280 nt lds\n"
  "v_exp_f32 v7, v7\n"
  "v_mfma_f32_16x16x32_bf16 v[16:19], a[112:115], v[48:51], v[16:19]\n"
  "s_waitcnt vmcnt(14)\n"
  "ds_read_b128 v[176:179], v87 offset:4096\n"
  "ds_read_b128 v[180:183], v88 offset:4096\n"
  "v_cvt_pk_bf16_f32 v2, v4, v5\n"
  "v_cvt_pk_bf16_f32 v3, v6, v7\n"
  "v_mfma_f32_16x16x32_bf16 v[16:19], a[116:119], v[52:55], v[16:19]\n"
  "s_add_u32 m0, s59, 1920\n"
  "ds_write_b128 v80, v[0:3] offset:0\n"
  "v_pk_fma_f32 v[8:9], v[8:9], s[60:61], s[60:61] op_sel:[0,0,1] op_sel_hi:[1,0,1]\n"
  "v_pk_fma_f32 v[10:11], v[10:11], s[60:61], s[60:61] op_sel:[0,0,1] op_sel_hi:[1,0,1]\n"
  "v_pk_fma_f32 v[12:13], v[12:13], s[60:61], s[60:61] op_sel:[0,0,1] op_sel_hi:[1,0,1]\n"
  "v_pk_fma_f32 v[14:15], v[14:15], s[60:61], s[60:61] op_sel:[0,0,1] op_sel_hi:[1,0,1]\n"
  "v_mfma_f32_16x16x32_bf16 v[20:23], a[140:143], v[52:55], 0\n"
  "s_waitcnt lgkmcnt(3)\n"
  "buffer_load_dwordx4 v101, s[40:43], s57 offen offset:128 nt lds\n"
  "buffer_load_dwordx4 v102, s[40:43], s57 offen offset:1152 nt lds\n"
  "v_exp_f32 v8, v8\n"
  "v_mfma_f32_16x16x32_bf16 v[20:23], a[136:139], v[48:51], v[20:23]\n"
  "s_waitcnt vmcnt(14)\n"
  "ds_read_b128 v[200:203], v87 offset:6144\n"
  "ds_read_b128 v[204:207], v88 offset:6144\n"
  "v_exp_f32 v9, v9\n"
  "v_mfma_f32_16x16x32_bf16 v[20:23], a[132:135], v[44:47], v[20:23]\n"
  "s_add_u32 m0, s59, 4096\n"
  "v_exp_f32 v10, v10\n"
  "v_mfma_f32_16x16x32_bf16 v[20:23], a[128:131], v[40:43], v[20:23]\n"
  "s_waitcnt lgkmcnt(3)\n"
  "buffer_load_dwordx4 v101, s[40:43], s57 offen offset:0 nt lds\n"
  "buffer_load_dwordx4 v102, s[40:43], s57 offen offset:1024 nt lds\n"
  "s_add_u32 s57, s57, 6144\n"
  "v_exp_f32 v11, v11\n"
  "v_mfma_f32_16x16x32_bf16 v[20:23], a[124:127], v[36:39], v[20:23]\n"
  "s_waitcnt vmcnt(14)\n"
  "ds_read_b128 v[192:195], v87 offset:8192\n"
  "ds_read_b128 v[196:199], v88 offset:8192\n"
  "v_cvt_pk_bf16_f32 v8, v8, v9\n"
  "v_cvt_pk_bf16_f32 v9, v10, v11\n"
  "v_mfma_f32_16x16x32_bf16 v[20:23], a[120:123], v[32:35], v[20:23]\n"
  "ds_read_b128 v[56:59], v81 offset:0\n"
  "ds_read_b128 v[60:63], v82 offset:0\n"
  "v_mfma_f32_16x16x32_bf16 v[24:27], a[144:147], v[32:35], 0\n"
  "v_exp_f32 v12, v12\n"
  "v_mfma_f32_16x16x32_bf16 v[24:27], a[148:151], v[36:39], v[24:27]\n"
  "s_waitcnt vmcnt(12)\n"
  "ds_read_b128 v[184:187], v87 offset:10240\n"
  "ds_read_b128 v[188:191], v88 offset:10240\n"
  "v_exp_f32 v13, v13\n"
  "v_mfma_f32_16x16x32_bf16 v[24:27], a[152:155], v[40:43], v[24:27]\n"
  "v_exp_f32 v14, v14\n"
  "v_mfma_f32_16x16x32_bf16 v[24:27], a[156:159], v[44:47], v[24:27]\n"
  "v_exp_f32 v15, v15\n"
  "v_mfma_f32_16x16x32_bf16 v[24:27], a[160:163], v[48:51], v[24:27]\n"
  "v_cvt_pk_bf16_f32 v10, v12, v13\n"
  "v_cvt_pk_bf16_f32 v11, v14, v15\n"
  "v_mfma_f32_16x16x32_bf16 v[24:27], a[164:167], v[52:55], v[24:27]\n"
  "s_waitcnt vmcnt(10)\n"
  "ds_read_b128 v[208:211], v87 offset:12288\n"
  "ds_read_b128 v[212:215], v88 offset:12288\n"
  "ds_write_b128 v80, v[8:11] offset:1024\n"
  "v_pk_fma_f32 v[16:17], v[16:17], s[60:61], s[60:61] op_sel:[0,0,1] op_sel_hi:[1,0,1]\n"
  "v_pk_fma_f32 v[18:19], v[18:19], s[60:61], s[60:61] op_sel:[0,0,1] op_sel_hi:[1,0,1]\n"
  "v_pk_fma_f32 v[20:21], v[20:21], s[60:61], s[60:61] op_sel:[0,0,1] op_sel_hi:[1,0,1]\n"
  "v_pk_fma_f32 v[22:23], v[22:23], s[60:61], s[60:61] op_sel:[0,0,1] op_sel_hi:[1,0,1]\n"
  "v_mfma_f32_16x16x32_bf16 v[28:31], a[188:191], v[52:55], 0\n"
  "v_exp_f32 v16, v16\n"
  "v_mfma_f32_16x16x32_bf16 v[28:31], a[184:187], v[48:51], v[28:31]\n"
  "v_exp_f32 v17, v17\n"
  "v_mfma_f32_16x16x32_bf16 v[28:31], a[180:183], v[44:47], v[28:31]\n"
  "s_waitcnt vmcnt(8)\n"
  "ds_read_b128 v[216:219], v87 offset:14336\n"
  "ds_read_b128 v[220:223], v88 offset:14336\n"
  "v_exp_f32 v18, v18\n"
  "v_mfma_f32_16x16x32_bf16 v[28:31], a[176:179], v[40:43], v[28:31]\n"
  "v_exp_f32 v19, v19\n"
  "v_mfma_f32_16x16x32_bf16 v[28:31], a[172:175], v[36:39], v[28:31]\n"
  "v_cvt_pk_bf16_f32 v16, v16, v17\n"
  "v_cvt_pk_bf16_f32 v17, v18, v19\n"
  "v_mfma_f32_16x16x32_bf16 v[28:31], a[168:171], v[32:35], v[28:31]\n"
  "s_waitcnt vmcnt(6)\n"
  "ds_read_b128 v[224:227], v87 offset:16384\n"
  "ds_read_b128 v[228:231], v88 offset:16384\n"
  "ds_read_b128 v[64:67], v81 offset:2048\n"
  "ds_read_b128 v[68:71], v82 offset:2048\n"
  "v_mfma_f32_16x16x32_bf16 v[0:3], a[192:195], v[32:35], 0\n"
  "v_exp_f32 v20, v20\n"
  "v_mfma_f32_16x16x32_bf16 v[0:3], a[196:199], v[36:39], v[0:3]\n"
  "v_exp_f32 v21, v21\n"
  "v_mfma_f32_16x16x32_bf16 v[0:3], a[200:203], v[40:43], v[0:3]\n"
  "s_waitcnt vmcnt(4)\n"
  "ds_read_b128 v[248:251], v87 offset:0\n"
  "ds_read_b128 v[252:255], v88 offset:0\n"
  "v_exp_f32 v22, v22\n"
  "v_mfma_f32_16x16x32_bf16 v[0:3], a[204:207], v[44:47], v[0:3]\n"
  "v_exp_f32 v23, v23\n"
  "v_mfma_f32_16x16x32_bf16 v[0:3], a[208:211], v[48:51], v[0:3]\n"
  "v_cvt_pk_bf16_f32 v18, v20, v21\n"
  "v_cvt_pk_bf16_f32 v19, v22, v23\n"
  "v_mfma_f32_16x16x32_bf16 v[0:3], a[212:215], v[52:55], v[0:3]\n"
  "s_waitcnt vmcnt(2)\n"
  "ds_read_b128 v[240:243], v87 offset:2048\n"
  "ds_read_b128 v[244:247], v88 offset:2048\n"
  "ds_write_b128 v80, v[16:19] offset:2048\n"
  "v_pk_fma_f32 v[24:25], v[24:25], s[60:61], s[60:61] op_sel:[0,0,1] op_sel_hi:[1,0,1]\n"
  "v_pk_fma_f32 v[26:27], v[26:27], s[60:61], s[60:61] op_sel:[0,0,1] op_sel_hi:[1,0,1]\n"
  "v_pk_fma_f32 v[28:29], v[28:29], s[60:61], s[60:61] op_sel:[0,0,1] op_sel_hi:[1,0,1]\n"
  "v_pk_fma_f32 v[30:31], v[30:31], s[60:61], s[60:61] op_sel:[0,0,1] op_sel_hi:[1,0,1]\n"
  "v_mfma_f32_16x16x32_bf16 v[4:7], a[236:239], v[52:55], 0\n"
  "v_exp_f32 v24, v24\n"
  "v_mfma_f32_16x16x32_bf16 v[4:7], a[232:235], v[48:51], v[4:7]\n"
  "v_exp_f32 v25, v25\n"
  "v_mfma_f32_16x16x32_bf16 v[4:7], a[228:231], v[44:47], v[4:7]\n"
  "v_exp_f32 v26, v26\n"
  "v_mfma_f32_16x16x32_bf16 v[4:7], a[224:227], v[40:43], v[4:7]\n"
  "s_waitcnt vmcnt(0)\n"
  "ds_read_b128 v[232:235], v87 offset:4096\n"
  "ds_read_b128 v[236:239], v88 offset:4096\n"
  "v_exp_f32 v27, v27\n"
  "v_mfma_f32_16x16x32_bf16 v[4:7], a[220:223], v[36:39], v[4:7]\n"
  "v_cvt_pk_bf16_f32 v24, v24, v25\n"
  "v_cvt_pk_bf16_f32 v25, v26, v27\n"
  "v_mfma_f32_16x16x32_bf16 v[4:7], a[216:219], v[32:35], v[4:7]\n"
  "ds_read_b128 v[72:75], v81 offset:4096\n"
  "ds_read_b128 v[76:79], v82 offset:4096\n"
  "v_mfma_f32_16x16x32_bf16 v[8:11], a[240:243], v[32:35], 0\n"
  "v_exp_f32 v28, v28\n"
  "v_mfma_f32_16x16x32_bf16 v[8:11], a[244:247], v[36:39], v[8:11]\n"
  "v_exp_f32 v29, v29\n"
  "v_mfma_f32_16x16x32_bf16 v[8:11], a[248:251], v[40:43], v[8:11]\n"
  "v_exp_f32 v30, v30\n"
  "v_mfma_f32_16x16x32_bf16 v[8:11], a[252:255], v[44:47], v[8:11]\n"
  "v_exp_f32 v31, v31\n"
  "v_mfma_f32_16x16x32_bf16 v[8:11], v[128:131], v[48:51], v[8:11]\n"
  "v_cvt_pk_bf16_f32 v26, v28, v29\n"
  "v_cvt_pk_bf16_f32 v27, v30, v31\n"
  "v_mfma_f32_16x16x32_bf16 v[8:11], v[132:135], v[52:55], v[8:11]\n"
  "ds_write_b128 v80, v[24:27] offset:3072\n"
  "v_pk_fma_f32 v[0:1], v[0:1], s[60:61], s[60:61] op_sel:[0,0,1] op_sel_hi:[1,0,1]\n"
  "v_pk_fma_f32 v[2:3], v[2:3], s[60:61], s[60:61] op_sel:[0,0,1] op_sel_hi:[1,0,1]\n"
  "v_pk_fma_f32 v[4:5], v[4:5], s[60:61], s[60:61] op_sel:[0,0,1] op_sel_hi:[1,0,1]\n"
  "v_pk_fma_f32 v[6:7], v[6:7], s[60:61], s[60:61] op_sel:[0,0,1] op_sel_hi:[1,0,1]\n"
  "v_mfma_f32_16x16x32_bf16 v[12:15], v[156:159], v[52:55], 0\n"
  "v_exp_f32 v0, v0\n"
  "v_mfma_f32_16x16x32_bf16 v[12:15], v[152:155], v[48:51], v[12:15]\n"
  "v_exp_f32 v1, v1\n"
  "v_mfma_f32_16x16x32_bf16 v[12:15], v[148:151], v[44:47], v[12:15]\n"
  "v_exp_f32 v2, v2\n"
  "v_mfma_f32_16x16x32_bf16 v[12:15], v[144:147], v[40:43], v[12:15]\n"
  "v_exp_f32 v3, v3\n"
  "v_mfma_f32_16x16x32_bf16 v[12:15], v[140:143], v[36:39], v[12:15]\n"
  "v_cvt_pk_bf16_f32 v0, v0, v1\n"
  "v_cvt_pk_bf16_f32 v1, v2, v3\n"
  "v_mfma_f32_16x16x32_bf16 v[12:15], v[136:139], v[32:35], v[12:15]\n"
  "v_mfma_f32_16x16x32_bf16 v[16:19], v[160:163], v[32:35], 0\n"
  "v_exp_f32 v4, v4\n"
  "v_mfma_f32_16x16x32_bf16 v[16:19], v[164:167], v[36:39], v[16:19]\n"
  "v_exp_f32 v5, v5\n"
  "v_mfma_f32_16x16x32_bf16 v[16:19], v[168:171], v[40:43], v[16:19]\n"
  "v_exp_f32 v6, v6\n"
  "v_mfma_f32_16x16x32_bf16 v[16:19], v[172:175], v[44:47], v[16:19]\n"
  "v_exp_f32 v7, v7\n"
  "v_mfma_f32_16x16x32_bf16 v[16:19], v[176:179], v[48:51], v[16:19]\n"
  "v_cvt_pk_bf16_f32 v2, v4, v5\n"
  "v_cvt_pk_bf16_f32 v3, v6, v7\n"
  "v_mfma_f32_16x16x32_bf16 v[16:19], v[180:183], v[52:55], v[16:19]\n"
  "s_waitcnt lgkmcnt(15)\n"
  "ds_write_b128 v80, v[0:3] offset:4096\n"
  "v_pk_fma_f32 v[8:9], v[8:9], s[60:61], s[60:61] op_sel:[0,0,1] op_sel_hi:[1,0,1]\n"
  "v_pk_fma_f32 v[10:11], v[10:11], s[60:61], s[60:61] op_sel:[0,0,1] op_sel_hi:[1,0,1]\n"
  "v_pk_fma_f32 v[12:13], v[12:13], s[60:61], s[60:61] op_sel:[0,0,1] op_sel_hi:[1,0,1]\n"
  "v_pk_fma_f32 v[14:15], v[14:15], s[60:61], s[60:61] op_sel:[0,0,1] op_sel_hi:[1,0,1]\n"
  "v_mfma_f32_16x16x32_bf16 v[20:23], v[204:207], v[52:55], 0\n"
  "v_exp_f32 v8, v8\n"
  "v_mfma_f32_16x16x32_bf16 v[20:23], v[200:203], v[48:51], v[20:23]\n"
  "v_exp_f32 v9, v9\n"
  "v_mfma_f32_16x16x32_bf16 v[20:23], v[196:199], v[44:47], v[20:23]\n"
  "v_exp_f32 v10, v10\n"
  "v_mfma_f32_16x16x32_bf16 v[20:23], v[192:195], v[40:43], v[20:23]\n"
  "v_exp_f32 v11, v11\n"
  "v_mfma_f32_16x16x32_bf16 v[20:23], v[188:191], v[36:39], v[20:23]\n"
  "v_cvt_pk_bf16_f32 v8, v8, v9\n"
  "v_cvt_pk_bf16_f32 v9, v10, v11\n"
  "v_mfma_f32_16x16x32_bf16 v[20:23], v[184:187], v[32:35], v[20:23]\n"
  "v_mfma_f32_16x16x32_bf16 v[24:27], v[208:211], v[32:35], 0\n"
  "v_exp_f32 v12, v12\n"
  "v_mfma_f32_16x16x32_bf16 v[24:27], v[212:215], v[36:39], v[24:27]\n"
  "s_waitcnt lgkmcnt(15)\n"
  "v_exp_f32 v13, v13\n"
  "v_mfma_f32_16x16x32_bf16 v[24:27], v[216:219], v[40:43], v[24:27]\n"
  "v_exp_f32 v14, v14\n"
  "v_mfma_f32_16x16x32_bf16 v[24:27], v[220:223], v[44:47], v[24:27]\n"
  "s_waitcnt lgkmcnt(13)\n"
  "v_exp_f32 v15, v15\n"
  "v_mfma_f32_16x16x32_bf16 v[24:27], v[224:227], v[48:51], v[24:27]\n"
  "v_cvt_pk_bf16_f32 v10, v12, v13\n"
  "v_cvt_pk_bf16_f32 v11, v14, v15\n"
  "v_mfma_f32_16x16x32_bf16 v[24:27], v[228:231], v[52:55], v[24:27]\n"
  "s_waitcnt lgkmcnt(9)\n"
  "ds_write_b128 v80, v[8:11] offset:5120\n"
  "v_pk_fma_f32 v[16:17], v[16:17], s[60:61], s[60:61] op_sel:[0,0,1] op_sel_hi:[1,0,1]\n"
  "v_pk_fma_f32 v[18:19], v[18:19], s[60:61], s[60:61] op_sel:[0,0,1] op_sel_hi:[1,0,1]\n"
  "v_pk_fma_f32 v[20:21], v[20:21], s[60:61], s[60:61] op_sel:[0,0,1] op_sel_hi:[1,0,1]\n"
  "v_pk_fma_f32 v[22:23], v[22:23], s[60:61], s[60:61] op_sel:[0,0,1] op_sel_hi:[1,0,1]\n"
  "v_mfma_f32_16x16x32_bf16 v[28:31], v[252:255], v[52:55], 0\n"
  "v_exp_f32 v16, v16\n"
  "v_mfma_f32_16x16x32_bf16 v[28:31], v[248:251], v[48:51], v[28:31]\n"
  "s_waitcnt lgkmcnt(8)\n"
  "v_exp_f32 v17, v17\n"
  "v_mfma_f32_16x16x32_bf16 v[28:31], v[244:247], v[44:47], v[28:31]\n"
  "v_exp_f32 v18, v18\n"
  "v_mfma_f32_16x16x32_bf16 v[28:31], v[240:243], v[40:43], v[28:31]\n"
  "s_waitcnt lgkmcnt(5)\n"
  "s_sub_u32 s56, s56, 1\n"
  "v_exp_f32 v19, v19\n"
  "v_mfma_f32_16x16x32_bf16 v[28:31], v[236:239], v[36:39], v[28:31]\n"
  "v_cvt_pk_bf16_f32 v16, v16, v17\n"
  "v_cvt_pk_bf16_f32 v17, v18, v19\n"
  "v_mfma_f32_16x16x32_bf16 v[28:31], v[232:235], v[32:35], v[28:31]\n"
  "s_waitcnt lgkmcnt(3)\n"
  "s_branch A_H1_%=\n"
  "A_X0_%=:\n"
  "s_mov_b32 s52, s53\n"
  "s_mov_b32 s53, s54\n"
  "s_mov_b32 s54, s55\n"
  "s_cmp_eq_u32 s52, 0\n"
  "s_cbranch_scc1 A_LAST_%=\n"
  "s_lshr_b32 s62, s52, 16\n"
  "s_lshl1_add_u32 s62, s62, s65\n"
  "s_mul_i32 s57, s62, 98304\n"
  "s_and_b32 s56, s52, 0xff\n"
  "s_cmp_lg_u32 s53, 0\n"
  "s_cselect_b32 s62, 1, 0\n"
  "s_sub_u32 s56, s56, s62\n"
  "s_add_u32 s57, s57, 18432\n"
  "v_mfma_f32_16x16x32_bf16 v[0:3], a[0:3], v[32:35], 0\n"
  "s_add_u32 s66, s66, 6144\n"
  "s_cmp_eq_u32 s66, 86016\n"
  "s_cselect_b32 s66, 49152, s66\n"
  "v_exp_f32 v20, v20\n"
  "v_mfma_f32_16x16x32_bf16 v[0:3], a[4:7], v[36:39], v[0:3]\n"
  "v_exp_f32 v21, v21\n"
  "v_mfma_f32_16x16x32_bf16 v[0:3], a[8:11], v[40:43], v[0:3]\n"
  "ds_read_b128 a[0:3], v87 offset:0\n"
  "ds_read_b128 a[4:7], v88 offset:0\n"
  "v_exp_f32 v22, v22\n"
  "v_mfma_f32_16x16x32_bf16 v[0:3], a[12:15], v[44:47], v[0:3]\n"
  "v_exp_f32 v23, v23\n"
  "v_mfma_f32_16x16x32_bf16 v[0:3], a[16:19], v[48:51], v[0:3]\n"
  "v_cvt_pk_bf16_f32 v18, v20, v21\n"
  "v_cvt_pk_bf16_f32 v19, v22, v23\n"
  "v_mfma_f32_16x16x32_bf16 v[0:3], a[20:23], v[52:55], v[0:3]\n"
  "ds_read_b128 a[8:11], v87 offset:2048\n"
  "ds_read_b128 a[12:15], v88 offset:2048\n"
  "ds_write_b128 v80, v[16:19] offset:6144\n"
  "v_pk_fma_f32 v[24:25], v[24:25], s[60:61], s[60:61] op_sel:[0,0,1] op_sel_hi:[1,0,1]\n"
  "v_pk_fma_f32 v[26:27], v[26:27], s[60:61], s[60:61] op_sel:[0,0,1] op_sel_hi:[1,0,1]\n"
  "v_pk_fma_f32 v[28:29], v[28:29], s[60:61], s[60:61] op_sel:[0,0,1] op_sel_hi:[1,0,1]\n"
  "v_pk_fma_f32 v[30:31], v[30:31], s[60:61], s[60:61] op_sel:[0,0,1] op_sel_hi:[1,0,1]\n"
  "v_mfma_f32_16x16x32_bf16 v[4:7], a[44:47], v[52:55], 0\n"
  "v_exp_f32 v24, v24\n"
  "v_mfma_f32_16x16x32_bf16 v[4:7], a[40:43], v[48:51], v[4:7]\n"
  "s_add_u32 m0, s59, -256\n"
  "v_exp_f32 v25, v25\n"
  "v_mfma_f32_16x16x32_bf16 v[4:7], a[36:39], v[44:47], v[4:7]\n"
  "s_waitcnt lgkmcnt(3)\n"
  "buffer_load_dwordx4 v101, s[40:43], s57 offen offset:256 nt lds\n"
  "buffer_load_dwordx4 v102, s[40:43], s57 offen offset:1280 nt lds\n"
  "v_exp_f32 v26, v26\n"
  "v_mfma_f32_16x16x32_bf16 v[4:7], a[32:35], v[40:43], v[4:7]\n"
  "ds_read_b128 a[16:19], v87 offset:4096\n"
  "ds_read_b128 a[20:23], v88 offset:4096\n"
  "v_exp_f32 v27, v27\n"
  "v_mfma_f32_16x16x32_bf16 v[4:7], a[28:31], v[36:39], v[4:7]\n"
  "s_add_u32 m0, s59, 1920\n"
  "v_cvt_pk_bf16_f32 v24, v24, v25\n"
  "v_cvt_pk_bf16_f32 v25, v26, v27\n"
  "v_mfma_f32_16x16x32_bf16 v[4:7], a[24:27], v[32:35], v[4:7]\n"
  "s_waitcnt lgkmcnt(3)\n"
  "buffer_load_dwordx4 v101, s[40:43], s57 offen offset:128 nt lds\n"
  "buffer_load_dwordx4 v102, s[40:43], s57 offen offset:1152 nt lds\n"
  "v_add_u32 v81, s66, v83\n"
  "v_add_u32 v82, s66, v84\n"
  "v_mfma_f32_16x16x32_bf16 v[8:11], a[48:51], v[32:35], 0\n"
  "ds_read_b128 a[40:43], v87 offset:6144\n"
  "ds_read_b128 a[44:47], v88 offset:6144\n"
  "v_exp_f32 v28, v28\n"
  "v_mfma_f32_16x16x32_bf16 v[8:11], a[52:55], v[36:39], v[8:11]\n"
  "s_add_u32 m0, s59, 4096\n"
  "v_exp_f32 v29, v29\n"
  "v_mfma_f32_16x16x32_bf16 v[8:11], a[56:59], v[40:43], v[8:11]\n"
  "s_waitcnt lgkmcnt(2)\n"
  "buffer_load_dwordx4 v101, s[40:43], s57 offen offset:0 nt lds\n"
  "buffer_load_dwordx4 v102, s[40:43], s57 offen offset:1024 nt lds\n"
  "s_add_u32 s57, s57, 6144\n"
  "v_exp_f32 v30, v30\n"
  "v_mfma_f32_16x16x32_bf16 v[8:11], a[60:63], v[44:47], v[8:11]\n"
  "ds_read_b128 a[32:35], v87 offset:8192\n"
  "ds_read_b128 a[36:39], v88 offset:8192\n"
  "v_exp_f32 v31, v31\n"
  "v_mfma_f32_16x16x32_bf16 v[8:11], a[64:67], v[48:51], v[8:11]\n"
  "s_add_u32 m0, s59, 6144\n"
  "v_cvt_pk_bf16_f32 v26, v28, v29\n"
  "v_cvt_pk_bf16_f32 v27, v30, v31\n"
  "v_mfma_f32_16x16x32_bf16 v[8:11], a[68:71], v[52:55], v[8:11]\n"
  "s_waitcnt lgkmcnt(2)\n"
  "buffer_load_dwordx4 v101, s[40:43], s57 offen offset:0 nt lds\n"
  "buffer_load_dwordx4 v102, s[40:43], s57 offen offset:1024 nt lds\n"
  "ds_write_b128 v80, v[24:27] offset:7168\n"
  "v_pk_fma_f32 v[0:1], v[0:1], s[60:61], s[60:61] op_sel:[0,0,1] op_sel_hi:[1,0,1]\n"
  "v_pk_fma_f32 v[2:3], v[2:3], s[60:61], s[60:61] op_sel:[0,0,1] op_sel_hi:[1,0,1]\n"
  "v_pk_fma_f32 v[4:5], v[4:5], s[60:61], s[60:61] op_sel:[0,0,1] op_sel_hi:[1,0,1]\n"
  "v_pk_fma_f32 v[6:7], v[6:7], s[60:61], s[60:61] op_sel:[0,0,1] op_sel_hi:[1,0,1]\n"
  "v_mfma_f32_16x16x32_bf16 v[12:15], a[92:95], v[52:55], 0\n"
  "ds_read_b128 a[24:27], v87 offset:10240\n"
  "ds_read_b128 a[28:31], v88 offset:10240\n"
  "v_exp_f32 v0, v0\n"
  "v_mfma_f32_16x16x32_bf16 v[12:15], a[88:91], v[48:51], v[12:15]\n"
  "s_add_u32 m0, s59, 8064\n"
  "v_exp_f32 v1, v1\n"
  "v_mfma_f32_16x16x32_bf16 v[12:15], a[84:87], v[44:47], v[12:15]\n"
  "s_waitcnt lgkmcnt(3)\n"
  "buffer_load_dwordx4 v101, s[40:43], s57 offen offset:128 nt lds\n"
  "buffer_load_dwordx4 v102, s[40:43], s57 offen offset:1152 nt lds\n"
  "v_exp_f32 v2, v2\n"
  "v_mfma_f32_16x16x32_bf16 v[12:15], a[80:83], v[40:43], v[12:15]\n"
  "ds_read_b128 a[48:51], v87 offset:12288\n"
  "ds_read_b128 a[52:55], v88 offset:12288\n"
  "v_exp_f32 v3, v3\n"
  "v_mfma_f32_16x16x32_bf16 v[12:15], a[76:79], v[36:39], v[12:15]\n"
  "v_cvt_pk_bf16_f32 v0, v0, v1\n"
  "v_cvt_pk_bf16_f32 v1, v2, v3\n"
  "v_mfma_f32_16x16x32_bf16 v[12:15], a[72:75], v[32:35], v[12:15]\n"
  "s_add_u32 m0, s59, 9984\n"
  "s_waitcnt lgkmcnt(4)\n"
  "s_barrier\n"
  "v_xor_b32 v80, 0x4000, v80\n"
  "v_mfma_f32_16x16x32_bf16 v[16:19], a[96:99], v[32:35], 0\n"
  "s_waitcnt lgkmcnt(2)\n"
  "buffer_load_dwordx4 v101, s[40:43], s57 offen offset:256 nt lds\n"
  "buffer_load_dwordx4 v102, s[40:43], s57 offen offset:1280 nt lds\n"
  "s_add_u32 s57, s57, 6144\n"
  "v_exp_f32 v4, v4\n"
  "v_mfma_f32_16x16x32_bf16 v[16:19], a[100:103], v[36:39], v[16:19]\n"
  "ds_read_b128 a[56:59], v87 offset:14336\n"
  "ds_read_b128 a[60:63], v88 offset:14336\n"
  "v_exp_f32 v5, v5\n"
  "v_mfma_f32_16x16x32_bf16 v[16:19], a[104:107], v[40:43], v[16:19]\n"
  "s_add_u32 m0, s59, 12032\n"
  "v_exp_f32 v6, v6\n"
  "v_mfma_f32_16x16x32_bf16 v[16:19], a[108:111], v[44:47], v[16:19]\n"
  "s_waitcnt lgkmcnt(2)\n"
  "buffer_load_dwordx4 v101, s[40:43], s57 offen offset:256 nt lds\n"
  "buffer_load_dwordx4 v102, s[40:43], s57 offen offset:1280 nt lds\n"
  "v_exp_f32 v7, v7\n"
  "v_mfma_f32_16x16x32_bf16 v[16:19], a[112:115], v[48:51], v[16:19]\n"
  "ds_read_b128 a[64:67], v87 offset:16384\n"
  "ds_read_b128 a[68:71], v88 offset:16384\n"
  "v_cvt_pk_bf16_f32 v2, v4, v5\n"
  "v_cvt_pk_bf16_f32 v3, v6, v7\n"
  "v_mfma_f32_16x16x32_bf16 v[16:19], a[116:119], v[52:55], v[16:19]\n"
  "s_add_u32 m0, s59, 14208\n"
  "ds_write_b128 v80, v[0:3] offset:0\n"
  "v_pk_fma_f32 v[8:9], v[8:9], s[60:61], s[60:61] op_sel:[0,0,1] op_sel_hi:[1,0,1]\n"
  "v_pk_fma_f32 v[10:11], v[10:11], s[60:61], s[60:61] op_sel:[0,0,1] op_sel_hi:[1,0,1]\n"
  "v_pk_fma_f32 v[12:13], v[12:13], s[60:61], s[60:61] op_sel:[0,0,1] op_sel_hi:[1,0,1]\n"
  "v_pk_fma_f32 v[14:15], v[14:15], s[60:61], s[60:61] op_sel:[0,0,1] op_sel_hi:[1,0,1]\n"
  "v_mfma_f32_16x16x32_bf16 v[20:23], a[140:143], v[52:55], 0\n"
  "s_waitcnt lgkmcnt(3)\n"
  "buffer_load_dwordx4 v101, s[40:43], s57 offen offset:128 nt lds\n"
  "buffer_load_dwordx4 v102, s[40:43], s57 offen offset:1152 nt lds\n"
  "v_exp_f32 v8, v8\n"
  "v_mfma_f32_16x16x32_bf16 v[20:23], a[136:139], v[48:51], v[20:23]\n"
  "s_waitcnt vmcnt(14)\n"
  "ds_read_b128 a[88:91], v87 offset:0\n"
  "ds_read_b128 a[92:95], v88 offset:0\n"
  "v_exp_f32 v9, v9\n"
  "v_mfma_f32_16x16x32_bf16 v[20:23], a[132:135], v[44:47], v[20:23]\n"
  "s_add_u32 m0, s59, 16384\n"
  "v_exp_f32 v10, v10\n"
  "v_mfma_f32_16x16x32_bf16 v[20:23], a[128:131], v[40:43], v[20:23]\n"
  "s_waitcnt lgkmcnt(3)\n"
  "buffer_load_dwordx4 v101, s[40:43], s57 offen offset:0 nt lds\n"
  "buffer_load_dwordx4 v102, s[40:43], s57 offen offset:1024 nt lds\n"
  "s_add_u32 s57, s57, 6144\n"
  "v_exp_f32 v11, v11\n"
  "v_mfma_f32_16x16x32_bf16 v[20:23], a[124:127], v[36:39], v[20:23]\n"
  "s_waitcnt vmcnt(14)\n"
  "ds_read_b128 a[80:83], v87 offset:2048\n"
  "ds_read_b128 a[84:87], v88 offset:2048\n"
  "v_cvt_pk_bf16_f32 v8, v8, v9\n"
  "v_cvt_pk_bf16_f32 v9, v10, v11\n"
  "v_mfma_f32_16x16x32_bf16 v[20:23], a[120:123], v[32:35], v[20:23]\n"
  "s_add_u32 m0, s59, 0\n"
  "ds_read_b128 v[56:59], v81 offset:0\n"
  "ds_read_b128 v[60:63], v82 offset:0\n"
  "v_mfma_f32_16x16x32_bf16 v[24:27], a[144:147], v[32:35], 0\n"
  "s_waitcnt lgkmcnt(4)\n"
  "buffer_load_dwordx4 v101, s[40:43], s57 offen offset:0 nt lds\n"
  "buffer_load_dwordx4 v102, s[40:43], s57 offen offset:1024 nt lds\n"
  "v_exp_f32 v12, v12\n"
  "v_mfma_f32_16x16x32_bf16 v[24:27], a[148:151], v[36:39], v[24:27]\n"
  "s_waitcnt vmcnt(14)\n"
  "ds_read_b128 a[72:75], v87 offset:4096\n"
  "ds_read_b128 a[76:79], v88 offset:4096\n"
  "v_exp_f32 v13, v13\n"
  "v_mfma_f32_16x16x32_bf16 v[24:27], a[152:155], v[40:43], v[24:27]\n"
  "v_exp_f32 v14, v14\n"
  "v_mfma_f32_16x16x32_bf16 v[24:27], a[156:159], v[44:47], v[24:27]\n"
  "s_add_u32 m0, s59, 1920\n"
  "v_exp_f32 v15, v15\n"
  "v_mfma_f32_16x16x32_bf16 v[24:27], a[160:163], v[48:51], v[24:27]\n"
  "s_waitcnt lgkmcnt(4)\n"
  "buffer_load_dwordx4 v101, s[40:43], s57 offen offset:128 nt lds\n"
  "buffer_load_dwordx4 v102, s[40:43], s57 offen offset:1152 nt lds\n"
  "v_cvt_pk_bf16_f32 v10, v12, v13\n"
  "v_cvt_pk_bf16_f32 v11, v14, v15\n"
  "v_mfma_f32_16x16x32_bf16 v[24:27], a[164:167], v[52:55], v[24:27]\n"
  "s_waitcnt vmcnt(14)\n"
  "ds_read_b128 a[96:99], v87 offset:6144\n"
  "ds_read_b128 a[100:103], v88 offset:6144\n"
  "ds_write_b128 v80, v[8:11] offset:1024\n"
  "v_pk_fma_f32 v[16:17], v[16:17], s[60:61], s[60:61] op_sel:[0,0,1] op_sel_hi:[1,0,1]\n"
  "v_pk_fma_f32 v[18:19], v[18:19], s[60:61], s[60:61] op_sel:[0,0,1] op_sel_hi:[1,0,1]\n"
  "v_pk_fma_f32 v[20:21], v[20:21], s[60:61], s[60:61] op_sel:[0,0,1] op_sel_hi:[1,0,1]\n"
  "v_pk_fma_f32 v[22:23], v[22:23], s[60:61], s[60:61] op_sel:[0,0,1] op_sel_hi:[1,0,1]\n"
  "v_mfma_f32_16x16x32_bf16 v[28:31], a[188:191], v[52:55], 0\n"
  "s_add_u32 m0, s59, 3840\n"
  "v_exp_f32 v16, v16\n"
  "v_mfma_f32_16x16x32_bf16 v[28:31], a[184:187], v[48:51], v[28:31]\n"
  "s_waitcnt lgkmcnt(3)\n"
  "buffer_load_dwordx4 v101, s[40:43], s57 offen offset:256 nt lds\n"
  "buffer_load_dwordx4 v102, s[40:43], s57 offen offset:1280 nt lds\n"
  "s_add_u32 s57, s57, 6144\n"
  "v_exp_f32 v17, v17\n"
  "v_mfma_f32_16x16x32_bf16 v[28:31], a[180:183], v[44:47], v[28:31]\n"
  "s_waitcnt vmcnt(14)\n"
  "ds_read_b128 a[104:107], v87 offset:8192\n"
  "ds_read_b128 a[108:111], v88 offset:8192\n"
  "v_exp_f32 v18, v18\n"
  "v_mfma_f32_16x16x32_bf16 v[28:31], a[176:179], v[40:43], v[28:31]\n"
  "s_add_u32 m0, s59, 5888\n"
  "v_exp_f32 v19, v19\n"
  "v_mfma_f32_16x16x32_bf16 v[28:31], a[172:175], v[36:39], v[28:31]\n"
  "s_waitcnt lgkmcnt(3)\n"
  "buffer_load_dwordx4 v101, s[40:43], s57 offen offset:256 nt lds\n"
  "buffer_load_dwordx4 v102, s[40:43], s57 offen offset:1280 nt lds\n"
  "v_cvt_pk_bf16_f32 v16, v16, v17\n"
  "v_cvt_pk_bf16_f32 v17, v18, v19\n"
  "v_mfma_f32_16x16x32_bf16 v[28:31], a[168:171], v[32:35], v[28:31]\n"
  "s_waitcnt vmcnt(14)\n"
  "ds_read_b128 a[112:115], v87 offset:10240\n"
  "ds_read_b128 a[116:119], v88 offset:10240\n"
  "ds_read_b128 v[64:67], v81 offset:2048\n"
  "ds_read_b128 v[68:71], v82 offset:2048\n"
  "v_mfma_f32_16x16x32_bf16 v[0:3], a[192:195], v[32:35], 0\n"
  "s_add_u32 m0, s59, 8064\n"
  "v_exp_f32 v20, v20\n"
  "v_mfma_f32_16x16x32_bf16 v[0:3], a[196:199], v[36:39], v[0:3]\n"
  "s_waitcnt lgkmcnt(4)\n"
  "buffer_load_dwordx4 v101, s[40:43], s57 offen offset:128 nt lds\n"
  "buffer_load_dwordx4 v102, s[40:43], s57 offen offset:1152 nt lds\n"
  "v_exp_f32 v21, v21\n"
  "v_mfma_f32_16x16x32_bf16 v[0:3], a[200:203], v[40:43], v[0:3]\n"
  "s_waitcnt vmcnt(14)\n"
  "ds_read_b128 a[136:139], v87 offset:12288\n"
  "ds_read_b128 a[140:143], v88 offset:12288\n"
  "v_exp_f32 v22, v22\n"
  "v_mfma_f32_16x16x32_bf16 v[0:3], a[204:207], v[44:47], v[0:3]\n"
  "s_add_u32 m0, s59, 10240\n"
  "v_exp_f32 v23, v23\n"
  "v_mfma_f32_16x16x32_bf16 v[0:3], a[208:211], v[48:51], v[0:3]\n"
  "s_waitcnt lgkmcnt(4)\n"
  "buffer_load_dwordx4 v101, s[40:43], s57 offen offset:0 nt lds\n"
  "buffer_load_dwordx4 v102, s[40:43], s57 offen offset:1024 nt lds\n"
  "s_add_u32 s57, s57, 6144\n"
  "v_cvt_pk_bf16_f32 v18, v20, v21\n"
  "v_cvt_pk_bf16_f32 v19, v22, v23\n"
  "v_mfma_f32_16x16x32_bf16 v[0:3], a[212:215], v[52:55], v[0:3]\n"
  "s_waitcnt vmcnt(14)\n"
  "ds_read_b128 a[128:131], v87 offset:14336\n"
  "ds_read_b128 a[132:135], v88 offset:14336\n"
  "ds_write_b128 v80, v[16:19] offset:2048\n"
  "v_pk_fma_f32 v[24:25], v[24:25], s[60:61], s[60:61] op_sel:[0,0,1] op_sel_hi:[1,0,1]\n"
  "v_pk_fma_f32 v[26:27], v[26:27], s[60:61], s[60:61] op_sel:[0,0,1] op_sel_hi:[1,0,1]\n"
  "v_pk_fma_f32 v[28:29], v[28:29], s[60:61], s[60:61] op_sel:[0,0,1] op_sel_hi:[1,0,1]\n"
  "v_pk_fma_f32 v[30:31], v[30:31], s[60:61], s[60:61] op_sel:[0,0,1] op_sel_hi:[1,0,1]\n"
  "v_mfma_f32_16x16x32_bf16 v[4:7], a[236:239], v[52:55], 0\n"
  "v_exp_f32 v24, v24\n"
  "v_mfma_f32_16x16x32_bf16 v[4:7], a[232:235], v[48:51], v[4:7]\n"
  "s_add_u32 m0, s59, 12288\n"
  "v_exp_f32 v25, v25\n"
  "v_mfma_f32_16x16x32_bf16 v[4:7], a[228:231], v[44:47], v[4:7]\n"
  "s_waitcnt lgkmcnt(3)\n"
  "buffer_load_dwordx4 v101, s[40:43], s57 offen offset:0 nt lds\n"
  "buffer_load_dwordx4 v102, s[40:43], s57 offen offset:1024 nt lds\n"
  "v_exp_f32 v26, v26\n"
  "v_mfma_f32_16x16x32_bf16 v[4:7], a[224:227], v[40:43], v[4:7]\n"
  "s_waitcnt vmcnt(14)\n"
  "ds_read_b128 a[120:123], v87 offset:16384\n"
  "ds_read_b128 a[124:127], v88 offset:16384\n"
  "v_exp_f32 v27, v27\n"
  "v_mfma_f32_16x16x32_bf16 v[4:7], a[220:223], v[36:39], v[4:7]\n"
  "s_add_u32 m0, s59, 14208\n"
  "v_cvt_pk_bf16_f32 v24, v24, v25\n"
  "v_cvt_pk_bf16_f32 v25, v26, v27\n"
  "v_mfma_f32_16x16x32_bf16 v[4:7], a[216:219], v[32:35], v[4:7]\n"
  "s_waitcnt lgkmcnt(3)\n"
  "buffer_load_dwordx4 v101, s[40:43], s57 offen offset:128 nt lds\n"
  "buffer_load_dwordx4 v102, s[40:43], s57 offen offset:1152 nt lds\n"
  "ds_read_b128 v[72:75], v81 offset:4096\n"
  "ds_read_b128 v[76:79], v82 offset:4096\n"
  "v_mfma_f32_16x16x32_bf16 v[8:11], a[240:243], v[32:35], 0\n"
  "s_waitcnt vmcnt(14)\n"
  "ds_read_b128 a[144:147], v87 offset:0\n"
  "ds_read_b128 a[148:151], v88 offset:0\n"
  "v_exp_f32 v28, v28\n"
  "v_mfma_f32_16x16x32_bf16 v[8:11], a[244:247], v[36:39], v[8:11]\n"
  "s_add_u32 m0, s59, 16128\n"
  "v_exp_f32 v29, v29\n"
  "v_mfma_f32_16x16x32_bf16 v[8:11], a[248:251], v[40:43], v[8:11]\n"
  "s_waitcnt lgkmcnt(4)\n"
  "buffer_load_dwordx4 v101, s[40:43], s57 offen offset:256 nt lds\n"
  "buffer_load_dwordx4 v102, s[40:43], s57 offen offset:1280 nt lds\n"
  "s_add_u32 s57, s57, 6144\n"
  "v_exp_f32 v30, v30\n"
  "v_mfma_f32_16x16x32_bf16 v[8:11], a[252:255], v[44:47], v[8:11]\n"
  "s_waitcnt vmcnt(14)\n"
  "ds_read_b128 a[152:155], v87 offset:2048\n"
  "ds_read_b128 a[156:159], v88 offset:2048\n"
  "v_exp_f32 v31, v31\n"
  "v_mfma_f32_16x16x32_bf16 v[8:11], v[128:131], v[48:51], v[8:11]\n"
  "s_add_u32 m0, s59, -256\n"
  "v_cvt_pk_bf16_f32 v26, v28, v29\n"
  "v_cvt_pk_bf16_f32 v27, v30, v31\n"
  "v_mfma_f32_16x16x32_bf16 v[8:11], v[132:135], v[52:55], v[8:11]\n"
  "s_waitcnt lgkmcnt(2)\n"
  "buffer_load_dwordx4 v101, s[40:43], s57 offen offset:256 nt lds\n"
  "buffer_load_dwordx4 v102, s[40:43], s57 offen offset:1280 nt lds\n"
  "ds_write_b128 v80, v[24:27] offset:3072\n"
  "v_pk_fma_f32 v[0:1], v[0:1], s[60:61], s[60:61] op_sel:[0,0,1] op_sel_hi:[1,0,1]\n"
  "v_pk_fma_f32 v[2:3], v[2:3], s[60:61], s[60:61] op_sel:[0,0,1] op_sel_hi:[1,0,1]\n"
  "v_pk_fma_f32 v[4:5], v[4:5], s[60:61], s[60:61] op_sel:[0,0,1] op_sel_hi:[1,0,1]\n"
  "v_pk_fma_f32 v[6:7], v[6:7], s[60:61], s[60:61] op_sel:[0,0,1] op_sel_hi:[1,0,1]\n"
  "v_mfma_f32_16x16x32_bf16 v[12:15], v[156:159], v[52:55], 0\n"
  "s_waitcnt vmcnt(14)\n"
  "ds_read_b128 a[160:163], v87 offset:4096\n"
  "ds_read_b128 a[164:167], v88 offset:4096\n"
  "v_exp_f32 v0, v0\n"
  "v_mfma_f32_16x16x32_bf16 v[12:15], v[152:155], v[48:51], v[12:15]\n"
  "s_add_u32 m0, s59, 1920\n"
  "v_exp_f32 v1, v1\n"
  "v_mfma_f32_16x16x32_bf16 v[12:15], v[148:151], v[44:47], v[12:15]\n"
  "s_waitcnt lgkmcnt(3)\n"
  "buffer_load_dwordx4 v101, s[40:43], s57 offen offset:128 nt lds\n"
  "buffer_load_dwordx4 v102, s[40:43], s57 offen offset:1152 nt lds\n"
  "v_exp_f32 v2, v2\n"
  "v_mfma_f32_16x16x32_bf16 v[12:15], v[144:147], v[40:43], v[12:15]\n"
  "s_waitcnt vmcnt(14)\n"
  "ds_read_b128 a[184:187], v87 offset:6144\n"
  "ds_read_b128 a[188:191], v88 offset:6144\n"
  "v_exp_f32 v3, v3\n"
  "v_mfma_f32_16x16x32_bf16 v[12:15], v[140:143], v[36:39], v[12:15]\n"
  "v_cvt_pk_bf16_f32 v0, v0, v1\n"
  "v_cvt_pk_bf16_f32 v1, v2, v3\n"
  "v_mfma_f32_16x16x32_bf16 v[12:15], v[136:139], v[32:35], v[12:15]\n"
  "s_add_u32 m0, s59, 4096\n"
  "v_mfma_f32_16x16x32_bf16 v[16:19], v[160:163], v[32:35], 0\n"
  "s_waitcnt lgkmcnt(2)\n"
  "buffer_load_dwordx4 v101, s[40:43], s57 offen offset:0 nt lds\n"
  "buffer_load_dwordx4 v102, s[40:43], s57 offen offset:1024 nt lds\n"
  "s_add_u32 s57, s57, 6144\n"
  "v_exp_f32 v4, v4\n"
  "v_mfma_f32_16x16x32_bf16 v[16:19], v[164:167], v[36:39], v[16:19]\n"
  "s_waitcnt vmcnt(14)\n"
  "ds_read_b128 a[176:179], v87 offset:8192\n"
  "ds_read_b128 a[180:183], v88 offset:8192\n"
  "v_exp_f32 v5, v5\n"
  "v_mfma_f32_16x16x32_bf16 v[16:19], v[168:171], v[40:43], v[16:19]\n"
  "s_add_u32 m0, s59, 6144\n"
  "v_exp_f32 v6, v6\n"
  "v_mfma_f32_16x16x32_bf16 v[16:19], v[172:175], v[44:47], v[16:19]\n"
  "s_waitcnt lgkmcnt(2)\n"
  "buffer_load_dwordx4 v101, s[40:43], s57 offen offset:0 nt lds\n"
  "buffer_load_dwordx4 v102, s[40:43], s57 offen offset:1024 nt lds\n"
  "v_exp_f32 v7, v7\n"
  "v_mfma_f32_16x16x32_bf16 v[16:19], v[176:179], v[48:51], v[16:19]\n"
  "s_waitcnt vmcnt(14)\n"
  "ds_read_b128 a[168:171], v87 offset:10240\n"
  "ds_read_b128 a[172:175], v88 offset:10240\n"
  "v_cvt_pk_bf16_f32 v2, v4, v5\n"
  "v_cvt_pk_bf16_f32 v3, v6, v7\n"
  "v_mfma_f32_16x16x32_bf16 v[16:19], v[180:183], v[52:55], v[16:19]\n"
  "s_add_u32 m0, s59, 8064\n"
  "ds_write_b128 v80, v[0:3] offset:4096\n"
  "v_pk_fma_f32 v[8:9], v[8:9], s[60:61], s[60:61] op_sel:[0,0,1] op_sel_hi:[1,0,1]\n"
  "v_pk_fma_f32 v[10:11], v[10:11], s[60:61], s[60:61] op_sel:[0,0,1] op_sel_hi:[1,0,1]\n"
  "v_pk_fma_f32 v[12:13], v[12:13], s[60:61], s[60:61] op_sel:[0,0,1] op_sel_hi:[1,0,1]\n"
  "v_pk_fma_f32 v[14:15], v[14:15], s[60:61], s[60:61] op_sel:[0,0,1] op_sel_hi:[1,0,1]\n"
  "v_mfma_f32_16x16x32_bf16 v[20:23], v[204:207], v[52:55], 0\n"
  "s_waitcnt lgkmcnt(3)\n"
  "buffer_load_dwordx4 v101, s[40:43], s57 offen offset:128 nt lds\n"
  "buffer_load_dwordx4 v102, s[40:43], s57 offen offset:1152 nt lds\n"
  "v_exp_f32 v8, v8\n"
  "v_mfma_f32_16x16x32_bf16 v[20:23], v[200:203], v[48:51], v[20:23]\n"
  "s_waitcnt vmcnt(14)\n"
  "ds_read_b128 a[192:195], v87 offset:12288\n"
  "ds_read_b128 a[196:199], v88 offset:12288\n"
  "v_exp_f32 v9, v9\n"
  "v_mfma_f32_16x16x32_bf16 v[20:23], v[196:199], v[44:47], v[20:23]\n"
  "s_add_u32 m0, s59, 9984\n"
  "v_exp_f32 v10, v10\n"
  "v_mfma_f32_16x16x32_bf16 v[20:23], v[192:195], v[40:43], v[20:23]\n"
  "s_waitcnt lgkmcnt(3)\n"
  "buffer_load_dwordx4 v101, s[40:43], s57 offen offset:256 nt lds\n"
  "buffer_load_dwordx4 v102, s[40:43], s57 offen offset:1280 nt lds\n"
  "s_add_u32 s57, s57, 6144\n"
  "v_exp_f32 v11, v11\n"
  "v_mfma_f32_16x16x32_bf16 v[20:23], v[188:191], v[36:39], v[20:23]\n"
  "s_waitcnt vmcnt(14)\n"
  "ds_read_b128 a[200:203], v87 offset:14336\n"
  "ds_read_b128 a[204:207], v88 offset:14336\n"
  "v_cvt_pk_bf16_f32 v8, v8, v9\n"
  "v_cvt_pk_bf16_f32 v9, v10, v11\n"
  "v_mfma_f32_16x16x32_bf16 v[20:23], v[184:187], v[32:35], v[20:23]\n"
  "s_add_u32 m0, s59, 12032\n"
  "v_mfma_f32_16x16x32_bf16 v[24:27], v[208:211], v[32:35], 0\n"
  "s_waitcnt lgkmcnt(2)\n"
  "buffer_load_dwordx4 v101, s[40:43], s57 offen offset:256 nt lds\n"
  "buffer_load_dwordx4 v102, s[40:43], s57 offen offset:1280 nt lds\n"
  "v_exp_f32 v12, v12\n"
  "v_mfma_f32_16x16x32_bf16 v[24:27], v[212:215], v[36:39], v[24:27]\n"
  "s_waitcnt vmcnt(14)\n"
  "ds_read_b128 a[208:211], v87 offset:16384\n"
  "ds_read_b128 a[212:215], v88 offset:16384\n"
  "v_exp_f32 v13, v13\n"
  "v_mfma_f32_16x16x32_bf16 v[24:27], v[216:219], v[40:43], v[24:27]\n"
  "v_exp_f32 v14, v14\n"
  "v_mfma_f32_16x16x32_bf16 v[24:27], v[220:223], v[44:47], v[24:27]\n"
  "s_add_u32 m0, s59, 14208\n"
  "v_exp_f32 v15, v15\n"
  "v_mfma_f32_16x16x32_bf16 v[24:27], v[224:227], v[48:51], v[24:27]\n"
  "s_waitcnt lgkmcnt(2)\n"
  "buffer_load_dwordx4 v101, s[40:43], s57 offen offset:128 nt lds\n"
  "buffer_load_dwordx4 v102, s[40:43], s57 offen offset:1152 nt lds\n"
  "v_cvt_pk_bf16_f32 v10, v12, v13\n"
  "v_cvt_pk_bf16_f32 v11, v14, v15\n"
  "v_mfma_f32_16x16x32_bf16 v[24:27], v[228:231], v[52:55], v[24:27]\n"
  "s_waitcnt vmcnt(14)\n"
  "ds_read_b128 a[232:235], v87 offset:0\n"
  "ds_read_b128 a[236:239], v88 offset:0\n"
  "ds_write_b128 v80, v[8:11] offset:5120\n"
  "v_pk_fma_f32 v[16:17], v[16:17], s[60:61], s[60:61] op_sel:[0,0,1] op_sel_hi:[1,0,1]\n"
  "v_pk_fma_f32 v[18:19], v[18:19], s[60:61], s[60:61] op_sel:[0,0,1] op_sel_hi:[1,0,1]\n"
  "v_pk_fma_f32 v[20:21], v[20:21], s[60:61], s[60:61] op_sel:[0,0,1] op_sel_hi:[1,0,1]\n"
  "v_pk_fma_f32 v[22:23], v[22:23], s[60:61], s[60:61] op_sel:[0,0,1] op_sel_hi:[1,0,1]\n"
  "v_mfma_f32_16x16x32_bf16 v[28:31], v[252:255], v[52:55], 0\n"
  "s_add_u32 m0, s59, 16384\n"
  "v_exp_f32 v16, v16\n"
  "v_mfma_f32_16x16x32_bf16 v[28:31], v[248:251], v[48:51], v[28:31]\n"
  "s_waitcnt lgkmcnt(3)\n"
  "buffer_load_dwordx4 v101, s[40:43], s57 offen offset:0 nt lds\n"
  "buffer_load_dwordx4 v102, s[40:43], s57 offen offset:1024 nt lds\n"
  "s_add_u32 s57, s57, 6144\n"
  "v_exp_f32 v17, v17\n"
  "v_mfma_f32_16x16x32_bf16 v[28:31], v[244:247], v[44:47], v[28:31]\n"
  "s_waitcnt vmcnt(14)\n"
  "ds_read_b128 a[224:227], v87 offset:2048\n"
  "ds_read_b128 a[228:231], v88 offset:2048\n"
  "v_exp_f32 v18, v18\n"
  "v_mfma_f32_16x16x32_bf16 v[28:31], v[240:243], v[40:43], v[28:31]\n"
  "s_add_u32 m0, s59, 0\n"
  "v_exp_f32 v19, v19\n"
  "v_mfma_f32_16x16x32_bf16 v[28:31], v[236:239], v[36:39], v[28:31]\n"
  "s_waitcnt lgkmcnt(3)\n"
  "buffer_load_dwordx4 v101, s[40:43], s57 offen offset:0 nt lds\n"
  "buffer_load_dwordx4 v102, s[40:43], s57 offen offset:1024 nt lds\n"
  "v_cvt_pk_bf16_f32 v16, v16, v17\n"
  "v_cvt_pk_bf16_f32 v17, v18, v19\n"
  "v_mfma_f32_16x16x32_bf16 v[28:31], v[232:235], v[32:35], v[28:31]\n"
  "s_waitcnt vmcnt(14)\n"
  "ds_read_b128 a[216:219], v87 offset:4096\n"
  "ds_read_b128 a[220:223], v88 offset:4096\n"
  "v_mfma_f32_16x16x32_bf16 v[0:3], a[0:3], v[56:59], 0\n"
  "s_add_u32 s66, s66, 6144\n"
  "s_cmp_eq_u32 s66, 86016\n"
  "s_cselect_b32 s66, 49152, s66\n"
  "s_add_u32 m0, s59, 1920\n"
  "v_exp_f32 v20, v20\n"
  "v_mfma_f32_16x16x32_bf16 v[0:3], a[4:7], v[60:63], v[0:3]\n"
  "s_waitcnt lgkmcnt(2)\n"
  "buffer_load_dwordx4 v101, s[40:43], s57 offen offset:128 nt lds\n"
  "buffer_load_dwordx4 v102, s[40:43], s57 offen offset:1152 nt lds\n"
  "v_exp_f32 v21, v21\n"
  "v_mfma_f32_16x16x32_bf16 v[0:3], a[8:11], v[64:67], v[0:3]\n"
  "s_waitcnt vmcnt(14)\n"
  "ds_read_b128 a[240:243], v87 offset:6144\n"
  "ds_read_b128 a[244:247], v88 offset:6144\n"
  "v_exp_f32 v22, v22\n"
  "v_mfma_f32_16x16x32_bf16 v[0:3], a[12:15], v[68:71], v[0:3]\n"
  "s_add_u32 m0, s59, 3840\n"
  "v_exp_f32 v23, v23\n"
  "v_mfma_f32_16x16x32_bf16 v[0:3], a[16:19], v[72:75], v[0:3]\n"
  "s_waitcnt lgkmcnt(2)\n"
  "buffer_load_dwordx4 v101, s[40:43], s57 offen offset:256 nt lds\n"
  "buffer_load_dwordx4 v102, s[40:43], s57 offen offset:1280 nt lds\n"
  "s_add_u32 s57, s57, 6144\n"
  "v_cvt_pk_bf16_f32 v18, v20, v21\n"
  "v_cvt_pk_bf16_f32 v19, v22, v23\n"
  "v_mfma_f32_16x16x32_bf16 v[0:3], a[20:23], v[76:79], v[0:3]\n"
  "s_waitcnt vmcnt(14)\n"
  "ds_read_b128 a[248:251], v87 offset:8192\n"
  "ds_read_b128 a[252:255], v88 offset:8192\n"
  "ds_write_b128 v80, v[16:19] offset:6144\n"
  "v_pk_fma_f32 v[24:25], v[24:25], s[60:61], s[60:61] op_sel:[0,0,1] op_sel_hi:[1,0,1]\n"
  "v_pk_fma_f32 v[26:27], v[26:27], s[60:61], s[60:61] op_sel:[0,0,1] op_sel_hi:[1,0,1]\n"
  "v_pk_fma_f32 v[28:29], v[28:29], s[60:61], s[60:61] op_sel:[0,0,1] op_sel_hi:[1,0,1]\n"
  "v_pk_fma_f32 v[30:31], v[30:31], s[60:61], s[60:61] op_sel:[0,0,1] op_sel_hi:[1,0,1]\n"
  "v_mfma_f32_16x16x32_bf16 v[4:7], a[44:47], v[76:79], 0\n"
  "v_exp_f32 v24, v24\n"
  "v_mfma_f32_16x16x32_bf16 v[4:7], a[40:43], v[72:75], v[4:7]\n"
  "s_add_u32 m0, s59, 5888\n"
  "v_exp_f32 v25, v25\n"
  "v_mfma_f32_16x16x32_bf16 v[4:7], a[36:39], v[68:71], v[4:7]\n"
  "s_waitcnt lgkmcnt(3)\n"
  "buffer_load_dwordx4 v101, s[40:43], s57 offen offset:256 nt lds\n"
  "buffer_load_dwordx4 v102, s[40:43], s57 offen offset:1280 nt lds\n"
  "v_exp_f32 v26, v26\n"
  "v_mfma_f32_16x16x32_bf16 v[4:7], a[32:35], v[64:67], v[4:7]\n"
  "s_waitcnt vmcnt(14)\n"
  "ds_read_b128 v[128:131], v87 offset:10240\n"
  "ds_read_b128 v[132:135], v88 offset:10240\n"
  "v_exp_f32 v27, v27\n"
  "v_mfma_f32_16x16x32_bf16 v[4:7], a[28:31], v[60:63], v[4:7]\n"
  "s_add_u32 m0, s59, 8064\n"
  "v_cvt_pk_bf16_f32 v24, v24, v25\n"
  "v_cvt_pk_bf16_f32 v25, v26, v27\n"
  "v_mfma_f32_16x16x32_bf16 v[4:7], a[24:27], v[56:59], v[4:7]\n"
  "s_waitcnt lgkmcnt(3)\n"
  "buffer_load_dwordx4 v101, s[40:43], s57 offen offset:128 nt lds\n"
  "buffer_load_dwordx4 v102, s[40:43], s57 offen offset:1152 nt lds\n"
  "v_add_u32 v81, s66, v83\n"
  "v_add_u32 v82, s66, v84\n"
  "v_mfma_f32_16x16x32_bf16 v[8:11], a[48:51], v[56:59], 0\n"
  "s_waitcnt vmcnt(14)\n"
  "ds_read_b128 v[152:155], v87 offset:12288\n"
  "ds_read_b128 v[156:159], v88 offset:12288\n"
  "v_exp_f32 v28, v28\n"
  "v_mfma_f32_16x16x32_bf16 v[8:11], a[52:55], v[60:63], v[8:11]\n"
  "s_add_u32 m0, s59, 10240\n"
  "v_exp_f32 v29, v29\n"
  "v_mfma_f32_16x16x32_bf16 v[8:11], a[56:59], v[64:67], v[8:11]\n"
  "s_waitcnt lgkmcnt(2)\n"
  "buffer_load_dwordx4 v101, s[40:43], s57 offen offset:0 nt lds\n"
  "buffer_load_dwordx4 v102, s[40:43], s57 offen offset:1024 nt lds\n"
  "s_add_u32 s57, s57, 6144\n"
  "v_exp_f32 v30, v30\n"
  "v_mfma_f32_16x16x32_bf16 v[8:11], a[60:63], v[68:71], v[8:11]\n"
  "s_waitcnt vmcnt(14)\n"
  "ds_read_b128 v[144:147], v87 offset:14336\n"
  "ds_read_b128 v[148:151], v88 offset:14336\n"
  "v_exp_f32 v31, v31\n"
  "v_mfma_f32_16x16x32_bf16 v[8:11], a[64:67], v[72:75], v[8:11]\n"
  "s_add_u32 m0, s59, 12288\n"
  "v_cvt_pk_bf16_f32 v26, v28, v29\n"
  "v_cvt_pk_bf16_f32 v27, v30, v31\n"
  "v_mfma_f32_16x16x32_bf16 v[8:11], a[68:71], v[76:79], v[8:11]\n"
  "s_waitcnt lgkmcnt(2)\n"
  "buffer_load_dwordx4 v101, s[40:43], s57 offen offset:0 nt lds\n"
  "buffer_load_dwordx4 v102, s[40:43], s57 offen offset:1024 nt lds\n"
  "ds_write_b128 v80, v[24:27] offset:7168\n"
  "v_pk_fma_f32 v[0:1], v[0:1], s[60:61], s[60:61] op_sel:[0,0,1] op_sel_hi:[1,0,1]\n"
  "v_pk_fma_f32 v[2:3], v[2:3], s[60:61], s[60:61] op_sel:[0,0,1] op_sel_hi:[1,0,1]\n"
  "v_pk_fma_f32 v[4:5], v[4:5], s[60:61], s[60:61] op_sel:[0,0,1] op_sel_hi:[1,0,1]\n"
  "v_pk_fma_f32 v[6:7], v[6:7], s[60:61], s[60:61] op_sel:[0,0,1] op_sel_hi:[1,0,1]\n"
  "v_mfma_f32_16x16x32_bf16 v[12:15], a[92:95], v[76:79], 0\n"
  "s_waitcnt vmcnt(14)\n"
  "ds_read_b128 v[136:139], v87 offset:16384\n"
  "ds_read_b128 v[140:143], v88 offset:16384\n"
  "v_exp_f32 v0, v0\n"
  "v_mfma_f32_16x16x32_bf16 v[12:15], a[88:91], v[72:75], v[12:15]\n"
  "s_add_u32 m0, s59, 14208\n"
  "v_exp_f32 v1, v1\n"
  "v_mfma_f32_16x16x32_bf16 v[12:15], a[84:87], v[68:71], v[12:15]\n"
  "s_waitcnt lgkmcnt(3)\n"
  "buffer_load_dwordx4 v101, s[40:43], s57 offen offset:128 nt lds\n"
  "buffer_load_dwordx4 v102, s[40:43], s57 offen offset:1152 nt lds\n"
  "v_exp_f32 v2, v2\n"
  "v_mfma_f32_16x16x32_bf16 v[12:15], a[80:83], v[64:67], v[12:15]\n"
  "s_waitcnt vmcnt(14)\n"
  "ds_read_b128 v[160:163], v87 offset:0\n"
  "ds_read_b128 v[164:167], v88 offset:0\n"
  "v_exp_f32 v3, v3\n"
  "v_mfma_f32_16x16x32_bf16 v[12:15], a[76:79], v[60:63], v[12:15]\n"
  "v_cvt_pk_bf16_f32 v0, v0, v1\n"
  "v_cvt_pk_bf16_f32 v1, v2, v3\n"
  "v_mfma_f32_16x16x32_bf16 v[12:15], a[72:75], v[56:59], v[12:15]\n"
  "s_add_u32 m0, s59, 16128\n"
  "s_waitcnt lgkmcnt(4)\n"
  "s_barrier\n"
  "v_xor_b32 v80, 0x4000, v80\n"
  "v_mfma_f32_16x16x32_bf16 v[16:19], a[96:99], v[56:59], 0\n"
  "s_waitcnt lgkmcnt(2)\n"
  "buffer_load_dwordx4 v101, s[40:43], s57 offen offset:256 nt lds\n"
  "buffer_load_dwordx4 v102, s[40:43], s57 offen offset:1280 nt lds\n"
  "s_add_u32 s57, s57, 6144\n"
  "v_exp_f32 v4, v4\n"
  "v_mfma_f32_16x16x32_bf16 v[16:19], a[100:103], v[60:63], v[16:19]\n"
  "s_waitcnt vmcnt(14)\n"
  "ds_read_b128 v[168:171], v87 offset:2048\n"
  "ds_read_b128 v[172:175], v88 offset:2048\n"
  "v_exp_f32 v5, v5\n"
  "v_mfma_f32_16x16x32_bf16 v[16:19], a[104:107], v[64:67], v[16:19]\n"
  "s_add_u32 m0, s59, -256\n"
  "v_exp_f32 v6, v6\n"
  "v_mfma_f32_16x16x32_bf16 v[16:19], a[108:111], v[68:71], v[16:19]\n"
  "s_waitcnt lgkmcnt(2)\n"
  "buffer_load_dwordx4 v101, s[40:43], s57 offen offset:256 nt lds\n"
  "buffer_load_dwordx4 v102, s[40:43], s57 offen offset:1280 nt lds\n"
  "v_exp_f32 v7, v7\n"
  "v_mfma_f32_16x16x32_bf16 v[16:19], a[112:115], v[72:75], v[16:19]\n"
  "s_waitcnt vmcnt(14)\n"
  "ds_read_b128 v[176:179], v87 offset:4096\n"
  "ds_read_b128 v[180:183], v88 offset:4096\n"
  "v_cvt_pk_bf16_f32 v2, v4, v5\n"
  "v_cvt_pk_bf16_f32 v3, v6, v7\n"
  "v_mfma_f32_16x16x32_bf16 v[16:19], a[116:119], v[76:79], v[16:19]\n"
  "s_add_u32 m0, s59, 1920\n"
  "ds_write_b128 v80, v[0:3] offset:0\n"
  "v_pk_fma_f32 v[8:9], v[8:9], s[60:61], s[60:61] op_sel:[0,0,1] op_sel_hi:[1,0,1]\n"
  "v_pk_fma_f32 v[10:11], v[10:11], s[60:61], s[60:61] op_sel:[0,0,1] op_sel_hi:[1,0,1]\n"
  "v_pk_fma_f32 v[12:13], v[12:13], s[60:61], s[60:61] op_sel:[0,0,1] op_sel_hi:[1,0,1]\n"
  "v_pk_fma_f32 v[14:15], v[14:15], s[60:61], s[60:61] op_sel:[0,0,1] op_sel_hi:[1,0,1]\n"
  "v_mfma_f32_16x16x32_bf16 v[20:23], a[140:143], v[76:79], 0\n"
  "s_waitcnt lgkmcnt(3)\n"
  "buffer_load_dwordx4 v101, s[40:43], s57 offen offset:128 nt lds\n"
  "buffer_load_dwordx4 v102, s[40:43], s57 offen offset:1152 nt lds\n"
  "v_exp_f32 v8, v8\n"
  "v_mfma_f32_16x16x32_bf16 v[20:23], a[136:139], v[72:75], v[20:23]\n"
  "s_waitcnt vmcnt(14)\n"
  "ds_read_b128 v[200:203], v87 offset:6144\n"
  "ds_read_b128 v[204:207], v88 offset:6144\n"
  "v_exp_f32 v9, v9\n"
  "v_mfma_f32_16x16x32_bf16 v[20:23], a[132:135], v[68:71], v[20:23]\n"
  "s_add_u32 m0, s59, 4096\n"
  "v_exp_f32 v10, v10\n"
  "v_mfma_f32_16x16x32_bf16 v[20:23], a[128:131], v[64:67], v[20:23]\n"
  "s_waitcnt lgkmcnt(3)\n"
  "buffer_load_dwordx4 v101, s[40:43], s57 offen offset:0 nt lds\n"
  "buffer_load_dwordx4 v102, s[40:43], s57 offen offset:1024 nt lds\n"
  "s_add_u32 s57, s57, 6144\n"
  "v_exp_f32 v11, v11\n"
  "v_mfma_f32_16x16x32_bf16 v[20:23], a[124:127], v[60:63], v[20:23]\n"
  "s_waitcnt vmcnt(14)\n"
  "ds_read_b128 v[192:195], v87 offset:8192\n"
  "ds_read_b128 v[196:199], v88 offset:8192\n"
  "v_cvt_pk_bf16_f32 v8, v8, v9\n"
  "v_cvt_pk_bf16_f32 v9, v10, v11\n"
  "v_mfma_f32_16x16x32_bf16 v[20:23], a[120:123], v[56:59], v[20:23]\n"
  "ds_read_b128 v[32:35], v81 offset:0\n"
  "ds_read_b128 v[36:39], v82 offset:0\n"
  "v_mfma_f32_16x16x32_bf16 v[24:27], a[144:147], v[56:59], 0\n"
  "v_exp_f32 v12, v12\n"
  "v_mfma_f32_16x16x32_bf16 v[24:27], a[148:151], v[60:63], v[24:27]\n"
  "s_waitcnt vmcnt(12)\n"
  "ds_read_b128 v[184:187], v87 offset:10240\n"
  "ds_read_b128 v[188:191], v88 offset:10240\n"
  "v_exp_f32 v13, v13\n"
  "v_mfma_f32_16x16x32_bf16 v[24:27], a[152:155], v[64:67], v[24:27]\n"
  "v_exp_f32 v14, v14\n"
  "v_mfma_f32_16x16x32_bf16 v[24:27], a[156:159], v[68:71], v[24:27]\n"
  "v_exp_f32 v15, v15\n"
  "v_mfma_f32_16x16x32_bf16 v[24:27], a[160:163], v[72:75], v[24:27]\n"
  "v_cvt_pk_bf16_f32 v10, v12, v13\n"
  "v_cvt_pk_bf16_f32 v11, v14, v15\n"
  "v_mfma_f32_16x16x32_bf16 v[24:27], a[164:167], v[76:79], v[24:27]\n"
  "s_waitcnt vmcnt(10)\n"
  "ds_read_b128 v[208:211], v87 offset:12288\n"
  "ds_read_b128 v[212:215], v88 offset:12288\n"
  "ds_write_b128 v80, v[8:11] offset:1024\n"
  "v_pk_fma_f32 v[16:17], v[16:17], s[60:61], s[60:61] op_sel:[0,0,1] op_sel_hi:[1,0,1]\n"
  "v_pk_fma_f32 v[18:19], v[18:19], s[60:61], s[60:61] op_sel:[0,0,1] op_sel_hi:[1,0,1]\n"
  "v_pk_fma_f32 v[20:21], v[20:21], s[60:61], s[60:61] op_sel:[0,0,1] op_sel_hi:[1,0,1]\n"
  "v_pk_fma_f32 v[22:23], v[22:23], s[60:61], s[60:61] op_sel:[0,0,1] op_sel_hi:[1,0,1]\n"
  "v_mfma_f32_16x16x32_bf16 v[28:31], a[188:191], v[76:79], 0\n"
  "v_exp_f32 v16, v16\n"
  "v_mfma_f32_16x16x32_bf16 v[28:31], a[184:187], v[72:75], v[28:31]\n"
  "v_exp_f32 v17, v17\n"
  "v_mfma_f32_16x16x32_bf16 v[28:31], a[180:183], v[68:71], v[28:31]\n"
  "s_waitcnt vmcnt(8)\n"
  "ds_read_b128 v[216:219], v87 offset:14336\n"
  "ds_read_b128 v[220:223], v88 offset:14336\n"
  "v_exp_f32 v18, v18\n"
  "v_mfma_f32_16x16x32_bf16 v[28:31], a[176:179], v[64:67], v[28:31]\n"
  "v_exp_f32 v19, v19\n"
  "v_mfma_f32_16x16x32_bf16 v[28:31], a[172:175], v[60:63], v[28:31]\n"
  "v_cvt_pk_bf16_f32 v16, v16, v17\n"
  "v_cvt_pk_bf16_f32 v17, v18, v19\n"
  "v_mfma_f32_16x16x32_bf16 v[28:31], a[168:171], v[56:59], v[28:31]\n"
  "s_waitcnt vmcnt(6)\n"
  "ds_read_b128 v[224:227], v87 offset:16384\n"
  "ds_read_b128 v[228:231], v88 offset:16384\n"
  "ds_read_b128 v[40:43], v81 offset:2048\n"
  "ds_read_b128 v[44:47], v82 offset:2048\n"
  "v_mfma_f32_16x16x32_bf16 v[0:3], a[192:195], v[56:59], 0\n"
  "v_exp_f32 v20, v20\n"
  "v_mfma_f32_16x16x32_bf16 v[0:3], a[196:199], v[60:63], v[0:3]\n"
  "v_exp_f32 v21, v21\n"
  "v_mfma_f32_16x16x32_bf16 v[0:3], a[200:203], v[64:67], v[0:3]\n"
  "s_waitcnt vmcnt(4)\n"
  "ds_read_b128 v[248:251], v87 offset:0\n"
  "ds_read_b128 v[252:255], v88 offset:0\n"
  "v_exp_f32 v22, v22\n"
  "v_mfma_f32_16x16x32_bf16 v[0:3], a[204:207], v[68:71], v[0:3]\n"
  "v_exp_f32 v23, v23\n"
  "v_mfma_f32_16x16x32_bf16 v[0:3], a[208:211], v[72:75], v[0:3]\n"
  "v_cvt_pk_bf16_f32 v18, v20, v21\n"
  "v_cvt_pk_bf16_f32 v19, v22, v23\n"
  "v_mfma_f32_16x16x32_bf16 v[0:3], a[212:215], v[76:79], v[0:3]\n"
  "s_waitcnt vmcnt(2)\n"
  "ds_read_b128 v[240:243], v87 offset:2048\n"
  "ds_read_b128 v[244:247], v88 offset:2048\n"
  "ds_write_b128 v80, v[16:19] offset:2048\n"
  "v_pk_fma_f32 v[24:25], v[24:25], s[60:61], s[60:61] op_sel:[0,0,1] op_sel_hi:[1,0,1]\n"
  "v_pk_fma_f32 v[26:27], v[26:27], s[60:61], s[60:61] op_sel:[0,0,1] op_sel_hi:[1,0,1]\n"
  "v_pk_fma_f32 v[28:29], v[28:29], s[60:61], s[60:61] op_sel:[0,0,1] op_sel_hi:[1,0,1]\n"
  "v_pk_fma_f32 v[30:31], v[30:31], s[60:61], s[60:61] op_sel:[0,0,1] op_sel_hi:[1,0,1]\n"
  "v_mfma_f32_16x16x32_bf16 v[4:7], a[236:239], v[76:79], 0\n"
  "v_exp_f32 v24, v24\n"
  "v_mfma_f32_16x16x32_bf16 v[4:7], a[232:235], v[72:75], v[4:7]\n"
  "v_exp_f32 v25, v25\n"
  "v_mfma_f32_16x16x32_bf16 v[4:7], a[228:231], v[68:71], v[4:7]\n"
  "v_exp_f32 v26, v26\n"
  "v_mfma_f32_16x16x32_bf16 v[4:7], a[224:227], v[64:67], v[4:7]\n"
  "s_waitcnt vmcnt(0)\n"
  "ds_read_b128 v[232:235], v87 offset:4096\n"
  "ds_read_b128 v[236:239], v88 offset:4096\n"
  "v_exp_f32 v27, v27\n"
  "v_mfma_f32_16x16x32_bf16 v[4:7], a[220:223], v[60:63], v[4:7]\n"
  "v_cvt_pk_bf16_f32 v24, v24, v25\n"
  "v_cvt_pk_bf16_f32 v25, v26, v27\n"
  "v_mfma_f32_16x16x32_bf16 v[4:7], a[216:219], v[56:59], v[4:7]\n"
  "ds_read_b128 v[48:51], v81 offset:4096\n"
  "ds_read_b128 v[52:55], v82 offset:4096\n"
  "v_mfma_f32_16x16x32_bf16 v[8:11], a[240:243], v[56:59], 0\n"
  "v_exp_f32 v28, v28\n"
  "v_mfma_f32_16x16x32_bf16 v[8:11], a[244:247], v[60:63], v[8:11]\n"
  "v_exp_f32 v29, v29\n"
  "v_mfma_f32_16x16x32_bf16 v[8:11], a[248:251], v[64:67], v[8:11]\n"
  "v_exp_f32 v30, v30\n"
  "v_mfma_f32_16x16x32_bf16 v[8:11], a[252:255], v[68:71], v[8:11]\n"
  "v_exp_f32 v31, v31\n"
  "v_mfma_f32_16x16x32_bf16 v[8:11], v[128:131], v[72:75], v[8:11]\n"
  "v_cvt_pk_bf16_f32 v26, v28, v29\n"
  "v_cvt_pk_bf16_f32 v27, v30, v31\n"
  "v_mfma_f32_16x16x32_bf16 v[8:11], v[132:135], v[76:79], v[8:11]\n"
  "ds_write_b128 v80, v[24:27] offset:3072\n"
  "v_pk_fma_f32 v[0:1], v[0:1], s[60:61], s[60:61] op_sel:[0,0,1] op_sel_hi:[1,0,1]\n"
  "v_pk_fma_f32 v[2:3], v[2:3], s[60:61], s[60:61] op_sel:[0,0,1] op_sel_hi:[1,0,1]\n"
  "v_pk_fma_f32 v[4:5], v[4:5], s[60:61], s[60:61] op_sel:[0,0,1] op_sel_hi:[1,0,1]\n"
  "v_pk_fma_f32 v[6:7], v[6:7], s[60:61], s[60:61] op_sel:[0,0,1] op_sel_hi:[1,0,1]\n"
  "v_mfma_f32_16x16x32_bf16 v[12:15], v[156:159], v[76:79], 0\n"
  "v_exp_f32 v0, v0\n"
  "v_mfma_f32_16x16x32_bf16 v[12:15], v[152:155], v[72:75], v[12:15]\n"
  "v_exp_f32 v1, v1\n"
  "v_mfma_f32_16x16x32_bf16 v[12:15], v[148:151], v[68:71], v[12:15]\n"
  "v_exp_f32 v2, v2\n"
  "v_mfma_f32_16x16x32_bf16 v[12:15], v[144:147], v[64:67], v[12:15]\n"
  "v_exp_f32 v3, v3\n"
  "v_mfma_f32_16x16x32_bf16 v[12:15], v[140:143], v[60:63], v[12:15]\n"
  "v_cvt_pk_bf16_f32 v0, v0, v1\n"
  "v_cvt_pk_bf16_f32 v1, v2, v3\n"
  "v_mfma_f32_16x16x32_bf16 v[12:15], v[136:139], v[56:59], v[12:15]\n"
  "v_mfma_f32_16x16x32_bf16 v[16:19], v[160:163], v[56:59], 0\n"
  "v_exp_f32 v4, v4\n"
  "v_mfma_f32_16x16x32_bf16 v[16:19], v[164:167], v[60:63], v[16:19]\n"
  "v_exp_f32 v5, v5\n"
  "v_mfma_f32_16x16x32_bf16 v[16:19], v[168:171], v[64:67], v[16:19]\n"
  "v_exp_f32 v6, v6\n"
  "v_mfma_f32_16x16x32_bf16 v[16:19], v[172:175], v[68:71], v[16:19]\n"
  "v_exp_f32 v7, v7\n"
  "v_mfma_f32_16x16x32_bf16 v[16:19], v[176:179], v[72:75], v[16:19]\n"
  "v_cvt_pk_bf16_f32 v2, v4, v5\n"
  "v_cvt_pk_bf16_f32 v3, v6, v7\n"
  "v_mfma_f32_16x16x32_bf16 v[16:19], v[180:183], v[76:79], v[16:19]\n"
  "s_waitcnt lgkmcnt(15)\n"
  "ds_write_b128 v80, v[0:3] offset:4096\n"
  "v_pk_fma_f32 v[8:9], v[8:9], s[60:61], s[60:61] op_sel:[0,0,1] op_sel_hi:[1,0,1]\n"
  "v_pk_fma_f32 v[10:11], v[10:11], s[60:61], s[60:61] op_sel:[0,0,1] op_sel_hi:[1,0,1]\n"
  "v_pk_fma_f32 v[12:13], v[12:13], s[60:61], s[60:61] op_sel:[0,0,1] op_sel_hi:[1,0,1]\n"
  "v_pk_fma_f32 v[14:15], v[14:15], s[60:61], s[60:61] op_sel:[0,0,1] op_sel_hi:[1,0,1]\n"
  "v_mfma_f32_16x16x32_bf16 v[20:23], v[204:207], v[76:79], 0\n"
  "v_exp_f32 v8, v8\n"
  "v_mfma_f32_16x16x32_bf16 v[20:23], v[200:203], v[72:75], v[20:23]\n"
  "v_exp_f32 v9, v9\n"
  "v_mfma_f32_16x16x32_bf16 v[20:23], v[196:199], v[68:71], v[20:23]\n"
  "v_exp_f32 v10, v10\n"
  "v_mfma_f32_16x16x32_bf16 v[20:23], v[192:195], v[64:67], v[20:23]\n"
  "v_exp_f32 v11, v11\n"
  "v_mfma_f32_16x16x32_bf16 v[20:23], v[188:191], v[60:63], v[20:23]\n"
  "v_cvt_pk_bf16_f32 v8, v8, v9\n"
  "v_cvt_pk_bf16_f32 v9, v10, v11\n"
  "v_mfma_f32_16x16x32_bf16 v[20:23], v[184:187], v[56:59], v[20:23]\n"
  "v_mfma_f32_16x16x32_bf16 v[24:27], v[208:211], v[56:59], 0\n"
  "v_exp_f32 v12, v12\n"
  "v_mfma_f32_16x16x32_bf16 v[24:27], v[212:215], v[60:63], v[24:27]\n"
  "s_waitcnt lgkmcnt(15)\n"
  "v_exp_f32 v13, v13\n"
  "v_mfma_f32_16x16x32_bf16 v[24:27], v[216:219], v[64:67], v[24:27]\n"
  "v_exp_f32 v14, v14\n"
  "v_mfma_f32_16x16x32_bf16 v[24:27], v[220:223], v[68:71], v[24:27]\n"
  "s_waitcnt lgkmcnt(13)\n"
  "v_exp_f32 v15, v15\n"
  "v_mfma_f32_16x16x32_bf16 v[24:27], v[224:227], v[72:75], v[24:27]\n"
  "v_cvt_pk_bf16_f32 v10, v12, v13\n"
  "v_cvt_pk_bf16_f32 v11, v14, v15\n"
  "v_mfma_f32_16x16x32_bf16 v[24:27], v[228:231], v[76:79], v[24:27]\n"
  "s_waitcnt lgkmcnt(9)\n"
  "ds_write_b128 v80, v[8:11] offset:5120\n"
  "v_pk_fma_f32 v[16:17], v[16:17], s[60:61], s[60:61] op_sel:[0,0,1] op_sel_hi:[1,0,1]\n"
  "v_pk_fma_f32 v[18:19], v[18:19], s[60:61], s[60:61] op_sel:[0,0,1] op_sel_hi:[1,0,1]\n"
  "v_pk_fma_f32 v[20:21], v[20:21], s[60:61], s[60:61] op_sel:[0,0,1] op_sel_hi:[1,0,1]\n"
  "v_pk_fma_f32 v[22:23], v[22:23], s[60:61], s[60:61] op_sel:[0,0,1] op_sel_hi:[1,0,1]\n"
  "v_mfma_f32_16x16x32_bf16 v[28:31], v[252:255], v[76:79], 0\n"
  "v_exp_f32 v16, v16\n"
  "v_mfma_f32_16x16x32_bf16 v[28:31], v[248:251], v[72:75], v[28:31]\n"
  "s_waitcnt lgkmcnt(8)\n"
  "v_exp_f32 v17, v17\n"
  "v_mfma_f32_16x16x32_bf16 v[28:31], v[244:247], v[68:71], v[28:31]\n"
  "v_exp_f32 v18, v18\n"
  "v_mfma_f32_16x16x32_bf16 v[28:31], v[240:243], v[64:67], v[28:31]\n"
  "s_waitcnt lgkmcnt(5)\n"
  "s_sub_u32 s56, s56, 1\n"
  "v_exp_f32 v19, v19\n"
  "v_mfma_f32_16x16x32_bf16 v[28:31], v[236:239], v[60:63], v[28:31]\n"
  "v_cvt_pk_bf16_f32 v16, v16, v17\n"
  "v_cvt_pk_bf16_f32 v17, v18, v19\n"
  "v_mfma_f32_16x16x32_bf16 v[28:31], v[232:235], v[56:59], v[28:31]\n"
  "s_waitcnt lgkmcnt(3)\n"
  "s_branch A_H0_%=\n"
  "A_LAST_%=:\n"
  "v_exp_f32 v20, v20\n"
  "v_exp_f32 v21, v21\n"
  "v_exp_f32 v22, v22\n"
  "v_exp_f32 v23, v23\n"
  "v_pk_fma_f32 v[24:25], v[24:25], s[60:61], s[60:61] op_sel:[0,0,1] op_sel_hi:[1,0,1]\n"
  "v_pk_fma_f32 v[26:27], v[26:27], s[60:61], s[60:61] op_sel:[0,0,1] op_sel_hi:[1,0,1]\n"
  "v_cvt_pk_bf16_f32 v18, v20, v21\n"
  "v_cvt_pk_bf16_f32 v19, v22, v23\n"
  "ds_write_b128 v80, v[16:19] offset:6144\n"
  "v_pk_fma_f32 v[28:29], v[28:29], s[60:61], s[60:61] op_sel:[0,0,1] op_sel_hi:[1,0,1]\n"
  "v_pk_fma_f32 v[30:31], v[30:31], s[60:61], s[60:61] op_sel:[0,0,1] op_sel_hi:[1,0,1]\n"
  "v_exp_f32 v24, v24\n"
  "v_exp_f32 v25, v25\n"
  "v_exp_f32 v26, v26\n"
  "v_exp_f32 v27, v27\n"
  "v_exp_f32 v28, v28\n"
  "v_exp_f32 v29, v29\n"
  "v_cvt_pk_bf16_f32 v24, v24, v25\n"
  "v_cvt_pk_bf16_f32 v25, v26, v27\n"
  "v_exp_f32 v30, v30\n"
  "v_exp_f32 v31, v31\n"
  "v_cvt_pk_bf16_f32 v26, v28, v29\n"
  "v_cvt_pk_bf16_f32 v27, v30, v31\n"
  "ds_write_b128 v80, v[24:27] offset:7168\n"
  "s_waitcnt lgkmcnt(0)\n"
  "s_barrier\n"
  "s_mov_b32 %0, 0\n"
  "s_branch L_END_%=\n"
  "B_START_%=:\n"
  "s_mov_b64 s[44:45], s[76:77]\n"
  "s_add_u32 m0, s66, 0\n"
  "s_nop 0\n"
  "buffer_load_dwordx4 v100, s[44:47], s67 offen offset:0 nt lds\n"
  "s_add_u32 m0, s66, 1920\n"
  "s_nop 0\n"
  "buffer_load_dwordx4 v100, s[44:47], s67 offen offset:128 nt lds\n"
  "s_add_u32 m0, s66, 3840\n"
  "s_nop 0\n"
  "buffer_load_dwordx4 v100, s[44:47], s67 offen offset:256 nt lds\n"
  "s_add_u32 m0, s66, 6144\n"
  "s_nop 0\n"
  "buffer_load_dwordx4 v100, s[44:47], s64 offen offset:0 nt lds\n"
  "s_add_u32 m0, s66, 8064\n"
  "s_nop 0\n"
  "buffer_load_dwordx4 v100, s[44:47], s64 offen offset:128 nt lds\n"
  "s_add_u32 m0, s66, 9984\n"
  "s_nop 0\n"
  "buffer_load_dwordx4 v100, s[44:47], s64 offen offset:256 nt lds\n"
  "s_sub_u32 s40, s80, 2048\n"
  "s_subb_u32 s41, s81, 0\n"
  "s_mov_b32 s42, 100665344\n"
  "s_mov_b64 s[48:49], s[82:83]\n"
  "s_mov_b32 s50, 166723584\n"
  "s_mov_b32 s51, 0x20000\n"
  "s_lshr_b32 s62, s52, 16\n"
  "s_lshl1_add_u32 s62, s62, s65\n"
  "s_mul_i32 s57, s62, 98304\n"
  "s_and_b32 s56, s52, 0xff\n"
  "s_cmp_lg_u32 s53, 0\n"
  "s_cselect_b32 s62, 5, 0\n"
  "s_sub_u32 s56, s56, s62\n"
  "s_mov_b32 s68, 0xf0000000\n"
  "s_mov_b32 s69, 0\n"
  "s_sub_u32 s70, s67, s68\n"
  "s_mov_b32 s64, s67\n"
  "s_and_b32 s90, s52, 0xff\n"
  "s_sub_u32 s90, s90, 1\n"
  "s_mul_i32 s91, s90, 6144\n"
  "s_add_u32 s91, s91, s64\n"
  "s_lshr_b32 s62, s53, 16\n"
  "s_mul_i32 s62, s62, 53\n"
  "s_bfe_u32 s63, s53, 0x80008\n"
  "s_add_u32 s62, s62, s63\n"
  "s_mul_i32 s64, s62, 6144\n"
  "s_cmp_eq_u32 s53, 0\n"
  "s_cmov_b32 s64, 0xf0000000\n"
  "s_sub_u32 s91, s64, s91\n"
  "s_mov_b32 s96, s91\n"
  "s_and_b32 s92, s53, 0xff\n"
  "s_sub_u32 s92, s92, 1\n"
  "s_mul_i32 s93, s92, 6144\n"
  "s_add_u32 s93, s93, s64\n"
  "s_lshr_b32 s62, s54, 16\n"
  "s_mul_i32 s62, s62, 53\n"
  "s_bfe_u32 s63, s54, 0x80008\n"
  "s_add_u32 s62, s62, s63\n"
  "s_mul_i32 s64, s62, 6144\n"
  "s_cmp_eq_u32 s54, 0\n"
  "s_cmov_b32 s64, 0xf0000000\n"
  "s_sub_u32 s93, s64, s93\n"
  "s_mov_b32 s97, s93\n"
  "s_and_b32 s94, s54, 0xff\n"
  "s_sub_u32 s94, s94, 1\n"
  "s_mul_i32 s95, s94, 6144\n"
  "s_add_u32 s95, s95, s64\n"
  "s_lshr_b32 s62, s55, 16\n"
  "s_mul_i32 s62, s62, 53\n"
  "s_bfe_u32 s63, s55, 0x80008\n"
  "s_add_u32 s62, s62, s63\n"
  "s_mul_i32 s64, s62, 6144\n"
  "s_cmp_eq_u32 s55, 0\n"
  "s_cmov_b32 s64, 0xf0000000\n"
  "s_sub_u32 s95, s64, s95\n"
  "s_add_u32 s66, s66, 6144\n"
  "s_cmp_ge_u32 s66, 86016\n"
  "s_cselect_b32 s64, 36864, 0\n"
  "s_sub_u32 s66, s66, s64\n"
  "s_sub_u32 s90, s90, 1\n"
  "s_cselect_b32 s63, s91, 6144\n"
  "s_cmov_b64 s[90:91], s[92:93]\n"
  "s_cmov_b64 s[92:93], s[94:95]\n"
  "s_add_u32 s67, s67, s63\n"
  "s_add_u32 s66, s66, 6144\n"
  "s_cmp_ge_u32 s66, 86016\n"
  "s_cselect_b32 s64, 36864, 0\n"
  "s_sub_u32 s66, s66, s64\n"
  "s_sub_u32 s90, s90, 1\n"
  "s_cselect_b32 s63, s91, 6144\n"
  "s_cmov_b64 s[90:91], s[92:93]\n"
  "s_cmov_b64 s[92:93], s[94:95]\n"
  "s_add_u32 s67, s67, s63\n"
  "v_lshrrev_b32 v1, 3, v0\n"
  "v_mul_u32_u24 v117, 0x180, v1\n"
  "v_lshrrev_b32 v1, 4, v0\n"
  "v_and_b32 v2, 7, v0\n"
  "v_xor_b32 v2, v2, v1\n"
  "v_lshl_add_u32 v117, v2, 4, v117\n"
  "s_lshl_b32 s62, s65, 8\n"
  "s_add_u32 s62, s62, 2048\n"
  "v_add_u32 v117, s62, v117\n"
  "v_add_u32 v118, 0x800, v117\n"
  "v_lshrrev_b32 v2, 2, v0\n"
  "v_mul_u32_u24 v119, 0x180, v2\n"
  "v_and_b32 v1, 1, v1\n"
  "v_and_b32 v2, 3, v0\n"
  "v_xor_b32 v2, v2, v1\n"
  "v_lshl_add_u32 v119, v2, 4, v119\n"
  "s_lshl_b32 s62, s65, 6\n"
  "s_add_u32 s62, s62, 128\n"
  "v_add_u32 v119, s62, v119\n"
  "s_mov_b32 s58, 0xfffd1800\n"
  "s_cmp_eq_u32 s65, 0\n"
  "s_cmov_b32 s58, 6144\n"
  "s_cselect_b64 s[80:81], s[78:79], s[80:81]\n"
  "s_mov_b32 s82, 100663296\n"
  "s_mov_b32 s83, 0x20000\n"
  "s_mov_b64 s[76:77], s[78:79]\n"
  "s_mov_b32 s78, 100663296\n"
  "s_mov_b32 s79, 0x20000\n"
  "v_lshrrev_b32 v1, 2, v0\n"
  "v_lshlrev_b32 v103, 7, v1\n"
  "v_lshlrev_b32 v107, 6, v1\n"
  "v_and_b32 v2, 1, v0\n"
  "v_lshl_add_u32 v103, v2, 3, v103\n"
  "v_lshl_add_u32 v107, v2, 3, v107\n"
  "v_bfe_u32 v2, v0, 1, 1\n"
  "v_lshl_add_u32 v103, v2, 6, v103\n"
  "v_lshl_add_u32 v107, v2, 5, v107\n"
  "v_bfe_u32 v2, v0, 3, 2\n"
  "v_lshl_add_u32 v103, v2, 4, v103\n"
  "v_bfe_u32 v2, v0, 4, 1\n"
  "v_lshl_add_u32 v107, v2, 4, v107\n"
  "v_add_u32 v103, s59, v103\n"
  "v_add_u32 v107, s59, v107\n"
  "v_xor_b32 v104, 16, v103\n"
  "v_xor_b32 v105, 32, v103\n"
  "v_xor_b32 v106, 48, v103\n"
  "v_xor_b32 v108, 16, v107\n"
  "s_lshl_b32 s62, s65, 13\n"
  "v_lshlrev_b32 v92, 4, v0\n"
  "v_add_u32 v92, s62, v92\n"
  "v_xor_b32 v93, 0x2000, v92\n"
  "v_lshrrev_b32 v1, 5, v0\n"
  "v_mul_u32_u24 v1, 0x480, v1\n"
  "v_and_b32 v2, 31, v0\n"
  "v_lshl_add_u32 v94, v2, 3, v1\n"
  "s_mul_i32 s62, s65, 4608\n"
  "s_add_u32 s62, s62, 32768\n"
  "v_add_u32 v94, s62, v94\n"
  "v_and_b32 v2, 7, v0\n"
  "v_lshl_add_u32 v95, v2, 3, v1\n"
  "v_bfe_u32 v2, v0, 3, 1\n"
  "v_lshl_add_u32 v95, v2, 2, v95\n"
  "v_bfe_u32 v2, v0, 4, 1\n"
  "v_lshl_add_u32 v95, v2, 7, v95\n"
  "s_lshl_b32 s62, s65, 6\n"
  "s_add_u32 s62, s62, 35072\n"
  "v_add_u32 v95, s62, v95\n"
  "v_lshrrev_b32 v1, 4, v0\n"
  "v_mul_u32_u24 v96, 0x120, v1\n"
  "v_and_b32 v2, 3, v0\n"
  "v_lshl_add_u32 v96, v2, 6, v96\n"
  "v_bfe_u32 v2, v0, 2, 2\n"
  "v_lshl_add_u32 v96, v2, 3, v96\n"
  "s_mul_i32 s62, s65, 1152\n"
  "s_add_u32 s62, s62, 40960\n"
  "v_add_u32 v96, s62, v96\n"
  "v_bfe_u32 v2, v0, 3, 1\n"
  "v_lshl_add_u32 v2, v2, 2, v1\n"
  "v_mul_u32_u24 v97, 0x180, v2\n"
  "v_and_b32 v2, 7, v0\n"
  "v_lshl_add_u32 v97, v2, 4, v97\n"
  "s_mul_i32 s62, s65, 3072\n"
  "v_add_u32 v97, s62, v97\n"
  "s_mov_b32 s62, 47872\n"
  "v_lshl_add_u32 v99, v1, 4, s62\n"
  "s_lshl_b32 s62, s65, 6\n"
  "s_add_u32 s62, s62, 39680\n"
  "v_lshl_add_u32 v98, v0, 2, s62\n"
  "v_mov_b32 v3, 0x40000\n"
  "v_cmp_gt_u32 vcc, 16, v0\n"
  "s_nop 1\n"
  "v_cndmask_b32 v98, v3, v98, vcc\n"
  "v_mov_b32 v68, 1.0\n"
  "ds_write_b32 v98, v68 offset:8192\n"
  "v_and_b32 v2, 15, v0\n"
  "v_mov_b32 v110, 0x44444444\n"
  "v_mov_b32 v3, 0xeeeeeeee\n"
  "v_cmp_eq_u32 vcc, 1, v2\n"
  "s_nop 1\n"
  "v_cndmask_b32 v110, v110, v3, vcc\n"
  "v_mov_b32 v3, 0x3f803f80\n"
  "v_cmp_gt_u32 vcc, 2, v2\n"
  "v_mov_b32 v64, 0\n"
  "v_mov_b32 v65, 0\n"
  "v_mov_b32 v66, 0\n"
  "v_mov_b32 v67, 0\n"
  "v_cndmask_b32 v76, 0, v3, vcc\n"
  "v_cndmask_b32 v77, 0, v3, vcc\n"
  "v_cndmask_b32 v78, 0, v3, vcc\n"
  "v_cndmask_b32 v79, 0, v3, vcc\n"
  "v_mov_b32 v109, 0\n"
  "s_waitcnt vmcnt(0)\n"
  "s_barrier\n"
  "v_lshlrev_b32 v111, 7, v0\n"
  "v_mov_b32 v116, 0xff00ff\n"
  "s_mov_b32 s62, 0x1120010\n"
  "v_writelane_b32 v116, s62, 1\n"
  "s_mov_b32 s62, 0xff0214\n"
  "v_writelane_b32 v116, s62, 2\n"
  "s_mov_b32 s62, 0x4180316\n"
  "v_writelane_b32 v116, s62, 3\n"
  "s_mov_b32 s62, 0x61c051a\n"
  "v_writelane_b32 v116, s62, 4\n"
  "s_mov_b32 s62, 0x71e00ff\n"
  "v_writelane_b32 v116, s62, 5\n"
  "s_mov_b32 s62, 0x9220820\n"
  "v_writelane_b32 v116, s62, 6\n"
  "s_mov_b32 s62, 0xff0a24\n"
  "v_writelane_b32 v116, s62, 7\n"
  "s_mov_b32 s62, 0xc280b26\n"
  "v_writelane_b32 v116, s62, 8\n"
  "s_mov_b32 s62, 0xe2c0d2a\n"
  "v_writelane_b32 v116, s62, 9\n"
  "s_mov_b32 s62, 0x102f0f2e\n"
  "v_writelane_b32 v116, s62, 10\n"
  "s_mov_b32 s62, 0x122b112d\n"
  "v_writelane_b32 v116, s62, 11\n"
  "s_mov_b32 s62, 0x14271329\n"
  "v_writelane_b32 v116, s62, 12\n"
  "s_mov_b32 s62, 0x152500ff\n"
  "v_writelane_b32 v116, s62, 13\n"
  "s_mov_b32 s62, 0x17211623\n"
  "v_writelane_b32 v116, s62, 14\n"
  "s_mov_b32 s62, 0xff181f\n"
  "v_writelane_b32 v116, s62, 15\n"
  "s_mov_b32 s62, 0x1a1b191d\n"
  "v_writelane_b32 v116, s62, 16\n"
  "s_mov_b32 s62, 0x1c171b19\n"
  "v_writelane_b32 v116, s62, 17\n"
  "s_mov_b32 s62, 0x1d1500ff\n"
  "v_writelane_b32 v116, s62, 18\n"
  "s_mov_b32 s62, 0x1f111e13\n"
  "v_writelane_b32 v116, s62, 19\n"
  "s_mov_b32 s62, 0x3000ff\n"
  "v_writelane_b32 v116, s62, 27\n"
  "s_mov_b32 s62, 0x2320131\n"
  "v_writelane_b32 v116, s62, 28\n"
  "s_mov_b32 s62, 0x33300ff\n"
  "v_writelane_b32 v116, s62, 29\n"
  "s_mov_b32 s62, 0x5350434\n"
  "v_writelane_b32 v116, s62, 30\n"
  "s_mov_b32 s62, 0xff0636\n"
  "v_writelane_b32 v116, s62, 31\n"
  "s_mov_b32 s62, 0x8380737\n"
  "v_writelane_b32 v116, s62, 32\n"
  "s_mov_b32 s62, 0xa3a0939\n"
  "v_writelane_b32 v116, s62, 33\n"
  "s_mov_b32 s62, 0xb3b00ff\n"
  "v_writelane_b32 v116, s62, 34\n"
  "s_mov_b32 s62, 0xd3d0c3c\n"
  "v_writelane_b32 v116, s62, 35\n"
  "s_mov_b32 s62, 0xf3f0e3e\n"
  "v_writelane_b32 v116, s62, 36\n"
  "s_mov_b32 s62, 0x113e103f\n"
  "v_writelane_b32 v116, s62, 37\n"
  "s_mov_b32 s62, 0x133c123d\n"
  "v_writelane_b32 v116, s62, 38\n"
  "s_mov_b32 s62, 0xff143b\n"
  "v_writelane_b32 v116, s62, 39\n"
  "s_mov_b32 s62, 0x1639153a\n"
  "v_writelane_b32 v116, s62, 40\n"
  "s_mov_b32 s62, 0x18371738\n"
  "v_writelane_b32 v116, s62, 41\n"
  "s_mov_b32 s62, 0x193600ff\n"
  "v_writelane_b32 v116, s62, 42\n"
  "s_mov_b32 s62, 0x1b341a35\n"
  "v_writelane_b32 v116, s62, 43\n"
  "s_mov_b32 s62, 0xff1c33\n"
  "v_writelane_b32 v116, s62, 44\n"
  "s_mov_b32 s62, 0x1e311d32\n"
  "v_writelane_b32 v116, s62, 45\n"
  "s_mov_b32 s62, 0xff1f30\n"
  "v_writelane_b32 v116, s62, 46\n"
  "s_lshr_b32 s62, %7, 8\n"
  "s_and_b32 s63, %7, 7\n"
  "s_mul_i32 s63, s63, 24\n"
  "v_mov_b32 v3, 0xffff\n"
  "v_bfe_u32 v4, v116, 0, 8\n"
  "v_mul_u32_u24 v4, 192, v4\n"
  "v_bfe_u32 v1, v116, 8, 8\n"
  "v_sub_u32 v1, s62, v1\n"
  "v_and_b32 v1, 31, v1\n"
  "v_cmp_gt_u32 vcc, 24, v1\n"
  "v_add3_u32 v4, v4, v1, s63\n"
  "s_nop 0\n"
  "v_cndmask_b32 v4, v3, v4, vcc\n"
  "v_bfe_u32 v5, v116, 16, 8\n"
  "v_mul_u32_u24 v5, 192, v5\n"
  "v_bfe_u32 v1, v116, 24, 8\n"
  "v_sub_u32 v1, s62, v1\n"
  "v_and_b32 v1, 31, v1\n"
  "v_cmp_gt_u32 vcc, 24, v1\n"
  "v_add3_u32 v5, v5, v1, s63\n"
  "s_nop 0\n"
  "v_cndmask_b32 v5, v3, v5, vcc\n"
  "v_lshl_or_b32 v116, v5, 16, v4\n"
  "s_mul_i32 s72, s65, -1\n"
  "s_add_u32 s72, s72, 1\n"
  "s_mov_b32 s71, 0x53800000\n"
  "s_sub_u32 s75, s59, 36864\n"
  "s_barrier\n"
  "s_add_u32 m0, s66, 0\n"
  "s_nop 0\n"
  "buffer_load_dwordx4 v100, s[44:47], s67 offen offset:0 nt lds\n"
  "s_add_u32 m0, s66, 1920\n"
  "s_nop 0\n"
  "buffer_load_dwordx4 v100, s[44:47], s67 offen offset:128 nt lds\n"
  "s_add_u32 m0, s66, 3840\n"
  "s_nop 0\n"
  "buffer_load_dwordx4 v100, s[44:47], s67 offen offset:256 nt lds\n"
  "s_add_u32 s66, s66, 6144\n"
  "s_cmp_ge_u32 s66, 86016\n"
  "s_cselect_b32 s64, 36864, 0\n"
  "s_sub_u32 s66, s66, s64\n"
  "s_sub_u32 s90, s90, 1\n"
  "s_cselect_b32 s63, s91, 6144\n"
  "s_cmov_b64 s[90:91], s[92:93]\n"
  "s_cmov_b64 s[92:93], s[94:95]\n"
  "s_add_u32 s67, s67, s63\n"
  "s_add_u32 m0, s66, 0\n"
  "s_nop 0\n"
  "buffer_load_dwordx4 v100, s[44:47], s67 offen offset:0 nt lds\n"
  "s_add_u32 m0, s66, 1920\n"
  "s_nop 0\n"
  "buffer_load_dwordx4 v100, s[44:47], s67 offen offset:128 nt lds\n"
  "s_add_u32 m0, s66, 3840\n"
  "s_nop 0\n"
  "buffer_load_dwordx4 v100, s[44:47], s67 offen offset:256 nt lds\n"
  "s_add_u32 s66, s66, 6144\n"
  "s_cmp_ge_u32 s66, 86016\n"
  "s_cselect_b32 s64, 36864, 0\n"
  "s_sub_u32 s66, s66, s64\n"
  "s_sub_u32 s90, s90, 1\n"
  "s_cselect_b32 s63, s91, 6144\n"
  "s_cmov_b64 s[90:91], s[92:93]\n"
  "s_cmov_b64 s[92:93], s[94:95]\n"
  "s_add_u32 s67, s67, s63\n"
  "s_add_u32 m0, s59, 0\n"
  "s_nop 0\n"
  "buffer_load_dwordx4 v117, s[40:43], s57 offen offset:0  lds\n"
  "buffer_load_dwordx4 v118, s[40:43], s57 offen offset:1024  lds\n"
  "buffer_load_dwordx4 v119, s[40:43], s57 offen offset:2048  lds\n"
  "s_add_u32 s57, s57, 6144\n"
  "s_add_u32 m0, s59, 3072\n"
  "s_nop 0\n"
  "buffer_load_dwordx4 v117, s[40:43], s57 offen offset:0  lds\n"
  "buffer_load_dwordx4 v118, s[40:43], s57 offen offset:1024  lds\n"
  "buffer_load_dwordx4 v119, s[40:43], s57 offen offset:2048  lds\n"
  "s_add_u32 s57, s57, 6144\n"
  "s_add_u32 m0, s59, 6144\n"
  "s_nop 0\n"
  "buffer_load_dwordx4 v117, s[40:43], s57 offen offset:0  lds\n"
  "buffer_load_dwordx4 v118, s[40:43], s57 offen offset:1024  lds\n"
  "buffer_load_dwordx4 v119, s[40:43], s57 offen offset:2048  lds\n"
  "s_add_u32 s57, s57, 6144\n"
  "s_add_u32 m0, s59, 9216\n"
  "s_nop 0\n"
  "buffer_load_dwordx4 v117, s[40:43], s57 offen offset:0  lds\n"
  "buffer_load_dwordx4 v118, s[40:43], s57 offen offset:1024  lds\n"
  "buffer_load_dwordx4 v119, s[40:43], s57 offen offset:2048  lds\n"
  "s_add_u32 s57, s57, 6144\n"
  "s_add_u32 m0, s59, 12288\n"
  "s_nop 0\n"
  "buffer_load_dwordx4 v117, s[40:43], s57 offen offset:0  lds\n"
  "buffer_load_dwordx4 v118, s[40:43], s57 offen offset:1024  lds\n"
  "buffer_load_dwordx4 v119, s[40:43], s57 offen offset:2048  lds\n"
  "s_add_u32 s57, s57, 6144\n"
  "s_add_u32 m0, s59, 15360\n"
  "s_nop 0\n"
  "buffer_load_dwordx4 v117, s[40:43], s57 offen offset:0  lds\n"
  "buffer_load_dwordx4 v118, s[40:43], s57 offen offset:1024  lds\n"
  "buffer_load_dwordx4 v119, s[40:43], s57 offen offset:2048  lds\n"
  "s_add_u32 s57, s57, 6144\n"
  "s_waitcnt vmcnt(15)\n"
  "ds_read_b64_tr_b16 a[0:1], v103 offset:0\n"
  "ds_read_b64_tr_b16 a[4:5], v104 offset:0\n"
  "ds_read_b64_tr_b16 a[8:9], v105 offset:0\n"
  "ds_read_b64_tr_b16 a[12:13], v106 offset:0\n"
  "ds_read_b64_tr_b16 a[16:17], v107 offset:2048\n"
  "ds_read_b64_tr_b16 a[20:21], v108 offset:2048\n"
  "s_waitcnt vmcnt(12)\n"
  "ds_read_b64_tr_b16 a[2:3], v103 offset:3072\n"
  "ds_read_b64_tr_b16 a[6:7], v104 offset:3072\n"
  "s_add_u32 m0, s59, 0\n"
  "ds_read_b64_tr_b16 a[10:11], v105 offset:3072\n"
  "ds_read_b64_tr_b16 a[14:15], v106 offset:3072\n"
  "s_waitcnt lgkmcnt(4)\n"
  "buffer_load_dwordx4 v117, s[40:43], s57 offen offset:0  lds\n"
  "buffer_load_dwordx4 v118, s[40:43], s57 offen offset:1024  lds\n"
  "ds_read_b64_tr_b16 a[18:19], v107 offset:5120\n"
  "ds_read_b64_tr_b16 a[22:23], v108 offset:5120\n"
  "s_waitcnt vmcnt(11)\n"
  "ds_read_b64_tr_b16 a[24:25], v103 offset:6144\n"
  "ds_read_b64_tr_b16 a[28:29], v104 offset:6144\n"
  "buffer_load_dwordx4 v119, s[40:43], s57 offen offset:2048  lds\n"
  "s_add_u32 s57, s57, 6144\n"
  "s_add_u32 m0, s59, 3072\n"
  "ds_read_b64_tr_b16 a[32:33], v105 offset:6144\n"
  "ds_read_b64_tr_b16 a[36:37], v106 offset:6144\n"
  "s_waitcnt lgkmcnt(4)\n"
  "buffer_load_dwordx4 v117, s[40:43], s57 offen offset:0  lds\n"
  "buffer_load_dwordx4 v118, s[40:43], s57 offen offset:1024  lds\n"
  "ds_read_b64_tr_b16 a[40:41], v107 offset:8192\n"
  "ds_read_b64_tr_b16 a[44:45], v108 offset:8192\n"
  "s_waitcnt vmcnt(11)\n"
  "ds_read_b64_tr_b16 a[26:27], v103 offset:9216\n"
  "ds_read_b64_tr_b16 a[30:31], v104 offset:9216\n"
  "buffer_load_dwordx4 v119, s[40:43], s57 offen offset:2048  lds\n"
  "s_add_u32 s57, s57, 6144\n"
  "s_add_u32 m0, s59, 6144\n"
  "ds_read_b64_tr_b16 a[34:35], v105 offset:9216\n"
  "ds_read_b64_tr_b16 a[38:39], v106 offset:9216\n"
  "s_waitcnt lgkmcnt(4)\n"
  "buffer_load_dwordx4 v117, s[40:43], s57 offen offset:0  lds\n"
  "buffer_load_dwordx4 v118, s[40:43], s57 offen offset:1024  lds\n"
  "ds_read_b64_tr_b16 a[42:43], v107 offset:11264\n"
  "ds_read_b64_tr_b16 a[46:47], v108 offset:11264\n"
  "s_waitcnt vmcnt(11)\n"
  "ds_read_b64_tr_b16 a[48:49], v103 offset:12288\n"
  "ds_read_b64_tr_b16 a[52:53], v104 offset:12288\n"
  "buffer_load_dwordx4 v119, s[40:43], s57 offen offset:2048  lds\n"
  "s_add_u32 s57, s57, 6144\n"
  "s_add_u32 m0, s59, 9216\n"
  "ds_read_b64_tr_b16 a[56:57], v105 offset:12288\n"
  "ds_read_b64_tr_b16 a[60:61], v106 offset:12288\n"
  "s_waitcnt lgkmcnt(4)\n"
  "buffer_load_dwordx4 v117, s[40:43], s57 offen offset:0  lds\n"
  "buffer_load_dwordx4 v118, s[40:43], s57 offen offset:1024  lds\n"
  "ds_read_b64_tr_b16 a[64:65], v107 offset:14336\n"
  "ds_read_b64_tr_b16 a[68:69], v108 offset:14336\n"
  "s_waitcnt vmcnt(11)\n"
  "ds_read_b64_tr_b16 a[50:51], v103 offset:15360\n"
  "ds_read_b64_tr_b16 a[54:55], v104 offset:15360\n"
  "buffer_load_dwordx4 v119, s[40:43], s57 offen offset:2048  lds\n"
  "s_add_u32 s57, s57, 6144\n"
  "s_add_u32 m0, s59, 12288\n"
  "ds_read_b64_tr_b16 a[58:59], v105 offset:15360\n"
  "ds_read_b64_tr_b16 a[62:63], v106 offset:15360\n"
  "s_waitcnt lgkmcnt(4)\n"
  "buffer_load_dwordx4 v117, s[40:43], s57 offen offset:0  lds\n"
  "buffer_load_dwordx4 v118, s[40:43], s57 offen offset:1024  lds\n"
  "ds_read_b64_tr_b16 a[66:67], v107 offset:17408\n"
  "ds_read_b64_tr_b16 a[70:71], v108 offset:17408\n"
  "s_waitcnt vmcnt(11)\n"
  "ds_read_b64_tr_b16 a[72:73], v103 offset:0\n"
  "ds_read_b64_tr_b16 a[76:77], v104 offset:0\n"
  "buffer_load_dwordx4 v119, s[40:43], s57 offen offset:2048  lds\n"
  "s_add_u32 s57, s57, 6144\n"
  "s_add_u32 m0, s59, 15360\n"
  "ds_read_b64_tr_b16 a[80:81], v105 offset:0\n"
  "ds_read_b64_tr_b16 a[84:85], v106 offset:0\n"
  "s_waitcnt lgkmcnt(4)\n"
  "buffer_load_dwordx4 v117, s[40:43], s57 offen offset:0  lds\n"
  "buffer_load_dwordx4 v118, s[40:43], s57 offen offset:1024  lds\n"
  "ds_read_b64_tr_b16 a[88:89], v107 offset:2048\n"
  "ds_read_b64_tr_b16 a[92:93], v108 offset:2048\n"
  "s_waitcnt vmcnt(11)\n"
  "ds_read_b64_tr_b16 a[74:75], v103 offset:3072\n"
  "ds_read_b64_tr_b16 a[78:79], v104 offset:3072\n"
  "buffer_load_dwordx4 v119, s[40:43], s57 offen offset:2048  lds\n"
  "s_add_u32 s57, s57, 6144\n"
  "s_add_u32 m0, s59, 0\n"
  "ds_read_b64_tr_b16 a[82:83], v105 offset:3072\n"
  "ds_read_b64_tr_b16 a[86:87], v106 offset:3072\n"
  "s_waitcnt lgkmcnt(0)\n"
  "s_barrier\n"
  "buffer_load_dwordx4 v117, s[40:43], s57 offen offset:0  lds\n"
  "buffer_load_dwordx4 v118, s[40:43], s57 offen offset:1024  lds\n"
  "ds_read_b128 v[48:51], v92 offset:0\n"
  "ds_read_b64_tr_b16 a[90:91], v107 offset:5120\n"
  "ds_read_b64_tr_b16 a[94:95], v108 offset:5120\n"
  "ds_read_b128 v[52:55], v92 offset:1024\n"
  "s_waitcnt vmcnt(11)\n"
  "ds_read_b64_tr_b16 a[96:97], v103 offset:6144\n"
  "ds_read_b64_tr_b16 a[100:101], v104 offset:6144\n"
  "s_sub_u32 s56, s56, 1\n"
  "buffer_load_dwordx4 v119, s[40:43], s57 offen offset:2048  lds\n"
  "ds_read_b64_tr_b16 v[80:81], v96 offset:0\n"
  "s_add_u32 s57, s57, 6144\n"
  "s_add_u32 m0, s59, 3072\n"
  "ds_read_b64_tr_b16 a[104:105], v105 offset:6144\n"
  "ds_read_b64_tr_b16 a[108:109], v106 offset:6144\n"
  "ds_read_b64_tr_b16 v[82:83], v96 offset:32\n"
  "s_waitcnt lgkmcnt(7)\n"
  "buffer_load_dwordx4 v117, s[40:43], s57 offen offset:0  lds\n"
  "buffer_load_dwordx4 v118, s[40:43], s57 offen offset:1024  lds\n"
  "ds_read_b64_tr_b16 a[112:113], v107 offset:8192\n"
  "ds_read_b64_tr_b16 a[116:117], v108 offset:8192\n"
  "s_waitcnt vmcnt(11)\n"
  "ds_read_b64_tr_b16 a[98:99], v103 offset:9216\n"
  "ds_read_b64_tr_b16 a[102:103], v104 offset:9216\n"
  "ds_read_b64_tr_b16 v[84:85], v96 offset:2304\n"
  "s_sub_i32 s61, s56, 5\n"
  "buffer_load_dwordx4 v119, s[40:43], s57 offen offset:2048  lds\n"
  "ds_read_b64_tr_b16 v[86:87], v96 offset:2336\n"
  "s_add_u32 s57, s57, 6144\n"
  "s_add_u32 m0, s59, 6144\n"
  "ds_read_b64_tr_b16 a[106:107], v105 offset:9216\n"
  "ds_read_b64_tr_b16 a[110:111], v106 offset:9216\n"
  "ds_read_b64_tr_b16 v[88:89], v96 offset:4608\n"
  "s_waitcnt lgkmcnt(7)\n"
  "buffer_load_dwordx4 v117, s[40:43], s57 offen offset:0  lds\n"
  "buffer_load_dwordx4 v118, s[40:43], s57 offen offset:1024  lds\n"
  "ds_read_b64_tr_b16 a[114:115], v107 offset:11264\n"
  "ds_read_b64_tr_b16 a[118:119], v108 offset:11264\n"
  "s_waitcnt vmcnt(11)\n"
  "ds_read_b64_tr_b16 a[120:121], v103 offset:12288\n"
  "ds_read_b64_tr_b16 a[124:125], v104 offset:12288\n"
  "ds_read_b64_tr_b16 v[90:91], v96 offset:4640\n"
  "buffer_load_dwordx4 v119, s[40:43], s57 offen offset:2048  lds\n"
  "ds_read_b128 v[72:75], v99\n"
  "s_add_u32 s57, s57, 6144\n"
  "s_add_u32 m0, s59, 9216\n"
  "ds_read_b64_tr_b16 a[128:129], v105 offset:12288\n"
  "ds_read_b64_tr_b16 a[132:133], v106 offset:12288\n"
  "ds_read_b128 v[112:115], v99 offset:64\n"
  "v_mfma_f32_16x16x32_bf16 v[0:3], v[48:51], a[0:3], 0\n"
  "s_waitcnt lgkmcnt(7)\n"
  "buffer_load_dwordx4 v117, s[40:43], s57 offen offset:0  lds\n"
  "buffer_load_dwordx4 v118, s[40:43], s57 offen offset:1024  lds\n"
  "ds_read_b128 v[56:59], v92 offset:2048\n"
  "ds_read_b64_tr_b16 a[136:137], v107 offset:14336\n"
  "ds_read_b64_tr_b16 a[140:141], v108 offset:14336\n"
  "v_mfma_f32_16x16x32_bf16 v[0:3], v[52:55], a[24:27], v[0:3]\n"
  "ds_read_b128 v[60:63], v92 offset:3072\n"
  "s_waitcnt vmcnt(11)\n"
  "ds_read_b64_tr_b16 a[122:123], v103 offset:15360\n"
  "ds_read_b64_tr_b16 a[126:127], v104 offset:15360\n"
  "v_mfma_f32_16x16x32_bf16 v[4:7], v[48:51], a[4:7], 0\n"
  "buffer_load_dwordx4 v119, s[40:43], s57 offen offset:2048  lds\n"
  "v_xor_b32 v96, 0x2000, v96\n"
  "v_mfma_f32_16x16x32_bf16 v[4:7], v[52:55], a[28:31], v[4:7]\n"
  "s_add_u32 s57, s57, s58\n"
  "s_add_u32 m0, s59, 12288\n"
  "ds_read_b64_tr_b16 a[130:131], v105 offset:15360\n"
  "ds_read_b64_tr_b16 a[134:135], v106 offset:15360\n"
  "v_xor_b32 v99, 0x2000, v99\n"
  "v_mfma_f32_16x16x32_bf16 v[8:11], v[48:51], a[8:11], 0\n"
  "s_waitcnt lgkmcnt(5)\n"
  "buffer_load_dwordx4 v117, s[40:43], s57 offen offset:0  lds\n"
  "buffer_load_dwordx4 v118, s[40:43], s57 offen offset:1024  lds\n"
  "ds_read_b64_tr_b16 a[138:139], v107 offset:17408\n"
  "ds_read_b64_tr_b16 a[142:143], v108 offset:17408\n"
  "v_mfma_f32_16x16x32_bf16 v[8:11], v[52:55], a[32:35], v[8:11]\n"
  "s_waitcnt vmcnt(11)\n"
  "ds_read_b64_tr_b16 a[144:145], v103 offset:0\n"
  "ds_read_b64_tr_b16 a[148:149], v104 offset:0\n"
  "v_smfmac_f32_16x16x64_bf16 v[64:67], v[76:79], v[48:55], v110\n"
  "buffer_load_dwordx4 v119, s[40:43], s57 offen offset:2048  lds\n"
  "v_add_f32 v72, v72, v112\n"
  "v_mfma_f32_16x16x32_bf16 v[12:15], v[48:51], a[12:15], 0\n"
  "s_add_u32 s57, s57, 6144\n"
  "s_add_u32 m0, s59, 15360\n"
  "ds_read_b64_tr_b16 a[152:153], v105 offset:0\n"
  "ds_read_b64_tr_b16 a[156:157], v106 offset:0\n"
  "v_add_f32 v73, v73, v113\n"
  "v_mfma_f32_16x16x32_bf16 v[12:15], v[52:55], a[36:39], v[12:15]\n"
  "s_waitcnt lgkmcnt(4)\n"
  "buffer_load_dwordx4 v117, s[40:43], s57 offen offset:0  lds\n"
  "buffer_load_dwordx4 v118, s[40:43], s57 offen offset:1024  lds\n"
  "ds_read_b64_tr_b16 a[160:161], v107 offset:2048\n"
  "ds_read_b64_tr_b16 a[164:165], v108 offset:2048\n"
  "v_mfma_f32_16x16x32_bf16 v[16:19], v[48:51], a[16:19], 0\n"
  "s_waitcnt vmcnt(11)\n"
  "ds_read_b64_tr_b16 a[146:147], v103 offset:3072\n"
  "ds_read_b64_tr_b16 a[150:151], v104 offset:3072\n"
  "v_add_f32 v74, v74, v114\n"
  "v_mfma_f32_16x16x32_bf16 v[16:19], v[52:55], a[40:43], v[16:19]\n"
  "buffer_load_dwordx4 v119, s[40:43], s57 offen offset:2048  lds\n"
  "v_add_f32 v75, v75, v115\n"
  "v_mfma_f32_16x16x32_bf16 v[20:23], v[48:51], a[20:23], 0\n"
  "s_add_u32 s57, s57, 6144\n"
  "s_add_u32 m0, s59, 0\n"
  "ds_read_b64_tr_b16 a[154:155], v105 offset:3072\n"
  "ds_read_b64_tr_b16 a[158:159], v106 offset:3072\n"
  "v_max3_f32 v109, v109, v72, v73\n"
  "v_mfma_f32_16x16x32_bf16 v[20:23], v[52:55], a[44:47], v[20:23]\n"
  "s_waitcnt lgkmcnt(4)\n"
  "buffer_load_dwordx4 v117, s[40:43], s57 offen offset:0  lds\n"
  "buffer_load_dwordx4 v118, s[40:43], s57 offen offset:1024  lds\n"
  "ds_read_b64_tr_b16 a[162:163], v107 offset:5120\n"
  "ds_read_b64_tr_b16 a[166:167], v108 offset:5120\n"
  "v_mfma_f32_16x16x32_bf16 v[20:23], v[56:59], a[68:71], v[20:23]\n"
  "buffer_store_dwordx4 v[80:83], v97, s[48:51], s68 offen offset:0 nt\n"
  "ds_read_b128 v[48:51], v92 offset:4096\n"
  "s_waitcnt vmcnt(12)\n"
  "ds_read_b64_tr_b16 a[168:169], v103 offset:6144\n"
  "ds_read_b64_tr_b16 a[172:173], v104 offset:6144\n"
  "v_mfma_f32_16x16x32_bf16 v[20:23], v[60:63], a[92:95], v[20:23]\n"
  "buffer_load_dwordx4 v119, s[40:43], s57 offen offset:2048  lds\n"
  "ds_read_b128 v[52:55], v92 offset:5120\n"
  "v_mfma_f32_16x16x32_bf16 v[16:19], v[56:59], a[64:67], v[16:19]\n"
  "s_add_u32 s57, s57, 6144\n"
  "s_add_u32 m0, s59, 3072\n"
  "ds_read_b64_tr_b16 a[176:177], v105 offset:6144\n"
  "ds_read_b64_tr_b16 a[180:181], v106 offset:6144\n"
  "v_max3_f32 v109, v109, v74, v75\n"
  "v_mfma_f32_16x16x32_bf16 v[16:19], v[60:63], a[88:91], v[16:19]\n"
  "s_waitcnt lgkmcnt(6)\n"
  "buffer_load_dwordx4 v117, s[40:43], s57 offen offset:0  lds\n"
  "buffer_load_dwordx4 v118, s[40:43], s57 offen offset:1024  lds\n"
  "ds_read_b64_tr_b16 a[184:185], v107 offset:8192\n"
  "ds_read_b64_tr_b16 a[188:189], v108 offset:8192\n"
  "v_mfma_f32_16x16x32_bf16 v[12:15], v[56:59], a[60:63], v[12:15]\n"
  "s_waitcnt vmcnt(12)\n"
  "ds_read_b64_tr_b16 a[170:171], v103 offset:9216\n"
  "ds_read_b64_tr_b16 a[174:175], v104 offset:9216\n"
  "v_mfma_f32_16x16x32_bf16 v[12:15], v[60:63], a[84:87], v[12:15]\n"
  "buffer_load_dwordx4 v119, s[40:43], s57 offen offset:2048  lds\n"
  "v_smfmac_f32_16x16x64_bf16 v[64:67], v[76:79], v[56:63], v110\n"
  "s_add_u32 s57, s57, 6144\n"
  "s_add_u32 m0, s59, 6144\n"
  "ds_read_b64_tr_b16 a[178:179], v105 offset:9216\n"
  "ds_read_b64_tr_b16 a[182:183], v106 offset:9216\n"
  "v_mfma_f32_16x16x32_bf16 v[8:11], v[56:59], a[56:59], v[8:11]\n"
  "s_waitcnt lgkmcnt(4)\n"
  "buffer_load_dwordx4 v117, s[40:43], s57 offen offset:0  lds\n"
  "buffer_load_dwordx4 v118, s[40:43], s57 offen offset:1024  lds\n"
  "ds_read_b64_tr_b16 a[186:187], v107 offset:11264\n"
  "ds_read_b64_tr_b16 a[190:191], v108 offset:11264\n"
  "v_mfma_f32_16x16x32_bf16 v[8:11], v[60:63], a[80:83], v[8:11]\n"
  "s_waitcnt vmcnt(12)\n"
  "ds_read_b64_tr_b16 a[192:193], v103 offset:12288\n"
  "ds_read_b64_tr_b16 a[196:197], v104 offset:12288\n"
  "v_mfma_f32_16x16x32_bf16 v[4:7], v[56:59], a[52:55], v[4:7]\n"
  "buffer_load_dwordx4 v119, s[40:43], s57 offen offset:2048  lds\n"
  "v_mfma_f32_16x16x32_bf16 v[4:7], v[60:63], a[76:79], v[4:7]\n"
  "s_add_u32 s57, s57, 6144\n"
  "s_add_u32 m0, s59, 9216\n"
  "ds_read_b64_tr_b16 a[200:201], v105 offset:12288\n"
  "ds_read_b64_tr_b16 a[204:205], v106 offset:12288\n"
  "v_mfma_f32_16x16x32_bf16 v[0:3], v[56:59], a[48:51], v[0:3]\n"
  "s_waitcnt lgkmcnt(4)\n"
  "buffer_load_dwordx4 v117, s[40:43], s57 offen offset:0  lds\n"
  "buffer_load_dwordx4 v118, s[40:43], s57 offen offset:1024  lds\n"
  "ds_read_b64_tr_b16 a[208:209], v107 offset:14336\n"
  "ds_read_b64_tr_b16 a[212:213], v108 offset:14336\n"
  "v_mfma_f32_16x16x32_bf16 v[0:3], v[60:63], a[72:75], v[0:3]\n"
  "s_waitcnt vmcnt(12)\n"
  "ds_read_b64_tr_b16 a[194:195], v103 offset:15360\n"
  "ds_read_b64_tr_b16 a[198:199], v104 offset:15360\n"
  "v_mfma_f32_16x16x32_bf16 v[0:3], v[48:51], a[96:99], v[0:3]\n"
  "buffer_load_dwordx4 v119, s[40:43], s57 offen offset:2048  lds\n"
  "ds_read_b128 v[56:59], v92 offset:6144\n"
  "v_mfma_f32_16x16x32_bf16 v[0:3], v[52:55], a[120:123], v[0:3]\n"
  "s_add_u32 s57, s57, 6144\n"
  "s_add_u32 m0, s59, 12288\n"
  "buffer_store_dwordx4 v[84:87], v97, s[48:51], s68 offen offset:128 nt\n"
  "ds_read_b128 v[60:63], v92 offset:7168\n"
  "ds_read_b64_tr_b16 a[202:203], v105 offset:15360\n"
  "ds_read_b64_tr_b16 a[206:207], v106 offset:15360\n"
  "v_mfma_f32_16x16x32_bf16 v[4:7], v[48:51], a[100:103], v[4:7]\n"
  "s_waitcnt lgkmcnt(6)\n"
  "buffer_load_dwordx4 v117, s[40:43], s57 offen offset:0  lds\n"
  "buffer_load_dwordx4 v118, s[40:43], s57 offen offset:1024  lds\n"
  "ds_read_b64_tr_b16 a[210:211], v107 offset:17408\n"
  "ds_read_b64_tr_b16 a[214:215], v108 offset:17408\n"
  "v_mfma_f32_16x16x32_bf16 v[4:7], v[52:55], a[124:127], v[4:7]\n"
  "s_waitcnt vmcnt(12)\n"
  "ds_read_b64_tr_b16 a[216:217], v103 offset:0\n"
  "ds_read_b64_tr_b16 a[220:221], v104 offset:0\n"
  "v_xor_b32 v92, 0x4000, v92\n"
  "v_mfma_f32_16x16x32_bf16 v[8:11], v[48:51], a[104:107], v[8:11]\n"
  "buffer_load_dwordx4 v119, s[40:43], s57 offen offset:2048  lds\n"
  "v_mfma_f32_16x16x32_bf16 v[8:11], v[52:55], a[128:131], v[8:11]\n"
  "s_add_u32 s57, s57, 6144\n"
  "s_add_u32 m0, s59, 15360\n"
  "ds_read_b64_tr_b16 a[224:225], v105 offset:0\n"
  "ds_read_b64_tr_b16 a[228:229], v106 offset:0\n"
  "v_smfmac_f32_16x16x64_bf16 v[64:67], v[76:79], v[48:55], v110\n"
  "s_waitcnt lgkmcnt(4)\n"
  "buffer_load_dwordx4 v117, s[40:43], s57 offen offset:0  lds\n"
  "buffer_load_dwordx4 v118, s[40:43], s57 offen offset:1024  lds\n"
  "ds_read_b64_tr_b16 a[232:233], v107 offset:2048\n"
  "ds_read_b64_tr_b16 a[236:237], v108 offset:2048\n"
  "v_mfma_f32_16x16x32_bf16 v[12:15], v[48:51], a[108:111], v[12:15]\n"
  "s_waitcnt vmcnt(12)\n"
  "ds_read_b64_tr_b16 a[218:219], v103 offset:3072\n"
  "ds_read_b64_tr_b16 a[222:223], v104 offset:3072\n"
  "v_mfma_f32_16x16x32_bf16 v[12:15], v[52:55], a[132:135], v[12:15]\n"
  "buffer_load_dwordx4 v119, s[40:43], s57 offen offset:2048  lds\n"
  "v_mfma_f32_16x16x32_bf16 v[16:19], v[48:51], a[112:115], v[16:19]\n"
  "s_add_u32 s57, s57, 6144\n"
  "s_add_u32 m0, s59, 0\n"
  "ds_read_b64_tr_b16 a[226:227], v105 offset:3072\n"
  "ds_read_b64_tr_b16 a[230:231], v106 offset:3072\n"
  "v_mfma_f32_16x16x32_bf16 v[16:19], v[52:55], a[136:139], v[16:19]\n"
  "s_waitcnt lgkmcnt(4)\n"
  "buffer_load_dwordx4 v117, s[40:43], s57 offen offset:0  lds\n"
  "buffer_load_dwordx4 v118, s[40:43], s57 offen offset:1024  lds\n"
  "ds_read_b64_tr_b16 a[234:235], v107 offset:5120\n"
  "ds_read_b64_tr_b16 a[238:239], v108 offset:5120\n"
  "v_mfma_f32_16x16x32_bf16 v[20:23], v[48:51], a[116:119], v[20:23]\n"
  "s_waitcnt vmcnt(12)\n"
  "ds_read_b64_tr_b16 a[240:241], v103 offset:6144\n"
  "ds_read_b64_tr_b16 a[244:245], v104 offset:6144\n"
  "v_mfma_f32_16x16x32_bf16 v[20:23], v[52:55], a[140:143], v[20:23]\n"
  "buffer_load_dwordx4 v119, s[40:43], s57 offen offset:2048  lds\n"
  "v_mfma_f32_16x16x32_bf16 v[20:23], v[56:59], a[164:167], v[20:23]\n"
  "s_add_u32 s57, s57, 6144\n"
  "s_add_u32 m0, s59, 3072\n"
  "ds_read_b128 v[48:51], v93 offset:0\n"
  "ds_read_b64_tr_b16 a[248:249], v105 offset:6144\n"
  "ds_read_b64_tr_b16 a[252:253], v106 offset:6144\n"
  "v_mfma_f32_16x16x32_bf16 v[20:23], v[60:63], a[188:191], v[20:23]\n"
  "s_waitcnt lgkmcnt(5)\n"
  "buffer_load_dwordx4 v117, s[40:43], s57 offen offset:0  lds\n"
  "buffer_load_dwordx4 v118, s[40:43], s57 offen offset:1024  lds\n"
  "ds_read_b128 v[52:55], v93 offset:1024\n"
  "ds_read_b64_tr_b16 v[128:129], v107 offset:8192\n"
  "ds_read_b64_tr_b16 v[132:133], v108 offset:8192\n"
  "v_mfma_f32_16x16x32_bf16 v[16:19], v[56:59], a[160:163], v[16:19]\n"
  "buffer_store_dwordx4 v[88:91], v97, s[48:51], s68 offen offset:256 nt\n"
  "s_waitcnt vmcnt(13)\n"
  "ds_read_b64_tr_b16 a[242:243], v103 offset:9216\n"
  "ds_read_b64_tr_b16 a[246:247], v104 offset:9216\n"
  "v_mfma_f32_16x16x32_bf16 v[16:19], v[60:63], a[184:187], v[16:19]\n"
  "s_add_u32 s68, s68, s69\n"
  "buffer_load_dwordx4 v119, s[40:43], s57 offen offset:2048  lds\n"
  "v_mfma_f32_16x16x32_bf16 v[12:15], v[56:59], a[156:159], v[12:15]\n"
  "s_add_u32 s57, s57, 6144\n"
  "s_add_u32 m0, s59, 6144\n"
  "ds_read_b64_tr_b16 a[250:251], v105 offset:9216\n"
  "ds_read_b64_tr_b16 a[254:255], v106 offset:9216\n"
  "v_mfma_f32_16x16x32_bf16 v[12:15], v[60:63], a[180:183], v[12:15]\n"
  "s_waitcnt lgkmcnt(4)\n"
  "buffer_load_dwordx4 v117, s[40:43], s57 offen offset:0  lds\n"
  "buffer_load_dwordx4 v118, s[40:43], s57 offen offset:1024  lds\n"
  "ds_read_b64_tr_b16 v[130:131], v107 offset:11264\n"
  "ds_read_b64_tr_b16 v[134:135], v108 offset:11264\n"
  "v_smfmac_f32_16x16x64_bf16 v[64:67], v[76:79], v[56:63], v110\n"
  "s_waitcnt vmcnt(12)\n"
  "ds_read_b64_tr_b16 v[136:137], v103 offset:12288\n"
  "ds_read_b64_tr_b16 v[140:141], v104 offset:12288\n"
  "v_mfma_f32_16x16x32_bf16 v[8:11], v[56:59], a[152:155], v[8:11]\n"
  "s_mov_b32 s69, s70\n"
  "buffer_load_dwordx4 v119, s[40:43], s57 offen offset:2048  lds\n"
  "v_mfma_f32_16x16x32_bf16 v[8:11], v[60:63], a[176:179], v[8:11]\n"
  "s_add_u32 s57, s57, 6144\n"
  "s_add_u32 m0, s59, 9216\n"
  "ds_read_b64_tr_b16 v[144:145], v105 offset:12288\n"
  "ds_read_b64_tr_b16 v[148:149], v106 offset:12288\n"
  "v_mfma_f32_16x16x32_bf16 v[4:7], v[56:59], a[148:151], v[4:7]\n"
  "s_waitcnt lgkmcnt(4)\n"
  "buffer_load_dwordx4 v117, s[40:43], s57 offen offset:0  lds\n"
  "buffer_load_dwordx4 v118, s[40:43], s57 offen offset:1024  lds\n"
  "ds_read_b64_tr_b16 v[152:153], v107 offset:14336\n"
  "ds_read_b64_tr_b16 v[156:157], v108 offset:14336\n"
  "v_mfma_f32_16x16x32_bf16 v[4:7], v[60:63], a[172:175], v[4:7]\n"
  "s_waitcnt vmcnt(12)\n"
  "ds_read_b64_tr_b16 v[138:139], v103 offset:15360\n"
  "ds_read_b64_tr_b16 v[142:143], v104 offset:15360\n"
  "v_add_f32 v68, v64, v65\n"
  "v_mfma_f32_16x16x32_bf16 v[0:3], v[56:59], a[144:147], v[0:3]\n"
  "s_movk_i32 s70, 6144\n"
  "buffer_load_dwordx4 v119, s[40:43], s57 offen offset:2048  lds\n"
  "v_mov_b32 v64, 0\n"
  "v_mfma_f32_16x16x32_bf16 v[0:3], v[60:63], a[168:171], v[0:3]\n"
  "s_add_u32 s57, s57, 6144\n"
  "s_add_u32 m0, s59, 12288\n"
  "ds_read_b64_tr_b16 v[146:147], v105 offset:15360\n"
  "ds_read_b64_tr_b16 v[150:151], v106 offset:15360\n"
  "v_mov_b32 v65, 0\n"
  "v_mfma_f32_16x16x32_bf16 v[0:3], v[48:51], a[192:195], v[0:3]\n"
  "s_waitcnt lgkmcnt(4)\n"
  "buffer_load_dwordx4 v117, s[40:43], s57 offen offset:0  lds\n"
  "buffer_load_dwordx4 v118, s[40:43], s57 offen offset:1024  lds\n"
  "ds_read_b128 v[56:59], v93 offset:2048\n"
  "ds_read_b64_tr_b16 v[154:155], v107 offset:17408\n"
  "ds_read_b64_tr_b16 v[158:159], v108 offset:17408\n"
  "v_mfma_f32_16x16x32_bf16 v[0:3], v[52:55], a[216:219], v[0:3]\n"
  "ds_read_b128 v[60:63], v93 offset:3072\n"
  "s_waitcnt vmcnt(12)\n"
  "ds_read_b64_tr_b16 v[160:161], v103 offset:0\n"
  "ds_read_b64_tr_b16 v[164:165], v104 offset:0\n"
  "v_mfma_f32_16x16x32_bf16 v[4:7], v[48:51], a[196:199], v[4:7]\n"
  "s_add_u32 s72, s72, 1\n"
  "buffer_load_dwordx4 v119, s[40:43], s57 offen offset:2048  lds\n"
  "v_mfma_f32_16x16x32_bf16 v[4:7], v[52:55], a[220:223], v[4:7]\n"
  "s_add_u32 s57, s57, 6144\n"
  "s_add_u32 m0, s59, 15360\n"
  "ds_read_b64_tr_b16 v[168:169], v105 offset:0\n"
  "ds_read_b64_tr_b16 v[172:173], v106 offset:0\n"
  "v_mfma_f32_16x16x32_bf16 v[8:11], v[48:51], a[200:203], v[8:11]\n"
  "s_waitcnt lgkmcnt(5)\n"
  "buffer_load_dwordx4 v117, s[40:43], s57 offen offset:0  lds\n"
  "buffer_load_dwordx4 v118, s[40:43], s57 offen offset:1024  lds\n"
  "ds_read_b64_tr_b16 v[176:177], v107 offset:2048\n"
  "ds_read_b64_tr_b16 v[180:181], v108 offset:2048\n"
  "v_mfma_f32_16x16x32_bf16 v[8:11], v[52:55], a[224:227], v[8:11]\n"
  "s_waitcnt vmcnt(11)\n"
  "ds_read_b64_tr_b16 v[162:163], v103 offset:3072\n"
  "ds_read_b64_tr_b16 v[166:167], v104 offset:3072\n"
  "v_mfma_f32_16x16x32_bf16 v[12:15], v[48:51], a[204:207], v[12:15]\n"
  "s_lshr_b32 s74, s72, 1\n"
  "buffer_load_dwordx4 v119, s[40:43], s57 offen offset:2048  lds\n"
  "v_readlane_b32 s73, v116, s74\n"
  "v_mfma_f32_16x16x32_bf16 v[12:15], v[52:55], a[228:231], v[12:15]\n"
  "s_add_u32 s57, s57, 6144\n"
  "s_add_u32 m0, s59, 0\n"
  "ds_read_b64_tr_b16 v[170:171], v105 offset:3072\n"
  "ds_read_b64_tr_b16 v[174:175], v106 offset:3072\n"
  "v_mfma_f32_16x16x32_bf16 v[16:19], v[48:51], a[208:211], v[16:19]\n"
  "s_waitcnt lgkmcnt(4)\n"
  "buffer_load_dwordx4 v117, s[40:43], s57 offen offset:0  lds\n"
  "buffer_load_dwordx4 v118, s[40:43], s57 offen offset:1024  lds\n"
  "ds_read_b64_tr_b16 v[178:179], v107 offset:5120\n"
  "ds_read_b64_tr_b16 v[182:183], v108 offset:5120\n"
  "v_mfma_f32_16x16x32_bf16 v[16:19], v[52:55], a[232:235], v[16:19]\n"
  "s_waitcnt vmcnt(11)\n"
  "ds_read_b64_tr_b16 v[184:185], v103 offset:6144\n"
  "ds_read_b64_tr_b16 v[188:189], v104 offset:6144\n"
  "v_mfma_f32_16x16x32_bf16 v[20:23], v[48:51], a[212:215], v[20:23]\n"
  "s_lshl_b32 s74, s72, 4\n"
  "buffer_load_dwordx4 v119, s[40:43], s57 offen offset:2048  lds\n"
  "v_mfma_f32_16x16x32_bf16 v[20:23], v[52:55], a[236:239], v[20:23]\n"
  "s_add_u32 s57, s57, 6144\n"
  "s_add_u32 m0, s59, 3072\n"
  "ds_read_b64_tr_b16 v[192:193], v105 offset:6144\n"
  "ds_read_b64_tr_b16 v[196:197], v106 offset:6144\n"
  "v_mfma_f32_16x16x32_bf16 v[20:23], v[56:59], v[132:135], v[20:23]\n"
  "s_waitcnt lgkmcnt(4)\n"
  "buffer_load_dwordx4 v117, s[40:43], s57 offen offset:0  lds\n"
  "buffer_load_dwordx4 v118, s[40:43], s57 offen offset:1024  lds\n"
  "ds_read_b128 v[48:51], v93 offset:4096\n"
  "ds_read_b64_tr_b16 v[200:201], v107 offset:8192\n"
  "ds_read_b64_tr_b16 v[204:205], v108 offset:8192\n"
  "v_mfma_f32_16x16x32_bf16 v[20:23], v[60:63], v[156:159], v[20:23]\n"
  "ds_read_b128 v[52:55], v93 offset:5120\n"
  "s_waitcnt vmcnt(11)\n"
  "ds_read_b64_tr_b16 v[186:187], v103 offset:9216\n"
  "ds_read_b64_tr_b16 v[190:191], v104 offset:9216\n"
  "v_mfma_f32_16x16x32_bf16 v[16:19], v[56:59], v[128:131], v[16:19]\n"
  "s_lshr_b32 s73, s73, s74\n"
  "buffer_load_dwordx4 v119, s[40:43], s57 offen offset:2048  lds\n"
  "v_mfma_f32_16x16x32_bf16 v[16:19], v[60:63], v[152:155], v[16:19]\n"
  "s_add_u32 s57, s57, 6144\n"
  "ds_read_b64_tr_b16 v[194:195], v105 offset:9216\n"
  "ds_read_b64_tr_b16 v[198:199], v106 offset:9216\n"
  "v_mfma_f32_16x16x32_bf16 v[12:15], v[56:59], a[252:255], v[12:15]\n"
  "s_and_b32 s73, s73, 0xffff\n"
  "ds_read_b64_tr_b16 v[202:203], v107 offset:11264\n"
  "ds_read_b64_tr_b16 v[206:207], v108 offset:11264\n"
  "v_mfma_f32_16x16x32_bf16 v[12:15], v[60:63], v[148:151], v[12:15]\n"
  "s_waitcnt vmcnt(9)\n"
  "ds_read_b64_tr_b16 v[208:209], v103 offset:12288\n"
  "ds_read_b64_tr_b16 v[212:213], v104 offset:12288\n"
  "v_mfma_f32_16x16x32_bf16 v[8:11], v[56:59], a[248:251], v[8:11]\n"
  "s_lshl_b32 s73, s73, 13\n"
  "ds_write_b32 v98, v68\n"
  "v_mfma_f32_16x16x32_bf16 v[8:11], v[60:63], v[144:147], v[8:11]\n"
  "ds_read_b64_tr_b16 v[216:217], v105 offset:12288\n"
  "ds_read_b64_tr_b16 v[220:221], v106 offset:12288\n"
  "v_mfma_f32_16x16x32_bf16 v[4:7], v[56:59], a[244:247], v[4:7]\n"
  "ds_read_b64_tr_b16 v[224:225], v107 offset:14336\n"
  "ds_read_b64_tr_b16 v[228:229], v108 offset:14336\n"
  "v_mfma_f32_16x16x32_bf16 v[4:7], v[60:63], v[140:143], v[4:7]\n"
  "s_waitcnt vmcnt(6)\n"
  "ds_read_b64_tr_b16 v[210:211], v103 offset:15360\n"
  "ds_read_b64_tr_b16 v[214:215], v104 offset:15360\n"
  "v_mfma_f32_16x16x32_bf16 v[0:3], v[56:59], a[240:243], v[0:3]\n"
  "v_rcp_f32 v72, v72\n"
  "v_mfma_f32_16x16x32_bf16 v[0:3], v[60:63], v[136:139], v[0:3]\n"
  "s_waitcnt lgkmcnt(15)\n"
  "s_waitcnt lgkmcnt(9)\n"
  "ds_read_b64_tr_b16 v[218:219], v105 offset:15360\n"
  "ds_read_b64_tr_b16 v[222:223], v106 offset:15360\n"
  "v_mul_f32 v40, v40, v72\n"
  "v_mfma_f32_16x16x32_bf16 v[0:3], v[48:51], v[160:163], v[0:3]\n"
  "ds_read_b128 v[56:59], v93 offset:6144\n"
  "ds_read_b64_tr_b16 v[226:227], v107 offset:17408\n"
  "ds_read_b64_tr_b16 v[230:231], v108 offset:17408\n"
  "v_mfma_f32_16x16x32_bf16 v[0:3], v[52:55], v[184:187], v[0:3]\n"
  "ds_read_b128 v[60:63], v93 offset:7168\n"
  "s_waitcnt vmcnt(3)\n"
  "ds_read_b64_tr_b16 v[232:233], v103 offset:0\n"
  "ds_read_b64_tr_b16 v[236:237], v104 offset:0\n"
  "v_mfma_f32_16x16x32_bf16 v[4:7], v[48:51], v[164:167], v[4:7]\n"
  "v_rcp_f32 v73, v73\n"
  "v_rcp_f32 v74, v74\n"
  "v_rcp_f32 v75, v75\n"
  "v_mfma_f32_16x16x32_bf16 v[4:7], v[52:55], v[188:191], v[4:7]\n"
  "ds_read_b64_tr_b16 v[240:241], v105 offset:0\n"
  "ds_read_b64_tr_b16 v[244:245], v106 offset:0\n"
  "v_mul_f32 v44, v44, v72\n"
  "v_cvt_pk_bf16_f32 v40, v40, v44\n"
  "v_mul_f32 v41, v41, v73\n"
  "v_mul_f32 v45, v45, v73\n"
  "v_cvt_pk_bf16_f32 v41, v41, v45\n"
  "v_mul_f32 v42, v42, v74\n"
  "v_mul_f32 v46, v46, v74\n"
  "v_cvt_pk_bf16_f32 v42, v42, v46\n"
  "v_mul_f32 v43, v43, v75\n"
  "v_mul_f32 v47, v47, v75\n"
  "v_cvt_pk_bf16_f32 v43, v43, v47\n"
  "v_mul_f32 v24, v24, v72\n"
  "v_mul_f32 v25, v25, v73\n"
  "v_mul_f32 v28, v28, v72\n"
  "v_mul_f32 v29, v29, v73\n"
  "v_mul_f32 v32, v32, v72\n"
  "v_mul_f32 v33, v33, v73\n"
  "v_mul_f32 v36, v36, v72\n"
  "v_mul_f32 v37, v37, v73\n"
  "v_cvt_pk_bf16_f32 v24, v24, v28\n"
  "v_cvt_pk_bf16_f32 v28, v25, v29\n"
  "v_cvt_pk_bf16_f32 v25, v32, v36\n"
  "v_cvt_pk_bf16_f32 v29, v33, v37\n"
  "v_mul_f32 v26, v26, v74\n"
  "v_mul_f32 v27, v27, v75\n"
  "v_mul_f32 v30, v30, v74\n"
  "v_mul_f32 v31, v31, v75\n"
  "v_mul_f32 v34, v34, v74\n"
  "v_mul_f32 v35, v35, v75\n"
  "v_mul_f32 v38, v38, v74\n"
  "v_mul_f32 v39, v39, v75\n"
  "v_cvt_pk_bf16_f32 v26, v26, v30\n"
  "v_cvt_pk_bf16_f32 v30, v27, v31\n"
  "v_cvt_pk_bf16_f32 v27, v34, v38\n"
  "v_cvt_pk_bf16_f32 v31, v35, v39\n"
  "v_mfma_f32_16x16x32_bf16 v[8:11], v[48:51], v[168:171], v[8:11]\n"
  "ds_read_b64_tr_b16 v[248:249], v107 offset:2048\n"
  "ds_read_b64_tr_b16 v[252:253], v108 offset:2048\n"
  "ds_write2_b32 v95, v40, v41 offset0:0 offset1:72\n"
  "ds_write2_b32 v95, v42, v43 offset0:144 offset1:216\n"
  "ds_write_b64 v94, v[24:25] offset:0\n"
  "ds_write_b64 v94, v[28:29] offset:288\n"
  "ds_write_b64 v94, v[26:27] offset:576\n"
  "ds_write_b64 v94, v[30:31] offset:864\n"
  "v_mfma_f32_16x16x32_bf16 v[8:11], v[52:55], v[192:195], v[8:11]\n"
  "s_waitcnt vmcnt(0)\n"
  "ds_read_b64_tr_b16 v[234:235], v103 offset:3072\n"
  "ds_read_b64_tr_b16 v[238:239], v104 offset:3072\n"
  "v_max3_f32 v109, v109, v72, v73\n"
  "v_mfma_f32_16x16x32_bf16 v[12:15], v[48:51], v[172:175], v[12:15]\n"
  "v_max3_f32 v109, v109, v74, v75\n"
  "v_xor_b32 v94, 0x2000, v94\n"
  "v_xor_b32 v95, 0x2000, v95\n"
  "v_mfma_f32_16x16x32_bf16 v[12:15], v[52:55], v[196:199], v[12:15]\n"
  "ds_read_b64_tr_b16 v[242:243], v105 offset:3072\n"
  "ds_read_b64_tr_b16 v[246:247], v106 offset:3072\n"
  "v_xor_b32 v98, 0x2000, v98\n"
  "v_mfma_f32_16x16x32_bf16 v[16:19], v[48:51], v[176:179], v[16:19]\n"
  "ds_read_b64_tr_b16 v[250:251], v107 offset:5120\n"
  "ds_read_b64_tr_b16 v[254:255], v108 offset:5120\n"
  "v_xor_b32 v93, 0x4000, v93\n"
  "v_mfma_f32_16x16x32_bf16 v[16:19], v[52:55], v[200:203], v[16:19]\n"
  "v_mfma_f32_16x16x32_bf16 v[20:23], v[48:51], v[180:183], v[20:23]\n"
  "v_mfma_f32_16x16x32_bf16 v[20:23], v[52:55], v[204:207], v[20:23]\n"
  "s_add_u32 m0, s66, 0\n"
  "s_nop 0\n"
  "buffer_load_dwordx4 v100, s[44:47], s67 offen offset:0 nt lds\n"
  "s_add_u32 m0, s66, 1920\n"
  "s_nop 0\n"
  "buffer_load_dwordx4 v100, s[44:47], s67 offen offset:128 nt lds\n"
  "s_add_u32 m0, s66, 3840\n"
  "s_nop 0\n"
  "buffer_load_dwordx4 v100, s[44:47], s67 offen offset:256 nt lds\n"
  "s_add_u32 s66, s66, 6144\n"
  "s_cmp_ge_u32 s66, 86016\n"
  "s_cselect_b32 s64, 36864, 0\n"
  "s_sub_u32 s66, s66, s64\n"
  "s_sub_u32 s90, s90, 1\n"
  "s_cselect_b32 s63, s91, 6144\n"
  "s_cmov_b64 s[90:91], s[92:93]\n"
  "s_cmov_b64 s[92:93], s[94:95]\n"
  "s_add_u32 s67, s67, s63\n"
  "s_add_u32 m0, s66, 0\n"
  "s_nop 0\n"
  "buffer_load_dwordx4 v100, s[44:47], s67 offen offset:0 nt lds\n"
  "s_add_u32 m0, s66, 1920\n"
  "s_nop 0\n"
  "buffer_load_dwordx4 v100, s[44:47], s67 offen offset:128 nt lds\n"
  "s_add_u32 m0, s66, 3840\n"
  "s_nop 0\n"
  "buffer_load_dwordx4 v100, s[44:47], s67 offen offset:256 nt lds\n"
  "s_add_u32 s66, s66, 6144\n"
  "s_cmp_ge_u32 s66, 86016\n"
  "s_cselect_b32 s64, 36864, 0\n"
  "s_sub_u32 s66, s66, s64\n"
  "s_sub_u32 s90, s90, 1\n"
  "s_cselect_b32 s63, s91, 6144\n"
  "s_cmov_b64 s[90:91], s[92:93]\n"
  "s_cmov_b64 s[92:93], s[94:95]\n"
  "s_add_u32 s67, s67, s63\n"
  "s_add_u32 m0, s66, 0\n"
  "s_nop 0\n"
  "buffer_load_dwordx4 v100, s[44:47], s67 offen offset:0 nt lds\n"
  "s_add_u32 m0, s66, 1920\n"
  "s_nop 0\n"
  "buffer_load_dwordx4 v100, s[44:47], s67 offen offset:128 nt lds\n"
  "s_add_u32 m0, s66, 3840\n"
  "s_nop 0\n"
  "buffer_load_dwordx4 v100, s[44:47], s67 offen offset:256 nt lds\n"
  "s_add_u32 s66, s66, 6144\n"
  "s_cmp_ge_u32 s66, 86016\n"
  "s_cselect_b32 s64, 36864, 0\n"
  "s_sub_u32 s66, s66, s64\n"
  "s_sub_u32 s90, s90, 1\n"
  "s_cselect_b32 s63, s91, 6144\n"
  "s_cmov_b64 s[90:91], s[92:93]\n"
  "s_cmov_b64 s[92:93], s[94:95]\n"
  "s_add_u32 s67, s67, s63\n"
  "s_add_u32 m0, s66, 0\n"
  "s_nop 0\n"
  "buffer_load_dwordx4 v100, s[44:47], s67 offen offset:0 nt lds\n"
  "s_add_u32 m0, s66, 1920\n"
  "s_nop 0\n"
  "buffer_load_dwordx4 v100, s[44:47], s67 offen offset:128 nt lds\n"
  "s_add_u32 m0, s66, 3840\n"
  "s_nop 0\n"
  "buffer_load_dwordx4 v100, s[44:47], s67 offen offset:256 nt lds\n"
  "s_add_u32 s66, s66, 6144\n"
  "s_cmp_ge_u32 s66, 86016\n"
  "s_cselect_b32 s64, 36864, 0\n"
  "s_sub_u32 s66, s66, s64\n"
  "s_sub_u32 s90, s90, 1\n"
  "s_cselect_b32 s63, s91, 6144\n"
  "s_cmov_b64 s[90:91], s[92:93]\n"
  "s_cmov_b64 s[92:93], s[94:95]\n"
  "s_add_u32 s67, s67, s63\n"
  "B_H_%=:\n"
  "s_cmp_gt_i32 s56, s61\n"
  "s_cbranch_scc0 B_H2_%=\n"
  "s_waitcnt vmcnt(16)\n"
  "B_H2_%=:\n"
  "s_waitcnt vmcnt(32) lgkmcnt(0)\n"
  "s_barrier\n"
  "v_mfma_f32_16x16x32_bf16 v[20:23], v[56:59], v[228:231], v[20:23]\n"
  "s_sub_u32 s56, s56, 1\n"
  "ds_read_b128 v[48:51], v92 offset:0\n"
  "ds_read_b128 v[72:75], v99\n"
  "v_mfma_f32_16x16x32_bf16 v[44:47], v[60:63], v[252:255], v[20:23]\n"
  "ds_read_b128 v[52:55], v92 offset:1024\n"
  "ds_read_b128 v[112:115], v99 offset:64\n"
  "v_mfma_f32_16x16x32_bf16 v[16:19], v[56:59], v[224:227], v[16:19]\n"
  "s_add_u32 m0, s66, 0\n"
  "ds_read_b64_tr_b16 v[80:81], v96 offset:0\n"
  "ds_read_b64_tr_b16 v[82:83], v96 offset:32\n"
  "v_mfma_f32_16x16x32_bf16 v[40:43], v[60:63], v[248:251], v[16:19]\n"
  "ds_read_b64_tr_b16 v[84:85], v96 offset:2304\n"
  "ds_read_b64_tr_b16 v[86:87], v96 offset:2336\n"
  "v_mfma_f32_16x16x32_bf16 v[12:15], v[56:59], v[220:223], v[12:15]\n"
  "ds_read_b64_tr_b16 v[88:89], v96 offset:4608\n"
  "ds_read_b64_tr_b16 v[90:91], v96 offset:4640\n"
  "v_mfma_f32_16x16x32_bf16 v[36:39], v[60:63], v[244:247], v[12:15]\n"
  "buffer_load_dwordx4 v100, s[44:47], s67 offen offset:0 nt lds\n"
  "v_mfma_f32_16x16x32_bf16 v[8:11], v[56:59], v[216:219], v[8:11]\n"
  "s_add_u32 m0, s66, 1920\n"
  "v_xor_b32 v96, 0x2000, v96\n"
  "v_xor_b32 v99, 0x2000, v99\n"
  "v_mfma_f32_16x16x32_bf16 v[32:35], v[60:63], v[240:243], v[8:11]\n"
  "s_waitcnt lgkmcnt(6)\n"
  "v_add_f32 v72, v72, v112\n"
  "v_add_f32 v73, v73, v113\n"
  "v_mfma_f32_16x16x32_bf16 v[4:7], v[56:59], v[212:215], v[4:7]\n"
  "v_add_f32 v74, v74, v114\n"
  "v_add_f32 v75, v75, v115\n"
  "v_mfma_f32_16x16x32_bf16 v[28:31], v[60:63], v[236:239], v[4:7]\n"
  "buffer_load_dwordx4 v100, s[44:47], s67 offen offset:128 nt lds\n"
  "v_mfma_f32_16x16x32_bf16 v[0:3], v[56:59], v[208:211], v[0:3]\n"
  "s_add_u32 m0, s66, 3840\n"
  "v_max3_f32 v109, v109, v72, v73\n"
  "v_max3_f32 v109, v109, v74, v75\n"
  "v_mfma_f32_16x16x32_bf16 v[24:27], v[60:63], v[232:235], v[0:3]\n"
  "v_rcp_f32 v72, v72\n"
  "v_mfma_f32_16x16x32_bf16 v[0:3], v[48:51], a[0:3], 0\n"
  "ds_read_b128 v[56:59], v92 offset:2048\n"
  "v_mul_f32 v40, v40, v72\n"
  "v_mfma_f32_16x16x32_bf16 v[0:3], v[52:55], a[24:27], v[0:3]\n"
  "buffer_load_dwordx4 v100, s[44:47], s67 offen offset:256 nt lds\n"
  "ds_read_b128 v[60:63], v92 offset:3072\n"
  "v_mfma_f32_16x16x32_bf16 v[4:7], v[48:51], a[4:7], 0\n"
  "s_mov_b32 m0, 159744\n"
  "v_rcp_f32 v73, v73\n"
  "v_mfma_f32_16x16x32_bf16 v[4:7], v[52:55], a[28:31], v[4:7]\n"
  "v_rcp_f32 v74, v74\n"
  "v_mfma_f32_16x16x32_bf16 v[8:11], v[48:51], a[8:11], 0\n"
  "v_rcp_f32 v75, v75\n"
  "v_mfma_f32_16x16x32_bf16 v[8:11], v[52:55], a[32:35], v[8:11]\n"
  "buffer_load_dword v111, s[80:83], s73 offen  lds\n"
  "v_smfmac_f32_16x16x64_bf16 v[64:67], v[76:79], v[48:55], v110\n"
  "v_mul_f32 v44, v44, v72\n"
  "v_cvt_pk_bf16_f32 v40, v40, v44\n"
  "v_mfma_f32_16x16x32_bf16 v[12:15], v[48:51], a[12:15], 0\n"
  "v_mul_f32 v41, v41, v73\n"
  "v_mul_f32 v45, v45, v73\n"
  "v_mfma_f32_16x16x32_bf16 v[12:15], v[52:55], a[36:39], v[12:15]\n"
  "v_cvt_pk_bf16_f32 v41, v41, v45\n"
  "v_mul_f32 v42, v42, v74\n"
  "v_mfma_f32_16x16x32_bf16 v[16:19], v[48:51], a[16:19], 0\n"
  "v_mul_f32 v46, v46, v74\n"
  "v_cvt_pk_bf16_f32 v42, v42, v46\n"
  "v_mfma_f32_16x16x32_bf16 v[16:19], v[52:55], a[40:43], v[16:19]\n"
  "v_mul_f32 v43, v43, v75\n"
  "v_mul_f32 v47, v47, v75\n"
  "v_mfma_f32_16x16x32_bf16 v[20:23], v[48:51], a[20:23], 0\n"
  "v_cvt_pk_bf16_f32 v43, v43, v47\n"
  "v_mul_f32 v24, v24, v72\n"
  "v_mfma_f32_16x16x32_bf16 v[20:23], v[52:55], a[44:47], v[20:23]\n"
  "s_waitcnt lgkmcnt(0)\n"
  "v_mul_f32 v25, v25, v73\n"
  "v_mul_f32 v28, v28, v72\n"
  "v_mfma_f32_16x16x32_bf16 v[20:23], v[56:59], a[68:71], v[20:23]\n"
  "ds_read_b128 v[48:51], v92 offset:4096\n"
  "v_mul_f32 v29, v29, v73\n"
  "v_mfma_f32_16x16x32_bf16 v[20:23], v[60:63], a[92:95], v[20:23]\n"
  "buffer_store_dwordx4 v[80:83], v97, s[48:51], s68 offen offset:0 nt\n"
  "ds_read_b128 v[52:55], v92 offset:5120\n"
  "v_mul_f32 v32, v32, v72\n"
  "v_mul_f32 v33, v33, v73\n"
  "v_mfma_f32_16x16x32_bf16 v[16:19], v[56:59], a[64:67], v[16:19]\n"
  "v_mul_f32 v36, v36, v72\n"
  "v_mul_f32 v37, v37, v73\n"
  "v_mfma_f32_16x16x32_bf16 v[16:19], v[60:63], a[88:91], v[16:19]\n"
  "v_cvt_pk_bf16_f32 v24, v24, v28\n"
  "v_cvt_pk_bf16_f32 v28, v25, v29\n"
  "v_mfma_f32_16x16x32_bf16 v[12:15], v[56:59], a[60:63], v[12:15]\n"
  "v_cvt_pk_bf16_f32 v25, v32, v36\n"
  "v_cvt_pk_bf16_f32 v29, v33, v37\n"
  "v_mfma_f32_16x16x32_bf16 v[12:15], v[60:63], a[84:87], v[12:15]\n"
  "v_mul_f32 v26, v26, v74\n"
  "v_mul_f32 v27, v27, v75\n"
  "v_smfmac_f32_16x16x64_bf16 v[64:67], v[76:79], v[56:63], v110\n"
  "v_mul_f32 v30, v30, v74\n"
  "v_mul_f32 v31, v31, v75\n"
  "v_mfma_f32_16x16x32_bf16 v[8:11], v[56:59], a[56:59], v[8:11]\n"
  "v_mul_f32 v34, v34, v74\n"
  "v_mul_f32 v35, v35, v75\n"
  "v_mfma_f32_16x16x32_bf16 v[8:11], v[60:63], a[80:83], v[8:11]\n"
  "v_mul_f32 v38, v38, v74\n"
  "v_mul_f32 v39, v39, v75\n"
  "v_mfma_f32_16x16x32_bf16 v[4:7], v[56:59], a[52:55], v[4:7]\n"
  "v_cvt_pk_bf16_f32 v26, v26, v30\n"
  "v_cvt_pk_bf16_f32 v30, v27, v31\n"
  "v_mfma_f32_16x16x32_bf16 v[4:7], v[60:63], a[76:79], v[4:7]\n"
  "v_cvt_pk_bf16_f32 v27, v34, v38\n"
  "v_cvt_pk_bf16_f32 v31, v35, v39\n"
  "v_mfma_f32_16x16x32_bf16 v[0:3], v[56:59], a[48:51], v[0:3]\n"
  "v_max3_f32 v109, v109, v72, v73\n"
  "v_max3_f32 v109, v109, v74, v75\n"
  "v_mfma_f32_16x16x32_bf16 v[0:3], v[60:63], a[72:75], v[0:3]\n"
  "s_waitcnt lgkmcnt(0)\n"
  "v_mfma_f32_16x16x32_bf16 v[0:3], v[48:51], a[96:99], v[0:3]\n"
  "ds_read_b128 v[56:59], v92 offset:6144\n"
  "v_mfma_f32_16x16x32_bf16 v[0:3], v[52:55], a[120:123], v[0:3]\n"
  "buffer_store_dwordx4 v[84:87], v97, s[48:51], s68 offen offset:128 nt\n"
  "ds_read_b128 v[60:63], v92 offset:7168\n"
  "ds_write2_b32 v95, v40, v41 offset0:0 offset1:72\n"
  "v_mfma_f32_16x16x32_bf16 v[4:7], v[48:51], a[100:103], v[4:7]\n"
  "v_xor_b32 v92, 0x4000, v92\n"
  "v_mfma_f32_16x16x32_bf16 v[4:7], v[52:55], a[124:127], v[4:7]\n"
  "ds_write2_b32 v95, v42, v43 offset0:144 offset1:216\n"
  "v_mfma_f32_16x16x32_bf16 v[8:11], v[48:51], a[104:107], v[8:11]\n"
  "v_mfma_f32_16x16x32_bf16 v[8:11], v[52:55], a[128:131], v[8:11]\n"
  "ds_write_b64 v94, v[24:25] offset:0\n"
  "v_smfmac_f32_16x16x64_bf16 v[64:67], v[76:79], v[48:55], v110\n"
  "ds_write_b64 v94, v[28:29] offset:288\n"
  "v_mfma_f32_16x16x32_bf16 v[12:15], v[48:51], a[108:111], v[12:15]\n"
  "v_mfma_f32_16x16x32_bf16 v[12:15], v[52:55], a[132:135], v[12:15]\n"
  "ds_write_b64 v94, v[26:27] offset:576\n"
  "v_mfma_f32_16x16x32_bf16 v[16:19], v[48:51], a[112:115], v[16:19]\n"
  "v_mfma_f32_16x16x32_bf16 v[16:19], v[52:55], a[136:139], v[16:19]\n"
  "ds_write_b64 v94, v[30:31] offset:864\n"
  "v_mfma_f32_16x16x32_bf16 v[20:23], v[48:51], a[116:119], v[20:23]\n"
  "v_mfma_f32_16x16x32_bf16 v[20:23], v[52:55], a[140:143], v[20:23]\n"
  "s_waitcnt lgkmcnt(6)\n"
  "v_mfma_f32_16x16x32_bf16 v[20:23], v[56:59], a[164:167], v[20:23]\n"
  "s_add_u32 s66, s66, 6144\n"
  "ds_read_b128 v[48:51], v93 offset:0\n"
  "v_mfma_f32_16x16x32_bf16 v[20:23], v[60:63], a[188:191], v[20:23]\n"
  "s_cmp_ge_u32 s66, 86016\n"
  "s_cselect_b32 s64, 36864, 0\n"
  "buffer_store_dwordx4 v[88:91], v97, s[48:51], s68 offen offset:256 nt\n"
  "ds_read_b128 v[52:55], v93 offset:1024\n"
  "v_mfma_f32_16x16x32_bf16 v[16:19], v[56:59], a[160:163], v[16:19]\n"
  "s_sub_u32 s66, s66, s64\n"
  "v_mfma_f32_16x16x32_bf16 v[16:19], v[60:63], a[184:187], v[16:19]\n"
  "s_sub_u32 s90, s90, 1\n"
  "s_cselect_b32 s63, s91, 6144\n"
  "s_cmov_b64 s[90:91], s[92:93]\n"
  "s_cmov_b64 s[92:93], s[94:95]\n"
  "v_mfma_f32_16x16x32_bf16 v[12:15], v[56:59], a[156:159], v[12:15]\n"
  "s_add_u32 s67, s67, s63\n"
  "v_mfma_f32_16x16x32_bf16 v[12:15], v[60:63], a[180:183], v[12:15]\n"
  "s_add_u32 s68, s68, s69\n"
  "v_smfmac_f32_16x16x64_bf16 v[64:67], v[76:79], v[56:63], v110\n"
  "s_mov_b32 s69, s70\n"
  "v_mfma_f32_16x16x32_bf16 v[8:11], v[56:59], a[152:155], v[8:11]\n"
  "s_movk_i32 s70, 6144\n"
  "v_mfma_f32_16x16x32_bf16 v[8:11], v[60:63], a[176:179], v[8:11]\n"
  "v_mfma_f32_16x16x32_bf16 v[4:7], v[56:59], a[148:151], v[4:7]\n"
  "v_add_f32 v68, v64, v65\n"
  "v_mov_b32 v64, 0\n"
  "v_mfma_f32_16x16x32_bf16 v[4:7], v[60:63], a[172:175], v[4:7]\n"
  "ds_write_b32 v98, v68\n"
  "v_mfma_f32_16x16x32_bf16 v[0:3], v[56:59], a[144:147], v[0:3]\n"
  "v_mov_b32 v65, 0\n"
  "v_mfma_f32_16x16x32_bf16 v[0:3], v[60:63], a[168:171], v[0:3]\n"
  "s_waitcnt lgkmcnt(1)\n"
  "v_mfma_f32_16x16x32_bf16 v[0:3], v[48:51], a[192:195], v[0:3]\n"
  "s_add_u32 s72, s72, 1\n"
  "ds_read_b128 v[56:59], v93 offset:2048\n"
  "v_mfma_f32_16x16x32_bf16 v[0:3], v[52:55], a[216:219], v[0:3]\n"
  "s_lshr_b32 s74, s72, 1\n"
  "ds_read_b128 v[60:63], v93 offset:3072\n"
  "v_readlane_b32 s73, v116, s74\n"
  "v_mfma_f32_16x16x32_bf16 v[4:7], v[48:51], a[196:199], v[4:7]\n"
  "s_lshl_b32 s74, s72, 4\n"
  "v_mfma_f32_16x16x32_bf16 v[4:7], v[52:55], a[220:223], v[4:7]\n"
  "s_lshr_b32 s73, s73, s74\n"
  "v_mfma_f32_16x16x32_bf16 v[8:11], v[48:51], a[200:203], v[8:11]\n"
  "s_and_b32 s73, s73, 0xffff\n"
  "v_mfma_f32_16x16x32_bf16 v[8:11], v[52:55], a[224:227], v[8:11]\n"
  "s_lshl_b32 s73, s73, 13\n"
  "v_mfma_f32_16x16x32_bf16 v[12:15], v[48:51], a[204:207], v[12:15]\n"
  "v_mfma_f32_16x16x32_bf16 v[12:15], v[52:55], a[228:231], v[12:15]\n"
  "v_mfma_f32_16x16x32_bf16 v[16:19], v[48:51], a[208:211], v[16:19]\n"
  "v_mfma_f32_16x16x32_bf16 v[16:19], v[52:55], a[232:235], v[16:19]\n"
  "v_mfma_f32_16x16x32_bf16 v[20:23], v[48:51], a[212:215], v[20:23]\n"
  "v_mfma_f32_16x16x32_bf16 v[20:23], v[52:55], a[236:239], v[20:23]\n"
  "s_waitcnt lgkmcnt(0)\n"
  "v_mfma_f32_16x16x32_bf16 v[20:23], v[56:59], v[132:135], v[20:23]\n"
  "ds_read_b128 v[48:51], v93 offset:4096\n"
  "v_mfma_f32_16x16x32_bf16 v[20:23], v[60:63], v[156:159], v[20:23]\n"
  "ds_read_b128 v[52:55], v93 offset:5120\n"
  "v_mfma_f32_16x16x32_bf16 v[16:19], v[56:59], v[128:131], v[16:19]\n"
  "v_mfma_f32_16x16x32_bf16 v[16:19], v[60:63], v[152:155], v[16:19]\n"
  "v_mfma_f32_16x16x32_bf16 v[12:15], v[56:59], a[252:255], v[12:15]\n"
  "v_mfma_f32_16x16x32_bf16 v[12:15], v[60:63], v[148:151], v[12:15]\n"
  "v_mfma_f32_16x16x32_bf16 v[8:11], v[56:59], a[248:251], v[8:11]\n"
  "v_mfma_f32_16x16x32_bf16 v[8:11], v[60:63], v[144:147], v[8:11]\n"
  "v_mfma_f32_16x16x32_bf16 v[4:7], v[56:59], a[244:247], v[4:7]\n"
  "v_mfma_f32_16x16x32_bf16 v[4:7], v[60:63], v[140:143], v[4:7]\n"
  "v_mfma_f32_16x16x32_bf16 v[0:3], v[56:59], a[240:243], v[0:3]\n"
  "v_mfma_f32_16x16x32_bf16 v[0:3], v[60:63], v[136:139], v[0:3]\n"
  "s_waitcnt lgkmcnt(0)\n"
  "v_mfma_f32_16x16x32_bf16 v[0:3], v[48:51], v[160:163], v[0:3]\n"
  "ds_read_b128 v[56:59], v93 offset:6144\n"
  "v_mfma_f32_16x16x32_bf16 v[0:3], v[52:55], v[184:187], v[0:3]\n"
  "ds_read_b128 v[60:63], v93 offset:7168\n"
  "v_xor_b32 v93, 0x4000, v93\n"
  "v_mfma_f32_16x16x32_bf16 v[4:7], v[48:51], v[164:167], v[4:7]\n"
  "v_mfma_f32_16x16x32_bf16 v[4:7], v[52:55], v[188:191], v[4:7]\n"
  "v_mfma_f32_16x16x32_bf16 v[8:11], v[48:51], v[168:171], v[8:11]\n"
  "v_mfma_f32_16x16x32_bf16 v[8:11], v[52:55], v[192:195], v[8:11]\n"
  "v_mfma_f32_16x16x32_bf16 v[12:15], v[48:51], v[172:175], v[12:15]\n"
  "v_xor_b32 v94, 0x2000, v94\n"
  "v_xor_b32 v95, 0x2000, v95\n"
  "v_mfma_f32_16x16x32_bf16 v[12:15], v[52:55], v[196:199], v[12:15]\n"
  "v_xor_b32 v98, 0x2000, v98\n"
  "v_mfma_f32_16x16x32_bf16 v[16:19], v[48:51], v[176:179], v[16:19]\n"
  "v_mfma_f32_16x16x32_bf16 v[16:19], v[52:55], v[200:203], v[16:19]\n"
  "v_mfma_f32_16x16x32_bf16 v[20:23], v[48:51], v[180:183], v[20:23]\n"
  "v_mfma_f32_16x16x32_bf16 v[20:23], v[52:55], v[204:207], v[20:23]\n"
  "s_cmp_eq_u32 s56, 0\n"
  "s_cbranch_scc0 B_H_%=\n"
  "s_mov_b32 s52, s53\n"
  "s_mov_b32 s53, s54\n"
  "s_mov_b32 s54, s55\n"
  "s_cmp_eq_u32 s52, 0\n"
  "s_cbranch_scc1 B_LAST_%=\n"
  "s_lshr_b32 s62, s52, 16\n"
  "s_lshl1_add_u32 s62, s62, s65\n"
  "s_mul_i32 s57, s62, 98304\n"
  "s_and_b32 s56, s52, 0xff\n"
  "s_cmp_lg_u32 s53, 0\n"
  "s_cselect_b32 s62, 5, 0\n"
  "s_sub_u32 s56, s56, s62\n"
  "s_waitcnt vmcnt(16) lgkmcnt(0)\n"
  "s_barrier\n"
  "v_mfma_f32_16x16x32_bf16 v[20:23], v[56:59], v[228:231], v[20:23]\n"
  "ds_read_b128 v[48:51], v92 offset:0\n"
  "ds_read_b128 v[72:75], v99\n"
  "v_mfma_f32_16x16x32_bf16 v[44:47], v[60:63], v[252:255], v[20:23]\n"
  "ds_read_b128 v[52:55], v92 offset:1024\n"
  "ds_read_b128 v[112:115], v99 offset:64\n"
  "v_mfma_f32_16x16x32_bf16 v[16:19], v[56:59], v[224:227], v[16:19]\n"
  "s_add_u32 m0, s66, 0\n"
  "ds_read_b64_tr_b16 v[80:81], v96 offset:0\n"
  "ds_read_b64_tr_b16 v[82:83], v96 offset:32\n"
  "v_mfma_f32_16x16x32_bf16 v[40:43], v[60:63], v[248:251], v[16:19]\n"
  "ds_read_b64_tr_b16 v[84:85], v96 offset:2304\n"
  "ds_read_b64_tr_b16 v[86:87], v96 offset:2336\n"
  "v_mfma_f32_16x16x32_bf16 v[12:15], v[56:59], v[220:223], v[12:15]\n"
  "ds_read_b64_tr_b16 v[88:89], v96 offset:4608\n"
  "ds_read_b64_tr_b16 v[90:91], v96 offset:4640\n"
  "v_mfma_f32_16x16x32_bf16 v[36:39], v[60:63], v[244:247], v[12:15]\n"
  "buffer_load_dwordx4 v100, s[44:47], s67 offen offset:0 nt lds\n"
  "v_mfma_f32_16x16x32_bf16 v[8:11], v[56:59], v[216:219], v[8:11]\n"
  "s_add_u32 m0, s66, 1920\n"
  "v_xor_b32 v96, 0x2000, v96\n"
  "v_xor_b32 v99, 0x2000, v99\n"
  "v_mfma_f32_16x16x32_bf16 v[32:35], v[60:63], v[240:243], v[8:11]\n"
  "s_waitcnt lgkmcnt(6)\n"
  "v_add_f32 v72, v72, v112\n"
  "v_add_f32 v73, v73, v113\n"
  "v_mfma_f32_16x16x32_bf16 v[4:7], v[56:59], v[212:215], v[4:7]\n"
  "v_add_f32 v74, v74, v114\n"
  "v_add_f32 v75, v75, v115\n"
  "v_mfma_f32_16x16x32_bf16 v[28:31], v[60:63], v[236:239], v[4:7]\n"
  "buffer_load_dwordx4 v100, s[44:47], s67 offen offset:128 nt lds\n"
  "v_mfma_f32_16x16x32_bf16 v[0:3], v[56:59], v[208:211], v[0:3]\n"
  "s_add_u32 m0, s66, 3840\n"
  "v_max3_f32 v109, v109, v72, v73\n"
  "v_max3_f32 v109, v109, v74, v75\n"
  "v_mfma_f32_16x16x32_bf16 v[24:27], v[60:63], v[232:235], v[0:3]\n"
  "v_rcp_f32 v72, v72\n"
  "v_mfma_f32_16x16x32_bf16 v[0:3], v[48:51], a[0:3], 0\n"
  "ds_read_b128 v[56:59], v92 offset:2048\n"
  "v_mul_f32 v40, v40, v72\n"
  "v_mfma_f32_16x16x32_bf16 v[0:3], v[52:55], a[24:27], v[0:3]\n"
  "buffer_load_dwordx4 v100, s[44:47], s67 offen offset:256 nt lds\n"
  "ds_read_b128 v[60:63], v92 offset:3072\n"
  "v_mfma_f32_16x16x32_bf16 v[4:7], v[48:51], a[4:7], 0\n"
  "v_rcp_f32 v73, v73\n"
  "v_mfma_f32_16x16x32_bf16 v[4:7], v[52:55], a[28:31], v[4:7]\n"
  "v_rcp_f32 v74, v74\n"
  "v_mfma_f32_16x16x32_bf16 v[8:11], v[48:51], a[8:11], 0\n"
  "v_rcp_f32 v75, v75\n"
  "v_mfma_f32_16x16x32_bf16 v[8:11], v[52:55], a[32:35], v[8:11]\n"
  "v_mul_f32 v44, v44, v72\n"
  "v_cvt_pk_bf16_f32 v40, v40, v44\n"
  "v_smfmac_f32_16x16x64_bf16 v[64:67], v[76:79], v[48:55], v110\n"
  "v_mul_f32 v41, v41, v73\n"
  "v_mul_f32 v45, v45, v73\n"
  "v_mfma_f32_16x16x32_bf16 v[12:15], v[48:51], a[12:15], 0\n"
  "v_cvt_pk_bf16_f32 v41, v41, v45\n"
  "v_mul_f32 v42, v42, v74\n"
  "v_mfma_f32_16x16x32_bf16 v[12:15], v[52:55], a[36:39], v[12:15]\n"
  "s_add_u32 m0, s75, 0\n"
  "v_mul_f32 v46, v46, v74\n"
  "v_cvt_pk_bf16_f32 v42, v42, v46\n"
  "v_mfma_f32_16x16x32_bf16 v[16:19], v[48:51], a[16:19], 0\n"
  "buffer_load_dwordx4 v101, s[76:79], s57 offen offset:0  lds\n"
  "v_mfma_f32_16x16x32_bf16 v[16:19], v[52:55], a[40:43], v[16:19]\n"
  "v_mul_f32 v43, v43, v75\n"
  "v_mul_f32 v47, v47, v75\n"
  "v_mfma_f32_16x16x32_bf16 v[20:23], v[48:51], a[20:23], 0\n"
  "v_cvt_pk_bf16_f32 v43, v43, v47\n"
  "v_mul_f32 v24, v24, v72\n"
  "v_mfma_f32_16x16x32_bf16 v[20:23], v[52:55], a[44:47], v[20:23]\n"
  "s_waitcnt lgkmcnt(0)\n"
  "v_mul_f32 v25, v25, v73\n"
  "v_mul_f32 v28, v28, v72\n"
  "v_mfma_f32_16x16x32_bf16 v[20:23], v[56:59], a[68:71], v[20:23]\n"
  "buffer_load_dwordx4 v102, s[76:79], s57 offen offset:1024  lds\n"
  "ds_read_b128 v[48:51], v92 offset:4096\n"
  "v_mfma_f32_16x16x32_bf16 v[20:23], v[60:63], a[92:95], v[20:23]\n"
  "ds_read_b128 v[52:55], v92 offset:5120\n"
  "v_mul_f32 v29, v29, v73\n"
  "v_mfma_f32_16x16x32_bf16 v[16:19], v[56:59], a[64:67], v[16:19]\n"
  "v_mul_f32 v32, v32, v72\n"
  "v_mul_f32 v33, v33, v73\n"
  "v_mfma_f32_16x16x32_bf16 v[16:19], v[60:63], a[88:91], v[16:19]\n"
  "s_add_u32 m0, s75, 1920\n"
  "buffer_store_dwordx4 v[80:83], v97, s[48:51], s68 offen offset:0 nt\n"
  "ds_write2_b32 v95, v40, v41 offset0:0 offset1:72\n"
  "v_mfma_f32_16x16x32_bf16 v[12:15], v[56:59], a[60:63], v[12:15]\n"
  "buffer_load_dwordx4 v101, s[76:79], s57 offen offset:128  lds\n"
  "v_mfma_f32_16x16x32_bf16 v[12:15], v[60:63], a[84:87], v[12:15]\n"
  "v_mul_f32 v36, v36, v72\n"
  "v_mul_f32 v37, v37, v73\n"
  "v_smfmac_f32_16x16x64_bf16 v[64:67], v[76:79], v[56:63], v110\n"
  "v_cvt_pk_bf16_f32 v24, v24, v28\n"
  "v_cvt_pk_bf16_f32 v28, v25, v29\n"
  "v_mfma_f32_16x16x32_bf16 v[8:11], v[56:59], a[56:59], v[8:11]\n"
  "v_cvt_pk_bf16_f32 v25, v32, v36\n"
  "v_cvt_pk_bf16_f32 v29, v33, v37\n"
  "v_mfma_f32_16x16x32_bf16 v[8:11], v[60:63], a[80:83], v[8:11]\n"
  "buffer_load_dwordx4 v102, s[76:79], s57 offen offset:1152  lds\n"
  "v_mfma_f32_16x16x32_bf16 v[4:7], v[56:59], a[52:55], v[4:7]\n"
  "v_mul_f32 v26, v26, v74\n"
  "v_mul_f32 v27, v27, v75\n"
  "v_mfma_f32_16x16x32_bf16 v[4:7], v[60:63], a[76:79], v[4:7]\n"
  "v_mul_f32 v30, v30, v74\n"
  "v_mul_f32 v31, v31, v75\n"
  "v_mfma_f32_16x16x32_bf16 v[0:3], v[56:59], a[48:51], v[0:3]\n"
  "s_add_u32 m0, s75, 3840\n"
  "v_mul_f32 v34, v34, v74\n"
  "v_mul_f32 v35, v35, v75\n"
  "v_mfma_f32_16x16x32_bf16 v[0:3], v[60:63], a[72:75], v[0:3]\n"
  "s_waitcnt lgkmcnt(1)\n"
  "buffer_load_dwordx4 v101, s[76:79], s57 offen offset:256  lds\n"
  "v_mfma_f32_16x16x32_bf16 v[0:3], v[48:51], a[96:99], v[0:3]\n"
  "ds_read_b128 v[56:59], v92 offset:6144\n"
  "v_mul_f32 v38, v38, v74\n"
  "v_mfma_f32_16x16x32_bf16 v[0:3], v[52:55], a[120:123], v[0:3]\n"
  "ds_read_b128 v[60:63], v92 offset:7168\n"
  "v_mul_f32 v39, v39, v75\n"
  "v_mfma_f32_16x16x32_bf16 v[4:7], v[48:51], a[100:103], v[4:7]\n"
  "v_cvt_pk_bf16_f32 v26, v26, v30\n"
  "v_cvt_pk_bf16_f32 v30, v27, v31\n"
  "v_mfma_f32_16x16x32_bf16 v[4:7], v[52:55], a[124:127], v[4:7]\n"
  "buffer_load_dwordx4 v102, s[76:79], s57 offen offset:1280  lds\n"
  "v_mfma_f32_16x16x32_bf16 v[8:11], v[48:51], a[104:107], v[8:11]\n"
  "s_add_u32 s57, s57, 6144\n"
  "v_cvt_pk_bf16_f32 v27, v34, v38\n"
  "v_cvt_pk_bf16_f32 v31, v35, v39\n"
  "v_mfma_f32_16x16x32_bf16 v[8:11], v[52:55], a[128:131], v[8:11]\n"
  "buffer_store_dwordx4 v[84:87], v97, s[48:51], s68 offen offset:128 nt\n"
  "ds_write2_b32 v95, v42, v43 offset0:144 offset1:216\n"
  "v_smfmac_f32_16x16x64_bf16 v[64:67], v[76:79], v[48:55], v110\n"
  "s_add_u32 m0, s75, 5888\n"
  "ds_write_b64 v94, v[24:25] offset:0\n"
  "v_mfma_f32_16x16x32_bf16 v[12:15], v[48:51], a[108:111], v[12:15]\n"
  "buffer_load_dwordx4 v101, s[76:79], s57 offen offset:256  lds\n"
  "v_mfma_f32_16x16x32_bf16 v[12:15], v[52:55], a[132:135], v[12:15]\n"
  "ds_write_b64 v94, v[28:29] offset:288\n"
  "v_mfma_f32_16x16x32_bf16 v[16:19], v[48:51], a[112:115], v[16:19]\n"
  "v_max3_f32 v109, v109, v72, v73\n"
  "v_max3_f32 v109, v109, v74, v75\n"
  "v_mfma_f32_16x16x32_bf16 v[16:19], v[52:55], a[136:139], v[16:19]\n"
  "ds_write_b64 v94, v[26:27] offset:576\n"
  "v_mfma_f32_16x16x32_bf16 v[20:23], v[48:51], a[116:119], v[20:23]\n"
  "buffer_load_dwordx4 v102, s[76:79], s57 offen offset:1280  lds\n"
  "v_mfma_f32_16x16x32_bf16 v[20:23], v[52:55], a[140:143], v[20:23]\n"
  "s_waitcnt lgkmcnt(4)\n"
  "v_xor_b32 v92, 0x4000, v92\n"
  "v_mfma_f32_16x16x32_bf16 v[20:23], v[56:59], a[164:167], v[20:23]\n"
  "s_add_u32 s66, s66, 6144\n"
  "ds_read_b128 v[48:51], v93 offset:0\n"
  "v_mfma_f32_16x16x32_bf16 v[20:23], v[60:63], a[188:191], v[20:23]\n"
  "s_add_u32 m0, s75, 8064\n"
  "ds_read_b128 v[52:55], v93 offset:1024\n"
  "v_mfma_f32_16x16x32_bf16 v[16:19], v[56:59], a[160:163], v[16:19]\n"
  "s_cmp_ge_u32 s66, 86016\n"
  "s_cselect_b32 s64, 36864, 0\n"
  "buffer_load_dwordx4 v101, s[76:79], s57 offen offset:128  lds\n"
  "v_mfma_f32_16x16x32_bf16 v[16:19], v[60:63], a[184:187], v[16:19]\n"
  "ds_write_b64 v94, v[30:31] offset:864\n"
  "v_mfma_f32_16x16x32_bf16 v[12:15], v[56:59], a[156:159], v[12:15]\n"
  "s_sub_u32 s66, s66, s64\n"
  "v_mfma_f32_16x16x32_bf16 v[12:15], v[60:63], a[180:183], v[12:15]\n"
  "s_sub_u32 s90, s90, 1\n"
  "s_cselect_b32 s63, s91, 6144\n"
  "s_cmov_b64 s[90:91], s[92:93]\n"
  "s_cmov_b64 s[92:93], s[94:95]\n"
  "buffer_store_dwordx4 v[88:91], v97, s[48:51], s68 offen offset:256 nt\n"
  "v_smfmac_f32_16x16x64_bf16 v[64:67], v[76:79], v[56:63], v110\n"
  "s_add_u32 s67, s67, s63\n"
  "buffer_load_dwordx4 v102, s[76:79], s57 offen offset:1152  lds\n"
  "v_mfma_f32_16x16x32_bf16 v[8:11], v[56:59], a[152:155], v[8:11]\n"
  "s_add_u32 s68, s68, s69\n"
  "v_mfma_f32_16x16x32_bf16 v[8:11], v[60:63], a[176:179], v[8:11]\n"
  "s_mov_b32 s69, s70\n"
  "v_mfma_f32_16x16x32_bf16 v[4:7], v[56:59], a[148:151], v[4:7]\n"
  "s_add_u32 m0, s75, 10240\n"
  "v_add_f32 v68, v64, v65\n"
  "v_mov_b32 v64, 0\n"
  "v_mfma_f32_16x16x32_bf16 v[4:7], v[60:63], a[172:175], v[4:7]\n"
  "s_movk_i32 s70, 6144\n"
  "buffer_load_dwordx4 v101, s[76:79], s57 offen offset:0  lds\n"
  "v_mfma_f32_16x16x32_bf16 v[0:3], v[56:59], a[144:147], v[0:3]\n"
  "v_mov_b32 v65, 0\n"
  "v_mfma_f32_16x16x32_bf16 v[0:3], v[60:63], a[168:171], v[0:3]\n"
  "s_waitcnt lgkmcnt(1)\n"
  "v_mfma_f32_16x16x32_bf16 v[0:3], v[48:51], a[192:195], v[0:3]\n"
  "ds_read_b128 v[56:59], v93 offset:2048\n"
  "v_mfma_f32_16x16x32_bf16 v[0:3], v[52:55], a[216:219], v[0:3]\n"
  "buffer_load_dwordx4 v102, s[76:79], s57 offen offset:1024  lds\n"
  "ds_read_b128 v[60:63], v93 offset:3072\n"
  "v_mfma_f32_16x16x32_bf16 v[4:7], v[48:51], a[196:199], v[4:7]\n"
  "s_add_u32 s57, s57, 6144\n"
  "v_mfma_f32_16x16x32_bf16 v[4:7], v[52:55], a[220:223], v[4:7]\n"
  "ds_write_b32 v98, v68\n"
  "v_mfma_f32_16x16x32_bf16 v[8:11], v[48:51], a[200:203], v[8:11]\n"
  "s_add_u32 m0, s75, 12288\n"
  "v_mfma_f32_16x16x32_bf16 v[8:11], v[52:55], a[224:227], v[8:11]\n"
  "buffer_load_dwordx4 v101, s[76:79], s57 offen offset:0  lds\n"
  "v_mfma_f32_16x16x32_bf16 v[12:15], v[48:51], a[204:207], v[12:15]\n"
  "v_mfma_f32_16x16x32_bf16 v[12:15], v[52:55], a[228:231], v[12:15]\n"
  "v_mfma_f32_16x16x32_bf16 v[16:19], v[48:51], a[208:211], v[16:19]\n"
  "v_mfma_f32_16x16x32_bf16 v[16:19], v[52:55], a[232:235], v[16:19]\n"
  "buffer_load_dwordx4 v102, s[76:79], s57 offen offset:1024  lds\n"
  "v_mfma_f32_16x16x32_bf16 v[20:23], v[48:51], a[212:215], v[20:23]\n"
  "v_mfma_f32_16x16x32_bf16 v[20:23], v[52:55], a[236:239], v[20:23]\n"
  "s_waitcnt lgkmcnt(1)\n"
  "v_mfma_f32_16x16x32_bf16 v[20:23], v[56:59], v[132:135], v[20:23]\n"
  "s_add_u32 m0, s75, 14208\n"
  "ds_read_b128 v[48:51], v93 offset:4096\n"
  "v_mfma_f32_16x16x32_bf16 v[20:23], v[60:63], v[156:159], v[20:23]\n"
  "buffer_load_dwordx4 v101, s[76:79], s57 offen offset:128  lds\n"
  "ds_read_b128 v[52:55], v93 offset:5120\n"
  "v_mfma_f32_16x16x32_bf16 v[16:19], v[56:59], v[128:131], v[16:19]\n"
  "v_mfma_f32_16x16x32_bf16 v[16:19], v[60:63], v[152:155], v[16:19]\n"
  "v_mfma_f32_16x16x32_bf16 v[12:15], v[56:59], a[252:255], v[12:15]\n"
  "v_mfma_f32_16x16x32_bf16 v[12:15], v[60:63], v[148:151], v[12:15]\n"
  "buffer_load_dwordx4 v102, s[76:79], s57 offen offset:1152  lds\n"
  "v_mfma_f32_16x16x32_bf16 v[8:11], v[56:59], a[248:251], v[8:11]\n"
  "v_mfma_f32_16x16x32_bf16 v[8:11], v[60:63], v[144:147], v[8:11]\n"
  "v_mfma_f32_16x16x32_bf16 v[4:7], v[56:59], a[244:247], v[4:7]\n"
  "s_add_u32 m0, s75, 16128\n"
  "v_mfma_f32_16x16x32_bf16 v[4:7], v[60:63], v[140:143], v[4:7]\n"
  "buffer_load_dwordx4 v101, s[76:79], s57 offen offset:256  lds\n"
  "v_mfma_f32_16x16x32_bf16 v[0:3], v[56:59], a[240:243], v[0:3]\n"
  "v_mfma_f32_16x16x32_bf16 v[0:3], v[60:63], v[136:139], v[0:3]\n"
  "s_waitcnt lgkmcnt(0)\n"
  "v_mfma_f32_16x16x32_bf16 v[0:3], v[48:51], v[160:163], v[0:3]\n"
  "ds_read_b128 v[56:59], v93 offset:6144\n"
  "v_mfma_f32_16x16x32_bf16 v[0:3], v[52:55], v[184:187], v[0:3]\n"
  "buffer_load_dwordx4 v102, s[76:79], s57 offen offset:1280  lds\n"
  "ds_read_b128 v[60:63], v93 offset:7168\n"
  "v_mfma_f32_16x16x32_bf16 v[4:7], v[48:51], v[164:167], v[4:7]\n"
  "s_sub_u32 s57, s57, 12288\n"
  "v_xor_b32 v93, 0x4000, v93\n"
  "v_mfma_f32_16x16x32_bf16 v[4:7], v[52:55], v[188:191], v[4:7]\n"
  "v_mfma_f32_16x16x32_bf16 v[8:11], v[48:51], v[168:171], v[8:11]\n"
  "v_mfma_f32_16x16x32_bf16 v[8:11], v[52:55], v[192:195], v[8:11]\n"
  "v_mfma_f32_16x16x32_bf16 v[12:15], v[48:51], v[172:175], v[12:15]\n"
  "v_xor_b32 v94, 0x2000, v94\n"
  "v_xor_b32 v95, 0x2000, v95\n"
  "v_mfma_f32_16x16x32_bf16 v[12:15], v[52:55], v[196:199], v[12:15]\n"
  "v_xor_b32 v98, 0x2000, v98\n"
  "v_mfma_f32_16x16x32_bf16 v[16:19], v[48:51], v[176:179], v[16:19]\n"
  "v_mfma_f32_16x16x32_bf16 v[16:19], v[52:55], v[200:203], v[16:19]\n"
  "v_mfma_f32_16x16x32_bf16 v[20:23], v[48:51], v[180:183], v[20:23]\n"
  "v_mfma_f32_16x16x32_bf16 v[20:23], v[52:55], v[204:207], v[20:23]\n"
  "s_waitcnt vmcnt(37) lgkmcnt(0)\n"
  "s_barrier\n"
  "v_mfma_f32_16x16x32_bf16 v[20:23], v[56:59], v[228:231], v[20:23]\n"
  "ds_read_b128 v[48:51], v92 offset:0\n"
  "ds_read_b128 v[72:75], v99\n"
  "v_mfma_f32_16x16x32_bf16 v[44:47], v[60:63], v[252:255], v[20:23]\n"
  "ds_read_b128 v[52:55], v92 offset:1024\n"
  "ds_read_b128 v[112:115], v99 offset:64\n"
  "v_mfma_f32_16x16x32_bf16 v[16:19], v[56:59], v[224:227], v[16:19]\n"
  "s_add_u32 m0, s66, 0\n"
  "ds_read_b64_tr_b16 v[80:81], v96 offset:0\n"
  "ds_read_b64_tr_b16 v[82:83], v96 offset:32\n"
  "v_mfma_f32_16x16x32_bf16 v[40:43], v[60:63], v[248:251], v[16:19]\n"
  "ds_read_b64_tr_b16 v[84:85], v96 offset:2304\n"
  "ds_read_b64_tr_b16 v[86:87], v96 offset:2336\n"
  "v_mfma_f32_16x16x32_bf16 v[12:15], v[56:59], v[220:223], v[12:15]\n"
  "ds_read_b64_tr_b16 v[88:89], v96 offset:4608\n"
  "ds_read_b64_tr_b16 v[90:91], v96 offset:4640\n"
  "v_mfma_f32_16x16x32_bf16 v[36:39], v[60:63], v[244:247], v[12:15]\n"
  "buffer_load_dwordx4 v100, s[44:47], s67 offen offset:0 nt lds\n"
  "v_mfma_f32_16x16x32_bf16 v[8:11], v[56:59], v[216:219], v[8:11]\n"
  "s_add_u32 m0, s66, 1920\n"
  "v_xor_b32 v96, 0x2000, v96\n"
  "v_xor_b32 v99, 0x2000, v99\n"
  "v_mfma_f32_16x16x32_bf16 v[32:35], v[60:63], v[240:243], v[8:11]\n"
  "s_waitcnt lgkmcnt(6)\n"
  "v_add_f32 v72, v72, v112\n"
  "v_add_f32 v73, v73, v113\n"
  "v_mfma_f32_16x16x32_bf16 v[4:7], v[56:59], v[212:215], v[4:7]\n"
  "v_add_f32 v74, v74, v114\n"
  "v_add_f32 v75, v75, v115\n"
  "v_mfma_f32_16x16x32_bf16 v[28:31], v[60:63], v[236:239], v[4:7]\n"
  "buffer_load_dwordx4 v100, s[44:47], s67 offen offset:128 nt lds\n"
  "v_mfma_f32_16x16x32_bf16 v[0:3], v[56:59], v[208:211], v[0:3]\n"
  "s_add_u32 m0, s66, 3840\n"
  "v_max3_f32 v109, v109, v72, v73\n"
  "v_max3_f32 v109, v109, v74, v75\n"
  "v_mfma_f32_16x16x32_bf16 v[24:27], v[60:63], v[232:235], v[0:3]\n"
  "v_rcp_f32 v72, v72\n"
  "v_mfma_f32_16x16x32_bf16 v[0:3], v[48:51], a[0:3], 0\n"
  "ds_read_b128 v[56:59], v92 offset:2048\n"
  "v_mul_f32 v40, v40, v72\n"
  "v_mfma_f32_16x16x32_bf16 v[0:3], v[52:55], a[24:27], v[0:3]\n"
  "buffer_load_dwordx4 v100, s[44:47], s67 offen offset:256 nt lds\n"
  "ds_read_b128 v[60:63], v92 offset:3072\n"
  "v_mfma_f32_16x16x32_bf16 v[4:7], v[48:51], a[4:7], 0\n"
  "v_rcp_f32 v73, v73\n"
  "v_mfma_f32_16x16x32_bf16 v[4:7], v[52:55], a[28:31], v[4:7]\n"
  "v_rcp_f32 v74, v74\n"
  "v_mfma_f32_16x16x32_bf16 v[8:11], v[48:51], a[8:11], 0\n"
  "v_rcp_f32 v75, v75\n"
  "v_mfma_f32_16x16x32_bf16 v[8:11], v[52:55], a[32:35], v[8:11]\n"
  "v_mul_f32 v44, v44, v72\n"
  "v_cvt_pk_bf16_f32 v40, v40, v44\n"
  "v_smfmac_f32_16x16x64_bf16 v[64:67], v[76:79], v[48:55], v110\n"
  "v_mul_f32 v41, v41, v73\n"
  "v_mul_f32 v45, v45, v73\n"
  "v_mfma_f32_16x16x32_bf16 v[12:15], v[48:51], a[12:15], 0\n"
  "v_cvt_pk_bf16_f32 v41, v41, v45\n"
  "v_mul_f32 v42, v42, v74\n"
  "v_mfma_f32_16x16x32_bf16 v[12:15], v[52:55], a[36:39], v[12:15]\n"
  "s_add_u32 m0, s59, 0\n"
  "v_mul_f32 v46, v46, v74\n"
  "v_cvt_pk_bf16_f32 v42, v42, v46\n"
  "v_mfma_f32_16x16x32_bf16 v[16:19], v[48:51], a[16:19], 0\n"
  "buffer_load_dwordx4 v117, s[40:43], s57 offen offset:0  lds\n"
  "v_mfma_f32_16x16x32_bf16 v[16:19], v[52:55], a[40:43], v[16:19]\n"
  "v_mul_f32 v43, v43, v75\n"
  "v_mul_f32 v47, v47, v75\n"
  "v_mfma_f32_16x16x32_bf16 v[20:23], v[48:51], a[20:23], 0\n"
  "v_cvt_pk_bf16_f32 v43, v43, v47\n"
  "v_mul_f32 v24, v24, v72\n"
  "v_mfma_f32_16x16x32_bf16 v[20:23], v[52:55], a[44:47], v[20:23]\n"
  "s_waitcnt lgkmcnt(0)\n"
  "v_mul_f32 v25, v25, v73\n"
  "v_mul_f32 v28, v28, v72\n"
  "v_mfma_f32_16x16x32_bf16 v[20:23], v[56:59], a[68:71], v[20:23]\n"
  "buffer_load_dwordx4 v118, s[40:43], s57 offen offset:1024  lds\n"
  "ds_read_b128 v[48:51], v92 offset:4096\n"
  "v_mfma_f32_16x16x32_bf16 v[20:23], v[60:63], a[92:95], v[20:23]\n"
  "ds_read_b128 v[52:55], v92 offset:5120\n"
  "v_mul_f32 v29, v29, v73\n"
  "v_mfma_f32_16x16x32_bf16 v[16:19], v[56:59], a[64:67], v[16:19]\n"
  "v_mul_f32 v32, v32, v72\n"
  "v_mul_f32 v33, v33, v73\n"
  "v_mfma_f32_16x16x32_bf16 v[16:19], v[60:63], a[88:91], v[16:19]\n"
  "buffer_store_dwordx4 v[80:83], v97, s[48:51], s68 offen offset:0 nt\n"
  "ds_write2_b32 v95, v40, v41 offset0:0 offset1:72\n"
  "v_mfma_f32_16x16x32_bf16 v[12:15], v[56:59], a[60:63], v[12:15]\n"
  "buffer_load_dwordx4 v119, s[40:43], s57 offen offset:2048  lds\n"
  "v_mfma_f32_16x16x32_bf16 v[12:15], v[60:63], a[84:87], v[12:15]\n"
  "s_add_u32 s57, s57, 6144\n"
  "v_mul_f32 v36, v36, v72\n"
  "v_mul_f32 v37, v37, v73\n"
  "v_smfmac_f32_16x16x64_bf16 v[64:67], v[76:79], v[56:63], v110\n"
  "v_cvt_pk_bf16_f32 v24, v24, v28\n"
  "v_cvt_pk_bf16_f32 v28, v25, v29\n"
  "v_mfma_f32_16x16x32_bf16 v[8:11], v[56:59], a[56:59], v[8:11]\n"
  "s_add_u32 m0, s59, 3072\n"
  "v_cvt_pk_bf16_f32 v25, v32, v36\n"
  "v_cvt_pk_bf16_f32 v29, v33, v37\n"
  "v_mfma_f32_16x16x32_bf16 v[8:11], v[60:63], a[80:83], v[8:11]\n"
  "buffer_load_dwordx4 v117, s[40:43], s57 offen offset:0  lds\n"
  "v_mfma_f32_16x16x32_bf16 v[4:7], v[56:59], a[52:55], v[4:7]\n"
  "v_mul_f32 v26, v26, v74\n"
  "v_mul_f32 v27, v27, v75\n"
  "v_mfma_f32_16x16x32_bf16 v[4:7], v[60:63], a[76:79], v[4:7]\n"
  "v_mul_f32 v30, v30, v74\n"
  "v_mul_f32 v31, v31, v75\n"
  "v_mfma_f32_16x16x32_bf16 v[0:3], v[56:59], a[48:51], v[0:3]\n"
  "v_mul_f32 v34, v34, v74\n"
  "v_mul_f32 v35, v35, v75\n"
  "v_mfma_f32_16x16x32_bf16 v[0:3], v[60:63], a[72:75], v[0:3]\n"
  "s_waitcnt lgkmcnt(1)\n"
  "buffer_load_dwordx4 v118, s[40:43], s57 offen offset:1024  lds\n"
  "v_mfma_f32_16x16x32_bf16 v[0:3], v[48:51], a[96:99], v[0:3]\n"
  "ds_read_b128 v[56:59], v92 offset:6144\n"
  "v_mul_f32 v38, v38, v74\n"
  "v_mfma_f32_16x16x32_bf16 v[0:3], v[52:55], a[120:123], v[0:3]\n"
  "ds_read_b128 v[60:63], v92 offset:7168\n"
  "v_mul_f32 v39, v39, v75\n"
  "v_mfma_f32_16x16x32_bf16 v[4:7], v[48:51], a[100:103], v[4:7]\n"
  "v_cvt_pk_bf16_f32 v26, v26, v30\n"
  "v_cvt_pk_bf16_f32 v30, v27, v31\n"
  "v_mfma_f32_16x16x32_bf16 v[4:7], v[52:55], a[124:127], v[4:7]\n"
  "buffer_load_dwordx4 v119, s[40:43], s57 offen offset:2048  lds\n"
  "v_mfma_f32_16x16x32_bf16 v[8:11], v[48:51], a[104:107], v[8:11]\n"
  "s_add_u32 s57, s57, 6144\n"
  "v_cvt_pk_bf16_f32 v27, v34, v38\n"
  "v_cvt_pk_bf16_f32 v31, v35, v39\n"
  "v_mfma_f32_16x16x32_bf16 v[8:11], v[52:55], a[128:131], v[8:11]\n"
  "buffer_store_dwordx4 v[84:87], v97, s[48:51], s68 offen offset:128 nt\n"
  "ds_write2_b32 v95, v42, v43 offset0:144 offset1:216\n"
  "v_smfmac_f32_16x16x64_bf16 v[64:67], v[76:79], v[48:55], v110\n"
  "s_add_u32 m0, s59, 6144\n"
  "ds_write_b64 v94, v[24:25] offset:0\n"
  "v_mfma_f32_16x16x32_bf16 v[12:15], v[48:51], a[108:111], v[12:15]\n"
  "buffer_load_dwordx4 v117, s[40:43], s57 offen offset:0  lds\n"
  "v_mfma_f32_16x16x32_bf16 v[12:15], v[52:55], a[132:135], v[12:15]\n"
  "ds_write_b64 v94, v[28:29] offset:288\n"
  "v_mfma_f32_16x16x32_bf16 v[16:19], v[48:51], a[112:115], v[16:19]\n"
  "v_max3_f32 v109, v109, v72, v73\n"
  "v_max3_f32 v109, v109, v74, v75\n"
  "v_mfma_f32_16x16x32_bf16 v[16:19], v[52:55], a[136:139], v[16:19]\n"
  "ds_write_b64 v94, v[26:27] offset:576\n"
  "v_mfma_f32_16x16x32_bf16 v[20:23], v[48:51], a[116:119], v[20:23]\n"
  "buffer_load_dwordx4 v118, s[40:43], s57 offen offset:1024  lds\n"
  "v_mfma_f32_16x16x32_bf16 v[20:23], v[52:55], a[140:143], v[20:23]\n"
  "s_waitcnt lgkmcnt(4)\n"
  "v_xor_b32 v92, 0x4000, v92\n"
  "v_mfma_f32_16x16x32_bf16 v[20:23], v[56:59], a[164:167], v[20:23]\n"
  "s_add_u32 s66, s66, 6144\n"
  "ds_read_b128 v[48:51], v93 offset:0\n"
  "v_mfma_f32_16x16x32_bf16 v[20:23], v[60:63], a[188:191], v[20:23]\n"
  "s_cmp_ge_u32 s66, 86016\n"
  "s_cselect_b32 s64, 36864, 0\n"
  "ds_read_b128 v[52:55], v93 offset:1024\n"
  "v_mfma_f32_16x16x32_bf16 v[16:19], v[56:59], a[160:163], v[16:19]\n"
  "s_sub_u32 s66, s66, s64\n"
  "buffer_load_dwordx4 v119, s[40:43], s57 offen offset:2048  lds\n"
  "v_mfma_f32_16x16x32_bf16 v[16:19], v[60:63], a[184:187], v[16:19]\n"
  "s_add_u32 s57, s57, 6144\n"
  "ds_write_b64 v94, v[30:31] offset:864\n"
  "v_mfma_f32_16x16x32_bf16 v[12:15], v[56:59], a[156:159], v[12:15]\n"
  "s_sub_u32 s90, s90, 1\n"
  "s_cselect_b32 s63, s91, 6144\n"
  "s_cmov_b64 s[90:91], s[92:93]\n"
  "s_cmov_b64 s[92:93], s[94:95]\n"
  "v_mfma_f32_16x16x32_bf16 v[12:15], v[60:63], a[180:183], v[12:15]\n"
  "s_add_u32 m0, s59, 9216\n"
  "buffer_store_dwordx4 v[88:91], v97, s[48:51], s68 offen offset:256 nt\n"
  "v_smfmac_f32_16x16x64_bf16 v[64:67], v[76:79], v[56:63], v110\n"
  "s_add_u32 s67, s67, s63\n"
  "buffer_load_dwordx4 v117, s[40:43], s57 offen offset:0  lds\n"
  "v_mfma_f32_16x16x32_bf16 v[8:11], v[56:59], a[152:155], v[8:11]\n"
  "s_add_u32 s68, s68, s69\n"
  "v_mfma_f32_16x16x32_bf16 v[8:11], v[60:63], a[176:179], v[8:11]\n"
  "s_mov_b32 s69, s70\n"
  "v_mfma_f32_16x16x32_bf16 v[4:7], v[56:59], a[148:151], v[4:7]\n"
  "s_movk_i32 s70, 6144\n"
  "v_add_f32 v68, v64, v65\n"
  "v_mov_b32 v64, 0\n"
  "v_mfma_f32_16x16x32_bf16 v[4:7], v[60:63], a[172:175], v[4:7]\n"
  "buffer_load_dwordx4 v118, s[40:43], s57 offen offset:1024  lds\n"
  "v_mfma_f32_16x16x32_bf16 v[0:3], v[56:59], a[144:147], v[0:3]\n"
  "v_mov_b32 v65, 0\n"
  "v_mfma_f32_16x16x32_bf16 v[0:3], v[60:63], a[168:171], v[0:3]\n"
  "s_waitcnt lgkmcnt(1)\n"
  "v_mfma_f32_16x16x32_bf16 v[0:3], v[48:51], a[192:195], v[0:3]\n"
  "ds_read_b128 v[56:59], v93 offset:2048\n"
  "v_mfma_f32_16x16x32_bf16 v[0:3], v[52:55], a[216:219], v[0:3]\n"
  "buffer_load_dwordx4 v119, s[40:43], s57 offen offset:2048  lds\n"
  "ds_read_b128 v[60:63], v93 offset:3072\n"
  "v_mfma_f32_16x16x32_bf16 v[4:7], v[48:51], a[196:199], v[4:7]\n"
  "s_add_u32 s57, s57, 6144\n"
  "v_mfma_f32_16x16x32_bf16 v[4:7], v[52:55], a[220:223], v[4:7]\n"
  "ds_write_b32 v98, v68\n"
  "v_mfma_f32_16x16x32_bf16 v[8:11], v[48:51], a[200:203], v[8:11]\n"
  "s_add_u32 m0, s59, 12288\n"
  "v_mfma_f32_16x16x32_bf16 v[8:11], v[52:55], a[224:227], v[8:11]\n"
  "buffer_load_dwordx4 v117, s[40:43], s57 offen offset:0  lds\n"
  "v_mfma_f32_16x16x32_bf16 v[12:15], v[48:51], a[204:207], v[12:15]\n"
  "v_mfma_f32_16x16x32_bf16 v[12:15], v[52:55], a[228:231], v[12:15]\n"
  "v_mfma_f32_16x16x32_bf16 v[16:19], v[48:51], a[208:211], v[16:19]\n"
  "v_mfma_f32_16x16x32_bf16 v[16:19], v[52:55], a[232:235], v[16:19]\n"
  "buffer_load_dwordx4 v118, s[40:43], s57 offen offset:1024  lds\n"
  "v_mfma_f32_16x16x32_bf16 v[20:23], v[48:51], a[212:215], v[20:23]\n"
  "v_mfma_f32_16x16x32_bf16 v[20:23], v[52:55], a[236:239], v[20:23]\n"
  "s_waitcnt lgkmcnt(1)\n"
  "v_mfma_f32_16x16x32_bf16 v[20:23], v[56:59], v[132:135], v[20:23]\n"
  "ds_read_b128 v[48:51], v93 offset:4096\n"
  "v_mfma_f32_16x16x32_bf16 v[20:23], v[60:63], v[156:159], v[20:23]\n"
  "buffer_load_dwordx4 v119, s[40:43], s57 offen offset:2048  lds\n"
  "ds_read_b128 v[52:55], v93 offset:5120\n"
  "v_mfma_f32_16x16x32_bf16 v[16:19], v[56:59], v[128:131], v[16:19]\n"
  "s_add_u32 s57, s57, 6144\n"
  "v_mfma_f32_16x16x32_bf16 v[16:19], v[60:63], v[152:155], v[16:19]\n"
  "v_mfma_f32_16x16x32_bf16 v[12:15], v[56:59], a[252:255], v[12:15]\n"
  "s_add_u32 m0, s59, 15360\n"
  "v_mfma_f32_16x16x32_bf16 v[12:15], v[60:63], v[148:151], v[12:15]\n"
  "buffer_load_dwordx4 v117, s[40:43], s57 offen offset:0  lds\n"
  "v_mfma_f32_16x16x32_bf16 v[8:11], v[56:59], a[248:251], v[8:11]\n"
  "v_mfma_f32_16x16x32_bf16 v[8:11], v[60:63], v[144:147], v[8:11]\n"
  "v_mfma_f32_16x16x32_bf16 v[4:7], v[56:59], a[244:247], v[4:7]\n"
  "v_mfma_f32_16x16x32_bf16 v[4:7], v[60:63], v[140:143], v[4:7]\n"
  "buffer_load_dwordx4 v118, s[40:43], s57 offen offset:1024  lds\n"
  "v_mfma_f32_16x16x32_bf16 v[0:3], v[56:59], a[240:243], v[0:3]\n"
  "v_mfma_f32_16x16x32_bf16 v[0:3], v[60:63], v[136:139], v[0:3]\n"
  "s_waitcnt lgkmcnt(0)\n"
  "v_mfma_f32_16x16x32_bf16 v[0:3], v[48:51], v[160:163], v[0:3]\n"
  "ds_read_b128 v[56:59], v93 offset:6144\n"
  "v_mfma_f32_16x16x32_bf16 v[0:3], v[52:55], v[184:187], v[0:3]\n"
  "buffer_load_dwordx4 v119, s[40:43], s57 offen offset:2048  lds\n"
  "ds_read_b128 v[60:63], v93 offset:7168\n"
  "v_mfma_f32_16x16x32_bf16 v[4:7], v[48:51], v[164:167], v[4:7]\n"
  "s_add_u32 s57, s57, 6144\n"
  "v_xor_b32 v93, 0x4000, v93\n"
  "v_mfma_f32_16x16x32_bf16 v[4:7], v[52:55], v[188:191], v[4:7]\n"
  "v_mfma_f32_16x16x32_bf16 v[8:11], v[48:51], v[168:171], v[8:11]\n"
  "v_mfma_f32_16x16x32_bf16 v[8:11], v[52:55], v[192:195], v[8:11]\n"
  "v_mfma_f32_16x16x32_bf16 v[12:15], v[48:51], v[172:175], v[12:15]\n"
  "v_xor_b32 v94, 0x2000, v94\n"
  "v_xor_b32 v95, 0x2000, v95\n"
  "v_mfma_f32_16x16x32_bf16 v[12:15], v[52:55], v[196:199], v[12:15]\n"
  "v_xor_b32 v98, 0x2000, v98\n"
  "v_mfma_f32_16x16x32_bf16 v[16:19], v[48:51], v[176:179], v[16:19]\n"
  "v_mfma_f32_16x16x32_bf16 v[16:19], v[52:55], v[200:203], v[16:19]\n"
  "v_mfma_f32_16x16x32_bf16 v[20:23], v[48:51], v[180:183], v[20:23]\n"
  "v_mfma_f32_16x16x32_bf16 v[20:23], v[52:55], v[204:207], v[20:23]\n"
  "s_waitcnt vmcnt(24) lgkmcnt(0)\n"
  "s_barrier\n"
  "v_mfma_f32_16x16x32_bf16 v[20:23], v[56:59], v[228:231], v[20:23]\n"
  "ds_read_b128 v[48:51], v92 offset:0\n"
  "ds_read_b128 v[72:75], v99\n"
  "v_mfma_f32_16x16x32_bf16 v[44:47], v[60:63], v[252:255], v[20:23]\n"
  "ds_read_b128 v[52:55], v92 offset:1024\n"
  "ds_read_b128 v[112:115], v99 offset:64\n"
  "v_mfma_f32_16x16x32_bf16 v[16:19], v[56:59], v[224:227], v[16:19]\n"
  "s_add_u32 m0, s66, 0\n"
  "ds_read_b64_tr_b16 v[80:81], v96 offset:0\n"
  "ds_read_b64_tr_b16 v[82:83], v96 offset:32\n"
  "v_mfma_f32_16x16x32_bf16 v[40:43], v[60:63], v[248:251], v[16:19]\n"
  "ds_read_b64_tr_b16 v[84:85], v96 offset:2304\n"
  "ds_read_b64_tr_b16 v[86:87], v96 offset:2336\n"
  "v_mfma_f32_16x16x32_bf16 v[12:15], v[56:59], v[220:223], v[12:15]\n"
  "ds_read_b64_tr_b16 v[88:89], v96 offset:4608\n"
  "ds_read_b64_tr_b16 v[90:91], v96 offset:4640\n"
  "v_mfma_f32_16x16x32_bf16 v[36:39], v[60:63], v[244:247], v[12:15]\n"
  "buffer_load_dwordx4 v100, s[44:47], s67 offen offset:0 nt lds\n"
  "v_mfma_f32_16x16x32_bf16 v[8:11], v[56:59], v[216:219], v[8:11]\n"
  "s_add_u32 m0, s66, 1920\n"
  "v_xor_b32 v96, 0x2000, v96\n"
  "v_xor_b32 v99, 0x2000, v99\n"
  "v_mfma_f32_16x16x32_bf16 v[32:35], v[60:63], v[240:243], v[8:11]\n"
  "s_waitcnt lgkmcnt(6)\n"
  "v_add_f32 v72, v72, v112\n"
  "v_add_f32 v73, v73, v113\n"
  "v_mfma_f32_16x16x32_bf16 v[4:7], v[56:59], v[212:215], v[4:7]\n"
  "v_add_f32 v74, v74, v114\n"
  "v_add_f32 v75, v75, v115\n"
  "v_mfma_f32_16x16x32_bf16 v[28:31], v[60:63], v[236:239], v[4:7]\n"
  "buffer_load_dwordx4 v100, s[44:47], s67 offen offset:128 nt lds\n"
  "v_mfma_f32_16x16x32_bf16 v[0:3], v[56:59], v[208:211], v[0:3]\n"
  "s_add_u32 m0, s66, 3840\n"
  "v_max3_f32 v109, v109, v72, v73\n"
  "v_max3_f32 v109, v109, v74, v75\n"
  "v_mfma_f32_16x16x32_bf16 v[24:27], v[60:63], v[232:235], v[0:3]\n"
  "v_rcp_f32 v72, v72\n"
  "v_mfma_f32_16x16x32_bf16 v[0:3], v[48:51], a[0:3], 0\n"
  "ds_read_b128 v[56:59], v92 offset:2048\n"
  "v_mul_f32 v40, v40, v72\n"
  "v_mfma_f32_16x16x32_bf16 v[0:3], v[52:55], a[24:27], v[0:3]\n"
  "buffer_load_dwordx4 v100, s[44:47], s67 offen offset:256 nt lds\n"
  "ds_read_b128 v[60:63], v92 offset:3072\n"
  "v_mfma_f32_16x16x32_bf16 v[4:7], v[48:51], a[4:7], 0\n"
  "v_rcp_f32 v73, v73\n"
  "v_mfma_f32_16x16x32_bf16 v[4:7], v[52:55], a[28:31], v[4:7]\n"
  "v_rcp_f32 v74, v74\n"
  "v_mfma_f32_16x16x32_bf16 v[8:11], v[48:51], a[8:11], 0\n"
  "v_rcp_f32 v75, v75\n"
  "v_mfma_f32_16x16x32_bf16 v[8:11], v[52:55], a[32:35], v[8:11]\n"
  "v_mul_f32 v44, v44, v72\n"
  "v_cvt_pk_bf16_f32 v40, v40, v44\n"
  "v_smfmac_f32_16x16x64_bf16 v[64:67], v[76:79], v[48:55], v110\n"
  "v_mul_f32 v41, v41, v73\n"
  "v_mul_f32 v45, v45, v73\n"
  "v_mfma_f32_16x16x32_bf16 v[12:15], v[48:51], a[12:15], 0\n"
  "v_cvt_pk_bf16_f32 v41, v41, v45\n"
  "v_mul_f32 v42, v42, v74\n"
  "v_mfma_f32_16x16x32_bf16 v[12:15], v[52:55], a[36:39], v[12:15]\n"
  "v_mul_f32 v46, v46, v74\n"
  "v_cvt_pk_bf16_f32 v42, v42, v46\n"
  "v_mfma_f32_16x16x32_bf16 v[16:19], v[48:51], a[16:19], 0\n"
  "v_mul_f32 v43, v43, v75\n"
  "v_mul_f32 v47, v47, v75\n"
  "v_mfma_f32_16x16x32_bf16 v[16:19], v[52:55], a[40:43], v[16:19]\n"
  "v_cvt_pk_bf16_f32 v43, v43, v47\n"
  "v_mul_f32 v24, v24, v72\n"
  "v_mfma_f32_16x16x32_bf16 v[20:23], v[48:51], a[20:23], 0\n"
  "v_mul_f32 v25, v25, v73\n"
  "v_mul_f32 v28, v28, v72\n"
  "v_mfma_f32_16x16x32_bf16 v[20:23], v[52:55], a[44:47], v[20:23]\n"
  "s_waitcnt lgkmcnt(0)\n"
  "v_mul_f32 v29, v29, v73\n"
  "v_mul_f32 v32, v32, v72\n"
  "v_mfma_f32_16x16x32_bf16 v[20:23], v[56:59], a[68:71], v[20:23]\n"
  "ds_read_b128 v[48:51], v92 offset:4096\n"
  "v_mul_f32 v33, v33, v73\n"
  "v_mfma_f32_16x16x32_bf16 v[20:23], v[60:63], a[92:95], v[20:23]\n"
  "buffer_store_dwordx4 v[80:83], v97, s[48:51], s68 offen offset:0 nt\n"
  "ds_read_b128 v[52:55], v92 offset:5120\n"
  "v_mul_f32 v36, v36, v72\n"
  "v_mul_f32 v37, v37, v73\n"
  "v_mfma_f32_16x16x32_bf16 v[16:19], v[56:59], a[64:67], v[16:19]\n"
  "v_cvt_pk_bf16_f32 v24, v24, v28\n"
  "v_cvt_pk_bf16_f32 v28, v25, v29\n"
  "v_mfma_f32_16x16x32_bf16 v[16:19], v[60:63], a[88:91], v[16:19]\n"
  "v_cvt_pk_bf16_f32 v25, v32, v36\n"
  "v_cvt_pk_bf16_f32 v29, v33, v37\n"
  "v_mfma_f32_16x16x32_bf16 v[12:15], v[56:59], a[60:63], v[12:15]\n"
  "v_mul_f32 v26, v26, v74\n"
  "v_mul_f32 v27, v27, v75\n"
  "v_mfma_f32_16x16x32_bf16 v[12:15], v[60:63], a[84:87], v[12:15]\n"
  "v_mul_f32 v30, v30, v74\n"
  "v_mul_f32 v31, v31, v75\n"
  "v_smfmac_f32_16x16x64_bf16 v[64:67], v[76:79], v[56:63], v110\n"
  "v_mul_f32 v34, v34, v74\n"
  "v_mul_f32 v35, v35, v75\n"
  "v_mfma_f32_16x16x32_bf16 v[8:11], v[56:59], a[56:59], v[8:11]\n"
  "v_mul_f32 v38, v38, v74\n"
  "v_mul_f32 v39, v39, v75\n"
  "v_mfma_f32_16x16x32_bf16 v[8:11], v[60:63], a[80:83], v[8:11]\n"
  "v_cvt_pk_bf16_f32 v26, v26, v30\n"
  "v_cvt_pk_bf16_f32 v30, v27, v31\n"
  "v_mfma_f32_16x16x32_bf16 v[4:7], v[56:59], a[52:55], v[4:7]\n"
  "v_cvt_pk_bf16_f32 v27, v34, v38\n"
  "v_cvt_pk_bf16_f32 v31, v35, v39\n"
  "v_mfma_f32_16x16x32_bf16 v[4:7], v[60:63], a[76:79], v[4:7]\n"
  "ds_write2_b32 v95, v40, v41 offset0:0 offset1:72\n"
  "v_mfma_f32_16x16x32_bf16 v[0:3], v[56:59], a[48:51], v[0:3]\n"
  "v_max3_f32 v109, v109, v72, v73\n"
  "v_max3_f32 v109, v109, v74, v75\n"
  "v_mfma_f32_16x16x32_bf16 v[0:3], v[60:63], a[72:75], v[0:3]\n"
  "s_waitcnt lgkmcnt(1)\n"
  "v_mfma_f32_16x16x32_bf16 v[0:3], v[48:51], a[96:99], v[0:3]\n"
  "ds_read_b128 v[56:59], v92 offset:6144\n"
  "v_mfma_f32_16x16x32_bf16 v[0:3], v[52:55], a[120:123], v[0:3]\n"
  "buffer_store_dwordx4 v[84:87], v97, s[48:51], s68 offen offset:128 nt\n"
  "ds_read_b128 v[60:63], v92 offset:7168\n"
  "ds_write2_b32 v95, v42, v43 offset0:144 offset1:216\n"
  "v_mfma_f32_16x16x32_bf16 v[4:7], v[48:51], a[100:103], v[4:7]\n"
  "v_xor_b32 v92, 0x4000, v92\n"
  "v_mfma_f32_16x16x32_bf16 v[4:7], v[52:55], a[124:127], v[4:7]\n"
  "ds_write_b64 v94, v[24:25] offset:0\n"
  "v_mfma_f32_16x16x32_bf16 v[8:11], v[48:51], a[104:107], v[8:11]\n"
  "v_mfma_f32_16x16x32_bf16 v[8:11], v[52:55], a[128:131], v[8:11]\n"
  "ds_write_b64 v94, v[28:29] offset:288\n"
  "v_smfmac_f32_16x16x64_bf16 v[64:67], v[76:79], v[48:55], v110\n"
  "ds_write_b64 v94, v[26:27] offset:576\n"
  "v_mfma_f32_16x16x32_bf16 v[12:15], v[48:51], a[108:111], v[12:15]\n"
  "v_mfma_f32_16x16x32_bf16 v[12:15], v[52:55], a[132:135], v[12:15]\n"
  "ds_write_b64 v94, v[30:31] offset:864\n"
  "v_mfma_f32_16x16x32_bf16 v[16:19], v[48:51], a[112:115], v[16:19]\n"
  "v_mfma_f32_16x16x32_bf16 v[16:19], v[52:55], a[136:139], v[16:19]\n"
  "v_mfma_f32_16x16x32_bf16 v[20:23], v[48:51], a[116:119], v[20:23]\n"
  "v_mfma_f32_16x16x32_bf16 v[20:23], v[52:55], a[140:143], v[20:23]\n"
  "s_waitcnt lgkmcnt(5)\n"
  "v_mfma_f32_16x16x32_bf16 v[20:23], v[56:59], a[164:167], v[20:23]\n"
  "s_add_u32 s66, s66, 6144\n"
  "ds_read_b128 v[48:51], v93 offset:0\n"
  "v_mfma_f32_16x16x32_bf16 v[20:23], v[60:63], a[188:191], v[20:23]\n"
  "s_cmp_ge_u32 s66, 86016\n"
  "s_cselect_b32 s64, 36864, 0\n"
  "buffer_store_dwordx4 v[88:91], v97, s[48:51], s68 offen offset:256 nt\n"
  "ds_read_b128 v[52:55], v93 offset:1024\n"
  "v_mfma_f32_16x16x32_bf16 v[16:19], v[56:59], a[160:163], v[16:19]\n"
  "s_sub_u32 s66, s66, s64\n"
  "v_mfma_f32_16x16x32_bf16 v[16:19], v[60:63], a[184:187], v[16:19]\n"
  "s_sub_u32 s90, s90, 1\n"
  "s_cselect_b32 s63, s91, 6144\n"
  "s_cmov_b64 s[90:91], s[92:93]\n"
  "s_cmov_b64 s[92:93], s[94:95]\n"
  "v_mfma_f32_16x16x32_bf16 v[12:15], v[56:59], a[156:159], v[12:15]\n"
  "s_add_u32 s67, s67, s63\n"
  "v_mfma_f32_16x16x32_bf16 v[12:15], v[60:63], a[180:183], v[12:15]\n"
  "s_add_u32 s68, s68, s69\n"
  "v_smfmac_f32_16x16x64_bf16 v[64:67], v[76:79], v[56:63], v110\n"
  "s_mov_b32 s69, s70\n"
  "v_mfma_f32_16x16x32_bf16 v[8:11], v[56:59], a[152:155], v[8:11]\n"
  "s_movk_i32 s70, 6144\n"
  "v_mfma_f32_16x16x32_bf16 v[8:11], v[60:63], a[176:179], v[8:11]\n"
  "v_mfma_f32_16x16x32_bf16 v[4:7], v[56:59], a[148:151], v[4:7]\n"
  "v_add_f32 v68, v64, v65\n"
  "v_mov_b32 v64, 0\n"
  "v_mfma_f32_16x16x32_bf16 v[4:7], v[60:63], a[172:175], v[4:7]\n"
  "ds_write_b32 v98, v68\n"
  "v_mfma_f32_16x16x32_bf16 v[0:3], v[56:59], a[144:147], v[0:3]\n"
  "v_mov_b32 v65, 0\n"
  "v_mfma_f32_16x16x32_bf16 v[0:3], v[60:63], a[168:171], v[0:3]\n"
  "s_waitcnt lgkmcnt(1)\n"
  "v_mfma_f32_16x16x32_bf16 v[0:3], v[48:51], a[192:195], v[0:3]\n"
  "ds_read_b128 v[56:59], v93 offset:2048\n"
  "v_mfma_f32_16x16x32_bf16 v[0:3], v[52:55], a[216:219], v[0:3]\n"
  "ds_read_b128 v[60:63], v93 offset:3072\n"
  "v_mfma_f32_16x16x32_bf16 v[4:7], v[48:51], a[196:199], v[4:7]\n"
  "v_mfma_f32_16x16x32_bf16 v[4:7], v[52:55], a[220:223], v[4:7]\n"
  "v_mfma_f32_16x16x32_bf16 v[8:11], v[48:51], a[200:203], v[8:11]\n"
  "v_mfma_f32_16x16x32_bf16 v[8:11], v[52:55], a[224:227], v[8:11]\n"
  "v_mfma_f32_16x16x32_bf16 v[12:15], v[48:51], a[204:207], v[12:15]\n"
  "v_mfma_f32_16x16x32_bf16 v[12:15], v[52:55], a[228:231], v[12:15]\n"
  "v_mfma_f32_16x16x32_bf16 v[16:19], v[48:51], a[208:211], v[16:19]\n"
  "v_mfma_f32_16x16x32_bf16 v[16:19], v[52:55], a[232:235], v[16:19]\n"
  "v_mfma_f32_16x16x32_bf16 v[20:23], v[48:51], a[212:215], v[20:23]\n"
  "v_mfma_f32_16x16x32_bf16 v[20:23], v[52:55], a[236:239], v[20:23]\n"
  "s_waitcnt lgkmcnt(0)\n"
  "v_mfma_f32_16x16x32_bf16 v[20:23], v[56:59], v[132:135], v[20:23]\n"
  "ds_read_b128 v[48:51], v93 offset:4096\n"
  "v_mfma_f32_16x16x32_bf16 v[20:23], v[60:63], v[156:159], v[20:23]\n"
  "ds_read_b128 v[52:55], v93 offset:5120\n"
  "v_mfma_f32_16x16x32_bf16 v[16:19], v[56:59], v[128:131], v[16:19]\n"
  "v_mfma_f32_16x16x32_bf16 v[16:19], v[60:63], v[152:155], v[16:19]\n"
  "v_mfma_f32_16x16x32_bf16 v[12:15], v[56:59], a[252:255], v[12:15]\n"
  "v_mfma_f32_16x16x32_bf16 v[12:15], v[60:63], v[148:151], v[12:15]\n"
  "v_mfma_f32_16x16x32_bf16 v[8:11], v[56:59], a[248:251], v[8:11]\n"
  "v_mfma_f32_16x16x32_bf16 v[8:11], v[60:63], v[144:147], v[8:11]\n"
  "v_mfma_f32_16x16x32_bf16 v[4:7], v[56:59], a[244:247], v[4:7]\n"
  "v_mfma_f32_16x16x32_bf16 v[4:7], v[60:63], v[140:143], v[4:7]\n"
  "v_mfma_f32_16x16x32_bf16 v[0:3], v[56:59], a[240:243], v[0:3]\n"
  "v_mfma_f32_16x16x32_bf16 v[0:3], v[60:63], v[136:139], v[0:3]\n"
  "s_waitcnt lgkmcnt(0)\n"
  "v_mfma_f32_16x16x32_bf16 v[0:3], v[48:51], v[160:163], v[0:3]\n"
  "ds_read_b128 v[56:59], v93 offset:6144\n"
  "v_mfma_f32_16x16x32_bf16 v[0:3], v[52:55], v[184:187], v[0:3]\n"
  "ds_read_b128 v[60:63], v93 offset:7168\n"
  "v_xor_b32 v93, 0x4000, v93\n"
  "v_mfma_f32_16x16x32_bf16 v[4:7], v[48:51], v[164:167], v[4:7]\n"
  "v_mfma_f32_16x16x32_bf16 v[4:7], v[52:55], v[188:191], v[4:7]\n"
  "v_mfma_f32_16x16x32_bf16 v[8:11], v[48:51], v[168:171], v[8:11]\n"
  "v_mfma_f32_16x16x32_bf16 v[8:11], v[52:55], v[192:195], v[8:11]\n"
  "v_mfma_f32_16x16x32_bf16 v[12:15], v[48:51], v[172:175], v[12:15]\n"
  "v_xor_b32 v94, 0x2000, v94\n"
  "v_xor_b32 v95, 0x2000, v95\n"
  "v_mfma_f32_16x16x32_bf16 v[12:15], v[52:55], v[196:199], v[12:15]\n"
  "v_xor_b32 v98, 0x2000, v98\n"
  "v_mfma_f32_16x16x32_bf16 v[16:19], v[48:51], v[176:179], v[16:19]\n"
  "v_mfma_f32_16x16x32_bf16 v[16:19], v[52:55], v[200:203], v[16:19]\n"
  "v_mfma_f32_16x16x32_bf16 v[20:23], v[48:51], v[180:183], v[20:23]\n"
  "v_mfma_f32_16x16x32_bf16 v[20:23], v[52:55], v[204:207], v[20:23]\n"
  "s_waitcnt lgkmcnt(0)\n"
  "s_barrier\n"
  "v_mfma_f32_16x16x32_bf16 v[20:23], v[56:59], v[228:231], v[20:23]\n"
  "ds_read_b128 v[48:51], v92 offset:0\n"
  "ds_read_b128 v[72:75], v99\n"
  "v_mfma_f32_16x16x32_bf16 v[44:47], v[60:63], v[252:255], v[20:23]\n"
  "ds_read_b128 v[52:55], v92 offset:1024\n"
  "ds_read_b128 v[112:115], v99 offset:64\n"
  "v_mfma_f32_16x16x32_bf16 v[16:19], v[56:59], v[224:227], v[16:19]\n"
  "ds_read_b64_tr_b16 v[80:81], v96 offset:0\n"
  "ds_read_b64_tr_b16 v[82:83], v96 offset:32\n"
  "v_mfma_f32_16x16x32_bf16 v[40:43], v[60:63], v[248:251], v[16:19]\n"
  "ds_read_b64_tr_b16 v[84:85], v96 offset:2304\n"
  "ds_read_b64_tr_b16 v[86:87], v96 offset:2336\n"
  "v_mfma_f32_16x16x32_bf16 v[12:15], v[56:59], v[220:223], v[12:15]\n"
  "ds_read_b64_tr_b16 v[88:89], v96 offset:4608\n"
  "ds_read_b64_tr_b16 v[90:91], v96 offset:4640\n"
  "v_mfma_f32_16x16x32_bf16 v[36:39], v[60:63], v[244:247], v[12:15]\n"
  "v_xor_b32 v96, 0x2000, v96\n"
  "v_xor_b32 v99, 0x2000, v99\n"
  "v_mfma_f32_16x16x32_bf16 v[8:11], v[56:59], v[216:219], v[8:11]\n"
  "v_mfma_f32_16x16x32_bf16 v[32:35], v[60:63], v[240:243], v[8:11]\n"
  "s_waitcnt lgkmcnt(6)\n"
  "v_add_f32 v72, v72, v112\n"
  "v_add_f32 v73, v73, v113\n"
  "v_mfma_f32_16x16x32_bf16 v[4:7], v[56:59], v[212:215], v[4:7]\n"
  "v_add_f32 v74, v74, v114\n"
  "v_add_f32 v75, v75, v115\n"
  "v_mfma_f32_16x16x32_bf16 v[28:31], v[60:63], v[236:239], v[4:7]\n"
  "v_max3_f32 v109, v109, v72, v73\n"
  "v_max3_f32 v109, v109, v74, v75\n"
  "v_mfma_f32_16x16x32_bf16 v[0:3], v[56:59], v[208:211], v[0:3]\n"
  "v_rcp_f32 v72, v72\n"
  "v_mfma_f32_16x16x32_bf16 v[24:27], v[60:63], v[232:235], v[0:3]\n"
  "v_rcp_f32 v73, v73\n"
  "v_mfma_f32_16x16x32_bf16 v[0:3], v[48:51], a[0:3], 0\n"
  "ds_read_b128 v[56:59], v92 offset:2048\n"
  "v_mul_f32 v40, v40, v72\n"
  "v_mfma_f32_16x16x32_bf16 v[0:3], v[52:55], a[24:27], v[0:3]\n"
  "ds_read_b128 v[60:63], v92 offset:3072\n"
  "v_mul_f32 v44, v44, v72\n"
  "v_mfma_f32_16x16x32_bf16 v[4:7], v[48:51], a[4:7], 0\n"
  "v_rcp_f32 v74, v74\n"
  "v_mfma_f32_16x16x32_bf16 v[4:7], v[52:55], a[28:31], v[4:7]\n"
  "v_rcp_f32 v75, v75\n"
  "v_mfma_f32_16x16x32_bf16 v[8:11], v[48:51], a[8:11], 0\n"
  "v_cvt_pk_bf16_f32 v40, v40, v44\n"
  "v_mul_f32 v41, v41, v73\n"
  "v_mfma_f32_16x16x32_bf16 v[8:11], v[52:55], a[32:35], v[8:11]\n"
  "v_mul_f32 v45, v45, v73\n"
  "v_cvt_pk_bf16_f32 v41, v41, v45\n"
  "v_smfmac_f32_16x16x64_bf16 v[64:67], v[76:79], v[48:55], v110\n"
  "v_mul_f32 v42, v42, v74\n"
  "v_mul_f32 v46, v46, v74\n"
  "v_mfma_f32_16x16x32_bf16 v[12:15], v[48:51], a[12:15], 0\n"
  "v_cvt_pk_bf16_f32 v42, v42, v46\n"
  "v_mul_f32 v43, v43, v75\n"
  "v_mfma_f32_16x16x32_bf16 v[12:15], v[52:55], a[36:39], v[12:15]\n"
  "v_mul_f32 v47, v47, v75\n"
  "v_cvt_pk_bf16_f32 v43, v43, v47\n"
  "v_mfma_f32_16x16x32_bf16 v[16:19], v[48:51], a[16:19], 0\n"
  "v_mul_f32 v24, v24, v72\n"
  "v_mul_f32 v25, v25, v73\n"
  "v_mfma_f32_16x16x32_bf16 v[16:19], v[52:55], a[40:43], v[16:19]\n"
  "v_mul_f32 v28, v28, v72\n"
  "v_mul_f32 v29, v29, v73\n"
  "v_mfma_f32_16x16x32_bf16 v[20:23], v[48:51], a[20:23], 0\n"
  "v_mul_f32 v32, v32, v72\n"
  "v_mul_f32 v33, v33, v73\n"
  "v_mfma_f32_16x16x32_bf16 v[20:23], v[52:55], a[44:47], v[20:23]\n"
  "s_waitcnt lgkmcnt(0)\n"
  "v_mul_f32 v36, v36, v72\n"
  "v_mul_f32 v37, v37, v73\n"
  "v_mfma_f32_16x16x32_bf16 v[20:23], v[56:59], a[68:71], v[20:23]\n"
  "ds_read_b128 v[48:51], v92 offset:4096\n"
  "v_cvt_pk_bf16_f32 v24, v24, v28\n"
  "v_mfma_f32_16x16x32_bf16 v[20:23], v[60:63], a[92:95], v[20:23]\n"
  "buffer_store_dwordx4 v[80:83], v97, s[48:51], s68 offen offset:0 nt\n"
  "ds_read_b128 v[52:55], v92 offset:5120\n"
  "v_cvt_pk_bf16_f32 v28, v25, v29\n"
  "v_cvt_pk_bf16_f32 v25, v32, v36\n"
  "v_mfma_f32_16x16x32_bf16 v[16:19], v[56:59], a[64:67], v[16:19]\n"
  "v_cvt_pk_bf16_f32 v29, v33, v37\n"
  "v_mul_f32 v26, v26, v74\n"
  "v_mfma_f32_16x16x32_bf16 v[16:19], v[60:63], a[88:91], v[16:19]\n"
  "v_mul_f32 v27, v27, v75\n"
  "v_mul_f32 v30, v30, v74\n"
  "v_mfma_f32_16x16x32_bf16 v[12:15], v[56:59], a[60:63], v[12:15]\n"
  "v_mul_f32 v31, v31, v75\n"
  "v_mul_f32 v34, v34, v74\n"
  "v_mfma_f32_16x16x32_bf16 v[12:15], v[60:63], a[84:87], v[12:15]\n"
  "v_mul_f32 v35, v35, v75\n"
  "v_mul_f32 v38, v38, v74\n"
  "v_smfmac_f32_16x16x64_bf16 v[64:67], v[76:79], v[56:63], v110\n"
  "v_mul_f32 v39, v39, v75\n"
  "v_cvt_pk_bf16_f32 v26, v26, v30\n"
  "v_mfma_f32_16x16x32_bf16 v[8:11], v[56:59], a[56:59], v[8:11]\n"
  "v_cvt_pk_bf16_f32 v30, v27, v31\n"
  "v_cvt_pk_bf16_f32 v27, v34, v38\n"
  "v_mfma_f32_16x16x32_bf16 v[8:11], v[60:63], a[80:83], v[8:11]\n"
  "v_cvt_pk_bf16_f32 v31, v35, v39\n"
  "v_max3_f32 v109, v109, v72, v73\n"
  "v_mfma_f32_16x16x32_bf16 v[4:7], v[56:59], a[52:55], v[4:7]\n"
  "v_max3_f32 v109, v109, v74, v75\n"
  "v_mfma_f32_16x16x32_bf16 v[4:7], v[60:63], a[76:79], v[4:7]\n"
  "ds_write2_b32 v95, v40, v41 offset0:0 offset1:72\n"
  "v_mfma_f32_16x16x32_bf16 v[0:3], v[56:59], a[48:51], v[0:3]\n"
  "v_mfma_f32_16x16x32_bf16 v[0:3], v[60:63], a[72:75], v[0:3]\n"
  "s_waitcnt lgkmcnt(1)\n"
  "v_mfma_f32_16x16x32_bf16 v[0:3], v[48:51], a[96:99], v[0:3]\n"
  "ds_read_b128 v[56:59], v92 offset:6144\n"
  "v_mfma_f32_16x16x32_bf16 v[0:3], v[52:55], a[120:123], v[0:3]\n"
  "buffer_store_dwordx4 v[84:87], v97, s[48:51], s68 offen offset:128 nt\n"
  "ds_read_b128 v[60:63], v92 offset:7168\n"
  "ds_write2_b32 v95, v42, v43 offset0:144 offset1:216\n"
  "v_mfma_f32_16x16x32_bf16 v[4:7], v[48:51], a[100:103], v[4:7]\n"
  "v_xor_b32 v92, 0x4000, v92\n"
  "v_mfma_f32_16x16x32_bf16 v[4:7], v[52:55], a[124:127], v[4:7]\n"
  "ds_write_b64 v94, v[24:25] offset:0\n"
  "v_mfma_f32_16x16x32_bf16 v[8:11], v[48:51], a[104:107], v[8:11]\n"
  "v_mfma_f32_16x16x32_bf16 v[8:11], v[52:55], a[128:131], v[8:11]\n"
  "ds_write_b64 v94, v[28:29] offset:288\n"
  "v_smfmac_f32_16x16x64_bf16 v[64:67], v[76:79], v[48:55], v110\n"
  "ds_write_b64 v94, v[26:27] offset:576\n"
  "v_mfma_f32_16x16x32_bf16 v[12:15], v[48:51], a[108:111], v[12:15]\n"
  "v_mfma_f32_16x16x32_bf16 v[12:15], v[52:55], a[132:135], v[12:15]\n"
  "ds_write_b64 v94, v[30:31] offset:864\n"
  "v_mfma_f32_16x16x32_bf16 v[16:19], v[48:51], a[112:115], v[16:19]\n"
  "v_mfma_f32_16x16x32_bf16 v[16:19], v[52:55], a[136:139], v[16:19]\n"
  "v_mfma_f32_16x16x32_bf16 v[20:23], v[48:51], a[116:119], v[20:23]\n"
  "v_mfma_f32_16x16x32_bf16 v[20:23], v[52:55], a[140:143], v[20:23]\n"
  "s_waitcnt lgkmcnt(5)\n"
  "v_mfma_f32_16x16x32_bf16 v[20:23], v[56:59], a[164:167], v[20:23]\n"
  "ds_read_b128 v[48:51], v93 offset:0\n"
  "v_mfma_f32_16x16x32_bf16 v[20:23], v[60:63], a[188:191], v[20:23]\n"
  "buffer_store_dwordx4 v[88:91], v97, s[48:51], s68 offen offset:256 nt\n"
  "ds_read_b128 v[52:55], v93 offset:1024\n"
  "v_mfma_f32_16x16x32_bf16 v[16:19], v[56:59], a[160:163], v[16:19]\n"
  "s_add_u32 s68, s68, s69\n"
  "v_mfma_f32_16x16x32_bf16 v[16:19], v[60:63], a[184:187], v[16:19]\n"
  "s_mov_b32 s69, s70\n"
  "v_mfma_f32_16x16x32_bf16 v[12:15], v[56:59], a[156:159], v[12:15]\n"
  "s_movk_i32 s70, 6144\n"
  "v_mfma_f32_16x16x32_bf16 v[12:15], v[60:63], a[180:183], v[12:15]\n"
  "v_smfmac_f32_16x16x64_bf16 v[64:67], v[76:79], v[56:63], v110\n"
  "v_mfma_f32_16x16x32_bf16 v[8:11], v[56:59], a[152:155], v[8:11]\n"
  "v_mfma_f32_16x16x32_bf16 v[8:11], v[60:63], a[176:179], v[8:11]\n"
  "v_mfma_f32_16x16x32_bf16 v[4:7], v[56:59], a[148:151], v[4:7]\n"
  "v_add_f32 v68, v64, v65\n"
  "v_mov_b32 v64, 0\n"
  "v_mfma_f32_16x16x32_bf16 v[4:7], v[60:63], a[172:175], v[4:7]\n"
  "ds_write_b32 v98, v68\n"
  "v_mfma_f32_16x16x32_bf16 v[0:3], v[56:59], a[144:147], v[0:3]\n"
  "v_mov_b32 v65, 0\n"
  "v_mfma_f32_16x16x32_bf16 v[0:3], v[60:63], a[168:171], v[0:3]\n"
  "s_waitcnt lgkmcnt(1)\n"
  "v_mfma_f32_16x16x32_bf16 v[0:3], v[48:51], a[192:195], v[0:3]\n"
  "ds_read_b128 v[56:59], v93 offset:2048\n"
  "v_mfma_f32_16x16x32_bf16 v[0:3], v[52:55], a[216:219], v[0:3]\n"
  "ds_read_b128 v[60:63], v93 offset:3072\n"
  "v_mfma_f32_16x16x32_bf16 v[4:7], v[48:51], a[196:199], v[4:7]\n"
  "v_mfma_f32_16x16x32_bf16 v[4:7], v[52:55], a[220:223], v[4:7]\n"
  "v_mfma_f32_16x16x32_bf16 v[8:11], v[48:51], a[200:203], v[8:11]\n"
  "v_mfma_f32_16x16x32_bf16 v[8:11], v[52:55], a[224:227], v[8:11]\n"
  "v_mfma_f32_16x16x32_bf16 v[12:15], v[48:51], a[204:207], v[12:15]\n"
  "v_mfma_f32_16x16x32_bf16 v[12:15], v[52:55], a[228:231], v[12:15]\n"
  "v_mfma_f32_16x16x32_bf16 v[16:19], v[48:51], a[208:211], v[16:19]\n"
  "v_mfma_f32_16x16x32_bf16 v[16:19], v[52:55], a[232:235], v[16:19]\n"
  "v_mfma_f32_16x16x32_bf16 v[20:23], v[48:51], a[212:215], v[20:23]\n"
  "v_mfma_f32_16x16x32_bf16 v[20:23], v[52:55], a[236:239], v[20:23]\n"
  "s_waitcnt lgkmcnt(0)\n"
  "v_mfma_f32_16x16x32_bf16 v[20:23], v[56:59], v[132:135], v[20:23]\n"
  "ds_read_b128 v[48:51], v93 offset:4096\n"
  "v_mfma_f32_16x16x32_bf16 v[20:23], v[60:63], v[156:159], v[20:23]\n"
  "ds_read_b128 v[52:55], v93 offset:5120\n"
  "v_mfma_f32_16x16x32_bf16 v[16:19], v[56:59], v[128:131], v[16:19]\n"
  "v_mfma_f32_16x16x32_bf16 v[16:19], v[60:63], v[152:155], v[16:19]\n"
  "v_mfma_f32_16x16x32_bf16 v[12:15], v[56:59], a[252:255], v[12:15]\n"
  "v_mfma_f32_16x16x32_bf16 v[12:15], v[60:63], v[148:151], v[12:15]\n"
  "v_mfma_f32_16x16x32_bf16 v[8:11], v[56:59], a[248:251], v[8:11]\n"
  "v_mfma_f32_16x16x32_bf16 v[8:11], v[60:63], v[144:147], v[8:11]\n"
  "v_mfma_f32_16x16x32_bf16 v[4:7], v[56:59], a[244:247], v[4:7]\n"
  "v_mfma_f32_16x16x32_bf16 v[4:7], v[60:63], v[140:143], v[4:7]\n"
  "v_mfma_f32_16x16x32_bf16 v[0:3], v[56:59], a[240:243], v[0:3]\n"
  "v_mfma_f32_16x16x32_bf16 v[0:3], v[60:63], v[136:139], v[0:3]\n"
  "s_waitcnt lgkmcnt(0)\n"
  "v_mfma_f32_16x16x32_bf16 v[0:3], v[48:51], v[160:163], v[0:3]\n"
  "ds_read_b128 v[56:59], v93 offset:6144\n"
  "v_mfma_f32_16x16x32_bf16 v[0:3], v[52:55], v[184:187], v[0:3]\n"
  "ds_read_b128 v[60:63], v93 offset:7168\n"
  "v_xor_b32 v93, 0x4000, v93\n"
  "v_mfma_f32_16x16x32_bf16 v[4:7], v[48:51], v[164:167], v[4:7]\n"
  "v_mfma_f32_16x16x32_bf16 v[4:7], v[52:55], v[188:191], v[4:7]\n"
  "v_mfma_f32_16x16x32_bf16 v[8:11], v[48:51], v[168:171], v[8:11]\n"
  "v_mfma_f32_16x16x32_bf16 v[8:11], v[52:55], v[192:195], v[8:11]\n"
  "v_mfma_f32_16x16x32_bf16 v[12:15], v[48:51], v[172:175], v[12:15]\n"
  "v_xor_b32 v94, 0x2000, v94\n"
  "v_xor_b32 v95, 0x2000, v95\n"
  "v_mfma_f32_16x16x32_bf16 v[12:15], v[52:55], v[196:199], v[12:15]\n"
  "v_xor_b32 v98, 0x2000, v98\n"
  "v_mfma_f32_16x16x32_bf16 v[16:19], v[48:51], v[176:179], v[16:19]\n"
  "v_mfma_f32_16x16x32_bf16 v[16:19], v[52:55], v[200:203], v[16:19]\n"
  "v_mfma_f32_16x16x32_bf16 v[20:23], v[48:51], v[180:183], v[20:23]\n"
  "v_mfma_f32_16x16x32_bf16 v[20:23], v[52:55], v[204:207], v[20:23]\n"
  "s_waitcnt lgkmcnt(0)\n"
  "s_barrier\n"
  "v_mfma_f32_16x16x32_bf16 v[20:23], v[56:59], v[228:231], v[20:23]\n"
  "ds_read_b128 v[48:51], v92 offset:0\n"
  "ds_read_b128 v[72:75], v99\n"
  "ds_read_b128 v[112:115], v99 offset:64\n"
  "v_mfma_f32_16x16x32_bf16 v[44:47], v[60:63], v[252:255], v[20:23]\n"
  "ds_read_b128 v[52:55], v92 offset:1024\n"
  "v_xor_b32 v99, 0x2000, v99\n"
  "v_mfma_f32_16x16x32_bf16 v[16:19], v[56:59], v[224:227], v[16:19]\n"
  "ds_read_b64_tr_b16 v[80:81], v96 offset:0\n"
  "ds_read_b64_tr_b16 v[82:83], v96 offset:32\n"
  "ds_read_b64_tr_b16 v[84:85], v96 offset:2304\n"
  "v_mfma_f32_16x16x32_bf16 v[40:43], v[60:63], v[248:251], v[16:19]\n"
  "ds_read_b64_tr_b16 v[86:87], v96 offset:2336\n"
  "ds_read_b64_tr_b16 v[88:89], v96 offset:4608\n"
  "ds_read_b64_tr_b16 v[90:91], v96 offset:4640\n"
  "v_mfma_f32_16x16x32_bf16 v[12:15], v[56:59], v[220:223], v[12:15]\n"
  "v_xor_b32 v96, 0x2000, v96\n"
  "v_mfma_f32_16x16x32_bf16 v[36:39], v[60:63], v[244:247], v[12:15]\n"
  "v_mfma_f32_16x16x32_bf16 v[8:11], v[56:59], v[216:219], v[8:11]\n"
  "s_waitcnt lgkmcnt(7)\n"
  "v_add_f32 v72, v72, v112\n"
  "v_add_f32 v73, v73, v113\n"
  "v_add_f32 v74, v74, v114\n"
  "v_mfma_f32_16x16x32_bf16 v[32:35], v[60:63], v[240:243], v[8:11]\n"
  "v_add_f32 v75, v75, v115\n"
  "v_max3_f32 v109, v109, v72, v73\n"
  "v_max3_f32 v109, v109, v74, v75\n"
  "v_mfma_f32_16x16x32_bf16 v[4:7], v[56:59], v[212:215], v[4:7]\n"
  "v_rcp_f32 v72, v72\n"
  "v_mfma_f32_16x16x32_bf16 v[28:31], v[60:63], v[236:239], v[4:7]\n"
  "v_rcp_f32 v73, v73\n"
  "v_mul_f32 v40, v40, v72\n"
  "v_mfma_f32_16x16x32_bf16 v[0:3], v[56:59], v[208:211], v[0:3]\n"
  "v_rcp_f32 v74, v74\n"
  "v_mul_f32 v44, v44, v72\n"
  "v_mfma_f32_16x16x32_bf16 v[24:27], v[60:63], v[232:235], v[0:3]\n"
  "s_waitcnt lgkmcnt(6)\n"
  "v_rcp_f32 v75, v75\n"
  "v_cvt_pk_bf16_f32 v40, v40, v44\n"
  "v_mfma_f32_16x16x32_bf16 v[0:3], v[48:51], a[0:3], 0\n"
  "ds_read_b128 v[56:59], v92 offset:2048\n"
  "v_mul_f32 v41, v41, v73\n"
  "v_mul_f32 v45, v45, v73\n"
  "v_mfma_f32_16x16x32_bf16 v[0:3], v[52:55], a[24:27], v[0:3]\n"
  "ds_read_b128 v[60:63], v92 offset:3072\n"
  "v_cvt_pk_bf16_f32 v41, v41, v45\n"
  "v_mul_f32 v42, v42, v74\n"
  "v_mfma_f32_16x16x32_bf16 v[4:7], v[48:51], a[4:7], 0\n"
  "ds_write2_b32 v95, v40, v41 offset0:0 offset1:72\n"
  "v_mfma_f32_16x16x32_bf16 v[4:7], v[52:55], a[28:31], v[4:7]\n"
  "v_mul_f32 v46, v46, v74\n"
  "v_cvt_pk_bf16_f32 v42, v42, v46\n"
  "v_mul_f32 v43, v43, v75\n"
  "v_mfma_f32_16x16x32_bf16 v[8:11], v[48:51], a[8:11], 0\n"
  "v_mul_f32 v47, v47, v75\n"
  "v_cvt_pk_bf16_f32 v43, v43, v47\n"
  "v_mul_f32 v24, v24, v72\n"
  "v_mfma_f32_16x16x32_bf16 v[8:11], v[52:55], a[32:35], v[8:11]\n"
  "ds_write2_b32 v95, v42, v43 offset0:144 offset1:216\n"
  "v_smfmac_f32_16x16x64_bf16 v[64:67], v[76:79], v[48:55], v110\n"
  "v_mul_f32 v25, v25, v73\n"
  "v_mul_f32 v28, v28, v72\n"
  "v_mul_f32 v29, v29, v73\n"
  "v_mfma_f32_16x16x32_bf16 v[12:15], v[48:51], a[12:15], 0\n"
  "v_mul_f32 v32, v32, v72\n"
  "v_mul_f32 v33, v33, v73\n"
  "v_mul_f32 v36, v36, v72\n"
  "v_mfma_f32_16x16x32_bf16 v[12:15], v[52:55], a[36:39], v[12:15]\n"
  "v_mul_f32 v37, v37, v73\n"
  "v_cvt_pk_bf16_f32 v24, v24, v28\n"
  "v_cvt_pk_bf16_f32 v28, v25, v29\n"
  "v_mfma_f32_16x16x32_bf16 v[16:19], v[48:51], a[16:19], 0\n"
  "v_cvt_pk_bf16_f32 v25, v32, v36\n"
  "v_cvt_pk_bf16_f32 v29, v33, v37\n"
  "v_mul_f32 v26, v26, v74\n"
  "v_mfma_f32_16x16x32_bf16 v[16:19], v[52:55], a[40:43], v[16:19]\n"
  "ds_write_b64 v94, v[24:25] offset:0\n"
  "v_mfma_f32_16x16x32_bf16 v[20:23], v[48:51], a[20:23], 0\n"
  "ds_write_b64 v94, v[28:29] offset:288\n"
  "v_mfma_f32_16x16x32_bf16 v[20:23], v[52:55], a[44:47], v[20:23]\n"
  "s_waitcnt lgkmcnt(4)\n"
  "buffer_store_dwordx4 v[80:83], v97, s[48:51], s68 offen offset:0 nt\n"
  "v_mul_f32 v27, v27, v75\n"
  "v_mul_f32 v30, v30, v74\n"
  "v_mul_f32 v31, v31, v75\n"
  "v_mfma_f32_16x16x32_bf16 v[20:23], v[56:59], a[68:71], v[20:23]\n"
  "ds_read_b128 v[48:51], v92 offset:4096\n"
  "v_mul_f32 v34, v34, v74\n"
  "v_mul_f32 v35, v35, v75\n"
  "v_mfma_f32_16x16x32_bf16 v[20:23], v[60:63], a[92:95], v[20:23]\n"
  "ds_read_b128 v[52:55], v92 offset:5120\n"
  "v_mul_f32 v38, v38, v74\n"
  "v_mul_f32 v39, v39, v75\n"
  "v_mfma_f32_16x16x32_bf16 v[16:19], v[56:59], a[64:67], v[16:19]\n"
  "s_waitcnt vmcnt(27)\n"
  "ds_read_b64_tr_b16 a[0:1], v103 offset:0\n"
  "ds_read_b64_tr_b16 a[4:5], v104 offset:0\n"
  "v_cvt_pk_bf16_f32 v26, v26, v30\n"
  "v_mfma_f32_16x16x32_bf16 v[16:19], v[60:63], a[88:91], v[16:19]\n"
  "v_cvt_pk_bf16_f32 v30, v27, v31\n"
  "v_cvt_pk_bf16_f32 v27, v34, v38\n"
  "v_cvt_pk_bf16_f32 v31, v35, v39\n"
  "v_mfma_f32_16x16x32_bf16 v[12:15], v[56:59], a[60:63], v[12:15]\n"
  "ds_read_b64_tr_b16 a[8:9], v105 offset:0\n"
  "ds_read_b64_tr_b16 a[12:13], v106 offset:0\n"
  "v_max3_f32 v109, v109, v72, v73\n"
  "v_mfma_f32_16x16x32_bf16 v[12:15], v[60:63], a[84:87], v[12:15]\n"
  "ds_read_b64_tr_b16 a[16:17], v107 offset:2048\n"
  "ds_read_b64_tr_b16 a[20:21], v108 offset:2048\n"
  "v_max3_f32 v109, v109, v74, v75\n"
  "v_smfmac_f32_16x16x64_bf16 v[64:67], v[76:79], v[56:63], v110\n"
  "ds_write_b64 v94, v[26:27] offset:576\n"
  "v_mfma_f32_16x16x32_bf16 v[8:11], v[56:59], a[56:59], v[8:11]\n"
  "s_waitcnt vmcnt(24)\n"
  "ds_read_b64_tr_b16 a[2:3], v103 offset:3072\n"
  "ds_read_b64_tr_b16 a[6:7], v104 offset:3072\n"
  "v_mfma_f32_16x16x32_bf16 v[8:11], v[60:63], a[80:83], v[8:11]\n"
  "ds_write_b64 v94, v[30:31] offset:864\n"
  "v_mfma_f32_16x16x32_bf16 v[4:7], v[56:59], a[52:55], v[4:7]\n"
  "ds_read_b64_tr_b16 a[10:11], v105 offset:3072\n"
  "ds_read_b64_tr_b16 a[14:15], v106 offset:3072\n"
  "v_mfma_f32_16x16x32_bf16 v[4:7], v[60:63], a[76:79], v[4:7]\n"
  "s_add_u32 m0, s59, 0\n"
  "ds_read_b64_tr_b16 a[18:19], v107 offset:5120\n"
  "ds_read_b64_tr_b16 a[22:23], v108 offset:5120\n"
  "v_mfma_f32_16x16x32_bf16 v[0:3], v[56:59], a[48:51], v[0:3]\n"
  "s_waitcnt lgkmcnt(8)\n"
  "buffer_load_dwordx4 v117, s[40:43], s57 offen offset:0 nt lds\n"
  "buffer_load_dwordx4 v118, s[40:43], s57 offen offset:1024 nt lds\n"
  "v_mfma_f32_16x16x32_bf16 v[0:3], v[60:63], a[72:75], v[0:3]\n"
  "s_waitcnt vmcnt(22)\n"
  "ds_read_b64_tr_b16 a[24:25], v103 offset:6144\n"
  "ds_read_b64_tr_b16 a[28:29], v104 offset:6144\n"
  "v_mfma_f32_16x16x32_bf16 v[0:3], v[48:51], a[96:99], v[0:3]\n"
  "buffer_load_dwordx4 v119, s[40:43], s57 offen offset:2048  lds\n"
  "ds_read_b128 v[56:59], v92 offset:6144\n"
  "v_mfma_f32_16x16x32_bf16 v[0:3], v[52:55], a[120:123], v[0:3]\n"
  "s_add_u32 s57, s57, 6144\n"
  "buffer_store_dwordx4 v[84:87], v97, s[48:51], s68 offen offset:128 nt\n"
  "ds_read_b128 v[60:63], v92 offset:7168\n"
  "ds_read_b64_tr_b16 a[32:33], v105 offset:6144\n"
  "ds_read_b64_tr_b16 a[36:37], v106 offset:6144\n"
  "v_mfma_f32_16x16x32_bf16 v[4:7], v[48:51], a[100:103], v[4:7]\n"
  "s_add_u32 m0, s59, 3072\n"
  "ds_read_b64_tr_b16 a[40:41], v107 offset:8192\n"
  "ds_read_b64_tr_b16 a[44:45], v108 offset:8192\n"
  "v_xor_b32 v92, 0x4000, v92\n"
  "v_mfma_f32_16x16x32_bf16 v[4:7], v[52:55], a[124:127], v[4:7]\n"
  "s_waitcnt lgkmcnt(8)\n"
  "buffer_load_dwordx4 v117, s[40:43], s57 offen offset:0 nt lds\n"
  "buffer_load_dwordx4 v118, s[40:43], s57 offen offset:1024 nt lds\n"
  "v_mfma_f32_16x16x32_bf16 v[8:11], v[48:51], a[104:107], v[8:11]\n"
  "s_waitcnt vmcnt(22)\n"
  "ds_read_b64_tr_b16 a[26:27], v103 offset:9216\n"
  "ds_read_b64_tr_b16 a[30:31], v104 offset:9216\n"
  "v_mfma_f32_16x16x32_bf16 v[8:11], v[52:55], a[128:131], v[8:11]\n"
  "buffer_load_dwordx4 v119, s[40:43], s57 offen offset:2048  lds\n"
  "v_smfmac_f32_16x16x64_bf16 v[64:67], v[76:79], v[48:55], v110\n"
  "s_add_u32 s57, s57, 6144\n"
  "ds_read_b64_tr_b16 a[34:35], v105 offset:9216\n"
  "ds_read_b64_tr_b16 a[38:39], v106 offset:9216\n"
  "v_mfma_f32_16x16x32_bf16 v[12:15], v[48:51], a[108:111], v[12:15]\n"
  "s_add_u32 m0, s59, 6144\n"
  "ds_read_b64_tr_b16 a[42:43], v107 offset:11264\n"
  "ds_read_b64_tr_b16 a[46:47], v108 offset:11264\n"
  "v_mfma_f32_16x16x32_bf16 v[12:15], v[52:55], a[132:135], v[12:15]\n"
  "s_waitcnt lgkmcnt(6)\n"
  "buffer_load_dwordx4 v117, s[40:43], s57 offen offset:0 nt lds\n"
  "buffer_load_dwordx4 v118, s[40:43], s57 offen offset:1024 nt lds\n"
  "v_mfma_f32_16x16x32_bf16 v[16:19], v[48:51], a[112:115], v[16:19]\n"
  "s_waitcnt vmcnt(22)\n"
  "ds_read_b64_tr_b16 a[48:49], v103 offset:12288\n"
  "ds_read_b64_tr_b16 a[52:53], v104 offset:12288\n"
  "v_mfma_f32_16x16x32_bf16 v[16:19], v[52:55], a[136:139], v[16:19]\n"
  "buffer_load_dwordx4 v119, s[40:43], s57 offen offset:2048  lds\n"
  "v_mfma_f32_16x16x32_bf16 v[20:23], v[48:51], a[116:119], v[20:23]\n"
  "s_add_u32 s57, s57, 6144\n"
  "ds_read_b64_tr_b16 a[56:57], v105 offset:12288\n"
  "ds_read_b64_tr_b16 a[60:61], v106 offset:12288\n"
  "v_mfma_f32_16x16x32_bf16 v[20:23], v[52:55], a[140:143], v[20:23]\n"
  "s_add_u32 m0, s59, 9216\n"
  "ds_read_b64_tr_b16 a[64:65], v107 offset:14336\n"
  "ds_read_b64_tr_b16 a[68:69], v108 offset:14336\n"
  "v_mfma_f32_16x16x32_bf16 v[20:23], v[56:59], a[164:167], v[20:23]\n"
  "s_waitcnt lgkmcnt(6)\n"
  "buffer_load_dwordx4 v117, s[40:43], s57 offen offset:0 nt lds\n"
  "buffer_load_dwordx4 v118, s[40:43], s57 offen offset:1024 nt lds\n"
  "ds_read_b128 v[48:51], v93 offset:0\n"
  "v_mfma_f32_16x16x32_bf16 v[20:23], v[60:63], a[188:191], v[20:23]\n"
  "buffer_store_dwordx4 v[88:91], v97, s[48:51], s68 offen offset:256 nt\n"
  "ds_read_b128 v[52:55], v93 offset:1024\n"
  "s_waitcnt vmcnt(23)\n"
  "ds_read_b64_tr_b16 a[50:51], v103 offset:15360\n"
  "ds_read_b64_tr_b16 a[54:55], v104 offset:15360\n"
  "v_mfma_f32_16x16x32_bf16 v[16:19], v[56:59], a[160:163], v[16:19]\n"
  "s_add_u32 s68, s68, s69\n"
  "buffer_load_dwordx4 v119, s[40:43], s57 offen offset:2048  lds\n"
  "v_mfma_f32_16x16x32_bf16 v[16:19], v[60:63], a[184:187], v[16:19]\n"
  "s_add_u32 s57, s57, 6144\n"
  "ds_read_b64_tr_b16 a[58:59], v105 offset:15360\n"
  "ds_read_b64_tr_b16 a[62:63], v106 offset:15360\n"
  "v_mfma_f32_16x16x32_bf16 v[12:15], v[56:59], a[156:159], v[12:15]\n"
  "s_add_u32 m0, s59, 12288\n"
  "ds_read_b64_tr_b16 a[66:67], v107 offset:17408\n"
  "ds_read_b64_tr_b16 a[70:71], v108 offset:17408\n"
  "v_mfma_f32_16x16x32_bf16 v[12:15], v[60:63], a[180:183], v[12:15]\n"
  "s_waitcnt lgkmcnt(8)\n"
  "buffer_load_dwordx4 v117, s[40:43], s57 offen offset:0 nt lds\n"
  "buffer_load_dwordx4 v118, s[40:43], s57 offen offset:1024 nt lds\n"
  "v_smfmac_f32_16x16x64_bf16 v[64:67], v[76:79], v[56:63], v110\n"
  "s_waitcnt vmcnt(13)\n"
  "ds_read_b64_tr_b16 a[72:73], v103 offset:0\n"
  "ds_read_b64_tr_b16 a[76:77], v104 offset:0\n"
  "v_mfma_f32_16x16x32_bf16 v[8:11], v[56:59], a[152:155], v[8:11]\n"
  "s_mov_b32 s69, s70\n"
  "buffer_load_dwordx4 v119, s[40:43], s57 offen offset:2048  lds\n"
  "v_mfma_f32_16x16x32_bf16 v[8:11], v[60:63], a[176:179], v[8:11]\n"
  "s_add_u32 s57, s57, 6144\n"
  "ds_read_b64_tr_b16 a[80:81], v105 offset:0\n"
  "ds_read_b64_tr_b16 a[84:85], v106 offset:0\n"
  "v_mfma_f32_16x16x32_bf16 v[4:7], v[56:59], a[148:151], v[4:7]\n"
  "s_add_u32 m0, s59, 15360\n"
  "ds_read_b64_tr_b16 a[88:89], v107 offset:2048\n"
  "ds_read_b64_tr_b16 a[92:93], v108 offset:2048\n"
  "v_add_f32 v68, v64, v65\n"
  "v_mfma_f32_16x16x32_bf16 v[4:7], v[60:63], a[172:175], v[4:7]\n"
  "s_waitcnt lgkmcnt(6)\n"
  "buffer_load_dwordx4 v117, s[40:43], s57 offen offset:0 nt lds\n"
  "buffer_load_dwordx4 v118, s[40:43], s57 offen offset:1024 nt lds\n"
  "v_mfma_f32_16x16x32_bf16 v[0:3], v[56:59], a[144:147], v[0:3]\n"
  "s_waitcnt vmcnt(12)\n"
  "ds_read_b64_tr_b16 a[74:75], v103 offset:3072\n"
  "ds_read_b64_tr_b16 a[78:79], v104 offset:3072\n"
  "v_mov_b32 v64, 0\n"
  "v_mfma_f32_16x16x32_bf16 v[0:3], v[60:63], a[168:171], v[0:3]\n"
  "buffer_load_dwordx4 v119, s[40:43], s57 offen offset:2048  lds\n"
  "v_mov_b32 v65, 0\n"
  "v_mfma_f32_16x16x32_bf16 v[0:3], v[48:51], a[192:195], v[0:3]\n"
  "s_add_u32 s57, s57, 6144\n"
  "ds_read_b128 v[56:59], v93 offset:2048\n"
  "ds_read_b64_tr_b16 a[82:83], v105 offset:3072\n"
  "ds_read_b64_tr_b16 a[86:87], v106 offset:3072\n"
  "v_mfma_f32_16x16x32_bf16 v[0:3], v[52:55], a[216:219], v[0:3]\n"
  "s_add_u32 m0, s59, 0\n"
  "ds_read_b128 v[60:63], v93 offset:3072\n"
  "ds_read_b64_tr_b16 a[90:91], v107 offset:5120\n"
  "ds_read_b64_tr_b16 a[94:95], v108 offset:5120\n"
  "v_mfma_f32_16x16x32_bf16 v[4:7], v[48:51], a[196:199], v[4:7]\n"
  "s_waitcnt lgkmcnt(8)\n"
  "buffer_load_dwordx4 v117, s[40:43], s57 offen offset:0 nt lds\n"
  "buffer_load_dwordx4 v118, s[40:43], s57 offen offset:1024 nt lds\n"
  "v_mfma_f32_16x16x32_bf16 v[4:7], v[52:55], a[220:223], v[4:7]\n"
  "s_waitcnt vmcnt(12)\n"
  "ds_read_b64_tr_b16 a[96:97], v103 offset:6144\n"
  "ds_read_b64_tr_b16 a[100:101], v104 offset:6144\n"
  "v_mfma_f32_16x16x32_bf16 v[8:11], v[48:51], a[200:203], v[8:11]\n"
  "s_mov_b32 s70, s96\n"
  "buffer_load_dwordx4 v119, s[40:43], s57 offen offset:2048  lds\n"
  "v_mfma_f32_16x16x32_bf16 v[8:11], v[52:55], a[224:227], v[8:11]\n"
  "s_add_u32 s57, s57, 6144\n"
  "ds_read_b64_tr_b16 a[104:105], v105 offset:6144\n"
  "ds_read_b64_tr_b16 a[108:109], v106 offset:6144\n"
  "v_mfma_f32_16x16x32_bf16 v[12:15], v[48:51], a[204:207], v[12:15]\n"
  "s_add_u32 m0, s59, 3072\n"
  "ds_read_b64_tr_b16 a[112:113], v107 offset:8192\n"
  "ds_read_b64_tr_b16 a[116:117], v108 offset:8192\n"
  "v_mfma_f32_16x16x32_bf16 v[12:15], v[52:55], a[228:231], v[12:15]\n"
  "s_waitcnt lgkmcnt(6)\n"
  "buffer_load_dwordx4 v117, s[40:43], s57 offen offset:0 nt lds\n"
  "buffer_load_dwordx4 v118, s[40:43], s57 offen offset:1024 nt lds\n"
  "v_mfma_f32_16x16x32_bf16 v[16:19], v[48:51], a[208:211], v[16:19]\n"
  "s_waitcnt vmcnt(11)\n"
  "ds_read_b64_tr_b16 a[98:99], v103 offset:9216\n"
  "ds_read_b64_tr_b16 a[102:103], v104 offset:9216\n"
  "v_mfma_f32_16x16x32_bf16 v[16:19], v[52:55], a[232:235], v[16:19]\n"
  "s_mov_b32 s96, s97\n"
  "buffer_load_dwordx4 v119, s[40:43], s57 offen offset:2048  lds\n"
  "v_mfma_f32_16x16x32_bf16 v[20:23], v[48:51], a[212:215], v[20:23]\n"
  "s_add_u32 s57, s57, 6144\n"
  "ds_read_b64_tr_b16 a[106:107], v105 offset:9216\n"
  "ds_read_b64_tr_b16 a[110:111], v106 offset:9216\n"
  "v_mfma_f32_16x16x32_bf16 v[20:23], v[52:55], a[236:239], v[20:23]\n"
  "s_add_u32 m0, s59, 6144\n"
  "ds_read_b64_tr_b16 a[114:115], v107 offset:11264\n"
  "ds_read_b64_tr_b16 a[118:119], v108 offset:11264\n"
  "v_mfma_f32_16x16x32_bf16 v[20:23], v[56:59], v[132:135], v[20:23]\n"
  "s_waitcnt lgkmcnt(6)\n"
  "buffer_load_dwordx4 v117, s[40:43], s57 offen offset:0 nt lds\n"
  "buffer_load_dwordx4 v118, s[40:43], s57 offen offset:1024 nt lds\n"
  "ds_read_b128 v[48:51], v93 offset:4096\n"
  "v_mfma_f32_16x16x32_bf16 v[20:23], v[60:63], v[156:159], v[20:23]\n"
  "ds_read_b128 v[52:55], v93 offset:5120\n"
  "s_waitcnt vmcnt(11)\n"
  "ds_read_b64_tr_b16 a[120:121], v103 offset:12288\n"
  "ds_read_b64_tr_b16 a[124:125], v104 offset:12288\n"
  "v_mfma_f32_16x16x32_bf16 v[16:19], v[56:59], v[128:131], v[16:19]\n"
  "buffer_load_dwordx4 v119, s[40:43], s57 offen offset:2048  lds\n"
  "v_mfma_f32_16x16x32_bf16 v[16:19], v[60:63], v[152:155], v[16:19]\n"
  "s_add_u32 s57, s57, 6144\n"
  "ds_read_b64_tr_b16 a[128:129], v105 offset:12288\n"
  "ds_read_b64_tr_b16 a[132:133], v106 offset:12288\n"
  "v_mfma_f32_16x16x32_bf16 v[12:15], v[56:59], a[252:255], v[12:15]\n"
  "s_add_u32 m0, s59, 9216\n"
  "ds_read_b64_tr_b16 a[136:137], v107 offset:14336\n"
  "ds_read_b64_tr_b16 a[140:141], v108 offset:14336\n"
  "v_mfma_f32_16x16x32_bf16 v[12:15], v[60:63], v[148:151], v[12:15]\n"
  "s_waitcnt lgkmcnt(8)\n"
  "buffer_load_dwordx4 v117, s[40:43], s57 offen offset:0 nt lds\n"
  "buffer_load_dwordx4 v118, s[40:43], s57 offen offset:1024 nt lds\n"
  "v_mfma_f32_16x16x32_bf16 v[8:11], v[56:59], a[248:251], v[8:11]\n"
  "s_waitcnt vmcnt(11)\n"
  "ds_read_b64_tr_b16 a[122:123], v103 offset:15360\n"
  "ds_read_b64_tr_b16 a[126:127], v104 offset:15360\n"
  "v_mfma_f32_16x16x32_bf16 v[8:11], v[60:63], v[144:147], v[8:11]\n"
  "buffer_load_dwordx4 v119, s[40:43], s57 offen offset:2048  lds\n"
  "v_mfma_f32_16x16x32_bf16 v[4:7], v[56:59], a[244:247], v[4:7]\n"
  "s_add_u32 s57, s57, s58\n"
  "ds_read_b64_tr_b16 a[130:131], v105 offset:15360\n"
  "ds_read_b64_tr_b16 a[134:135], v106 offset:15360\n"
  "v_mfma_f32_16x16x32_bf16 v[4:7], v[60:63], v[140:143], v[4:7]\n"
  "s_add_u32 m0, s59, 12288\n"
  "ds_read_b64_tr_b16 a[138:139], v107 offset:17408\n"
  "ds_read_b64_tr_b16 a[142:143], v108 offset:17408\n"
  "v_mfma_f32_16x16x32_bf16 v[0:3], v[56:59], a[240:243], v[0:3]\n"
  "s_waitcnt lgkmcnt(6)\n"
  "buffer_load_dwordx4 v117, s[40:43], s57 offen offset:0 nt lds\n"
  "buffer_load_dwordx4 v118, s[40:43], s57 offen offset:1024 nt lds\n"
  "v_mfma_f32_16x16x32_bf16 v[0:3], v[60:63], v[136:139], v[0:3]\n"
  "s_waitcnt vmcnt(11)\n"
  "ds_read_b64_tr_b16 a[144:145], v103 offset:0\n"
  "ds_read_b64_tr_b16 a[148:149], v104 offset:0\n"
  "v_mfma_f32_16x16x32_bf16 v[0:3], v[48:51], v[160:163], v[0:3]\n"
  "buffer_load_dwordx4 v119, s[40:43], s57 offen offset:2048  lds\n"
  "ds_read_b128 v[56:59], v93 offset:6144\n"
  "v_mfma_f32_16x16x32_bf16 v[0:3], v[52:55], v[184:187], v[0:3]\n"
  "s_add_u32 s57, s57, 6144\n"
  "ds_read_b128 v[60:63], v93 offset:7168\n"
  "ds_read_b64_tr_b16 a[152:153], v105 offset:0\n"
  "ds_read_b64_tr_b16 a[156:157], v106 offset:0\n"
  "v_mfma_f32_16x16x32_bf16 v[4:7], v[48:51], v[164:167], v[4:7]\n"
  "s_add_u32 m0, s59, 15360\n"
  "ds_read_b64_tr_b16 a[160:161], v107 offset:2048\n"
  "ds_read_b64_tr_b16 a[164:165], v108 offset:2048\n"
  "v_xor_b32 v93, 0x4000, v93\n"
  "v_mfma_f32_16x16x32_bf16 v[4:7], v[52:55], v[188:191], v[4:7]\n"
  "s_waitcnt lgkmcnt(8)\n"
  "buffer_load_dwordx4 v117, s[40:43], s57 offen offset:0 nt lds\n"
  "buffer_load_dwordx4 v118, s[40:43], s57 offen offset:1024 nt lds\n"
  "v_mfma_f32_16x16x32_bf16 v[8:11], v[48:51], v[168:171], v[8:11]\n"
  "s_waitcnt vmcnt(11)\n"
  "ds_read_b64_tr_b16 a[146:147], v103 offset:3072\n"
  "ds_read_b64_tr_b16 a[150:151], v104 offset:3072\n"
  "ds_write_b32 v98, v68\n"
  "v_mfma_f32_16x16x32_bf16 v[8:11], v[52:55], v[192:195], v[8:11]\n"
  "buffer_load_dwordx4 v119, s[40:43], s57 offen offset:2048  lds\n"
  "v_mfma_f32_16x16x32_bf16 v[12:15], v[48:51], v[172:175], v[12:15]\n"
  "s_add_u32 s57, s57, 6144\n"
  "ds_read_b64_tr_b16 a[154:155], v105 offset:3072\n"
  "ds_read_b64_tr_b16 a[158:159], v106 offset:3072\n"
  "v_xor_b32 v94, 0x2000, v94\n"
  "v_mfma_f32_16x16x32_bf16 v[12:15], v[52:55], v[196:199], v[12:15]\n"
  "s_add_u32 m0, s59, 0\n"
  "ds_read_b64_tr_b16 a[162:163], v107 offset:5120\n"
  "ds_read_b64_tr_b16 a[166:167], v108 offset:5120\n"
  "v_xor_b32 v95, 0x2000, v95\n"
  "v_mfma_f32_16x16x32_bf16 v[16:19], v[48:51], v[176:179], v[16:19]\n"
  "s_waitcnt lgkmcnt(7)\n"
  "buffer_load_dwordx4 v117, s[40:43], s57 offen offset:0 nt lds\n"
  "buffer_load_dwordx4 v118, s[40:43], s57 offen offset:1024 nt lds\n"
  "v_mfma_f32_16x16x32_bf16 v[16:19], v[52:55], v[200:203], v[16:19]\n"
  "s_waitcnt vmcnt(11)\n"
  "ds_read_b64_tr_b16 a[168:169], v103 offset:6144\n"
  "ds_read_b64_tr_b16 a[172:173], v104 offset:6144\n"
  "v_xor_b32 v98, 0x2000, v98\n"
  "v_mfma_f32_16x16x32_bf16 v[20:23], v[48:51], v[180:183], v[20:23]\n"
  "buffer_load_dwordx4 v119, s[40:43], s57 offen offset:2048  lds\n"
  "v_mfma_f32_16x16x32_bf16 v[20:23], v[52:55], v[204:207], v[20:23]\n"
  "s_add_u32 s57, s57, 6144\n"
  "ds_read_b64_tr_b16 a[176:177], v105 offset:6144\n"
  "ds_read_b64_tr_b16 a[180:181], v106 offset:6144\n"
  "s_waitcnt lgkmcnt(0)\n"
  "s_barrier\n"
  "v_mfma_f32_16x16x32_bf16 v[20:23], v[56:59], v[228:231], v[20:23]\n"
  "s_add_u32 m0, s59, 3072\n"
  "ds_read_b128 v[48:51], v92 offset:0\n"
  "ds_read_b64_tr_b16 a[184:185], v107 offset:8192\n"
  "ds_read_b64_tr_b16 a[188:189], v108 offset:8192\n"
  "v_mfma_f32_16x16x32_bf16 v[44:47], v[60:63], v[252:255], v[20:23]\n"
  "buffer_load_dwordx4 v117, s[40:43], s57 offen offset:0 nt lds\n"
  "buffer_load_dwordx4 v118, s[40:43], s57 offen offset:1024 nt lds\n"
  "ds_read_b128 v[52:55], v92 offset:1024\n"
  "v_mfma_f32_16x16x32_bf16 v[16:19], v[56:59], v[224:227], v[16:19]\n"
  "s_waitcnt vmcnt(11)\n"
  "ds_read_b64_tr_b16 a[170:171], v103 offset:9216\n"
  "ds_read_b64_tr_b16 a[174:175], v104 offset:9216\n"
  "ds_read_b64_tr_b16 v[80:81], v96 offset:0\n"
  "v_mfma_f32_16x16x32_bf16 v[40:43], v[60:63], v[248:251], v[16:19]\n"
  "s_sub_u32 s90, s90, 1\n"
  "s_cselect_b32 s63, s91, 6144\n"
  "s_cmov_b64 s[90:91], s[92:93]\n"
  "s_cmov_b64 s[92:93], s[94:95]\n"
  "buffer_load_dwordx4 v119, s[40:43], s57 offen offset:2048  lds\n"
  "ds_read_b64_tr_b16 v[82:83], v96 offset:32\n"
  "v_mfma_f32_16x16x32_bf16 v[12:15], v[56:59], v[220:223], v[12:15]\n"
  "s_add_u32 s57, s57, 6144\n"
  "ds_read_b64_tr_b16 a[178:179], v105 offset:9216\n"
  "ds_read_b64_tr_b16 a[182:183], v106 offset:9216\n"
  "ds_read_b64_tr_b16 v[84:85], v96 offset:2304\n"
  "v_mfma_f32_16x16x32_bf16 v[36:39], v[60:63], v[244:247], v[12:15]\n"
  "s_add_u32 m0, s59, 6144\n"
  "ds_read_b64_tr_b16 a[186:187], v107 offset:11264\n"
  "ds_read_b64_tr_b16 a[190:191], v108 offset:11264\n"
  "ds_read_b64_tr_b16 v[86:87], v96 offset:2336\n"
  "v_mfma_f32_16x16x32_bf16 v[8:11], v[56:59], v[216:219], v[8:11]\n"
  "s_waitcnt lgkmcnt(11)\n"
  "buffer_load_dwordx4 v117, s[40:43], s57 offen offset:0 nt lds\n"
  "buffer_load_dwordx4 v118, s[40:43], s57 offen offset:1024 nt lds\n"
  "v_mfma_f32_16x16x32_bf16 v[32:35], v[60:63], v[240:243], v[8:11]\n"
  "s_waitcnt vmcnt(11)\n"
  "ds_read_b64_tr_b16 a[192:193], v103 offset:12288\n"
  "ds_read_b64_tr_b16 a[196:197], v104 offset:12288\n"
  "ds_read_b64_tr_b16 v[88:89], v96 offset:4608\n"
  "v_mfma_f32_16x16x32_bf16 v[4:7], v[56:59], v[212:215], v[4:7]\n"
  "s_add_u32 s72, s72, 5\n"
  "buffer_load_dwordx4 v119, s[40:43], s57 offen offset:2048  lds\n"
  "ds_read_b64_tr_b16 v[90:91], v96 offset:4640\n"
  "v_mfma_f32_16x16x32_bf16 v[28:31], v[60:63], v[236:239], v[4:7]\n"
  "s_add_u32 s57, s57, 6144\n"
  "ds_read_b64_tr_b16 a[200:201], v105 offset:12288\n"
  "ds_read_b64_tr_b16 a[204:205], v106 offset:12288\n"
  "ds_read_b128 v[72:75], v99\n"
  "v_mfma_f32_16x16x32_bf16 v[0:3], v[56:59], v[208:211], v[0:3]\n"
  "s_add_u32 m0, s59, 9216\n"
  "ds_read_b64_tr_b16 a[208:209], v107 offset:14336\n"
  "ds_read_b64_tr_b16 a[212:213], v108 offset:14336\n"
  "ds_read_b128 v[112:115], v99 offset:64\n"
  "v_mfma_f32_16x16x32_bf16 v[24:27], v[60:63], v[232:235], v[0:3]\n"
  "s_waitcnt lgkmcnt(15)\n"
  "s_waitcnt lgkmcnt(11)\n"
  "buffer_load_dwordx4 v117, s[40:43], s57 offen offset:0 nt lds\n"
  "buffer_load_dwordx4 v118, s[40:43], s57 offen offset:1024 nt lds\n"
  "v_mfma_f32_16x16x32_bf16 v[0:3], v[48:51], a[0:3], 0\n"
  "ds_read_b128 v[56:59], v92 offset:2048\n"
  "s_waitcnt vmcnt(11)\n"
  "ds_read_b64_tr_b16 a[194:195], v103 offset:15360\n"
  "ds_read_b64_tr_b16 a[198:199], v104 offset:15360\n"
  "v_mfma_f32_16x16x32_bf16 v[0:3], v[52:55], a[24:27], v[0:3]\n"
  "s_sub_u32 s56, s56, 1\n"
  "buffer_load_dwordx4 v119, s[40:43], s57 offen offset:2048  lds\n"
  "ds_read_b128 v[60:63], v92 offset:3072\n"
  "v_mfma_f32_16x16x32_bf16 v[4:7], v[48:51], a[4:7], 0\n"
  "s_add_u32 s57, s57, 6144\n"
  "ds_read_b64_tr_b16 a[202:203], v105 offset:15360\n"
  "ds_read_b64_tr_b16 a[206:207], v106 offset:15360\n"
  "v_xor_b32 v96, 0x2000, v96\n"
  "v_mfma_f32_16x16x32_bf16 v[4:7], v[52:55], a[28:31], v[4:7]\n"
  "s_add_u32 m0, s59, 12288\n"
  "ds_read_b64_tr_b16 a[210:211], v107 offset:17408\n"
  "ds_read_b64_tr_b16 a[214:215], v108 offset:17408\n"
  "v_xor_b32 v99, 0x2000, v99\n"
  "v_mfma_f32_16x16x32_bf16 v[8:11], v[48:51], a[8:11], 0\n"
  "s_waitcnt lgkmcnt(9)\n"
  "buffer_load_dwordx4 v117, s[40:43], s57 offen offset:0 nt lds\n"
  "buffer_load_dwordx4 v118, s[40:43], s57 offen offset:1024 nt lds\n"
  "v_mfma_f32_16x16x32_bf16 v[8:11], v[52:55], a[32:35], v[8:11]\n"
  "s_waitcnt vmcnt(11)\n"
  "ds_read_b64_tr_b16 a[216:217], v103 offset:0\n"
  "ds_read_b64_tr_b16 a[220:221], v104 offset:0\n"
  "v_smfmac_f32_16x16x64_bf16 v[64:67], v[76:79], v[48:55], v110\n"
  "s_waitcnt lgkmcnt(10)\n"
  "buffer_load_dwordx4 v119, s[40:43], s57 offen offset:2048  lds\n"
  "v_add_f32 v72, v72, v112\n"
  "v_mfma_f32_16x16x32_bf16 v[12:15], v[48:51], a[12:15], 0\n"
  "s_add_u32 s57, s57, 6144\n"
  "ds_read_b64_tr_b16 a[224:225], v105 offset:0\n"
  "ds_read_b64_tr_b16 a[228:229], v106 offset:0\n"
  "v_add_f32 v73, v73, v113\n"
  "v_mfma_f32_16x16x32_bf16 v[12:15], v[52:55], a[36:39], v[12:15]\n"
  "s_add_u32 m0, s59, 15360\n"
  "ds_read_b64_tr_b16 a[232:233], v107 offset:2048\n"
  "ds_read_b64_tr_b16 a[236:237], v108 offset:2048\n"
  "v_add_f32 v74, v74, v114\n"
  "v_mfma_f32_16x16x32_bf16 v[16:19], v[48:51], a[16:19], 0\n"
  "s_waitcnt lgkmcnt(6)\n"
  "buffer_load_dwordx4 v117, s[40:43], s57 offen offset:0 nt lds\n"
  "buffer_load_dwordx4 v118, s[40:43], s57 offen offset:1024 nt lds\n"
  "v_mfma_f32_16x16x32_bf16 v[16:19], v[52:55], a[40:43], v[16:19]\n"
  "s_waitcnt vmcnt(11)\n"
  "ds_read_b64_tr_b16 a[218:219], v103 offset:3072\n"
  "ds_read_b64_tr_b16 a[222:223], v104 offset:3072\n"
  "v_add_f32 v75, v75, v115\n"
  "v_mfma_f32_16x16x32_bf16 v[20:23], v[48:51], a[20:23], 0\n"
  "s_sub_i32 s61, s56, 5\n"
  "buffer_load_dwordx4 v119, s[40:43], s57 offen offset:2048  lds\n"
  "v_max3_f32 v109, v109, v72, v73\n"
  "v_mfma_f32_16x16x32_bf16 v[20:23], v[52:55], a[44:47], v[20:23]\n"
  "s_add_u32 s57, s57, 6144\n"
  "buffer_store_dwordx4 v[80:83], v97, s[48:51], s68 offen offset:0 nt\n"
  "ds_read_b64_tr_b16 a[226:227], v105 offset:3072\n"
  "ds_read_b64_tr_b16 a[230:231], v106 offset:3072\n"
  "v_max3_f32 v109, v109, v74, v75\n"
  "v_mfma_f32_16x16x32_bf16 v[20:23], v[56:59], a[68:71], v[20:23]\n"
  "s_add_u32 m0, s59, 0\n"
  "ds_read_b128 v[48:51], v92 offset:4096\n"
  "ds_read_b64_tr_b16 a[234:235], v107 offset:5120\n"
  "ds_read_b64_tr_b16 a[238:239], v108 offset:5120\n"
  "v_mfma_f32_16x16x32_bf16 v[20:23], v[60:63], a[92:95], v[20:23]\n"
  "s_waitcnt lgkmcnt(7)\n"
  "buffer_load_dwordx4 v117, s[40:43], s57 offen offset:0 nt lds\n"
  "buffer_load_dwordx4 v118, s[40:43], s57 offen offset:1024 nt lds\n"
  "ds_read_b128 v[52:55], v92 offset:5120\n"
  "v_mfma_f32_16x16x32_bf16 v[16:19], v[56:59], a[64:67], v[16:19]\n"
  "s_waitcnt vmcnt(12)\n"
  "ds_read_b64_tr_b16 a[240:241], v103 offset:6144\n"
  "ds_read_b64_tr_b16 a[244:245], v104 offset:6144\n"
  "v_mfma_f32_16x16x32_bf16 v[16:19], v[60:63], a[88:91], v[16:19]\n"
  "buffer_load_dwordx4 v119, s[40:43], s57 offen offset:2048  lds\n"
  "v_mfma_f32_16x16x32_bf16 v[12:15], v[56:59], a[60:63], v[12:15]\n"
  "s_add_u32 s57, s57, 6144\n"
  "ds_read_b64_tr_b16 a[248:249], v105 offset:6144\n"
  "ds_read_b64_tr_b16 a[252:253], v106 offset:6144\n"
  "v_mfma_f32_16x16x32_bf16 v[12:15], v[60:63], a[84:87], v[12:15]\n"
  "s_add_u32 m0, s59, 3072\n"
  "ds_read_b64_tr_b16 v[128:129], v107 offset:8192\n"
  "ds_read_b64_tr_b16 v[132:133], v108 offset:8192\n"
  "v_smfmac_f32_16x16x64_bf16 v[64:67], v[76:79], v[56:63], v110\n"
  "s_waitcnt lgkmcnt(7)\n"
  "buffer_load_dwordx4 v117, s[40:43], s57 offen offset:0 nt lds\n"
  "buffer_load_dwordx4 v118, s[40:43], s57 offen offset:1024 nt lds\n"
  "v_mfma_f32_16x16x32_bf16 v[8:11], v[56:59], a[56:59], v[8:11]\n"
  "s_waitcnt vmcnt(12)\n"
  "ds_read_b64_tr_b16 a[242:243], v103 offset:9216\n"
  "ds_read_b64_tr_b16 a[246:247], v104 offset:9216\n"
  "v_mfma_f32_16x16x32_bf16 v[8:11], v[60:63], a[80:83], v[8:11]\n"
  "buffer_load_dwordx4 v119, s[40:43], s57 offen offset:2048  lds\n"
  "v_mfma_f32_16x16x32_bf16 v[4:7], v[56:59], a[52:55], v[4:7]\n"
  "s_add_u32 s57, s57, 6144\n"
  "ds_read_b64_tr_b16 a[250:251], v105 offset:9216\n"
  "ds_read_b64_tr_b16 a[254:255], v106 offset:9216\n"
  "v_mfma_f32_16x16x32_bf16 v[4:7], v[60:63], a[76:79], v[4:7]\n"
  "s_add_u32 m0, s59, 6144\n"
  "ds_read_b64_tr_b16 v[130:131], v107 offset:11264\n"
  "ds_read_b64_tr_b16 v[134:135], v108 offset:11264\n"
  "v_mfma_f32_16x16x32_bf16 v[0:3], v[56:59], a[48:51], v[0:3]\n"
  "s_waitcnt lgkmcnt(6)\n"
  "buffer_load_dwordx4 v117, s[40:43], s57 offen offset:0 nt lds\n"
  "buffer_load_dwordx4 v118, s[40:43], s57 offen offset:1024 nt lds\n"
  "v_mfma_f32_16x16x32_bf16 v[0:3], v[60:63], a[72:75], v[0:3]\n"
  "s_waitcnt vmcnt(12)\n"
  "ds_read_b64_tr_b16 v[136:137], v103 offset:12288\n"
  "ds_read_b64_tr_b16 v[140:141], v104 offset:12288\n"
  "v_mfma_f32_16x16x32_bf16 v[0:3], v[48:51], a[96:99], v[0:3]\n"
  "buffer_load_dwordx4 v119, s[40:43], s57 offen offset:2048  lds\n"
  "ds_read_b128 v[56:59], v92 offset:6144\n"
  "v_mfma_f32_16x16x32_bf16 v[0:3], v[52:55], a[120:123], v[0:3]\n"
  "s_add_u32 s57, s57, 6144\n"
  "buffer_store_dwordx4 v[84:87], v97, s[48:51], s68 offen offset:128 nt\n"
  "ds_read_b128 v[60:63], v92 offset:7168\n"
  "ds_read_b64_tr_b16 v[144:145], v105 offset:12288\n"
  "ds_read_b64_tr_b16 v[148:149], v106 offset:12288\n"
  "v_mfma_f32_16x16x32_bf16 v[4:7], v[48:51], a[100:103], v[4:7]\n"
  "s_add_u32 m0, s59, 9216\n"
  "ds_read_b64_tr_b16 v[152:153], v107 offset:14336\n"
  "ds_read_b64_tr_b16 v[156:157], v108 offset:14336\n"
  "v_xor_b32 v92, 0x4000, v92\n"
  "v_mfma_f32_16x16x32_bf16 v[4:7], v[52:55], a[124:127], v[4:7]\n"
  "s_waitcnt lgkmcnt(8)\n"
  "buffer_load_dwordx4 v117, s[40:43], s57 offen offset:0 nt lds\n"
  "buffer_load_dwordx4 v118, s[40:43], s57 offen offset:1024 nt lds\n"
  "v_mfma_f32_16x16x32_bf16 v[8:11], v[48:51], a[104:107], v[8:11]\n"
  "s_waitcnt vmcnt(13)\n"
  "ds_read_b64_tr_b16 v[138:139], v103 offset:15360\n"
  "ds_read_b64_tr_b16 v[142:143], v104 offset:15360\n"
  "v_mfma_f32_16x16x32_bf16 v[8:11], v[52:55], a[128:131], v[8:11]\n"
  "buffer_load_dwordx4 v119, s[40:43], s57 offen offset:2048  lds\n"
  "v_smfmac_f32_16x16x64_bf16 v[64:67], v[76:79], v[48:55], v110\n"
  "s_add_u32 s57, s57, 6144\n"
  "ds_read_b64_tr_b16 v[146:147], v105 offset:15360\n"
  "ds_read_b64_tr_b16 v[150:151], v106 offset:15360\n"
  "v_mfma_f32_16x16x32_bf16 v[12:15], v[48:51], a[108:111], v[12:15]\n"
  "s_add_u32 m0, s59, 12288\n"
  "ds_read_b64_tr_b16 v[154:155], v107 offset:17408\n"
  "ds_read_b64_tr_b16 v[158:159], v108 offset:17408\n"
  "v_mfma_f32_16x16x32_bf16 v[12:15], v[52:55], a[132:135], v[12:15]\n"
  "s_waitcnt lgkmcnt(6)\n"
  "buffer_load_dwordx4 v117, s[40:43], s57 offen offset:0 nt lds\n"
  "buffer_load_dwordx4 v118, s[40:43], s57 offen offset:1024 nt lds\n"
  "v_mfma_f32_16x16x32_bf16 v[16:19], v[48:51], a[112:115], v[16:19]\n"
  "s_waitcnt vmcnt(12)\n"
  "ds_read_b64_tr_b16 v[160:161], v103 offset:0\n"
  "ds_read_b64_tr_b16 v[164:165], v104 offset:0\n"
  "v_mfma_f32_16x16x32_bf16 v[16:19], v[52:55], a[136:139], v[16:19]\n"
  "buffer_load_dwordx4 v119, s[40:43], s57 offen offset:2048  lds\n"
  "v_mfma_f32_16x16x32_bf16 v[20:23], v[48:51], a[116:119], v[20:23]\n"
  "s_add_u32 s57, s57, 6144\n"
  "ds_read_b64_tr_b16 v[168:169], v105 offset:0\n"
  "ds_read_b64_tr_b16 v[172:173], v106 offset:0\n"
  "v_mfma_f32_16x16x32_bf16 v[20:23], v[52:55], a[140:143], v[20:23]\n"
  "s_add_u32 m0, s59, 15360\n"
  "ds_read_b64_tr_b16 v[176:177], v107 offset:2048\n"
  "ds_read_b64_tr_b16 v[180:181], v108 offset:2048\n"
  "v_mfma_f32_16x16x32_bf16 v[20:23], v[56:59], a[164:167], v[20:23]\n"
  "s_waitcnt lgkmcnt(6)\n"
  "buffer_load_dwordx4 v117, s[40:43], s57 offen offset:0 nt lds\n"
  "buffer_load_dwordx4 v118, s[40:43], s57 offen offset:1024 nt lds\n"
  "ds_read_b128 v[48:51], v93 offset:0\n"
  "v_mfma_f32_16x16x32_bf16 v[20:23], v[60:63], a[188:191], v[20:23]\n"
  "buffer_store_dwordx4 v[88:91], v97, s[48:51], s68 offen offset:256 nt\n"
  "ds_read_b128 v[52:55], v93 offset:1024\n"
  "s_waitcnt vmcnt(13)\n"
  "ds_read_b64_tr_b16 v[162:163], v103 offset:3072\n"
  "ds_read_b64_tr_b16 v[166:167], v104 offset:3072\n"
  "v_mfma_f32_16x16x32_bf16 v[16:19], v[56:59], a[160:163], v[16:19]\n"
  "s_add_u32 s68, s68, s69\n"
  "buffer_load_dwordx4 v119, s[40:43], s57 offen offset:2048  lds\n"
  "v_mfma_f32_16x16x32_bf16 v[16:19], v[60:63], a[184:187], v[16:19]\n"
  "s_add_u32 s57, s57, 6144\n"
  "ds_read_b64_tr_b16 v[170:171], v105 offset:3072\n"
  "ds_read_b64_tr_b16 v[174:175], v106 offset:3072\n"
  "v_mfma_f32_16x16x32_bf16 v[12:15], v[56:59], a[156:159], v[12:15]\n"
  "s_add_u32 m0, s59, 0\n"
  "ds_read_b64_tr_b16 v[178:179], v107 offset:5120\n"
  "ds_read_b64_tr_b16 v[182:183], v108 offset:5120\n"
  "v_mfma_f32_16x16x32_bf16 v[12:15], v[60:63], a[180:183], v[12:15]\n"
  "s_waitcnt lgkmcnt(8)\n"
  "buffer_load_dwordx4 v117, s[40:43], s57 offen offset:0 nt lds\n"
  "buffer_load_dwordx4 v118, s[40:43], s57 offen offset:1024 nt lds\n"
  "v_smfmac_f32_16x16x64_bf16 v[64:67], v[76:79], v[56:63], v110\n"
  "s_waitcnt vmcnt(13)\n"
  "ds_read_b64_tr_b16 v[184:185], v103 offset:6144\n"
  "ds_read_b64_tr_b16 v[188:189], v104 offset:6144\n"
  "v_mfma_f32_16x16x32_bf16 v[8:11], v[56:59], a[152:155], v[8:11]\n"
  "s_mov_b32 s69, s70\n"
  "buffer_load_dwordx4 v119, s[40:43], s57 offen offset:2048  lds\n"
  "v_mfma_f32_16x16x32_bf16 v[8:11], v[60:63], a[176:179], v[8:11]\n"
  "s_add_u32 s57, s57, 6144\n"
  "ds_read_b64_tr_b16 v[192:193], v105 offset:6144\n"
  "ds_read_b64_tr_b16 v[196:197], v106 offset:6144\n"
  "v_mfma_f32_16x16x32_bf16 v[4:7], v[56:59], a[148:151], v[4:7]\n"
  "s_add_u32 m0, s59, 3072\n"
  "ds_read_b64_tr_b16 v[200:201], v107 offset:8192\n"
  "ds_read_b64_tr_b16 v[204:205], v108 offset:8192\n"
  "v_add_f32 v68, v64, v65\n"
  "v_mfma_f32_16x16x32_bf16 v[4:7], v[60:63], a[172:175], v[4:7]\n"
  "s_waitcnt lgkmcnt(6)\n"
  "buffer_load_dwordx4 v117, s[40:43], s57 offen offset:0 nt lds\n"
  "buffer_load_dwordx4 v118, s[40:43], s57 offen offset:1024 nt lds\n"
  "v_mfma_f32_16x16x32_bf16 v[0:3], v[56:59], a[144:147], v[0:3]\n"
  "s_waitcnt vmcnt(12)\n"
  "ds_read_b64_tr_b16 v[186:187], v103 offset:9216\n"
  "ds_read_b64_tr_b16 v[190:191], v104 offset:9216\n"
  "v_mov_b32 v64, 0\n"
  "v_mfma_f32_16x16x32_bf16 v[0:3], v[60:63], a[168:171], v[0:3]\n"
  "buffer_load_dwordx4 v119, s[40:43], s57 offen offset:2048  lds\n"
  "v_mov_b32 v65, 0\n"
  "v_mfma_f32_16x16x32_bf16 v[0:3], v[48:51], a[192:195], v[0:3]\n"
  "s_add_u32 s57, s57, 6144\n"
  "s_add_u32 m0, s66, 0\n"
  "ds_read_b128 v[56:59], v93 offset:2048\n"
  "ds_read_b64_tr_b16 v[194:195], v105 offset:9216\n"
  "ds_read_b64_tr_b16 v[198:199], v106 offset:9216\n"
  "v_mfma_f32_16x16x32_bf16 v[0:3], v[52:55], a[216:219], v[0:3]\n"
  "buffer_load_dwordx4 v100, s[44:47], s67 offen offset:0 nt lds\n"
  "ds_read_b128 v[60:63], v93 offset:3072\n"
  "ds_read_b64_tr_b16 v[202:203], v107 offset:11264\n"
  "ds_read_b64_tr_b16 v[206:207], v108 offset:11264\n"
  "v_mfma_f32_16x16x32_bf16 v[4:7], v[48:51], a[196:199], v[4:7]\n"
  "s_add_u32 m0, s66, 1920\n"
  "ds_write_b32 v98, v68\n"
  "v_mfma_f32_16x16x32_bf16 v[4:7], v[52:55], a[220:223], v[4:7]\n"
  "buffer_load_dwordx4 v100, s[44:47], s67 offen offset:128 nt lds\n"
  "s_waitcnt vmcnt(12)\n"
  "ds_read_b64_tr_b16 v[208:209], v103 offset:12288\n"
  "ds_read_b64_tr_b16 v[212:213], v104 offset:12288\n"
  "v_mfma_f32_16x16x32_bf16 v[8:11], v[48:51], a[200:203], v[8:11]\n"
  "s_add_u32 m0, s66, 3840\n"
  "v_rcp_f32 v72, v72\n"
  "v_mfma_f32_16x16x32_bf16 v[8:11], v[52:55], a[224:227], v[8:11]\n"
  "buffer_load_dwordx4 v100, s[44:47], s67 offen offset:256 nt lds\n"
  "ds_read_b64_tr_b16 v[216:217], v105 offset:12288\n"
  "ds_read_b64_tr_b16 v[220:221], v106 offset:12288\n"
  "v_mfma_f32_16x16x32_bf16 v[12:15], v[48:51], a[204:207], v[12:15]\n"
  "s_add_u32 s66, s66, 6144\n"
  "ds_read_b64_tr_b16 v[224:225], v107 offset:14336\n"
  "ds_read_b64_tr_b16 v[228:229], v108 offset:14336\n"
  "v_mul_f32 v40, v40, v72\n"
  "v_mfma_f32_16x16x32_bf16 v[12:15], v[52:55], a[228:231], v[12:15]\n"
  "s_cmp_ge_u32 s66, 86016\n"
  "s_cselect_b32 s64, 36864, 0\n"
  "v_rcp_f32 v73, v73\n"
  "v_mul_f32 v44, v44, v72\n"
  "v_mfma_f32_16x16x32_bf16 v[16:19], v[48:51], a[208:211], v[16:19]\n"
  "s_sub_u32 s66, s66, s64\n"
  "s_add_u32 m0, s66, 0\n"
  "s_waitcnt vmcnt(9)\n"
  "ds_read_b64_tr_b16 v[210:211], v103 offset:15360\n"
  "ds_read_b64_tr_b16 v[214:215], v104 offset:15360\n"
  "v_cvt_pk_bf16_f32 v40, v40, v44\n"
  "v_mfma_f32_16x16x32_bf16 v[16:19], v[52:55], a[232:235], v[16:19]\n"
  "s_add_u32 s67, s67, s63\n"
  "buffer_load_dwordx4 v100, s[44:47], s67 offen offset:0 nt lds\n"
  "v_mfma_f32_16x16x32_bf16 v[20:23], v[48:51], a[212:215], v[20:23]\n"
  "s_add_u32 m0, s66, 1920\n"
  "ds_read_b64_tr_b16 v[218:219], v105 offset:15360\n"
  "ds_read_b64_tr_b16 v[222:223], v106 offset:15360\n"
  "v_mul_f32 v41, v41, v73\n"
  "v_mfma_f32_16x16x32_bf16 v[20:23], v[52:55], a[236:239], v[20:23]\n"
  "s_waitcnt lgkmcnt(13)\n"
  "buffer_load_dwordx4 v100, s[44:47], s67 offen offset:128 nt lds\n"
  "ds_read_b64_tr_b16 v[226:227], v107 offset:17408\n"
  "ds_read_b64_tr_b16 v[230:231], v108 offset:17408\n"
  "v_mfma_f32_16x16x32_bf16 v[20:23], v[56:59], v[132:135], v[20:23]\n"
  "s_add_u32 m0, s66, 3840\n"
  "ds_read_b128 v[48:51], v93 offset:4096\n"
  "v_rcp_f32 v74, v74\n"
  "v_mfma_f32_16x16x32_bf16 v[20:23], v[60:63], v[156:159], v[20:23]\n"
  "buffer_load_dwordx4 v100, s[44:47], s67 offen offset:256 nt lds\n"
  "ds_read_b128 v[52:55], v93 offset:5120\n"
  "s_waitcnt vmcnt(9)\n"
  "ds_read_b64_tr_b16 v[232:233], v103 offset:0\n"
  "ds_read_b64_tr_b16 v[236:237], v104 offset:0\n"
  "v_mfma_f32_16x16x32_bf16 v[16:19], v[56:59], v[128:131], v[16:19]\n"
  "s_add_u32 s66, s66, 6144\n"
  "v_rcp_f32 v75, v75\n"
  "v_mul_f32 v45, v45, v73\n"
  "v_mfma_f32_16x16x32_bf16 v[16:19], v[60:63], v[152:155], v[16:19]\n"
  "s_cmp_ge_u32 s66, 86016\n"
  "s_cselect_b32 s64, 36864, 0\n"
  "ds_read_b64_tr_b16 v[240:241], v105 offset:0\n"
  "ds_read_b64_tr_b16 v[244:245], v106 offset:0\n"
  "v_cvt_pk_bf16_f32 v41, v41, v45\n"
  "v_mfma_f32_16x16x32_bf16 v[12:15], v[56:59], a[252:255], v[12:15]\n"
  "s_sub_u32 s66, s66, s64\n"
  "s_add_u32 m0, s66, 0\n"
  "ds_read_b64_tr_b16 v[248:249], v107 offset:2048\n"
  "ds_read_b64_tr_b16 v[252:253], v108 offset:2048\n"
  "v_mul_f32 v42, v42, v74\n"
  "v_mfma_f32_16x16x32_bf16 v[12:15], v[60:63], v[148:151], v[12:15]\n"
  "s_sub_u32 s90, s90, 1\n"
  "s_cselect_b32 s63, s91, 6144\n"
  "s_cmov_b64 s[90:91], s[92:93]\n"
  "s_cmov_b64 s[92:93], s[94:95]\n"
  "s_add_u32 s67, s67, s63\n"
  "buffer_load_dwordx4 v100, s[44:47], s67 offen offset:0 nt lds\n"
  "v_mfma_f32_16x16x32_bf16 v[8:11], v[56:59], a[248:251], v[8:11]\n"
  "s_add_u32 m0, s66, 1920\n"
  "s_waitcnt vmcnt(7)\n"
  "ds_read_b64_tr_b16 v[234:235], v103 offset:3072\n"
  "ds_read_b64_tr_b16 v[238:239], v104 offset:3072\n"
  "v_mul_f32 v46, v46, v74\n"
  "v_mfma_f32_16x16x32_bf16 v[8:11], v[60:63], v[144:147], v[8:11]\n"
  "buffer_load_dwordx4 v100, s[44:47], s67 offen offset:128 nt lds\n"
  "v_mfma_f32_16x16x32_bf16 v[4:7], v[56:59], a[244:247], v[4:7]\n"
  "s_add_u32 m0, s66, 3840\n"
  "ds_read_b64_tr_b16 v[242:243], v105 offset:3072\n"
  "ds_read_b64_tr_b16 v[246:247], v106 offset:3072\n"
  "v_cvt_pk_bf16_f32 v42, v42, v46\n"
  "v_mfma_f32_16x16x32_bf16 v[4:7], v[60:63], v[140:143], v[4:7]\n"
  "buffer_load_dwordx4 v100, s[44:47], s67 offen offset:256 nt lds\n"
  "ds_read_b64_tr_b16 v[250:251], v107 offset:5120\n"
  "ds_read_b64_tr_b16 v[254:255], v108 offset:5120\n"
  "v_mfma_f32_16x16x32_bf16 v[0:3], v[56:59], a[240:243], v[0:3]\n"
  "s_add_u32 s72, s72, 1\n"
  "ds_write2_b32 v95, v40, v41 offset0:0 offset1:72\n"
  "v_mfma_f32_16x16x32_bf16 v[0:3], v[60:63], v[136:139], v[0:3]\n"
  "s_waitcnt lgkmcnt(13)\n"
  "v_mul_f32 v43, v43, v75\n"
  "v_mul_f32 v47, v47, v75\n"
  "v_cvt_pk_bf16_f32 v43, v43, v47\n"
  "v_mfma_f32_16x16x32_bf16 v[0:3], v[48:51], v[160:163], v[0:3]\n"
  "s_lshr_b32 s74, s72, 1\n"
  "ds_read_b128 v[56:59], v93 offset:6144\n"
  "v_mul_f32 v24, v24, v72\n"
  "v_mul_f32 v25, v25, v73\n"
  "v_mfma_f32_16x16x32_bf16 v[0:3], v[52:55], v[184:187], v[0:3]\n"
  "s_add_u32 s66, s66, 6144\n"
  "ds_read_b128 v[60:63], v93 offset:7168\n"
  "v_mul_f32 v28, v28, v72\n"
  "v_mul_f32 v29, v29, v73\n"
  "v_mfma_f32_16x16x32_bf16 v[4:7], v[48:51], v[164:167], v[4:7]\n"
  "s_cmp_ge_u32 s66, 86016\n"
  "s_cselect_b32 s64, 36864, 0\n"
  "ds_write2_b32 v95, v42, v43 offset0:144 offset1:216\n"
  "v_mfma_f32_16x16x32_bf16 v[4:7], v[52:55], v[188:191], v[4:7]\n"
  "v_mul_f32 v32, v32, v72\n"
  "v_mul_f32 v33, v33, v73\n"
  "v_mul_f32 v36, v36, v72\n"
  "v_mul_f32 v37, v37, v73\n"
  "v_cvt_pk_bf16_f32 v24, v24, v28\n"
  "v_cvt_pk_bf16_f32 v28, v25, v29\n"
  "v_cvt_pk_bf16_f32 v25, v32, v36\n"
  "v_cvt_pk_bf16_f32 v29, v33, v37\n"
  "v_mul_f32 v26, v26, v74\n"
  "v_mul_f32 v27, v27, v75\n"
  "v_mul_f32 v30, v30, v74\n"
  "v_mul_f32 v31, v31, v75\n"
  "v_mul_f32 v34, v34, v74\n"
  "v_mul_f32 v35, v35, v75\n"
  "v_mul_f32 v38, v38, v74\n"
  "v_mul_f32 v39, v39, v75\n"
  "v_cvt_pk_bf16_f32 v26, v26, v30\n"
  "v_cvt_pk_bf16_f32 v30, v27, v31\n"
  "v_cvt_pk_bf16_f32 v27, v34, v38\n"
  "v_cvt_pk_bf16_f32 v31, v35, v39\n"
  "v_mfma_f32_16x16x32_bf16 v[8:11], v[48:51], v[168:171], v[8:11]\n"
  "ds_write_b64 v94, v[24:25] offset:0\n"
  "ds_write_b64 v94, v[28:29] offset:288\n"
  "ds_write_b64 v94, v[26:27] offset:576\n"
  "ds_write_b64 v94, v[30:31] offset:864\n"
  "v_mfma_f32_16x16x32_bf16 v[8:11], v[52:55], v[192:195], v[8:11]\n"
  "s_sub_u32 s66, s66, s64\n"
  "v_readlane_b32 s73, v116, s74\n"
  "v_max3_f32 v109, v109, v72, v73\n"
  "v_max3_f32 v109, v109, v74, v75\n"
  "v_mfma_f32_16x16x32_bf16 v[12:15], v[48:51], v[172:175], v[12:15]\n"
  "s_sub_u32 s90, s90, 1\n"
  "s_cselect_b32 s63, s91, 6144\n"
  "s_cmov_b64 s[90:91], s[92:93]\n"
  "s_cmov_b64 s[92:93], s[94:95]\n"
  "v_xor_b32 v94, 0x2000, v94\n"
  "v_xor_b32 v95, 0x2000, v95\n"
  "v_xor_b32 v98, 0x2000, v98\n"
  "v_mfma_f32_16x16x32_bf16 v[12:15], v[52:55], v[196:199], v[12:15]\n"
  "s_add_u32 s67, s67, s63\n"
  "v_xor_b32 v93, 0x4000, v93\n"
  "v_mfma_f32_16x16x32_bf16 v[16:19], v[48:51], v[176:179], v[16:19]\n"
  "s_lshl_b32 s74, s72, 4\n"
  "v_mfma_f32_16x16x32_bf16 v[16:19], v[52:55], v[200:203], v[16:19]\n"
  "s_lshr_b32 s73, s73, s74\n"
  "v_mfma_f32_16x16x32_bf16 v[20:23], v[48:51], v[180:183], v[20:23]\n"
  "s_and_b32 s73, s73, 0xffff\n"
  "v_mfma_f32_16x16x32_bf16 v[20:23], v[52:55], v[204:207], v[20:23]\n"
  "s_lshl_b32 s73, s73, 13\n"
  "s_movk_i32 s70, 6144\n"
  "s_branch B_H_%=\n"
  "B_LAST_%=:\n"
  "s_waitcnt lgkmcnt(0)\n"
  "v_mfma_f32_16x16x32_bf16 v[20:23], v[56:59], v[228:231], v[20:23]\n"
  "v_mfma_f32_16x16x32_bf16 v[44:47], v[60:63], v[252:255], v[20:23]\n"
  "v_mfma_f32_16x16x32_bf16 v[16:19], v[56:59], v[224:227], v[16:19]\n"
  "v_mfma_f32_16x16x32_bf16 v[40:43], v[60:63], v[248:251], v[16:19]\n"
  "v_mfma_f32_16x16x32_bf16 v[12:15], v[56:59], v[220:223], v[12:15]\n"
  "v_mfma_f32_16x16x32_bf16 v[36:39], v[60:63], v[244:247], v[12:15]\n"
  "v_mfma_f32_16x16x32_bf16 v[8:11], v[56:59], v[216:219], v[8:11]\n"
  "v_mfma_f32_16x16x32_bf16 v[32:35], v[60:63], v[240:243], v[8:11]\n"
  "v_mfma_f32_16x16x32_bf16 v[4:7], v[56:59], v[212:215], v[4:7]\n"
  "v_mfma_f32_16x16x32_bf16 v[28:31], v[60:63], v[236:239], v[4:7]\n"
  "v_mfma_f32_16x16x32_bf16 v[0:3], v[56:59], v[208:211], v[0:3]\n"
  "v_mfma_f32_16x16x32_bf16 v[24:27], v[60:63], v[232:235], v[0:3]\n"
  "s_barrier\n"
  "ds_read_b128 v[72:75], v99\n"
  "ds_read_b128 v[112:115], v99 offset:64\n"
  "ds_read_b64_tr_b16 v[80:81], v96 offset:0\n"
  "ds_read_b64_tr_b16 v[82:83], v96 offset:32\n"
  "ds_read_b64_tr_b16 v[84:85], v96 offset:2304\n"
  "ds_read_b64_tr_b16 v[86:87], v96 offset:2336\n"
  "ds_read_b64_tr_b16 v[88:89], v96 offset:4608\n"
  "ds_read_b64_tr_b16 v[90:91], v96 offset:4640\n"
  "v_xor_b32 v96, 0x2000, v96\n"
  "v_xor_b32 v99, 0x2000, v99\n"
  "s_waitcnt lgkmcnt(6)\n"
  "v_add_f32 v72, v72, v112\n"
  "v_add_f32 v73, v73, v113\n"
  "v_add_f32 v74, v74, v114\n"
  "v_add_f32 v75, v75, v115\n"
  "v_max3_f32 v109, v109, v72, v73\n"
  "v_max3_f32 v109, v109, v74, v75\n"
  "v_rcp_f32 v72, v72\n"
  "s_nop 0\n"
  "v_mul_f32 v40, v40, v72\n"
  "v_rcp_f32 v73, v73\n"
  "v_rcp_f32 v74, v74\n"
  "v_rcp_f32 v75, v75\n"
  "v_mul_f32 v44, v44, v72\n"
  "v_cvt_pk_bf16_f32 v40, v40, v44\n"
  "v_mul_f32 v41, v41, v73\n"
  "v_mul_f32 v45, v45, v73\n"
  "v_cvt_pk_bf16_f32 v41, v41, v45\n"
  "v_mul_f32 v42, v42, v74\n"
  "v_mul_f32 v46, v46, v74\n"
  "v_cvt_pk_bf16_f32 v42, v42, v46\n"
  "v_mul_f32 v43, v43, v75\n"
  "v_mul_f32 v47, v47, v75\n"
  "v_cvt_pk_bf16_f32 v43, v43, v47\n"
  "v_mul_f32 v24, v24, v72\n"
  "v_mul_f32 v25, v25, v73\n"
  "v_mul_f32 v28, v28, v72\n"
  "v_mul_f32 v29, v29, v73\n"
  "s_waitcnt lgkmcnt(4)\n"
  "buffer_store_dwordx4 v[80:83], v97, s[48:51], s68 offen offset:0 nt\n"
  "v_mul_f32 v32, v32, v72\n"
  "v_mul_f32 v33, v33, v73\n"
  "v_mul_f32 v36, v36, v72\n"
  "v_mul_f32 v37, v37, v73\n"
  "v_cvt_pk_bf16_f32 v24, v24, v28\n"
  "v_cvt_pk_bf16_f32 v28, v25, v29\n"
  "v_cvt_pk_bf16_f32 v25, v32, v36\n"
  "v_cvt_pk_bf16_f32 v29, v33, v37\n"
  "v_mul_f32 v26, v26, v74\n"
  "v_mul_f32 v27, v27, v75\n"
  "v_mul_f32 v30, v30, v74\n"
  "v_mul_f32 v31, v31, v75\n"
  "v_mul_f32 v34, v34, v74\n"
  "v_mul_f32 v35, v35, v75\n"
  "v_mul_f32 v38, v38, v74\n"
  "v_mul_f32 v39, v39, v75\n"
  "v_cvt_pk_bf16_f32 v26, v26, v30\n"
  "v_cvt_pk_bf16_f32 v30, v27, v31\n"
  "v_cvt_pk_bf16_f32 v27, v34, v38\n"
  "v_cvt_pk_bf16_f32 v31, v35, v39\n"
  "v_max3_f32 v109, v109, v72, v73\n"
  "v_max3_f32 v109, v109, v74, v75\n"
  "s_waitcnt lgkmcnt(2)\n"
  "buffer_store_dwordx4 v[84:87], v97, s[48:51], s68 offen offset:128 nt\n"
  "ds_write2_b32 v95, v40, v41 offset0:0 offset1:72\n"
  "v_xor_b32 v92, 0x4000, v92\n"
  "ds_write2_b32 v95, v42, v43 offset0:144 offset1:216\n"
  "ds_write_b64 v94, v[24:25] offset:0\n"
  "ds_write_b64 v94, v[28:29] offset:288\n"
  "ds_write_b64 v94, v[26:27] offset:576\n"
  "ds_write_b64 v94, v[30:31] offset:864\n"
  "s_waitcnt lgkmcnt(6)\n"
  "buffer_store_dwordx4 v[88:91], v97, s[48:51], s68 offen offset:256 nt\n"
  "s_nop 1\n"
  "s_add_u32 s68, s68, s69\n"
  "s_mov_b32 s69, s70\n"
  "s_movk_i32 s70, 6144\n"
  "v_xor_b32 v93, 0x4000, v93\n"
  "v_xor_b32 v94, 0x2000, v94\n"
  "v_xor_b32 v95, 0x2000, v95\n"
  "v_xor_b32 v98, 0x2000, v98\n"
  "s_waitcnt lgkmcnt(0)\n"
  "s_barrier\n"
  "ds_read_b64_tr_b16 v[80:81], v96 offset:0\n"
  "ds_read_b64_tr_b16 v[82:83], v96 offset:32\n"
  "ds_read_b64_tr_b16 v[84:85], v96 offset:2304\n"
  "ds_read_b64_tr_b16 v[86:87], v96 offset:2336\n"
  "ds_read_b64_tr_b16 v[88:89], v96 offset:4608\n"
  "ds_read_b64_tr_b16 v[90:91], v96 offset:4640\n"
  "s_waitcnt lgkmcnt(4)\n"
  "buffer_store_dwordx4 v[80:83], v97, s[48:51], s68 offen offset:0 nt\n"
  "s_waitcnt lgkmcnt(2)\n"
  "buffer_store_dwordx4 v[84:87], v97, s[48:51], s68 offen offset:128 nt\n"
  "s_waitcnt lgkmcnt(0)\n"
  "buffer_store_dwordx4 v[88:91], v97, s[48:51], s68 offen offset:256 nt\n"
  "v_cmp_ngt_f32 vcc, s71, v109\n"
  "s_nop 4\n"
  "s_cmp_lg_u64 vcc, 0\n"
  "s_cselect_b32 %0, 1, 0\n"
  "L_END_%=:\n"
  : "=&s"(bad)
  : "s"((unsigned long long)__builtin_amdgcn_kernarg_segment_ptr()), "s"(wid), "s"(it[0]), "s"(it[1]), "s"(it[2]), "s"(it[3]), "s"(xcd | (side ? 31 - p : p) << 8)
  : "v0", "v1", "v2", "v3", "v4", "v5", "v6", "v7", "v8", "v9", "v10", "v11", "v12", "v13", "v14", "v15", "v16", "v17", "v18", "v19", "v20", "v21", "v22", "v23", "v24", "v25", "v26", "v27", "v28", "v29", "v30", "v31", "v32", "v33", "v34", "v35", "v36", "v37", "v38", "v39", "v40", "v41", "v42", "v43", "v44", "v45", "v46", "v47", "v48", "v49", "v50", "v51", "v52", "v53", "v54", "v55", "v56", "v57", "v58", "v59", "v60", "v61", "v62", "v63", "v64", "v65", "v66", "v67", "v68", "v69", "v70", "v71", "v72", "v73", "v74", "v75", "v76", "v77", "v78", "v79", "v80", "v81", "v82", "v83", "v84", "v85", "v86", "v87", "v88", "v89", "v90", "v91", "v92", "v93", "v94", "v95", "v96", "v97", "v98", "v99", "v100", "v101", "v102", "v103", "v104", "v105", "v106", "v107", "v108", "v109", "v110", "v111", "v112", "v113", "v114", "v115", "v116", "v117", "v118", "v119", "v120", "v121", "v122", "v123", "v124", "v125", "v126", "v127", "v128", "v129", "v130", "v131", "v132", "v133", "v134", "v135", "v136", "v137", "v138", "v139", "v140", "v141", "v142", "v143", "v144", "v145", "v146", "v147", "v148", "v149", "v150", "v151", "v152", "v153", "v154", "v155", "v156", "v157", "v158", "v159", "v160", "v161", "v162", "v163", "v164", "v165", "v166", "v167", "v168", "v169", "v170", "v171", "v172", "v173", "v174", "v175", "v176", "v177", "v178", "v179", "v180", "v181", "v182", "v183", "v184", "v185", "v186", "v187", "v188", "v189", "v190", "v191", "v192", "v193", "v194", "v195", "v196", "v197", "v198", "v199", "v200", "v201", "v202", "v203", "v204", "v205", "v206", "v207", "v208", "v209", "v210", "v211", "v212", "v213", "v214", "v215", "v216", "v217", "v218", "v219", "v220", "v221", "v222", "v223", "v224", "v225", "v226", "v227", "v228", "v229", "v230", "v231", "v232", "v233", "v234", "v235", "v236", "v237", "v238", "v239", "v240", "v241", "v242", "v243", "v244", "v245", "v246", "v247", "v248", "v249", "v250", "v251", "v252", "v253", "v254", "v255", "a0", "a1", "a2", "a3", "a4", "a5", "a6", "a7", "a8", "a9", "a10", "a11", "a12", "a13", "a14", "a15", "a16", "a17", "a18", "a19", "a20", "a21", "a22", "a23", "a24", "a25", "a26", "a27", "a28", "a29", "a30", "a31", "a32", "a33", "a34", "a35", "a36", "a37", "a38", "a39", "a40", "a41", "a42", "a43", "a44", "a45", "a46", "a47", "a48", "a49", "a50", "a51", "a52", "a53", "a54", "a55", "a56", "a57", "a58", "a59", "a60", "a61", "a62", "a63", "a64", "a65", "a66", "a67", "a68", "a69", "a70", "a71", "a72", "a73", "a74", "a75", "a76", "a77", "a78", "a79", "a80", "a81", "a82", "a83", "a84", "a85", "a86", "a87", "a88", "a89", "a90", "a91", "a92", "a93", "a94", "a95", "a96", "a97", "a98", "a99", "a100", "a101", "a102", "a103", "a104", "a105", "a106", "a107", "a108", "a109", "a110", "a111", "a112", "a113", "a114", "a115", "a116", "a117", "a118", "a119", "a120", "a121", "a122", "a123", "a124", "a125", "a126", "a127", "a128", "a129", "a130", "a131", "a132", "a133", "a134", "a135", "a136", "a137", "a138", "a139", "a140", "a141", "a142", "a143", "a144", "a145", "a146", "a147", "a148", "a149", "a150", "a151", "a152", "a153", "a154", "a155", "a156", "a157", "a158", "a159", "a160", "a161", "a162", "a163", "a164", "a165", "a166", "a167", "a168", "a169", "a170", "a171", "a172", "a173", "a174", "a175", "a176", "a177", "a178", "a179", "a180", "a181", "a182", "a183", "a184", "a185", "a186", "a187", "a188", "a189", "a190", "a191", "a192", "a193", "a194", "a195", "a196", "a197", "a198", "a199", "a200", "a201", "a202", "a203", "a204", "a205", "a206", "a207", "a208", "a209", "a210", "a211", "a212", "a213", "a214", "a215", "a216", "a217", "a218", "a219", "a220", "a221", "a222", "a223", "a224", "a225", "a226", "a227", "a228", "a229", "a230", "a231", "a232", "a233", "a234", "a235", "a236", "a237", "a238", "a239", "a240", "a241", "a242", "a243", "a244", "a245", "a246", "a247", "a248", "a249", "a250", "a251", "a252", "a253", "a254", "a255", "s40", "s41", "s42", "s43", "s44", "s45", "s46", "s47", "s48", "s49", "s50", "s51", "s52", "s53", "s54", "s55", "s56", "s57", "s58", "s59", "s60", "s61", "s62", "s63", "s64", "s65", "s66", "s67", "s68", "s69", "s70", "s71", "s72", "s73", "s74", "s75", "s76", "s77", "s78", "s79", "s80", "s81", "s82", "s83", "s84", "s85", "s86", "s87", "s88", "s89", "s90", "s91", "s92", "s93", "s94", "s95", "s96", "s97", "s98", "s99", "vcc", "scc", "memory");
  if (bad) {
    unsigned long long ka = (unsigned long long)__builtin_amdgcn_kernarg_segment_ptr();
    asm volatile("s_waitcnt vmcnt(0)\n\ts_barrier" : "+s"(ka) :: "memory");
    const unsigned short* const* arg = (const unsigned short* const*)ka;
#pragma unroll 1
    for (int i = 0; i < 4; ++i) {
      const unsigned x = i == 0 ? it[0] : i == 1 ? it[1] : i == 2 ? it[2] : it[3];
#pragma unroll 1
      for (int t = wid & 1; t < (int)(x & 255); t += 2) exact_tile(arg[0], arg[1], arg[2], (unsigned short*)arg[3], x >> 16, 16 * (((x >> 8) & 255) + t));
    }
  }
}

extern "C" void kernel_launch(void* const* d_in, const int* in_sizes, int n_in,
                              void* d_out, int out_size, void* d_ws, size_t ws_size,
                              hipStream_t stream) {
  (void)hipFuncSetAttribute((const void*)attn_fwd_g, hipFuncAttributeMaxDynamicSharedMemorySize, LDS_BYTES);
  hipLaunchKernelGGL(attn_fwd_g, dim3(256), dim3(256), LDS_BYTES, stream, (const unsigned short*)d_in[0], (const unsigned short*)d_in[1],
                     (const unsigned short*)d_in[2], (unsigned short*)d_out);
}
```

```cpp
#include <hip/hip_runtime.h>
#include <hip/hip_bf16.h>

typedef __bf16   bf16x8 __attribute__((ext_vector_type(8)));
typedef __bf16   bf16x2 __attribute__((ext_vector_type(2)));
typedef float    f32x4  __attribute__((ext_vector_type(4)));
typedef float    f32x2  __attribute__((ext_vector_type(2)));
typedef unsigned u32x4  __attribute__((ext_vector_type(4)));
typedef unsigned u32x2  __attribute__((ext_vector_type(2)));
typedef unsigned short u16x8 __attribute__((ext_vector_type(8)));

constexpr int SQ = 848, SK = 512, DH = 192, ROWB = 384;
constexpr int LDS_BYTES = 163840;
constexpr float CSC = 0.07216878364870322f * 1.4426950408889634f;

#define DI __device__ __forceinline__
DI unsigned cvtpk(float lo, float hi) { f32x2 v = {lo, hi}; bf16x2 b = __builtin_convertvector(v, bf16x2); return __builtin_bit_cast(unsigned, b); }
DI float xmax(float x) {
  auto r = __builtin_amdgcn_permlane32_swap(__float_as_uint(x), __float_as_uint(x), false, false);
  x = fmaxf(__uint_as_float(r[0]), __uint_as_float(r[1]));
  auto q = __builtin_amdgcn_permlane16_swap(__float_as_uint(x), __float_as_uint(x), false, false);
  return fmaxf(__uint_as_float(q[0]), __uint_as_float(q[1]));
}
DI float xsum(float x) {
  auto r = __builtin_amdgcn_permlane32_swap(__float_as_uint(x), __float_as_uint(x), false, false);
  x = __uint_as_float(r[0]) + __uint_as_float(r[1]);
  auto q = __builtin_amdgcn_permlane16_swap(__float_as_uint(x), __float_as_uint(x), false, false);
  return __uint_as_float(q[0]) + __uint_as_float(q[1]);
}
DI f32x4 score_tile(const unsigned short* krow, const bf16x8 (&qf)[6]) {
  f32x4 acc = {0.f, 0.f, 0.f, 0.f};
#pragma unroll
  for (int s = 0; s < 6; ++s) acc = __builtin_amdgcn_mfma_f32_16x16x32_bf16(*(const bf16x8*)(krow + 32 * s), qf[s], acc, 0, 0, 0);
  return acc;
}
DI void exact_tile(const unsigned short* Q, const unsigned short* K, const unsigned short* V, unsigned short* O, int head, int row0) {
  const unsigned lane = __builtin_amdgcn_mbcnt_hi(~0u, __builtin_amdgcn_mbcnt_lo(~0u, 0u));
  const int c = lane & 15, g4 = lane >> 4;
  const int qrow = min(row0 + c, SQ - 1);
  bf16x8 qf[6];
#pragma unroll
  for (int s = 0; s < 6; ++s) qf[s] = *(const bf16x8*)(Q + ((size_t)head * SQ + qrow) * DH + 32 * s + 8 * g4);
  const unsigned short* Kh = K + ((size_t)head * SK + c) * DH + 8 * g4;
  float m = -__builtin_huge_valf();
#pragma unroll 1
  for (int kb = 0; kb < SK / 16; ++kb) {
    const f32x4 a = score_tile(Kh + (size_t)kb * 16 * DH, qf);
    m = fmaxf(fmaxf(m, fmaxf(a[0], a[1])), fmaxf(a[2], a[3]));
  }
  const float mc = -xmax(m) * CSC;
  float l = 0.f;
  f32x4 o[12];
#pragma unroll
  for (int dt = 0; dt < 12; ++dt) o[dt] = f32x4{0.f, 0.f, 0.f, 0.f};
#pragma unroll 1
  for (int j = 0; j < SK / 32; ++j) {
    u32x4 p;
#pragma unroll
    for (int kt = 0; kt < 2; ++kt) {
      f32x4 a = score_tile(Kh + (size_t)(32 * j + 16 * kt) * DH, qf);
#pragma unroll
      for (int r = 0; r < 4; ++r) { a[r] = __builtin_amdgcn_exp2f(fmaf(a[r], CSC, mc)); l += a[r]; }
      p[2 * kt] = cvtpk(a[0], a[1]); p[2 * kt + 1] = cvtpk(a[2], a[3]);
    }
    const unsigned short* Vj = V + ((size_t)head * SK + 32 * j + 4 * g4) * DH + c;
#pragma unroll
    for (int dt = 0; dt < 12; ++dt) {
      u16x8 vf;
#pragma unroll
      for (int i = 0; i < 8; ++i) vf[i] = Vj[(16 * (i >> 2) + (i & 3)) * DH + 16 * dt];
      o[dt] = __builtin_amdgcn_mfma_f32_16x16x32_bf16(__builtin_bit_cast(bf16x8, vf), __builtin_bit_cast(bf16x8, p), o[dt], 0, 0, 0);
    }
  }
  const float inv = 1.0f / xsum(l);
  if (row0 + c < SQ) {
#pragma unroll
    for (int dt = 0; dt < 12; ++dt)
      *(u32x2*)(O + ((size_t)head * SQ + qrow) * DH + 16 * dt + 4 * g4) = u32x2{cvtpk(o[dt][0] * inv, o[dt][1] * inv), cvtpk(o[dt][2] * inv, o[dt][3] * inv)};
  }
}

__global__ __launch_bounds__(256, 1)
void attn_fwd_g(const unsigned short* __restrict__ Qg, const unsigned short* __restrict__ Kg,
                const unsigned short* __restrict__ Vg, unsigned short* __restrict__ Og) {
  const int wid = __builtin_amdgcn_readfirstlane(threadIdx.x >> 6);
  const int wg = blockIdx.x;
  unsigned it[4];
  const unsigned xcd = wg & 7, p = wg >> 4, side = (wg >> 3) & 1, x = ((p < 8 ? 0x110f0e0d0c0a0908ull : 0x1a19181716141312ull) >> (8 * (p & 7))) & 255;
  it[0] = (8 * p + xcd) << 16 | (side ? x << 8 | (53 - x) : x);
  it[1] = (8 * (16 + 2 * p + side) + xcd) << 16 | 53;
  const unsigned y = (side ? x : 53 - x) + (xcd & 1 ? -(1) : 1);
  it[2] = (8 * (48 + p) + (xcd ^ side)) << 16 | (side ? (53 - y) << 8 | y : y);
  it[3] = 0;
  unsigned bad;
  asm volatile(
  "s_load_dwordx8 s[76:83], %1, 0\n"
  "v_mbcnt_lo_u32_b32 v0, -1, 0\n"
  "v_mbcnt_hi_u32_b32 v0, -1, v0\n"
  "L_PC_%=:\n"
  "s_getpc_b64 s[84:85]\n"
  "s_lshr_b32 s62, %7, 6\n"
  "s_add_u32 s62, s62, %2\n"
  "s_lshl_b32 s62, s62, 13\n"
  "s_add_u32 s84, s84, s62\n"
  "s_addc_u32 s85, s85, 0\n"
  "s_sub_i32 s86, L_END_%=-L_PC_%=, s62\n"
  "s_max_i32 s86, s86, 0\n"
  "s_mov_b32 s87, 0x20000\n"
  "s_mov_b32 m0, 159744\n"
  "v_lshlrev_b32 v1, 7, v0\n"
  "buffer_load_dword v1, s[84:87], 0 offen lds\n"
  "s_mov_b32 s52, %3\n"
  "s_mov_b32 s53, %4\n"
  "s_mov_b32 s54, %5\n"
  "s_mov_b32 s55, %6\n"
  "s_and_b32 s65, %2, 1\n"
  "s_mul_i32 s59, %2, 18432\n"
  "s_add_u32 s59, s59, 86016\n"
  "s_mov_b32 s42, 100663296\n"
  "s_mov_b32 s43, 0x20000\n"
  "s_mov_b32 s47, 0x20000\n"
  "s_lshr_b32 s62, s52, 16\n"
  "s_lshl1_add_u32 s62, s62, s65\n"
  "s_mul_i32 s57, s62, 98304\n"
  "s_and_b32 s56, s52, 0xff\n"
  "s_cmp_lg_u32 s53, 0\n"
  "s_cselect_b32 s62, 1, 0\n"
  "s_sub_u32 s56, s56, s62\n"
  "s_bitcmp1_b32 %7, 3\n"
  "s_cselect_b32 s62, 49152, 0\n"
  "s_add_u32 s57, s57, s62\n"
  "s_lshl_b32 s62, s62, 1\n"
  "s_sub_u32 s86, 6144, s62\n"
  "s_lshr_b32 s62, s52, 16\n"
  "s_mul_i32 s62, s62, 53\n"
  "s_bfe_u32 s63, s52, 0x80008\n"
  "s_add_u32 s62, s62, s63\n"
  "s_mul_i32 s67, s62, 6144\n"
  "s_add_u32 s64, s67, 6144\n"
  "s_lshl_b32 s66, s65, 10\n"
  "s_add_u32 s66, s66, 49152\n"
  "v_lshrrev_b32 v2, 3, v0\n"
  "v_and_b32 v1, 7, v0\n"
  "v_xor_b32 v1, v1, v2\n"
  "v_mul_u32_u24 v112, 0x180, v2\n"
  "v_lshl_add_u32 v112, v1, 4, v112\n"
  "v_add_u32 v113, 0x800, v112\n"
  "s_mul_i32 s62, s65, 3072\n"
  "s_sub_u32 s46, 166723584, s62\n"
  "s_cmp_gt_u32 %2, 1\n"
  "s_waitcnt lgkmcnt(0)\n"
  "s_cbranch_scc1 B_START_%=\n"
  "A_START_%=:\n"
  "s_mov_b64 s[40:41], s[78:79]\n"
  "s_add_u32 m0, s59, 0\n"
  "s_nop 0\n"
  "buffer_load_dwordx4 v112, s[40:43], s57 offen offset:0  lds\n"
  "buffer_load_dwordx4 v113, s[40:43], s57 offen offset:1024  lds\n"
  "s_add_u32 m0, s59, 1920\n"
  "s_nop 0\n"
  "buffer_load_dwordx4 v112, s[40:43], s57 offen offset:128  lds\n"
  "buffer_load_dwordx4 v113, s[40:43], s57 offen offset:1152  lds\n"
  "s_add_u32 m0, s59, 3840\n"
  "s_nop 0\n"
  "buffer_load_dwordx4 v112, s[40:43], s57 offen offset:256  lds\n"
  "buffer_load_dwordx4 v113, s[40:43], s57 offen offset:1280  lds\n"
  "s_add_u32 s57, s57, 6144\n"
  "s_add_u32 m0, s59, 5888\n"
  "s_nop 0\n"
  "buffer_load_dwordx4 v112, s[40:43], s57 offen offset:256  lds\n"
  "buffer_load_dwordx4 v113, s[40:43], s57 offen offset:1280  lds\n"
  "s_add_u32 m0, s59, 8064\n"
  "s_nop 0\n"
  "buffer_load_dwordx4 v112, s[40:43], s57 offen offset:128  lds\n"
  "buffer_load_dwordx4 v113, s[40:43], s57 offen offset:1152  lds\n"
  "s_add_u32 m0, s59, 10240\n"
  "s_nop 0\n"
  "buffer_load_dwordx4 v112, s[40:43], s57 offen offset:0  lds\n"
  "buffer_load_dwordx4 v113, s[40:43], s57 offen offset:1024  lds\n"
  "s_add_u32 s57, s57, 6144\n"
  "s_add_u32 m0, s59, 12288\n"
  "s_nop 0\n"
  "buffer_load_dwordx4 v112, s[40:43], s57 offen offset:0  lds\n"
  "buffer_load_dwordx4 v113, s[40:43], s57 offen offset:1024  lds\n"
  "s_add_u32 m0, s59, 14208\n"
  "s_nop 0\n"
  "buffer_load_dwordx4 v112, s[40:43], s57 offen offset:128  lds\n"
  "buffer_load_dwordx4 v113, s[40:43], s57 offen offset:1152  lds\n"
  "s_add_u32 m0, s59, 16128\n"
  "s_nop 0\n"
  "buffer_load_dwordx4 v112, s[40:43], s57 offen offset:256  lds\n"
  "buffer_load_dwordx4 v113, s[40:43], s57 offen offset:1280  lds\n"
  "s_add_u32 s57, s57, 6144\n"
  "v_and_b32 v2, 7, v0\n"
  "v_lshrrev_b32 v83, 4, v0\n"
  "v_xor_b32 v83, v83, v2\n"
  "v_lshlrev_b32 v83, 4, v83\n"
  "v_lshl_add_u32 v83, v2, 7, v83\n"
  "v_bfe_u32 v1, v0, 3, 1\n"
  "v_lshl_add_u32 v83, v1, 10, v83\n"
  "v_xor_b32 v84, 64, v83\n"
  "v_add_u32 v87, s59, v83\n"
  "v_add_u32 v88, s59, v84\n"
  "s_mov_b32 s66, 49152\n"
  "v_add_u32 v81, s66, v83\n"
  "v_add_u32 v82, s66, v84\n"
  "s_lshl_b32 s62, s65, 13\n"
  "v_lshlrev_b32 v80, 4, v0\n"
  "v_add_u32 v80, s62, v80\n"
  "s_mov_b32 s60, 0x3dd53b94\n"
  "s_mov_b32 s61, 0xc0800000\n"
  "s_waitcnt vmcnt(16)\n"
  "ds_read_b128 a[0:3], v87 offset:0\n"
  "ds_read_b128 a[4:7], v88 offset:0\n"
  "s_waitcnt vmcnt(14)\n"
  "ds_read_b128 a[8:11], v87 offset:2048\n"
  "ds_read_b128 a[12:15], v88 offset:2048\n"
  "s_add_u32 m0, s59, -256\n"
  "s_waitcnt lgkmcnt(2)\n"
  "buffer_load_dwordx4 v112, s[40:43], s57 offen offset:256  lds\n"
  "buffer_load_dwordx4 v113, s[40:43], s57 offen offset:1280  lds\n"
  "s_waitcnt vmcnt(14)\n"
  "ds_read_b128 a[16:19], v87 offset:4096\n"
  "ds_read_b128 a[20:23], v88 offset:4096\n"
  "s_add_u32 m0, s59, 1920\n"
  "s_waitcnt lgkmcnt(2)\n"
  "buffer_load_dwordx4 v112, s[40:43], s57 offen offset:128  lds\n"
  "buffer_load_dwordx4 v113, s[40:43], s57 offen offset:1152  lds\n"
  "s_waitcnt vmcnt(14)\n"
  "ds_read_b128 a[40:43], v87 offset:6144\n"
  "ds_read_b128 a[44:47], v88 offset:6144\n"
  "s_add_u32 m0, s59, 4096\n"
  "s_waitcnt lgkmcnt(2)\n"
  "buffer_load_dwordx4 v112, s[40:43], s57 offen offset:0  lds\n"
  "buffer_load_dwordx4 v113, s[40:43], s57 offen offset:1024  lds\n"
  "s_add_u32 s57, s57, 6144\n"
  "s_waitcnt vmcnt(14)\n"
  "ds_read_b128 a[32:35], v87 offset:8192\n"
  "ds_read_b128 a[36:39], v88 offset:8192\n"
  "s_add_u32 m0, s59, 6144\n"
  "s_waitcnt lgkmcnt(2)\n"
  "buffer_load_dwordx4 v112, s[40:43], s57 offen offset:0  lds\n"
  "buffer_load_dwordx4 v113, s[40:43], s57 offen offset:1024  lds\n"
  "s_waitcnt vmcnt(14)\n"
  "ds_read_b128 a[24:27], v87 offset:10240\n"
  "ds_read_b128 a[28:31], v88 offset:10240\n"
  "s_add_u32 m0, s59, 8064\n"
  "s_waitcnt lgkmcnt(2)\n"
  "buffer_load_dwordx4 v112, s[40:43], s57 offen offset:128  lds\n"
  "buffer_load_dwordx4 v113, s[40:43], s57 offen offset:1152  lds\n"
  "s_waitcnt vmcnt(14)\n"
  "ds_read_b128 a[48:51], v87 offset:12288\n"
  "ds_read_b128 a[52:55], v88 offset:12288\n"
  "s_add_u32 m0, s59, 9984\n"
  "s_waitcnt lgkmcnt(2)\n"
  "buffer_load_dwordx4 v112, s[40:43], s57 offen offset:256  lds\n"
  "buffer_load_dwordx4 v113, s[40:43], s57 offen offset:1280  lds\n"
  "s_add_u32 s57, s57, 6144\n"
  "s_waitcnt vmcnt(14)\n"
  "ds_read_b128 a[56:59], v87 offset:14336\n"
  "ds_read_b128 a[60:63], v88 offset:14336\n"
  "s_add_u32 m0, s59, 12032\n"
  "s_waitcnt lgkmcnt(2)\n"
  "buffer_load_dwordx4 v112, s[40:43], s57 offen offset:256  lds\n"
  "buffer_load_dwordx4 v113, s[40:43], s57 offen offset:1280  lds\n"
  "s_waitcnt vmcnt(14)\n"
  "ds_read_b128 a[64:67], v87 offset:16384\n"
  "ds_read_b128 a[68:71], v88 offset:16384\n"
  "s_add_u32 m0, s59, 14208\n"
  "s_waitcnt lgkmcnt(2)\n"
  "buffer_load_dwordx4 v112, s[40:43], s57 offen offset:128  lds\n"
  "buffer_load_dwordx4 v113, s[40:43], s57 offen offset:1152  lds\n"
  "s_waitcnt vmcnt(14)\n"
  "ds_read_b128 a[88:91], v87 offset:0\n"
  "ds_read_b128 a[92:95], v88 offset:0\n"
  "s_add_u32 m0, s59, 16384\n"
  "s_barrier\n"
  "ds_read_b128 v[32:35], v81 offset:0\n"
  "ds_read_b128 v[36:39], v82 offset:0\n"
  "ds_read_b128 v[40:43], v81 offset:2048\n"
  "ds_read_b128 v[44:47], v82 offset:2048\n"
  "ds_read_b128 v[48:51], v81 offset:4096\n"
  "ds_read_b128 v[52:55], v82 offset:4096\n"
  "s_waitcnt lgkmcnt(0)\n"
  "v_mfma_f32_16x16x32_bf16 v[0:3], a[0:3], v[32:35], 0\n"
  "s_add_u32 s66, s66, 6144\n"
  "s_cmp_eq_u32 s66, 86016\n"
  "s_cselect_b32 s66, 49152, s66\n"
  "buffer_load_dwordx4 v112, s[40:43], s57 offen offset:0  lds\n"
  "buffer_load_dwordx4 v113, s[40:43], s57 offen offset:1024  lds\n"
  "s_add_u32 s57, s57, 6144\n"
  "v_mfma_f32_16x16x32_bf16 v[0:3], a[4:7], v[36:39], v[0:3]\n"
  "s_waitcnt vmcnt(14)\n"
  "ds_read_b128 a[80:83], v87 offset:2048\n"
  "ds_read_b128 a[84:87], v88 offset:2048\n"
  "s_add_u32 m0, s59, 0\n"
  "v_mfma_f32_16x16x32_bf16 v[0:3], a[8:11], v[40:43], v[0:3]\n"
  "buffer_load_dwordx4 v112, s[40:43], s57 offen offset:0  lds\n"
  "buffer_load_dwordx4 v113, s[40:43], s57 offen offset:1024  lds\n"
  "v_mfma_f32_16x16x32_bf16 v[0:3], a[12:15], v[44:47], v[0:3]\n"
  "s_waitcnt vmcnt(14)\n"
  "ds_read_b128 a[72:75], v87 offset:4096\n"
  "ds_read_b128 a[76:79], v88 offset:4096\n"
  "s_add_u32 m0, s59, 1920\n"
  "v_mfma_f32_16x16x32_bf16 v[0:3], a[16:19], v[48:51], v[0:3]\n"
  "s_waitcnt lgkmcnt(2)\n"
  "buffer_load_dwordx4 v112, s[40:43], s57 offen offset:128  lds\n"
  "buffer_load_dwordx4 v113, s[40:43], s57 offen offset:1152  lds\n"
  "v_mfma_f32_16x16x32_bf16 v[0:3], a[20:23], v[52:55], v[0:3]\n"
  "s_waitcnt vmcnt(14)\n"
  "ds_read_b128 a[96:99], v87 offset:6144\n"
  "ds_read_b128 a[100:103], v88 offset:6144\n"
  "s_add_u32 m0, s59, 3840\n"
  "v_mfma_f32_16x16x32_bf16 v[4:7], a[44:47], v[52:55], 0\n"
  "s_waitcnt lgkmcnt(2)\n"
  "buffer_load_dwordx4 v112, s[40:43], s57 offen offset:256  lds\n"
  "buffer_load_dwordx4 v113, s[40:43], s57 offen offset:1280  lds\n"
  "s_add_u32 s57, s57, 6144\n"
  "v_mfma_f32_16x16x32_bf16 v[4:7], a[40:43], v[48:51], v[4:7]\n"
  "s_waitcnt vmcnt(14)\n"
  "ds_read_b128 a[104:107], v87 offset:8192\n"
  "ds_read_b128 a[108:111], v88 offset:8192\n"
  "s_add_u32 m0, s59, 5888\n"
  "v_mfma_f32_16x16x32_bf16 v[4:7], a[36:39], v[44:47], v[4:7]\n"
  "s_waitcnt lgkmcnt(2)\n"
  "buffer_load_dwordx4 v112, s[40:43], s57 offen offset:256  lds\n"
  "buffer_load_dwordx4 v113, s[40:43], s57 offen offset:1280  lds\n"
  "v_mfma_f32_16x16x32_bf16 v[4:7], a[32:35], v[40:43], v[4:7]\n"
  "s_waitcnt vmcnt(14)\n"
  "ds_read_b128 a[112:115], v87 offset:10240\n"
  "ds_read_b128 a[116:119], v88 offset:10240\n"
  "s_add_u32 m0, s59, 8064\n"
  "v_mfma_f32_16x16x32_bf16 v[4:7], a[28:31], v[36:39], v[4:7]\n"
  "s_waitcnt lgkmcnt(2)\n"
  "buffer_load_dwordx4 v112, s[40:43], s57 offen offset:128  lds\n"
  "buffer_load_dwordx4 v113, s[40:43], s57 offen offset:1152  lds\n"
  "v_mfma_f32_16x16x32_bf16 v[4:7], a[24:27], v[32:35], v[4:7]\n"
  "s_waitcnt vmcnt(14)\n"
  "ds_read_b128 a[136:139], v87 offset:12288\n"
  "ds_read_b128 a[140:143], v88 offset:12288\n"
  "s_add_u32 m0, s59, 10240\n"
  "v_add_u32 v81, s66, v83\n"
  "v_add_u32 v82, s66, v84\n"
  "v_mfma_f32_16x16x32_bf16 v[8:11], a[48:51], v[32:35], 0\n"
  "s_waitcnt lgkmcnt(2)\n"
  "buffer_load_dwordx4 v112, s[40:43], s57 offen offset:0  lds\n"
  "buffer_load_dwordx4 v113, s[40:43], s57 offen offset:1024  lds\n"
  "s_add_u32 s57, s57, s86\n"
  "v_mfma_f32_16x16x32_bf16 v[8:11], a[52:55], v[36:39], v[8:11]\n"
  "s_waitcnt vmcnt(14)\n"
  "ds_read_b128 a[128:131], v87 offset:14336\n"
  "ds_read_b128 a[132:135], v88 offset:14336\n"
  "s_add_u32 m0, s59, 12288\n"
  "v_mfma_f32_16x16x32_bf16 v[8:11], a[56:59], v[40:43], v[8:11]\n"
  "s_waitcnt lgkmcnt(2)\n"
  "buffer_load_dwordx4 v112, s[40:43], s57 offen offset:0  lds\n"
  "buffer_load_dwordx4 v113, s[40:43], s57 offen offset:1024  lds\n"
  "v_mfma_f32_16x16x32_bf16 v[8:11], a[60:63], v[44:47], v[8:11]\n"
  "s_waitcnt vmcnt(14)\n"
  "ds_read_b128 a[120:123], v87 offset:16384\n"
  "ds_read_b128 a[124:127], v88 offset:16384\n"
  "s_add_u32 m0, s59, 14208\n"
  "v_mfma_f32_16x16x32_bf16 v[8:11], a[64:67], v[48:51], v[8:11]\n"
  "s_waitcnt lgkmcnt(2)\n"
  "buffer_load_dwordx4 v112, s[40:43], s57 offen offset:128  lds\n"
  "buffer_load_dwordx4 v113, s[40:43], s57 offen offset:1152  lds\n"
  "v_mfma_f32_16x16x32_bf16 v[8:11], a[68:71], v[52:55], v[8:11]\n"
  "s_waitcnt vmcnt(14)\n"
  "ds_read_b128 a[144:147], v87 offset:0\n"
  "ds_read_b128 a[148:151], v88 offset:0\n"
  "s_add_u32 m0, s59, 16128\n"
  "v_pk_fma_f32 v[0:1], v[0:1], s[60:61], s[60:61] op_sel:[0,0,1] op_sel_hi:[1,0,1]\n"
  "v_pk_fma_f32 v[2:3], v[2:3], s[60:61], s[60:61] op_sel:[0,0,1] op_sel_hi:[1,0,1]\n"
  "v_pk_fma_f32 v[4:5], v[4:5], s[60:61], s[60:61] op_sel:[0,0,1] op_sel_hi:[1,0,1]\n"
  "v_pk_fma_f32 v[6:7], v[6:7], s[60:61], s[60:61] op_sel:[0,0,1] op_sel_hi:[1,0,1]\n"
  "v_mfma_f32_16x16x32_bf16 v[12:15], a[92:95], v[52:55], 0\n"
  "s_waitcnt lgkmcnt(2)\n"
  "buffer_load_dwordx4 v112, s[40:43], s57 offen offset:256  lds\n"
  "buffer_load_dwordx4 v113, s[40:43], s57 offen offset:1280  lds\n"
  "s_add_u32 s57, s57, 6144\n"
  "v_exp_f32 v0, v0\n"
  "v_mfma_f32_16x16x32_bf16 v[12:15], a[88:91], v[48:51], v[12:15]\n"
  "s_waitcnt vmcnt(14)\n"
  "ds_read_b128 a[152:155], v87 offset:2048\n"
  "ds_read_b128 a[156:159], v88 offset:2048\n"
  "s_add_u32 m0, s59, -256\n"
  "v_exp_f32 v1, v1\n"
  "v_mfma_f32_16x16x32_bf16 v[12:15], a[84:87], v[44:47], v[12:15]\n"
  "s_waitcnt lgkmcnt(2)\n"
  "buffer_load_dwordx4 v112, s[40:43], s57 offen offset:256  lds\n"
  "buffer_load_dwordx4 v113, s[40:43], s57 offen offset:1280  lds\n"
  "v_exp_f32 v2, v2\n"
  "v_mfma_f32_16x16x32_bf16 v[12:15], a[80:83], v[40:43], v[12:15]\n"
  "s_waitcnt vmcnt(14)\n"
  "ds_read_b128 a[160:163], v87 offset:4096\n"
  "ds_read_b128 a[164:167], v88 offset:4096\n"
  "s_add_u32 m0, s59, 1920\n"
  "v_exp_f32 v3, v3\n"
  "v_mfma_f32_16x16x32_bf16 v[12:15], a[76:79], v[36:39], v[12:15]\n"
  "s_waitcnt lgkmcnt(2)\n"
  "buffer_load_dwordx4 v112, s[40:43], s57 offen offset:128  lds\n"
  "buffer_load_dwordx4 v113, s[40:43], s57 offen offset:1152  lds\n"
  "v_cvt_pk_bf16_f32 v0, v0, v1\n"
  "v_cvt_pk_bf16_f32 v1, v2, v3\n"
  "v_mfma_f32_16x16x32_bf16 v[12:15], a[72:75], v[32:35], v[12:15]\n"
  "s_waitcnt vmcnt(14)\n"
  "ds_read_b128 a[184:187], v87 offset:6144\n"
  "ds_read_b128 a[188:191], v88 offset:6144\n"
  "s_add_u32 m0, s59, 4096\n"
  "v_mfma_f32_16x16x32_bf16 v[16:19], a[96:99], v[32:35], 0\n"
  "s_waitcnt lgkmcnt(2)\n"
  "buffer_load_dwordx4 v112, s[40:43], s57 offen offset:0  lds\n"
  "buffer_load_dwordx4 v113, s[40:43], s57 offen offset:1024  lds\n"
  "s_add_u32 s57, s57, 6144\n"
  "v_exp_f32 v4, v4\n"
  "v_mfma_f32_16x16x32_bf16 v[16:19], a[100:103], v[36:39], v[16:19]\n"
  "s_waitcnt vmcnt(14)\n"
  "ds_read_b128 a[176:179], v87 offset:8192\n"
  "ds_read_b128 a[180:183], v88 offset:8192\n"
  "s_add_u32 m0, s59, 6144\n"
  "v_exp_f32 v5, v5\n"
  "v_mfma_f32_16x16x32_bf16 v[16:19], a[104:107], v[40:43], v[16:19]\n"
  "s_waitcnt lgkmcnt(2)\n"
  "buffer_load_dwordx4 v112, s[40:43], s57 offen offset:0  lds\n"
  "buffer_load_dwordx4 v113, s[40:43], s57 offen offset:1024  lds\n"
  "v_exp_f32 v6, v6\n"
  "v_mfma_f32_16x16x32_bf16 v[16:19], a[108:111], v[44:47], v[16:19]\n"
  "s_waitcnt vmcnt(14)\n"
  "ds_read_b128 a[168:171], v87 offset:10240\n"
  "ds_read_b128 a[172:175], v88 offset:10240\n"
  "s_add_u32 m0, s59, 8064\n"
  "v_exp_f32 v7, v7\n"
  "v_mfma_f32_16x16x32_bf16 v[16:19], a[112:115], v[48:51], v[16:19]\n"
  "s_waitcnt lgkmcnt(2)\n"
  "buffer_load_dwordx4 v112, s[40:43], s57 offen offset:128  lds\n"
  "buffer_load_dwordx4 v113, s[40:43], s57 offen offset:1152  lds\n"
  "v_cvt_pk_bf16_f32 v2, v4, v5\n"
  "v_cvt_pk_bf16_f32 v3, v6, v7\n"
  "v_mfma_f32_16x16x32_bf16 v[16:19], a[116:119], v[52:55], v[16:19]\n"
  "s_waitcnt vmcnt(14)\n"
  "ds_read_b128 a[192:195], v87 offset:12288\n"
  "ds_read_b128 a[196:199], v88 offset:12288\n"
  "s_add_u32 m0, s59, 9984\n"
  "ds_write_b128 v80, v[0:3] offset:0\n"
  "v_pk_fma_f32 v[8:9], v[8:9], s[60:61], s[60:61] op_sel:[0,0,1] op_sel_hi:[1,0,1]\n"
  "v_pk_fma_f32 v[10:11], v[10:11], s[60:61], s[60:61] op_sel:[0,0,1] op_sel_hi:[1,0,1]\n"
  "v_pk_fma_f32 v[12:13], v[12:13], s[60:61], s[60:61] op_sel:[0,0,1] op_sel_hi:[1,0,1]\n"
  "v_pk_fma_f32 v[14:15], v[14:15], s[60:61], s[60:61] op_sel:[0,0,1] op_sel_hi:[1,0,1]\n"
  "v_mfma_f32_16x16x32_bf16 v[20:23], a[140:143], v[52:55], 0\n"
  "s_waitcnt lgkmcnt(3)\n"
  "buffer_load_dwordx4 v112, s[40:43], s57 offen offset:256  lds\n"
  "buffer_load_dwordx4 v113, s[40:43], s57 offen offset:1280  lds\n"
  "s_add_u32 s57, s57, 6144\n"
  "v_exp_f32 v8, v8\n"
  "v_mfma_f32_16x16x32_bf16 v[20:23], a[136:139], v[48:51], v[20:23]\n"
  "s_waitcnt vmcnt(14)\n"
  "ds_read_b128 a[200:203], v87 offset:14336\n"
  "ds_read_b128 a[204:207], v88 offset:14336\n"
  "s_add_u32 m0, s59, 12032\n"
  "v_exp_f32 v9, v9\n"
  "v_mfma_f32_16x16x32_bf16 v[20:23], a[132:135], v[44:47], v[20:23]\n"
  "s_waitcnt lgkmcnt(3)\n"
  "buffer_load_dwordx4 v112, s[40:43], s57 offen offset:256  lds\n"
  "buffer_load_dwordx4 v113, s[40:43], s57 offen offset:1280  lds\n"
  "v_exp_f32 v10, v10\n"
  "v_mfma_f32_16x16x32_bf16 v[20:23], a[128:131], v[40:43], v[20:23]\n"
  "s_waitcnt vmcnt(14)\n"
  "ds_read_b128 a[208:211], v87 offset:16384\n"
  "ds_read_b128 a[212:215], v88 offset:16384\n"
  "s_add_u32 m0, s59, 14208\n"
  "v_exp_f32 v11, v11\n"
  "v_mfma_f32_16x16x32_bf16 v[20:23], a[124:127], v[36:39], v[20:23]\n"
  "s_waitcnt lgkmcnt(2)\n"
  "buffer_load_dwordx4 v112, s[40:43], s57 offen offset:128  lds\n"
  "buffer_load_dwordx4 v113, s[40:43], s57 offen offset:1152  lds\n"
  "v_cvt_pk_bf16_f32 v8, v8, v9\n"
  "v_cvt_pk_bf16_f32 v9, v10, v11\n"
  "v_mfma_f32_16x16x32_bf16 v[20:23], a[120:123], v[32:35], v[20:23]\n"
  "s_waitcnt vmcnt(14)\n"
  "ds_read_b128 a[232:235], v87 offset:0\n"
  "ds_read_b128 a[236:239], v88 offset:0\n"
  "s_add_u32 m0, s59, 16384\n"
  "ds_read_b128 v[56:59], v81 offset:0\n"
  "ds_read_b128 v[60:63], v82 offset:0\n"
  "v_mfma_f32_16x16x32_bf16 v[24:27], a[144:147], v[32:35], 0\n"
  "s_waitcnt lgkmcnt(4)\n"
  "buffer_load_dwordx4 v112, s[40:43], s57 offen offset:0  lds\n"
  "buffer_load_dwordx4 v113, s[40:43], s57 offen offset:1024  lds\n"
  "s_add_u32 s57, s57, 6144\n"
  "v_exp_f32 v12, v12\n"
  "v_mfma_f32_16x16x32_bf16 v[24:27], a[148:151], v[36:39], v[24:27]\n"
  "s_waitcnt vmcnt(14)\n"
  "ds_read_b128 a[224:227], v87 offset:2048\n"
  "ds_read_b128 a[228:231], v88 offset:2048\n"
  "s_add_u32 m0, s59, 0\n"
  "v_exp_f32 v13, v13\n"
  "v_mfma_f32_16x16x32_bf16 v[24:27], a[152:155], v[40:43], v[24:27]\n"
  "s_waitcnt lgkmcnt(4)\n"
  "buffer_load_dwordx4 v112, s[40:43], s57 offen offset:0  lds\n"
  "buffer_load_dwordx4 v113, s[40:43], s57 offen offset:1024  lds\n"
  "v_exp_f32 v14, v14\n"
  "v_mfma_f32_16x16x32_bf16 v[24:27], a[156:159], v[44:47], v[24:27]\n"
  "s_waitcnt vmcnt(14)\n"
  "ds_read_b128 a[216:219], v87 offset:4096\n"
  "ds_read_b128 a[220:223], v88 offset:4096\n"
  "s_add_u32 m0, s59, 1920\n"
  "v_exp_f32 v15, v15\n"
  "v_mfma_f32_16x16x32_bf16 v[24:27], a[160:163], v[48:51], v[24:27]\n"
  "s_waitcnt lgkmcnt(2)\n"
  "buffer_load_dwordx4 v112, s[40:43], s57 offen offset:128  lds\n"
  "buffer_load_dwordx4 v113, s[40:43], s57 offen offset:1152  lds\n"
  "v_cvt_pk_bf16_f32 v10, v12, v13\n"
  "v_cvt_pk_bf16_f32 v11, v14, v15\n"
  "v_mfma_f32_16x16x32_bf16 v[24:27], a[164:167], v[52:55], v[24:27]\n"
  "s_waitcnt vmcnt(14)\n"
  "ds_read_b128 a[240:243], v87 offset:6144\n"
  "ds_read_b128 a[244:247], v88 offset:6144\n"
  "s_add_u32 m0, s59, 3840\n"
  "ds_write_b128 v80, v[8:11] offset:1024\n"
  "v_pk_fma_f32 v[16:17], v[16:17], s[60:61], s[60:61] op_sel:[0,0,1] op_sel_hi:[1,0,1]\n"
  "v_pk_fma_f32 v[18:19], v[18:19], s[60:61], s[60:61] op_sel:[0,0,1] op_sel_hi:[1,0,1]\n"
  "v_pk_fma_f32 v[20:21], v[20:21], s[60:61], s[60:61] op_sel:[0,0,1] op_sel_hi:[1,0,1]\n"
  "v_pk_fma_f32 v[22:23], v[22:23], s[60:61], s[60:61] op_sel:[0,0,1] op_sel_hi:[1,0,1]\n"
  "v_mfma_f32_16x16x32_bf16 v[28:31], a[188:191], v[52:55], 0\n"
  "s_waitcnt lgkmcnt(3)\n"
  "buffer_load_dwordx4 v112, s[40:43], s57 offen offset:256  lds\n"
  "buffer_load_dwordx4 v113, s[40:43], s57 offen offset:1280  lds\n"
  "s_add_u32 s57, s57, 6144\n"
  "v_exp_f32 v16, v16\n"
  "v_mfma_f32_16x16x32_bf16 v[28:31], a[184:187], v[48:51], v[28:31]\n"
  "s_waitcnt vmcnt(14)\n"
  "ds_read_b128 a[248:251], v87 offset:8192\n"
  "ds_read_b128 a[252:255], v88 offset:8192\n"
  "s_add_u32 m0, s59, 5888\n"
  "v_exp_f32 v17, v17\n"
  "v_mfma_f32_16x16x32_bf16 v[28:31], a[180:183], v[44:47], v[28:31]\n"
  "s_waitcnt lgkmcnt(3)\n"
  "buffer_load_dwordx4 v112, s[40:43], s57 offen offset:256  lds\n"
  "buffer_load_dwordx4 v113, s[40:43], s57 offen offset:1280  lds\n"
  "v_exp_f32 v18, v18\n"
  "v_mfma_f32_16x16x32_bf16 v[28:31], a[176:179], v[40:43], v[28:31]\n"
  "s_waitcnt vmcnt(14)\n"
  "ds_read_b128 v[128:131], v87 offset:10240\n"
  "ds_read_b128 v[132:135], v88 offset:10240\n"
  "s_add_u32 m0, s59, 8064\n"
  "v_exp_f32 v19, v19\n"
  "v_mfma_f32_16x16x32_bf16 v[28:31], a[172:175], v[36:39], v[28:31]\n"
  "s_waitcnt lgkmcnt(2)\n"
  "buffer_load_dwordx4 v112, s[40:43], s57 offen offset:128  lds\n"
  "buffer_load_dwordx4 v113, s[40:43], s57 offen offset:1152  lds\n"
  "v_cvt_pk_bf16_f32 v16, v16, v17\n"
  "v_cvt_pk_bf16_f32 v17, v18, v19\n"
  "v_mfma_f32_16x16x32_bf16 v[28:31], a[168:171], v[32:35], v[28:31]\n"
  "s_waitcnt vmcnt(14)\n"
  "ds_read_b128 v[152:155], v87 offset:12288\n"
  "ds_read_b128 v[156:159], v88 offset:12288\n"
  "s_add_u32 m0, s59, 10240\n"
  "ds_read_b128 v[64:67], v81 offset:2048\n"
  "ds_read_b128 v[68:71], v82 offset:2048\n"
  "v_mfma_f32_16x16x32_bf16 v[0:3], a[192:195], v[32:35], 0\n"
  "s_waitcnt lgkmcnt(4)\n"
  "buffer_load_dwordx4 v112, s[40:43], s57 offen offset:0  lds\n"
  "buffer_load_dwordx4 v113, s[40:43], s57 offen offset:1024  lds\n"
  "s_add_u32 s57, s57, 6144\n"
  "v_exp_f32 v20, v20\n"
  "v_mfma_f32_16x16x32_bf16 v[0:3], a[196:199], v[36:39], v[0:3]\n"
  "s_waitcnt vmcnt(14)\n"
  "ds_read_b128 v[144:147], v87 offset:14336\n"
  "ds_read_b128 v[148:151], v88 offset:14336\n"
  "s_add_u32 m0, s59, 12288\n"
  "v_exp_f32 v21, v21\n"
  "v_mfma_f32_16x16x32_bf16 v[0:3], a[200:203], v[40:43], v[0:3]\n"
  "s_waitcnt lgkmcnt(4)\n"
  "buffer_load_dwordx4 v112, s[40:43], s57 offen offset:0  lds\n"
  "buffer_load_dwordx4 v113, s[40:43], s57 offen offset:1024  lds\n"
  "v_exp_f32 v22, v22\n"
  "v_mfma_f32_16x16x32_bf16 v[0:3], a[204:207], v[44:47], v[0:3]\n"
  "s_waitcnt vmcnt(14)\n"
  "ds_read_b128 v[136:139], v87 offset:16384\n"
  "ds_read_b128 v[140:143], v88 offset:16384\n"
  "s_add_u32 m0, s59, 14208\n"
  "v_exp_f32 v23, v23\n"
  "v_mfma_f32_16x16x32_bf16 v[0:3], a[208:211], v[48:51], v[0:3]\n"
  "s_waitcnt lgkmcnt(2)\n"
  "buffer_load_dwordx4 v112, s[40:43], s57 offen offset:128  lds\n"
  "buffer_load_dwordx4 v113, s[40:43], s57 offen offset:1152  lds\n"
  "v_cvt_pk_bf16_f32 v18, v20, v21\n"
  "v_cvt_pk_bf16_f32 v19, v22, v23\n"
  "v_mfma_f32_16x16x32_bf16 v[0:3], a[212:215], v[52:55], v[0:3]\n"
  "s_waitcnt vmcnt(14)\n"
  "ds_read_b128 v[160:163], v87 offset:0\n"
  "ds_read_b128 v[164:167], v88 offset:0\n"
  "s_add_u32 m0, s59, 16128\n"
  "ds_write_b128 v80, v[16:19] offset:2048\n"
  "v_pk_fma_f32 v[24:25], v[24:25], s[60:61], s[60:61] op_sel:[0,0,1] op_sel_hi:[1,0,1]\n"
  "v_pk_fma_f32 v[26:27], v[26:27], s[60:61], s[60:61] op_sel:[0,0,1] op_sel_hi:[1,0,1]\n"
  "v_pk_fma_f32 v[28:29], v[28:29], s[60:61], s[60:61] op_sel:[0,0,1] op_sel_hi:[1,0,1]\n"
  "v_pk_fma_f32 v[30:31], v[30:31], s[60:61], s[60:61] op_sel:[0,0,1] op_sel_hi:[1,0,1]\n"
  "v_mfma_f32_16x16x32_bf16 v[4:7], a[236:239], v[52:55], 0\n"
  "s_waitcnt lgkmcnt(3)\n"
  "buffer_load_dwordx4 v112, s[40:43], s57 offen offset:256  lds\n"
  "buffer_load_dwordx4 v113, s[40:43], s57 offen offset:1280  lds\n"
  "s_add_u32 s57, s57, 6144\n"
  "v_exp_f32 v24, v24\n"
  "v_mfma_f32_16x16x32_bf16 v[4:7], a[232:235], v[48:51], v[4:7]\n"
  "s_waitcnt vmcnt(14)\n"
  "ds_read_b128 v[168:171], v87 offset:2048\n"
  "ds_read_b128 v[172:175], v88 offset:2048\n"
  "s_add_u32 m0, s59, -256\n"
  "v_exp_f32 v25, v25\n"
  "v_mfma_f32_16x16x32_bf16 v[4:7], a[228:231], v[44:47], v[4:7]\n"
  "s_waitcnt lgkmcnt(3)\n"
  "buffer_load_dwordx4 v112, s[40:43], s57 offen offset:256  lds\n"
  "buffer_load_dwordx4 v113, s[40:43], s57 offen offset:1280  lds\n"
  "v_exp_f32 v26, v26\n"
  "v_mfma_f32_16x16x32_bf16 v[4:7], a[224:227], v[40:43], v[4:7]\n"
  "s_waitcnt vmcnt(14)\n"
  "ds_read_b128 v[176:179], v87 offset:4096\n"
  "ds_read_b128 v[180:183], v88 offset:4096\n"
  "s_add_u32 m0, s59, 1920\n"
  "v_exp_f32 v27, v27\n"
  "v_mfma_f32_16x16x32_bf16 v[4:7], a[220:223], v[36:39], v[4:7]\n"
  "s_waitcnt lgkmcnt(2)\n"
  "buffer_load_dwordx4 v112, s[40:43], s57 offen offset:128  lds\n"
  "buffer_load_dwordx4 v113, s[40:43], s57 offen offset:1152  lds\n"
  "v_cvt_pk_bf16_f32 v24, v24, v25\n"
  "v_cvt_pk_bf16_f32 v25, v26, v27\n"
  "v_mfma_f32_16x16x32_bf16 v[4:7], a[216:219], v[32:35], v[4:7]\n"
  "s_waitcnt vmcnt(14)\n"
  "ds_read_b128 v[200:203], v87 offset:6144\n"
  "ds_read_b128 v[204:207], v88 offset:6144\n"
  "s_add_u32 m0, s59, 4096\n"
  "ds_read_b128 v[72:75], v81 offset:4096\n"
  "ds_read_b128 v[76:79], v82 offset:4096\n"
  "v_mfma_f32_16x16x32_bf16 v[8:11], a[240:243], v[32:35], 0\n"
  "s_waitcnt lgkmcnt(4)\n"
  "buffer_load_dwordx4 v112, s[40:43], s57 offen offset:0  lds\n"
  "buffer_load_dwordx4 v113, s[40:43], s57 offen offset:1024  lds\n"
  "s_add_u32 s57, s57, 6144\n"
  "s_barrier\n"
  "v_exp_f32 v28, v28\n"
  "v_mfma_f32_16x16x32_bf16 v[8:11], a[244:247], v[36:39], v[8:11]\n"
  "s_waitcnt vmcnt(14)\n"
  "ds_read_b128 v[192:195], v87 offset:8192\n"
  "ds_read_b128 v[196:199], v88 offset:8192\n"
  "v_exp_f32 v29, v29\n"
  "v_mfma_f32_16x16x32_bf16 v[8:11], a[248:251], v[40:43], v[8:11]\n"
  "v_exp_f32 v30, v30\n"
  "v_mfma_f32_16x16x32_bf16 v[8:11], a[252:255], v[44:47], v[8:11]\n"
  "s_waitcnt vmcnt(12)\n"
  "ds_read_b128 v[184:187], v87 offset:10240\n"
  "ds_read_b128 v[188:191], v88 offset:10240\n"
  "v_exp_f32 v31, v31\n"
  "v_mfma_f32_16x16x32_bf16 v[8:11], v[128:131], v[48:51], v[8:11]\n"
  "v_cvt_pk_bf16_f32 v26, v28, v29\n"
  "v_cvt_pk_bf16_f32 v27, v30, v31\n"
  "v_mfma_f32_16x16x32_bf16 v[8:11], v[132:135], v[52:55], v[8:11]\n"
  "s_waitcnt vmcnt(10)\n"
  "ds_read_b128 v[208:211], v87 offset:12288\n"
  "ds_read_b128 v[212:215], v88 offset:12288\n"
  "ds_write_b128 v80, v[24:27] offset:3072\n"
  "v_pk_fma_f32 v[0:1], v[0:1], s[60:61], s[60:61] op_sel:[0,0,1] op_sel_hi:[1,0,1]\n"
  "v_pk_fma_f32 v[2:3], v[2:3], s[60:61], s[60:61] op_sel:[0,0,1] op_sel_hi:[1,0,1]\n"
  "v_pk_fma_f32 v[4:5], v[4:5], s[60:61], s[60:61] op_sel:[0,0,1] op_sel_hi:[1,0,1]\n"
  "v_pk_fma_f32 v[6:7], v[6:7], s[60:61], s[60:61] op_sel:[0,0,1] op_sel_hi:[1,0,1]\n"
  "v_mfma_f32_16x16x32_bf16 v[12:15], v[156:159], v[52:55], 0\n"
  "v_exp_f32 v0, v0\n"
  "v_mfma_f32_16x16x32_bf16 v[12:15], v[152:155], v[48:51], v[12:15]\n"
  "s_waitcnt vmcnt(8)\n"
  "ds_read_b128 v[216:219], v87 offset:14336\n"
  "ds_read_b128 v[220:223], v88 offset:14336\n"
  "v_exp_f32 v1, v1\n"
  "v_mfma_f32_16x16x32_bf16 v[12:15], v[148:151], v[44:47], v[12:15]\n"
  "v_exp_f32 v2, v2\n"
  "v_mfma_f32_16x16x32_bf16 v[12:15], v[144:147], v[40:43], v[12:15]\n"
  "s_waitcnt vmcnt(6)\n"
  "ds_read_b128 v[224:227], v87 offset:16384\n"
  "ds_read_b128 v[228:231], v88 offset:16384\n"
  "v_exp_f32 v3, v3\n"
  "v_mfma_f32_16x16x32_bf16 v[12:15], v[140:143], v[36:39], v[12:15]\n"
  "v_cvt_pk_bf16_f32 v0, v0, v1\n"
  "v_cvt_pk_bf16_f32 v1, v2, v3\n"
  "v_mfma_f32_16x16x32_bf16 v[12:15], v[136:139], v[32:35], v[12:15]\n"
  "s_waitcnt vmcnt(4)\n"
  "ds_read_b128 v[248:251], v87 offset:0\n"
  "ds_read_b128 v[252:255], v88 offset:0\n"
  "v_mfma_f32_16x16x32_bf16 v[16:19], v[160:163], v[32:35], 0\n"
  "v_exp_f32 v4, v4\n"
  "v_mfma_f32_16x16x32_bf16 v[16:19], v[164:167], v[36:39], v[16:19]\n"
  "s_waitcnt vmcnt(2)\n"
  "ds_read_b128 v[240:243], v87 offset:2048\n"
  "ds_read_b128 v[244:247], v88 offset:2048\n"
  "v_exp_f32 v5, v5\n"
  "v_mfma_f32_16x16x32_bf16 v[16:19], v[168:171], v[40:43], v[16:19]\n"
  "v_exp_f32 v6, v6\n"
  "v_mfma_f32_16x16x32_bf16 v[16:19], v[172:175], v[44:47], v[16:19]\n"
  "s_waitcnt vmcnt(0)\n"
  "ds_read_b128 v[232:235], v87 offset:4096\n"
  "ds_read_b128 v[236:239], v88 offset:4096\n"
  "v_exp_f32 v7, v7\n"
  "v_mfma_f32_16x16x32_bf16 v[16:19], v[176:179], v[48:51], v[16:19]\n"
  "v_cvt_pk_bf16_f32 v2, v4, v5\n"
  "v_cvt_pk_bf16_f32 v3, v6, v7\n"
  "v_mfma_f32_16x16x32_bf16 v[16:19], v[180:183], v[52:55], v[16:19]\n"
  "s_waitcnt lgkmcnt(15)\n"
  "ds_write_b128 v80, v[0:3] offset:4096\n"
  "v_pk_fma_f32 v[8:9], v[8:9], s[60:61], s[60:61] op_sel:[0,0,1] op_sel_hi:[1,0,1]\n"
  "v_pk_fma_f32 v[10:11], v[10:11], s[60:61], s[60:61] op_sel:[0,0,1] op_sel_hi:[1,0,1]\n"
  "v_pk_fma_f32 v[12:13], v[12:13], s[60:61], s[60:61] op_sel:[0,0,1] op_sel_hi:[1,0,1]\n"
  "v_pk_fma_f32 v[14:15], v[14:15], s[60:61], s[60:61] op_sel:[0,0,1] op_sel_hi:[1,0,1]\n"
  "v_mfma_f32_16x16x32_bf16 v[20:23], v[204:207], v[52:55], 0\n"
  "v_exp_f32 v8, v8\n"
  "v_mfma_f32_16x16x32_bf16 v[20:23], v[200:203], v[48:51], v[20:23]\n"
  "v_exp_f32 v9, v9\n"
  "v_mfma_f32_16x16x32_bf16 v[20:23], v[196:199], v[44:47], v[20:23]\n"
  "v_exp_f32 v10, v10\n"
  "v_mfma_f32_16x16x32_bf16 v[20:23], v[192:195], v[40:43], v[20:23]\n"
  "s_waitcnt lgkmcnt(14)\n"
  "v_exp_f32 v11, v11\n"
  "v_mfma_f32_16x16x32_bf16 v[20:23], v[188:191], v[36:39], v[20:23]\n"
  "v_cvt_pk_bf16_f32 v8, v8, v9\n"
  "v_cvt_pk_bf16_f32 v9, v10, v11\n"
  "v_mfma_f32_16x16x32_bf16 v[20:23], v[184:187], v[32:35], v[20:23]\n"
  "s_waitcnt lgkmcnt(12)\n"
  "v_mfma_f32_16x16x32_bf16 v[24:27], v[208:211], v[32:35], 0\n"
  "v_exp_f32 v12, v12\n"
  "v_mfma_f32_16x16x32_bf16 v[24:27], v[212:215], v[36:39], v[24:27]\n"
  "s_waitcnt lgkmcnt(9)\n"
  "v_exp_f32 v13, v13\n"
  "v_mfma_f32_16x16x32_bf16 v[24:27], v[216:219], v[40:43], v[24:27]\n"
  "v_exp_f32 v14, v14\n"
  "v_mfma_f32_16x16x32_bf16 v[24:27], v[220:223], v[44:47], v[24:27]\n"
  "s_waitcnt lgkmcnt(7)\n"
  "v_exp_f32 v15, v15\n"
  "v_mfma_f32_16x16x32_bf16 v[24:27], v[224:227], v[48:51], v[24:27]\n"
  "v_cvt_pk_bf16_f32 v10, v12, v13\n"
  "v_cvt_pk_bf16_f32 v11, v14, v15\n"
  "v_mfma_f32_16x16x32_bf16 v[24:27], v[228:231], v[52:55], v[24:27]\n"
  "s_waitcnt lgkmcnt(5)\n"
  "ds_write_b128 v80, v[8:11] offset:5120\n"
  "v_pk_fma_f32 v[16:17], v[16:17], s[60:61], s[60:61] op_sel:[0,0,1] op_sel_hi:[1,0,1]\n"
  "v_pk_fma_f32 v[18:19], v[18:19], s[60:61], s[60:61] op_sel:[0,0,1] op_sel_hi:[1,0,1]\n"
  "v_pk_fma_f32 v[20:21], v[20:21], s[60:61], s[60:61] op_sel:[0,0,1] op_sel_hi:[1,0,1]\n"
  "v_pk_fma_f32 v[22:23], v[22:23], s[60:61], s[60:61] op_sel:[0,0,1] op_sel_hi:[1,0,1]\n"
  "v_mfma_f32_16x16x32_bf16 v[28:31], v[252:255], v[52:55], 0\n"
  "v_exp_f32 v16, v16\n"
  "v_mfma_f32_16x16x32_bf16 v[28:31], v[248:251], v[48:51], v[28:31]\n"
  "s_waitcnt lgkmcnt(4)\n"
  "v_exp_f32 v17, v17\n"
  "v_mfma_f32_16x16x32_bf16 v[28:31], v[244:247], v[44:47], v[28:31]\n"
  "v_exp_f32 v18, v18\n"
  "v_mfma_f32_16x16x32_bf16 v[28:31], v[240:243], v[40:43], v[28:31]\n"
  "s_waitcnt lgkmcnt(2)\n"
  "s_sub_u32 s56, s56, 1\n"
  "v_exp_f32 v19, v19\n"
  "v_mfma_f32_16x16x32_bf16 v[28:31], v[236:239], v[36:39], v[28:31]\n"
  "v_cvt_pk_bf16_f32 v16, v16, v17\n"
  "v_cvt_pk_bf16_f32 v17, v18, v19\n"
  "v_mfma_f32_16x16x32_bf16 v[28:31], v[232:235], v[32:35], v[28:31]\n"
  ".p2align 6\n"
  "A_H1_%=:\n"
  "v_mfma_f32_16x16x32_bf16 v[0:3], a[0:3], v[56:59], 0\n"
  "s_add_u32 s66, s66, 6144\n"
  "v_exp_f32 v20, v20\n"
  "v_mfma_f32_16x16x32_bf16 v[0:3], a[4:7], v[60:63], v[0:3]\n"
  "s_cmp_eq_u32 s66, 86016\n"
  "v_exp_f32 v21, v21\n"
  "v_mfma_f32_16x16x32_bf16 v[0:3], a[8:11], v[64:67], v[0:3]\n"
  "s_cselect_b32 s66, 49152, s66\n"
  "v_exp_f32 v22, v22\n"
  "v_mfma_f32_16x16x32_bf16 v[0:3], a[12:15], v[68:71], v[0:3]\n"
  "v_exp_f32 v23, v23\n"
  "v_mfma_f32_16x16x32_bf16 v[0:3], a[16:19], v[72:75], v[0:3]\n"
  "v_cvt_pk_bf16_f32 v18, v20, v21\n"
  "v_cvt_pk_bf16_f32 v19, v22, v23\n"
  "v_mfma_f32_16x16x32_bf16 v[0:3], a[20:23], v[76:79], v[0:3]\n"
  "ds_write_b128 v80, v[16:19] offset:6144\n"
  "v_pk_fma_f32 v[24:25], v[24:25], s[60:61], s[60:61] op_sel:[0,0,1] op_sel_hi:[1,0,1]\n"
  "v_pk_fma_f32 v[26:27], v[26:27], s[60:61], s[60:61] op_sel:[0,0,1] op_sel_hi:[1,0,1]\n"
  "v_pk_fma_f32 v[28:29], v[28:29], s[60:61], s[60:61] op_sel:[0,0,1] op_sel_hi:[1,0,1]\n"
  "v_pk_fma_f32 v[30:31], v[30:31], s[60:61], s[60:61] op_sel:[0,0,1] op_sel_hi:[1,0,1]\n"
  "v_mfma_f32_16x16x32_bf16 v[4:7], a[44:47], v[76:79], 0\n"
  "v_exp_f32 v24, v24\n"
  "v_mfma_f32_16x16x32_bf16 v[4:7], a[40:43], v[72:75], v[4:7]\n"
  "v_exp_f32 v25, v25\n"
  "v_mfma_f32_16x16x32_bf16 v[4:7], a[36:39], v[68:71], v[4:7]\n"
  "v_exp_f32 v26, v26\n"
  "v_mfma_f32_16x16x32_bf16 v[4:7], a[32:35], v[64:67], v[4:7]\n"
  "v_exp_f32 v27, v27\n"
  "v_mfma_f32_16x16x32_bf16 v[4:7], a[28:31], v[60:63], v[4:7]\n"
  "v_cvt_pk_bf16_f32 v24, v24, v25\n"
  "v_cvt_pk_bf16_f32 v25, v26, v27\n"
  "v_mfma_f32_16x16x32_bf16 v[4:7], a[24:27], v[56:59], v[4:7]\n"
  "v_add_u32 v81, s66, v83\n"
  "v_add_u32 v82, s66, v84\n"
  "v_mfma_f32_16x16x32_bf16 v[8:11], a[48:51], v[56:59], 0\n"
  "v_exp_f32 v28, v28\n"
  "v_mfma_f32_16x16x32_bf16 v[8:11], a[52:55], v[60:63], v[8:11]\n"
  "v_exp_f32 v29, v29\n"
  "v_mfma_f32_16x16x32_bf16 v[8:11], a[56:59], v[64:67], v[8:11]\n"
  "v_exp_f32 v30, v30\n"
  "v_mfma_f32_16x16x32_bf16 v[8:11], a[60:63], v[68:71], v[8:11]\n"
  "v_exp_f32 v31, v31\n"
  "v_mfma_f32_16x16x32_bf16 v[8:11], a[64:67], v[72:75], v[8:11]\n"
  "v_cvt_pk_bf16_f32 v26, v28, v29\n"
  "v_cvt_pk_bf16_f32 v27, v30, v31\n"
  "v_mfma_f32_16x16x32_bf16 v[8:11], a[68:71], v[76:79], v[8:11]\n"
  "ds_write_b128 v80, v[24:27] offset:7168\n"
  "v_pk_fma_f32 v[0:1], v[0:1], s[60:61], s[60:61] op_sel:[0,0,1] op_sel_hi:[1,0,1]\n"
  "v_pk_fma_f32 v[2:3], v[2:3], s[60:61], s[60:61] op_sel:[0,0,1] op_sel_hi:[1,0,1]\n"
  "v_pk_fma_f32 v[4:5], v[4:5], s[60:61], s[60:61] op_sel:[0,0,1] op_sel_hi:[1,0,1]\n"
  "v_pk_fma_f32 v[6:7], v[6:7], s[60:61], s[60:61] op_sel:[0,0,1] op_sel_hi:[1,0,1]\n"
  "v_mfma_f32_16x16x32_bf16 v[12:15], a[92:95], v[76:79], 0\n"
  "v_exp_f32 v0, v0\n"
  "v_mfma_f32_16x16x32_bf16 v[12:15], a[88:91], v[72:75], v[12:15]\n"
  "v_exp_f32 v1, v1\n"
  "v_mfma_f32_16x16x32_bf16 v[12:15], a[84:87], v[68:71], v[12:15]\n"
  "v_exp_f32 v2, v2\n"
  "v_mfma_f32_16x16x32_bf16 v[12:15], a[80:83], v[64:67], v[12:15]\n"
  "v_exp_f32 v3, v3\n"
  "v_mfma_f32_16x16x32_bf16 v[12:15], a[76:79], v[60:63], v[12:15]\n"
  "v_cvt_pk_bf16_f32 v0, v0, v1\n"
  "v_cvt_pk_bf16_f32 v1, v2, v3\n"
  "v_mfma_f32_16x16x32_bf16 v[12:15], a[72:75], v[56:59], v[12:15]\n"
  "s_waitcnt lgkmcnt(0)\n"
  "s_barrier\n"
  "v_xor_b32 v80, 0x4000, v80\n"
  "v_mfma_f32_16x16x32_bf16 v[16:19], a[96:99], v[56:59], 0\n"
  "v_exp_f32 v4, v4\n"
  "v_mfma_f32_16x16x32_bf16 v[16:19], a[100:103], v[60:63], v[16:19]\n"
  "v_exp_f32 v5, v5\n"
  "v_mfma_f32_16x16x32_bf16 v[16:19], a[104:107], v[64:67], v[16:19]\n"
  "v_exp_f32 v6, v6\n"
  "v_mfma_f32_16x16x32_bf16 v[16:19], a[108:111], v[68:71], v[16:19]\n"
  "v_exp_f32 v7, v7\n"
  "v_mfma_f32_16x16x32_bf16 v[16:19], a[112:115], v[72:75], v[16:19]\n"
  "v_cvt_pk_bf16_f32 v2, v4, v5\n"
  "v_cvt_pk_bf16_f32 v3, v6, v7\n"
  "v_mfma_f32_16x16x32_bf16 v[16:19], a[116:119], v[76:79], v[16:19]\n"
  "ds_write_b128 v80, v[0:3] offset:0\n"
  "v_pk_fma_f32 v[8:9], v[8:9], s[60:61], s[60:61] op_sel:[0,0,1] op_sel_hi:[1,0,1]\n"
  "v_pk_fma_f32 v[10:11], v[10:11], s[60:61], s[60:61] op_sel:[0,0,1] op_sel_hi:[1,0,1]\n"
  "v_pk_fma_f32 v[12:13], v[12:13], s[60:61], s[60:61] op_sel:[0,0,1] op_sel_hi:[1,0,1]\n"
  "v_pk_fma_f32 v[14:15], v[14:15], s[60:61], s[60:61] op_sel:[0,0,1] op_sel_hi:[1,0,1]\n"
  "v_mfma_f32_16x16x32_bf16 v[20:23], a[140:143], v[76:79], 0\n"
  "v_exp_f32 v8, v8\n"
  "v_mfma_f32_16x16x32_bf16 v[20:23], a[136:139], v[72:75], v[20:23]\n"
  "v_exp_f32 v9, v9\n"
  "v_mfma_f32_16x16x32_bf16 v[20:23], a[132:135], v[68:71], v[20:23]\n"
  "v_exp_f32 v10, v10\n"
  "v_mfma_f32_16x16x32_bf16 v[20:23], a[128:131], v[64:67], v[20:23]\n"
  "v_exp_f32 v11, v11\n"
  "v_mfma_f32_16x16x32_bf16 v[20:23], a[124:127], v[60:63], v[20:23]\n"
  "v_cvt_pk_bf16_f32 v8, v8, v9\n"
  "v_cvt_pk_bf16_f32 v9, v10, v11\n"
  "v_mfma_f32_16x16x32_bf16 v[20:23], a[120:123], v[56:59], v[20:23]\n"
  "ds_read_b128 v[32:35], v81 offset:0\n"
  "ds_read_b128 v[36:39], v82 offset:0\n"
  "v_mfma_f32_16x16x32_bf16 v[24:27], a[144:147], v[56:59], 0\n"
  "v_exp_f32 v12, v12\n"
  "v_mfma_f32_16x16x32_bf16 v[24:27], a[148:151], v[60:63], v[24:27]\n"
  "v_exp_f32 v13, v13\n"
  "v_mfma_f32_16x16x32_bf16 v[24:27], a[152:155], v[64:67], v[24:27]\n"
  "v_exp_f32 v14, v14\n"
  "v_mfma_f32_16x16x32_bf16 v[24:27], a[156:159], v[68:71], v[24:27]\n"
  "v_exp_f32 v15, v15\n"
  "v_mfma_f32_16x16x32_bf16 v[24:27], a[160:163], v[72:75], v[24:27]\n"
  "v_cvt_pk_bf16_f32 v10, v12, v13\n"
  "v_cvt_pk_bf16_f32 v11, v14, v15\n"
  "v_mfma_f32_16x16x32_bf16 v[24:27], a[164:167], v[76:79], v[24:27]\n"
  "ds_write_b128 v80, v[8:11] offset:1024\n"
  "v_pk_fma_f32 v[16:17], v[16:17], s[60:61], s[60:61] op_sel:[0,0,1] op_sel_hi:[1,0,1]\n"
  "v_pk_fma_f32 v[18:19], v[18:19], s[60:61], s[60:61] op_sel:[0,0,1] op_sel_hi:[1,0,1]\n"
  "v_pk_fma_f32 v[20:21], v[20:21], s[60:61], s[60:61] op_sel:[0,0,1] op_sel_hi:[1,0,1]\n"
  "v_pk_fma_f32 v[22:23], v[22:23], s[60:61], s[60:61] op_sel:[0,0,1] op_sel_hi:[1,0,1]\n"
  "v_mfma_f32_16x16x32_bf16 v[28:31], a[188:191], v[76:79], 0\n"
  "v_exp_f32 v16, v16\n"
  "v_mfma_f32_16x16x32_bf16 v[28:31], a[184:187], v[72:75], v[28:31]\n"
  "v_exp_f32 v17, v17\n"
  "v_mfma_f32_16x16x32_bf16 v[28:31], a[180:183], v[68:71], v[28:31]\n"
  "v_exp_f32 v18, v18\n"
  "v_mfma_f32_16x16x32_bf16 v[28:31], a[176:179], v[64:67], v[28:31]\n"
  "v_exp_f32 v19, v19\n"
  "v_mfma_f32_16x16x32_bf16 v[28:31], a[172:175], v[60:63], v[28:31]\n"
  "v_cvt_pk_bf16_f32 v16, v16, v17\n"
  "v_cvt_pk_bf16_f32 v17, v18, v19\n"
  "v_mfma_f32_16x16x32_bf16 v[28:31], a[168:171], v[56:59], v[28:31]\n"
  "ds_read_b128 v[40:43], v81 offset:2048\n"
  "ds_read_b128 v[44:47], v82 offset:2048\n"
  "v_mfma_f32_16x16x32_bf16 v[0:3], a[192:195], v[56:59], 0\n"
  "v_exp_f32 v20, v20\n"
  "v_mfma_f32_16x16x32_bf16 v[0:3], a[196:199], v[60:63], v[0:3]\n"
  "v_exp_f32 v21, v21\n"
  "v_mfma_f32_16x16x32_bf16 v[0:3], a[200:203], v[64:67], v[0:3]\n"
  "v_exp_f32 v22, v22\n"
  "v_mfma_f32_16x16x32_bf16 v[0:3], a[204:207], v[68:71], v[0:3]\n"
  "v_exp_f32 v23, v23\n"
  "v_mfma_f32_16x16x32_bf16 v[0:3], a[208:211], v[72:75], v[0:3]\n"
  "v_cvt_pk_bf16_f32 v18, v20, v21\n"
  "v_cvt_pk_bf16_f32 v19, v22, v23\n"
  "v_mfma_f32_16x16x32_bf16 v[0:3], a[212:215], v[76:79], v[0:3]\n"
  "ds_write_b128 v80, v[16:19] offset:2048\n"
  "v_pk_fma_f32 v[24:25], v[24:25], s[60:61], s[60:61] op_sel:[0,0,1] op_sel_hi:[1,0,1]\n"
  "v_pk_fma_f32 v[26:27], v[26:27], s[60:61], s[60:61] op_sel:[0,0,1] op_sel_hi:[1,0,1]\n"
  "v_pk_fma_f32 v[28:29], v[28:29], s[60:61], s[60:61] op_sel:[0,0,1] op_sel_hi:[1,0,1]\n"
  "v_pk_fma_f32 v[30:31], v[30:31], s[60:61], s[60:61] op_sel:[0,0,1] op_sel_hi:[1,0,1]\n"
  "v_mfma_f32_16x16x32_bf16 v[4:7], a[236:239], v[76:79], 0\n"
  "v_exp_f32 v24, v24\n"
  "v_mfma_f32_16x16x32_bf16 v[4:7], a[232:235], v[72:75], v[4:7]\n"
  "v_exp_f32 v25, v25\n"
  "v_mfma_f32_16x16x32_bf16 v[4:7], a[228:231], v[68:71], v[4:7]\n"
  "v_exp_f32 v26, v26\n"
  "v_mfma_f32_16x16x32_bf16 v[4:7], a[224:227], v[64:67], v[4:7]\n"
  "v_exp_f32 v27, v27\n"
  "v_mfma_f32_16x16x32_bf16 v[4:7], a[220:223], v[60:63], v[4:7]\n"
  "v_cvt_pk_bf16_f32 v24, v24, v25\n"
  "v_cvt_pk_bf16_f32 v25, v26, v27\n"
  "v_mfma_f32_16x16x32_bf16 v[4:7], a[216:219], v[56:59], v[4:7]\n"
  "ds_read_b128 v[48:51], v81 offset:4096\n"
  "ds_read_b128 v[52:55], v82 offset:4096\n"
  "v_mfma_f32_16x16x32_bf16 v[8:11], a[240:243], v[56:59], 0\n"
  "v_exp_f32 v28, v28\n"
  "v_mfma_f32_16x16x32_bf16 v[8:11], a[244:247], v[60:63], v[8:11]\n"
  "v_exp_f32 v29, v29\n"
  "v_mfma_f32_16x16x32_bf16 v[8:11], a[248:251], v[64:67], v[8:11]\n"
  "v_exp_f32 v30, v30\n"
  "v_mfma_f32_16x16x32_bf16 v[8:11], a[252:255], v[68:71], v[8:11]\n"
  "v_exp_f32 v31, v31\n"
  "v_mfma_f32_16x16x32_bf16 v[8:11], v[128:131], v[72:75], v[8:11]\n"
  "v_cvt_pk_bf16_f32 v26, v28, v29\n"
  "v_cvt_pk_bf16_f32 v27, v30, v31\n"
  "v_mfma_f32_16x16x32_bf16 v[8:11], v[132:135], v[76:79], v[8:11]\n"
  "ds_write_b128 v80, v[24:27] offset:3072\n"
  "v_pk_fma_f32 v[0:1], v[0:1], s[60:61], s[60:61] op_sel:[0,0,1] op_sel_hi:[1,0,1]\n"
  "v_pk_fma_f32 v[2:3], v[2:3], s[60:61], s[60:61] op_sel:[0,0,1] op_sel_hi:[1,0,1]\n"
  "v_pk_fma_f32 v[4:5], v[4:5], s[60:61], s[60:61] op_sel:[0,0,1] op_sel_hi:[1,0,1]\n"
  "v_pk_fma_f32 v[6:7], v[6:7], s[60:61], s[60:61] op_sel:[0,0,1] op_sel_hi:[1,0,1]\n"
  "v_mfma_f32_16x16x32_bf16 v[12:15], v[156:159], v[76:79], 0\n"
  "v_exp_f32 v0, v0\n"
  "v_mfma_f32_16x16x32_bf16 v[12:15], v[152:155], v[72:75], v[12:15]\n"
  "v_exp_f32 v1, v1\n"
  "v_mfma_f32_16x16x32_bf16 v[12:15], v[148:151], v[68:71], v[12:15]\n"
  "v_exp_f32 v2, v2\n"
  "v_mfma_f32_16x16x32_bf16 v[12:15], v[144:147], v[64:67], v[12:15]\n"
  "v_exp_f32 v3, v3\n"
  "v_mfma_f32_16x16x32_bf16 v[12:15], v[140:143], v[60:63], v[12:15]\n"
  "v_cvt_pk_bf16_f32 v0, v0, v1\n"
  "v_cvt_pk_bf16_f32 v1, v2, v3\n"
  "v_mfma_f32_16x16x32_bf16 v[12:15], v[136:139], v[56:59], v[12:15]\n"
  "v_mfma_f32_16x16x32_bf16 v[16:19], v[160:163], v[56:59], 0\n"
  "v_exp_f32 v4, v4\n"
  "v_mfma_f32_16x16x32_bf16 v[16:19], v[164:167], v[60:63], v[16:19]\n"
  "v_exp_f32 v5, v5\n"
  "v_mfma_f32_16x16x32_bf16 v[16:19], v[168:171], v[64:67], v[16:19]\n"
  "v_exp_f32 v6, v6\n"
  "v_mfma_f32_16x16x32_bf16 v[16:19], v[172:175], v[68:71], v[16:19]\n"
  "v_exp_f32 v7, v7\n"
  "v_mfma_f32_16x16x32_bf16 v[16:19], v[176:179], v[72:75], v[16:19]\n"
  "v_cvt_pk_bf16_f32 v2, v4, v5\n"
  "v_cvt_pk_bf16_f32 v3, v6, v7\n"
  "v_mfma_f32_16x16x32_bf16 v[16:19], v[180:183], v[76:79], v[16:19]\n"
  "ds_write_b128 v80, v[0:3] offset:4096\n"
  "v_pk_fma_f32 v[8:9], v[8:9], s[60:61], s[60:61] op_sel:[0,0,1] op_sel_hi:[1,0,1]\n"
  "v_pk_fma_f32 v[10:11], v[10:11], s[60:61], s[60:61] op_sel:[0,0,1] op_sel_hi:[1,0,1]\n"
  "v_pk_fma_f32 v[12:13], v[12:13], s[60:61], s[60:61] op_sel:[0,0,1] op_sel_hi:[1,0,1]\n"
  "v_pk_fma_f32 v[14:15], v[14:15], s[60:61], s[60:61] op_sel:[0,0,1] op_sel_hi:[1,0,1]\n"
  "v_mfma_f32_16x16x32_bf16 v[20:23], v[204:207], v[76:79], 0\n"
  "v_exp_f32 v8, v8\n"
  "v_mfma_f32_16x16x32_bf16 v[20:23], v[200:203], v[72:75], v[20:23]\n"
  "v_exp_f32 v9, v9\n"
  "v_mfma_f32_16x16x32_bf16 v[20:23], v[196:199], v[68:71], v[20:23]\n"
  "v_exp_f32 v10, v10\n"
  "v_mfma_f32_16x16x32_bf16 v[20:23], v[192:195], v[64:67], v[20:23]\n"
  "v_exp_f32 v11, v11\n"
  "v_mfma_f32_16x16x32_bf16 v[20:23], v[188:191], v[60:63], v[20:23]\n"
  "v_cvt_pk_bf16_f32 v8, v8, v9\n"
  "v_cvt_pk_bf16_f32 v9, v10, v11\n"
  "v_mfma_f32_16x16x32_bf16 v[20:23], v[184:187], v[56:59], v[20:23]\n"
  "v_mfma_f32_16x16x32_bf16 v[24:27], v[208:211], v[56:59], 0\n"
  "v_exp_f32 v12, v12\n"
  "v_mfma_f32_16x16x32_bf16 v[24:27], v[212:215], v[60:63], v[24:27]\n"
  "v_exp_f32 v13, v13\n"
  "v_mfma_f32_16x16x32_bf16 v[24:27], v[216:219], v[64:67], v[24:27]\n"
  "v_exp_f32 v14, v14\n"
  "v_mfma_f32_16x16x32_bf16 v[24:27], v[220:223], v[68:71], v[24:27]\n"
  "v_exp_f32 v15, v15\n"
  "v_mfma_f32_16x16x32_bf16 v[24:27], v[224:227], v[72:75], v[24:27]\n"
  "v_cvt_pk_bf16_f32 v10, v12, v13\n"
  "v_cvt_pk_bf16_f32 v11, v14, v15\n"
  "v_mfma_f32_16x16x32_bf16 v[24:27], v[228:231], v[76:79], v[24:27]\n"
  "ds_write_b128 v80, v[8:11] offset:5120\n"
  "v_pk_fma_f32 v[16:17], v[16:17], s[60:61], s[60:61] op_sel:[0,0,1] op_sel_hi:[1,0,1]\n"
  "v_pk_fma_f32 v[18:19], v[18:19], s[60:61], s[60:61] op_sel:[0,0,1] op_sel_hi:[1,0,1]\n"
  "v_pk_fma_f32 v[20:21], v[20:21], s[60:61], s[60:61] op_sel:[0,0,1] op_sel_hi:[1,0,1]\n"
  "v_pk_fma_f32 v[22:23], v[22:23], s[60:61], s[60:61] op_sel:[0,0,1] op_sel_hi:[1,0,1]\n"
  "v_mfma_f32_16x16x32_bf16 v[28:31], v[252:255], v[76:79], 0\n"
  "v_exp_f32 v16, v16\n"
  "v_mfma_f32_16x16x32_bf16 v[28:31], v[248:251], v[72:75], v[28:31]\n"
  "v_exp_f32 v17, v17\n"
  "v_mfma_f32_16x16x32_bf16 v[28:31], v[244:247], v[68:71], v[28:31]\n"
  "v_exp_f32 v18, v18\n"
  "v_mfma_f32_16x16x32_bf16 v[28:31], v[240:243], v[64:67], v[28:31]\n"
  "s_sub_u32 s56, s56, 1\n"
  "v_exp_f32 v19, v19\n"
  "v_mfma_f32_16x16x32_bf16 v[28:31], v[236:239], v[60:63], v[28:31]\n"
  "s_cmp_eq_u32 s56, 0\n"
  "v_cvt_pk_bf16_f32 v16, v16, v17\n"
  "v_cvt_pk_bf16_f32 v17, v18, v19\n"
  "v_mfma_f32_16x16x32_bf16 v[28:31], v[232:235], v[56:59], v[28:31]\n"
  "s_waitcnt lgkmcnt(3)\n"
  "s_cbranch_scc1 A_X0_%=\n"
  "A_H0_%=:\n"
  "v_mfma_f32_16x16x32_bf16 v[0:3], a[0:3], v[32:35], 0\n"
  "s_add_u32 s66, s66, 6144\n"
  "v_exp_f32 v20, v20\n"
  "v_mfma_f32_16x16x32_bf16 v[0:3], a[4:7], v[36:39], v[0:3]\n"
  "s_cmp_eq_u32 s66, 86016\n"
  "v_exp_f32 v21, v21\n"
  "v_mfma_f32_16x16x32_bf16 v[0:3], a[8:11], v[40:43], v[0:3]\n"
  "s_cselect_b32 s66, 49152, s66\n"
  "v_exp_f32 v22, v22\n"
  "v_mfma_f32_16x16x32_bf16 v[0:3], a[12:15], v[44:47], v[0:3]\n"
  "v_exp_f32 v23, v23\n"
  "v_mfma_f32_16x16x32_bf16 v[0:3], a[16:19], v[48:51], v[0:3]\n"
  "v_cvt_pk_bf16_f32 v18, v20, v21\n"
  "v_cvt_pk_bf16_f32 v19, v22, v23\n"
  "v_mfma_f32_16x16x32_bf16 v[0:3], a[20:23], v[52:55], v[0:3]\n"
  "ds_write_b128 v80, v[16:19] offset:6144\n"
  "v_pk_fma_f32 v[24:25], v[24:25], s[60:61], s[60:61] op_sel:[0,0,1] op_sel_hi:[1,0,1]\n"
  "v_pk_fma_f32 v[26:27], v[26:27], s[60:61], s[60:61] op_sel:[0,0,1] op_sel_hi:[1,0,1]\n"
  "v_pk_fma_f32 v[28:29], v[28:29], s[60:61], s[60:61] op_sel:[0,0,1] op_sel_hi:[1,0,1]\n"
  "v_pk_fma_f32 v[30:31], v[30:31], s[60:61], s[60:61] op_sel:[0,0,1] op_sel_hi:[1,0,1]\n"
  "v_mfma_f32_16x16x32_bf16 v[4:7], a[44:47], v[52:55], 0\n"
  "v_exp_f32 v24, v24\n"
  "v_mfma_f32_16x16x32_bf16 v[4:7], a[40:43], v[48:51], v[4:7]\n"
  "v_exp_f32 v25, v25\n"
  "v_mfma_f32_16x16x32_bf16 v[4:7], a[36:39], v[44:47], v[4:7]\n"
  "v_exp_f32 v26, v26\n"
  "v_mfma_f32_16x16x32_bf16 v[4:7], a[32:35], v[40:43], v[4:7]\n"
  "v_exp_f32 v27, v27\n"
  "v_mfma_f32_16x16x32_bf16 v[4:7], a[28:31], v[36:39], v[4:7]\n"
  "v_cvt_pk_bf16_f32 v24, v24, v25\n"
  "v_cvt_pk_bf16_f32 v25, v26, v27\n"
  "v_mfma_f32_16x16x32_bf16 v[4:7], a[24:27], v[32:35], v[4:7]\n"
  "v_add_u32 v81, s66, v83\n"
  "v_add_u32 v82, s66, v84\n"
  "v_mfma_f32_16x16x32_bf16 v[8:11], a[48:51], v[32:35], 0\n"
  "v_exp_f32 v28, v28\n"
  "v_mfma_f32_16x16x32_bf16 v[8:11], a[52:55], v[36:39], v[8:11]\n"
  "v_exp_f32 v29, v29\n"
  "v_mfma_f32_16x16x32_bf16 v[8:11], a[56:59], v[40:43], v[8:11]\n"
  "v_exp_f32 v30, v30\n"
  "v_mfma_f32_16x16x32_bf16 v[8:11], a[60:63], v[44:47], v[8:11]\n"
  "v_exp_f32 v31, v31\n"
  "v_mfma_f32_16x16x32_bf16 v[8:11], a[64:67], v[48:51], v[8:11]\n"
  "v_cvt_pk_bf16_f32 v26, v28, v29\n"
  "v_cvt_pk_bf16_f32 v27, v30, v31\n"
  "v_mfma_f32_16x16x32_bf16 v[8:11], a[68:71], v[52:55], v[8:11]\n"
  "ds_write_b128 v80, v[24:27] offset:7168\n"
  "v_pk_fma_f32 v[0:1], v[0:1], s[60:61], s[60:61] op_sel:[0,0,1] op_sel_hi:[1,0,1]\n"
  "v_pk_fma_f32 v[2:3], v[2:3], s[60:61], s[60:61] op_sel:[0,0,1] op_sel_hi:[1,0,1]\n"
  "v_pk_fma_f32 v[4:5], v[4:5], s[60:61], s[60:61] op_sel:[0,0,1] op_sel_hi:[1,0,1]\n"
  "v_pk_fma_f32 v[6:7], v[6:7], s[60:61], s[60:61] op_sel:[0,0,1] op_sel_hi:[1,0,1]\n"
  "v_mfma_f32_16x16x32_bf16 v[12:15], a[92:95], v[52:55], 0\n"
  "v_exp_f32 v0, v0\n"
  "v_mfma_f32_16x16x32_bf16 v[12:15], a[88:91], v[48:51], v[12:15]\n"
  "v_exp_f32 v1, v1\n"
  "v_mfma_f32_16x16x32_bf16 v[12:15], a[84:87], v[44:47], v[12:15]\n"
  "v_exp_f32 v2, v2\n"
  "v_mfma_f32_16x16x32_bf16 v[12:15], a[80:83], v[40:43], v[12:15]\n"
  "v_exp_f32 v3, v3\n"
  "v_mfma_f32_16x16x32_bf16 v[12:15], a[76:79], v[36:39], v[12:15]\n"
  "v_cvt_pk_bf16_f32 v0, v0, v1\n"
  "v_cvt_pk_bf16_f32 v1, v2, v3\n"
  "v_mfma_f32_16x16x32_bf16 v[12:15], a[72:75], v[32:35], v[12:15]\n"
  "s_waitcnt lgkmcnt(0)\n"
  "s_barrier\n"
  "v_xor_b32 v80, 0x4000, v80\n"
  "v_mfma_f32_16x16x32_bf16 v[16:19], a[96:99], v[32:35], 0\n"
  "v_exp_f32 v4, v4\n"
  "v_mfma_f32_16x16x32_bf16 v[16:19], a[100:103], v[36:39], v[16:19]\n"
  "v_exp_f32 v5, v5\n"
  "v_mfma_f32_16x16x32_bf16 v[16:19], a[104:107], v[40:43], v[16:19]\n"
  "v_exp_f32 v6, v6\n"
  "v_mfma_f32_16x16x32_bf16 v[16:19], a[108:111], v[44:47], v[16:19]\n"
  "v_exp_f32 v7, v7\n"
  "v_mfma_f32_16x16x32_bf16 v[16:19], a[112:115], v[48:51], v[16:19]\n"
  "v_cvt_pk_bf16_f32 v2, v4, v5\n"
  "v_cvt_pk_bf16_f32 v3, v6, v7\n"
  "v_mfma_f32_16x16x32_bf16 v[16:19], a[116:119], v[52:55], v[16:19]\n"
  "ds_write_b128 v80, v[0:3] offset:0\n"
  "v_pk_fma_f32 v[8:9], v[8:9], s[60:61], s[60:61] op_sel:[0,0,1] op_sel_hi:[1,0,1]\n"
  "v_pk_fma_f32 v[10:11], v[10:11], s[60:61], s[60:61] op_sel:[0,0,1] op_sel_hi:[1,0,1]\n"
  "v_pk_fma_f32 v[12:13], v[12:13], s[60:61], s[60:61] op_sel:[0,0,1] op_sel_hi:[1,0,1]\n"
  "v_pk_fma_f32 v[14:15], v[14:15], s[60:61], s[60:61] op_sel:[0,0,1] op_sel_hi:[1,0,1]\n"
  "v_mfma_f32_16x16x32_bf16 v[20:23], a[140:143], v[52:55], 0\n"
  "v_exp_f32 v8, v8\n"
  "v_mfma_f32_16x16x32_bf16 v[20:23], a[136:139], v[48:51], v[20:23]\n"
  "v_exp_f32 v9, v9\n"
  "v_mfma_f32_16x16x32_bf16 v[20:23], a[132:135], v[44:47], v[20:23]\n"
  "v_exp_f32 v10, v10\n"
  "v_mfma_f32_16x16x32_bf16 v[20:23], a[128:131], v[40:43], v[20:23]\n"
  "v_exp_f32 v11, v11\n"
  "v_mfma_f32_16x16x32_bf16 v[20:23], a[124:127], v[36:39], v[20:23]\n"
  "v_cvt_pk_bf16_f32 v8, v8, v9\n"
  "v_cvt_pk_bf16_f32 v9, v10, v11\n"
  "v_mfma_f32_16x16x32_bf16 v[20:23], a[120:123], v[32:35], v[20:23]\n"
  "ds_read_b128 v[56:59], v81 offset:0\n"
  "ds_read_b128 v[60:63], v82 offset:0\n"
  "v_mfma_f32_16x16x32_bf16 v[24:27], a[144:147], v[32:35], 0\n"
  "v_exp_f32 v12, v12\n"
  "v_mfma_f32_16x16x32_bf16 v[24:27], a[148:151], v[36:39], v[24:27]\n"
  "v_exp_f32 v13, v13\n"
  "v_mfma_f32_16x16x32_bf16 v[24:27], a[152:155], v[40:43], v[24:27]\n"
  "v_exp_f32 v14, v14\n"
  "v_mfma_f32_16x16x32_bf16 v[24:27], a[156:159], v[44:47], v[24:27]\n"
  "v_exp_f32 v15, v15\n"
  "v_mfma_f32_16x16x32_bf16 v[24:27], a[160:163], v[48:51], v[24:27]\n"
  "v_cvt_pk_bf16_f32 v10, v12, v13\n"
  "v_cvt_pk_bf16_f32 v11, v14, v15\n"
  "v_mfma_f32_16x16x32_bf16 v[24:27], a[164:167], v[52:55], v[24:27]\n"
  "ds_write_b128 v80, v[8:11] offset:1024\n"
  "v_pk_fma_f32 v[16:17], v[16:17], s[60:61], s[60:61] op_sel:[0,0,1] op_sel_hi:[1,0,1]\n"
  "v_pk_fma_f32 v[18:19], v[18:19], s[60:61], s[60:61] op_sel:[0,0,1] op_sel_hi:[1,0,1]\n"
  "v_pk_fma_f32 v[20:21], v[20:21], s[60:61], s[60:61] op_sel:[0,0,1] op_sel_hi:[1,0,1]\n"
  "v_pk_fma_f32 v[22:23], v[22:23], s[60:61], s[60:61] op_sel:[0,0,1] op_sel_hi:[1,0,1]\n"
  "v_mfma_f32_16x16x32_bf16 v[28:31], a[188:191], v[52:55], 0\n"
  "v_exp_f32 v16, v16\n"
  "v_mfma_f32_16x16x32_bf16 v[28:31], a[184:187], v[48:51], v[28:31]\n"
  "v_exp_f32 v17, v17\n"
  "v_mfma_f32_16x16x32_bf16 v[28:31], a[180:183], v[44:47], v[28:31]\n"
  "v_exp_f32 v18, v18\n"
  "v_mfma_f32_16x16x32_bf16 v[28:31], a[176:179], v[40:43], v[28:31]\n"
  "v_exp_f32 v19, v19\n"
  "v_mfma_f32_16x16x32_bf16 v[28:31], a[172:175], v[36:39], v[28:31]\n"
  "v_cvt_pk_bf16_f32 v16, v16, v17\n"
  "v_cvt_pk_bf16_f32 v17, v18, v19\n"
  "v_mfma_f32_16x16x32_bf16 v[28:31], a[168:171], v[32:35], v[28:31]\n"
  "ds_read_b128 v[64:67], v81 offset:2048\n"
  "ds_read_b128 v[68:71], v82 offset:2048\n"
  "v_mfma_f32_16x16x32_bf16 v[0:3], a[192:195], v[32:35], 0\n"
  "v_exp_f32 v20, v20\n"
  "v_mfma_f32_16x16x32_bf16 v[0:3], a[196:199], v[36:39], v[0:3]\n"
  "v_exp_f32 v21, v21\n"
  "v_mfma_f32_16x16x32_bf16 v[0:3], a[200:203], v[40:43], v[0:3]\n"
  "v_exp_f32 v22, v22\n"
  "v_mfma_f32_16x16x32_bf16 v[0:3], a[204:207], v[44:47], v[0:3]\n"
  "v_exp_f32 v23, v23\n"
  "v_mfma_f32_16x16x32_bf16 v[0:3], a[208:211], v[48:51], v[0:3]\n"
  "v_cvt_pk_bf16_f32 v18, v20, v21\n"
  "v_cvt_pk_bf16_f32 v19, v22, v23\n"
  "v_mfma_f32_16x16x32_bf16 v[0:3], a[212:215], v[52:55], v[0:3]\n"
  "ds_write_b128 v80, v[16:19] offset:2048\n"
  "v_pk_fma_f32 v[24:25], v[24:25], s[60:61], s[60:61] op_sel:[0,0,1] op_sel_hi:[1,0,1]\n"
  "v_pk_fma_f32 v[26:27], v[26:27], s[60:61], s[60:61] op_sel:[0,0,1] op_sel_hi:[1,0,1]\n"
  "v_pk_fma_f32 v[28:29], v[28:29], s[60:61], s[60:61] op_sel:[0,0,1] op_sel_hi:[1,0,1]\n"
  "v_pk_fma_f32 v[30:31], v[30:31], s[60:61], s[60:61] op_sel:[0,0,1] op_sel_hi:[1,0,1]\n"
  "v_mfma_f32_16x16x32_bf16 v[4:7], a[236:239], v[52:55], 0\n"
  "v_exp_f32 v24, v24\n"
  "v_mfma_f32_16x16x32_bf16 v[4:7], a[232:235], v[48:51], v[4:7]\n"
  "v_exp_f32 v25, v25\n"
  "v_mfma_f32_16x16x32_bf16 v[4:7], a[228:231], v[44:47], v[4:7]\n"
  "v_exp_f32 v26, v26\n"
  "v_mfma_f32_16x16x32_bf16 v[4:7], a[224:227], v[40:43], v[4:7]\n"
  "v_exp_f32 v27, v27\n"
  "v_mfma_f32_16x16x32_bf16 v[4:7], a[220:223], v[36:39], v[4:7]\n"
  "v_cvt_pk_bf16_f32 v24, v24, v25\n"
  "v_cvt_pk_bf16_f32 v25, v26, v27\n"
  "v_mfma_f32_16x16x32_bf16 v[4:7], a[216:219], v[32:35], v[4:7]\n"
  "ds_read_b128 v[72:75], v81 offset:4096\n"
  "ds_read_b128 v[76:79], v82 offset:4096\n"
  "v_mfma_f32_16x16x32_bf16 v[8:11], a[240:243], v[32:35], 0\n"
  "v_exp_f32 v28, v28\n"
  "v_mfma_f32_16x16x32_bf16 v[8:11], a[244:247], v[36:39], v[8:11]\n"
  "v_exp_f32 v29, v29\n"
  "v_mfma_f32_16x16x32_bf16 v[8:11], a[248:251], v[40:43], v[8:11]\n"
  "v_exp_f32 v30, v30\n"
  "v_mfma_f32_16x16x32_bf16 v[8:11], a[252:255], v[44:47], v[8:11]\n"
  "v_exp_f32 v31, v31\n"
  "v_mfma_f32_16x16x32_bf16 v[8:11], v[128:131], v[48:51], v[8:11]\n"
  "v_cvt_pk_bf16_f32 v26, v28, v29\n"
  "v_cvt_pk_bf16_f32 v27, v30, v31\n"
  "v_mfma_f32_16x16x32_bf16 v[8:11], v[132:135], v[52:55], v[8:11]\n"
  "ds_write_b128 v80, v[24:27] offset:3072\n"
  "v_pk_fma_f32 v[0:1], v[0:1], s[60:61], s[60:61] op_sel:[0,0,1] op_sel_hi:[1,0,1]\n"
  "v_pk_fma_f32 v[2:3], v[2:3], s[60:61], s[60:61] op_sel:[0,0,1] op_sel_hi:[1,0,1]\n"
  "v_pk_fma_f32 v[4:5], v[4:5], s[60:61], s[60:61] op_sel:[0,0,1] op_sel_hi:[1,0,1]\n"
  "v_pk_fma_f32 v[6:7], v[6:7], s[60:61], s[60:61] op_sel:[0,0,1] op_sel_hi:[1,0,1]\n"
  "v_mfma_f32_16x16x32_bf16 v[12:15], v[156:159], v[52:55], 0\n"
  "v_exp_f32 v0, v0\n"
  "v_mfma_f32_16x16x32_bf16 v[12:15], v[152:155], v[48:51], v[12:15]\n"
  "v_exp_f32 v1, v1\n"
  "v_mfma_f32_16x16x32_bf16 v[12:15], v[148:151], v[44:47], v[12:15]\n"
  "v_exp_f32 v2, v2\n"
  "v_mfma_f32_16x16x32_bf16 v[12:15], v[144:147], v[40:43], v[12:15]\n"
  "v_exp_f32 v3, v3\n"
  "v_mfma_f32_16x16x32_bf16 v[12:15], v[140:143], v[36:39], v[12:15]\n"
  "v_cvt_pk_bf16_f32 v0, v0, v1\n"
  "v_cvt_pk_bf16_f32 v1, v2, v3\n"
  "v_mfma_f32_16x16x32_bf16 v[12:15], v[136:139], v[32:35], v[12:15]\n"
  "v_mfma_f32_16x16x32_bf16 v[16:19], v[160:163], v[32:35], 0\n"
  "v_exp_f32 v4, v4\n"
  "v_mfma_f32_16x16x32_bf16 v[16:19], v[164:167], v[36:39], v[16:19]\n"
  "v_exp_f32 v5, v5\n"
  "v_mfma_f32_16x16x32_bf16 v[16:19], v[168:171], v[40:43], v[16:19]\n"
  "v_exp_f32 v6, v6\n"
  "v_mfma_f32_16x16x32_bf16 v[16:19], v[172:175], v[44:47], v[16:19]\n"
  "v_exp_f32 v7, v7\n"
  "v_mfma_f32_16x16x32_bf16 v[16:19], v[176:179], v[48:51], v[16:19]\n"
  "v_cvt_pk_bf16_f32 v2, v4, v5\n"
  "v_cvt_pk_bf16_f32 v3, v6, v7\n"
  "v_mfma_f32_16x16x32_bf16 v[16:19], v[180:183], v[52:55], v[16:19]\n"
  "ds_write_b128 v80, v[0:3] offset:4096\n"
  "v_pk_fma_f32 v[8:9], v[8:9], s[60:61], s[60:61] op_sel:[0,0,1] op_sel_hi:[1,0,1]\n"
  "v_pk_fma_f32 v[10:11], v[10:11], s[60:61], s[60:61] op_sel:[0,0,1] op_sel_hi:[1,0,1]\n"
  "v_pk_fma_f32 v[12:13], v[12:13], s[60:61], s[60:61] op_sel:[0,0,1] op_sel_hi:[1,0,1]\n"
  "v_pk_fma_f32 v[14:15], v[14:15], s[60:61], s[60:61] op_sel:[0,0,1] op_sel_hi:[1,0,1]\n"
  "v_mfma_f32_16x16x32_bf16 v[20:23], v[204:207], v[52:55], 0\n"
  "v_exp_f32 v8, v8\n"
  "v_mfma_f32_16x16x32_bf16 v[20:23], v[200:203], v[48:51], v[20:23]\n"
  "v_exp_f32 v9, v9\n"
  "v_mfma_f32_16x16x32_bf16 v[20:23], v[196:199], v[44:47], v[20:23]\n"
  "v_exp_f32 v10, v10\n"
  "v_mfma_f32_16x16x32_bf16 v[20:23], v[192:195], v[40:43], v[20:23]\n"
  "v_exp_f32 v11, v11\n"
  "v_mfma_f32_16x16x32_bf16 v[20:23], v[188:191], v[36:39], v[20:23]\n"
  "v_cvt_pk_bf16_f32 v8, v8, v9\n"
  "v_cvt_pk_bf16_f32 v9, v10, v11\n"
  "v_mfma_f32_16x16x32_bf16 v[20:23], v[184:187], v[32:35], v[20:23]\n"
  "v_mfma_f32_16x16x32_bf16 v[24:27], v[208:211], v[32:35], 0\n"
  "v_exp_f32 v12, v12\n"
  "v_mfma_f32_16x16x32_bf16 v[24:27], v[212:215], v[36:39], v[24:27]\n"
  "v_exp_f32 v13, v13\n"
  "v_mfma_f32_16x16x32_bf16 v[24:27], v[216:219], v[40:43], v[24:27]\n"
  "v_exp_f32 v14, v14\n"
  "v_mfma_f32_16x16x32_bf16 v[24:27], v[220:223], v[44:47], v[24:27]\n"
  "v_exp_f32 v15, v15\n"
  "v_mfma_f32_16x16x32_bf16 v[24:27], v[224:227], v[48:51], v[24:27]\n"
  "v_cvt_pk_bf16_f32 v10, v12, v13\n"
  "v_cvt_pk_bf16_f32 v11, v14, v15\n"
  "v_mfma_f32_16x16x32_bf16 v[24:27], v[228:231], v[52:55], v[24:27]\n"
  "ds_write_b128 v80, v[8:11] offset:5120\n"
  "v_pk_fma_f32 v[16:17], v[16:17], s[60:61], s[60:61] op_sel:[0,0,1] op_sel_hi:[1,0,1]\n"
  "v_pk_fma_f32 v[18:19], v[18:19], s[60:61], s[60:61] op_sel:[0,0,1] op_sel_hi:[1,0,1]\n"
  "v_pk_fma_f32 v[20:21], v[20:21], s[60:61], s[60:61] op_sel:[0,0,1] op_sel_hi:[1,0,1]\n"
  "v_pk_fma_f32 v[22:23], v[22:23], s[60:61], s[60:61] op_sel:[0,0,1] op_sel_hi:[1,0,1]\n"
  "v_mfma_f32_16x16x32_bf16 v[28:31], v[252:255], v[52:55], 0\n"
  "v_exp_f32 v16, v16\n"
  "v_mfma_f32_16x16x32_bf16 v[28:31], v[248:251], v[48:51], v[28:31]\n"
  "v_exp_f32 v17, v17\n"
  "v_mfma_f32_16x16x32_bf16 v[28:31], v[244:247], v[44:47], v[28:31]\n"
  "v_exp_f32 v18, v18\n"
  "v_mfma_f32_16x16x32_bf16 v[28:31], v[240:243], v[40:43], v[28:31]\n"
  "s_sub_u32 s56, s56, 1\n"
  "v_exp_f32 v19, v19\n"
  "v_mfma_f32_16x16x32_bf16 v[28:31], v[236:239], v[36:39], v[28:31]\n"
  "s_cmp_eq_u32 s56, 0\n"
  "v_cvt_pk_bf16_f32 v16, v16, v17\n"
  "v_cvt_pk_bf16_f32 v17, v18, v19\n"
  "v_mfma_f32_16x16x32_bf16 v[28:31], v[232:235], v[32:35], v[28:31]\n"
  "s_waitcnt lgkmcnt(3)\n"
  "s_cbranch_scc0 A_H1_%=\n"
  "A_X1_%=:\n"
  "s_mov_b32 s52, s53\n"
  "s_mov_b32 s53, s54\n"
  "s_mov_b32 s54, s55\n"
  "s_cmp_eq_u32 s52, 0\n"
  "s_cbranch_scc1 A_LAST_%=\n"
  "s_lshr_b32 s62, s52, 16\n"
  "s_lshl1_add_u32 s62, s62, s65\n"
  "s_mul_i32 s57, s62, 98304\n"
  "s_and_b32 s56, s52, 0xff\n"
  "s_cmp_lg_u32 s53, 0\n"
  "s_cselect_b32 s62, 1, 0\n"
  "s_sub_u32 s56, s56, s62\n"
  "s_add_u32 s57, s57, 18432\n"
  "v_mfma_f32_16x16x32_bf16 v[0:3], a[0:3], v[56:59], 0\n"
  "s_add_u32 s66, s66, 6144\n"
  "s_cmp_eq_u32 s66, 86016\n"
  "s_cselect_b32 s66, 49152, s66\n"
  "v_exp_f32 v20, v20\n"
  "v_mfma_f32_16x16x32_bf16 v[0:3], a[4:7], v[60:63], v[0:3]\n"
  "v_exp_f32 v21, v21\n"
  "v_mfma_f32_16x16x32_bf16 v[0:3], a[8:11], v[64:67], v[0:3]\n"
  "ds_read_b128 a[0:3], v87 offset:0\n"
  "ds_read_b128 a[4:7], v88 offset:0\n"
  "v_exp_f32 v22, v22\n"
  "v_mfma_f32_16x16x32_bf16 v[0:3], a[12:15], v[68:71], v[0:3]\n"
  "v_exp_f32 v23, v23\n"
  "v_mfma_f32_16x16x32_bf16 v[0:3], a[16:19], v[72:75], v[0:3]\n"
  "v_cvt_pk_bf16_f32 v18, v20, v21\n"
  "v_cvt_pk_bf16_f32 v19, v22, v23\n"
  "v_mfma_f32_16x16x32_bf16 v[0:3], a[20:23], v[76:79], v[0:3]\n"
  "ds_read_b128 a[8:11], v87 offset:2048\n"
  "ds_read_b128 a[12:15], v88 offset:2048\n"
  "ds_write_b128 v80, v[16:19] offset:6144\n"
  "v_pk_fma_f32 v[24:25], v[24:25], s[60:61], s[60:61] op_sel:[0,0,1] op_sel_hi:[1,0,1]\n"
  "v_pk_fma_f32 v[26:27], v[26:27], s[60:61], s[60:61] op_sel:[0,0,1] op_sel_hi:[1,0,1]\n"
  "v_pk_fma_f32 v[28:29], v[28:29], s[60:61], s[60:61] op_sel:[0,0,1] op_sel_hi:[1,0,1]\n"
  "v_pk_fma_f32 v[30:31], v[30:31], s[60:61], s[60:61] op_sel:[0,0,1] op_sel_hi:[1,0,1]\n"
  "v_mfma_f32_16x16x32_bf16 v[4:7], a[44:47], v[76:79], 0\n"
  "v_exp_f32 v24, v24\n"
  "v_mfma_f32_16x16x32_bf16 v[4:7], a[40:43], v[72:75], v[4:7]\n"
  "s_add_u32 m0, s59, -256\n"
  "v_exp_f32 v25, v25\n"
  "v_mfma_f32_16x16x32_bf16 v[4:7], a[36:39], v[68:71], v[4:7]\n"
  "s_waitcnt lgkmcnt(3)\n"
  "buffer_load_dwordx4 v112, s[40:43], s57 offen offset:256 nt lds\n"
  "buffer_load_dwordx4 v113, s[40:43], s57 offen offset:1280 nt lds\n"
  "v_exp_f32 v26, v26\n"
  "v_mfma_f32_16x16x32_bf16 v[4:7], a[32:35], v[64:67], v[4:7]\n"
  "ds_read_b128 a[16:19], v87 offset:4096\n"
  "ds_read_b128 a[20:23], v88 offset:4096\n"
  "v_exp_f32 v27, v27\n"
  "v_mfma_f32_16x16x32_bf16 v[4:7], a[28:31], v[60:63], v[4:7]\n"
  "s_add_u32 m0, s59, 1920\n"
  "v_cvt_pk_bf16_f32 v24, v24, v25\n"
  "v_cvt_pk_bf16_f32 v25, v26, v27\n"
  "v_mfma_f32_16x16x32_bf16 v[4:7], a[24:27], v[56:59], v[4:7]\n"
  "s_waitcnt lgkmcnt(3)\n"
  "buffer_load_dwordx4 v112, s[40:43], s57 offen offset:128 nt lds\n"
  "buffer_load_dwordx4 v113, s[40:43], s57 offen offset:1152 nt lds\n"
  "v_add_u32 v81, s66, v83\n"
  "v_add_u32 v82, s66, v84\n"
  "v_mfma_f32_16x16x32_bf16 v[8:11], a[48:51], v[56:59], 0\n"
  "ds_read_b128 a[40:43], v87 offset:6144\n"
  "ds_read_b128 a[44:47], v88 offset:6144\n"
  "v_exp_f32 v28, v28\n"
  "v_mfma_f32_16x16x32_bf16 v[8:11], a[52:55], v[60:63], v[8:11]\n"
  "s_add_u32 m0, s59, 4096\n"
  "v_exp_f32 v29, v29\n"
  "v_mfma_f32_16x16x32_bf16 v[8:11], a[56:59], v[64:67], v[8:11]\n"
  "s_waitcnt lgkmcnt(2)\n"
  "buffer_load_dwordx4 v112, s[40:43], s57 offen offset:0 nt lds\n"
  "buffer_load_dwordx4 v113, s[40:43], s57 offen offset:1024 nt lds\n"
  "s_add_u32 s57, s57, 6144\n"
  "v_exp_f32 v30, v30\n"
  "v_mfma_f32_16x16x32_bf16 v[8:11], a[60:63], v[68:71], v[8:11]\n"
  "ds_read_b128 a[32:35], v87 offset:8192\n"
  "ds_read_b128 a[36:39], v88 offset:8192\n"
  "v_exp_f32 v31, v31\n"
  "v_mfma_f32_16x16x32_bf16 v[8:11], a[64:67], v[72:75], v[8:11]\n"
  "s_add_u32 m0, s59, 6144\n"
  "v_cvt_pk_bf16_f32 v26, v28, v29\n"
  "v_cvt_pk_bf16_f32 v27, v30, v31\n"
  "v_mfma_f32_16x16x32_bf16 v[8:11], a[68:71], v[76:79], v[8:11]\n"
  "s_waitcnt lgkmcnt(2)\n"
  "buffer_load_dwordx4 v112, s[40:43], s57 offen offset:0 nt lds\n"
  "buffer_load_dwordx4 v113, s[40:43], s57 offen offset:1024 nt lds\n"
  "ds_write_b128 v80, v[24:27] offset:7168\n"
  "v_pk_fma_f32 v[0:1], v[0:1], s[60:61], s[60:61] op_sel:[0,0,1] op_sel_hi:[1,0,1]\n"
  "v_pk_fma_f32 v[2:3], v[2:3], s[60:61], s[60:61] op_sel:[0,0,1] op_sel_hi:[1,0,1]\n"
  "v_pk_fma_f32 v[4:5], v[4:5], s[60:61], s[60:61] op_sel:[0,0,1] op_sel_hi:[1,0,1]\n"
  "v_pk_fma_f32 v[6:7], v[6:7], s[60:61], s[60:61] op_sel:[0,0,1] op_sel_hi:[1,0,1]\n"
  "v_mfma_f32_16x16x32_bf16 v[12:15], a[92:95], v[76:79], 0\n"
  "ds_read_b128 a[24:27], v87 offset:10240\n"
  "ds_read_b128 a[28:31], v88 offset:10240\n"
  "v_exp_f32 v0, v0\n"
  "v_mfma_f32_16x16x32_bf16 v[12:15], a[88:91], v[72:75], v[12:15]\n"
  "s_add_u32 m0, s59, 8064\n"
  "v_exp_f32 v1, v1\n"
  "v_mfma_f32_16x16x32_bf16 v[12:15], a[84:87], v[68:71], v[12:15]\n"
  "s_waitcnt lgkmcnt(3)\n"
  "buffer_load_dwordx4 v112, s[40:43], s57 offen offset:128 nt lds\n"
  "buffer_load_dwordx4 v113, s[40:43], s57 offen offset:1152 nt lds\n"
  "v_exp_f32 v2, v2\n"
  "v_mfma_f32_16x16x32_bf16 v[12:15], a[80:83], v[64:67], v[12:15]\n"
  "ds_read_b128 a[48:51], v87 offset:12288\n"
  "ds_read_b128 a[52:55], v88 offset:12288\n"
  "v_exp_f32 v3, v3\n"
  "v_mfma_f32_16x16x32_bf16 v[12:15], a[76:79], v[60:63], v[12:15]\n"
  "v_cvt_pk_bf16_f32 v0, v0, v1\n"
  "v_cvt_pk_bf16_f32 v1, v2, v3\n"
  "v_mfma_f32_16x16x32_bf16 v[12:15], a[72:75], v[56:59], v[12:15]\n"
  "s_add_u32 m0, s59, 9984\n"
  "s_waitcnt lgkmcnt(4)\n"
  "s_barrier\n"
  "v_xor_b32 v80, 0x4000, v80\n"
  "v_mfma_f32_16x16x32_bf16 v[16:19], a[96:99], v[56:59], 0\n"
  "s_waitcnt lgkmcnt(2)\n"
  "buffer_load_dwordx4 v112, s[40:43], s57 offen offset:256 nt lds\n"
  "buffer_load_dwordx4 v113, s[40:43], s57 offen offset:1280 nt lds\n"
  "s_add_u32 s57, s57, 6144\n"
  "v_exp_f32 v4, v4\n"
  "v_mfma_f32_16x16x32_bf16 v[16:19], a[100:103], v[60:63], v[16:19]\n"
  "ds_read_b128 a[56:59], v87 offset:14336\n"
  "ds_read_b128 a[60:63], v88 offset:14336\n"
  "v_exp_f32 v5, v5\n"
  "v_mfma_f32_16x16x32_bf16 v[16:19], a[104:107], v[64:67], v[16:19]\n"
  "s_add_u32 m0, s59, 12032\n"
  "v_exp_f32 v6, v6\n"
  "v_mfma_f32_16x16x32_bf16 v[16:19], a[108:111], v[68:71], v[16:19]\n"
  "s_waitcnt lgkmcnt(2)\n"
  "buffer_load_dwordx4 v112, s[40:43], s57 offen offset:256 nt lds\n"
  "buffer_load_dwordx4 v113, s[40:43], s57 offen offset:1280 nt lds\n"
  "v_exp_f32 v7, v7\n"
  "v_mfma_f32_16x16x32_bf16 v[16:19], a[112:115], v[72:75], v[16:19]\n"
  "ds_read_b128 a[64:67], v87 offset:16384\n"
  "ds_read_b128 a[68:71], v88 offset:16384\n"
  "v_cvt_pk_bf16_f32 v2, v4, v5\n"
  "v_cvt_pk_bf16_f32 v3, v6, v7\n"
  "v_mfma_f32_16x16x32_bf16 v[16:19], a[116:119], v[76:79], v[16:19]\n"
  "s_add_u32 m0, s59, 14208\n"
  "ds_write_b128 v80, v[0:3] offset:0\n"
  "v_pk_fma_f32 v[8:9], v[8:9], s[60:61], s[60:61] op_sel:[0,0,1] op_sel_hi:[1,0,1]\n"
  "v_pk_fma_f32 v[10:11], v[10:11], s[60:61], s[60:61] op_sel:[0,0,1] op_sel_hi:[1,0,1]\n"
  "v_pk_fma_f32 v[12:13], v[12:13], s[60:61], s[60:61] op_sel:[0,0,1] op_sel_hi:[1,0,1]\n"
  "v_pk_fma_f32 v[14:15], v[14:15], s[60:61], s[60:61] op_sel:[0,0,1] op_sel_hi:[1,0,1]\n"
  "v_mfma_f32_16x16x32_bf16 v[20:23], a[140:143], v[76:79], 0\n"
  "s_waitcnt lgkmcnt(3)\n"
  "buffer_load_dwordx4 v112, s[40:43], s57 offen offset:128 nt lds\n"
  "buffer_load_dwordx4 v113, s[40:43], s57 offen offset:1152 nt lds\n"
  "v_exp_f32 v8, v8\n"
  "v_mfma_f32_16x16x32_bf16 v[20:23], a[136:139], v[72:75], v[20:23]\n"
  "s_waitcnt vmcnt(14)\n"
  "ds_read_b128 a[88:91], v87 offset:0\n"
  "ds_read_b128 a[92:95], v88 offset:0\n"
  "v_exp_f32 v9, v9\n"
  "v_mfma_f32_16x16x32_bf16 v[20:23], a[132:135], v[68:71], v[20:23]\n"
  "s_add_u32 m0, s59, 16384\n"
  "v_exp_f32 v10, v10\n"
  "v_mfma_f32_16x16x32_bf16 v[20:23], a[128:131], v[64:67], v[20:23]\n"
  "s_waitcnt lgkmcnt(3)\n"
  "buffer_load_dwordx4 v112, s[40:43], s57 offen offset:0 nt lds\n"
  "buffer_load_dwordx4 v113, s[40:43], s57 offen offset:1024 nt lds\n"
  "s_add_u32 s57, s57, 6144\n"
  "v_exp_f32 v11, v11\n"
  "v_mfma_f32_16x16x32_bf16 v[20:23], a[124:127], v[60:63], v[20:23]\n"
  "s_waitcnt vmcnt(14)\n"
  "ds_read_b128 a[80:83], v87 offset:2048\n"
  "ds_read_b128 a[84:87], v88 offset:2048\n"
  "v_cvt_pk_bf16_f32 v8, v8, v9\n"
  "v_cvt_pk_bf16_f32 v9, v10, v11\n"
  "v_mfma_f32_16x16x32_bf16 v[20:23], a[120:123], v[56:59], v[20:23]\n"
  "s_add_u32 m0, s59, 0\n"
  "ds_read_b128 v[32:35], v81 offset:0\n"
  "ds_read_b128 v[36:39], v82 offset:0\n"
  "v_mfma_f32_16x16x32_bf16 v[24:27], a[144:147], v[56:59], 0\n"
  "s_waitcnt lgkmcnt(4)\n"
  "buffer_load_dwordx4 v112, s[40:43], s57 offen offset:0 nt lds\n"
  "buffer_load_dwordx4 v113, s[40:43], s57 offen offset:1024 nt lds\n"
  "v_exp_f32 v12, v12\n"
  "v_mfma_f32_16x16x32_bf16 v[24:27], a[148:151], v[60:63], v[24:27]\n"
  "s_waitcnt vmcnt(14)\n"
  "ds_read_b128 a[72:75], v87 offset:4096\n"
  "ds_read_b128 a[76:79], v88 offset:4096\n"
  "v_exp_f32 v13, v13\n"
  "v_mfma_f32_16x16x32_bf16 v[24:27], a[152:155], v[64:67], v[24:27]\n"
  "v_exp_f32 v14, v14\n"
  "v_mfma_f32_16x16x32_bf16 v[24:27], a[156:159], v[68:71], v[24:27]\n"
  "s_add_u32 m0, s59, 1920\n"
  "v_exp_f32 v15, v15\n"
  "v_mfma_f32_16x16x32_bf16 v[24:27], a[160:163], v[72:75], v[24:27]\n"
  "s_waitcnt lgkmcnt(4)\n"
  "buffer_load_dwordx4 v112, s[40:43], s57 offen offset:128 nt lds\n"
  "buffer_load_dwordx4 v113, s[40:43], s57 offen offset:1152 nt lds\n"
  "v_cvt_pk_bf16_f32 v10, v12, v13\n"
  "v_cvt_pk_bf16_f32 v11, v14, v15\n"
  "v_mfma_f32_16x16x32_bf16 v[24:27], a[164:167], v[76:79], v[24:27]\n"
  "s_waitcnt vmcnt(14)\n"
  "ds_read_b128 a[96:99], v87 offset:6144\n"
  "ds_read_b128 a[100:103], v88 offset:6144\n"
  "ds_write_b128 v80, v[8:11] offset:1024\n"
  "v_pk_fma_f32 v[16:17], v[16:17], s[60:61], s[60:61] op_sel:[0,0,1] op_sel_hi:[1,0,1]\n"
  "v_pk_fma_f32 v[18:19], v[18:19], s[60:61], s[60:61] op_sel:[0,0,1] op_sel_hi:[1,0,1]\n"
  "v_pk_fma_f32 v[20:21], v[20:21], s[60:61], s[60:61] op_sel:[0,0,1] op_sel_hi:[1,0,1]\n"
  "v_pk_fma_f32 v[22:23], v[22:23], s[60:61], s[60:61] op_sel:[0,0,1] op_sel_hi:[1,0,1]\n"
  "v_mfma_f32_16x16x32_bf16 v[28:31], a[188:191], v[76:79], 0\n"
  "s_add_u32 m0, s59, 3840\n"
  "v_exp_f32 v16, v16\n"
  "v_mfma_f32_16x16x32_bf16 v[28:31], a[184:187], v[72:75], v[28:31]\n"
  "s_waitcnt lgkmcnt(3)\n"
  "buffer_load_dwordx4 v112, s[40:43], s57 offen offset:256 nt lds\n"
  "buffer_load_dwordx4 v113, s[40:43], s57 offen offset:1280 nt lds\n"
  "s_add_u32 s57, s57, 6144\n"
  "v_exp_f32 v17, v17\n"
  "v_mfma_f32_16x16x32_bf16 v[28:31], a[180:183], v[68:71], v[28:31]\n"
  "s_waitcnt vmcnt(14)\n"
  "ds_read_b128 a[104:107], v87 offset:8192\n"
  "ds_read_b128 a[108:111], v88 offset:8192\n"
  "v_exp_f32 v18, v18\n"
  "v_mfma_f32_16x16x32_bf16 v[28:31], a[176:179], v[64:67], v[28:31]\n"
  "s_add_u32 m0, s59, 5888\n"
  "v_exp_f32 v19, v19\n"
  "v_mfma_f32_16x16x32_bf16 v[28:31], a[172:175], v[60:63], v[28:31]\n"
  "s_waitcnt lgkmcnt(3)\n"
  "buffer_load_dwordx4 v112, s[40:43], s57 offen offset:256 nt lds\n"
  "buffer_load_dwordx4 v113, s[40:43], s57 offen offset:1280 nt lds\n"
  "v_cvt_pk_bf16_f32 v16, v16, v17\n"
  "v_cvt_pk_bf16_f32 v17, v18, v19\n"
  "v_mfma_f32_16x16x32_bf16 v[28:31], a[168:171], v[56:59], v[28:31]\n"
  "s_waitcnt vmcnt(14)\n"
  "ds_read_b128 a[112:115], v87 offset:10240\n"
  "ds_read_b128 a[116:119], v88 offset:10240\n"
  "ds_read_b128 v[40:43], v81 offset:2048\n"
  "ds_read_b128 v[44:47], v82 offset:2048\n"
  "v_mfma_f32_16x16x32_bf16 v[0:3], a[192:195], v[56:59], 0\n"
  "s_add_u32 m0, s59, 8064\n"
  "v_exp_f32 v20, v20\n"
  "v_mfma_f32_16x16x32_bf16 v[0:3], a[196:199], v[60:63], v[0:3]\n"
  "s_waitcnt lgkmcnt(4)\n"
  "buffer_load_dwordx4 v112, s[40:43], s57 offen offset:128 nt lds\n"
  "buffer_load_dwordx4 v113, s[40:43], s57 offen offset:1152 nt lds\n"
  "v_exp_f32 v21, v21\n"
  "v_mfma_f32_16x16x32_bf16 v[0:3], a[200:203], v[64:67], v[0:3]\n"
  "s_waitcnt vmcnt(14)\n"
  "ds_read_b128 a[136:139], v87 offset:12288\n"
  "ds_read_b128 a[140:143], v88 offset:12288\n"
  "v_exp_f32 v22, v22\n"
  "v_mfma_f32_16x16x32_bf16 v[0:3], a[204:207], v[68:71], v[0:3]\n"
  "s_add_u32 m0, s59, 10240\n"
  "v_exp_f32 v23, v23\n"
  "v_mfma_f32_16x16x32_bf16 v[0:3], a[208:211], v[72:75], v[0:3]\n"
  "s_waitcnt lgkmcnt(4)\n"
  "buffer_load_dwordx4 v112, s[40:43], s57 offen offset:0 nt lds\n"
  "buffer_load_dwordx4 v113, s[40:43], s57 offen offset:1024 nt lds\n"
  "s_add_u32 s57, s57, 6144\n"
  "v_cvt_pk_bf16_f32 v18, v20, v21\n"
  "v_cvt_pk_bf16_f32 v19, v22, v23\n"
  "v_mfma_f32_16x16x32_bf16 v[0:3], a[212:215], v[76:79], v[0:3]\n"
  "s_waitcnt vmcnt(14)\n"
  "ds_read_b128 a[128:131], v87 offset:14336\n"
  "ds_read_b128 a[132:135], v88 offset:14336\n"
  "ds_write_b128 v80, v[16:19] offset:2048\n"
  "v_pk_fma_f32 v[24:25], v[24:25], s[60:61], s[60:61] op_sel:[0,0,1] op_sel_hi:[1,0,1]\n"
  "v_pk_fma_f32 v[26:27], v[26:27], s[60:61], s[60:61] op_sel:[0,0,1] op_sel_hi:[1,0,1]\n"
  "v_pk_fma_f32 v[28:29], v[28:29], s[60:61], s[60:61] op_sel:[0,0,1] op_sel_hi:[1,0,1]\n"
  "v_pk_fma_f32 v[30:31], v[30:31], s[60:61], s[60:61] op_sel:[0,0,1] op_sel_hi:[1,0,1]\n"
  "v_mfma_f32_16x16x32_bf16 v[4:7], a[236:239], v[76:79], 0\n"
  "v_exp_f32 v24, v24\n"
  "v_mfma_f32_16x16x32_bf16 v[4:7], a[232:235], v[72:75], v[4:7]\n"
  "s_add_u32 m0, s59, 12288\n"
  "v_exp_f32 v25, v25\n"
  "v_mfma_f32_16x16x32_bf16 v[4:7], a[228:231], v[68:71], v[4:7]\n"
  "s_waitcnt lgkmcnt(3)\n"
  "buffer_load_dwordx4 v112, s[40:43], s57 offen offset:0 nt lds\n"
  "buffer_load_dwordx4 v113, s[40:43], s57 offen offset:1024 nt lds\n"
  "v_exp_f32 v26, v26\n"
  "v_mfma_f32_16x16x32_bf16 v[4:7], a[224:227], v[64:67], v[4:7]\n"
  "s_waitcnt vmcnt(14)\n"
  "ds_read_b128 a[120:123], v87 offset:16384\n"
  "ds_read_b128 a[124:127], v88 offset:16384\n"
  "v_exp_f32 v27, v27\n"
  "v_mfma_f32_16x16x32_bf16 v[4:7], a[220:223], v[60:63], v[4:7]\n"
  "s_add_u32 m0, s59, 14208\n"
  "v_cvt_pk_bf16_f32 v24, v24, v25\n"
  "v_cvt_pk_bf16_f32 v25, v26, v27\n"
  "v_mfma_f32_16x16x32_bf16 v[4:7], a[216:219], v[56:59], v[4:7]\n"
  "s_waitcnt lgkmcnt(3)\n"
  "buffer_load_dwordx4 v112, s[40:43], s57 offen offset:128 nt lds\n"
  "buffer_load_dwordx4 v113, s[40:43], s57 offen offset:1152 nt lds\n"
  "ds_read_b128 v[48:51], v81 offset:4096\n"
  "ds_read_b128 v[52:55], v82 offset:4096\n"
  "v_mfma_f32_16x16x32_bf16 v[8:11], a[240:243], v[56:59], 0\n"
  "s_waitcnt vmcnt(14)\n"
  "ds_read_b128 a[144:147], v87 offset:0\n"
  "ds_read_b128 a[148:151], v88 offset:0\n"
  "v_exp_f32 v28, v28\n"
  "v_mfma_f32_16x16x32_bf16 v[8:11], a[244:247], v[60:63], v[8:11]\n"
  "s_add_u32 m0, s59, 16128\n"
  "v_exp_f32 v29, v29\n"
  "v_mfma_f32_16x16x32_bf16 v[8:11], a[248:251], v[64:67], v[8:11]\n"
  "s_waitcnt lgkmcnt(4)\n"
  "buffer_load_dwordx4 v112, s[40:43], s57 offen offset:256 nt lds\n"
  "buffer_load_dwordx4 v113, s[40:43], s57 offen offset:1280 nt lds\n"
  "s_add_u32 s57, s57, 6144\n"
  "v_exp_f32 v30, v30\n"
  "v_mfma_f32_16x16x32_bf16 v[8:11], a[252:255], v[68:71], v[8:11]\n"
  "s_waitcnt vmcnt(14)\n"
  "ds_read_b128 a[152:155], v87 offset:2048\n"
  "ds_read_b128 a[156:159], v88 offset:2048\n"
  "v_exp_f32 v31, v31\n"
  "v_mfma_f32_16x16x32_bf16 v[8:11], v[128:131], v[72:75], v[8:11]\n"
  "s_add_u32 m0, s59, -256\n"
  "v_cvt_pk_bf16_f32 v26, v28, v29\n"
  "v_cvt_pk_bf16_f32 v27, v30, v31\n"
  "v_mfma_f32_16x16x32_bf16 v[8:11], v[132:135], v[76:79], v[8:11]\n"
  "s_waitcnt lgkmcnt(2)\n"
  "buffer_load_dwordx4 v112, s[40:43], s57 offen offset:256 nt lds\n"
  "buffer_load_dwordx4 v113, s[40:43], s57 offen offset:1280 nt lds\n"
  "ds_write_b128 v80, v[24:27] offset:3072\n"
  "v_pk_fma_f32 v[0:1], v[0:1], s[60:61], s[60:61] op_sel:[0,0,1] op_sel_hi:[1,0,1]\n"
  "v_pk_fma_f32 v[2:3], v[2:3], s[60:61], s[60:61] op_sel:[0,0,1] op_sel_hi:[1,0,1]\n"
  "v_pk_fma_f32 v[4:5], v[4:5], s[60:61], s[60:61] op_sel:[0,0,1] op_sel_hi:[1,0,1]\n"
  "v_pk_fma_f32 v[6:7], v[6:7], s[60:61], s[60:61] op_sel:[0,0,1] op_sel_hi:[1,0,1]\n"
  "v_mfma_f32_16x16x32_bf16 v[12:15], v[156:159], v[76:79], 0\n"
  "s_waitcnt vmcnt(14)\n"
  "ds_read_b128 a[160:163], v87 offset:4096\n"
  "ds_read_b128 a[164:167], v88 offset:4096\n"
  "v_exp_f32 v0, v0\n"
  "v_mfma_f32_16x16x32_bf16 v[12:15], v[152:155], v[72:75], v[12:15]\n"
  "s_add_u32 m0, s59, 1920\n"
  "v_exp_f32 v1, v1\n"
  "v_mfma_f32_16x16x32_bf16 v[12:15], v[148:151], v[68:71], v[12:15]\n"
  "s_waitcnt lgkmcnt(3)\n"
  "buffer_load_dwordx4 v112, s[40:43], s57 offen offset:128 nt lds\n"
  "buffer_load_dwordx4 v113, s[40:43], s57 offen offset:1152 nt lds\n"
  "v_exp_f32 v2, v2\n"
  "v_mfma_f32_16x16x32_bf16 v[12:15], v[144:147], v[64:67], v[12:15]\n"
  "s_waitcnt vmcnt(14)\n"
  "ds_read_b128 a[184:187], v87 offset:6144\n"
  "ds_read_b128 a[188:191], v88 offset:6144\n"
  "v_exp_f32 v3, v3\n"
  "v_mfma_f32_16x16x32_bf16 v[12:15], v[140:143], v[60:63], v[12:15]\n"
  "v_cvt_pk_bf16_f32 v0, v0, v1\n"
  "v_cvt_pk_bf16_f32 v1, v2, v3\n"
  "v_mfma_f32_16x16x32_bf16 v[12:15], v[136:139], v[56:59], v[12:15]\n"
  "s_add_u32 m0, s59, 4096\n"
  "v_mfma_f32_16x16x32_bf16 v[16:19], v[160:163], v[56:59], 0\n"
  "s_waitcnt lgkmcnt(2)\n"
  "buffer_load_dwordx4 v112, s[40:43], s57 offen offset:0 nt lds\n"
  "buffer_load_dwordx4 v113, s[40:43], s57 offen offset:1024 nt lds\n"
  "s_add_u32 s57, s57, 6144\n"
  "v_exp_f32 v4, v4\n"
  "v_mfma_f32_16x16x32_bf16 v[16:19], v[164:167], v[60:63], v[16:19]\n"
  "s_waitcnt vmcnt(14)\n"
  "ds_read_b128 a[176:179], v87 offset:8192\n"
  "ds_read_b128 a[180:183], v88 offset:8192\n"
  "v_exp_f32 v5, v5\n"
  "v_mfma_f32_16x16x32_bf16 v[16:19], v[168:171], v[64:67], v[16:19]\n"
  "s_add_u32 m0, s59, 6144\n"
  "v_exp_f32 v6, v6\n"
  "v_mfma_f32_16x16x32_bf16 v[16:19], v[172:175], v[68:71], v[16:19]\n"
  "s_waitcnt lgkmcnt(2)\n"
  "buffer_load_dwordx4 v112, s[40:43], s57 offen offset:0 nt lds\n"
  "buffer_load_dwordx4 v113, s[40:43], s57 offen offset:1024 nt lds\n"
  "v_exp_f32 v7, v7\n"
  "v_mfma_f32_16x16x32_bf16 v[16:19], v[176:179], v[72:75], v[16:19]\n"
  "s_waitcnt vmcnt(14)\n"
  "ds_read_b128 a[168:171], v87 offset:10240\n"
  "ds_read_b128 a[172:175], v88 offset:10240\n"
  "v_cvt_pk_bf16_f32 v2, v4, v5\n"
  "v_cvt_pk_bf16_f32 v3, v6, v7\n"
  "v_mfma_f32_16x16x32_bf16 v[16:19], v[180:183], v[76:79], v[16:19]\n"
  "s_add_u32 m0, s59, 8064\n"
  "ds_write_b128 v80, v[0:3] offset:4096\n"
  "v_pk_fma_f32 v[8:9], v[8:9], s[60:61], s[60:61] op_sel:[0,0,1] op_sel_hi:[1,0,1]\n"
  "v_pk_fma_f32 v[10:11], v[10:11], s[60:61], s[60:61] op_sel:[0,0,1] op_sel_hi:[1,0,1]\n"
  "v_pk_fma_f32 v[12:13], v[12:13], s[60:61], s[60:61] op_sel:[0,0,1] op_sel_hi:[1,0,1]\n"
  "v_pk_fma_f32 v[14:15], v[14:15], s[60:61], s[60:61] op_sel:[0,0,1] op_sel_hi:[1,0,1]\n"
  "v_mfma_f32_16x16x32_bf16 v[20:23], v[204:207], v[76:79], 0\n"
  "s_waitcnt lgkmcnt(3)\n"
  "buffer_load_dwordx4 v112, s[40:43], s57 offen offset:128 nt lds\n"
  "buffer_load_dwordx4 v113, s[40:43], s57 offen offset:1152 nt lds\n"
  "v_exp_f32 v8, v8\n"
  "v_mfma_f32_16x16x32_bf16 v[20:23], v[200:203], v[72:75], v[20:23]\n"
  "s_waitcnt vmcnt(14)\n"
  "ds_read_b128 a[192:195], v87 offset:12288\n"
  "ds_read_b128 a[196:199], v88 offset:12288\n"
  "v_exp_f32 v9, v9\n"
  "v_mfma_f32_16x16x32_bf16 v[20:23], v[196:199], v[68:71], v[20:23]\n"
  "s_add_u32 m0, s59, 9984\n"
  "v_exp_f32 v10, v10\n"
  "v_mfma_f32_16x16x32_bf16 v[20:23], v[192:195], v[64:67], v[20:23]\n"
  "s_waitcnt lgkmcnt(3)\n"
  "buffer_load_dwordx4 v112, s[40:43], s57 offen offset:256 nt lds\n"
  "buffer_load_dwordx4 v113, s[40:43], s57 offen offset:1280 nt lds\n"
  "s_add_u32 s57, s57, 6144\n"
  "v_exp_f32 v11, v11\n"
  "v_mfma_f32_16x16x32_bf16 v[20:23], v[188:191], v[60:63], v[20:23]\n"
  "s_waitcnt vmcnt(14)\n"
  "ds_read_b128 a[200:203], v87 offset:14336\n"
  "ds_read_b128 a[204:207], v88 offset:14336\n"
  "v_cvt_pk_bf16_f32 v8, v8, v9\n"
  "v_cvt_pk_bf16_f32 v9, v10, v11\n"
  "v_mfma_f32_16x16x32_bf16 v[20:23], v[184:187], v[56:59], v[20:23]\n"
  "s_add_u32 m0, s59, 12032\n"
  "v_mfma_f32_16x16x32_bf16 v[24:27], v[208:211], v[56:59], 0\n"
  "s_waitcnt lgkmcnt(2)\n"
  "buffer_load_dwordx4 v112, s[40:43], s57 offen offset:256 nt lds\n"
  "buffer_load_dwordx4 v113, s[40:43], s57 offen offset:1280 nt lds\n"
  "v_exp_f32 v12, v12\n"
  "v_mfma_f32_16x16x32_bf16 v[24:27], v[212:215], v[60:63], v[24:27]\n"
  "s_waitcnt vmcnt(14)\n"
  "ds_read_b128 a[208:211], v87 offset:16384\n"
  "ds_read_b128 a[212:215], v88 offset:16384\n"
  "v_exp_f32 v13, v13\n"
  "v_mfma_f32_16x16x32_bf16 v[24:27], v[216:219], v[64:67], v[24:27]\n"
  "v_exp_f32 v14, v14\n"
  "v_mfma_f32_16x16x32_bf16 v[24:27], v[220:223], v[68:71], v[24:27]\n"
  "s_add_u32 m0, s59, 14208\n"
  "v_exp_f32 v15, v15\n"
  "v_mfma_f32_16x16x32_bf16 v[24:27], v[224:227], v[72:75], v[24:27]\n"
  "s_waitcnt lgkmcnt(2)\n"
  "buffer_load_dwordx4 v112, s[40:43], s57 offen offset:128 nt lds\n"
  "buffer_load_dwordx4 v113, s[40:43], s57 offen offset:1152 nt lds\n"
  "v_cvt_pk_bf16_f32 v10, v12, v13\n"
  "v_cvt_pk_bf16_f32 v11, v14, v15\n"
  "v_mfma_f32_16x16x32_bf16 v[24:27], v[228:231], v[76:79], v[24:27]\n"
  "s_waitcnt vmcnt(14)\n"
  "ds_read_b128 a[232:235], v87 offset:0\n"
  "ds_read_b128 a[236:239], v88 offset:0\n"
  "ds_write_b128 v80, v[8:11] offset:5120\n"
  "v_pk_fma_f32 v[16:17], v[16:17], s[60:61], s[60:61] op_sel:[0,0,1] op_sel_hi:[1,0,1]\n"
  "v_pk_fma_f32 v[18:19], v[18:19], s[60:61], s[60:61] op_sel:[0,0,1] op_sel_hi:[1,0,1]\n"
  "v_pk_fma_f32 v[20:21], v[20:21], s[60:61], s[60:61] op_sel:[0,0,1] op_sel_hi:[1,0,1]\n"
  "v_pk_fma_f32 v[22:23], v[22:23], s[60:61], s[60:61] op_sel:[0,0,1] op_sel_hi:[1,0,1]\n"
  "v_mfma_f32_16x16x32_bf16 v[28:31], v[252:255], v[76:79], 0\n"
  "s_add_u32 m0, s59, 16384\n"
  "v_exp_f32 v16, v16\n"
  "v_mfma_f32_16x16x32_bf16 v[28:31], v[248:251], v[72:75], v[28:31]\n"
  "s_waitcnt lgkmcnt(3)\n"
  "buffer_load_dwordx4 v112, s[40:43], s57 offen offset:0 nt lds\n"
  "buffer_load_dwordx4 v113, s[40:43], s57 offen offset:1024 nt lds\n"
  "s_add_u32 s57, s57, 6144\n"
  "v_exp_f32 v17, v17\n"
  "v_mfma_f32_16x16x32_bf16 v[28:31], v[244:247], v[68:71], v[28:31]\n"
  "s_waitcnt vmcnt(14)\n"
  "ds_read_b128 a[224:227], v87 offset:2048\n"
  "ds_read_b128 a[228:231], v88 offset:2048\n"
  "v_exp_f32 v18, v18\n"
  "v_mfma_f32_16x16x32_bf16 v[28:31], v[240:243], v[64:67], v[28:31]\n"
  "s_add_u32 m0, s59, 0\n"
  "v_exp_f32 v19, v19\n"
  "v_mfma_f32_16x16x32_bf16 v[28:31], v[236:239], v[60:63], v[28:31]\n"
  "s_waitcnt lgkmcnt(3)\n"
  "buffer_load_dwordx4 v112, s[40:43], s57 offen offset:0 nt lds\n"
  "buffer_load_dwordx4 v113, s[40:43], s57 offen offset:1024 nt lds\n"
  "v_cvt_pk_bf16_f32 v16, v16, v17\n"
  "v_cvt_pk_bf16_f32 v17, v18, v19\n"
  "v_mfma_f32_16x16x32_bf16 v[28:31], v[232:235], v[56:59], v[28:31]\n"
  "s_waitcnt vmcnt(14)\n"
  "ds_read_b128 a[216:219], v87 offset:4096\n"
  "ds_read_b128 a[220:223], v88 offset:4096\n"
  "v_mfma_f32_16x16x32_bf16 v[0:3], a[0:3], v[32:35], 0\n"
  "s_add_u32 s66, s66, 6144\n"
  "s_cmp_eq_u32 s66, 86016\n"
  "s_cselect_b32 s66, 49152, s66\n"
  "s_add_u32 m0, s59, 1920\n"
  "v_exp_f32 v20, v20\n"
  "v_mfma_f32_16x16x32_bf16 v[0:3], a[4:7], v[36:39], v[0:3]\n"
  "s_waitcnt lgkmcnt(2)\n"
  "buffer_load_dwordx4 v112, s[40:43], s57 offen offset:128 nt lds\n"
  "buffer_load_dwordx4 v113, s[40:43], s57 offen offset:1152 nt lds\n"
  "v_exp_f32 v21, v21\n"
  "v_mfma_f32_16x16x32_bf16 v[0:3], a[8:11], v[40:43], v[0:3]\n"
  "s_waitcnt vmcnt(14)\n"
  "ds_read_b128 a[240:243], v87 offset:6144\n"
  "ds_read_b128 a[244:247], v88 offset:6144\n"
  "v_exp_f32 v22, v22\n"
  "v_mfma_f32_16x16x32_bf16 v[0:3], a[12:15], v[44:47], v[0:3]\n"
  "s_add_u32 m0, s59, 3840\n"
  "v_exp_f32 v23, v23\n"
  "v_mfma_f32_16x16x32_bf16 v[0:3], a[16:19], v[48:51], v[0:3]\n"
  "s_waitcnt lgkmcnt(2)\n"
  "buffer_load_dwordx4 v112, s[40:43], s57 offen offset:256 nt lds\n"
  "buffer_load_dwordx4 v113, s[40:43], s57 offen offset:1280 nt lds\n"
  "s_add_u32 s57, s57, 6144\n"
  "v_cvt_pk_bf16_f32 v18, v20, v21\n"
  "v_cvt_pk_bf16_f32 v19, v22, v23\n"
  "v_mfma_f32_16x16x32_bf16 v[0:3], a[20:23], v[52:55], v[0:3]\n"
  "s_waitcnt vmcnt(14)\n"
  "ds_read_b128 a[248:251], v87 offset:8192\n"
  "ds_read_b128 a[252:255], v88 offset:8192\n"
  "ds_write_b128 v80, v[16:19] offset:6144\n"
  "v_pk_fma_f32 v[24:25], v[24:25], s[60:61], s[60:61] op_sel:[0,0,1] op_sel_hi:[1,0,1]\n"
  "v_pk_fma_f32 v[26:27], v[26:27], s[60:61], s[60:61] op_sel:[0,0,1] op_sel_hi:[1,0,1]\n"
  "v_pk_fma_f32 v[28:29], v[28:29], s[60:61], s[60:61] op_sel:[0,0,1] op_sel_hi:[1,0,1]\n"
  "v_pk_fma_f32 v[30:31], v[30:31], s[60:61], s[60:61] op_sel:[0,0,1] op_sel_hi:[1,0,1]\n"
  "v_mfma_f32_16x16x32_bf16 v[4:7], a[44:47], v[52:55], 0\n"
  "v_exp_f32 v24, v24\n"
  "v_mfma_f32_16x16x32_bf16 v[4:7], a[40:43], v[48:51], v[4:7]\n"
  "s_add_u32 m0, s59, 5888\n"
  "v_exp_f32 v25, v25\n"
  "v_mfma_f32_16x16x32_bf16 v[4:7], a[36:39], v[44:47], v[4:7]\n"
  "s_waitcnt lgkmcnt(3)\n"
  "buffer_load_dwordx4 v112, s[40:43], s57 offen offset:256 nt lds\n"
  "buffer_load_dwordx4 v113, s[40:43], s57 offen offset:1280 nt lds\n"
  "v_exp_f32 v26, v26\n"
  "v_mfma_f32_16x16x32_bf16 v[4:7], a[32:35], v[40:43], v[4:7]\n"
  "s_waitcnt vmcnt(14)\n"
  "ds_read_b128 v[128:131], v87 offset:10240\n"
  "ds_read_b128 v[132:135], v88 offset:10240\n"
  "v_exp_f32 v27, v27\n"
  "v_mfma_f32_16x16x32_bf16 v[4:7], a[28:31], v[36:39], v[4:7]\n"
  "s_add_u32 m0, s59, 8064\n"
  "v_cvt_pk_bf16_f32 v24, v24, v25\n"
  "v_cvt_pk_bf16_f32 v25, v26, v27\n"
  "v_mfma_f32_16x16x32_bf16 v[4:7], a[24:27], v[32:35], v[4:7]\n"
  "s_waitcnt lgkmcnt(3)\n"
  "buffer_load_dwordx4 v112, s[40:43], s57 offen offset:128 nt lds\n"
  "buffer_load_dwordx4 v113, s[40:43], s57 offen offset:1152 nt lds\n"
  "v_add_u32 v81, s66, v83\n"
  "v_add_u32 v82, s66, v84\n"
  "v_mfma_f32_16x16x32_bf16 v[8:11], a[48:51], v[32:35], 0\n"
  "s_waitcnt vmcnt(14)\n"
  "ds_read_b128 v[152:155], v87 offset:12288\n"
  "ds_read_b128 v[156:159], v88 offset:12288\n"
  "v_exp_f32 v28, v28\n"
  "v_mfma_f32_16x16x32_bf16 v[8:11], a[52:55], v[36:39], v[8:11]\n"
  "s_add_u32 m0, s59, 10240\n"
  "v_exp_f32 v29, v29\n"
  "v_mfma_f32_16x16x32_bf16 v[8:11], a[56:59], v[40:43], v[8:11]\n"
  "s_waitcnt lgkmcnt(2)\n"
  "buffer_load_dwordx4 v112, s[40:43], s57 offen offset:0 nt lds\n"
  "buffer_load_dwordx4 v113, s[40:43], s57 offen offset:1024 nt lds\n"
  "s_add_u32 s57, s57, 6144\n"
  "v_exp_f32 v30, v30\n"
  "v_mfma_f32_16x16x32_bf16 v[8:11], a[60:63], v[44:47], v[8:11]\n"
  "s_waitcnt vmcnt(14)\n"
  "ds_read_b128 v[144:147], v87 offset:14336\n"
  "ds_read_b128 v[148:151], v88 offset:14336\n"
  "v_exp_f32 v31, v31\n"
  "v_mfma_f32_16x16x32_bf16 v[8:11], a[64:67], v[48:51], v[8:11]\n"
  "s_add_u32 m0, s59, 12288\n"
  "v_cvt_pk_bf16_f32 v26, v28, v29\n"
  "v_cvt_pk_bf16_f32 v27, v30, v31\n"
  "v_mfma_f32_16x16x32_bf16 v[8:11], a[68:71], v[52:55], v[8:11]\n"
  "s_waitcnt lgkmcnt(2)\n"
  "buffer_load_dwordx4 v112, s[40:43], s57 offen offset:0 nt lds\n"
  "buffer_load_dwordx4 v113, s[40:43], s57 offen offset:1024 nt lds\n"
  "ds_write_b128 v80, v[24:27] offset:7168\n"
  "v_pk_fma_f32 v[0:1], v[0:1], s[60:61], s[60:61] op_sel:[0,0,1] op_sel_hi:[1,0,1]\n"
  "v_pk_fma_f32 v[2:3], v[2:3], s[60:61], s[60:61] op_sel:[0,0,1] op_sel_hi:[1,0,1]\n"
  "v_pk_fma_f32 v[4:5], v[4:5], s[60:61], s[60:61] op_sel:[0,0,1] op_sel_hi:[1,0,1]\n"
  "v_pk_fma_f32 v[6:7], v[6:7], s[60:61], s[60:61] op_sel:[0,0,1] op_sel_hi:[1,0,1]\n"
  "v_mfma_f32_16x16x32_bf16 v[12:15], a[92:95], v[52:55], 0\n"
  "s_waitcnt vmcnt(14)\n"
  "ds_read_b128 v[136:139], v87 offset:16384\n"
  "ds_read_b128 v[140:143], v88 offset:16384\n"
  "v_exp_f32 v0, v0\n"
  "v_mfma_f32_16x16x32_bf16 v[12:15], a[88:91], v[48:51], v[12:15]\n"
  "s_add_u32 m0, s59, 14208\n"
  "v_exp_f32 v1, v1\n"
  "v_mfma_f32_16x16x32_bf16 v[12:15], a[84:87], v[44:47], v[12:15]\n"
  "s_waitcnt lgkmcnt(3)\n"
  "buffer_load_dwordx4 v112, s[40:43], s57 offen offset:128 nt lds\n"
  "buffer_load_dwordx4 v113, s[40:43], s57 offen offset:1152 nt lds\n"
  "v_exp_f32 v2, v2\n"
  "v_mfma_f32_16x16x32_bf16 v[12:15], a[80:83], v[40:43], v[12:15]\n"
  "s_waitcnt vmcnt(14)\n"
  "ds_read_b128 v[160:163], v87 offset:0\n"
  "ds_read_b128 v[164:167], v88 offset:0\n"
  "v_exp_f32 v3, v3\n"
  "v_mfma_f32_16x16x32_bf16 v[12:15], a[76:79], v[36:39], v[12:15]\n"
  "v_cvt_pk_bf16_f32 v0, v0, v1\n"
  "v_cvt_pk_bf16_f32 v1, v2, v3\n"
  "v_mfma_f32_16x16x32_bf16 v[12:15], a[72:75], v[32:35], v[12:15]\n"
  "s_add_u32 m0, s59, 16128\n"
  "s_waitcnt lgkmcnt(4)\n"
  "s_barrier\n"
  "v_xor_b32 v80, 0x4000, v80\n"
  "v_mfma_f32_16x16x32_bf16 v[16:19], a[96:99], v[32:35], 0\n"
  "s_waitcnt lgkmcnt(2)\n"
  "buffer_load_dwordx4 v112, s[40:43], s57 offen offset:256 nt lds\n"
  "buffer_load_dwordx4 v113, s[40:43], s57 offen offset:1280 nt lds\n"
  "s_add_u32 s57, s57, 6144\n"
  "v_exp_f32 v4, v4\n"
  "v_mfma_f32_16x16x32_bf16 v[16:19], a[100:103], v[36:39], v[16:19]\n"
  "s_waitcnt vmcnt(14)\n"
  "ds_read_b128 v[168:171], v87 offset:2048\n"
  "ds_read_b128 v[172:175], v88 offset:2048\n"
  "v_exp_f32 v5, v5\n"
  "v_mfma_f32_16x16x32_bf16 v[16:19], a[104:107], v[40:43], v[16:19]\n"
  "s_add_u32 m0, s59, -256\n"
  "v_exp_f32 v6, v6\n"
  "v_mfma_f32_16x16x32_bf16 v[16:19], a[108:111], v[44:47], v[16:19]\n"
  "s_waitcnt lgkmcnt(2)\n"
  "buffer_load_dwordx4 v112, s[40:43], s57 offen offset:256 nt lds\n"
  "buffer_load_dwordx4 v113, s[40:43], s57 offen offset:1280 nt lds\n"
  "v_exp_f32 v7, v7\n"
  "v_mfma_f32_16x16x32_bf16 v[16:19], a[112:115], v[48:51], v[16:19]\n"
  "s_waitcnt vmcnt(14)\n"
  "ds_read_b128 v[176:179], v87 offset:4096\n"
  "ds_read_b128 v[180:183], v88 offset:4096\n"
  "v_cvt_pk_bf16_f32 v2, v4, v5\n"
  "v_cvt_pk_bf16_f32 v3, v6, v7\n"
  "v_mfma_f32_16x16x32_bf16 v[16:19], a[116:119], v[52:55], v[16:19]\n"
  "s_add_u32 m0, s59, 1920\n"
  "ds_write_b128 v80, v[0:3] offset:0\n"
  "v_pk_fma_f32 v[8:9], v[8:9], s[60:61], s[60:61] op_sel:[0,0,1] op_sel_hi:[1,0,1]\n"
  "v_pk_fma_f32 v[10:11], v[10:11], s[60:61], s[60:61] op_sel:[0,0,1] op_sel_hi:[1,0,1]\n"
  "v_pk_fma_f32 v[12:13], v[12:13], s[60:61], s[60:61] op_sel:[0,0,1] op_sel_hi:[1,0,1]\n"
  "v_pk_fma_f32 v[14:15], v[14:15], s[60:61], s[60:61] op_sel:[0,0,1] op_sel_hi:[1,0,1]\n"
  "v_mfma_f32_16x16x32_bf16 v[20:23], a[140:143], v[52:55], 0\n"
  "s_waitcnt lgkmcnt(3)\n"
  "buffer_load_dwordx4 v112, s[40:43], s57 offen offset:128 nt lds\n"
  "buffer_load_dwordx4 v113, s[40:43], s57 offen offset:1152 nt lds\n"
  "v_exp_f32 v8, v8\n"
  "v_mfma_f32_16x16x32_bf16 v[20:23], a[136:139], v[48:51], v[20:23]\n"
  "s_waitcnt vmcnt(14)\n"
  "ds_read_b128 v[200:203], v87 offset:6144\n"
  "ds_read_b128 v[204:207], v88 offset:6144\n"
  "v_exp_f32 v9, v9\n"
  "v_mfma_f32_16x16x32_bf16 v[20:23], a[132:135], v[44:47], v[20:23]\n"
  "s_add_u32 m0, s59, 4096\n"
  "v_exp_f32 v10, v10\n"
  "v_mfma_f32_16x16x32_bf16 v[20:23], a[128:131], v[40:43], v[20:23]\n"
  "s_waitcnt lgkmcnt(3)\n"
  "buffer_load_dwordx4 v112, s[40:43], s57 offen offset:0 nt lds\n"
  "buffer_load_dwordx4 v113, s[40:43], s57 offen offset:1024 nt lds\n"
  "s_add_u32 s57, s57, 6144\n"
  "v_exp_f32 v11, v11\n"
  "v_mfma_f32_16x16x32_bf16 v[20:23], a[124:127], v[36:39], v[20:23]\n"
  "s_waitcnt vmcnt(14)\n"
  "ds_read_b128 v[192:195], v87 offset:8192\n"
  "ds_read_b128 v[196:199], v88 offset:8192\n"
  "v_cvt_pk_bf16_f32 v8, v8, v9\n"
  "v_cvt_pk_bf16_f32 v9, v10, v11\n"
  "v_mfma_f32_16x16x32_bf16 v[20:23], a[120:123], v[32:35], v[20:23]\n"
  "ds_read_b128 v[56:59], v81 offset:0\n"
  "ds_read_b128 v[60:63], v82 offset:0\n"
  "v_mfma_f32_16x16x32_bf16 v[24:27], a[144:147], v[32:35], 0\n"
  "v_exp_f32 v12, v12\n"
  "v_mfma_f32_16x16x32_bf16 v[24:27], a[148:151], v[36:39], v[24:27]\n"
  "s_waitcnt vmcnt(12)\n"
  "ds_read_b128 v[184:187], v87 offset:10240\n"
  "ds_read_b128 v[188:191], v88 offset:10240\n"
  "v_exp_f32 v13, v13\n"
  "v_mfma_f32_16x16x32_bf16 v[24:27], a[152:155], v[40:43], v[24:27]\n"
  "v_exp_f32 v14, v14\n"
  "v_mfma_f32_16x16x32_bf16 v[24:27], a[156:159], v[44:47], v[24:27]\n"
  "v_exp_f32 v15, v15\n"
  "v_mfma_f32_16x16x32_bf16 v[24:27], a[160:163], v[48:51], v[24:27]\n"
  "v_cvt_pk_bf16_f32 v10, v12, v13\n"
  "v_cvt_pk_bf16_f32 v11, v14, v15\n"
  "v_mfma_f32_16x16x32_bf16 v[24:27], a[164:167], v[52:55], v[24:27]\n"
  "s_waitcnt vmcnt(10)\n"
  "ds_read_b128 v[208:211], v87 offset:12288\n"
  "ds_read_b128 v[212:215], v88 offset:12288\n"
  "ds_write_b128 v80, v[8:11] offset:1024\n"
  "v_pk_fma_f32 v[16:17], v[16:17], s[60:61], s[60:61] op_sel:[0,0,1] op_sel_hi:[1,0,1]\n"
  "v_pk_fma_f32 v[18:19], v[18:19], s[60:61], s[60:61] op_sel:[0,0,1] op_sel_hi:[1,0,1]\n"
  "v_pk_fma_f32 v[20:21], v[20:21], s[60:61], s[60:61] op_sel:[0,0,1] op_sel_hi:[1,0,1]\n"
  "v_pk_fma_f32 v[22:23], v[22:23], s[60:61], s[60:61] op_sel:[0,0,1] op_sel_hi:[1,0,1]\n"
  "v_mfma_f32_16x16x32_bf16 v[28:31], a[188:191], v[52:55], 0\n"
  "v_exp_f32 v16, v16\n"
  "v_mfma_f32_16x16x32_bf16 v[28:31], a[184:187], v[48:51], v[28:31]\n"
  "v_exp_f32 v17, v17\n"
  "v_mfma_f32_16x16x32_bf16 v[28:31], a[180:183], v[44:47], v[28:31]\n"
  "s_waitcnt vmcnt(8)\n"
  "ds_read_b128 v[216:219], v87 offset:14336\n"
  "ds_read_b128 v[220:223], v88 offset:14336\n"
  "v_exp_f32 v18, v18\n"
  "v_mfma_f32_16x16x32_bf16 v[28:31], a[176:179], v[40:43], v[28:31]\n"
  "v_exp_f32 v19, v19\n"
  "v_mfma_f32_16x16x32_bf16 v[28:31], a[172:175], v[36:39], v[28:31]\n"
  "v_cvt_pk_bf16_f32 v16, v16, v17\n"
  "v_cvt_pk_bf16_f32 v17, v18, v19\n"
  "v_mfma_f32_16x16x32_bf16 v[28:31], a[168:171], v[32:35], v[28:31]\n"
  "s_waitcnt vmcnt(6)\n"
  "ds_read_b128 v[224:227], v87 offset:16384\n"
  "ds_read_b128 v[228:231], v88 offset:16384\n"
  "ds_read_b128 v[64:67], v81 offset:2048\n"
  "ds_read_b128 v[68:71], v82 offset:2048\n"
  "v_mfma_f32_16x16x32_bf16 v[0:3], a[192:195], v[32:35], 0\n"
  "v_exp_f32 v20, v20\n"
  "v_mfma_f32_16x16x32_bf16 v[0:3], a[196:199], v[36:39], v[0:3]\n"
  "v_exp_f32 v21, v21\n"
  "v_mfma_f32_16x16x32_bf16 v[0:3], a[200:203], v[40:43], v[0:3]\n"
  "s_waitcnt vmcnt(4)\n"
  "ds_read_b128 v[248:251], v87 offset:0\n"
  "ds_read_b128 v[252:255], v88 offset:0\n"
  "v_exp_f32 v22, v22\n"
  "v_mfma_f32_16x16x32_bf16 v[0:3], a[204:207], v[44:47], v[0:3]\n"
  "v_exp_f32 v23, v23\n"
  "v_mfma_f32_16x16x32_bf16 v[0:3], a[208:211], v[48:51], v[0:3]\n"
  "v_cvt_pk_bf16_f32 v18, v20, v21\n"
  "v_cvt_pk_bf16_f32 v19, v22, v23\n"
  "v_mfma_f32_16x16x32_bf16 v[0:3], a[212:215], v[52:55], v[0:3]\n"
  "s_waitcnt vmcnt(2)\n"
  "ds_read_b128 v[240:243], v87 offset:2048\n"
  "ds_read_b128 v[244:247], v88 offset:2048\n"
  "ds_write_b128 v80, v[16:19] offset:2048\n"
  "v_pk_fma_f32 v[24:25], v[24:25], s[60:61], s[60:61] op_sel:[0,0,1] op_sel_hi:[1,0,1]\n"
  "v_pk_fma_f32 v[26:27], v[26:27], s[60:61], s[60:61] op_sel:[0,0,1] op_sel_hi:[1,0,1]\n"
  "v_pk_fma_f32 v[28:29], v[28:29], s[60:61], s[60:61] op_sel:[0,0,1] op_sel_hi:[1,0,1]\n"
  "v_pk_fma_f32 v[30:31], v[30:31], s[60:61], s[60:61] op_sel:[0,0,1] op_sel_hi:[1,0,1]\n"
  "v_mfma_f32_16x16x32_bf16 v[4:7], a[236:239], v[52:55], 0\n"
  "v_exp_f32 v24, v24\n"
  "v_mfma_f32_16x16x32_bf16 v[4:7], a[232:235], v[48:51], v[4:7]\n"
  "v_exp_f32 v25, v25\n"
  "v_mfma_f32_16x16x32_bf16 v[4:7], a[228:231], v[44:47], v[4:7]\n"
  "v_exp_f32 v26, v26\n"
  "v_mfma_f32_16x16x32_bf16 v[4:7], a[224:227], v[40:43], v[4:7]\n"
  "s_waitcnt vmcnt(0)\n"
  "ds_read_b128 v[232:235], v87 offset:4096\n"
  "ds_read_b128 v[236:239], v88 offset:4096\n"
  "v_exp_f32 v27, v27\n"
  "v_mfma_f32_16x16x32_bf16 v[4:7], a[220:223], v[36:39], v[4:7]\n"
  "v_cvt_pk_bf16_f32 v24, v24, v25\n"
  "v_cvt_pk_bf16_f32 v25, v26, v27\n"
  "v_mfma_f32_16x16x32_bf16 v[4:7], a[216:219], v[32:35], v[4:7]\n"
  "ds_read_b128 v[72:75], v81 offset:4096\n"
  "ds_read_b128 v[76:79], v82 offset:4096\n"
  "v_mfma_f32_16x16x32_bf16 v[8:11], a[240:243], v[32:35], 0\n"
  "v_exp_f32 v28, v28\n"
  "v_mfma_f32_16x16x32_bf16 v[8:11], a[244:247], v[36:39], v[8:11]\n"
  "v_exp_f32 v29, v29\n"
  "v_mfma_f32_16x16x32_bf16 v[8:11], a[248:251], v[40:43], v[8:11]\n"
  "v_exp_f32 v30, v30\n"
  "v_mfma_f32_16x16x32_bf16 v[8:11], a[252:255], v[44:47], v[8:11]\n"
  "v_exp_f32 v31, v31\n"
  "v_mfma_f32_16x16x32_bf16 v[8:11], v[128:131], v[48:51], v[8:11]\n"
  "v_cvt_pk_bf16_f32 v26, v28, v29\n"
  "v_cvt_pk_bf16_f32 v27, v30, v31\n"
  "v_mfma_f32_16x16x32_bf16 v[8:11], v[132:135], v[52:55], v[8:11]\n"
  "ds_write_b128 v80, v[24:27] offset:3072\n"
  "v_pk_fma_f32 v[0:1], v[0:1], s[60:61], s[60:61] op_sel:[0,0,1] op_sel_hi:[1,0,1]\n"
  "v_pk_fma_f32 v[2:3], v[2:3], s[60:61], s[60:61] op_sel:[0,0,1] op_sel_hi:[1,0,1]\n"
  "v_pk_fma_f32 v[4:5], v[4:5], s[60:61], s[60:61] op_sel:[0,0,1] op_sel_hi:[1,0,1]\n"
  "v_pk_fma_f32 v[6:7], v[6:7], s[60:61], s[60:61] op_sel:[0,0,1] op_sel_hi:[1,0,1]\n"
  "v_mfma_f32_16x16x32_bf16 v[12:15], v[156:159], v[52:55], 0\n"
  "v_exp_f32 v0, v0\n"
  "v_mfma_f32_16x16x32_bf16 v[12:15], v[152:155], v[48:51], v[12:15]\n"
  "v_exp_f32 v1, v1\n"
  "v_mfma_f32_16x16x32_bf16 v[12:15], v[148:151], v[44:47], v[12:15]\n"
  "v_exp_f32 v2, v2\n"
  "v_mfma_f32_16x16x32_bf16 v[12:15], v[144:147], v[40:43], v[12:15]\n"
  "v_exp_f32 v3, v3\n"
  "v_mfma_f32_16x16x32_bf16 v[12:15], v[140:143], v[36:39], v[12:15]\n"
  "v_cvt_pk_bf16_f32 v0, v0, v1\n"
  "v_cvt_pk_bf16_f32 v1, v2, v3\n"
  "v_mfma_f32_16x16x32_bf16 v[12:15], v[136:139], v[32:35], v[12:15]\n"
  "v_mfma_f32_16x16x32_bf16 v[16:19], v[160:163], v[32:35], 0\n"
  "v_exp_f32 v4, v4\n"
  "v_mfma_f32_16x16x32_bf16 v[16:19], v[164:167], v[36:39], v[16:19]\n"
  "v_exp_f32 v5, v5\n"
  "v_mfma_f32_16x16x32_bf16 v[16:19], v[168:171], v[40:43], v[16:19]\n"
  "v_exp_f32 v6, v6\n"
  "v_mfma_f32_16x16x32_bf16 v[16:19], v[172:175], v[44:47], v[16:19]\n"
  "v_exp_f32 v7, v7\n"
  "v_mfma_f32_16x16x32_bf16 v[16:19], v[176:179], v[48:51], v[16:19]\n"
  "v_cvt_pk_bf16_f32 v2, v4, v5\n"
  "v_cvt_pk_bf16_f32 v3, v6, v7\n"
  "v_mfma_f32_16x16x32_bf16 v[16:19], v[180:183], v[52:55], v[16:19]\n"
  "s_waitcnt lgkmcnt(15)\n"
  "ds_write_b128 v80, v[0:3] offset:4096\n"
  "v_pk_fma_f32 v[8:9], v[8:9], s[60:61], s[60:61] op_sel:[0,0,1] op_sel_hi:[1,0,1]\n"
  "v_pk_fma_f32 v[10:11], v[10:11], s[60:61], s[60:61] op_sel:[0,0,1] op_sel_hi:[1,0,1]\n"
  "v_pk_fma_f32 v[12:13], v[12:13], s[60:61], s[60:61] op_sel:[0,0,1] op_sel_hi:[1,0,1]\n"
  "v_pk_fma_f32 v[14:15], v[14:15], s[60:61], s[60:61] op_sel:[0,0,1] op_sel_hi:[1,0,1]\n"
  "v_mfma_f32_16x16x32_bf16 v[20:23], v[204:207], v[52:55], 0\n"
  "v_exp_f32 v8, v8\n"
  "v_mfma_f32_16x16x32_bf16 v[20:23], v[200:203], v[48:51], v[20:23]\n"
  "v_exp_f32 v9, v9\n"
  "v_mfma_f32_16x16x32_bf16 v[20:23], v[196:199], v[44:47], v[20:23]\n"
  "v_exp_f32 v10, v10\n"
  "v_mfma_f32_16x16x32_bf16 v[20:23], v[192:195], v[40:43], v[20:23]\n"
  "v_exp_f32 v11, v11\n"
  "v_mfma_f32_16x16x32_bf16 v[20:23], v[188:191], v[36:39], v[20:23]\n"
  "v_cvt_pk_bf16_f32 v8, v8, v9\n"
  "v_cvt_pk_bf16_f32 v9, v10, v11\n"
  "v_mfma_f32_16x16x32_bf16 v[20:23], v[184:187], v[32:35], v[20:23]\n"
  "v_mfma_f32_16x16x32_bf16 v[24:27], v[208:211], v[32:35], 0\n"
  "v_exp_f32 v12, v12\n"
  "v_mfma_f32_16x16x32_bf16 v[24:27], v[212:215], v[36:39], v[24:27]\n"
  "s_waitcnt lgkmcnt(15)\n"
  "v_exp_f32 v13, v13\n"
  "v_mfma_f32_16x16x32_bf16 v[24:27], v[216:219], v[40:43], v[24:27]\n"
  "v_exp_f32 v14, v14\n"
  "v_mfma_f32_16x16x32_bf16 v[24:27], v[220:223], v[44:47], v[24:27]\n"
  "s_waitcnt lgkmcnt(13)\n"
  "v_exp_f32 v15, v15\n"
  "v_mfma_f32_16x16x32_bf16 v[24:27], v[224:227], v[48:51], v[24:27]\n"
  "v_cvt_pk_bf16_f32 v10, v12, v13\n"
  "v_cvt_pk_bf16_f32 v11, v14, v15\n"
  "v_mfma_f32_16x16x32_bf16 v[24:27], v[228:231], v[52:55], v[24:27]\n"
  "s_waitcnt lgkmcnt(9)\n"
  "ds_write_b128 v80, v[8:11] offset:5120\n"
  "v_pk_fma_f32 v[16:17], v[16:17], s[60:61], s[60:61] op_sel:[0,0,1] op_sel_hi:[1,0,1]\n"
  "v_pk_fma_f32 v[18:19], v[18:19], s[60:61], s[60:61] op_sel:[0,0,1] op_sel_hi:[1,0,1]\n"
  "v_pk_fma_f32 v[20:21], v[20:21], s[60:61], s[60:61] op_sel:[0,0,1] op_sel_hi:[1,0,1]\n"
  "v_pk_fma_f32 v[22:23], v[22:23], s[60:61], s[60:61] op_sel:[0,0,1] op_sel_hi:[1,0,1]\n"
  "v_mfma_f32_16x16x32_bf16 v[28:31], v[252:255], v[52:55], 0\n"
  "v_exp_f32 v16, v16\n"
  "v_mfma_f32_16x16x32_bf16 v[28:31], v[248:251], v[48:51], v[28:31]\n"
  "s_waitcnt lgkmcnt(8)\n"
  "v_exp_f32 v17, v17\n"
  "v_mfma_f32_16x16x32_bf16 v[28:31], v[244:247], v[44:47], v[28:31]\n"
  "v_exp_f32 v18, v18\n"
  "v_mfma_f32_16x16x32_bf16 v[28:31], v[240:243], v[40:43], v[28:31]\n"
  "s_waitcnt lgkmcnt(5)\n"
  "s_sub_u32 s56, s56, 1\n"
  "v_exp_f32 v19, v19\n"
  "v_mfma_f32_16x16x32_bf16 v[28:31], v[236:239], v[36:39], v[28:31]\n"
  "v_cvt_pk_bf16_f32 v16, v16, v17\n"
  "v_cvt_pk_bf16_f32 v17, v18, v19\n"
  "v_mfma_f32_16x16x32_bf16 v[28:31], v[232:235], v[32:35], v[28:31]\n"
  "s_waitcnt lgkmcnt(3)\n"
  "s_branch A_H1_%=\n"
  "A_X0_%=:\n"
  "s_mov_b32 s52, s53\n"
  "s_mov_b32 s53, s54\n"
  "s_mov_b32 s54, s55\n"
  "s_cmp_eq_u32 s52, 0\n"
  "s_cbranch_scc1 A_LAST_%=\n"
  "s_lshr_b32 s62, s52, 16\n"
  "s_lshl1_add_u32 s62, s62, s65\n"
  "s_mul_i32 s57, s62, 98304\n"
  "s_and_b32 s56, s52, 0xff\n"
  "s_cmp_lg_u32 s53, 0\n"
  "s_cselect_b32 s62, 1, 0\n"
  "s_sub_u32 s56, s56, s62\n"
  "s_add_u32 s57, s57, 18432\n"
  "v_mfma_f32_16x16x32_bf16 v[0:3], a[0:3], v[32:35], 0\n"
  "s_add_u32 s66, s66, 6144\n"
  "s_cmp_eq_u32 s66, 86016\n"
  "s_cselect_b32 s66, 49152, s66\n"
  "v_exp_f32 v20, v20\n"
  "v_mfma_f32_16x16x32_bf16 v[0:3], a[4:7], v[36:39], v[0:3]\n"
  "v_exp_f32 v21, v21\n"
  "v_mfma_f32_16x16x32_bf16 v[0:3], a[8:11], v[40:43], v[0:3]\n"
  "ds_read_b128 a[0:3], v87 offset:0\n"
  "ds_read_b128 a[4:7], v88 offset:0\n"
  "v_exp_f32 v22, v22\n"
  "v_mfma_f32_16x16x32_bf16 v[0:3], a[12:15], v[44:47], v[0:3]\n"
  "v_exp_f32 v23, v23\n"
  "v_mfma_f32_16x16x32_bf16 v[0:3], a[16:19], v[48:51], v[0:3]\n"
  "v_cvt_pk_bf16_f32 v18, v20, v21\n"
  "v_cvt_pk_bf16_f32 v19, v22, v23\n"
  "v_mfma_f32_16x16x32_bf16 v[0:3], a[20:23], v[52:55], v[0:3]\n"
  "ds_read_b128 a[8:11], v87 offset:2048\n"
  "ds_read_b128 a[12:15], v88 offset:2048\n"
  "ds_write_b128 v80, v[16:19] offset:6144\n"
  "v_pk_fma_f32 v[24:25], v[24:25], s[60:61], s[60:61] op_sel:[0,0,1] op_sel_hi:[1,0,1]\n"
  "v_pk_fma_f32 v[26:27], v[26:27], s[60:61], s[60:61] op_sel:[0,0,1] op_sel_hi:[1,0,1]\n"
  "v_pk_fma_f32 v[28:29], v[28:29], s[60:61], s[60:61] op_sel:[0,0,1] op_sel_hi:[1,0,1]\n"
  "v_pk_fma_f32 v[30:31], v[30:31], s[60:61], s[60:61] op_sel:[0,0,1] op_sel_hi:[1,0,1]\n"
  "v_mfma_f32_16x16x32_bf16 v[4:7], a[44:47], v[52:55], 0\n"
  "v_exp_f32 v24, v24\n"
  "v_mfma_f32_16x16x32_bf16 v[4:7], a[40:43], v[48:51], v[4:7]\n"
  "s_add_u32 m0, s59, -256\n"
  "v_exp_f32 v25, v25\n"
  "v_mfma_f32_16x16x32_bf16 v[4:7], a[36:39], v[44:47], v[4:7]\n"
  "s_waitcnt lgkmcnt(3)\n"
  "buffer_load_dwordx4 v112, s[40:43], s57 offen offset:256 nt lds\n"
  "buffer_load_dwordx4 v113, s[40:43], s57 offen offset:1280 nt lds\n"
  "v_exp_f32 v26, v26\n"
  "v_mfma_f32_16x16x32_bf16 v[4:7], a[32:35], v[40:43], v[4:7]\n"
  "ds_read_b128 a[16:19], v87 offset:4096\n"
  "ds_read_b128 a[20:23], v88 offset:4096\n"
  "v_exp_f32 v27, v27\n"
  "v_mfma_f32_16x16x32_bf16 v[4:7], a[28:31], v[36:39], v[4:7]\n"
  "s_add_u32 m0, s59, 1920\n"
  "v_cvt_pk_bf16_f32 v24, v24, v25\n"
  "v_cvt_pk_bf16_f32 v25, v26, v27\n"
  "v_mfma_f32_16x16x32_bf16 v[4:7], a[24:27], v[32:35], v[4:7]\n"
  "s_waitcnt lgkmcnt(3)\n"
  "buffer_load_dwordx4 v112, s[40:43], s57 offen offset:128 nt lds\n"
  "buffer_load_dwordx4 v113, s[40:43], s57 offen offset:1152 nt lds\n"
  "v_add_u32 v81, s66, v83\n"
  "v_add_u32 v82, s66, v84\n"
  "v_mfma_f32_16x16x32_bf16 v[8:11], a[48:51], v[32:35], 0\n"
  "ds_read_b128 a[40:43], v87 offset:6144\n"
  "ds_read_b128 a[44:47], v88 offset:6144\n"
  "v_exp_f32 v28, v28\n"
  "v_mfma_f32_16x16x32_bf16 v[8:11], a[52:55], v[36:39], v[8:11]\n"
  "s_add_u32 m0, s59, 4096\n"
  "v_exp_f32 v29, v29\n"
  "v_mfma_f32_16x16x32_bf16 v[8:11], a[56:59], v[40:43], v[8:11]\n"
  "s_waitcnt lgkmcnt(2)\n"
  "buffer_load_dwordx4 v112, s[40:43], s57 offen offset:0 nt lds\n"
  "buffer_load_dwordx4 v113, s[40:43], s57 offen offset:1024 nt lds\n"
  "s_add_u32 s57, s57, 6144\n"
  "v_exp_f32 v30, v30\n"
  "v_mfma_f32_16x16x32_bf16 v[8:11], a[60:63], v[44:47], v[8:11]\n"
  "ds_read_b128 a[32:35], v87 offset:8192\n"
  "ds_read_b128 a[36:39], v88 offset:8192\n"
  "v_exp_f32 v31, v31\n"
  "v_mfma_f32_16x16x32_bf16 v[8:11], a[64:67], v[48:51], v[8:11]\n"
  "s_add_u32 m0, s59, 6144\n"
  "v_cvt_pk_bf16_f32 v26, v28, v29\n"
  "v_cvt_pk_bf16_f32 v27, v30, v31\n"
  "v_mfma_f32_16x16x32_bf16 v[8:11], a[68:71], v[52:55], v[8:11]\n"
  "s_waitcnt lgkmcnt(2)\n"
  "buffer_load_dwordx4 v112, s[40:43], s57 offen offset:0 nt lds\n"
  "buffer_load_dwordx4 v113, s[40:43], s57 offen offset:1024 nt lds\n"
  "ds_write_b128 v80, v[24:27] offset:7168\n"
  "v_pk_fma_f32 v[0:1], v[0:1], s[60:61], s[60:61] op_sel:[0,0,1] op_sel_hi:[1,0,1]\n"
  "v_pk_fma_f32 v[2:3], v[2:3], s[60:61], s[60:61] op_sel:[0,0,1] op_sel_hi:[1,0,1]\n"
  "v_pk_fma_f32 v[4:5], v[4:5], s[60:61], s[60:61] op_sel:[0,0,1] op_sel_hi:[1,0,1]\n"
  "v_pk_fma_f32 v[6:7], v[6:7], s[60:61], s[60:61] op_sel:[0,0,1] op_sel_hi:[1,0,1]\n"
  "v_mfma_f32_16x16x32_bf16 v[12:15], a[92:95], v[52:55], 0\n"
  "ds_read_b128 a[24:27], v87 offset:10240\n"
  "ds_read_b128 a[28:31], v88 offset:10240\n"
  "v_exp_f32 v0, v0\n"
  "v_mfma_f32_16x16x32_bf16 v[12:15], a[88:91], v[48:51], v[12:15]\n"
  "s_add_u32 m0, s59, 8064\n"
  "v_exp_f32 v1, v1\n"
  "v_mfma_f32_16x16x32_bf16 v[12:15], a[84:87], v[44:47], v[12:15]\n"
  "s_waitcnt lgkmcnt(3)\n"
  "buffer_load_dwordx4 v112, s[40:43], s57 offen offset:128 nt lds\n"
  "buffer_load_dwordx4 v113, s[40:43], s57 offen offset:1152 nt lds\n"
  "v_exp_f32 v2, v2\n"
  "v_mfma_f32_16x16x32_bf16 v[12:15], a[80:83], v[40:43], v[12:15]\n"
  "ds_read_b128 a[48:51], v87 offset:12288\n"
  "ds_read_b128 a[52:55], v88 offset:12288\n"
  "v_exp_f32 v3, v3\n"
  "v_mfma_f32_16x16x32_bf16 v[12:15], a[76:79], v[36:39], v[12:15]\n"
  "v_cvt_pk_bf16_f32 v0, v0, v1\n"
  "v_cvt_pk_bf16_f32 v1, v2, v3\n"
  "v_mfma_f32_16x16x32_bf16 v[12:15], a[72:75], v[32:35], v[12:15]\n"
  "s_add_u32 m0, s59, 9984\n"
  "s_waitcnt lgkmcnt(4)\n"
  "s_barrier\n"
  "v_xor_b32 v80, 0x4000, v80\n"
  "v_mfma_f32_16x16x32_bf16 v[16:19], a[96:99], v[32:35], 0\n"
  "s_waitcnt lgkmcnt(2)\n"
  "buffer_load_dwordx4 v112, s[40:43], s57 offen offset:256 nt lds\n"
  "buffer_load_dwordx4 v113, s[40:43], s57 offen offset:1280 nt lds\n"
  "s_add_u32 s57, s57, 6144\n"
  "v_exp_f32 v4, v4\n"
  "v_mfma_f32_16x16x32_bf16 v[16:19], a[100:103], v[36:39], v[16:19]\n"
  "ds_read_b128 a[56:59], v87 offset:14336\n"
  "ds_read_b128 a[60:63], v88 offset:14336\n"
  "v_exp_f32 v5, v5\n"
  "v_mfma_f32_16x16x32_bf16 v[16:19], a[104:107], v[40:43], v[16:19]\n"
  "s_add_u32 m0, s59, 12032\n"
  "v_exp_f32 v6, v6\n"
  "v_mfma_f32_16x16x32_bf16 v[16:19], a[108:111], v[44:47], v[16:19]\n"
  "s_waitcnt lgkmcnt(2)\n"
  "buffer_load_dwordx4 v112, s[40:43], s57 offen offset:256 nt lds\n"
  "buffer_load_dwordx4 v113, s[40:43], s57 offen offset:1280 nt lds\n"
  "v_exp_f32 v7, v7\n"
  "v_mfma_f32_16x16x32_bf16 v[16:19], a[112:115], v[48:51], v[16:19]\n"
  "ds_read_b128 a[64:67], v87 offset:16384\n"
  "ds_read_b128 a[68:71], v88 offset:16384\n"
  "v_cvt_pk_bf16_f32 v2, v4, v5\n"
  "v_cvt_pk_bf16_f32 v3, v6, v7\n"
  "v_mfma_f32_16x16x32_bf16 v[16:19], a[116:119], v[52:55], v[16:19]\n"
  "s_add_u32 m0, s59, 14208\n"
  "ds_write_b128 v80, v[0:3] offset:0\n"
  "v_pk_fma_f32 v[8:9], v[8:9], s[60:61], s[60:61] op_sel:[0,0,1] op_sel_hi:[1,0,1]\n"
  "v_pk_fma_f32 v[10:11], v[10:11], s[60:61], s[60:61] op_sel:[0,0,1] op_sel_hi:[1,0,1]\n"
  "v_pk_fma_f32 v[12:13], v[12:13], s[60:61], s[60:61] op_sel:[0,0,1] op_sel_hi:[1,0,1]\n"
  "v_pk_fma_f32 v[14:15], v[14:15], s[60:61], s[60:61] op_sel:[0,0,1] op_sel_hi:[1,0,1]\n"
  "v_mfma_f32_16x16x32_bf16 v[20:23], a[140:143], v[52:55], 0\n"
  "s_waitcnt lgkmcnt(3)\n"
  "buffer_load_dwordx4 v112, s[40:43], s57 offen offset:128 nt lds\n"
  "buffer_load_dwordx4 v113, s[40:43], s57 offen offset:1152 nt lds\n"
  "v_exp_f32 v8, v8\n"
  "v_mfma_f32_16x16x32_bf16 v[20:23], a[136:139], v[48:51], v[20:23]\n"
  "s_waitcnt vmcnt(14)\n"
  "ds_read_b128 a[88:91], v87 offset:0\n"
  "ds_read_b128 a[92:95], v88 offset:0\n"
  "v_exp_f32 v9, v9\n"
  "v_mfma_f32_16x16x32_bf16 v[20:23], a[132:135], v[44:47], v[20:23]\n"
  "s_add_u32 m0, s59, 16384\n"
  "v_exp_f32 v10, v10\n"
  "v_mfma_f32_16x16x32_bf16 v[20:23], a[128:131], v[40:43], v[20:23]\n"
  "s_waitcnt lgkmcnt(3)\n"
  "buffer_load_dwordx4 v112, s[40:43], s57 offen offset:0 nt lds\n"
  "buffer_load_dwordx4 v113, s[40:43], s57 offen offset:1024 nt lds\n"
  "s_add_u32 s57, s57, 6144\n"
  "v_exp_f32 v11, v11\n"
  "v_mfma_f32_16x16x32_bf16 v[20:23], a[124:127], v[36:39], v[20:23]\n"
  "s_waitcnt vmcnt(14)\n"
  "ds_read_b128 a[80:83], v87 offset:2048\n"
  "ds_read_b128 a[84:87], v88 offset:2048\n"
  "v_cvt_pk_bf16_f32 v8, v8, v9\n"
  "v_cvt_pk_bf16_f32 v9, v10, v11\n"
  "v_mfma_f32_16x16x32_bf16 v[20:23], a[120:123], v[32:35], v[20:23]\n"
  "s_add_u32 m0, s59, 0\n"
  "ds_read_b128 v[56:59], v81 offset:0\n"
  "ds_read_b128 v[60:63], v82 offset:0\n"
  "v_mfma_f32_16x16x32_bf16 v[24:27], a[144:147], v[32:35], 0\n"
  "s_waitcnt lgkmcnt(4)\n"
  "buffer_load_dwordx4 v112, s[40:43], s57 offen offset:0 nt lds\n"
  "buffer_load_dwordx4 v113, s[40:43], s57 offen offset:1024 nt lds\n"
  "v_exp_f32 v12, v12\n"
  "v_mfma_f32_16x16x32_bf16 v[24:27], a[148:151], v[36:39], v[24:27]\n"
  "s_waitcnt vmcnt(14)\n"
  "ds_read_b128 a[72:75], v87 offset:4096\n"
  "ds_read_b128 a[76:79], v88 offset:4096\n"
  "v_exp_f32 v13, v13\n"
  "v_mfma_f32_16x16x32_bf16 v[24:27], a[152:155], v[40:43], v[24:27]\n"
  "v_exp_f32 v14, v14\n"
  "v_mfma_f32_16x16x32_bf16 v[24:27], a[156:159], v[44:47], v[24:27]\n"
  "s_add_u32 m0, s59, 1920\n"
  "v_exp_f32 v15, v15\n"
  "v_mfma_f32_16x16x32_bf16 v[24:27], a[160:163], v[48:51], v[24:27]\n"
  "s_waitcnt lgkmcnt(4)\n"
  "buffer_load_dwordx4 v112, s[40:43], s57 offen offset:128 nt lds\n"
  "buffer_load_dwordx4 v113, s[40:43], s57 offen offset:1152 nt lds\n"
  "v_cvt_pk_bf16_f32 v10, v12, v13\n"
  "v_cvt_pk_bf16_f32 v11, v14, v15\n"
  "v_mfma_f32_16x16x32_bf16 v[24:27], a[164:167], v[52:55], v[24:27]\n"
  "s_waitcnt vmcnt(14)\n"
  "ds_read_b128 a[96:99], v87 offset:6144\n"
  "ds_read_b128 a[100:103], v88 offset:6144\n"
  "ds_write_b128 v80, v[8:11] offset:1024\n"
  "v_pk_fma_f32 v[16:17], v[16:17], s[60:61], s[60:61] op_sel:[0,0,1] op_sel_hi:[1,0,1]\n"
  "v_pk_fma_f32 v[18:19], v[18:19], s[60:61], s[60:61] op_sel:[0,0,1] op_sel_hi:[1,0,1]\n"
  "v_pk_fma_f32 v[20:21], v[20:21], s[60:61], s[60:61] op_sel:[0,0,1] op_sel_hi:[1,0,1]\n"
  "v_pk_fma_f32 v[22:23], v[22:23], s[60:61], s[60:61] op_sel:[0,0,1] op_sel_hi:[1,0,1]\n"
  "v_mfma_f32_16x16x32_bf16 v[28:31], a[188:191], v[52:55], 0\n"
  "s_add_u32 m0, s59, 3840\n"
  "v_exp_f32 v16, v16\n"
  "v_mfma_f32_16x16x32_bf16 v[28:31], a[184:187], v[48:51], v[28:31]\n"
  "s_waitcnt lgkmcnt(3)\n"
  "buffer_load_dwordx4 v112, s[40:43], s57 offen offset:256 nt lds\n"
  "buffer_load_dwordx4 v113, s[40:43], s57 offen offset:1280 nt lds\n"
  "s_add_u32 s57, s57, 6144\n"
  "v_exp_f32 v17, v17\n"
  "v_mfma_f32_16x16x32_bf16 v[28:31], a[180:183], v[44:47], v[28:31]\n"
  "s_waitcnt vmcnt(14)\n"
  "ds_read_b128 a[104:107], v87 offset:8192\n"
  "ds_read_b128 a[108:111], v88 offset:8192\n"
  "v_exp_f32 v18, v18\n"
  "v_mfma_f32_16x16x32_bf16 v[28:31], a[176:179], v[40:43], v[28:31]\n"
  "s_add_u32 m0, s59, 5888\n"
  "v_exp_f32 v19, v19\n"
  "v_mfma_f32_16x16x32_bf16 v[28:31], a[172:175], v[36:39], v[28:31]\n"
  "s_waitcnt lgkmcnt(3)\n"
  "buffer_load_dwordx4 v112, s[40:43], s57 offen offset:256 nt lds\n"
  "buffer_load_dwordx4 v113, s[40:43], s57 offen offset:1280 nt lds\n"
  "v_cvt_pk_bf16_f32 v16, v16, v17\n"
  "v_cvt_pk_bf16_f32 v17, v18, v19\n"
  "v_mfma_f32_16x16x32_bf16 v[28:31], a[168:171], v[32:35], v[28:31]\n"
  "s_waitcnt vmcnt(14)\n"
  "ds_read_b128 a[112:115], v87 offset:10240\n"
  "ds_read_b128 a[116:119], v88 offset:10240\n"
  "ds_read_b128 v[64:67], v81 offset:2048\n"
  "ds_read_b128 v[68:71], v82 offset:2048\n"
  "v_mfma_f32_16x16x32_bf16 v[0:3], a[192:195], v[32:35], 0\n"
  "s_add_u32 m0, s59, 8064\n"
  "v_exp_f32 v20, v20\n"
  "v_mfma_f32_16x16x32_bf16 v[0:3], a[196:199], v[36:39], v[0:3]\n"
  "s_waitcnt lgkmcnt(4)\n"
  "buffer_load_dwordx4 v112, s[40:43], s57 offen offset:128 nt lds\n"
  "buffer_load_dwordx4 v113, s[40:43], s57 offen offset:1152 nt lds\n"
  "v_exp_f32 v21, v21\n"
  "v_mfma_f32_16x16x32_bf16 v[0:3], a[200:203], v[40:43], v[0:3]\n"
  "s_waitcnt vmcnt(14)\n"
  "ds_read_b128 a[136:139], v87 offset:12288\n"
  "ds_read_b128 a[140:143], v88 offset:12288\n"
  "v_exp_f32 v22, v22\n"
  "v_mfma_f32_16x16x32_bf16 v[0:3], a[204:207], v[44:47], v[0:3]\n"
  "s_add_u32 m0, s59, 10240\n"
  "v_exp_f32 v23, v23\n"
  "v_mfma_f32_16x16x32_bf16 v[0:3], a[208:211], v[48:51], v[0:3]\n"
  "s_waitcnt lgkmcnt(4)\n"
  "buffer_load_dwordx4 v112, s[40:43], s57 offen offset:0 nt lds\n"
  "buffer_load_dwordx4 v113, s[40:43], s57 offen offset:1024 nt lds\n"
  "s_add_u32 s57, s57, 6144\n"
  "v_cvt_pk_bf16_f32 v18, v20, v21\n"
  "v_cvt_pk_bf16_f32 v19, v22, v23\n"
  "v_mfma_f32_16x16x32_bf16 v[0:3], a[212:215], v[52:55], v[0:3]\n"
  "s_waitcnt vmcnt(14)\n"
  "ds_read_b128 a[128:131], v87 offset:14336\n"
  "ds_read_b128 a[132:135], v88 offset:14336\n"
  "ds_write_b128 v80, v[16:19] offset:2048\n"
  "v_pk_fma_f32 v[24:25], v[24:25], s[60:61], s[60:61] op_sel:[0,0,1] op_sel_hi:[1,0,1]\n"
  "v_pk_fma_f32 v[26:27], v[26:27], s[60:61], s[60:61] op_sel:[0,0,1] op_sel_hi:[1,0,1]\n"
  "v_pk_fma_f32 v[28:29], v[28:29], s[60:61], s[60:61] op_sel:[0,0,1] op_sel_hi:[1,0,1]\n"
  "v_pk_fma_f32 v[30:31], v[30:31], s[60:61], s[60:61] op_sel:[0,0,1] op_sel_hi:[1,0,1]\n"
  "v_mfma_f32_16x16x32_bf16 v[4:7], a[236:239], v[52:55], 0\n"
  "v_exp_f32 v24, v24\n"
  "v_mfma_f32_16x16x32_bf16 v[4:7], a[232:235], v[48:51], v[4:7]\n"
  "s_add_u32 m0, s59, 12288\n"
  "v_exp_f32 v25, v25\n"
  "v_mfma_f32_16x16x32_bf16 v[4:7], a[228:231], v[44:47], v[4:7]\n"
  "s_waitcnt lgkmcnt(3)\n"
  "buffer_load_dwordx4 v112, s[40:43], s57 offen offset:0 nt lds\n"
  "buffer_load_dwordx4 v113, s[40:43], s57 offen offset:1024 nt lds\n"
  "v_exp_f32 v26, v26\n"
  "v_mfma_f32_16x16x32_bf16 v[4:7], a[224:227], v[40:43], v[4:7]\n"
  "s_waitcnt vmcnt(14)\n"
  "ds_read_b128 a[120:123], v87 offset:16384\n"
  "ds_read_b128 a[124:127], v88 offset:16384\n"
  "v_exp_f32 v27, v27\n"
  "v_mfma_f32_16x16x32_bf16 v[4:7], a[220:223], v[36:39], v[4:7]\n"
  "s_add_u32 m0, s59, 14208\n"
  "v_cvt_pk_bf16_f32 v24, v24, v25\n"
  "v_cvt_pk_bf16_f32 v25, v26, v27\n"
  "v_mfma_f32_16x16x32_bf16 v[4:7], a[216:219], v[32:35], v[4:7]\n"
  "s_waitcnt lgkmcnt(3)\n"
  "buffer_load_dwordx4 v112, s[40:43], s57 offen offset:128 nt lds\n"
  "buffer_load_dwordx4 v113, s[40:43], s57 offen offset:1152 nt lds\n"
  "ds_read_b128 v[72:75], v81 offset:4096\n"
  "ds_read_b128 v[76:79], v82 offset:4096\n"
  "v_mfma_f32_16x16x32_bf16 v[8:11], a[240:243], v[32:35], 0\n"
  "s_waitcnt vmcnt(14)\n"
  "ds_read_b128 a[144:147], v87 offset:0\n"
  "ds_read_b128 a[148:151], v88 offset:0\n"
  "v_exp_f32 v28, v28\n"
  "v_mfma_f32_16x16x32_bf16 v[8:11], a[244:247], v[36:39], v[8:11]\n"
  "s_add_u32 m0, s59, 16128\n"
  "v_exp_f32 v29, v29\n"
  "v_mfma_f32_16x16x32_bf16 v[8:11], a[248:251], v[40:43], v[8:11]\n"
  "s_waitcnt lgkmcnt(4)\n"
  "buffer_load_dwordx4 v112, s[40:43], s57 offen offset:256 nt lds\n"
  "buffer_load_dwordx4 v113, s[40:43], s57 offen offset:1280 nt lds\n"
  "s_add_u32 s57, s57, 6144\n"
  "v_exp_f32 v30, v30\n"
  "v_mfma_f32_16x16x32_bf16 v[8:11], a[252:255], v[44:47], v[8:11]\n"
  "s_waitcnt vmcnt(14)\n"
  "ds_read_b128 a[152:155], v87 offset:2048\n"
  "ds_read_b128 a[156:159], v88 offset:2048\n"
  "v_exp_f32 v31, v31\n"
  "v_mfma_f32_16x16x32_bf16 v[8:11], v[128:131], v[48:51], v[8:11]\n"
  "s_add_u32 m0, s59, -256\n"
  "v_cvt_pk_bf16_f32 v26, v28, v29\n"
  "v_cvt_pk_bf16_f32 v27, v30, v31\n"
  "v_mfma_f32_16x16x32_bf16 v[8:11], v[132:135], v[52:55], v[8:11]\n"
  "s_waitcnt lgkmcnt(2)\n"
  "buffer_load_dwordx4 v112, s[40:43], s57 offen offset:256 nt lds\n"
  "buffer_load_dwordx4 v113, s[40:43], s57 offen offset:1280 nt lds\n"
  "ds_write_b128 v80, v[24:27] offset:3072\n"
  "v_pk_fma_f32 v[0:1], v[0:1], s[60:61], s[60:61] op_sel:[0,0,1] op_sel_hi:[1,0,1]\n"
  "v_pk_fma_f32 v[2:3], v[2:3], s[60:61], s[60:61] op_sel:[0,0,1] op_sel_hi:[1,0,1]\n"
  "v_pk_fma_f32 v[4:5], v[4:5], s[60:61], s[60:61] op_sel:[0,0,1] op_sel_hi:[1,0,1]\n"
  "v_pk_fma_f32 v[6:7], v[6:7], s[60:61], s[60:61] op_sel:[0,0,1] op_sel_hi:[1,0,1]\n"
  "v_mfma_f32_16x16x32_bf16 v[12:15], v[156:159], v[52:55], 0\n"
  "s_waitcnt vmcnt(14)\n"
  "ds_read_b128 a[160:163], v87 offset:4096\n"
  "ds_read_b128 a[164:167], v88 offset:4096\n"
  "v_exp_f32 v0, v0\n"
  "v_mfma_f32_16x16x32_bf16 v[12:15], v[152:155], v[48:51], v[12:15]\n"
  "s_add_u32 m0, s59, 1920\n"
  "v_exp_f32 v1, v1\n"
  "v_mfma_f32_16x16x32_bf16 v[12:15], v[148:151], v[44:47], v[12:15]\n"
  "s_waitcnt lgkmcnt(3)\n"
  "buffer_load_dwordx4 v112, s[40:43], s57 offen offset:128 nt lds\n"
  "buffer_load_dwordx4 v113, s[40:43], s57 offen offset:1152 nt lds\n"
  "v_exp_f32 v2, v2\n"
  "v_mfma_f32_16x16x32_bf16 v[12:15], v[144:147], v[40:43], v[12:15]\n"
  "s_waitcnt vmcnt(14)\n"
  "ds_read_b128 a[184:187], v87 offset:6144\n"
  "ds_read_b128 a[188:191], v88 offset:6144\n"
  "v_exp_f32 v3, v3\n"
  "v_mfma_f32_16x16x32_bf16 v[12:15], v[140:143], v[36:39], v[12:15]\n"
  "v_cvt_pk_bf16_f32 v0, v0, v1\n"
  "v_cvt_pk_bf16_f32 v1, v2, v3\n"
  "v_mfma_f32_16x16x32_bf16 v[12:15], v[136:139], v[32:35], v[12:15]\n"
  "s_add_u32 m0, s59, 4096\n"
  "v_mfma_f32_16x16x32_bf16 v[16:19], v[160:163], v[32:35], 0\n"
  "s_waitcnt lgkmcnt(2)\n"
  "buffer_load_dwordx4 v112, s[40:43], s57 offen offset:0 nt lds\n"
  "buffer_load_dwordx4 v113, s[40:43], s57 offen offset:1024 nt lds\n"
  "s_add_u32 s57, s57, 6144\n"
  "v_exp_f32 v4, v4\n"
  "v_mfma_f32_16x16x32_bf16 v[16:19], v[164:167], v[36:39], v[16:19]\n"
  "s_waitcnt vmcnt(14)\n"
  "ds_read_b128 a[176:179], v87 offset:8192\n"
  "ds_read_b128 a[180:183], v88 offset:8192\n"
  "v_exp_f32 v5, v5\n"
  "v_mfma_f32_16x16x32_bf16 v[16:19], v[168:171], v[40:43], v[16:19]\n"
  "s_add_u32 m0, s59, 6144\n"
  "v_exp_f32 v6, v6\n"
  "v_mfma_f32_16x16x32_bf16 v[16:19], v[172:175], v[44:47], v[16:19]\n"
  "s_waitcnt lgkmcnt(2)\n"
  "buffer_load_dwordx4 v112, s[40:43], s57 offen offset:0 nt lds\n"
  "buffer_load_dwordx4 v113, s[40:43], s57 offen offset:1024 nt lds\n"
  "v_exp_f32 v7, v7\n"
  "v_mfma_f32_16x16x32_bf16 v[16:19], v[176:179], v[48:51], v[16:19]\n"
  "s_waitcnt vmcnt(14)\n"
  "ds_read_b128 a[168:171], v87 offset:10240\n"
  "ds_read_b128 a[172:175], v88 offset:10240\n"
  "v_cvt_pk_bf16_f32 v2, v4, v5\n"
  "v_cvt_pk_bf16_f32 v3, v6, v7\n"
  "v_mfma_f32_16x16x32_bf16 v[16:19], v[180:183], v[52:55], v[16:19]\n"
  "s_add_u32 m0, s59, 8064\n"
  "ds_write_b128 v80, v[0:3] offset:4096\n"
  "v_pk_fma_f32 v[8:9], v[8:9], s[60:61], s[60:61] op_sel:[0,0,1] op_sel_hi:[1,0,1]\n"
  "v_pk_fma_f32 v[10:11], v[10:11], s[60:61], s[60:61] op_sel:[0,0,1] op_sel_hi:[1,0,1]\n"
  "v_pk_fma_f32 v[12:13], v[12:13], s[60:61], s[60:61] op_sel:[0,0,1] op_sel_hi:[1,0,1]\n"
  "v_pk_fma_f32 v[14:15], v[14:15], s[60:61], s[60:61] op_sel:[0,0,1] op_sel_hi:[1,0,1]\n"
  "v_mfma_f32_16x16x32_bf16 v[20:23], v[204:207], v[52:55], 0\n"
  "s_waitcnt lgkmcnt(3)\n"
  "buffer_load_dwordx4 v112, s[40:43], s57 offen offset:128 nt lds\n"
  "buffer_load_dwordx4 v113, s[40:43], s57 offen offset:1152 nt lds\n"
  "v_exp_f32 v8, v8\n"
  "v_mfma_f32_16x16x32_bf16 v[20:23], v[200:203], v[48:51], v[20:23]\n"
  "s_waitcnt vmcnt(14)\n"
  "ds_read_b128 a[192:195], v87 offset:12288\n"
  "ds_read_b128 a[196:199], v88 offset:12288\n"
  "v_exp_f32 v9, v9\n"
  "v_mfma_f32_16x16x32_bf16 v[20:23], v[196:199], v[44:47], v[20:23]\n"
  "s_add_u32 m0, s59, 9984\n"
  "v_exp_f32 v10, v10\n"
  "v_mfma_f32_16x16x32_bf16 v[20:23], v[192:195], v[40:43], v[20:23]\n"
  "s_waitcnt lgkmcnt(3)\n"
  "buffer_load_dwordx4 v112, s[40:43], s57 offen offset:256 nt lds\n"
  "buffer_load_dwordx4 v113, s[40:43], s57 offen offset:1280 nt lds\n"
  "s_add_u32 s57, s57, 6144\n"
  "v_exp_f32 v11, v11\n"
  "v_mfma_f32_16x16x32_bf16 v[20:23], v[188:191], v[36:39], v[20:23]\n"
  "s_waitcnt vmcnt(14)\n"
  "ds_read_b128 a[200:203], v87 offset:14336\n"
  "ds_read_b128 a[204:207], v88 offset:14336\n"
  "v_cvt_pk_bf16_f32 v8, v8, v9\n"
  "v_cvt_pk_bf16_f32 v9, v10, v11\n"
  "v_mfma_f32_16x16x32_bf16 v[20:23], v[184:187], v[32:35], v[20:23]\n"
  "s_add_u32 m0, s59, 12032\n"
  "v_mfma_f32_16x16x32_bf16 v[24:27], v[208:211], v[32:35], 0\n"
  "s_waitcnt lgkmcnt(2)\n"
  "buffer_load_dwordx4 v112, s[40:43], s57 offen offset:256 nt lds\n"
  "buffer_load_dwordx4 v113, s[40:43], s57 offen offset:1280 nt lds\n"
  "v_exp_f32 v12, v12\n"
  "v_mfma_f32_16x16x32_bf16 v[24:27], v[212:215], v[36:39], v[24:27]\n"
  "s_waitcnt vmcnt(14)\n"
  "ds_read_b128 a[208:211], v87 offset:16384\n"
  "ds_read_b128 a[212:215], v88 offset:16384\n"
  "v_exp_f32 v13, v13\n"
  "v_mfma_f32_16x16x32_bf16 v[24:27], v[216:219], v[40:43], v[24:27]\n"
  "v_exp_f32 v14, v14\n"
  "v_mfma_f32_16x16x32_bf16 v[24:27], v[220:223], v[44:47], v[24:27]\n"
  "s_add_u32 m0, s59, 14208\n"
  "v_exp_f32 v15, v15\n"
  "v_mfma_f32_16x16x32_bf16 v[24:27], v[224:227], v[48:51], v[24:27]\n"
  "s_waitcnt lgkmcnt(2)\n"
  "buffer_load_dwordx4 v112, s[40:43], s57 offen offset:128 nt lds\n"
  "buffer_load_dwordx4 v113, s[40:43], s57 offen offset:1152 nt lds\n"
  "v_cvt_pk_bf16_f32 v10, v12, v13\n"
  "v_cvt_pk_bf16_f32 v11, v14, v15\n"
  "v_mfma_f32_16x16x32_bf16 v[24:27], v[228:231], v[52:55], v[24:27]\n"
  "s_waitcnt vmcnt(14)\n"
  "ds_read_b128 a[232:235], v87 offset:0\n"
  "ds_read_b128 a[236:239], v88 offset:0\n"
  "ds_write_b128 v80, v[8:11] offset:5120\n"
  "v_pk_fma_f32 v[16:17], v[16:17], s[60:61], s[60:61] op_sel:[0,0,1] op_sel_hi:[1,0,1]\n"
  "v_pk_fma_f32 v[18:19], v[18:19], s[60:61], s[60:61] op_sel:[0,0,1] op_sel_hi:[1,0,1]\n"
  "v_pk_fma_f32 v[20:21], v[20:21], s[60:61], s[60:61] op_sel:[0,0,1] op_sel_hi:[1,0,1]\n"
  "v_pk_fma_f32 v[22:23], v[22:23], s[60:61], s[60:61] op_sel:[0,0,1] op_sel_hi:[1,0,1]\n"
  "v_mfma_f32_16x16x32_bf16 v[28:31], v[252:255], v[52:55], 0\n"
  "s_add_u32 m0, s59, 16384\n"
  "v_exp_f32 v16, v16\n"
  "v_mfma_f32_16x16x32_bf16 v[28:31], v[248:251], v[48:51], v[28:31]\n"
  "s_waitcnt lgkmcnt(3)\n"
  "buffer_load_dwordx4 v112, s[40:43], s57 offen offset:0 nt lds\n"
  "buffer_load_dwordx4 v113, s[40:43], s57 offen offset:1024 nt lds\n"
  "s_add_u32 s57, s57, 6144\n"
  "v_exp_f32 v17, v17\n"
  "v_mfma_f32_16x16x32_bf16 v[28:31], v[244:247], v[44:47], v[28:31]\n"
  "s_waitcnt vmcnt(14)\n"
  "ds_read_b128 a[224:227], v87 offset:2048\n"
  "ds_read_b128 a[228:231], v88 offset:2048\n"
  "v_exp_f32 v18, v18\n"
  "v_mfma_f32_16x16x32_bf16 v[28:31], v[240:243], v[40:43], v[28:31]\n"
  "s_add_u32 m0, s59, 0\n"
  "v_exp_f32 v19, v19\n"
  "v_mfma_f32_16x16x32_bf16 v[28:31], v[236:239], v[36:39], v[28:31]\n"
  "s_waitcnt lgkmcnt(3)\n"
  "buffer_load_dwordx4 v112, s[40:43], s57 offen offset:0 nt lds\n"
  "buffer_load_dwordx4 v113, s[40:43], s57 offen offset:1024 nt lds\n"
  "v_cvt_pk_bf16_f32 v16, v16, v17\n"
  "v_cvt_pk_bf16_f32 v17, v18, v19\n"
  "v_mfma_f32_16x16x32_bf16 v[28:31], v[232:235], v[32:35], v[28:31]\n"
  "s_waitcnt vmcnt(14)\n"
  "ds_read_b128 a[216:219], v87 offset:4096\n"
  "ds_read_b128 a[220:223], v88 offset:4096\n"
  "v_mfma_f32_16x16x32_bf16 v[0:3], a[0:3], v[56:59], 0\n"
  "s_add_u32 s66, s66, 6144\n"
  "s_cmp_eq_u32 s66, 86016\n"
  "s_cselect_b32 s66, 49152, s66\n"
  "s_add_u32 m0, s59, 1920\n"
  "v_exp_f32 v20, v20\n"
  "v_mfma_f32_16x16x32_bf16 v[0:3], a[4:7], v[60:63], v[0:3]\n"
  "s_waitcnt lgkmcnt(2)\n"
  "buffer_load_dwordx4 v112, s[40:43], s57 offen offset:128 nt lds\n"
  "buffer_load_dwordx4 v113, s[40:43], s57 offen offset:1152 nt lds\n"
  "v_exp_f32 v21, v21\n"
  "v_mfma_f32_16x16x32_bf16 v[0:3], a[8:11], v[64:67], v[0:3]\n"
  "s_waitcnt vmcnt(14)\n"
  "ds_read_b128 a[240:243], v87 offset:6144\n"
  "ds_read_b128 a[244:247], v88 offset:6144\n"
  "v_exp_f32 v22, v22\n"
  "v_mfma_f32_16x16x32_bf16 v[0:3], a[12:15], v[68:71], v[0:3]\n"
  "s_add_u32 m0, s59, 3840\n"
  "v_exp_f32 v23, v23\n"
  "v_mfma_f32_16x16x32_bf16 v[0:3], a[16:19], v[72:75], v[0:3]\n"
  "s_waitcnt lgkmcnt(2)\n"
  "buffer_load_dwordx4 v112, s[40:43], s57 offen offset:256 nt lds\n"
  "buffer_load_dwordx4 v113, s[40:43], s57 offen offset:1280 nt lds\n"
  "s_add_u32 s57, s57, 6144\n"
  "v_cvt_pk_bf16_f32 v18, v20, v21\n"
  "v_cvt_pk_bf16_f32 v19, v22, v23\n"
  "v_mfma_f32_16x16x32_bf16 v[0:3], a[20:23], v[76:79], v[0:3]\n"
  "s_waitcnt vmcnt(14)\n"
  "ds_read_b128 a[248:251], v87 offset:8192\n"
  "ds_read_b128 a[252:255], v88 offset:8192\n"
  "ds_write_b128 v80, v[16:19] offset:6144\n"
  "v_pk_fma_f32 v[24:25], v[24:25], s[60:61], s[60:61] op_sel:[0,0,1] op_sel_hi:[1,0,1]\n"
  "v_pk_fma_f32 v[26:27], v[26:27], s[60:61], s[60:61] op_sel:[0,0,1] op_sel_hi:[1,0,1]\n"
  "v_pk_fma_f32 v[28:29], v[28:29], s[60:61], s[60:61] op_sel:[0,0,1] op_sel_hi:[1,0,1]\n"
  "v_pk_fma_f32 v[30:31], v[30:31], s[60:61], s[60:61] op_sel:[0,0,1] op_sel_hi:[1,0,1]\n"
  "v_mfma_f32_16x16x32_bf16 v[4:7], a[44:47], v[76:79], 0\n"
  "v_exp_f32 v24, v24\n"
  "v_mfma_f32_16x16x32_bf16 v[4:7], a[40:43], v[72:75], v[4:7]\n"
  "s_add_u32 m0, s59, 5888\n"
  "v_exp_f32 v25, v25\n"
  "v_mfma_f32_16x16x32_bf16 v[4:7], a[36:39], v[68:71], v[4:7]\n"
  "s_waitcnt lgkmcnt(3)\n"
  "buffer_load_dwordx4 v112, s[40:43], s57 offen offset:256 nt lds\n"
  "buffer_load_dwordx4 v113, s[40:43], s57 offen offset:1280 nt lds\n"
  "v_exp_f32 v26, v26\n"
  "v_mfma_f32_16x16x32_bf16 v[4:7], a[32:35], v[64:67], v[4:7]\n"
  "s_waitcnt vmcnt(14)\n"
  "ds_read_b128 v[128:131], v87 offset:10240\n"
  "ds_read_b128 v[132:135], v88 offset:10240\n"
  "v_exp_f32 v27, v27\n"
  "v_mfma_f32_16x16x32_bf16 v[4:7], a[28:31], v[60:63], v[4:7]\n"
  "s_add_u32 m0, s59, 8064\n"
  "v_cvt_pk_bf16_f32 v24, v24, v25\n"
  "v_cvt_pk_bf16_f32 v25, v26, v27\n"
  "v_mfma_f32_16x16x32_bf16 v[4:7], a[24:27], v[56:59], v[4:7]\n"
  "s_waitcnt lgkmcnt(3)\n"
  "buffer_load_dwordx4 v112, s[40:43], s57 offen offset:128 nt lds\n"
  "buffer_load_dwordx4 v113, s[40:43], s57 offen offset:1152 nt lds\n"
  "v_add_u32 v81, s66, v83\n"
  "v_add_u32 v82, s66, v84\n"
  "v_mfma_f32_16x16x32_bf16 v[8:11], a[48:51], v[56:59], 0\n"
  "s_waitcnt vmcnt(14)\n"
  "ds_read_b128 v[152:155], v87 offset:12288\n"
  "ds_read_b128 v[156:159], v88 offset:12288\n"
  "v_exp_f32 v28, v28\n"
  "v_mfma_f32_16x16x32_bf16 v[8:11], a[52:55], v[60:63], v[8:11]\n"
  "s_add_u32 m0, s59, 10240\n"
  "v_exp_f32 v29, v29\n"
  "v_mfma_f32_16x16x32_bf16 v[8:11], a[56:59], v[64:67], v[8:11]\n"
  "s_waitcnt lgkmcnt(2)\n"
  "buffer_load_dwordx4 v112, s[40:43], s57 offen offset:0 nt lds\n"
  "buffer_load_dwordx4 v113, s[40:43], s57 offen offset:1024 nt lds\n"
  "s_add_u32 s57, s57, 6144\n"
  "v_exp_f32 v30, v30\n"
  "v_mfma_f32_16x16x32_bf16 v[8:11], a[60:63], v[68:71], v[8:11]\n"
  "s_waitcnt vmcnt(14)\n"
  "ds_read_b128 v[144:147], v87 offset:14336\n"
  "ds_read_b128 v[148:151], v88 offset:14336\n"
  "v_exp_f32 v31, v31\n"
  "v_mfma_f32_16x16x32_bf16 v[8:11], a[64:67], v[72:75], v[8:11]\n"
  "s_add_u32 m0, s59, 12288\n"
  "v_cvt_pk_bf16_f32 v26, v28, v29\n"
  "v_cvt_pk_bf16_f32 v27, v30, v31\n"
  "v_mfma_f32_16x16x32_bf16 v[8:11], a[68:71], v[76:79], v[8:11]\n"
  "s_waitcnt lgkmcnt(2)\n"
  "buffer_load_dwordx4 v112, s[40:43], s57 offen offset:0 nt lds\n"
  "buffer_load_dwordx4 v113, s[40:43], s57 offen offset:1024 nt lds\n"
  "ds_write_b128 v80, v[24:27] offset:7168\n"
  "v_pk_fma_f32 v[0:1], v[0:1], s[60:61], s[60:61] op_sel:[0,0,1] op_sel_hi:[1,0,1]\n"
  "v_pk_fma_f32 v[2:3], v[2:3], s[60:61], s[60:61] op_sel:[0,0,1] op_sel_hi:[1,0,1]\n"
  "v_pk_fma_f32 v[4:5], v[4:5], s[60:61], s[60:61] op_sel:[0,0,1] op_sel_hi:[1,0,1]\n"
  "v_pk_fma_f32 v[6:7], v[6:7], s[60:61], s[60:61] op_sel:[0,0,1] op_sel_hi:[1,0,1]\n"
  "v_mfma_f32_16x16x32_bf16 v[12:15], a[92:95], v[76:79], 0\n"
  "s_waitcnt vmcnt(14)\n"
  "ds_read_b128 v[136:139], v87 offset:16384\n"
  "ds_read_b128 v[140:143], v88 offset:16384\n"
  "v_exp_f32 v0, v0\n"
  "v_mfma_f32_16x16x32_bf16 v[12:15], a[88:91], v[72:75], v[12:15]\n"
  "s_add_u32 m0, s59, 14208\n"
  "v_exp_f32 v1, v1\n"
  "v_mfma_f32_16x16x32_bf16 v[12:15], a[84:87], v[68:71], v[12:15]\n"
  "s_waitcnt lgkmcnt(3)\n"
  "buffer_load_dwordx4 v112, s[40:43], s57 offen offset:128 nt lds\n"
  "buffer_load_dwordx4 v113, s[40:43], s57 offen offset:1152 nt lds\n"
  "v_exp_f32 v2, v2\n"
  "v_mfma_f32_16x16x32_bf16 v[12:15], a[80:83], v[64:67], v[12:15]\n"
  "s_waitcnt vmcnt(14)\n"
  "ds_read_b128 v[160:163], v87 offset:0\n"
  "ds_read_b128 v[164:167], v88 offset:0\n"
  "v_exp_f32 v3, v3\n"
  "v_mfma_f32_16x16x32_bf16 v[12:15], a[76:79], v[60:63], v[12:15]\n"
  "v_cvt_pk_bf16_f32 v0, v0, v1\n"
  "v_cvt_pk_bf16_f32 v1, v2, v3\n"
  "v_mfma_f32_16x16x32_bf16 v[12:15], a[72:75], v[56:59], v[12:15]\n"
  "s_add_u32 m0, s59, 16128\n"
  "s_waitcnt lgkmcnt(4)\n"
  "s_barrier\n"
  "v_xor_b32 v80, 0x4000, v80\n"
  "v_mfma_f32_16x16x32_bf16 v[16:19], a[96:99], v[56:59], 0\n"
  "s_waitcnt lgkmcnt(2)\n"
  "buffer_load_dwordx4 v112, s[40:43], s57 offen offset:256 nt lds\n"
  "buffer_load_dwordx4 v113, s[40:43], s57 offen offset:1280 nt lds\n"
  "s_add_u32 s57, s57, 6144\n"
  "v_exp_f32 v4, v4\n"
  "v_mfma_f32_16x16x32_bf16 v[16:19], a[100:103], v[60:63], v[16:19]\n"
  "s_waitcnt vmcnt(14)\n"
  "ds_read_b128 v[168:171], v87 offset:2048\n"
  "ds_read_b128 v[172:175], v88 offset:2048\n"
  "v_exp_f32 v5, v5\n"
  "v_mfma_f32_16x16x32_bf16 v[16:19], a[104:107], v[64:67], v[16:19]\n"
  "s_add_u32 m0, s59, -256\n"
  "v_exp_f32 v6, v6\n"
  "v_mfma_f32_16x16x32_bf16 v[16:19], a[108:111], v[68:71], v[16:19]\n"
  "s_waitcnt lgkmcnt(2)\n"
  "buffer_load_dwordx4 v112, s[40:43], s57 offen offset:256 nt lds\n"
  "buffer_load_dwordx4 v113, s[40:43], s57 offen offset:1280 nt lds\n"
  "v_exp_f32 v7, v7\n"
  "v_mfma_f32_16x16x32_bf16 v[16:19], a[112:115], v[72:75], v[16:19]\n"
  "s_waitcnt vmcnt(14)\n"
  "ds_read_b128 v[176:179], v87 offset:4096\n"
  "ds_read_b128 v[180:183], v88 offset:4096\n"
  "v_cvt_pk_bf16_f32 v2, v4, v5\n"
  "v_cvt_pk_bf16_f32 v3, v6, v7\n"
  "v_mfma_f32_16x16x32_bf16 v[16:19], a[116:119], v[76:79], v[16:19]\n"
  "s_add_u32 m0, s59, 1920\n"
  "ds_write_b128 v80, v[0:3] offset:0\n"
  "v_pk_fma_f32 v[8:9], v[8:9], s[60:61], s[60:61] op_sel:[0,0,1] op_sel_hi:[1,0,1]\n"
  "v_pk_fma_f32 v[10:11], v[10:11], s[60:61], s[60:61] op_sel:[0,0,1] op_sel_hi:[1,0,1]\n"
  "v_pk_fma_f32 v[12:13], v[12:13], s[60:61], s[60:61] op_sel:[0,0,1] op_sel_hi:[1,0,1]\n"
  "v_pk_fma_f32 v[14:15], v[14:15], s[60:61], s[60:61] op_sel:[0,0,1] op_sel_hi:[1,0,1]\n"
  "v_mfma_f32_16x16x32_bf16 v[20:23], a[140:143], v[76:79], 0\n"
  "s_waitcnt lgkmcnt(3)\n"
  "buffer_load_dwordx4 v112, s[40:43], s57 offen offset:128 nt lds\n"
  "buffer_load_dwordx4 v113, s[40:43], s57 offen offset:1152 nt lds\n"
  "v_exp_f32 v8, v8\n"
  "v_mfma_f32_16x16x32_bf16 v[20:23], a[136:139], v[72:75], v[20:23]\n"
  "s_waitcnt vmcnt(14)\n"
  "ds_read_b128 v[200:203], v87 offset:6144\n"
  "ds_read_b128 v[204:207], v88 offset:6144\n"
  "v_exp_f32 v9, v9\n"
  "v_mfma_f32_16x16x32_bf16 v[20:23], a[132:135], v[68:71], v[20:23]\n"
  "s_add_u32 m0, s59, 4096\n"
  "v_exp_f32 v10, v10\n"
  "v_mfma_f32_16x16x32_bf16 v[20:23], a[128:131], v[64:67], v[20:23]\n"
  "s_waitcnt lgkmcnt(3)\n"
  "buffer_load_dwordx4 v112, s[40:43], s57 offen offset:0 nt lds\n"
  "buffer_load_dwordx4 v113, s[40:43], s57 offen offset:1024 nt lds\n"
  "s_add_u32 s57, s57, 6144\n"
  "v_exp_f32 v11, v11\n"
  "v_mfma_f32_16x16x32_bf16 v[20:23], a[124:127], v[60:63], v[20:23]\n"
  "s_waitcnt vmcnt(14)\n"
  "ds_read_b128 v[192:195], v87 offset:8192\n"
  "ds_read_b128 v[196:199], v88 offset:8192\n"
  "v_cvt_pk_bf16_f32 v8, v8, v9\n"
  "v_cvt_pk_bf16_f32 v9, v10, v11\n"
  "v_mfma_f32_16x16x32_bf16 v[20:23], a[120:123], v[56:59], v[20:23]\n"
  "ds_read_b128 v[32:35], v81 offset:0\n"
  "ds_read_b128 v[36:39], v82 offset:0\n"
  "v_mfma_f32_16x16x32_bf16 v[24:27], a[144:147], v[56:59], 0\n"
  "v_exp_f32 v12, v12\n"
  "v_mfma_f32_16x16x32_bf16 v[24:27], a[148:151], v[60:63], v[24:27]\n"
  "s_waitcnt vmcnt(12)\n"
  "ds_read_b128 v[184:187], v87 offset:10240\n"
  "ds_read_b128 v[188:191], v88 offset:10240\n"
  "v_exp_f32 v13, v13\n"
  "v_mfma_f32_16x16x32_bf16 v[24:27], a[152:155], v[64:67], v[24:27]\n"
  "v_exp_f32 v14, v14\n"
  "v_mfma_f32_16x16x32_bf16 v[24:27], a[156:159], v[68:71], v[24:27]\n"
  "v_exp_f32 v15, v15\n"
  "v_mfma_f32_16x16x32_bf16 v[24:27], a[160:163], v[72:75], v[24:27]\n"
  "v_cvt_pk_bf16_f32 v10, v12, v13\n"
  "v_cvt_pk_bf16_f32 v11, v14, v15\n"
  "v_mfma_f32_16x16x32_bf16 v[24:27], a[164:167], v[76:79], v[24:27]\n"
  "s_waitcnt vmcnt(10)\n"
  "ds_read_b128 v[208:211], v87 offset:12288\n"
  "ds_read_b128 v[212:215], v88 offset:12288\n"
  "ds_write_b128 v80, v[8:11] offset:1024\n"
  "v_pk_fma_f32 v[16:17], v[16:17], s[60:61], s[60:61] op_sel:[0,0,1] op_sel_hi:[1,0,1]\n"
  "v_pk_fma_f32 v[18:19], v[18:19], s[60:61], s[60:61] op_sel:[0,0,1] op_sel_hi:[1,0,1]\n"
  "v_pk_fma_f32 v[20:21], v[20:21], s[60:61], s[60:61] op_sel:[0,0,1] op_sel_hi:[1,0,1]\n"
  "v_pk_fma_f32 v[22:23], v[22:23], s[60:61], s[60:61] op_sel:[0,0,1] op_sel_hi:[1,0,1]\n"
  "v_mfma_f32_16x16x32_bf16 v[28:31], a[188:191], v[76:79], 0\n"
  "v_exp_f32 v16, v16\n"
  "v_mfma_f32_16x16x32_bf16 v[28:31], a[184:187], v[72:75], v[28:31]\n"
  "v_exp_f32 v17, v17\n"
  "v_mfma_f32_16x16x32_bf16 v[28:31], a[180:183], v[68:71], v[28:31]\n"
  "s_waitcnt vmcnt(8)\n"
  "ds_read_b128 v[216:219], v87 offset:14336\n"
  "ds_read_b128 v[220:223], v88 offset:14336\n"
  "v_exp_f32 v18, v18\n"
  "v_mfma_f32_16x16x32_bf16 v[28:31], a[176:179], v[64:67], v[28:31]\n"
  "v_exp_f32 v19, v19\n"
  "v_mfma_f32_16x16x32_bf16 v[28:31], a[172:175], v[60:63], v[28:31]\n"
  "v_cvt_pk_bf16_f32 v16, v16, v17\n"
  "v_cvt_pk_bf16_f32 v17, v18, v19\n"
  "v_mfma_f32_16x16x32_bf16 v[28:31], a[168:171], v[56:59], v[28:31]\n"
  "s_waitcnt vmcnt(6)\n"
  "ds_read_b128 v[224:227], v87 offset:16384\n"
  "ds_read_b128 v[228:231], v88 offset:16384\n"
  "ds_read_b128 v[40:43], v81 offset:2048\n"
  "ds_read_b128 v[44:47], v82 offset:2048\n"
  "v_mfma_f32_16x16x32_bf16 v[0:3], a[192:195], v[56:59], 0\n"
  "v_exp_f32 v20, v20\n"
  "v_mfma_f32_16x16x32_bf16 v[0:3], a[196:199], v[60:63], v[0:3]\n"
  "v_exp_f32 v21, v21\n"
  "v_mfma_f32_16x16x32_bf16 v[0:3], a[200:203], v[64:67], v[0:3]\n"
  "s_waitcnt vmcnt(4)\n"
  "ds_read_b128 v[248:251], v87 offset:0\n"
  "ds_read_b128 v[252:255], v88 offset:0\n"
  "v_exp_f32 v22, v22\n"
  "v_mfma_f32_16x16x32_bf16 v[0:3], a[204:207], v[68:71], v[0:3]\n"
  "v_exp_f32 v23, v23\n"
  "v_mfma_f32_16x16x32_bf16 v[0:3], a[208:211], v[72:75], v[0:3]\n"
  "v_cvt_pk_bf16_f32 v18, v20, v21\n"
  "v_cvt_pk_bf16_f32 v19, v22, v23\n"
  "v_mfma_f32_16x16x32_bf16 v[0:3], a[212:215], v[76:79], v[0:3]\n"
  "s_waitcnt vmcnt(2)\n"
  "ds_read_b128 v[240:243], v87 offset:2048\n"
  "ds_read_b128 v[244:247], v88 offset:2048\n"
  "ds_write_b128 v80, v[16:19] offset:2048\n"
  "v_pk_fma_f32 v[24:25], v[24:25], s[60:61], s[60:61] op_sel:[0,0,1] op_sel_hi:[1,0,1]\n"
  "v_pk_fma_f32 v[26:27], v[26:27], s[60:61], s[60:61] op_sel:[0,0,1] op_sel_hi:[1,0,1]\n"
  "v_pk_fma_f32 v[28:29], v[28:29], s[60:61], s[60:61] op_sel:[0,0,1] op_sel_hi:[1,0,1]\n"
  "v_pk_fma_f32 v[30:31], v[30:31], s[60:61], s[60:61] op_sel:[0,0,1] op_sel_hi:[1,0,1]\n"
  "v_mfma_f32_16x16x32_bf16 v[4:7], a[236:239], v[76:79], 0\n"
  "v_exp_f32 v24, v24\n"
  "v_mfma_f32_16x16x32_bf16 v[4:7], a[232:235], v[72:75], v[4:7]\n"
  "v_exp_f32 v25, v25\n"
  "v_mfma_f32_16x16x32_bf16 v[4:7], a[228:231], v[68:71], v[4:7]\n"
  "v_exp_f32 v26, v26\n"
  "v_mfma_f32_16x16x32_bf16 v[4:7], a[224:227], v[64:67], v[4:7]\n"
  "s_waitcnt vmcnt(0)\n"
  "ds_read_b128 v[232:235], v87 offset:4096\n"
  "ds_read_b128 v[236:239], v88 offset:4096\n"
  "v_exp_f32 v27, v27\n"
  "v_mfma_f32_16x16x32_bf16 v[4:7], a[220:223], v[60:63], v[4:7]\n"
  "v_cvt_pk_bf16_f32 v24, v24, v25\n"
  "v_cvt_pk_bf16_f32 v25, v26, v27\n"
  "v_mfma_f32_16x16x32_bf16 v[4:7], a[216:219], v[56:59], v[4:7]\n"
  "ds_read_b128 v[48:51], v81 offset:4096\n"
  "ds_read_b128 v[52:55], v82 offset:4096\n"
  "v_mfma_f32_16x16x32_bf16 v[8:11], a[240:243], v[56:59], 0\n"
  "v_exp_f32 v28, v28\n"
  "v_mfma_f32_16x16x32_bf16 v[8:11], a[244:247], v[60:63], v[8:11]\n"
  "v_exp_f32 v29, v29\n"
  "v_mfma_f32_16x16x32_bf16 v[8:11], a[248:251], v[64:67], v[8:11]\n"
  "v_exp_f32 v30, v30\n"
  "v_mfma_f32_16x16x32_bf16 v[8:11], a[252:255], v[68:71], v[8:11]\n"
  "v_exp_f32 v31, v31\n"
  "v_mfma_f32_16x16x32_bf16 v[8:11], v[128:131], v[72:75], v[8:11]\n"
  "v_cvt_pk_bf16_f32 v26, v28, v29\n"
  "v_cvt_pk_bf16_f32 v27, v30, v31\n"
  "v_mfma_f32_16x16x32_bf16 v[8:11], v[132:135], v[76:79], v[8:11]\n"
  "ds_write_b128 v80, v[24:27] offset:3072\n"
  "v_pk_fma_f32 v[0:1], v[0:1], s[60:61], s[60:61] op_sel:[0,0,1] op_sel_hi:[1,0,1]\n"
  "v_pk_fma_f32 v[2:3], v[2:3], s[60:61], s[60:61] op_sel:[0,0,1] op_sel_hi:[1,0,1]\n"
  "v_pk_fma_f32 v[4:5], v[4:5], s[60:61], s[60:61] op_sel:[0,0,1] op_sel_hi:[1,0,1]\n"
  "v_pk_fma_f32 v[6:7], v[6:7], s[60:61], s[60:61] op_sel:[0,0,1] op_sel_hi:[1,0,1]\n"
  "v_mfma_f32_16x16x32_bf16 v[12:15], v[156:159], v[76:79], 0\n"
  "v_exp_f32 v0, v0\n"
  "v_mfma_f32_16x16x32_bf16 v[12:15], v[152:155], v[72:75], v[12:15]\n"
  "v_exp_f32 v1, v1\n"
  "v_mfma_f32_16x16x32_bf16 v[12:15], v[148:151], v[68:71], v[12:15]\n"
  "v_exp_f32 v2, v2\n"
  "v_mfma_f32_16x16x32_bf16 v[12:15], v[144:147], v[64:67], v[12:15]\n"
  "v_exp_f32 v3, v3\n"
  "v_mfma_f32_16x16x32_bf16 v[12:15], v[140:143], v[60:63], v[12:15]\n"
  "v_cvt_pk_bf16_f32 v0, v0, v1\n"
  "v_cvt_pk_bf16_f32 v1, v2, v3\n"
  "v_mfma_f32_16x16x32_bf16 v[12:15], v[136:139], v[56:59], v[12:15]\n"
  "v_mfma_f32_16x16x32_bf16 v[16:19], v[160:163], v[56:59], 0\n"
  "v_exp_f32 v4, v4\n"
  "v_mfma_f32_16x16x32_bf16 v[16:19], v[164:167], v[60:63], v[16:19]\n"
  "v_exp_f32 v5, v5\n"
  "v_mfma_f32_16x16x32_bf16 v[16:19], v[168:171], v[64:67], v[16:19]\n"
  "v_exp_f32 v6, v6\n"
  "v_mfma_f32_16x16x32_bf16 v[16:19], v[172:175], v[68:71], v[16:19]\n"
  "v_exp_f32 v7, v7\n"
  "v_mfma_f32_16x16x32_bf16 v[16:19], v[176:179], v[72:75], v[16:19]\n"
  "v_cvt_pk_bf16_f32 v2, v4, v5\n"
  "v_cvt_pk_bf16_f32 v3, v6, v7\n"
  "v_mfma_f32_16x16x32_bf16 v[16:19], v[180:183], v[76:79], v[16:19]\n"
  "s_waitcnt lgkmcnt(15)\n"
  "ds_write_b128 v80, v[0:3] offset:4096\n"
  "v_pk_fma_f32 v[8:9], v[8:9], s[60:61], s[60:61] op_sel:[0,0,1] op_sel_hi:[1,0,1]\n"
  "v_pk_fma_f32 v[10:11], v[10:11], s[60:61], s[60:61] op_sel:[0,0,1] op_sel_hi:[1,0,1]\n"
  "v_pk_fma_f32 v[12:13], v[12:13], s[60:61], s[60:61] op_sel:[0,0,1] op_sel_hi:[1,0,1]\n"
  "v_pk_fma_f32 v[14:15], v[14:15], s[60:61], s[60:61] op_sel:[0,0,1] op_sel_hi:[1,0,1]\n"
  "v_mfma_f32_16x16x32_bf16 v[20:23], v[204:207], v[76:79], 0\n"
  "v_exp_f32 v8, v8\n"
  "v_mfma_f32_16x16x32_bf16 v[20:23], v[200:203], v[72:75], v[20:23]\n"
  "v_exp_f32 v9, v9\n"
  "v_mfma_f32_16x16x32_bf16 v[20:23], v[196:199], v[68:71], v[20:23]\n"
  "v_exp_f32 v10, v10\n"
  "v_mfma_f32_16x16x32_bf16 v[20:23], v[192:195], v[64:67], v[20:23]\n"
  "v_exp_f32 v11, v11\n"
  "v_mfma_f32_16x16x32_bf16 v[20:23], v[188:191], v[60:63], v[20:23]\n"
  "v_cvt_pk_bf16_f32 v8, v8, v9\n"
  "v_cvt_pk_bf16_f32 v9, v10, v11\n"
  "v_mfma_f32_16x16x32_bf16 v[20:23], v[184:187], v[56:59], v[20:23]\n"
  "v_mfma_f32_16x16x32_bf16 v[24:27], v[208:211], v[56:59], 0\n"
  "v_exp_f32 v12, v12\n"
  "v_mfma_f32_16x16x32_bf16 v[24:27], v[212:215], v[60:63], v[24:27]\n"
  "s_waitcnt lgkmcnt(15)\n"
  "v_exp_f32 v13, v13\n"
  "v_mfma_f32_16x16x32_bf16 v[24:27], v[216:219], v[64:67], v[24:27]\n"
  "v_exp_f32 v14, v14\n"
  "v_mfma_f32_16x16x32_bf16 v[24:27], v[220:223], v[68:71], v[24:27]\n"
  "s_waitcnt lgkmcnt(13)\n"
  "v_exp_f32 v15, v15\n"
  "v_mfma_f32_16x16x32_bf16 v[24:27], v[224:227], v[72:75], v[24:27]\n"
  "v_cvt_pk_bf16_f32 v10, v12, v13\n"
  "v_cvt_pk_bf16_f32 v11, v14, v15\n"
  "v_mfma_f32_16x16x32_bf16 v[24:27], v[228:231], v[76:79], v[24:27]\n"
  "s_waitcnt lgkmcnt(9)\n"
  "ds_write_b128 v80, v[8:11] offset:5120\n"
  "v_pk_fma_f32 v[16:17], v[16:17], s[60:61], s[60:61] op_sel:[0,0,1] op_sel_hi:[1,0,1]\n"
  "v_pk_fma_f32 v[18:19], v[18:19], s[60:61], s[60:61] op_sel:[0,0,1] op_sel_hi:[1,0,1]\n"
  "v_pk_fma_f32 v[20:21], v[20:21], s[60:61], s[60:61] op_sel:[0,0,1] op_sel_hi:[1,0,1]\n"
  "v_pk_fma_f32 v[22:23], v[22:23], s[60:61], s[60:61] op_sel:[0,0,1] op_sel_hi:[1,0,1]\n"
  "v_mfma_f32_16x16x32_bf16 v[28:31], v[252:255], v[76:79], 0\n"
  "v_exp_f32 v16, v16\n"
  "v_mfma_f32_16x16x32_bf16 v[28:31], v[248:251], v[72:75], v[28:31]\n"
  "s_waitcnt lgkmcnt(8)\n"
  "v_exp_f32 v17, v17\n"
  "v_mfma_f32_16x16x32_bf16 v[28:31], v[244:247], v[68:71], v[28:31]\n"
  "v_exp_f32 v18, v18\n"
  "v_mfma_f32_16x16x32_bf16 v[28:31], v[240:243], v[64:67], v[28:31]\n"
  "s_waitcnt lgkmcnt(5)\n"
  "s_sub_u32 s56, s56, 1\n"
  "v_exp_f32 v19, v19\n"
  "v_mfma_f32_16x16x32_bf16 v[28:31], v[236:239], v[60:63], v[28:31]\n"
  "v_cvt_pk_bf16_f32 v16, v16, v17\n"
  "v_cvt_pk_bf16_f32 v17, v18, v19\n"
  "v_mfma_f32_16x16x32_bf16 v[28:31], v[232:235], v[56:59], v[28:31]\n"
  "s_waitcnt lgkmcnt(3)\n"
  "s_branch A_H0_%=\n"
  "A_LAST_%=:\n"
  "v_exp_f32 v20, v20\n"
  "v_exp_f32 v21, v21\n"
  "v_exp_f32 v22, v22\n"
  "v_exp_f32 v23, v23\n"
  "v_pk_fma_f32 v[24:25], v[24:25], s[60:61], s[60:61] op_sel:[0,0,1] op_sel_hi:[1,0,1]\n"
  "v_pk_fma_f32 v[26:27], v[26:27], s[60:61], s[60:61] op_sel:[0,0,1] op_sel_hi:[1,0,1]\n"
  "v_cvt_pk_bf16_f32 v18, v20, v21\n"
  "v_cvt_pk_bf16_f32 v19, v22, v23\n"
  "ds_write_b128 v80, v[16:19] offset:6144\n"
  "v_pk_fma_f32 v[28:29], v[28:29], s[60:61], s[60:61] op_sel:[0,0,1] op_sel_hi:[1,0,1]\n"
  "v_pk_fma_f32 v[30:31], v[30:31], s[60:61], s[60:61] op_sel:[0,0,1] op_sel_hi:[1,0,1]\n"
  "v_exp_f32 v24, v24\n"
  "v_exp_f32 v25, v25\n"
  "v_exp_f32 v26, v26\n"
  "v_exp_f32 v27, v27\n"
  "v_exp_f32 v28, v28\n"
  "v_exp_f32 v29, v29\n"
  "v_cvt_pk_bf16_f32 v24, v24, v25\n"
  "v_cvt_pk_bf16_f32 v25, v26, v27\n"
  "v_exp_f32 v30, v30\n"
  "v_exp_f32 v31, v31\n"
  "v_cvt_pk_bf16_f32 v26, v28, v29\n"
  "v_cvt_pk_bf16_f32 v27, v30, v31\n"
  "ds_write_b128 v80, v[24:27] offset:7168\n"
  "s_waitcnt lgkmcnt(0)\n"
  "s_barrier\n"
  "s_mov_b32 %0, 0\n"
  "s_branch L_END_%=\n"
  "B_START_%=:\n"
  "s_add_u32 s44, s76, s62\n"
  "s_addc_u32 s45, s77, 0\n"
  "s_add_u32 m0, s66, 0\n"
  "s_nop 0\n"
  "buffer_load_dwordx4 v112, s[44:47], s67 offen offset:0 nt lds\n"
  "s_add_u32 m0, s66, 1920\n"
  "s_nop 0\n"
  "buffer_load_dwordx4 v112, s[44:47], s67 offen offset:128 nt lds\n"
  "s_add_u32 m0, s66, 3840\n"
  "s_nop 0\n"
  "buffer_load_dwordx4 v112, s[44:47], s67 offen offset:256 nt lds\n"
  "s_add_u32 m0, s66, 6144\n"
  "s_nop 0\n"
  "buffer_load_dwordx4 v112, s[44:47], s64 offen offset:0 nt lds\n"
  "s_add_u32 m0, s66, 8064\n"
  "s_nop 0\n"
  "buffer_load_dwordx4 v112, s[44:47], s64 offen offset:128 nt lds\n"
  "s_add_u32 m0, s66, 9984\n"
  "s_nop 0\n"
  "buffer_load_dwordx4 v112, s[44:47], s64 offen offset:256 nt lds\n"
  "s_sub_u32 s40, s80, 2048\n"
  "s_subb_u32 s41, s81, 0\n"
  "s_mov_b32 s42, 100665344\n"
  "s_mov_b64 s[48:49], s[82:83]\n"
  "s_mov_b32 s50, 166723584\n"
  "s_mov_b32 s51, 0x20000\n"
  "s_lshr_b32 s62, s52, 16\n"
  "s_lshl1_add_u32 s62, s62, s65\n"
  "s_mul_i32 s57, s62, 98304\n"
  "s_and_b32 s56, s52, 0xff\n"
  "s_cmp_lg_u32 s53, 0\n"
  "s_cselect_b32 s62, 6, 0\n"
  "s_sub_u32 s56, s56, s62\n"
  "s_bitcmp1_b32 %7, 3\n"
  "s_cselect_b32 s62, 49152, 0\n"
  "s_add_u32 s57, s57, s62\n"
  "s_mov_b32 s68, 0xf0000000\n"
  "s_mov_b32 s69, 0\n"
  "s_sub_u32 s70, s67, s68\n"
  "s_mov_b32 s64, s67\n"
  "s_and_b32 s90, s52, 0xff\n"
  "s_sub_u32 s90, s90, 1\n"
  "s_mul_i32 s91, s90, 6144\n"
  "s_add_u32 s91, s91, s64\n"
  "s_lshr_b32 s62, s53, 16\n"
  "s_mul_i32 s62, s62, 53\n"
  "s_bfe_u32 s63, s53, 0x80008\n"
  "s_add_u32 s62, s62, s63\n"
  "s_mul_i32 s64, s62, 6144\n"
  "s_cmp_eq_u32 s53, 0\n"
  "s_cmov_b32 s64, 0xf0000000\n"
  "s_sub_u32 s91, s64, s91\n"
  "s_mov_b32 s96, s91\n"
  "s_and_b32 s92, s53, 0xff\n"
  "s_sub_u32 s92, s92, 1\n"
  "s_mul_i32 s93, s92, 6144\n"
  "s_add_u32 s93, s93, s64\n"
  "s_lshr_b32 s62, s54, 16\n"
  "s_mul_i32 s62, s62, 53\n"
  "s_bfe_u32 s63, s54, 0x80008\n"
  "s_add_u32 s62, s62, s63\n"
  "s_mul_i32 s64, s62, 6144\n"
  "s_cmp_eq_u32 s54, 0\n"
  "s_cmov_b32 s64, 0xf0000000\n"
  "s_sub_u32 s93, s64, s93\n"
  "s_mov_b32 s97, s93\n"
  "s_and_b32 s94, s54, 0xff\n"
  "s_sub_u32 s94, s94, 1\n"
  "s_mul_i32 s95, s94, 6144\n"
  "s_add_u32 s95, s95, s64\n"
  "s_lshr_b32 s62, s55, 16\n"
  "s_mul_i32 s62, s62, 53\n"
  "s_bfe_u32 s63, s55, 0x80008\n"
  "s_add_u32 s62, s62, s63\n"
  "s_mul_i32 s64, s62, 6144\n"
  "s_cmp_eq_u32 s55, 0\n"
  "s_cmov_b32 s64, 0xf0000000\n"
  "s_sub_u32 s95, s64, s95\n"
  "s_add_u32 s66, s66, 6144\n"
  "s_cmp_ge_u32 s66, 86016\n"
  "s_cselect_b32 s64, 36864, 0\n"
  "s_sub_u32 s66, s66, s64\n"
  "s_sub_u32 s90, s90, 1\n"
  "s_cselect_b32 s63, s91, 6144\n"
  "s_cmov_b64 s[90:91], s[92:93]\n"
  "s_cmov_b64 s[92:93], s[94:95]\n"
  "s_add_u32 s67, s67, s63\n"
  "s_add_u32 s66, s66, 6144\n"
  "s_cmp_ge_u32 s66, 86016\n"
  "s_cselect_b32 s64, 36864, 0\n"
  "s_sub_u32 s66, s66, s64\n"
  "s_sub_u32 s90, s90, 1\n"
  "s_cselect_b32 s63, s91, 6144\n"
  "s_cmov_b64 s[90:91], s[92:93]\n"
  "s_cmov_b64 s[92:93], s[94:95]\n"
  "s_add_u32 s67, s67, s63\n"
  "v_lshrrev_b32 v1, 3, v0\n"
  "v_mul_u32_u24 v114, 0x180, v1\n"
  "v_lshrrev_b32 v1, 4, v0\n"
  "v_and_b32 v2, 7, v0\n"
  "v_xor_b32 v2, v2, v1\n"
  "v_lshl_add_u32 v114, v2, 4, v114\n"
  "s_lshl_b32 s62, s65, 8\n"
  "s_add_u32 s62, s62, 2048\n"
  "v_add_u32 v114, s62, v114\n"
  "v_add_u32 v115, 0x800, v114\n"
  "v_lshrrev_b32 v2, 2, v0\n"
  "v_mul_u32_u24 v116, 0x180, v2\n"
  "v_and_b32 v1, 1, v1\n"
  "v_and_b32 v2, 3, v0\n"
  "v_xor_b32 v2, v2, v1\n"
  "v_lshl_add_u32 v116, v2, 4, v116\n"
  "s_lshl_b32 s62, s65, 6\n"
  "s_add_u32 s62, s62, 128\n"
  "v_add_u32 v116, s62, v116\n"
  "s_mov_b32 s58, 0xfffd1800\n"
  "s_cmp_eq_u32 s65, 0\n"
  "s_cmov_b32 s58, 6144\n"
  "s_cselect_b64 s[80:81], s[78:79], s[80:81]\n"
  "s_mov_b32 s82, 100663296\n"
  "s_mov_b32 s83, 0x20000\n"
  "s_mov_b64 s[76:77], s[78:79]\n"
  "s_mov_b32 s78, 100663296\n"
  "s_mov_b32 s79, 0x20000\n"
  "s_add_u32 s87, s58, 6144\n"
  "s_sub_u32 s87, s87, s86\n"
  "v_lshrrev_b32 v1, 2, v0\n"
  "v_lshlrev_b32 v117, 7, v1\n"
  "v_lshlrev_b32 v121, 6, v1\n"
  "v_and_b32 v2, 1, v0\n"
  "v_lshl_add_u32 v117, v2, 3, v117\n"
  "v_lshl_add_u32 v121, v2, 3, v121\n"
  "v_bfe_u32 v2, v0, 1, 1\n"
  "v_lshl_add_u32 v117, v2, 6, v117\n"
  "v_lshl_add_u32 v121, v2, 5, v121\n"
  "v_bfe_u32 v2, v0, 3, 2\n"
  "v_lshl_add_u32 v117, v2, 4, v117\n"
  "v_bfe_u32 v2, v0, 4, 1\n"
  "v_lshl_add_u32 v121, v2, 4, v121\n"
  "v_add_u32 v117, s59, v117\n"
  "v_add_u32 v121, s59, v121\n"
  "v_xor_b32 v118, 16, v117\n"
  "v_xor_b32 v119, 32, v117\n"
  "v_xor_b32 v120, 48, v117\n"
  "v_xor_b32 v122, 16, v121\n"
  "s_lshl_b32 s62, s65, 13\n"
  "v_lshlrev_b32 v104, 4, v0\n"
  "v_add_u32 v104, s62, v104\n"
  "v_xor_b32 v105, 0x2000, v104\n"
  "v_lshrrev_b32 v1, 5, v0\n"
  "v_mul_u32_u24 v1, 0x480, v1\n"
  "v_and_b32 v2, 31, v0\n"
  "v_lshl_add_u32 v106, v2, 3, v1\n"
  "s_mul_i32 s62, s65, 4608\n"
  "s_add_u32 s62, s62, 32768\n"
  "v_add_u32 v106, s62, v106\n"
  "v_and_b32 v2, 7, v0\n"
  "v_lshl_add_u32 v107, v2, 3, v1\n"
  "v_bfe_u32 v2, v0, 3, 1\n"
  "v_lshl_add_u32 v107, v2, 2, v107\n"
  "v_bfe_u32 v2, v0, 4, 1\n"
  "v_lshl_add_u32 v107, v2, 7, v107\n"
  "s_lshl_b32 s62, s65, 6\n"
  "s_add_u32 s62, s62, 35072\n"
  "v_add_u32 v107, s62, v107\n"
  "v_lshrrev_b32 v1, 4, v0\n"
  "v_mul_u32_u24 v108, 0x120, v1\n"
  "v_and_b32 v2, 3, v0\n"
  "v_lshl_add_u32 v108, v2, 6, v108\n"
  "v_bfe_u32 v2, v0, 2, 2\n"
  "v_lshl_add_u32 v108, v2, 3, v108\n"
  "s_mul_i32 s62, s65, 1152\n"
  "s_add_u32 s62, s62, 40960\n"
  "v_add_u32 v108, s62, v108\n"
  "v_bfe_u32 v2, v0, 3, 1\n"
  "v_lshl_add_u32 v2, v2, 2, v1\n"
  "v_mul_u32_u24 v109, 0x180, v2\n"
  "v_and_b32 v2, 7, v0\n"
  "v_lshl_add_u32 v109, v2, 4, v109\n"
  "s_mul_i32 s62, s65, 3072\n"
  "v_add_u32 v109, s62, v109\n"
  "s_mov_b32 s62, 47872\n"
  "v_lshl_add_u32 v111, v1, 4, s62\n"
  "s_lshl_b32 s62, s65, 6\n"
  "s_add_u32 s62, s62, 39680\n"
  "v_lshl_add_u32 v110, v0, 2, s62\n"
  "v_mov_b32 v3, 0x40000\n"
  "v_cmp_gt_u32 vcc, 16, v0\n"
  "s_nop 1\n"
  "v_cndmask_b32 v110, v3, v110, vcc\n"
  "v_mov_b32 v44, 1.0\n"
  "ds_write_b32 v110, v44 offset:8192\n"
  "v_and_b32 v2, 15, v0\n"
  "v_mov_b32 v124, 0x44444444\n"
  "v_mov_b32 v3, 0xeeeeeeee\n"
  "v_cmp_eq_u32 vcc, 1, v2\n"
  "s_nop 1\n"
  "v_cndmask_b32 v124, v124, v3, vcc\n"
  "v_mov_b32 v3, 0x3f803f80\n"
  "v_cmp_gt_u32 vcc, 2, v2\n"
  "v_mov_b32 v80, 0\n"
  "v_mov_b32 v81, 0\n"
  "v_mov_b32 v82, 0\n"
  "v_mov_b32 v83, 0\n"
  "v_cndmask_b32 v88, 0, v3, vcc\n"
  "v_cndmask_b32 v89, 0, v3, vcc\n"
  "v_cndmask_b32 v90, 0, v3, vcc\n"
  "v_cndmask_b32 v91, 0, v3, vcc\n"
  "v_mov_b32 v123, 0\n"
  "s_waitcnt vmcnt(0)\n"
  "s_barrier\n"
  "v_lshlrev_b32 v125, 7, v0\n"
  "v_mov_b32 v126, 0\n"
  "s_mov_b32 s62, 0x20d00000\n"
  "v_writelane_b32 v126, s62, 2\n"
  "s_mov_b32 s62, 0x311428d2\n"
  "v_writelane_b32 v126, s62, 3\n"
  "s_mov_b32 s62, 0x39960000\n"
  "v_writelane_b32 v126, s62, 4\n"
  "s_mov_b32 s62, 0x49da41d8\n"
  "v_writelane_b32 v126, s62, 5\n"
  "s_mov_b32 s62, 0x521c\n"
  "v_writelane_b32 v126, s62, 6\n"
  "s_mov_b32 s62, 0x62e05a9e\n"
  "v_writelane_b32 v126, s62, 7\n"
  "s_mov_b32 s62, 0x73246ae2\n"
  "v_writelane_b32 v126, s62, 8\n"
  "s_mov_b32 s62, 0x83a60000\n"
  "v_writelane_b32 v126, s62, 9\n"
  "s_mov_b32 s62, 0x942a8be8\n"
  "v_writelane_b32 v126, s62, 10\n"
  "s_mov_b32 s62, 0xa4ae9c6c\n"
  "v_writelane_b32 v126, s62, 11\n"
  "s_mov_b32 s62, 0xad2dacef\n"
  "v_writelane_b32 v126, s62, 12\n"
  "s_mov_b32 s62, 0xbd69b56b\n"
  "v_writelane_b32 v126, s62, 13\n"
  "s_mov_b32 s62, 0xc5a7\n"
  "v_writelane_b32 v126, s62, 14\n"
  "s_mov_b32 s62, 0xd663ce25\n"
  "v_writelane_b32 v126, s62, 15\n"
  "s_mov_b32 s62, 0xe69fde61\n"
  "v_writelane_b32 v126, s62, 16\n"
  "s_mov_b32 s62, 0xef1d0000\n"
  "v_writelane_b32 v126, s62, 17\n"
  "s_mov_b32 s62, 0xff59f75b\n"
  "v_writelane_b32 v126, s62, 18\n"
  "s_mov_b32 s62, 0x797\n"
  "v_writelane_b32 v126, s62, 19\n"
  "s_mov_b32 s62, 0x130015\n"
  "v_writelane_b32 v126, s62, 20\n"
  "s_mov_b32 s62, 0x11\n"
  "v_writelane_b32 v126, s62, 21\n"
  "s_mov_b32 s62, 0x20f00000\n"
  "v_writelane_b32 v126, s62, 29\n"
  "s_mov_b32 s62, 0x313228f1\n"
  "v_writelane_b32 v126, s62, 30\n"
  "s_mov_b32 s62, 0x39b30000\n"
  "v_writelane_b32 v126, s62, 31\n"
  "s_mov_b32 s62, 0x49f541f4\n"
  "v_writelane_b32 v126, s62, 32\n"
  "s_mov_b32 s62, 0x5236\n"
  "v_writelane_b32 v126, s62, 33\n"
  "s_mov_b32 s62, 0x62f85ab7\n"
  "v_writelane_b32 v126, s62, 34\n"
  "s_mov_b32 s62, 0x733a6af9\n"
  "v_writelane_b32 v126, s62, 35\n"
  "s_mov_b32 s62, 0x83bb0000\n"
  "v_writelane_b32 v126, s62, 36\n"
  "s_mov_b32 s62, 0x943d8bfc\n"
  "v_writelane_b32 v126, s62, 37\n"
  "s_mov_b32 s62, 0xa4bf9c7e\n"
  "v_writelane_b32 v126, s62, 38\n"
  "s_mov_b32 s62, 0xad0eaccf\n"
  "v_writelane_b32 v126, s62, 39\n"
  "s_mov_b32 s62, 0xbd4cb54d\n"
  "v_writelane_b32 v126, s62, 40\n"
  "s_mov_b32 s62, 0xc58b\n"
  "v_writelane_b32 v126, s62, 41\n"
  "s_mov_b32 s62, 0xd649ce0a\n"
  "v_writelane_b32 v126, s62, 42\n"
  "s_mov_b32 s62, 0xe687de48\n"
  "v_writelane_b32 v126, s62, 43\n"
  "s_mov_b32 s62, 0xef060000\n"
  "v_writelane_b32 v126, s62, 44\n"
  "s_mov_b32 s62, 0xff44f745\n"
  "v_writelane_b32 v126, s62, 45\n"
  "s_mov_b32 s62, 0x783\n"
  "v_writelane_b32 v126, s62, 46\n"
  "s_mov_b32 s62, 0x10002\n"
  "v_writelane_b32 v126, s62, 47\n"
  "s_mov_b32 s62, 0x800\n"
  "v_writelane_b32 v126, s62, 48\n"
  "s_lshr_b32 s62, %7, 8\n"
  "s_and_b32 s63, %7, 7\n"
  "s_mul_i32 s63, s63, 24\n"
  "v_mov_b32 v3, 0xffff\n"
  "s_mul_i32 s64, s65, 5\n"
  "v_mov_b32 v6, s63\n"
  "s_and_b32 s63, %7, 7\n"
  "s_xor_b32 s63, s63, 1\n"
  "s_mul_i32 s63, s63, 24\n"
  "s_add_u32 s63, s63, 9216\n"
  "v_mov_b32 v7, s63\n"
  "s_add_u32 s64, s64, 6\n"
  "v_bfe_u32 v2, v126, 0, 6\n"
  "v_mul_u32_u24 v4, 192, v2\n"
  "v_lshrrev_b32 v1, s64, v126\n"
  "v_cmp_gt_u32 vcc, 16, v2\n"
  "s_nop 1\n"
  "v_cndmask_b32 v8, v6, v7, vcc\n"
  "v_bfe_u32 v2, v126, 0, 16\n"
  "v_sub_u32 v1, s62, v1\n"
  "v_and_b32 v1, 31, v1\n"
  "v_cmp_gt_u32 vcc, 24, v1\n"
  "v_add3_u32 v4, v4, v1, v8\n"
  "s_nop 0\n"
  "v_cndmask_b32 v4, v3, v4, vcc\n"
  "v_cmp_ne_u32 vcc, 0, v2\n"
  "s_nop 1\n"
  "v_cndmask_b32 v4, v3, v4, vcc\n"
  "s_add_u32 s64, s64, 16\n"
  "v_bfe_u32 v2, v126, 16, 6\n"
  "v_mul_u32_u24 v5, 192, v2\n"
  "v_lshrrev_b32 v1, s64, v126\n"
  "v_cmp_gt_u32 vcc, 16, v2\n"
  "s_nop 1\n"
  "v_cndmask_b32 v8, v6, v7, vcc\n"
  "v_bfe_u32 v2, v126, 16, 16\n"
  "v_sub_u32 v1, s62, v1\n"
  "v_and_b32 v1, 31, v1\n"
  "v_cmp_gt_u32 vcc, 24, v1\n"
  "v_add3_u32 v5, v5, v1, v8\n"
  "s_nop 0\n"
  "v_cndmask_b32 v5, v3, v5, vcc\n"
  "v_cmp_ne_u32 vcc, 0, v2\n"
  "s_nop 1\n"
  "v_cndmask_b32 v5, v3, v5, vcc\n"
  "v_lshl_or_b32 v126, v5, 16, v4\n"
  "s_mul_i32 s72, s65, -2\n"
  "s_add_u32 s72, s72, 1\n"
  "s_mov_b32 s71, 0x53800000\n"
  "s_sub_u32 s75, s59, 36864\n"
  "s_barrier\n"
  "s_add_u32 m0, s66, 0\n"
  "s_nop 0\n"
  "buffer_load_dwordx4 v112, s[44:47], s67 offen offset:0 nt lds\n"
  "s_add_u32 m0, s66, 1920\n"
  "s_nop 0\n"
  "buffer_load_dwordx4 v112, s[44:47], s67 offen offset:128 nt lds\n"
  "s_add_u32 m0, s66, 3840\n"
  "s_nop 0\n"
  "buffer_load_dwordx4 v112, s[44:47], s67 offen offset:256 nt lds\n"
  "s_add_u32 s66, s66, 6144\n"
  "s_cmp_ge_u32 s66, 86016\n"
  "s_cselect_b32 s64, 36864, 0\n"
  "s_sub_u32 s66, s66, s64\n"
  "s_sub_u32 s90, s90, 1\n"
  "s_cselect_b32 s63, s91, 6144\n"
  "s_cmov_b64 s[90:91], s[92:93]\n"
  "s_cmov_b64 s[92:93], s[94:95]\n"
  "s_add_u32 s67, s67, s63\n"
  "s_add_u32 m0, s66, 0\n"
  "s_nop 0\n"
  "buffer_load_dwordx4 v112, s[44:47], s67 offen offset:0 nt lds\n"
  "s_add_u32 m0, s66, 1920\n"
  "s_nop 0\n"
  "buffer_load_dwordx4 v112, s[44:47], s67 offen offset:128 nt lds\n"
  "s_add_u32 m0, s66, 3840\n"
  "s_nop 0\n"
  "buffer_load_dwordx4 v112, s[44:47], s67 offen offset:256 nt lds\n"
  "s_add_u32 s66, s66, 6144\n"
  "s_cmp_ge_u32 s66, 86016\n"
  "s_cselect_b32 s64, 36864, 0\n"
  "s_sub_u32 s66, s66, s64\n"
  "s_sub_u32 s90, s90, 1\n"
  "s_cselect_b32 s63, s91, 6144\n"
  "s_cmov_b64 s[90:91], s[92:93]\n"
  "s_cmov_b64 s[92:93], s[94:95]\n"
  "s_add_u32 s67, s67, s63\n"
  "s_add_u32 m0, s59, 0\n"
  "s_nop 0\n"
  "buffer_load_dwordx4 v114, s[40:43], s57 offen offset:0  lds\n"
  "buffer_load_dwordx4 v115, s[40:43], s57 offen offset:1024  lds\n"
  "buffer_load_dwordx4 v116, s[40:43], s57 offen offset:2048  lds\n"
  "s_add_u32 s57, s57, 6144\n"
  "s_add_u32 m0, s59, 3072\n"
  "s_nop 0\n"
  "buffer_load_dwordx4 v114, s[40:43], s57 offen offset:0  lds\n"
  "buffer_load_dwordx4 v115, s[40:43], s57 offen offset:1024  lds\n"
  "buffer_load_dwordx4 v116, s[40:43], s57 offen offset:2048  lds\n"
  "s_add_u32 s57, s57, 6144\n"
  "s_add_u32 m0, s59, 6144\n"
  "s_nop 0\n"
  "buffer_load_dwordx4 v114, s[40:43], s57 offen offset:0  lds\n"
  "buffer_load_dwordx4 v115, s[40:43], s57 offen offset:1024  lds\n"
  "buffer_load_dwordx4 v116, s[40:43], s57 offen offset:2048  lds\n"
  "s_add_u32 s57, s57, 6144\n"
  "s_add_u32 m0, s59, 9216\n"
  "s_nop 0\n"
  "buffer_load_dwordx4 v114, s[40:43], s57 offen offset:0  lds\n"
  "buffer_load_dwordx4 v115, s[40:43], s57 offen offset:1024  lds\n"
  "buffer_load_dwordx4 v116, s[40:43], s57 offen offset:2048  lds\n"
  "s_add_u32 s57, s57, 6144\n"
  "s_add_u32 m0, s59, 12288\n"
  "s_nop 0\n"
  "buffer_load_dwordx4 v114, s[40:43], s57 offen offset:0  lds\n"
  "buffer_load_dwordx4 v115, s[40:43], s57 offen offset:1024  lds\n"
  "buffer_load_dwordx4 v116, s[40:43], s57 offen offset:2048  lds\n"
  "s_add_u32 s57, s57, 6144\n"
  "s_add_u32 m0, s59, 15360\n"
  "s_nop 0\n"
  "buffer_load_dwordx4 v114, s[40:43], s57 offen offset:0  lds\n"
  "buffer_load_dwordx4 v115, s[40:43], s57 offen offset:1024  lds\n"
  "buffer_load_dwordx4 v116, s[40:43], s57 offen offset:2048  lds\n"
  "s_add_u32 s57, s57, 6144\n"
  "s_waitcnt vmcnt(15)\n"
  "ds_read_b64_tr_b16 a[0:1], v117 offset:0\n"
  "ds_read_b64_tr_b16 a[4:5], v118 offset:0\n"
  "ds_read_b64_tr_b16 a[8:9], v119 offset:0\n"
  "ds_read_b64_tr_b16 a[12:13], v120 offset:0\n"
  "ds_read_b64_tr_b16 a[16:17], v121 offset:2048\n"
  "ds_read_b64_tr_b16 a[20:21], v122 offset:2048\n"
  "s_waitcnt vmcnt(12)\n"
  "ds_read_b64_tr_b16 a[2:3], v117 offset:3072\n"
  "ds_read_b64_tr_b16 a[6:7], v118 offset:3072\n"
  "s_add_u32 m0, s59, 0\n"
  "ds_read_b64_tr_b16 a[10:11], v119 offset:3072\n"
  "ds_read_b64_tr_b16 a[14:15], v120 offset:3072\n"
  "s_waitcnt lgkmcnt(4)\n"
  "buffer_load_dwordx4 v114, s[40:43], s57 offen offset:0  lds\n"
  "buffer_load_dwordx4 v115, s[40:43], s57 offen offset:1024  lds\n"
  "ds_read_b64_tr_b16 a[18:19], v121 offset:5120\n"
  "ds_read_b64_tr_b16 a[22:23], v122 offset:5120\n"
  "s_waitcnt vmcnt(11)\n"
  "ds_read_b64_tr_b16 a[24:25], v117 offset:6144\n"
  "ds_read_b64_tr_b16 a[28:29], v118 offset:6144\n"
  "buffer_load_dwordx4 v116, s[40:43], s57 offen offset:2048  lds\n"
  "s_add_u32 s57, s57, 6144\n"
  "s_add_u32 m0, s59, 3072\n"
  "ds_read_b64_tr_b16 a[32:33], v119 offset:6144\n"
  "ds_read_b64_tr_b16 a[36:37], v120 offset:6144\n"
  "s_waitcnt lgkmcnt(4)\n"
  "buffer_load_dwordx4 v114, s[40:43], s57 offen offset:0  lds\n"
  "buffer_load_dwordx4 v115, s[40:43], s57 offen offset:1024  lds\n"
  "ds_read_b64_tr_b16 a[40:41], v121 offset:8192\n"
  "ds_read_b64_tr_b16 a[44:45], v122 offset:8192\n"
  "s_waitcnt vmcnt(11)\n"
  "ds_read_b64_tr_b16 a[26:27], v117 offset:9216\n"
  "ds_read_b64_tr_b16 a[30:31], v118 offset:9216\n"
  "buffer_load_dwordx4 v116, s[40:43], s57 offen offset:2048  lds\n"
  "s_add_u32 s57, s57, s86\n"
  "s_add_u32 m0, s59, 6144\n"
  "ds_read_b64_tr_b16 a[34:35], v119 offset:9216\n"
  "ds_read_b64_tr_b16 a[38:39], v120 offset:9216\n"
  "s_waitcnt lgkmcnt(4)\n"
  "buffer_load_dwordx4 v114, s[40:43], s57 offen offset:0  lds\n"
  "buffer_load_dwordx4 v115, s[40:43], s57 offen offset:1024  lds\n"
  "ds_read_b64_tr_b16 a[42:43], v121 offset:11264\n"
  "ds_read_b64_tr_b16 a[46:47], v122 offset:11264\n"
  "s_waitcnt vmcnt(11)\n"
  "ds_read_b64_tr_b16 a[48:49], v117 offset:12288\n"
  "ds_read_b64_tr_b16 a[52:53], v118 offset:12288\n"
  "buffer_load_dwordx4 v116, s[40:43], s57 offen offset:2048  lds\n"
  "s_add_u32 s57, s57, 6144\n"
  "s_add_u32 m0, s59, 9216\n"
  "ds_read_b64_tr_b16 a[56:57], v119 offset:12288\n"
  "ds_read_b64_tr_b16 a[60:61], v120 offset:12288\n"
  "s_waitcnt lgkmcnt(4)\n"
  "buffer_load_dwordx4 v114, s[40:43], s57 offen offset:0  lds\n"
  "buffer_load_dwordx4 v115, s[40:43], s57 offen offset:1024  lds\n"
  "ds_read_b64_tr_b16 a[64:65], v121 offset:14336\n"
  "ds_read_b64_tr_b16 a[68:69], v122 offset:14336\n"
  "s_waitcnt vmcnt(11)\n"
  "ds_read_b64_tr_b16 a[50:51], v117 offset:15360\n"
  "ds_read_b64_tr_b16 a[54:55], v118 offset:15360\n"
  "buffer_load_dwordx4 v116, s[40:43], s57 offen offset:2048  lds\n"
  "s_add_u32 s57, s57, 6144\n"
  "s_add_u32 m0, s59, 12288\n"
  "ds_read_b64_tr_b16 a[58:59], v119 offset:15360\n"
  "ds_read_b64_tr_b16 a[62:63], v120 offset:15360\n"
  "s_waitcnt lgkmcnt(4)\n"
  "buffer_load_dwordx4 v114, s[40:43], s57 offen offset:0  lds\n"
  "buffer_load_dwordx4 v115, s[40:43], s57 offen offset:1024  lds\n"
  "ds_read_b64_tr_b16 a[66:67], v121 offset:17408\n"
  "ds_read_b64_tr_b16 a[70:71], v122 offset:17408\n"
  "s_waitcnt vmcnt(11)\n"
  "ds_read_b64_tr_b16 a[72:73], v117 offset:0\n"
  "ds_read_b64_tr_b16 a[76:77], v118 offset:0\n"
  "buffer_load_dwordx4 v116, s[40:43], s57 offen offset:2048  lds\n"
  "s_add_u32 s57, s57, 6144\n"
  "s_add_u32 m0, s59, 15360\n"
  "ds_read_b64_tr_b16 a[80:81], v119 offset:0\n"
  "ds_read_b64_tr_b16 a[84:85], v120 offset:0\n"
  "s_waitcnt lgkmcnt(4)\n"
  "buffer_load_dwordx4 v114, s[40:43], s57 offen offset:0  lds\n"
  "buffer_load_dwordx4 v115, s[40:43], s57 offen offset:1024  lds\n"
  "ds_read_b64_tr_b16 a[88:89], v121 offset:2048\n"
  "ds_read_b64_tr_b16 a[92:93], v122 offset:2048\n"
  "s_waitcnt vmcnt(11)\n"
  "ds_read_b64_tr_b16 a[74:75], v117 offset:3072\n"
  "ds_read_b64_tr_b16 a[78:79], v118 offset:3072\n"
  "buffer_load_dwordx4 v116, s[40:43], s57 offen offset:2048  lds\n"
  "s_add_u32 s57, s57, 6144\n"
  "s_add_u32 m0, s59, 0\n"
  "ds_read_b64_tr_b16 a[82:83], v119 offset:3072\n"
  "ds_read_b64_tr_b16 a[86:87], v120 offset:3072\n"
  "s_waitcnt lgkmcnt(0)\n"
  "s_barrier\n"
  "buffer_load_dwordx4 v114, s[40:43], s57 offen offset:0  lds\n"
  "buffer_load_dwordx4 v115, s[40:43], s57 offen offset:1024  lds\n"
  "ds_read_b128 v[48:51], v104 offset:0\n"
  "ds_read_b64_tr_b16 a[90:91], v121 offset:5120\n"
  "ds_read_b64_tr_b16 a[94:95], v122 offset:5120\n"
  "s_waitcnt vmcnt(11)\n"
  "ds_read_b64_tr_b16 a[96:97], v117 offset:6144\n"
  "ds_read_b64_tr_b16 a[100:101], v118 offset:6144\n"
  "ds_read_b128 v[84:87], v111\n"
  "s_sub_u32 s56, s56, 1\n"
  "buffer_load_dwordx4 v116, s[40:43], s57 offen offset:2048  lds\n"
  "ds_read_b128 v[52:55], v104 offset:1024\n"
  "s_add_u32 s57, s57, 6144\n"
  "s_add_u32 m0, s59, 3072\n"
  "ds_read_b64_tr_b16 a[104:105], v119 offset:6144\n"
  "ds_read_b64_tr_b16 a[108:109], v120 offset:6144\n"
  "ds_read_b128 v[100:103], v111 offset:64\n"
  "s_waitcnt lgkmcnt(7)\n"
  "buffer_load_dwordx4 v114, s[40:43], s57 offen offset:0  lds\n"
  "buffer_load_dwordx4 v115, s[40:43], s57 offen offset:1024  lds\n"
  "ds_read_b64_tr_b16 a[112:113], v121 offset:8192\n"
  "ds_read_b64_tr_b16 a[116:117], v122 offset:8192\n"
  "s_waitcnt vmcnt(11)\n"
  "ds_read_b64_tr_b16 a[98:99], v117 offset:9216\n"
  "ds_read_b64_tr_b16 a[102:103], v118 offset:9216\n"
  "v_xor_b32 v111, 0x2000, v111\n"
  "s_sub_i32 s61, s56, 5\n"
  "s_max_i32 s61, s61, 0\n"
  "buffer_load_dwordx4 v116, s[40:43], s57 offen offset:2048  lds\n"
  "ds_read_b128 v[56:59], v104 offset:2048\n"
  "s_add_u32 s57, s57, 6144\n"
  "s_add_u32 m0, s59, 6144\n"
  "ds_read_b64_tr_b16 a[106:107], v119 offset:9216\n"
  "ds_read_b64_tr_b16 a[110:111], v120 offset:9216\n"
  "s_waitcnt lgkmcnt(5)\n"
  "buffer_load_dwordx4 v114, s[40:43], s57 offen offset:0  lds\n"
  "buffer_load_dwordx4 v115, s[40:43], s57 offen offset:1024  lds\n"
  "ds_read_b64_tr_b16 a[114:115], v121 offset:11264\n"
  "ds_read_b64_tr_b16 a[118:119], v122 offset:11264\n"
  "s_waitcnt vmcnt(11)\n"
  "ds_read_b64_tr_b16 a[120:121], v117 offset:12288\n"
  "ds_read_b64_tr_b16 a[124:125], v118 offset:12288\n"
  "buffer_load_dwordx4 v116, s[40:43], s57 offen offset:2048  lds\n"
  "ds_read_b128 v[60:63], v104 offset:3072\n"
  "s_add_u32 s57, s57, 6144\n"
  "s_add_u32 m0, s59, 9216\n"
  "ds_read_b64_tr_b16 a[128:129], v119 offset:12288\n"
  "ds_read_b64_tr_b16 a[132:133], v120 offset:12288\n"
  "v_add_f32 v84, v84, v100\n"
  "s_waitcnt lgkmcnt(5)\n"
  "buffer_load_dwordx4 v114, s[40:43], s57 offen offset:0  lds\n"
  "buffer_load_dwordx4 v115, s[40:43], s57 offen offset:1024  lds\n"
  "ds_read_b64_tr_b16 a[136:137], v121 offset:14336\n"
  "ds_read_b64_tr_b16 a[140:141], v122 offset:14336\n"
  "s_waitcnt vmcnt(11)\n"
  "ds_read_b64_tr_b16 a[122:123], v117 offset:15360\n"
  "ds_read_b64_tr_b16 a[126:127], v118 offset:15360\n"
  "v_add_f32 v85, v85, v101\n"
  "buffer_load_dwordx4 v116, s[40:43], s57 offen offset:2048  lds\n"
  "v_add_f32 v86, v86, v102\n"
  "s_add_u32 s57, s57, s87\n"
  "s_add_u32 m0, s59, 12288\n"
  "ds_read_b64_tr_b16 a[130:131], v119 offset:15360\n"
  "ds_read_b64_tr_b16 a[134:135], v120 offset:15360\n"
  "v_add_f32 v87, v87, v103\n"
  "s_waitcnt lgkmcnt(4)\n"
  "buffer_load_dwordx4 v114, s[40:43], s57 offen offset:0  lds\n"
  "buffer_load_dwordx4 v115, s[40:43], s57 offen offset:1024  lds\n"
  "ds_read_b64_tr_b16 a[138:139], v121 offset:17408\n"
  "ds_read_b64_tr_b16 a[142:143], v122 offset:17408\n"
  "s_waitcnt vmcnt(11)\n"
  "ds_read_b64_tr_b16 a[144:145], v117 offset:0\n"
  "ds_read_b64_tr_b16 a[148:149], v118 offset:0\n"
  "v_max3_f32 v123, v123, v84, v85\n"
  "buffer_load_dwordx4 v116, s[40:43], s57 offen offset:2048  lds\n"
  "v_max3_f32 v123, v123, v86, v87\n"
  "s_add_u32 s57, s57, 6144\n"
  "s_add_u32 m0, s59, 15360\n"
  "ds_read_b64_tr_b16 a[152:153], v119 offset:0\n"
  "ds_read_b64_tr_b16 a[156:157], v120 offset:0\n"
  "s_waitcnt lgkmcnt(4)\n"
  "buffer_load_dwordx4 v114, s[40:43], s57 offen offset:0  lds\n"
  "buffer_load_dwordx4 v115, s[40:43], s57 offen offset:1024  lds\n"
  "ds_read_b64_tr_b16 a[160:161], v121 offset:2048\n"
  "ds_read_b64_tr_b16 a[164:165], v122 offset:2048\n"
  "s_waitcnt vmcnt(11)\n"
  "ds_read_b64_tr_b16 a[146:147], v117 offset:3072\n"
  "ds_read_b64_tr_b16 a[150:151], v118 offset:3072\n"
  "buffer_load_dwordx4 v116, s[40:43], s57 offen offset:2048  lds\n"
  "s_add_u32 s57, s57, 6144\n"
  "s_add_u32 m0, s59, 0\n"
  "ds_read_b64_tr_b16 a[154:155], v119 offset:3072\n"
  "ds_read_b64_tr_b16 a[158:159], v120 offset:3072\n"
  "v_mfma_f32_16x16x32_bf16 v[0:3], v[48:51], a[0:3], 0\n"
  "s_waitcnt lgkmcnt(4)\n"
  "buffer_load_dwordx4 v114, s[40:43], s57 offen offset:0  lds\n"
  "buffer_load_dwordx4 v115, s[40:43], s57 offen offset:1024  lds\n"
  "ds_read_b128 v[64:67], v104 offset:4096\n"
  "ds_read_b64_tr_b16 a[162:163], v121 offset:5120\n"
  "ds_read_b64_tr_b16 a[166:167], v122 offset:5120\n"
  "v_mfma_f32_16x16x32_bf16 v[0:3], v[52:55], a[24:27], v[0:3]\n"
  "s_waitcnt vmcnt(11)\n"
  "ds_read_b64_tr_b16 a[168:169], v117 offset:6144\n"
  "ds_read_b64_tr_b16 a[172:173], v118 offset:6144\n"
  "v_mfma_f32_16x16x32_bf16 v[4:7], v[52:55], a[28:31], 0\n"
  "buffer_load_dwordx4 v116, s[40:43], s57 offen offset:2048  lds\n"
  "ds_read_b128 v[68:71], v104 offset:5120\n"
  "v_mfma_f32_16x16x32_bf16 v[4:7], v[48:51], a[4:7], v[4:7]\n"
  "s_add_u32 s57, s57, 6144\n"
  "s_add_u32 m0, s59, 3072\n"
  "ds_read_b64_tr_b16 a[176:177], v119 offset:6144\n"
  "ds_read_b64_tr_b16 a[180:181], v120 offset:6144\n"
  "ds_read_b64_tr_b16 v[92:93], v108 offset:0\n"
  "v_mfma_f32_16x16x32_bf16 v[8:11], v[48:51], a[8:11], 0\n"
  "s_waitcnt lgkmcnt(6)\n"
  "buffer_load_dwordx4 v114, s[40:43], s57 offen offset:0  lds\n"
  "buffer_load_dwordx4 v115, s[40:43], s57 offen offset:1024  lds\n"
  "ds_read_b64_tr_b16 a[184:185], v121 offset:8192\n"
  "ds_read_b64_tr_b16 a[188:189], v122 offset:8192\n"
  "v_mfma_f32_16x16x32_bf16 v[8:11], v[52:55], a[32:35], v[8:11]\n"
  "s_waitcnt vmcnt(11)\n"
  "ds_read_b64_tr_b16 a[170:171], v117 offset:9216\n"
  "ds_read_b64_tr_b16 a[174:175], v118 offset:9216\n"
  "ds_read_b64_tr_b16 v[94:95], v108 offset:32\n"
  "v_smfmac_f32_16x16x64_bf16 v[80:83], v[88:91], v[48:55], v124\n"
  "buffer_load_dwordx4 v116, s[40:43], s57 offen offset:2048  lds\n"
  "ds_read_b128 v[72:75], v104 offset:6144\n"
  "v_mfma_f32_16x16x32_bf16 v[12:15], v[52:55], a[36:39], 0\n"
  "s_add_u32 s57, s57, 6144\n"
  "s_add_u32 m0, s59, 6144\n"
  "ds_read_b64_tr_b16 a[178:179], v119 offset:9216\n"
  "ds_read_b64_tr_b16 a[182:183], v120 offset:9216\n"
  "v_mfma_f32_16x16x32_bf16 v[12:15], v[48:51], a[12:15], v[12:15]\n"
  "s_waitcnt lgkmcnt(6)\n"
  "buffer_load_dwordx4 v114, s[40:43], s57 offen offset:0  lds\n"
  "buffer_load_dwordx4 v115, s[40:43], s57 offen offset:1024  lds\n"
  "ds_read_b64_tr_b16 a[186:187], v121 offset:11264\n"
  "ds_read_b64_tr_b16 a[190:191], v122 offset:11264\n"
  "v_mfma_f32_16x16x32_bf16 v[16:19], v[48:51], a[16:19], 0\n"
  "s_waitcnt vmcnt(11)\n"
  "ds_read_b64_tr_b16 a[192:193], v117 offset:12288\n"
  "ds_read_b64_tr_b16 a[196:197], v118 offset:12288\n"
  "v_mfma_f32_16x16x32_bf16 v[16:19], v[52:55], a[40:43], v[16:19]\n"
  "buffer_load_dwordx4 v116, s[40:43], s57 offen offset:2048  lds\n"
  "ds_read_b128 v[76:79], v104 offset:7168\n"
  "v_mfma_f32_16x16x32_bf16 v[20:23], v[52:55], a[44:47], 0\n"
  "s_add_u32 s57, s57, 6144\n"
  "s_add_u32 m0, s59, 9216\n"
  "ds_read_b64_tr_b16 a[200:201], v119 offset:12288\n"
  "ds_read_b64_tr_b16 a[204:205], v120 offset:12288\n"
  "v_xor_b32 v104, 0x4000, v104\n"
  "v_mfma_f32_16x16x32_bf16 v[20:23], v[48:51], a[20:23], v[20:23]\n"
  "s_waitcnt lgkmcnt(5)\n"
  "buffer_load_dwordx4 v114, s[40:43], s57 offen offset:0  lds\n"
  "buffer_load_dwordx4 v115, s[40:43], s57 offen offset:1024  lds\n"
  "ds_read_b64_tr_b16 a[208:209], v121 offset:14336\n"
  "ds_read_b64_tr_b16 a[212:213], v122 offset:14336\n"
  "v_mfma_f32_16x16x32_bf16 v[20:23], v[56:59], a[68:71], v[20:23]\n"
  "buffer_store_dwordx4 v[92:95], v109, s[48:51], s68 offen offset:0 nt\n"
  "s_waitcnt vmcnt(12)\n"
  "ds_read_b64_tr_b16 a[194:195], v117 offset:15360\n"
  "ds_read_b64_tr_b16 a[198:199], v118 offset:15360\n"
  "v_mfma_f32_16x16x32_bf16 v[20:23], v[60:63], a[92:95], v[20:23]\n"
  "buffer_load_dwordx4 v116, s[40:43], s57 offen offset:2048  lds\n"
  "v_mfma_f32_16x16x32_bf16 v[16:19], v[60:63], a[88:91], v[16:19]\n"
  "s_add_u32 s57, s57, 6144\n"
  "s_add_u32 m0, s59, 12288\n"
  "ds_read_b64_tr_b16 a[202:203], v119 offset:15360\n"
  "ds_read_b64_tr_b16 a[206:207], v120 offset:15360\n"
  "v_mfma_f32_16x16x32_bf16 v[16:19], v[56:59], a[64:67], v[16:19]\n"
  "s_waitcnt lgkmcnt(4)\n"
  "buffer_load_dwordx4 v114, s[40:43], s57 offen offset:0  lds\n"
  "buffer_load_dwordx4 v115, s[40:43], s57 offen offset:1024  lds\n"
  "ds_read_b64_tr_b16 a[210:211], v121 offset:17408\n"
  "ds_read_b64_tr_b16 a[214:215], v122 offset:17408\n"
  "v_mfma_f32_16x16x32_bf16 v[12:15], v[56:59], a[60:63], v[12:15]\n"
  "s_waitcnt vmcnt(12)\n"
  "ds_read_b64_tr_b16 a[216:217], v117 offset:0\n"
  "ds_read_b64_tr_b16 a[220:221], v118 offset:0\n"
  "v_mfma_f32_16x16x32_bf16 v[12:15], v[60:63], a[84:87], v[12:15]\n"
  "buffer_load_dwordx4 v116, s[40:43], s57 offen offset:2048  lds\n"
  "v_smfmac_f32_16x16x64_bf16 v[80:83], v[88:91], v[56:63], v124\n"
  "s_add_u32 s57, s57, 6144\n"
  "s_add_u32 m0, s59, 15360\n"
  "ds_read_b64_tr_b16 a[224:225], v119 offset:0\n"
  "ds_read_b64_tr_b16 a[228:229], v120 offset:0\n"
  "v_mfma_f32_16x16x32_bf16 v[8:11], v[60:63], a[80:83], v[8:11]\n"
  "s_waitcnt lgkmcnt(4)\n"
  "buffer_load_dwordx4 v114, s[40:43], s57 offen offset:0  lds\n"
  "buffer_load_dwordx4 v115, s[40:43], s57 offen offset:1024  lds\n"
  "ds_read_b64_tr_b16 a[232:233], v121 offset:2048\n"
  "ds_read_b64_tr_b16 a[236:237], v122 offset:2048\n"
  "v_mfma_f32_16x16x32_bf16 v[8:11], v[56:59], a[56:59], v[8:11]\n"
  "s_waitcnt vmcnt(12)\n"
  "ds_read_b64_tr_b16 a[218:219], v117 offset:3072\n"
  "ds_read_b64_tr_b16 a[222:223], v118 offset:3072\n"
  "v_mfma_f32_16x16x32_bf16 v[4:7], v[56:59], a[52:55], v[4:7]\n"
  "buffer_load_dwordx4 v116, s[40:43], s57 offen offset:2048  lds\n"
  "v_mfma_f32_16x16x32_bf16 v[4:7], v[60:63], a[76:79], v[4:7]\n"
  "s_add_u32 s57, s57, s86\n"
  "s_add_u32 m0, s59, 0\n"
  "ds_read_b64_tr_b16 a[226:227], v119 offset:3072\n"
  "ds_read_b64_tr_b16 a[230:231], v120 offset:3072\n"
  "v_mfma_f32_16x16x32_bf16 v[0:3], v[60:63], a[72:75], v[0:3]\n"
  "s_waitcnt lgkmcnt(4)\n"
  "buffer_load_dwordx4 v114, s[40:43], s57 offen offset:0  lds\n"
  "buffer_load_dwordx4 v115, s[40:43], s57 offen offset:1024  lds\n"
  "ds_read_b64_tr_b16 a[234:235], v121 offset:5120\n"
  "ds_read_b64_tr_b16 a[238:239], v122 offset:5120\n"
  "v_mfma_f32_16x16x32_bf16 v[0:3], v[56:59], a[48:51], v[0:3]\n"
  "s_waitcnt vmcnt(12)\n"
  "ds_read_b64_tr_b16 a[240:241], v117 offset:6144\n"
  "ds_read_b64_tr_b16 a[244:245], v118 offset:6144\n"
  "v_mfma_f32_16x16x32_bf16 v[0:3], v[64:67], a[96:99], v[0:3]\n"
  "buffer_load_dwordx4 v116, s[40:43], s57 offen offset:2048  lds\n"
  "ds_read_b128 v[48:51], v105 offset:0\n"
  "v_mfma_f32_16x16x32_bf16 v[0:3], v[68:71], a[120:123], v[0:3]\n"
  "s_add_u32 s57, s57, 6144\n"
  "s_add_u32 m0, s59, 3072\n"
  "ds_read_b64_tr_b16 a[248:249], v119 offset:6144\n"
  "ds_read_b64_tr_b16 a[252:253], v120 offset:6144\n"
  "v_mfma_f32_16x16x32_bf16 v[4:7], v[68:71], a[124:127], v[4:7]\n"
  "s_waitcnt lgkmcnt(5)\n"
  "buffer_load_dwordx4 v114, s[40:43], s57 offen offset:0  lds\n"
  "buffer_load_dwordx4 v115, s[40:43], s57 offen offset:1024  lds\n"
  "ds_read_b128 v[52:55], v105 offset:1024\n"
  "ds_read_b64_tr_b16 v[128:129], v121 offset:8192\n"
  "ds_read_b64_tr_b16 v[132:133], v122 offset:8192\n"
  "v_mfma_f32_16x16x32_bf16 v[4:7], v[64:67], a[100:103], v[4:7]\n"
  "s_waitcnt vmcnt(11)\n"
  "ds_read_b64_tr_b16 a[242:243], v117 offset:9216\n"
  "ds_read_b64_tr_b16 a[246:247], v118 offset:9216\n"
  "ds_read_b64_tr_b16 v[96:97], v108 offset:2304\n"
  "v_mfma_f32_16x16x32_bf16 v[8:11], v[64:67], a[104:107], v[8:11]\n"
  "buffer_load_dwordx4 v116, s[40:43], s57 offen offset:2048  lds\n"
  "ds_read_b64_tr_b16 v[98:99], v108 offset:2336\n"
  "v_mfma_f32_16x16x32_bf16 v[8:11], v[68:71], a[128:131], v[8:11]\n"
  "s_add_u32 s57, s57, 6144\n"
  "s_add_u32 m0, s59, 6144\n"
  "ds_read_b64_tr_b16 a[250:251], v119 offset:9216\n"
  "ds_read_b64_tr_b16 a[254:255], v120 offset:9216\n"
  "v_smfmac_f32_16x16x64_bf16 v[80:83], v[88:91], v[64:71], v124\n"
  "s_waitcnt lgkmcnt(6)\n"
  "buffer_load_dwordx4 v114, s[40:43], s57 offen offset:0  lds\n"
  "buffer_load_dwordx4 v115, s[40:43], s57 offen offset:1024  lds\n"
  "ds_read_b128 v[56:59], v105 offset:2048\n"
  "ds_read_b64_tr_b16 v[130:131], v121 offset:11264\n"
  "ds_read_b64_tr_b16 v[134:135], v122 offset:11264\n"
  "v_mfma_f32_16x16x32_bf16 v[12:15], v[68:71], a[132:135], v[12:15]\n"
  "s_waitcnt vmcnt(11)\n"
  "ds_read_b64_tr_b16 v[136:137], v117 offset:12288\n"
  "ds_read_b64_tr_b16 v[140:141], v118 offset:12288\n"
  "v_mfma_f32_16x16x32_bf16 v[12:15], v[64:67], a[108:111], v[12:15]\n"
  "buffer_load_dwordx4 v116, s[40:43], s57 offen offset:2048  lds\n"
  "v_mfma_f32_16x16x32_bf16 v[16:19], v[64:67], a[112:115], v[16:19]\n"
  "s_add_u32 s57, s57, 6144\n"
  "s_add_u32 m0, s59, 9216\n"
  "ds_read_b64_tr_b16 v[144:145], v119 offset:12288\n"
  "ds_read_b64_tr_b16 v[148:149], v120 offset:12288\n"
  "v_mfma_f32_16x16x32_bf16 v[16:19], v[68:71], a[136:139], v[16:19]\n"
  "s_waitcnt lgkmcnt(4)\n"
  "buffer_load_dwordx4 v114, s[40:43], s57 offen offset:0  lds\n"
  "buffer_load_dwordx4 v115, s[40:43], s57 offen offset:1024  lds\n"
  "ds_read_b128 v[60:63], v105 offset:3072\n"
  "ds_read_b64_tr_b16 v[152:153], v121 offset:14336\n"
  "ds_read_b64_tr_b16 v[156:157], v122 offset:14336\n"
  "v_mfma_f32_16x16x32_bf16 v[20:23], v[68:71], a[140:143], v[20:23]\n"
  "s_waitcnt vmcnt(11)\n"
  "ds_read_b64_tr_b16 v[138:139], v117 offset:15360\n"
  "ds_read_b64_tr_b16 v[142:143], v118 offset:15360\n"
  "v_mfma_f32_16x16x32_bf16 v[20:23], v[64:67], a[116:119], v[20:23]\n"
  "buffer_load_dwordx4 v116, s[40:43], s57 offen offset:2048  lds\n"
  "v_mfma_f32_16x16x32_bf16 v[20:23], v[72:75], a[164:167], v[20:23]\n"
  "s_add_u32 s57, s57, 6144\n"
  "s_add_u32 m0, s59, 12288\n"
  "buffer_store_dwordx4 v[96:99], v109, s[48:51], s68 offen offset:128 nt\n"
  "ds_read_b64_tr_b16 v[146:147], v119 offset:15360\n"
  "ds_read_b64_tr_b16 v[150:151], v120 offset:15360\n"
  "v_mfma_f32_16x16x32_bf16 v[20:23], v[76:79], a[188:191], v[20:23]\n"
  "s_waitcnt lgkmcnt(4)\n"
  "buffer_load_dwordx4 v114, s[40:43], s57 offen offset:0  lds\n"
  "buffer_load_dwordx4 v115, s[40:43], s57 offen offset:1024  lds\n"
  "ds_read_b64_tr_b16 v[154:155], v121 offset:17408\n"
  "ds_read_b64_tr_b16 v[158:159], v122 offset:17408\n"
  "v_mfma_f32_16x16x32_bf16 v[16:19], v[76:79], a[184:187], v[16:19]\n"
  "s_waitcnt vmcnt(12)\n"
  "ds_read_b64_tr_b16 v[160:161], v117 offset:0\n"
  "ds_read_b64_tr_b16 v[164:165], v118 offset:0\n"
  "v_mfma_f32_16x16x32_bf16 v[16:19], v[72:75], a[160:163], v[16:19]\n"
  "buffer_load_dwordx4 v116, s[40:43], s57 offen offset:2048  lds\n"
  "v_mfma_f32_16x16x32_bf16 v[12:15], v[72:75], a[156:159], v[12:15]\n"
  "s_add_u32 s57, s57, 6144\n"
  "s_add_u32 m0, s59, 15360\n"
  "ds_read_b64_tr_b16 v[168:169], v119 offset:0\n"
  "ds_read_b64_tr_b16 v[172:173], v120 offset:0\n"
  "v_mfma_f32_16x16x32_bf16 v[12:15], v[76:79], a[180:183], v[12:15]\n"
  "s_waitcnt lgkmcnt(4)\n"
  "buffer_load_dwordx4 v114, s[40:43], s57 offen offset:0  lds\n"
  "buffer_load_dwordx4 v115, s[40:43], s57 offen offset:1024  lds\n"
  "ds_read_b64_tr_b16 v[176:177], v121 offset:2048\n"
  "ds_read_b64_tr_b16 v[180:181], v122 offset:2048\n"
  "v_smfmac_f32_16x16x64_bf16 v[80:83], v[88:91], v[72:79], v124\n"
  "s_waitcnt vmcnt(12)\n"
  "ds_read_b64_tr_b16 v[162:163], v117 offset:3072\n"
  "ds_read_b64_tr_b16 v[166:167], v118 offset:3072\n"
  "v_mfma_f32_16x16x32_bf16 v[8:11], v[76:79], a[176:179], v[8:11]\n"
  "buffer_load_dwordx4 v116, s[40:43], s57 offen offset:2048  lds\n"
  "ds_read_b64_tr_b16 v[100:101], v108 offset:4608\n"
  "v_mfma_f32_16x16x32_bf16 v[8:11], v[72:75], a[152:155], v[8:11]\n"
  "s_add_u32 s57, s57, 6144\n"
  "s_add_u32 m0, s59, 0\n"
  "ds_read_b64_tr_b16 v[170:171], v119 offset:3072\n"
  "ds_read_b64_tr_b16 v[174:175], v120 offset:3072\n"
  "ds_read_b64_tr_b16 v[102:103], v108 offset:4640\n"
  "v_mfma_f32_16x16x32_bf16 v[4:7], v[72:75], a[148:151], v[4:7]\n"
  "s_waitcnt lgkmcnt(6)\n"
  "buffer_load_dwordx4 v114, s[40:43], s57 offen offset:0  lds\n"
  "buffer_load_dwordx4 v115, s[40:43], s57 offen offset:1024  lds\n"
  "ds_read_b64_tr_b16 v[178:179], v121 offset:5120\n"
  "ds_read_b64_tr_b16 v[182:183], v122 offset:5120\n"
  "v_mfma_f32_16x16x32_bf16 v[4:7], v[76:79], a[172:175], v[4:7]\n"
  "s_waitcnt vmcnt(12)\n"
  "ds_read_b64_tr_b16 v[184:185], v117 offset:6144\n"
  "ds_read_b64_tr_b16 v[188:189], v118 offset:6144\n"
  "v_xor_b32 v108, 0x2000, v108\n"
  "v_mfma_f32_16x16x32_bf16 v[0:3], v[76:79], a[168:171], v[0:3]\n"
  "buffer_load_dwordx4 v116, s[40:43], s57 offen offset:2048  lds\n"
  "v_mfma_f32_16x16x32_bf16 v[0:3], v[72:75], a[144:147], v[0:3]\n"
  "s_add_u32 s57, s57, 6144\n"
  "s_add_u32 m0, s59, 3072\n"
  "ds_read_b64_tr_b16 v[192:193], v119 offset:6144\n"
  "ds_read_b64_tr_b16 v[196:197], v120 offset:6144\n"
  "v_mfma_f32_16x16x32_bf16 v[0:3], v[48:51], a[192:195], v[0:3]\n"
  "s_waitcnt lgkmcnt(4)\n"
  "buffer_load_dwordx4 v114, s[40:43], s57 offen offset:0  lds\n"
  "buffer_load_dwordx4 v115, s[40:43], s57 offen offset:1024  lds\n"
  "ds_read_b128 v[64:67], v105 offset:4096\n"
  "ds_read_b64_tr_b16 v[200:201], v121 offset:8192\n"
  "ds_read_b64_tr_b16 v[204:205], v122 offset:8192\n"
  "v_mfma_f32_16x16x32_bf16 v[0:3], v[52:55], a[216:219], v[0:3]\n"
  "s_waitcnt vmcnt(12)\n"
  "ds_read_b64_tr_b16 v[186:187], v117 offset:9216\n"
  "ds_read_b64_tr_b16 v[190:191], v118 offset:9216\n"
  "v_mfma_f32_16x16x32_bf16 v[4:7], v[52:55], a[220:223], v[4:7]\n"
  "s_add_u32 s72, s72, 1\n"
  "buffer_load_dwordx4 v116, s[40:43], s57 offen offset:2048  lds\n"
  "ds_read_b128 v[68:71], v105 offset:5120\n"
  "v_mfma_f32_16x16x32_bf16 v[4:7], v[48:51], a[196:199], v[4:7]\n"
  "s_add_u32 s57, s57, 6144\n"
  "ds_read_b64_tr_b16 v[194:195], v119 offset:9216\n"
  "ds_read_b64_tr_b16 v[198:199], v120 offset:9216\n"
  "v_mfma_f32_16x16x32_bf16 v[8:11], v[48:51], a[200:203], v[8:11]\n"
  "s_lshr_b32 s74, s72, 1\n"
  "buffer_store_dwordx4 v[100:103], v109, s[48:51], s68 offen offset:256 nt\n"
  "ds_read_b64_tr_b16 v[202:203], v121 offset:11264\n"
  "ds_read_b64_tr_b16 v[206:207], v122 offset:11264\n"
  "v_mfma_f32_16x16x32_bf16 v[8:11], v[52:55], a[224:227], v[8:11]\n"
  "s_waitcnt vmcnt(10)\n"
  "ds_read_b64_tr_b16 v[208:209], v117 offset:12288\n"
  "ds_read_b64_tr_b16 v[212:213], v118 offset:12288\n"
  "v_mfma_f32_16x16x32_bf16 v[12:15], v[52:55], a[228:231], v[12:15]\n"
  "s_add_u32 s68, s68, s69\n"
  "ds_read_b128 v[72:75], v105 offset:6144\n"
  "v_rcp_f32 v84, v84\n"
  "v_mfma_f32_16x16x32_bf16 v[12:15], v[48:51], a[204:207], v[12:15]\n"
  "s_mov_b32 s69, s70\n"
  "ds_read_b64_tr_b16 v[216:217], v119 offset:12288\n"
  "ds_read_b64_tr_b16 v[220:221], v120 offset:12288\n"
  "v_mul_f32 v40, v40, v84\n"
  "v_mfma_f32_16x16x32_bf16 v[16:19], v[48:51], a[208:211], v[16:19]\n"
  "s_movk_i32 s70, 6144\n"
  "ds_read_b64_tr_b16 v[224:225], v121 offset:14336\n"
  "ds_read_b64_tr_b16 v[228:229], v122 offset:14336\n"
  "v_mul_f32 v44, v44, v84\n"
  "v_mfma_f32_16x16x32_bf16 v[16:19], v[52:55], a[232:235], v[16:19]\n"
  "s_waitcnt vmcnt(7)\n"
  "ds_read_b64_tr_b16 v[210:211], v117 offset:15360\n"
  "ds_read_b64_tr_b16 v[214:215], v118 offset:15360\n"
  "v_cvt_pk_bf16_f32 v40, v40, v44\n"
  "v_mfma_f32_16x16x32_bf16 v[20:23], v[52:55], a[236:239], v[20:23]\n"
  "ds_read_b128 v[76:79], v105 offset:7168\n"
  "v_rcp_f32 v85, v85\n"
  "v_mfma_f32_16x16x32_bf16 v[20:23], v[48:51], a[212:215], v[20:23]\n"
  "ds_read_b64_tr_b16 v[218:219], v119 offset:15360\n"
  "ds_read_b64_tr_b16 v[222:223], v120 offset:15360\n"
  "v_mul_f32 v41, v41, v85\n"
  "v_mfma_f32_16x16x32_bf16 v[20:23], v[56:59], v[132:135], v[20:23]\n"
  "ds_read_b64_tr_b16 v[226:227], v121 offset:17408\n"
  "ds_read_b64_tr_b16 v[230:231], v122 offset:17408\n"
  "v_mul_f32 v45, v45, v85\n"
  "v_mfma_f32_16x16x32_bf16 v[20:23], v[60:63], v[156:159], v[20:23]\n"
  "s_waitcnt vmcnt(4)\n"
  "ds_read_b64_tr_b16 v[232:233], v117 offset:0\n"
  "ds_read_b64_tr_b16 v[236:237], v118 offset:0\n"
  "v_cvt_pk_bf16_f32 v41, v41, v45\n"
  "v_mfma_f32_16x16x32_bf16 v[16:19], v[60:63], v[152:155], v[16:19]\n"
  "v_rcp_f32 v86, v86\n"
  "v_rcp_f32 v87, v87\n"
  "v_mfma_f32_16x16x32_bf16 v[16:19], v[56:59], v[128:131], v[16:19]\n"
  "ds_read_b64_tr_b16 v[240:241], v119 offset:0\n"
  "ds_read_b64_tr_b16 v[244:245], v120 offset:0\n"
  "v_mul_f32 v42, v42, v86\n"
  "v_mul_f32 v46, v46, v86\n"
  "v_cvt_pk_bf16_f32 v42, v42, v46\n"
  "v_mul_f32 v43, v43, v87\n"
  "v_mul_f32 v47, v47, v87\n"
  "v_cvt_pk_bf16_f32 v43, v43, v47\n"
  "v_mul_f32 v24, v24, v84\n"
  "v_mul_f32 v25, v25, v85\n"
  "v_mul_f32 v28, v28, v84\n"
  "v_mul_f32 v29, v29, v85\n"
  "v_mul_f32 v32, v32, v84\n"
  "v_mul_f32 v33, v33, v85\n"
  "v_mul_f32 v36, v36, v84\n"
  "v_mul_f32 v37, v37, v85\n"
  "v_cvt_pk_bf16_f32 v24, v24, v28\n"
  "v_cvt_pk_bf16_f32 v28, v25, v29\n"
  "v_cvt_pk_bf16_f32 v25, v32, v36\n"
  "v_cvt_pk_bf16_f32 v29, v33, v37\n"
  "v_mul_f32 v26, v26, v86\n"
  "v_mul_f32 v27, v27, v87\n"
  "v_mul_f32 v30, v30, v86\n"
  "v_mul_f32 v31, v31, v87\n"
  "v_mul_f32 v34, v34, v86\n"
  "v_mul_f32 v35, v35, v87\n"
  "v_mul_f32 v38, v38, v86\n"
  "v_mul_f32 v39, v39, v87\n"
  "v_cvt_pk_bf16_f32 v26, v26, v30\n"
  "v_cvt_pk_bf16_f32 v30, v27, v31\n"
  "v_cvt_pk_bf16_f32 v27, v34, v38\n"
  "v_cvt_pk_bf16_f32 v31, v35, v39\n"
  "v_add_f32 v44, v80, v81\n"
  "v_mfma_f32_16x16x32_bf16 v[12:15], v[56:59], a[252:255], v[12:15]\n"
  "ds_read_b64_tr_b16 v[248:249], v121 offset:2048\n"
  "ds_read_b64_tr_b16 v[252:253], v122 offset:2048\n"
  "ds_write2_b32 v107, v40, v41 offset0:0 offset1:72\n"
  "ds_write2_b32 v107, v42, v43 offset0:144 offset1:216\n"
  "ds_write_b64 v106, v[24:25] offset:0\n"
  "ds_write_b64 v106, v[28:29] offset:288\n"
  "ds_write_b64 v106, v[26:27] offset:576\n"
  "ds_write_b64 v106, v[30:31] offset:864\n"
  "ds_write_b32 v110, v44\n"
  "v_mfma_f32_16x16x32_bf16 v[12:15], v[60:63], v[148:151], v[12:15]\n"
  "s_waitcnt vmcnt(1)\n"
  "ds_read_b64_tr_b16 v[234:235], v117 offset:3072\n"
  "ds_read_b64_tr_b16 v[238:239], v118 offset:3072\n"
  "v_max3_f32 v123, v123, v84, v85\n"
  "v_mfma_f32_16x16x32_bf16 v[8:11], v[60:63], v[144:147], v[8:11]\n"
  "v_readlane_b32 s73, v126, s74\n"
  "v_max3_f32 v123, v123, v86, v87\n"
  "v_mfma_f32_16x16x32_bf16 v[8:11], v[56:59], a[248:251], v[8:11]\n"
  "s_lshl_b32 s74, s72, 4\n"
  "ds_read_b64_tr_b16 v[242:243], v119 offset:3072\n"
  "ds_read_b64_tr_b16 v[246:247], v120 offset:3072\n"
  "v_xor_b32 v106, 0x2000, v106\n"
  "v_mfma_f32_16x16x32_bf16 v[4:7], v[56:59], a[244:247], v[4:7]\n"
  "s_lshr_b32 s73, s73, s74\n"
  "ds_read_b64_tr_b16 v[250:251], v121 offset:5120\n"
  "ds_read_b64_tr_b16 v[254:255], v122 offset:5120\n"
  "v_xor_b32 v107, 0x2000, v107\n"
  "v_mfma_f32_16x16x32_bf16 v[4:7], v[60:63], v[140:143], v[4:7]\n"
  "s_and_b32 s73, s73, 0xffff\n"
  "v_mov_b32 v80, 0\n"
  "v_mov_b32 v81, 0\n"
  "v_xor_b32 v110, 0x2000, v110\n"
  "v_mfma_f32_16x16x32_bf16 v[0:3], v[60:63], v[136:139], v[0:3]\n"
  "s_lshl_b32 s73, s73, 13\n"
  "v_xor_b32 v105, 0x4000, v105\n"
  "v_mfma_f32_16x16x32_bf16 v[0:3], v[56:59], a[240:243], v[0:3]\n"
  "s_add_u32 m0, s66, 0\n"
  "s_nop 0\n"
  "buffer_load_dwordx4 v112, s[44:47], s67 offen offset:0 nt lds\n"
  "s_add_u32 m0, s66, 1920\n"
  "s_nop 0\n"
  "buffer_load_dwordx4 v112, s[44:47], s67 offen offset:128 nt lds\n"
  "s_add_u32 m0, s66, 3840\n"
  "s_nop 0\n"
  "buffer_load_dwordx4 v112, s[44:47], s67 offen offset:256 nt lds\n"
  "s_add_u32 s66, s66, 6144\n"
  "s_cmp_ge_u32 s66, 86016\n"
  "s_cselect_b32 s64, 36864, 0\n"
  "s_sub_u32 s66, s66, s64\n"
  "s_sub_u32 s90, s90, 1\n"
  "s_cselect_b32 s63, s91, 6144\n"
  "s_cmov_b64 s[90:91], s[92:93]\n"
  "s_cmov_b64 s[92:93], s[94:95]\n"
  "s_add_u32 s67, s67, s63\n"
  "s_add_u32 m0, s66, 0\n"
  "s_nop 0\n"
  "buffer_load_dwordx4 v112, s[44:47], s67 offen offset:0 nt lds\n"
  "s_add_u32 m0, s66, 1920\n"
  "s_nop 0\n"
  "buffer_load_dwordx4 v112, s[44:47], s67 offen offset:128 nt lds\n"
  "s_add_u32 m0, s66, 3840\n"
  "s_nop 0\n"
  "buffer_load_dwordx4 v112, s[44:47], s67 offen offset:256 nt lds\n"
  "s_add_u32 s66, s66, 6144\n"
  "s_cmp_ge_u32 s66, 86016\n"
  "s_cselect_b32 s64, 36864, 0\n"
  "s_sub_u32 s66, s66, s64\n"
  "s_sub_u32 s90, s90, 1\n"
  "s_cselect_b32 s63, s91, 6144\n"
  "s_cmov_b64 s[90:91], s[92:93]\n"
  "s_cmov_b64 s[92:93], s[94:95]\n"
  "s_add_u32 s67, s67, s63\n"
  "s_add_u32 m0, s66, 0\n"
  "s_nop 0\n"
  "buffer_load_dwordx4 v112, s[44:47], s67 offen offset:0 nt lds\n"
  "s_add_u32 m0, s66, 1920\n"
  "s_nop 0\n"
  "buffer_load_dwordx4 v112, s[44:47], s67 offen offset:128 nt lds\n"
  "s_add_u32 m0, s66, 3840\n"
  "s_nop 0\n"
  "buffer_load_dwordx4 v112, s[44:47], s67 offen offset:256 nt lds\n"
  "s_add_u32 s66, s66, 6144\n"
  "s_cmp_ge_u32 s66, 86016\n"
  "s_cselect_b32 s64, 36864, 0\n"
  "s_sub_u32 s66, s66, s64\n"
  "s_sub_u32 s90, s90, 1\n"
  "s_cselect_b32 s63, s91, 6144\n"
  "s_cmov_b64 s[90:91], s[92:93]\n"
  "s_cmov_b64 s[92:93], s[94:95]\n"
  "s_add_u32 s67, s67, s63\n"
  "s_add_u32 m0, s66, 0\n"
  "s_nop 0\n"
  "buffer_load_dwordx4 v112, s[44:47], s67 offen offset:0 nt lds\n"
  "s_add_u32 m0, s66, 1920\n"
  "s_nop 0\n"
  "buffer_load_dwordx4 v112, s[44:47], s67 offen offset:128 nt lds\n"
  "s_add_u32 m0, s66, 3840\n"
  "s_nop 0\n"
  "buffer_load_dwordx4 v112, s[44:47], s67 offen offset:256 nt lds\n"
  "s_add_u32 s66, s66, 6144\n"
  "s_cmp_ge_u32 s66, 86016\n"
  "s_cselect_b32 s64, 36864, 0\n"
  "s_sub_u32 s66, s66, s64\n"
  "s_sub_u32 s90, s90, 1\n"
  "s_cselect_b32 s63, s91, 6144\n"
  "s_cmov_b64 s[90:91], s[92:93]\n"
  "s_cmov_b64 s[92:93], s[94:95]\n"
  "s_add_u32 s67, s67, s63\n"
  ".p2align 6\n"
  "s_nop 0\n"
  "B_H1_%=:\n"
  "s_waitcnt vmcnt(16)\n"
  "B_H_%=:\n"
  "s_waitcnt vmcnt(32) lgkmcnt(0)\n"
  "s_barrier\n"
  "v_mfma_f32_16x16x32_bf16 v[20:23], v[64:67], v[180:183], v[20:23]\n"
  "s_add_u32 m0, s66, 0\n"
  "ds_read_b128 v[48:51], v104 offset:0\n"
  "ds_read_b128 v[84:87], v111\n"
  "v_mfma_f32_16x16x32_bf16 v[20:23], v[68:71], v[204:207], v[20:23]\n"
  "s_sub_u32 s56, s56, 1\n"
  "ds_read_b128 v[100:103], v111 offset:64\n"
  "v_xor_b32 v111, 0x2000, v111\n"
  "v_mfma_f32_16x16x32_bf16 v[20:23], v[72:75], v[228:231], v[20:23]\n"
  "ds_read_b128 v[52:55], v104 offset:1024\n"
  "v_mfma_f32_16x16x32_bf16 v[44:47], v[76:79], v[252:255], v[20:23]\n"
  "buffer_load_dwordx4 v112, s[44:47], s67 offen offset:0 nt lds\n"
  "v_mfma_f32_16x16x32_bf16 v[16:19], v[76:79], v[248:251], v[16:19]\n"
  "s_add_u32 m0, s66, 1920\n"
  "v_mfma_f32_16x16x32_bf16 v[16:19], v[72:75], v[224:227], v[16:19]\n"
  "v_mfma_f32_16x16x32_bf16 v[16:19], v[68:71], v[200:203], v[16:19]\n"
  "ds_read_b128 v[56:59], v104 offset:2048\n"
  "v_mfma_f32_16x16x32_bf16 v[40:43], v[64:67], v[176:179], v[16:19]\n"
  "buffer_load_dwordx4 v112, s[44:47], s67 offen offset:128 nt lds\n"
  "v_mfma_f32_16x16x32_bf16 v[12:15], v[64:67], v[172:175], v[12:15]\n"
  "s_add_u32 m0, s66, 3840\n"
  "v_mfma_f32_16x16x32_bf16 v[12:15], v[68:71], v[196:199], v[12:15]\n"
  "v_mfma_f32_16x16x32_bf16 v[12:15], v[72:75], v[220:223], v[12:15]\n"
  "s_waitcnt lgkmcnt(2)\n"
  "ds_read_b128 v[60:63], v104 offset:3072\n"
  "v_add_f32 v84, v84, v100\n"
  "v_mfma_f32_16x16x32_bf16 v[36:39], v[76:79], v[244:247], v[12:15]\n"
  "buffer_load_dwordx4 v112, s[44:47], s67 offen offset:256 nt lds\n"
  "v_mfma_f32_16x16x32_bf16 v[8:11], v[76:79], v[240:243], v[8:11]\n"
  "s_mov_b32 m0, 159744\n"
  "v_add_f32 v85, v85, v101\n"
  "v_max3_f32 v123, v123, v84, v85\n"
  "v_mfma_f32_16x16x32_bf16 v[8:11], v[72:75], v[216:219], v[8:11]\n"
  "v_rcp_f32 v84, v84\n"
  "v_mfma_f32_16x16x32_bf16 v[8:11], v[68:71], v[192:195], v[8:11]\n"
  "v_rcp_f32 v85, v85\n"
  "v_mfma_f32_16x16x32_bf16 v[32:35], v[64:67], v[168:171], v[8:11]\n"
  "buffer_load_dword v125, s[80:83], s73 offen  lds\n"
  "v_mfma_f32_16x16x32_bf16 v[4:7], v[64:67], v[164:167], v[4:7]\n"
  "v_mul_f32 v40, v40, v84\n"
  "v_mul_f32 v44, v44, v84\n"
  "v_mfma_f32_16x16x32_bf16 v[4:7], v[68:71], v[188:191], v[4:7]\n"
  "v_cvt_pk_bf16_f32 v40, v40, v44\n"
  "v_mul_f32 v41, v41, v85\n"
  "v_mfma_f32_16x16x32_bf16 v[4:7], v[72:75], v[212:215], v[4:7]\n"
  "v_mul_f32 v45, v45, v85\n"
  "v_cvt_pk_bf16_f32 v41, v41, v45\n"
  "v_mfma_f32_16x16x32_bf16 v[28:31], v[76:79], v[236:239], v[4:7]\n"
  "v_add_f32 v86, v86, v102\n"
  "v_add_f32 v87, v87, v103\n"
  "v_mfma_f32_16x16x32_bf16 v[0:3], v[76:79], v[232:235], v[0:3]\n"
  "v_max3_f32 v123, v123, v86, v87\n"
  "v_mul_f32 v32, v32, v84\n"
  "v_mfma_f32_16x16x32_bf16 v[0:3], v[72:75], v[208:211], v[0:3]\n"
  "v_rcp_f32 v86, v86\n"
  "v_mfma_f32_16x16x32_bf16 v[0:3], v[68:71], v[184:187], v[0:3]\n"
  "v_rcp_f32 v87, v87\n"
  "v_mfma_f32_16x16x32_bf16 v[24:27], v[64:67], v[160:163], v[0:3]\n"
  "s_waitcnt lgkmcnt(0)\n"
  "v_mul_f32 v42, v42, v86\n"
  "v_mul_f32 v46, v46, v86\n"
  "v_mfma_f32_16x16x32_bf16 v[0:3], v[48:51], a[0:3], 0\n"
  "ds_read_b128 v[64:67], v104 offset:4096\n"
  "v_cvt_pk_bf16_f32 v42, v42, v46\n"
  "v_mfma_f32_16x16x32_bf16 v[0:3], v[52:55], a[24:27], v[0:3]\n"
  "v_mul_f32 v43, v43, v87\n"
  "v_mul_f32 v47, v47, v87\n"
  "v_mfma_f32_16x16x32_bf16 v[0:3], v[56:59], a[48:51], v[0:3]\n"
  "ds_read_b128 v[68:71], v104 offset:5120\n"
  "v_cvt_pk_bf16_f32 v43, v43, v47\n"
  "v_mfma_f32_16x16x32_bf16 v[0:3], v[60:63], a[72:75], v[0:3]\n"
  "ds_read_b64_tr_b16 v[92:93], v108 offset:0\n"
  "ds_read_b64_tr_b16 v[94:95], v108 offset:32\n"
  "v_mfma_f32_16x16x32_bf16 v[4:7], v[60:63], a[76:79], 0\n"
  "v_mul_f32 v24, v24, v84\n"
  "v_mul_f32 v25, v25, v85\n"
  "v_mfma_f32_16x16x32_bf16 v[4:7], v[56:59], a[52:55], v[4:7]\n"
  "v_mul_f32 v28, v28, v84\n"
  "v_mul_f32 v29, v29, v85\n"
  "v_mfma_f32_16x16x32_bf16 v[4:7], v[52:55], a[28:31], v[4:7]\n"
  "ds_read_b128 v[72:75], v104 offset:6144\n"
  "v_mul_f32 v36, v36, v84\n"
  "v_mfma_f32_16x16x32_bf16 v[4:7], v[48:51], a[4:7], v[4:7]\n"
  "v_cvt_pk_bf16_f32 v24, v24, v28\n"
  "v_cvt_pk_bf16_f32 v28, v25, v29\n"
  "v_mfma_f32_16x16x32_bf16 v[8:11], v[48:51], a[8:11], 0\n"
  "v_cvt_pk_bf16_f32 v25, v32, v36\n"
  "v_mul_f32 v33, v33, v85\n"
  "v_mfma_f32_16x16x32_bf16 v[8:11], v[52:55], a[32:35], v[8:11]\n"
  "v_mul_f32 v37, v37, v85\n"
  "v_cvt_pk_bf16_f32 v29, v33, v37\n"
  "v_mfma_f32_16x16x32_bf16 v[8:11], v[56:59], a[56:59], v[8:11]\n"
  "ds_read_b128 v[76:79], v104 offset:7168\n"
  "v_mul_f32 v26, v26, v86\n"
  "v_mfma_f32_16x16x32_bf16 v[8:11], v[60:63], a[80:83], v[8:11]\n"
  "v_mul_f32 v27, v27, v87\n"
  "v_mul_f32 v30, v30, v86\n"
  "v_smfmac_f32_16x16x64_bf16 v[80:83], v[88:91], v[48:55], v124\n"
  "v_mul_f32 v31, v31, v87\n"
  "v_smfmac_f32_16x16x64_bf16 v[80:83], v[88:91], v[56:63], v124\n"
  "s_waitcnt lgkmcnt(2)\n"
  "buffer_store_dwordx4 v[92:95], v109, s[48:51], s68 offen offset:0 nt\n"
  "ds_write2_b32 v107, v40, v41 offset0:0 offset1:72\n"
  "v_mfma_f32_16x16x32_bf16 v[12:15], v[60:63], a[84:87], 0\n"
  "v_mul_f32 v34, v34, v86\n"
  "v_mul_f32 v38, v38, v86\n"
  "v_mfma_f32_16x16x32_bf16 v[12:15], v[56:59], a[60:63], v[12:15]\n"
  "v_cvt_pk_bf16_f32 v26, v26, v30\n"
  "v_cvt_pk_bf16_f32 v30, v27, v31\n"
  "v_mfma_f32_16x16x32_bf16 v[12:15], v[52:55], a[36:39], v[12:15]\n"
  "v_cvt_pk_bf16_f32 v27, v34, v38\n"
  "v_mul_f32 v35, v35, v87\n"
  "v_mfma_f32_16x16x32_bf16 v[12:15], v[48:51], a[12:15], v[12:15]\n"
  "ds_write2_b32 v107, v42, v43 offset0:144 offset1:216\n"
  "v_mfma_f32_16x16x32_bf16 v[16:19], v[48:51], a[16:19], 0\n"
  "v_mul_f32 v39, v39, v87\n"
  "v_cvt_pk_bf16_f32 v31, v35, v39\n"
  "v_mfma_f32_16x16x32_bf16 v[16:19], v[52:55], a[40:43], v[16:19]\n"
  "v_max3_f32 v123, v123, v84, v85\n"
  "v_max3_f32 v123, v123, v86, v87\n"
  "v_mfma_f32_16x16x32_bf16 v[16:19], v[56:59], a[64:67], v[16:19]\n"
  "v_xor_b32 v104, 0x4000, v104\n"
  "v_mfma_f32_16x16x32_bf16 v[16:19], v[60:63], a[88:91], v[16:19]\n"
  "v_mfma_f32_16x16x32_bf16 v[20:23], v[60:63], a[92:95], 0\n"
  "v_mfma_f32_16x16x32_bf16 v[20:23], v[56:59], a[68:71], v[20:23]\n"
  "v_mfma_f32_16x16x32_bf16 v[20:23], v[52:55], a[44:47], v[20:23]\n"
  "v_mfma_f32_16x16x32_bf16 v[20:23], v[48:51], a[20:23], v[20:23]\n"
  "s_waitcnt lgkmcnt(2)\n"
  "v_mfma_f32_16x16x32_bf16 v[20:23], v[64:67], a[116:119], v[20:23]\n"
  "s_add_u32 s66, s66, 6144\n"
  "ds_read_b128 v[48:51], v105 offset:0\n"
  "v_mfma_f32_16x16x32_bf16 v[20:23], v[68:71], a[140:143], v[20:23]\n"
  "s_cmp_ge_u32 s66, 86016\n"
  "s_cselect_b32 s64, 36864, 0\n"
  "v_mfma_f32_16x16x32_bf16 v[20:23], v[72:75], a[164:167], v[20:23]\n"
  "s_sub_u32 s66, s66, s64\n"
  "ds_read_b128 v[52:55], v105 offset:1024\n"
  "v_mfma_f32_16x16x32_bf16 v[20:23], v[76:79], a[188:191], v[20:23]\n"
  "ds_write_b64 v106, v[24:25] offset:0\n"
  "v_mfma_f32_16x16x32_bf16 v[16:19], v[76:79], a[184:187], v[16:19]\n"
  "s_sub_u32 s90, s90, 1\n"
  "s_cselect_b32 s63, s91, 6144\n"
  "s_cmov_b64 s[90:91], s[92:93]\n"
  "s_cmov_b64 s[92:93], s[94:95]\n"
  "ds_read_b64_tr_b16 v[96:97], v108 offset:2304\n"
  "ds_read_b64_tr_b16 v[98:99], v108 offset:2336\n"
  "v_mfma_f32_16x16x32_bf16 v[16:19], v[72:75], a[160:163], v[16:19]\n"
  "s_add_u32 s67, s67, s63\n"
  "v_mfma_f32_16x16x32_bf16 v[16:19], v[68:71], a[136:139], v[16:19]\n"
  "ds_read_b128 v[56:59], v105 offset:2048\n"
  "v_mfma_f32_16x16x32_bf16 v[16:19], v[64:67], a[112:115], v[16:19]\n"
  "v_mfma_f32_16x16x32_bf16 v[12:15], v[64:67], a[108:111], v[12:15]\n"
  "v_mfma_f32_16x16x32_bf16 v[12:15], v[68:71], a[132:135], v[12:15]\n"
  "v_mfma_f32_16x16x32_bf16 v[12:15], v[72:75], a[156:159], v[12:15]\n"
  "ds_read_b128 v[60:63], v105 offset:3072\n"
  "v_mfma_f32_16x16x32_bf16 v[12:15], v[76:79], a[180:183], v[12:15]\n"
  "v_smfmac_f32_16x16x64_bf16 v[80:83], v[88:91], v[64:71], v124\n"
  "v_smfmac_f32_16x16x64_bf16 v[80:83], v[88:91], v[72:79], v124\n"
  "s_waitcnt lgkmcnt(2)\n"
  "buffer_store_dwordx4 v[96:99], v109, s[48:51], s68 offen offset:128 nt\n"
  "ds_write_b64 v106, v[28:29] offset:288\n"
  "v_mfma_f32_16x16x32_bf16 v[8:11], v[76:79], a[176:179], v[8:11]\n"
  "v_mfma_f32_16x16x32_bf16 v[8:11], v[72:75], a[152:155], v[8:11]\n"
  "v_mfma_f32_16x16x32_bf16 v[8:11], v[68:71], a[128:131], v[8:11]\n"
  "v_add_f32 v44, v80, v81\n"
  "v_mov_b32 v80, 0\n"
  "v_mfma_f32_16x16x32_bf16 v[8:11], v[64:67], a[104:107], v[8:11]\n"
  "ds_write_b32 v110, v44\n"
  "v_mfma_f32_16x16x32_bf16 v[4:7], v[64:67], a[100:103], v[4:7]\n"
  "v_mov_b32 v81, 0\n"
  "v_mfma_f32_16x16x32_bf16 v[4:7], v[68:71], a[124:127], v[4:7]\n"
  "ds_read_b64_tr_b16 v[100:101], v108 offset:4608\n"
  "ds_read_b64_tr_b16 v[102:103], v108 offset:4640\n"
  "v_mfma_f32_16x16x32_bf16 v[4:7], v[72:75], a[148:151], v[4:7]\n"
  "v_xor_b32 v108, 0x2000, v108\n"
  "v_mfma_f32_16x16x32_bf16 v[4:7], v[76:79], a[172:175], v[4:7]\n"
  "v_mfma_f32_16x16x32_bf16 v[0:3], v[76:79], a[168:171], v[0:3]\n"
  "v_mfma_f32_16x16x32_bf16 v[0:3], v[72:75], a[144:147], v[0:3]\n"
  "v_mfma_f32_16x16x32_bf16 v[0:3], v[68:71], a[120:123], v[0:3]\n"
  "v_mfma_f32_16x16x32_bf16 v[0:3], v[64:67], a[96:99], v[0:3]\n"
  "s_waitcnt lgkmcnt(4)\n"
  "v_mfma_f32_16x16x32_bf16 v[0:3], v[48:51], a[192:195], v[0:3]\n"
  "s_add_u32 s72, s72, 1\n"
  "ds_read_b128 v[64:67], v105 offset:4096\n"
  "v_mfma_f32_16x16x32_bf16 v[0:3], v[52:55], a[216:219], v[0:3]\n"
  "s_lshr_b32 s74, s72, 1\n"
  "v_readlane_b32 s73, v126, s74\n"
  "v_mfma_f32_16x16x32_bf16 v[0:3], v[56:59], a[240:243], v[0:3]\n"
  "s_lshl_b32 s74, s72, 4\n"
  "ds_read_b128 v[68:71], v105 offset:5120\n"
  "v_mfma_f32_16x16x32_bf16 v[0:3], v[60:63], v[136:139], v[0:3]\n"
  "s_lshr_b32 s73, s73, s74\n"
  "s_waitcnt lgkmcnt(2)\n"
  "buffer_store_dwordx4 v[100:103], v109, s[48:51], s68 offen offset:256 nt\n"
  "ds_write_b64 v106, v[26:27] offset:576\n"
  "v_mfma_f32_16x16x32_bf16 v[4:7], v[60:63], v[140:143], v[4:7]\n"
  "s_and_b32 s73, s73, 0xffff\n"
  "v_mfma_f32_16x16x32_bf16 v[4:7], v[56:59], a[244:247], v[4:7]\n"
  "s_lshl_b32 s73, s73, 13\n"
  "v_mfma_f32_16x16x32_bf16 v[4:7], v[52:55], a[220:223], v[4:7]\n"
  "s_add_u32 s68, s68, s69\n"
  "ds_read_b128 v[72:75], v105 offset:6144\n"
  "v_mfma_f32_16x16x32_bf16 v[4:7], v[48:51], a[196:199], v[4:7]\n"
  "s_mov_b32 s69, s70\n"
  "v_mfma_f32_16x16x32_bf16 v[8:11], v[48:51], a[200:203], v[8:11]\n"
  "s_movk_i32 s70, 6144\n"
  "v_mfma_f32_16x16x32_bf16 v[8:11], v[52:55], a[224:227], v[8:11]\n"
  "v_mfma_f32_16x16x32_bf16 v[8:11], v[56:59], a[248:251], v[8:11]\n"
  "ds_read_b128 v[76:79], v105 offset:7168\n"
  "v_xor_b32 v105, 0x4000, v105\n"
  "v_mfma_f32_16x16x32_bf16 v[8:11], v[60:63], v[144:147], v[8:11]\n"
  "v_mfma_f32_16x16x32_bf16 v[12:15], v[60:63], v[148:151], v[12:15]\n"
  "v_mfma_f32_16x16x32_bf16 v[12:15], v[56:59], a[252:255], v[12:15]\n"
  "v_mfma_f32_16x16x32_bf16 v[12:15], v[52:55], a[228:231], v[12:15]\n"
  "v_mfma_f32_16x16x32_bf16 v[12:15], v[48:51], a[204:207], v[12:15]\n"
  "ds_write_b64 v106, v[30:31] offset:864\n"
  "v_mfma_f32_16x16x32_bf16 v[16:19], v[48:51], a[208:211], v[16:19]\n"
  "v_mfma_f32_16x16x32_bf16 v[16:19], v[52:55], a[232:235], v[16:19]\n"
  "v_mfma_f32_16x16x32_bf16 v[16:19], v[56:59], v[128:131], v[16:19]\n"
  "v_mfma_f32_16x16x32_bf16 v[16:19], v[60:63], v[152:155], v[16:19]\n"
  "v_xor_b32 v106, 0x2000, v106\n"
  "v_xor_b32 v107, 0x2000, v107\n"
  "v_mfma_f32_16x16x32_bf16 v[20:23], v[60:63], v[156:159], v[20:23]\n"
  "v_xor_b32 v110, 0x2000, v110\n"
  "v_mfma_f32_16x16x32_bf16 v[20:23], v[56:59], v[132:135], v[20:23]\n"
  "v_mfma_f32_16x16x32_bf16 v[20:23], v[52:55], a[236:239], v[20:23]\n"
  "s_sub_u32 s62, s56, 1\n"
  "s_cmp_lt_u32 s62, s61\n"
  "v_mfma_f32_16x16x32_bf16 v[20:23], v[48:51], a[212:215], v[20:23]\n"
  "s_cbranch_scc1 B_H_%=\n"
  "s_cmp_lg_u32 s56, 0\n"
  "s_cbranch_scc1 B_H1_%=\n"
  "s_mov_b32 s52, s53\n"
  "s_mov_b32 s53, s54\n"
  "s_mov_b32 s54, s55\n"
  "s_cmp_eq_u32 s52, 0\n"
  "s_cbranch_scc1 B_LAST_%=\n"
  "s_lshr_b32 s62, s52, 16\n"
  "s_lshl1_add_u32 s62, s62, s65\n"
  "s_mul_i32 s57, s62, 98304\n"
  "s_and_b32 s56, s52, 0xff\n"
  "s_cmp_lg_u32 s53, 0\n"
  "s_cselect_b32 s62, 6, 0\n"
  "s_sub_u32 s56, s56, s62\n"
  "s_waitcnt vmcnt(16) lgkmcnt(0)\n"
  "s_barrier\n"
  "v_mfma_f32_16x16x32_bf16 v[20:23], v[64:67], v[180:183], v[20:23]\n"
  "s_add_u32 m0, s66, 0\n"
  "ds_read_b128 v[48:51], v104 offset:0\n"
  "ds_read_b128 v[84:87], v111\n"
  "v_mfma_f32_16x16x32_bf16 v[20:23], v[68:71], v[204:207], v[20:23]\n"
  "ds_read_b128 v[100:103], v111 offset:64\n"
  "v_xor_b32 v111, 0x2000, v111\n"
  "v_mfma_f32_16x16x32_bf16 v[20:23], v[72:75], v[228:231], v[20:23]\n"
  "ds_read_b128 v[52:55], v104 offset:1024\n"
  "v_mfma_f32_16x16x32_bf16 v[44:47], v[76:79], v[252:255], v[20:23]\n"
  "buffer_load_dwordx4 v112, s[44:47], s67 offen offset:0  lds\n"
  "v_mfma_f32_16x16x32_bf16 v[16:19], v[76:79], v[248:251], v[16:19]\n"
  "s_add_u32 m0, s66, 1920\n"
  "v_mfma_f32_16x16x32_bf16 v[16:19], v[72:75], v[224:227], v[16:19]\n"
  "v_mfma_f32_16x16x32_bf16 v[16:19], v[68:71], v[200:203], v[16:19]\n"
  "ds_read_b128 v[56:59], v104 offset:2048\n"
  "v_mfma_f32_16x16x32_bf16 v[40:43], v[64:67], v[176:179], v[16:19]\n"
  "buffer_load_dwordx4 v112, s[44:47], s67 offen offset:128  lds\n"
  "v_mfma_f32_16x16x32_bf16 v[12:15], v[64:67], v[172:175], v[12:15]\n"
  "s_add_u32 m0, s66, 3840\n"
  "v_mfma_f32_16x16x32_bf16 v[12:15], v[68:71], v[196:199], v[12:15]\n"
  "v_mfma_f32_16x16x32_bf16 v[12:15], v[72:75], v[220:223], v[12:15]\n"
  "s_waitcnt lgkmcnt(2)\n"
  "ds_read_b128 v[60:63], v104 offset:3072\n"
  "v_add_f32 v84, v84, v100\n"
  "v_mfma_f32_16x16x32_bf16 v[36:39], v[76:79], v[244:247], v[12:15]\n"
  "buffer_load_dwordx4 v112, s[44:47], s67 offen offset:256  lds\n"
  "v_mfma_f32_16x16x32_bf16 v[8:11], v[76:79], v[240:243], v[8:11]\n"
  "v_add_f32 v85, v85, v101\n"
  "v_add_f32 v86, v86, v102\n"
  "v_mfma_f32_16x16x32_bf16 v[8:11], v[72:75], v[216:219], v[8:11]\n"
  "v_add_f32 v87, v87, v103\n"
  "v_max3_f32 v123, v123, v84, v85\n"
  "v_mfma_f32_16x16x32_bf16 v[8:11], v[68:71], v[192:195], v[8:11]\n"
  "v_rcp_f32 v84, v84\n"
  "v_mfma_f32_16x16x32_bf16 v[32:35], v[64:67], v[168:171], v[8:11]\n"
  "v_rcp_f32 v85, v85\n"
  "v_mfma_f32_16x16x32_bf16 v[4:7], v[64:67], v[164:167], v[4:7]\n"
  "v_max3_f32 v123, v123, v86, v87\n"
  "v_mul_f32 v40, v40, v84\n"
  "v_mfma_f32_16x16x32_bf16 v[4:7], v[68:71], v[188:191], v[4:7]\n"
  "v_rcp_f32 v86, v86\n"
  "v_mfma_f32_16x16x32_bf16 v[4:7], v[72:75], v[212:215], v[4:7]\n"
  "v_rcp_f32 v87, v87\n"
  "v_mfma_f32_16x16x32_bf16 v[28:31], v[76:79], v[236:239], v[4:7]\n"
  "v_mul_f32 v44, v44, v84\n"
  "v_cvt_pk_bf16_f32 v40, v40, v44\n"
  "v_mfma_f32_16x16x32_bf16 v[0:3], v[76:79], v[232:235], v[0:3]\n"
  "s_add_u32 m0, s75, 0\n"
  "v_mul_f32 v41, v41, v85\n"
  "v_mul_f32 v45, v45, v85\n"
  "v_mfma_f32_16x16x32_bf16 v[0:3], v[72:75], v[208:211], v[0:3]\n"
  "buffer_load_dwordx4 v112, s[76:79], s57 offen offset:0  lds\n"
  "v_mfma_f32_16x16x32_bf16 v[0:3], v[68:71], v[184:187], v[0:3]\n"
  "v_cvt_pk_bf16_f32 v41, v41, v45\n"
  "v_mul_f32 v42, v42, v86\n"
  "v_mfma_f32_16x16x32_bf16 v[24:27], v[64:67], v[160:163], v[0:3]\n"
  "s_waitcnt lgkmcnt(0)\n"
  "v_mul_f32 v46, v46, v86\n"
  "v_cvt_pk_bf16_f32 v42, v42, v46\n"
  "v_mfma_f32_16x16x32_bf16 v[0:3], v[48:51], a[0:3], 0\n"
  "ds_read_b128 v[64:67], v104 offset:4096\n"
  "v_mul_f32 v43, v43, v87\n"
  "v_mfma_f32_16x16x32_bf16 v[0:3], v[52:55], a[24:27], v[0:3]\n"
  "v_mul_f32 v47, v47, v87\n"
  "v_cvt_pk_bf16_f32 v43, v43, v47\n"
  "v_mfma_f32_16x16x32_bf16 v[0:3], v[56:59], a[48:51], v[0:3]\n"
  "ds_read_b128 v[68:71], v104 offset:5120\n"
  "v_mul_f32 v24, v24, v84\n"
  "v_mfma_f32_16x16x32_bf16 v[0:3], v[60:63], a[72:75], v[0:3]\n"
  "ds_read_b64_tr_b16 v[92:93], v108 offset:0\n"
  "ds_read_b64_tr_b16 v[94:95], v108 offset:32\n"
  "v_mfma_f32_16x16x32_bf16 v[4:7], v[60:63], a[76:79], 0\n"
  "buffer_load_dwordx4 v113, s[76:79], s57 offen offset:1024  lds\n"
  "v_mfma_f32_16x16x32_bf16 v[4:7], v[56:59], a[52:55], v[4:7]\n"
  "v_mul_f32 v25, v25, v85\n"
  "v_mul_f32 v28, v28, v84\n"
  "v_mfma_f32_16x16x32_bf16 v[4:7], v[52:55], a[28:31], v[4:7]\n"
  "ds_read_b128 v[72:75], v104 offset:6144\n"
  "v_mul_f32 v29, v29, v85\n"
  "v_mfma_f32_16x16x32_bf16 v[4:7], v[48:51], a[4:7], v[4:7]\n"
  "v_mul_f32 v32, v32, v84\n"
  "v_mul_f32 v33, v33, v85\n"
  "v_mfma_f32_16x16x32_bf16 v[8:11], v[48:51], a[8:11], 0\n"
  "v_mul_f32 v36, v36, v84\n"
  "v_mul_f32 v37, v37, v85\n"
  "v_mfma_f32_16x16x32_bf16 v[8:11], v[52:55], a[32:35], v[8:11]\n"
  "v_cvt_pk_bf16_f32 v24, v24, v28\n"
  "v_cvt_pk_bf16_f32 v28, v25, v29\n"
  "v_mfma_f32_16x16x32_bf16 v[8:11], v[56:59], a[56:59], v[8:11]\n"
  "s_add_u32 m0, s75, 1920\n"
  "ds_read_b128 v[76:79], v104 offset:7168\n"
  "v_cvt_pk_bf16_f32 v25, v32, v36\n"
  "v_mfma_f32_16x16x32_bf16 v[8:11], v[60:63], a[80:83], v[8:11]\n"
  "buffer_load_dwordx4 v112, s[76:79], s57 offen offset:128  lds\n"
  "v_smfmac_f32_16x16x64_bf16 v[80:83], v[88:91], v[48:55], v124\n"
  "v_cvt_pk_bf16_f32 v29, v33, v37\n"
  "v_smfmac_f32_16x16x64_bf16 v[80:83], v[88:91], v[56:63], v124\n"
  "s_waitcnt lgkmcnt(2)\n"
  "buffer_store_dwordx4 v[92:95], v109, s[48:51], s68 offen offset:0 nt\n"
  "v_mul_f32 v26, v26, v86\n"
  "v_mul_f32 v27, v27, v87\n"
  "v_mfma_f32_16x16x32_bf16 v[12:15], v[60:63], a[84:87], 0\n"
  "v_mul_f32 v30, v30, v86\n"
  "v_mul_f32 v31, v31, v87\n"
  "v_mfma_f32_16x16x32_bf16 v[12:15], v[56:59], a[60:63], v[12:15]\n"
  "v_mul_f32 v34, v34, v86\n"
  "v_mul_f32 v35, v35, v87\n"
  "v_mfma_f32_16x16x32_bf16 v[12:15], v[52:55], a[36:39], v[12:15]\n"
  "v_mul_f32 v38, v38, v86\n"
  "v_mul_f32 v39, v39, v87\n"
  "v_mfma_f32_16x16x32_bf16 v[12:15], v[48:51], a[12:15], v[12:15]\n"
  "v_cvt_pk_bf16_f32 v26, v26, v30\n"
  "v_cvt_pk_bf16_f32 v30, v27, v31\n"
  "v_mfma_f32_16x16x32_bf16 v[16:19], v[48:51], a[16:19], 0\n"
  "buffer_load_dwordx4 v113, s[76:79], s57 offen offset:1152  lds\n"
  "v_mfma_f32_16x16x32_bf16 v[16:19], v[52:55], a[40:43], v[16:19]\n"
  "v_cvt_pk_bf16_f32 v27, v34, v38\n"
  "v_cvt_pk_bf16_f32 v31, v35, v39\n"
  "v_mfma_f32_16x16x32_bf16 v[16:19], v[56:59], a[64:67], v[16:19]\n"
  "v_max3_f32 v123, v123, v84, v85\n"
  "v_max3_f32 v123, v123, v86, v87\n"
  "v_mfma_f32_16x16x32_bf16 v[16:19], v[60:63], a[88:91], v[16:19]\n"
  "ds_write2_b32 v107, v40, v41 offset0:0 offset1:72\n"
  "v_mfma_f32_16x16x32_bf16 v[20:23], v[60:63], a[92:95], 0\n"
  "v_xor_b32 v104, 0x4000, v104\n"
  "v_mfma_f32_16x16x32_bf16 v[20:23], v[56:59], a[68:71], v[20:23]\n"
  "v_mfma_f32_16x16x32_bf16 v[20:23], v[52:55], a[44:47], v[20:23]\n"
  "s_add_u32 m0, s75, 3840\n"
  "v_mfma_f32_16x16x32_bf16 v[20:23], v[48:51], a[20:23], v[20:23]\n"
  "s_waitcnt lgkmcnt(1)\n"
  "buffer_load_dwordx4 v112, s[76:79], s57 offen offset:256  lds\n"
  "v_mfma_f32_16x16x32_bf16 v[20:23], v[64:67], a[116:119], v[20:23]\n"
  "s_add_u32 s66, s66, 6144\n"
  "ds_read_b128 v[48:51], v105 offset:0\n"
  "v_mfma_f32_16x16x32_bf16 v[20:23], v[68:71], a[140:143], v[20:23]\n"
  "s_cmp_ge_u32 s66, 86016\n"
  "s_cselect_b32 s64, 36864, 0\n"
  "v_mfma_f32_16x16x32_bf16 v[20:23], v[72:75], a[164:167], v[20:23]\n"
  "s_sub_u32 s66, s66, s64\n"
  "ds_read_b128 v[52:55], v105 offset:1024\n"
  "v_mfma_f32_16x16x32_bf16 v[20:23], v[76:79], a[188:191], v[20:23]\n"
  "s_sub_u32 s90, s90, 1\n"
  "s_cselect_b32 s63, s91, 6144\n"
  "s_cmov_b64 s[90:91], s[92:93]\n"
  "s_cmov_b64 s[92:93], s[94:95]\n"
  "ds_read_b64_tr_b16 v[96:97], v108 offset:2304\n"
  "ds_read_b64_tr_b16 v[98:99], v108 offset:2336\n"
  "v_mfma_f32_16x16x32_bf16 v[16:19], v[76:79], a[184:187], v[16:19]\n"
  "s_add_u32 s67, s67, s63\n"
  "v_mfma_f32_16x16x32_bf16 v[16:19], v[72:75], a[160:163], v[16:19]\n"
  "v_mfma_f32_16x16x32_bf16 v[16:19], v[68:71], a[136:139], v[16:19]\n"
  "buffer_load_dwordx4 v113, s[76:79], s57 offen offset:1280  lds\n"
  "ds_read_b128 v[56:59], v105 offset:2048\n"
  "v_mfma_f32_16x16x32_bf16 v[16:19], v[64:67], a[112:115], v[16:19]\n"
  "s_add_u32 s57, s57, 6144\n"
  "ds_write2_b32 v107, v42, v43 offset0:144 offset1:216\n"
  "v_mfma_f32_16x16x32_bf16 v[12:15], v[64:67], a[108:111], v[12:15]\n"
  "v_mfma_f32_16x16x32_bf16 v[12:15], v[68:71], a[132:135], v[12:15]\n"
  "v_mfma_f32_16x16x32_bf16 v[12:15], v[72:75], a[156:159], v[12:15]\n"
  "ds_read_b128 v[60:63], v105 offset:3072\n"
  "v_mfma_f32_16x16x32_bf16 v[12:15], v[76:79], a[180:183], v[12:15]\n"
  "ds_write_b64 v106, v[24:25] offset:0\n"
  "v_smfmac_f32_16x16x64_bf16 v[80:83], v[88:91], v[64:71], v124\n"
  "s_add_u32 m0, s75, 5888\n"
  "v_smfmac_f32_16x16x64_bf16 v[80:83], v[88:91], v[72:79], v124\n"
  "buffer_load_dwordx4 v112, s[76:79], s57 offen offset:256  lds\n"
  "v_mfma_f32_16x16x32_bf16 v[8:11], v[76:79], a[176:179], v[8:11]\n"
  "v_mfma_f32_16x16x32_bf16 v[8:11], v[72:75], a[152:155], v[8:11]\n"
  "v_mfma_f32_16x16x32_bf16 v[8:11], v[68:71], a[128:131], v[8:11]\n"
  "v_add_f32 v44, v80, v81\n"
  "v_mov_b32 v80, 0\n"
  "v_mfma_f32_16x16x32_bf16 v[8:11], v[64:67], a[104:107], v[8:11]\n"
  "s_waitcnt lgkmcnt(4)\n"
  "buffer_store_dwordx4 v[96:99], v109, s[48:51], s68 offen offset:128 nt\n"
  "ds_write_b64 v106, v[28:29] offset:288\n"
  "v_mfma_f32_16x16x32_bf16 v[4:7], v[64:67], a[100:103], v[4:7]\n"
  "v_mov_b32 v81, 0\n"
  "v_mfma_f32_16x16x32_bf16 v[4:7], v[68:71], a[124:127], v[4:7]\n"
  "ds_read_b64_tr_b16 v[100:101], v108 offset:4608\n"
  "ds_read_b64_tr_b16 v[102:103], v108 offset:4640\n"
  "v_mfma_f32_16x16x32_bf16 v[4:7], v[72:75], a[148:151], v[4:7]\n"
  "buffer_load_dwordx4 v113, s[76:79], s57 offen offset:1280  lds\n"
  "v_mfma_f32_16x16x32_bf16 v[4:7], v[76:79], a[172:175], v[4:7]\n"
  "ds_write_b64 v106, v[26:27] offset:576\n"
  "v_mfma_f32_16x16x32_bf16 v[0:3], v[76:79], a[168:171], v[0:3]\n"
  "v_xor_b32 v108, 0x2000, v108\n"
  "v_mfma_f32_16x16x32_bf16 v[0:3], v[72:75], a[144:147], v[0:3]\n"
  "v_mfma_f32_16x16x32_bf16 v[0:3], v[68:71], a[120:123], v[0:3]\n"
  "v_mfma_f32_16x16x32_bf16 v[0:3], v[64:67], a[96:99], v[0:3]\n"
  "s_waitcnt lgkmcnt(5)\n"
  "v_mfma_f32_16x16x32_bf16 v[0:3], v[48:51], a[192:195], v[0:3]\n"
  "s_add_u32 m0, s75, 8064\n"
  "ds_read_b128 v[64:67], v105 offset:4096\n"
  "v_mfma_f32_16x16x32_bf16 v[0:3], v[52:55], a[216:219], v[0:3]\n"
  "buffer_load_dwordx4 v112, s[76:79], s57 offen offset:128  lds\n"
  "v_mfma_f32_16x16x32_bf16 v[0:3], v[56:59], a[240:243], v[0:3]\n"
  "ds_read_b128 v[68:71], v105 offset:5120\n"
  "v_mfma_f32_16x16x32_bf16 v[0:3], v[60:63], v[136:139], v[0:3]\n"
  "s_waitcnt lgkmcnt(3)\n"
  "buffer_store_dwordx4 v[100:103], v109, s[48:51], s68 offen offset:256 nt\n"
  "ds_write_b64 v106, v[30:31] offset:864\n"
  "v_mfma_f32_16x16x32_bf16 v[4:7], v[60:63], v[140:143], v[4:7]\n"
  "s_add_u32 s68, s68, s69\n"
  "v_mfma_f32_16x16x32_bf16 v[4:7], v[56:59], a[244:247], v[4:7]\n"
  "s_mov_b32 s69, s70\n"
  "v_mfma_f32_16x16x32_bf16 v[4:7], v[52:55], a[220:223], v[4:7]\n"
  "s_movk_i32 s70, 6144\n"
  "ds_read_b128 v[72:75], v105 offset:6144\n"
  "v_mfma_f32_16x16x32_bf16 v[4:7], v[48:51], a[196:199], v[4:7]\n"
  "ds_write_b32 v110, v44\n"
  "v_mfma_f32_16x16x32_bf16 v[8:11], v[48:51], a[200:203], v[8:11]\n"
  "buffer_load_dwordx4 v113, s[76:79], s57 offen offset:1152  lds\n"
  "v_mfma_f32_16x16x32_bf16 v[8:11], v[52:55], a[224:227], v[8:11]\n"
  "v_mfma_f32_16x16x32_bf16 v[8:11], v[56:59], a[248:251], v[8:11]\n"
  "ds_read_b128 v[76:79], v105 offset:7168\n"
  "v_xor_b32 v105, 0x4000, v105\n"
  "v_mfma_f32_16x16x32_bf16 v[8:11], v[60:63], v[144:147], v[8:11]\n"
  "v_mfma_f32_16x16x32_bf16 v[12:15], v[60:63], v[148:151], v[12:15]\n"
  "v_mfma_f32_16x16x32_bf16 v[12:15], v[56:59], a[252:255], v[12:15]\n"
  "v_mfma_f32_16x16x32_bf16 v[12:15], v[52:55], a[228:231], v[12:15]\n"
  "v_mfma_f32_16x16x32_bf16 v[12:15], v[48:51], a[204:207], v[12:15]\n"
  "v_mfma_f32_16x16x32_bf16 v[16:19], v[48:51], a[208:211], v[16:19]\n"
  "v_mfma_f32_16x16x32_bf16 v[16:19], v[52:55], a[232:235], v[16:19]\n"
  "v_mfma_f32_16x16x32_bf16 v[16:19], v[56:59], v[128:131], v[16:19]\n"
  "v_mfma_f32_16x16x32_bf16 v[16:19], v[60:63], v[152:155], v[16:19]\n"
  "v_xor_b32 v106, 0x2000, v106\n"
  "v_xor_b32 v107, 0x2000, v107\n"
  "v_mfma_f32_16x16x32_bf16 v[20:23], v[60:63], v[156:159], v[20:23]\n"
  "v_xor_b32 v110, 0x2000, v110\n"
  "v_mfma_f32_16x16x32_bf16 v[20:23], v[56:59], v[132:135], v[20:23]\n"
  "v_mfma_f32_16x16x32_bf16 v[20:23], v[52:55], a[236:239], v[20:23]\n"
  "v_mfma_f32_16x16x32_bf16 v[20:23], v[48:51], a[212:215], v[20:23]\n"
  "s_waitcnt vmcnt(29) lgkmcnt(0)\n"
  "s_barrier\n"
  "v_mfma_f32_16x16x32_bf16 v[20:23], v[64:67], v[180:183], v[20:23]\n"
  "s_add_u32 m0, s66, 0\n"
  "ds_read_b128 v[48:51], v104 offset:0\n"
  "ds_read_b128 v[84:87], v111\n"
  "v_mfma_f32_16x16x32_bf16 v[20:23], v[68:71], v[204:207], v[20:23]\n"
  "ds_read_b128 v[100:103], v111 offset:64\n"
  "v_xor_b32 v111, 0x2000, v111\n"
  "v_mfma_f32_16x16x32_bf16 v[20:23], v[72:75], v[228:231], v[20:23]\n"
  "ds_read_b128 v[52:55], v104 offset:1024\n"
  "v_mfma_f32_16x16x32_bf16 v[44:47], v[76:79], v[252:255], v[20:23]\n"
  "buffer_load_dwordx4 v112, s[44:47], s67 offen offset:0  lds\n"
  "v_mfma_f32_16x16x32_bf16 v[16:19], v[76:79], v[248:251], v[16:19]\n"
  "s_add_u32 m0, s66, 1920\n"
  "v_mfma_f32_16x16x32_bf16 v[16:19], v[72:75], v[224:227], v[16:19]\n"
  "v_mfma_f32_16x16x32_bf16 v[16:19], v[68:71], v[200:203], v[16:19]\n"
  "ds_read_b128 v[56:59], v104 offset:2048\n"
  "v_mfma_f32_16x16x32_bf16 v[40:43], v[64:67], v[176:179], v[16:19]\n"
  "buffer_load_dwordx4 v112, s[44:47], s67 offen offset:128  lds\n"
  "v_mfma_f32_16x16x32_bf16 v[12:15], v[64:67], v[172:175], v[12:15]\n"
  "s_add_u32 m0, s66, 3840\n"
  "v_mfma_f32_16x16x32_bf16 v[12:15], v[68:71], v[196:199], v[12:15]\n"
  "v_mfma_f32_16x16x32_bf16 v[12:15], v[72:75], v[220:223], v[12:15]\n"
  "s_waitcnt lgkmcnt(2)\n"
  "ds_read_b128 v[60:63], v104 offset:3072\n"
  "v_add_f32 v84, v84, v100\n"
  "v_mfma_f32_16x16x32_bf16 v[36:39], v[76:79], v[244:247], v[12:15]\n"
  "buffer_load_dwordx4 v112, s[44:47], s67 offen offset:256  lds\n"
  "v_mfma_f32_16x16x32_bf16 v[8:11], v[76:79], v[240:243], v[8:11]\n"
  "v_add_f32 v85, v85, v101\n"
  "v_add_f32 v86, v86, v102\n"
  "v_mfma_f32_16x16x32_bf16 v[8:11], v[72:75], v[216:219], v[8:11]\n"
  "v_add_f32 v87, v87, v103\n"
  "v_max3_f32 v123, v123, v84, v85\n"
  "v_mfma_f32_16x16x32_bf16 v[8:11], v[68:71], v[192:195], v[8:11]\n"
  "v_rcp_f32 v84, v84\n"
  "v_mfma_f32_16x16x32_bf16 v[32:35], v[64:67], v[168:171], v[8:11]\n"
  "v_rcp_f32 v85, v85\n"
  "v_mfma_f32_16x16x32_bf16 v[4:7], v[64:67], v[164:167], v[4:7]\n"
  "v_max3_f32 v123, v123, v86, v87\n"
  "v_mul_f32 v40, v40, v84\n"
  "v_mfma_f32_16x16x32_bf16 v[4:7], v[68:71], v[188:191], v[4:7]\n"
  "v_rcp_f32 v86, v86\n"
  "v_mfma_f32_16x16x32_bf16 v[4:7], v[72:75], v[212:215], v[4:7]\n"
  "v_rcp_f32 v87, v87\n"
  "v_mfma_f32_16x16x32_bf16 v[28:31], v[76:79], v[236:239], v[4:7]\n"
  "v_mul_f32 v44, v44, v84\n"
  "v_cvt_pk_bf16_f32 v40, v40, v44\n"
  "v_mfma_f32_16x16x32_bf16 v[0:3], v[76:79], v[232:235], v[0:3]\n"
  "s_add_u32 m0, s75, 10240\n"
  "v_mul_f32 v41, v41, v85\n"
  "v_mul_f32 v45, v45, v85\n"
  "v_mfma_f32_16x16x32_bf16 v[0:3], v[72:75], v[208:211], v[0:3]\n"
  "buffer_load_dwordx4 v112, s[76:79], s57 offen offset:0  lds\n"
  "v_mfma_f32_16x16x32_bf16 v[0:3], v[68:71], v[184:187], v[0:3]\n"
  "v_cvt_pk_bf16_f32 v41, v41, v45\n"
  "v_mul_f32 v42, v42, v86\n"
  "v_mfma_f32_16x16x32_bf16 v[24:27], v[64:67], v[160:163], v[0:3]\n"
  "s_waitcnt lgkmcnt(0)\n"
  "v_mul_f32 v46, v46, v86\n"
  "v_cvt_pk_bf16_f32 v42, v42, v46\n"
  "v_mfma_f32_16x16x32_bf16 v[0:3], v[48:51], a[0:3], 0\n"
  "ds_read_b128 v[64:67], v104 offset:4096\n"
  "v_mul_f32 v43, v43, v87\n"
  "v_mfma_f32_16x16x32_bf16 v[0:3], v[52:55], a[24:27], v[0:3]\n"
  "v_mul_f32 v47, v47, v87\n"
  "v_cvt_pk_bf16_f32 v43, v43, v47\n"
  "v_mfma_f32_16x16x32_bf16 v[0:3], v[56:59], a[48:51], v[0:3]\n"
  "ds_read_b128 v[68:71], v104 offset:5120\n"
  "v_mul_f32 v24, v24, v84\n"
  "v_mfma_f32_16x16x32_bf16 v[0:3], v[60:63], a[72:75], v[0:3]\n"
  "buffer_load_dwordx4 v113, s[76:79], s57 offen offset:1024  lds\n"
  "v_mfma_f32_16x16x32_bf16 v[4:7], v[60:63], a[76:79], 0\n"
  "s_add_u32 s57, s57, 6144\n"
  "ds_read_b64_tr_b16 v[92:93], v108 offset:0\n"
  "ds_read_b64_tr_b16 v[94:95], v108 offset:32\n"
  "v_mfma_f32_16x16x32_bf16 v[4:7], v[56:59], a[52:55], v[4:7]\n"
  "v_mul_f32 v25, v25, v85\n"
  "v_mul_f32 v28, v28, v84\n"
  "v_mfma_f32_16x16x32_bf16 v[4:7], v[52:55], a[28:31], v[4:7]\n"
  "ds_read_b128 v[72:75], v104 offset:6144\n"
  "v_mul_f32 v29, v29, v85\n"
  "v_mfma_f32_16x16x32_bf16 v[4:7], v[48:51], a[4:7], v[4:7]\n"
  "v_mul_f32 v32, v32, v84\n"
  "v_mul_f32 v33, v33, v85\n"
  "v_mfma_f32_16x16x32_bf16 v[8:11], v[48:51], a[8:11], 0\n"
  "s_add_u32 m0, s75, 12288\n"
  "v_mul_f32 v36, v36, v84\n"
  "v_mul_f32 v37, v37, v85\n"
  "v_mfma_f32_16x16x32_bf16 v[8:11], v[52:55], a[32:35], v[8:11]\n"
  "buffer_load_dwordx4 v112, s[76:79], s57 offen offset:0  lds\n"
  "v_mfma_f32_16x16x32_bf16 v[8:11], v[56:59], a[56:59], v[8:11]\n"
  "ds_read_b128 v[76:79], v104 offset:7168\n"
  "v_cvt_pk_bf16_f32 v24, v24, v28\n"
  "v_mfma_f32_16x16x32_bf16 v[8:11], v[60:63], a[80:83], v[8:11]\n"
  "v_cvt_pk_bf16_f32 v28, v25, v29\n"
  "v_cvt_pk_bf16_f32 v25, v32, v36\n"
  "v_smfmac_f32_16x16x64_bf16 v[80:83], v[88:91], v[48:55], v124\n"
  "v_cvt_pk_bf16_f32 v29, v33, v37\n"
  "v_smfmac_f32_16x16x64_bf16 v[80:83], v[88:91], v[56:63], v124\n"
  "s_waitcnt lgkmcnt(2)\n"
  "buffer_store_dwordx4 v[92:95], v109, s[48:51], s68 offen offset:0 nt\n"
  "v_mul_f32 v26, v26, v86\n"
  "v_mul_f32 v27, v27, v87\n"
  "v_mfma_f32_16x16x32_bf16 v[12:15], v[60:63], a[84:87], 0\n"
  "v_mul_f32 v30, v30, v86\n"
  "v_mul_f32 v31, v31, v87\n"
  "v_mfma_f32_16x16x32_bf16 v[12:15], v[56:59], a[60:63], v[12:15]\n"
  "buffer_load_dwordx4 v113, s[76:79], s57 offen offset:1024  lds\n"
  "v_mfma_f32_16x16x32_bf16 v[12:15], v[52:55], a[36:39], v[12:15]\n"
  "v_mul_f32 v34, v34, v86\n"
  "v_mul_f32 v35, v35, v87\n"
  "v_mfma_f32_16x16x32_bf16 v[12:15], v[48:51], a[12:15], v[12:15]\n"
  "v_mul_f32 v38, v38, v86\n"
  "v_mul_f32 v39, v39, v87\n"
  "v_mfma_f32_16x16x32_bf16 v[16:19], v[48:51], a[16:19], 0\n"
  "v_cvt_pk_bf16_f32 v26, v26, v30\n"
  "v_cvt_pk_bf16_f32 v30, v27, v31\n"
  "v_mfma_f32_16x16x32_bf16 v[16:19], v[52:55], a[40:43], v[16:19]\n"
  "v_cvt_pk_bf16_f32 v27, v34, v38\n"
  "v_cvt_pk_bf16_f32 v31, v35, v39\n"
  "v_mfma_f32_16x16x32_bf16 v[16:19], v[56:59], a[64:67], v[16:19]\n"
  "s_add_u32 m0, s75, 14208\n"
  "v_max3_f32 v123, v123, v84, v85\n"
  "v_max3_f32 v123, v123, v86, v87\n"
  "v_mfma_f32_16x16x32_bf16 v[16:19], v[60:63], a[88:91], v[16:19]\n"
  "buffer_load_dwordx4 v112, s[76:79], s57 offen offset:128  lds\n"
  "v_mfma_f32_16x16x32_bf16 v[20:23], v[60:63], a[92:95], 0\n"
  "v_xor_b32 v104, 0x4000, v104\n"
  "v_mfma_f32_16x16x32_bf16 v[20:23], v[56:59], a[68:71], v[20:23]\n"
  "v_mfma_f32_16x16x32_bf16 v[20:23], v[52:55], a[44:47], v[20:23]\n"
  "v_mfma_f32_16x16x32_bf16 v[20:23], v[48:51], a[20:23], v[20:23]\n"
  "s_waitcnt lgkmcnt(0)\n"
  "v_mfma_f32_16x16x32_bf16 v[20:23], v[64:67], a[116:119], v[20:23]\n"
  "s_add_u32 s66, s66, 6144\n"
  "ds_read_b128 v[48:51], v105 offset:0\n"
  "v_mfma_f32_16x16x32_bf16 v[20:23], v[68:71], a[140:143], v[20:23]\n"
  "s_cmp_ge_u32 s66, 86016\n"
  "s_cselect_b32 s64, 36864, 0\n"
  "buffer_load_dwordx4 v113, s[76:79], s57 offen offset:1152  lds\n"
  "v_mfma_f32_16x16x32_bf16 v[20:23], v[72:75], a[164:167], v[20:23]\n"
  "s_sub_u32 s66, s66, s64\n"
  "ds_read_b128 v[52:55], v105 offset:1024\n"
  "v_mfma_f32_16x16x32_bf16 v[20:23], v[76:79], a[188:191], v[20:23]\n"
  "s_sub_u32 s90, s90, 1\n"
  "s_cselect_b32 s63, s91, 6144\n"
  "s_cmov_b64 s[90:91], s[92:93]\n"
  "s_cmov_b64 s[92:93], s[94:95]\n"
  "ds_read_b64_tr_b16 v[96:97], v108 offset:2304\n"
  "ds_read_b64_tr_b16 v[98:99], v108 offset:2336\n"
  "v_mfma_f32_16x16x32_bf16 v[16:19], v[76:79], a[184:187], v[16:19]\n"
  "s_add_u32 s67, s67, s63\n"
  "v_mfma_f32_16x16x32_bf16 v[16:19], v[72:75], a[160:163], v[16:19]\n"
  "v_mfma_f32_16x16x32_bf16 v[16:19], v[68:71], a[136:139], v[16:19]\n"
  "s_add_u32 m0, s75, 16128\n"
  "ds_read_b128 v[56:59], v105 offset:2048\n"
  "v_mfma_f32_16x16x32_bf16 v[16:19], v[64:67], a[112:115], v[16:19]\n"
  "buffer_load_dwordx4 v112, s[76:79], s57 offen offset:256  lds\n"
  "v_mfma_f32_16x16x32_bf16 v[12:15], v[64:67], a[108:111], v[12:15]\n"
  "v_mfma_f32_16x16x32_bf16 v[12:15], v[68:71], a[132:135], v[12:15]\n"
  "v_mfma_f32_16x16x32_bf16 v[12:15], v[72:75], a[156:159], v[12:15]\n"
  "ds_read_b128 v[60:63], v105 offset:3072\n"
  "v_mfma_f32_16x16x32_bf16 v[12:15], v[76:79], a[180:183], v[12:15]\n"
  "ds_write2_b32 v107, v40, v41 offset0:0 offset1:72\n"
  "v_smfmac_f32_16x16x64_bf16 v[80:83], v[88:91], v[64:71], v124\n"
  "v_smfmac_f32_16x16x64_bf16 v[80:83], v[88:91], v[72:79], v124\n"
  "buffer_load_dwordx4 v113, s[76:79], s57 offen offset:1280  lds\n"
  "v_mfma_f32_16x16x32_bf16 v[8:11], v[76:79], a[176:179], v[8:11]\n"
  "s_sub_u32 s57, s57, 12288\n"
  "v_mfma_f32_16x16x32_bf16 v[8:11], v[72:75], a[152:155], v[8:11]\n"
  "v_mfma_f32_16x16x32_bf16 v[8:11], v[68:71], a[128:131], v[8:11]\n"
  "v_add_f32 v44, v80, v81\n"
  "v_mov_b32 v80, 0\n"
  "v_mfma_f32_16x16x32_bf16 v[8:11], v[64:67], a[104:107], v[8:11]\n"
  "s_waitcnt lgkmcnt(3)\n"
  "buffer_store_dwordx4 v[96:99], v109, s[48:51], s68 offen offset:128 nt\n"
  "ds_write2_b32 v107, v42, v43 offset0:144 offset1:216\n"
  "v_mfma_f32_16x16x32_bf16 v[4:7], v[64:67], a[100:103], v[4:7]\n"
  "s_add_u32 m0, s59, 0\n"
  "v_mov_b32 v81, 0\n"
  "v_mfma_f32_16x16x32_bf16 v[4:7], v[68:71], a[124:127], v[4:7]\n"
  "buffer_load_dwordx4 v114, s[40:43], s57 offen offset:0  lds\n"
  "v_mfma_f32_16x16x32_bf16 v[4:7], v[72:75], a[148:151], v[4:7]\n"
  "ds_read_b64_tr_b16 v[100:101], v108 offset:4608\n"
  "ds_read_b64_tr_b16 v[102:103], v108 offset:4640\n"
  "v_mfma_f32_16x16x32_bf16 v[4:7], v[76:79], a[172:175], v[4:7]\n"
  "ds_write_b64 v106, v[24:25] offset:0\n"
  "v_mfma_f32_16x16x32_bf16 v[0:3], v[76:79], a[168:171], v[0:3]\n"
  "v_xor_b32 v108, 0x2000, v108\n"
  "v_mfma_f32_16x16x32_bf16 v[0:3], v[72:75], a[144:147], v[0:3]\n"
  "v_mfma_f32_16x16x32_bf16 v[0:3], v[68:71], a[120:123], v[0:3]\n"
  "v_mfma_f32_16x16x32_bf16 v[0:3], v[64:67], a[96:99], v[0:3]\n"
  "s_waitcnt lgkmcnt(5)\n"
  "buffer_load_dwordx4 v115, s[40:43], s57 offen offset:1024  lds\n"
  "v_mfma_f32_16x16x32_bf16 v[0:3], v[48:51], a[192:195], v[0:3]\n"
  "ds_read_b128 v[64:67], v105 offset:4096\n"
  "v_mfma_f32_16x16x32_bf16 v[0:3], v[52:55], a[216:219], v[0:3]\n"
  "v_mfma_f32_16x16x32_bf16 v[0:3], v[56:59], a[240:243], v[0:3]\n"
  "ds_read_b128 v[68:71], v105 offset:5120\n"
  "v_mfma_f32_16x16x32_bf16 v[0:3], v[60:63], v[136:139], v[0:3]\n"
  "s_waitcnt lgkmcnt(3)\n"
  "buffer_store_dwordx4 v[100:103], v109, s[48:51], s68 offen offset:256 nt\n"
  "ds_write_b64 v106, v[28:29] offset:288\n"
  "v_mfma_f32_16x16x32_bf16 v[4:7], v[60:63], v[140:143], v[4:7]\n"
  "s_add_u32 s68, s68, s69\n"
  "v_mfma_f32_16x16x32_bf16 v[4:7], v[56:59], a[244:247], v[4:7]\n"
  "s_mov_b32 s69, s70\n"
  "buffer_load_dwordx4 v116, s[40:43], s57 offen offset:2048  lds\n"
  "v_mfma_f32_16x16x32_bf16 v[4:7], v[52:55], a[220:223], v[4:7]\n"
  "s_add_u32 s57, s57, 6144\n"
  "ds_read_b128 v[72:75], v105 offset:6144\n"
  "v_mfma_f32_16x16x32_bf16 v[4:7], v[48:51], a[196:199], v[4:7]\n"
  "ds_write_b64 v106, v[26:27] offset:576\n"
  "v_mfma_f32_16x16x32_bf16 v[8:11], v[48:51], a[200:203], v[8:11]\n"
  "s_movk_i32 s70, 6144\n"
  "v_mfma_f32_16x16x32_bf16 v[8:11], v[52:55], a[224:227], v[8:11]\n"
  "v_mfma_f32_16x16x32_bf16 v[8:11], v[56:59], a[248:251], v[8:11]\n"
  "ds_read_b128 v[76:79], v105 offset:7168\n"
  "v_xor_b32 v105, 0x4000, v105\n"
  "v_mfma_f32_16x16x32_bf16 v[8:11], v[60:63], v[144:147], v[8:11]\n"
  "ds_write_b64 v106, v[30:31] offset:864\n"
  "v_mfma_f32_16x16x32_bf16 v[12:15], v[60:63], v[148:151], v[12:15]\n"
  "v_mfma_f32_16x16x32_bf16 v[12:15], v[56:59], a[252:255], v[12:15]\n"
  "v_mfma_f32_16x16x32_bf16 v[12:15], v[52:55], a[228:231], v[12:15]\n"
  "v_mfma_f32_16x16x32_bf16 v[12:15], v[48:51], a[204:207], v[12:15]\n"
  "ds_write_b32 v110, v44\n"
  "v_mfma_f32_16x16x32_bf16 v[16:19], v[48:51], a[208:211], v[16:19]\n"
  "v_mfma_f32_16x16x32_bf16 v[16:19], v[52:55], a[232:235], v[16:19]\n"
  "v_mfma_f32_16x16x32_bf16 v[16:19], v[56:59], v[128:131], v[16:19]\n"
  "v_mfma_f32_16x16x32_bf16 v[16:19], v[60:63], v[152:155], v[16:19]\n"
  "v_xor_b32 v106, 0x2000, v106\n"
  "v_xor_b32 v107, 0x2000, v107\n"
  "v_mfma_f32_16x16x32_bf16 v[20:23], v[60:63], v[156:159], v[20:23]\n"
  "v_xor_b32 v110, 0x2000, v110\n"
  "v_mfma_f32_16x16x32_bf16 v[20:23], v[56:59], v[132:135], v[20:23]\n"
  "v_mfma_f32_16x16x32_bf16 v[20:23], v[52:55], a[236:239], v[20:23]\n"
  "v_mfma_f32_16x16x32_bf16 v[20:23], v[48:51], a[212:215], v[20:23]\n"
  "s_waitcnt vmcnt(43) lgkmcnt(0)\n"
  "s_barrier\n"
  "v_mfma_f32_16x16x32_bf16 v[20:23], v[64:67], v[180:183], v[20:23]\n"
  "s_add_u32 m0, s66, 0\n"
  "ds_read_b128 v[48:51], v104 offset:0\n"
  "ds_read_b128 v[84:87], v111\n"
  "v_mfma_f32_16x16x32_bf16 v[20:23], v[68:71], v[204:207], v[20:23]\n"
  "ds_read_b128 v[100:103], v111 offset:64\n"
  "v_xor_b32 v111, 0x2000, v111\n"
  "v_mfma_f32_16x16x32_bf16 v[20:23], v[72:75], v[228:231], v[20:23]\n"
  "ds_read_b128 v[52:55], v104 offset:1024\n"
  "v_mfma_f32_16x16x32_bf16 v[44:47], v[76:79], v[252:255], v[20:23]\n"
  "buffer_load_dwordx4 v112, s[44:47], s67 offen offset:0  lds\n"
  "v_mfma_f32_16x16x32_bf16 v[16:19], v[76:79], v[248:251], v[16:19]\n"
  "s_add_u32 m0, s66, 1920\n"
  "v_mfma_f32_16x16x32_bf16 v[16:19], v[72:75], v[224:227], v[16:19]\n"
  "v_mfma_f32_16x16x32_bf16 v[16:19], v[68:71], v[200:203], v[16:19]\n"
  "ds_read_b128 v[56:59], v104 offset:2048\n"
  "v_mfma_f32_16x16x32_bf16 v[40:43], v[64:67], v[176:179], v[16:19]\n"
  "buffer_load_dwordx4 v112, s[44:47], s67 offen offset:128  lds\n"
  "v_mfma_f32_16x16x32_bf16 v[12:15], v[64:67], v[172:175], v[12:15]\n"
  "s_add_u32 m0, s66, 3840\n"
  "v_mfma_f32_16x16x32_bf16 v[12:15], v[68:71], v[196:199], v[12:15]\n"
  "v_mfma_f32_16x16x32_bf16 v[12:15], v[72:75], v[220:223], v[12:15]\n"
  "s_waitcnt lgkmcnt(2)\n"
  "ds_read_b128 v[60:63], v104 offset:3072\n"
  "v_add_f32 v84, v84, v100\n"
  "v_mfma_f32_16x16x32_bf16 v[36:39], v[76:79], v[244:247], v[12:15]\n"
  "buffer_load_dwordx4 v112, s[44:47], s67 offen offset:256  lds\n"
  "v_mfma_f32_16x16x32_bf16 v[8:11], v[76:79], v[240:243], v[8:11]\n"
  "v_add_f32 v85, v85, v101\n"
  "v_add_f32 v86, v86, v102\n"
  "v_mfma_f32_16x16x32_bf16 v[8:11], v[72:75], v[216:219], v[8:11]\n"
  "v_add_f32 v87, v87, v103\n"
  "v_max3_f32 v123, v123, v84, v85\n"
  "v_mfma_f32_16x16x32_bf16 v[8:11], v[68:71], v[192:195], v[8:11]\n"
  "v_rcp_f32 v84, v84\n"
  "v_mfma_f32_16x16x32_bf16 v[32:35], v[64:67], v[168:171], v[8:11]\n"
  "v_rcp_f32 v85, v85\n"
  "v_mfma_f32_16x16x32_bf16 v[4:7], v[64:67], v[164:167], v[4:7]\n"
  "v_max3_f32 v123, v123, v86, v87\n"
  "v_mul_f32 v40, v40, v84\n"
  "v_mfma_f32_16x16x32_bf16 v[4:7], v[68:71], v[188:191], v[4:7]\n"
  "v_rcp_f32 v86, v86\n"
  "v_mfma_f32_16x16x32_bf16 v[4:7], v[72:75], v[212:215], v[4:7]\n"
  "v_rcp_f32 v87, v87\n"
  "v_mfma_f32_16x16x32_bf16 v[28:31], v[76:79], v[236:239], v[4:7]\n"
  "v_mul_f32 v44, v44, v84\n"
  "v_cvt_pk_bf16_f32 v40, v40, v44\n"
  "v_mfma_f32_16x16x32_bf16 v[0:3], v[76:79], v[232:235], v[0:3]\n"
  "s_add_u32 m0, s59, 3072\n"
  "v_mul_f32 v41, v41, v85\n"
  "v_mul_f32 v45, v45, v85\n"
  "v_mfma_f32_16x16x32_bf16 v[0:3], v[72:75], v[208:211], v[0:3]\n"
  "buffer_load_dwordx4 v114, s[40:43], s57 offen offset:0  lds\n"
  "v_mfma_f32_16x16x32_bf16 v[0:3], v[68:71], v[184:187], v[0:3]\n"
  "v_cvt_pk_bf16_f32 v41, v41, v45\n"
  "v_mul_f32 v42, v42, v86\n"
  "v_mfma_f32_16x16x32_bf16 v[24:27], v[64:67], v[160:163], v[0:3]\n"
  "s_waitcnt lgkmcnt(0)\n"
  "v_mul_f32 v46, v46, v86\n"
  "v_cvt_pk_bf16_f32 v42, v42, v46\n"
  "v_mfma_f32_16x16x32_bf16 v[0:3], v[48:51], a[0:3], 0\n"
  "ds_read_b128 v[64:67], v104 offset:4096\n"
  "v_mul_f32 v43, v43, v87\n"
  "v_mfma_f32_16x16x32_bf16 v[0:3], v[52:55], a[24:27], v[0:3]\n"
  "v_mul_f32 v47, v47, v87\n"
  "v_cvt_pk_bf16_f32 v43, v43, v47\n"
  "v_mfma_f32_16x16x32_bf16 v[0:3], v[56:59], a[48:51], v[0:3]\n"
  "ds_read_b128 v[68:71], v104 offset:5120\n"
  "v_mul_f32 v24, v24, v84\n"
  "v_mfma_f32_16x16x32_bf16 v[0:3], v[60:63], a[72:75], v[0:3]\n"
  "ds_read_b64_tr_b16 v[92:93], v108 offset:0\n"
  "ds_read_b64_tr_b16 v[94:95], v108 offset:32\n"
  "v_mfma_f32_16x16x32_bf16 v[4:7], v[60:63], a[76:79], 0\n"
  "v_mul_f32 v25, v25, v85\n"
  "v_mul_f32 v28, v28, v84\n"
  "v_mfma_f32_16x16x32_bf16 v[4:7], v[56:59], a[52:55], v[4:7]\n"
  "buffer_load_dwordx4 v115, s[40:43], s57 offen offset:1024  lds\n"
  "v_mfma_f32_16x16x32_bf16 v[4:7], v[52:55], a[28:31], v[4:7]\n"
  "ds_read_b128 v[72:75], v104 offset:6144\n"
  "v_mul_f32 v29, v29, v85\n"
  "v_mfma_f32_16x16x32_bf16 v[4:7], v[48:51], a[4:7], v[4:7]\n"
  "v_mul_f32 v32, v32, v84\n"
  "v_mul_f32 v33, v33, v85\n"
  "v_mfma_f32_16x16x32_bf16 v[8:11], v[48:51], a[8:11], 0\n"
  "v_mul_f32 v36, v36, v84\n"
  "v_mul_f32 v37, v37, v85\n"
  "v_mfma_f32_16x16x32_bf16 v[8:11], v[52:55], a[32:35], v[8:11]\n"
  "v_cvt_pk_bf16_f32 v24, v24, v28\n"
  "v_cvt_pk_bf16_f32 v28, v25, v29\n"
  "v_mfma_f32_16x16x32_bf16 v[8:11], v[56:59], a[56:59], v[8:11]\n"
  "ds_read_b128 v[76:79], v104 offset:7168\n"
  "v_cvt_pk_bf16_f32 v25, v32, v36\n"
  "v_mfma_f32_16x16x32_bf16 v[8:11], v[60:63], a[80:83], v[8:11]\n"
  "v_cvt_pk_bf16_f32 v29, v33, v37\n"
  "v_mul_f32 v26, v26, v86\n"
  "v_smfmac_f32_16x16x64_bf16 v[80:83], v[88:91], v[48:55], v124\n"
  "v_mul_f32 v27, v27, v87\n"
  "v_smfmac_f32_16x16x64_bf16 v[80:83], v[88:91], v[56:63], v124\n"
  "buffer_load_dwordx4 v116, s[40:43], s57 offen offset:2048  lds\n"
  "v_mfma_f32_16x16x32_bf16 v[12:15], v[60:63], a[84:87], 0\n"
  "s_add_u32 s57, s57, 6144\n"
  "v_mul_f32 v30, v30, v86\n"
  "v_mul_f32 v31, v31, v87\n"
  "v_mfma_f32_16x16x32_bf16 v[12:15], v[56:59], a[60:63], v[12:15]\n"
  "v_mul_f32 v34, v34, v86\n"
  "v_mul_f32 v35, v35, v87\n"
  "v_mfma_f32_16x16x32_bf16 v[12:15], v[52:55], a[36:39], v[12:15]\n"
  "v_mul_f32 v38, v38, v86\n"
  "v_mul_f32 v39, v39, v87\n"
  "v_mfma_f32_16x16x32_bf16 v[12:15], v[48:51], a[12:15], v[12:15]\n"
  "s_waitcnt lgkmcnt(2)\n"
  "buffer_store_dwordx4 v[92:95], v109, s[48:51], s68 offen offset:0 nt\n"
  "ds_write2_b32 v107, v40, v41 offset0:0 offset1:72\n"
  "v_mfma_f32_16x16x32_bf16 v[16:19], v[48:51], a[16:19], 0\n"
  "v_cvt_pk_bf16_f32 v26, v26, v30\n"
  "v_cvt_pk_bf16_f32 v30, v27, v31\n"
  "v_mfma_f32_16x16x32_bf16 v[16:19], v[52:55], a[40:43], v[16:19]\n"
  "v_cvt_pk_bf16_f32 v27, v34, v38\n"
  "v_cvt_pk_bf16_f32 v31, v35, v39\n"
  "v_mfma_f32_16x16x32_bf16 v[16:19], v[56:59], a[64:67], v[16:19]\n"
  "s_add_u32 m0, s59, 6144\n"
  "v_max3_f32 v123, v123, v84, v85\n"
  "v_max3_f32 v123, v123, v86, v87\n"
  "v_mfma_f32_16x16x32_bf16 v[16:19], v[60:63], a[88:91], v[16:19]\n"
  "buffer_load_dwordx4 v114, s[40:43], s57 offen offset:0  lds\n"
  "v_mfma_f32_16x16x32_bf16 v[20:23], v[60:63], a[92:95], 0\n"
  "v_xor_b32 v104, 0x4000, v104\n"
  "v_mfma_f32_16x16x32_bf16 v[20:23], v[56:59], a[68:71], v[20:23]\n"
  "v_mfma_f32_16x16x32_bf16 v[20:23], v[52:55], a[44:47], v[20:23]\n"
  "v_mfma_f32_16x16x32_bf16 v[20:23], v[48:51], a[20:23], v[20:23]\n"
  "s_waitcnt lgkmcnt(1)\n"
  "v_mfma_f32_16x16x32_bf16 v[20:23], v[64:67], a[116:119], v[20:23]\n"
  "s_add_u32 s66, s66, 6144\n"
  "ds_read_b128 v[48:51], v105 offset:0\n"
  "v_mfma_f32_16x16x32_bf16 v[20:23], v[68:71], a[140:143], v[20:23]\n"
  "s_cmp_ge_u32 s66, 86016\n"
  "s_cselect_b32 s64, 36864, 0\n"
  "v_mfma_f32_16x16x32_bf16 v[20:23], v[72:75], a[164:167], v[20:23]\n"
  "s_sub_u32 s66, s66, s64\n"
  "ds_read_b128 v[52:55], v105 offset:1024\n"
  "v_mfma_f32_16x16x32_bf16 v[20:23], v[76:79], a[188:191], v[20:23]\n"
  "s_sub_u32 s90, s90, 1\n"
  "s_cselect_b32 s63, s91, 6144\n"
  "s_cmov_b64 s[90:91], s[92:93]\n"
  "s_cmov_b64 s[92:93], s[94:95]\n"
  "buffer_load_dwordx4 v115, s[40:43], s57 offen offset:1024  lds\n"
  "v_mfma_f32_16x16x32_bf16 v[16:19], v[76:79], a[184:187], v[16:19]\n"
  "s_add_u32 s67, s67, s63\n"
  "ds_read_b64_tr_b16 v[96:97], v108 offset:2304\n"
  "ds_read_b64_tr_b16 v[98:99], v108 offset:2336\n"
  "v_mfma_f32_16x16x32_bf16 v[16:19], v[72:75], a[160:163], v[16:19]\n"
  "v_mfma_f32_16x16x32_bf16 v[16:19], v[68:71], a[136:139], v[16:19]\n"
  "ds_read_b128 v[56:59], v105 offset:2048\n"
  "v_mfma_f32_16x16x32_bf16 v[16:19], v[64:67], a[112:115], v[16:19]\n"
  "ds_write2_b32 v107, v42, v43 offset0:144 offset1:216\n"
  "v_mfma_f32_16x16x32_bf16 v[12:15], v[64:67], a[108:111], v[12:15]\n"
  "v_mfma_f32_16x16x32_bf16 v[12:15], v[68:71], a[132:135], v[12:15]\n"
  "v_mfma_f32_16x16x32_bf16 v[12:15], v[72:75], a[156:159], v[12:15]\n"
  "ds_read_b128 v[60:63], v105 offset:3072\n"
  "v_mfma_f32_16x16x32_bf16 v[12:15], v[76:79], a[180:183], v[12:15]\n"
  "buffer_load_dwordx4 v116, s[40:43], s57 offen offset:2048  lds\n"
  "v_smfmac_f32_16x16x64_bf16 v[80:83], v[88:91], v[64:71], v124\n"
  "s_add_u32 s57, s57, 6144\n"
  "v_smfmac_f32_16x16x64_bf16 v[80:83], v[88:91], v[72:79], v124\n"
  "s_waitcnt lgkmcnt(3)\n"
  "buffer_store_dwordx4 v[96:99], v109, s[48:51], s68 offen offset:128 nt\n"
  "ds_write_b64 v106, v[24:25] offset:0\n"
  "v_mfma_f32_16x16x32_bf16 v[8:11], v[76:79], a[176:179], v[8:11]\n"
  "v_mfma_f32_16x16x32_bf16 v[8:11], v[72:75], a[152:155], v[8:11]\n"
  "v_mfma_f32_16x16x32_bf16 v[8:11], v[68:71], a[128:131], v[8:11]\n"
  "v_add_f32 v44, v80, v81\n"
  "v_mov_b32 v80, 0\n"
  "v_mfma_f32_16x16x32_bf16 v[8:11], v[64:67], a[104:107], v[8:11]\n"
  "ds_write_b64 v106, v[28:29] offset:288\n"
  "v_mfma_f32_16x16x32_bf16 v[4:7], v[64:67], a[100:103], v[4:7]\n"
  "s_add_u32 m0, s59, 9216\n"
  "v_mov_b32 v81, 0\n"
  "v_mfma_f32_16x16x32_bf16 v[4:7], v[68:71], a[124:127], v[4:7]\n"
  "buffer_load_dwordx4 v114, s[40:43], s57 offen offset:0  lds\n"
  "v_mfma_f32_16x16x32_bf16 v[4:7], v[72:75], a[148:151], v[4:7]\n"
  "ds_read_b64_tr_b16 v[100:101], v108 offset:4608\n"
  "ds_read_b64_tr_b16 v[102:103], v108 offset:4640\n"
  "v_mfma_f32_16x16x32_bf16 v[4:7], v[76:79], a[172:175], v[4:7]\n"
  "ds_write_b64 v106, v[26:27] offset:576\n"
  "v_mfma_f32_16x16x32_bf16 v[0:3], v[76:79], a[168:171], v[0:3]\n"
  "v_xor_b32 v108, 0x2000, v108\n"
  "v_mfma_f32_16x16x32_bf16 v[0:3], v[72:75], a[144:147], v[0:3]\n"
  "v_mfma_f32_16x16x32_bf16 v[0:3], v[68:71], a[120:123], v[0:3]\n"
  "v_mfma_f32_16x16x32_bf16 v[0:3], v[64:67], a[96:99], v[0:3]\n"
  "s_waitcnt lgkmcnt(5)\n"
  "v_mfma_f32_16x16x32_bf16 v[0:3], v[48:51], a[192:195], v[0:3]\n"
  "ds_read_b128 v[64:67], v105 offset:4096\n"
  "v_mfma_f32_16x16x32_bf16 v[0:3], v[52:55], a[216:219], v[0:3]\n"
  "buffer_load_dwordx4 v115, s[40:43], s57 offen offset:1024  lds\n"
  "v_mfma_f32_16x16x32_bf16 v[0:3], v[56:59], a[240:243], v[0:3]\n"
  "ds_read_b128 v[68:71], v105 offset:5120\n"
  "v_mfma_f32_16x16x32_bf16 v[0:3], v[60:63], v[136:139], v[0:3]\n"
  "s_waitcnt lgkmcnt(3)\n"
  "buffer_store_dwordx4 v[100:103], v109, s[48:51], s68 offen offset:256 nt\n"
  "ds_write_b64 v106, v[30:31] offset:864\n"
  "v_mfma_f32_16x16x32_bf16 v[4:7], v[60:63], v[140:143], v[4:7]\n"
  "s_add_u32 s68, s68, s69\n"
  "v_mfma_f32_16x16x32_bf16 v[4:7], v[56:59], a[244:247], v[4:7]\n"
  "s_mov_b32 s69, s70\n"
  "v_mfma_f32_16x16x32_bf16 v[4:7], v[52:55], a[220:223], v[4:7]\n"
  "s_movk_i32 s70, 6144\n"
  "ds_read_b128 v[72:75], v105 offset:6144\n"
  "v_mfma_f32_16x16x32_bf16 v[4:7], v[48:51], a[196:199], v[4:7]\n"
  "ds_write_b32 v110, v44\n"
  "v_mfma_f32_16x16x32_bf16 v[8:11], v[48:51], a[200:203], v[8:11]\n"
  "v_mfma_f32_16x16x32_bf16 v[8:11], v[52:55], a[224:227], v[8:11]\n"
  "buffer_load_dwordx4 v116, s[40:43], s57 offen offset:2048  lds\n"
  "v_mfma_f32_16x16x32_bf16 v[8:11], v[56:59], a[248:251], v[8:11]\n"
  "s_add_u32 s57, s57, 6144\n"
  "ds_read_b128 v[76:79], v105 offset:7168\n"
  "v_xor_b32 v105, 0x4000, v105\n"
  "v_mfma_f32_16x16x32_bf16 v[8:11], v[60:63], v[144:147], v[8:11]\n"
  "v_mfma_f32_16x16x32_bf16 v[12:15], v[60:63], v[148:151], v[12:15]\n"
  "v_mfma_f32_16x16x32_bf16 v[12:15], v[56:59], a[252:255], v[12:15]\n"
  "v_mfma_f32_16x16x32_bf16 v[12:15], v[52:55], a[228:231], v[12:15]\n"
  "v_mfma_f32_16x16x32_bf16 v[12:15], v[48:51], a[204:207], v[12:15]\n"
  "v_mfma_f32_16x16x32_bf16 v[16:19], v[48:51], a[208:211], v[16:19]\n"
  "v_mfma_f32_16x16x32_bf16 v[16:19], v[52:55], a[232:235], v[16:19]\n"
  "v_mfma_f32_16x16x32_bf16 v[16:19], v[56:59], v[128:131], v[16:19]\n"
  "v_mfma_f32_16x16x32_bf16 v[16:19], v[60:63], v[152:155], v[16:19]\n"
  "v_xor_b32 v106, 0x2000, v106\n"
  "v_xor_b32 v107, 0x2000, v107\n"
  "v_mfma_f32_16x16x32_bf16 v[20:23], v[60:63], v[156:159], v[20:23]\n"
  "v_xor_b32 v110, 0x2000, v110\n"
  "v_mfma_f32_16x16x32_bf16 v[20:23], v[56:59], v[132:135], v[20:23]\n"
  "v_mfma_f32_16x16x32_bf16 v[20:23], v[52:55], a[236:239], v[20:23]\n"
  "v_mfma_f32_16x16x32_bf16 v[20:23], v[48:51], a[212:215], v[20:23]\n"
  "s_waitcnt vmcnt(20) lgkmcnt(0)\n"
  "s_barrier\n"
  "v_mfma_f32_16x16x32_bf16 v[20:23], v[64:67], v[180:183], v[20:23]\n"
  "s_add_u32 m0, s66, 0\n"
  "ds_read_b128 v[48:51], v104 offset:0\n"
  "ds_read_b128 v[84:87], v111\n"
  "v_mfma_f32_16x16x32_bf16 v[20:23], v[68:71], v[204:207], v[20:23]\n"
  "ds_read_b128 v[100:103], v111 offset:64\n"
  "v_xor_b32 v111, 0x2000, v111\n"
  "v_mfma_f32_16x16x32_bf16 v[20:23], v[72:75], v[228:231], v[20:23]\n"
  "ds_read_b128 v[52:55], v104 offset:1024\n"
  "v_mfma_f32_16x16x32_bf16 v[44:47], v[76:79], v[252:255], v[20:23]\n"
  "buffer_load_dwordx4 v112, s[44:47], s67 offen offset:0  lds\n"
  "v_mfma_f32_16x16x32_bf16 v[16:19], v[76:79], v[248:251], v[16:19]\n"
  "s_add_u32 m0, s66, 1920\n"
  "v_mfma_f32_16x16x32_bf16 v[16:19], v[72:75], v[224:227], v[16:19]\n"
  "v_mfma_f32_16x16x32_bf16 v[16:19], v[68:71], v[200:203], v[16:19]\n"
  "ds_read_b128 v[56:59], v104 offset:2048\n"
  "v_mfma_f32_16x16x32_bf16 v[40:43], v[64:67], v[176:179], v[16:19]\n"
  "buffer_load_dwordx4 v112, s[44:47], s67 offen offset:128  lds\n"
  "v_mfma_f32_16x16x32_bf16 v[12:15], v[64:67], v[172:175], v[12:15]\n"
  "s_add_u32 m0, s66, 3840\n"
  "v_mfma_f32_16x16x32_bf16 v[12:15], v[68:71], v[196:199], v[12:15]\n"
  "v_mfma_f32_16x16x32_bf16 v[12:15], v[72:75], v[220:223], v[12:15]\n"
  "s_waitcnt lgkmcnt(2)\n"
  "ds_read_b128 v[60:63], v104 offset:3072\n"
  "v_add_f32 v84, v84, v100\n"
  "v_mfma_f32_16x16x32_bf16 v[36:39], v[76:79], v[244:247], v[12:15]\n"
  "buffer_load_dwordx4 v112, s[44:47], s67 offen offset:256  lds\n"
  "v_mfma_f32_16x16x32_bf16 v[8:11], v[76:79], v[240:243], v[8:11]\n"
  "v_add_f32 v85, v85, v101\n"
  "v_add_f32 v86, v86, v102\n"
  "v_mfma_f32_16x16x32_bf16 v[8:11], v[72:75], v[216:219], v[8:11]\n"
  "v_add_f32 v87, v87, v103\n"
  "v_max3_f32 v123, v123, v84, v85\n"
  "v_mfma_f32_16x16x32_bf16 v[8:11], v[68:71], v[192:195], v[8:11]\n"
  "s_add_u32 m0, s59, 12288\n"
  "v_rcp_f32 v84, v84\n"
  "v_mfma_f32_16x16x32_bf16 v[32:35], v[64:67], v[168:171], v[8:11]\n"
  "buffer_load_dwordx4 v114, s[40:43], s57 offen offset:0  lds\n"
  "v_mfma_f32_16x16x32_bf16 v[4:7], v[64:67], v[164:167], v[4:7]\n"
  "v_rcp_f32 v85, v85\n"
  "v_mfma_f32_16x16x32_bf16 v[4:7], v[68:71], v[188:191], v[4:7]\n"
  "v_max3_f32 v123, v123, v86, v87\n"
  "v_mul_f32 v40, v40, v84\n"
  "v_mfma_f32_16x16x32_bf16 v[4:7], v[72:75], v[212:215], v[4:7]\n"
  "v_rcp_f32 v86, v86\n"
  "v_mfma_f32_16x16x32_bf16 v[28:31], v[76:79], v[236:239], v[4:7]\n"
  "buffer_load_dwordx4 v115, s[40:43], s57 offen offset:1024  lds\n"
  "v_mfma_f32_16x16x32_bf16 v[0:3], v[76:79], v[232:235], v[0:3]\n"
  "v_rcp_f32 v87, v87\n"
  "v_mfma_f32_16x16x32_bf16 v[0:3], v[72:75], v[208:211], v[0:3]\n"
  "v_mul_f32 v44, v44, v84\n"
  "v_cvt_pk_bf16_f32 v40, v40, v44\n"
  "v_mfma_f32_16x16x32_bf16 v[0:3], v[68:71], v[184:187], v[0:3]\n"
  "v_mul_f32 v41, v41, v85\n"
  "v_mul_f32 v45, v45, v85\n"
  "v_mfma_f32_16x16x32_bf16 v[24:27], v[64:67], v[160:163], v[0:3]\n"
  "s_waitcnt lgkmcnt(0)\n"
  "buffer_load_dwordx4 v116, s[40:43], s57 offen offset:2048  lds\n"
  "v_mfma_f32_16x16x32_bf16 v[0:3], v[48:51], a[0:3], 0\n"
  "s_add_u32 s57, s57, 6144\n"
  "ds_read_b128 v[64:67], v104 offset:4096\n"
  "v_cvt_pk_bf16_f32 v41, v41, v45\n"
  "v_mfma_f32_16x16x32_bf16 v[0:3], v[52:55], a[24:27], v[0:3]\n"
  "v_mul_f32 v42, v42, v86\n"
  "v_mul_f32 v46, v46, v86\n"
  "v_mfma_f32_16x16x32_bf16 v[0:3], v[56:59], a[48:51], v[0:3]\n"
  "s_add_u32 m0, s59, 15360\n"
  "ds_read_b128 v[68:71], v104 offset:5120\n"
  "v_cvt_pk_bf16_f32 v42, v42, v46\n"
  "v_mfma_f32_16x16x32_bf16 v[0:3], v[60:63], a[72:75], v[0:3]\n"
  "buffer_load_dwordx4 v114, s[40:43], s57 offen offset:0  lds\n"
  "v_mfma_f32_16x16x32_bf16 v[4:7], v[60:63], a[76:79], 0\n"
  "ds_read_b64_tr_b16 v[92:93], v108 offset:0\n"
  "ds_read_b64_tr_b16 v[94:95], v108 offset:32\n"
  "v_mfma_f32_16x16x32_bf16 v[4:7], v[56:59], a[52:55], v[4:7]\n"
  "v_mul_f32 v43, v43, v87\n"
  "v_mul_f32 v47, v47, v87\n"
  "v_mfma_f32_16x16x32_bf16 v[4:7], v[52:55], a[28:31], v[4:7]\n"
  "ds_read_b128 v[72:75], v104 offset:6144\n"
  "v_cvt_pk_bf16_f32 v43, v43, v47\n"
  "v_mfma_f32_16x16x32_bf16 v[4:7], v[48:51], a[4:7], v[4:7]\n"
  "buffer_load_dwordx4 v115, s[40:43], s57 offen offset:1024  lds\n"
  "v_mfma_f32_16x16x32_bf16 v[8:11], v[48:51], a[8:11], 0\n"
  "v_mul_f32 v24, v24, v84\n"
  "v_mul_f32 v25, v25, v85\n"
  "v_mfma_f32_16x16x32_bf16 v[8:11], v[52:55], a[32:35], v[8:11]\n"
  "v_mul_f32 v28, v28, v84\n"
  "v_mul_f32 v29, v29, v85\n"
  "v_mfma_f32_16x16x32_bf16 v[8:11], v[56:59], a[56:59], v[8:11]\n"
  "ds_read_b128 v[76:79], v104 offset:7168\n"
  "v_mul_f32 v32, v32, v84\n"
  "v_mfma_f32_16x16x32_bf16 v[8:11], v[60:63], a[80:83], v[8:11]\n"
  "buffer_load_dwordx4 v116, s[40:43], s57 offen offset:2048  lds\n"
  "v_smfmac_f32_16x16x64_bf16 v[80:83], v[88:91], v[48:55], v124\n"
  "s_add_u32 s57, s57, 6144\n"
  "v_mul_f32 v33, v33, v85\n"
  "v_smfmac_f32_16x16x64_bf16 v[80:83], v[88:91], v[56:63], v124\n"
  "s_waitcnt lgkmcnt(2)\n"
  "buffer_store_dwordx4 v[92:95], v109, s[48:51], s68 offen offset:0 nt\n"
  "v_mul_f32 v36, v36, v84\n"
  "v_mul_f32 v37, v37, v85\n"
  "v_mfma_f32_16x16x32_bf16 v[12:15], v[60:63], a[84:87], 0\n"
  "v_cvt_pk_bf16_f32 v24, v24, v28\n"
  "v_cvt_pk_bf16_f32 v28, v25, v29\n"
  "v_mfma_f32_16x16x32_bf16 v[12:15], v[56:59], a[60:63], v[12:15]\n"
  "v_cvt_pk_bf16_f32 v25, v32, v36\n"
  "v_cvt_pk_bf16_f32 v29, v33, v37\n"
  "v_mfma_f32_16x16x32_bf16 v[12:15], v[52:55], a[36:39], v[12:15]\n"
  "v_mul_f32 v26, v26, v86\n"
  "v_mul_f32 v27, v27, v87\n"
  "v_mfma_f32_16x16x32_bf16 v[12:15], v[48:51], a[12:15], v[12:15]\n"
  "v_mul_f32 v30, v30, v86\n"
  "v_mul_f32 v31, v31, v87\n"
  "v_mfma_f32_16x16x32_bf16 v[16:19], v[48:51], a[16:19], 0\n"
  "v_mul_f32 v34, v34, v86\n"
  "v_mul_f32 v35, v35, v87\n"
  "v_mfma_f32_16x16x32_bf16 v[16:19], v[52:55], a[40:43], v[16:19]\n"
  "v_mul_f32 v38, v38, v86\n"
  "v_mul_f32 v39, v39, v87\n"
  "v_mfma_f32_16x16x32_bf16 v[16:19], v[56:59], a[64:67], v[16:19]\n"
  "v_cvt_pk_bf16_f32 v26, v26, v30\n"
  "v_cvt_pk_bf16_f32 v30, v27, v31\n"
  "v_mfma_f32_16x16x32_bf16 v[16:19], v[60:63], a[88:91], v[16:19]\n"
  "v_cvt_pk_bf16_f32 v27, v34, v38\n"
  "v_cvt_pk_bf16_f32 v31, v35, v39\n"
  "v_mfma_f32_16x16x32_bf16 v[20:23], v[60:63], a[92:95], 0\n"
  "v_max3_f32 v123, v123, v84, v85\n"
  "v_max3_f32 v123, v123, v86, v87\n"
  "v_mfma_f32_16x16x32_bf16 v[20:23], v[56:59], a[68:71], v[20:23]\n"
  "v_xor_b32 v104, 0x4000, v104\n"
  "v_mfma_f32_16x16x32_bf16 v[20:23], v[52:55], a[44:47], v[20:23]\n"
  "v_mfma_f32_16x16x32_bf16 v[20:23], v[48:51], a[20:23], v[20:23]\n"
  "s_waitcnt lgkmcnt(0)\n"
  "v_mfma_f32_16x16x32_bf16 v[20:23], v[64:67], a[116:119], v[20:23]\n"
  "s_add_u32 s66, s66, 6144\n"
  "ds_read_b128 v[48:51], v105 offset:0\n"
  "v_mfma_f32_16x16x32_bf16 v[20:23], v[68:71], a[140:143], v[20:23]\n"
  "s_cmp_ge_u32 s66, 86016\n"
  "s_cselect_b32 s64, 36864, 0\n"
  "v_mfma_f32_16x16x32_bf16 v[20:23], v[72:75], a[164:167], v[20:23]\n"
  "s_sub_u32 s66, s66, s64\n"
  "ds_read_b128 v[52:55], v105 offset:1024\n"
  "v_mfma_f32_16x16x32_bf16 v[20:23], v[76:79], a[188:191], v[20:23]\n"
  "s_sub_u32 s90, s90, 1\n"
  "s_cselect_b32 s63, s91, 6144\n"
  "s_cmov_b64 s[90:91], s[92:93]\n"
  "s_cmov_b64 s[92:93], s[94:95]\n"
  "ds_read_b64_tr_b16 v[96:97], v108 offset:2304\n"
  "ds_read_b64_tr_b16 v[98:99], v108 offset:2336\n"
  "v_mfma_f32_16x16x32_bf16 v[16:19], v[76:79], a[184:187], v[16:19]\n"
  "s_add_u32 s67, s67, s63\n"
  "v_mfma_f32_16x16x32_bf16 v[16:19], v[72:75], a[160:163], v[16:19]\n"
  "v_mfma_f32_16x16x32_bf16 v[16:19], v[68:71], a[136:139], v[16:19]\n"
  "ds_read_b128 v[56:59], v105 offset:2048\n"
  "v_mfma_f32_16x16x32_bf16 v[16:19], v[64:67], a[112:115], v[16:19]\n"
  "ds_write2_b32 v107, v40, v41 offset0:0 offset1:72\n"
  "v_mfma_f32_16x16x32_bf16 v[12:15], v[64:67], a[108:111], v[12:15]\n"
  "v_mfma_f32_16x16x32_bf16 v[12:15], v[68:71], a[132:135], v[12:15]\n"
  "v_mfma_f32_16x16x32_bf16 v[12:15], v[72:75], a[156:159], v[12:15]\n"
  "ds_read_b128 v[60:63], v105 offset:3072\n"
  "v_mfma_f32_16x16x32_bf16 v[12:15], v[76:79], a[180:183], v[12:15]\n"
  "ds_write2_b32 v107, v42, v43 offset0:144 offset1:216\n"
  "v_smfmac_f32_16x16x64_bf16 v[80:83], v[88:91], v[64:71], v124\n"
  "v_smfmac_f32_16x16x64_bf16 v[80:83], v[88:91], v[72:79], v124\n"
  "s_waitcnt lgkmcnt(4)\n"
  "buffer_store_dwordx4 v[96:99], v109, s[48:51], s68 offen offset:128 nt\n"
  "ds_write_b64 v106, v[24:25] offset:0\n"
  "v_mfma_f32_16x16x32_bf16 v[8:11], v[76:79], a[176:179], v[8:11]\n"
  "v_mfma_f32_16x16x32_bf16 v[8:11], v[72:75], a[152:155], v[8:11]\n"
  "v_mfma_f32_16x16x32_bf16 v[8:11], v[68:71], a[128:131], v[8:11]\n"
  "v_add_f32 v44, v80, v81\n"
  "v_mov_b32 v80, 0\n"
  "v_mfma_f32_16x16x32_bf16 v[8:11], v[64:67], a[104:107], v[8:11]\n"
  "ds_write_b64 v106, v[28:29] offset:288\n"
  "v_mfma_f32_16x16x32_bf16 v[4:7], v[64:67], a[100:103], v[4:7]\n"
  "v_mov_b32 v81, 0\n"
  "v_mfma_f32_16x16x32_bf16 v[4:7], v[68:71], a[124:127], v[4:7]\n"
  "ds_read_b64_tr_b16 v[100:101], v108 offset:4608\n"
  "ds_read_b64_tr_b16 v[102:103], v108 offset:4640\n"
  "v_mfma_f32_16x16x32_bf16 v[4:7], v[72:75], a[148:151], v[4:7]\n"
  "v_xor_b32 v108, 0x2000, v108\n"
  "v_mfma_f32_16x16x32_bf16 v[4:7], v[76:79], a[172:175], v[4:7]\n"
  "ds_write_b64 v106, v[26:27] offset:576\n"
  "v_mfma_f32_16x16x32_bf16 v[0:3], v[76:79], a[168:171], v[0:3]\n"
  "v_mfma_f32_16x16x32_bf16 v[0:3], v[72:75], a[144:147], v[0:3]\n"
  "v_mfma_f32_16x16x32_bf16 v[0:3], v[68:71], a[120:123], v[0:3]\n"
  "v_mfma_f32_16x16x32_bf16 v[0:3], v[64:67], a[96:99], v[0:3]\n"
  "s_waitcnt lgkmcnt(6)\n"
  "v_mfma_f32_16x16x32_bf16 v[0:3], v[48:51], a[192:195], v[0:3]\n"
  "ds_read_b128 v[64:67], v105 offset:4096\n"
  "v_mfma_f32_16x16x32_bf16 v[0:3], v[52:55], a[216:219], v[0:3]\n"
  "v_mfma_f32_16x16x32_bf16 v[0:3], v[56:59], a[240:243], v[0:3]\n"
  "ds_read_b128 v[68:71], v105 offset:5120\n"
  "v_mfma_f32_16x16x32_bf16 v[0:3], v[60:63], v[136:139], v[0:3]\n"
  "s_waitcnt lgkmcnt(3)\n"
  "buffer_store_dwordx4 v[100:103], v109, s[48:51], s68 offen offset:256 nt\n"
  "ds_write_b64 v106, v[30:31] offset:864\n"
  "v_mfma_f32_16x16x32_bf16 v[4:7], v[60:63], v[140:143], v[4:7]\n"
  "s_add_u32 s68, s68, s69\n"
  "v_mfma_f32_16x16x32_bf16 v[4:7], v[56:59], a[244:247], v[4:7]\n"
  "s_mov_b32 s69, s70\n"
  "v_mfma_f32_16x16x32_bf16 v[4:7], v[52:55], a[220:223], v[4:7]\n"
  "s_movk_i32 s70, 6144\n"
  "ds_read_b128 v[72:75], v105 offset:6144\n"
  "v_mfma_f32_16x16x32_bf16 v[4:7], v[48:51], a[196:199], v[4:7]\n"
  "ds_write_b32 v110, v44\n"
  "v_mfma_f32_16x16x32_bf16 v[8:11], v[48:51], a[200:203], v[8:11]\n"
  "v_mfma_f32_16x16x32_bf16 v[8:11], v[52:55], a[224:227], v[8:11]\n"
  "v_mfma_f32_16x16x32_bf16 v[8:11], v[56:59], a[248:251], v[8:11]\n"
  "ds_read_b128 v[76:79], v105 offset:7168\n"
  "v_xor_b32 v105, 0x4000, v105\n"
  "v_mfma_f32_16x16x32_bf16 v[8:11], v[60:63], v[144:147], v[8:11]\n"
  "v_mfma_f32_16x16x32_bf16 v[12:15], v[60:63], v[148:151], v[12:15]\n"
  "v_mfma_f32_16x16x32_bf16 v[12:15], v[56:59], a[252:255], v[12:15]\n"
  "v_mfma_f32_16x16x32_bf16 v[12:15], v[52:55], a[228:231], v[12:15]\n"
  "v_mfma_f32_16x16x32_bf16 v[12:15], v[48:51], a[204:207], v[12:15]\n"
  "v_mfma_f32_16x16x32_bf16 v[16:19], v[48:51], a[208:211], v[16:19]\n"
  "v_mfma_f32_16x16x32_bf16 v[16:19], v[52:55], a[232:235], v[16:19]\n"
  "v_mfma_f32_16x16x32_bf16 v[16:19], v[56:59], v[128:131], v[16:19]\n"
  "v_mfma_f32_16x16x32_bf16 v[16:19], v[60:63], v[152:155], v[16:19]\n"
  "v_xor_b32 v106, 0x2000, v106\n"
  "v_xor_b32 v107, 0x2000, v107\n"
  "v_mfma_f32_16x16x32_bf16 v[20:23], v[60:63], v[156:159], v[20:23]\n"
  "v_xor_b32 v110, 0x2000, v110\n"
  "v_mfma_f32_16x16x32_bf16 v[20:23], v[56:59], v[132:135], v[20:23]\n"
  "v_mfma_f32_16x16x32_bf16 v[20:23], v[52:55], a[236:239], v[20:23]\n"
  "v_mfma_f32_16x16x32_bf16 v[20:23], v[48:51], a[212:215], v[20:23]\n"
  "s_waitcnt lgkmcnt(0)\n"
  "s_barrier\n"
  "v_mfma_f32_16x16x32_bf16 v[20:23], v[64:67], v[180:183], v[20:23]\n"
  "ds_read_b128 v[48:51], v104 offset:0\n"
  "ds_read_b128 v[84:87], v111\n"
  "v_mfma_f32_16x16x32_bf16 v[20:23], v[68:71], v[204:207], v[20:23]\n"
  "ds_read_b128 v[100:103], v111 offset:64\n"
  "v_xor_b32 v111, 0x2000, v111\n"
  "v_mfma_f32_16x16x32_bf16 v[20:23], v[72:75], v[228:231], v[20:23]\n"
  "ds_read_b128 v[52:55], v104 offset:1024\n"
  "v_mfma_f32_16x16x32_bf16 v[44:47], v[76:79], v[252:255], v[20:23]\n"
  "v_mfma_f32_16x16x32_bf16 v[16:19], v[76:79], v[248:251], v[16:19]\n"
  "v_mfma_f32_16x16x32_bf16 v[16:19], v[72:75], v[224:227], v[16:19]\n"
  "v_mfma_f32_16x16x32_bf16 v[16:19], v[68:71], v[200:203], v[16:19]\n"
  "ds_read_b128 v[56:59], v104 offset:2048\n"
  "v_mfma_f32_16x16x32_bf16 v[40:43], v[64:67], v[176:179], v[16:19]\n"
  "v_mfma_f32_16x16x32_bf16 v[12:15], v[64:67], v[172:175], v[12:15]\n"
  "v_mfma_f32_16x16x32_bf16 v[12:15], v[68:71], v[196:199], v[12:15]\n"
  "v_mfma_f32_16x16x32_bf16 v[12:15], v[72:75], v[220:223], v[12:15]\n"
  "s_waitcnt lgkmcnt(2)\n"
  "ds_read_b128 v[60:63], v104 offset:3072\n"
  "v_add_f32 v84, v84, v100\n"
  "v_mfma_f32_16x16x32_bf16 v[36:39], v[76:79], v[244:247], v[12:15]\n"
  "v_add_f32 v85, v85, v101\n"
  "v_max3_f32 v123, v123, v84, v85\n"
  "v_mfma_f32_16x16x32_bf16 v[8:11], v[76:79], v[240:243], v[8:11]\n"
  "v_rcp_f32 v84, v84\n"
  "v_mfma_f32_16x16x32_bf16 v[8:11], v[72:75], v[216:219], v[8:11]\n"
  "v_rcp_f32 v85, v85\n"
  "v_mfma_f32_16x16x32_bf16 v[8:11], v[68:71], v[192:195], v[8:11]\n"
  "v_mul_f32 v40, v40, v84\n"
  "v_mul_f32 v44, v44, v84\n"
  "v_mfma_f32_16x16x32_bf16 v[32:35], v[64:67], v[168:171], v[8:11]\n"
  "v_cvt_pk_bf16_f32 v40, v40, v44\n"
  "v_mul_f32 v41, v41, v85\n"
  "v_mfma_f32_16x16x32_bf16 v[4:7], v[64:67], v[164:167], v[4:7]\n"
  "v_mul_f32 v45, v45, v85\n"
  "v_cvt_pk_bf16_f32 v41, v41, v45\n"
  "v_mfma_f32_16x16x32_bf16 v[4:7], v[68:71], v[188:191], v[4:7]\n"
  "v_add_f32 v86, v86, v102\n"
  "v_add_f32 v87, v87, v103\n"
  "v_mfma_f32_16x16x32_bf16 v[4:7], v[72:75], v[212:215], v[4:7]\n"
  "v_max3_f32 v123, v123, v86, v87\n"
  "v_mul_f32 v32, v32, v84\n"
  "v_mfma_f32_16x16x32_bf16 v[28:31], v[76:79], v[236:239], v[4:7]\n"
  "v_rcp_f32 v86, v86\n"
  "v_mfma_f32_16x16x32_bf16 v[0:3], v[76:79], v[232:235], v[0:3]\n"
  "v_rcp_f32 v87, v87\n"
  "v_mfma_f32_16x16x32_bf16 v[0:3], v[72:75], v[208:211], v[0:3]\n"
  "v_mul_f32 v42, v42, v86\n"
  "v_mul_f32 v46, v46, v86\n"
  "v_mfma_f32_16x16x32_bf16 v[0:3], v[68:71], v[184:187], v[0:3]\n"
  "v_cvt_pk_bf16_f32 v42, v42, v46\n"
  "v_mul_f32 v43, v43, v87\n"
  "v_mfma_f32_16x16x32_bf16 v[24:27], v[64:67], v[160:163], v[0:3]\n"
  "s_waitcnt lgkmcnt(0)\n"
  "v_mul_f32 v47, v47, v87\n"
  "v_cvt_pk_bf16_f32 v43, v43, v47\n"
  "v_mfma_f32_16x16x32_bf16 v[0:3], v[48:51], a[0:3], 0\n"
  "ds_read_b128 v[64:67], v104 offset:4096\n"
  "v_mul_f32 v28, v28, v84\n"
  "v_mfma_f32_16x16x32_bf16 v[0:3], v[52:55], a[24:27], v[0:3]\n"
  "v_mul_f32 v29, v29, v85\n"
  "v_mul_f32 v36, v36, v84\n"
  "v_mfma_f32_16x16x32_bf16 v[0:3], v[56:59], a[48:51], v[0:3]\n"
  "ds_read_b128 v[68:71], v104 offset:5120\n"
  "v_mul_f32 v24, v24, v84\n"
  "v_mfma_f32_16x16x32_bf16 v[0:3], v[60:63], a[72:75], v[0:3]\n"
  "ds_read_b64_tr_b16 v[92:93], v108 offset:0\n"
  "ds_read_b64_tr_b16 v[94:95], v108 offset:32\n"
  "v_mfma_f32_16x16x32_bf16 v[4:7], v[60:63], a[76:79], 0\n"
  "v_mul_f32 v25, v25, v85\n"
  "v_cvt_pk_bf16_f32 v24, v24, v28\n"
  "v_mfma_f32_16x16x32_bf16 v[4:7], v[56:59], a[52:55], v[4:7]\n"
  "v_cvt_pk_bf16_f32 v28, v25, v29\n"
  "v_cvt_pk_bf16_f32 v25, v32, v36\n"
  "v_mfma_f32_16x16x32_bf16 v[4:7], v[52:55], a[28:31], v[4:7]\n"
  "ds_read_b128 v[72:75], v104 offset:6144\n"
  "v_mul_f32 v33, v33, v85\n"
  "v_mfma_f32_16x16x32_bf16 v[4:7], v[48:51], a[4:7], v[4:7]\n"
  "v_mul_f32 v37, v37, v85\n"
  "v_cvt_pk_bf16_f32 v29, v33, v37\n"
  "v_mfma_f32_16x16x32_bf16 v[8:11], v[48:51], a[8:11], 0\n"
  "v_mul_f32 v26, v26, v86\n"
  "v_mul_f32 v27, v27, v87\n"
  "v_mfma_f32_16x16x32_bf16 v[8:11], v[52:55], a[32:35], v[8:11]\n"
  "v_mul_f32 v30, v30, v86\n"
  "v_mul_f32 v31, v31, v87\n"
  "v_mfma_f32_16x16x32_bf16 v[8:11], v[56:59], a[56:59], v[8:11]\n"
  "ds_read_b128 v[76:79], v104 offset:7168\n"
  "v_mul_f32 v34, v34, v86\n"
  "v_mfma_f32_16x16x32_bf16 v[8:11], v[60:63], a[80:83], v[8:11]\n"
  "v_mul_f32 v38, v38, v86\n"
  "v_cvt_pk_bf16_f32 v26, v26, v30\n"
  "v_smfmac_f32_16x16x64_bf16 v[80:83], v[88:91], v[48:55], v124\n"
  "v_cvt_pk_bf16_f32 v30, v27, v31\n"
  "v_smfmac_f32_16x16x64_bf16 v[80:83], v[88:91], v[56:63], v124\n"
  "s_waitcnt lgkmcnt(2)\n"
  "buffer_store_dwordx4 v[92:95], v109, s[48:51], s68 offen offset:0 nt\n"
  "ds_write2_b32 v107, v40, v41 offset0:0 offset1:72\n"
  "v_mfma_f32_16x16x32_bf16 v[12:15], v[60:63], a[84:87], 0\n"
  "v_cvt_pk_bf16_f32 v27, v34, v38\n"
  "v_mul_f32 v35, v35, v87\n"
  "v_mfma_f32_16x16x32_bf16 v[12:15], v[56:59], a[60:63], v[12:15]\n"
  "v_mul_f32 v39, v39, v87\n"
  "v_cvt_pk_bf16_f32 v31, v35, v39\n"
  "v_mfma_f32_16x16x32_bf16 v[12:15], v[52:55], a[36:39], v[12:15]\n"
  "v_max3_f32 v123, v123, v84, v85\n"
  "v_max3_f32 v123, v123, v86, v87\n"
  "v_mfma_f32_16x16x32_bf16 v[12:15], v[48:51], a[12:15], v[12:15]\n"
  "ds_write2_b32 v107, v42, v43 offset0:144 offset1:216\n"
  "v_mfma_f32_16x16x32_bf16 v[16:19], v[48:51], a[16:19], 0\n"
  "v_xor_b32 v104, 0x4000, v104\n"
  "v_mfma_f32_16x16x32_bf16 v[16:19], v[52:55], a[40:43], v[16:19]\n"
  "v_mfma_f32_16x16x32_bf16 v[16:19], v[56:59], a[64:67], v[16:19]\n"
  "v_mfma_f32_16x16x32_bf16 v[16:19], v[60:63], a[88:91], v[16:19]\n"
  "v_mfma_f32_16x16x32_bf16 v[20:23], v[60:63], a[92:95], 0\n"
  "v_mfma_f32_16x16x32_bf16 v[20:23], v[56:59], a[68:71], v[20:23]\n"
  "v_mfma_f32_16x16x32_bf16 v[20:23], v[52:55], a[44:47], v[20:23]\n"
  "v_mfma_f32_16x16x32_bf16 v[20:23], v[48:51], a[20:23], v[20:23]\n"
  "s_waitcnt lgkmcnt(2)\n"
  "v_mfma_f32_16x16x32_bf16 v[20:23], v[64:67], a[116:119], v[20:23]\n"
  "ds_read_b128 v[48:51], v105 offset:0\n"
  "v_mfma_f32_16x16x32_bf16 v[20:23], v[68:71], a[140:143], v[20:23]\n"
  "v_mfma_f32_16x16x32_bf16 v[20:23], v[72:75], a[164:167], v[20:23]\n"
  "ds_read_b128 v[52:55], v105 offset:1024\n"
  "v_mfma_f32_16x16x32_bf16 v[20:23], v[76:79], a[188:191], v[20:23]\n"
  "ds_write_b64 v106, v[24:25] offset:0\n"
  "v_mfma_f32_16x16x32_bf16 v[16:19], v[76:79], a[184:187], v[16:19]\n"
  "ds_read_b64_tr_b16 v[96:97], v108 offset:2304\n"
  "ds_read_b64_tr_b16 v[98:99], v108 offset:2336\n"
  "v_mfma_f32_16x16x32_bf16 v[16:19], v[72:75], a[160:163], v[16:19]\n"
  "v_mfma_f32_16x16x32_bf16 v[16:19], v[68:71], a[136:139], v[16:19]\n"
  "ds_read_b128 v[56:59], v105 offset:2048\n"
  "v_mfma_f32_16x16x32_bf16 v[16:19], v[64:67], a[112:115], v[16:19]\n"
  "v_mfma_f32_16x16x32_bf16 v[12:15], v[64:67], a[108:111], v[12:15]\n"
  "v_mfma_f32_16x16x32_bf16 v[12:15], v[68:71], a[132:135], v[12:15]\n"
  "v_mfma_f32_16x16x32_bf16 v[12:15], v[72:75], a[156:159], v[12:15]\n"
  "ds_read_b128 v[60:63], v105 offset:3072\n"
  "v_mfma_f32_16x16x32_bf16 v[12:15], v[76:79], a[180:183], v[12:15]\n"
  "v_smfmac_f32_16x16x64_bf16 v[80:83], v[88:91], v[64:71], v124\n"
  "v_smfmac_f32_16x16x64_bf16 v[80:83], v[88:91], v[72:79], v124\n"
  "s_waitcnt lgkmcnt(2)\n"
  "buffer_store_dwordx4 v[96:99], v109, s[48:51], s68 offen offset:128 nt\n"
  "ds_write_b64 v106, v[28:29] offset:288\n"
  "v_mfma_f32_16x16x32_bf16 v[8:11], v[76:79], a[176:179], v[8:11]\n"
  "v_mfma_f32_16x16x32_bf16 v[8:11], v[72:75], a[152:155], v[8:11]\n"
  "v_mfma_f32_16x16x32_bf16 v[8:11], v[68:71], a[128:131], v[8:11]\n"
  "v_add_f32 v44, v80, v81\n"
  "v_mov_b32 v80, 0\n"
  "v_mfma_f32_16x16x32_bf16 v[8:11], v[64:67], a[104:107], v[8:11]\n"
  "ds_write_b32 v110, v44\n"
  "v_mfma_f32_16x16x32_bf16 v[4:7], v[64:67], a[100:103], v[4:7]\n"
  "v_mov_b32 v81, 0\n"
  "v_mfma_f32_16x16x32_bf16 v[4:7], v[68:71], a[124:127], v[4:7]\n"
  "ds_read_b64_tr_b16 v[100:101], v108 offset:4608\n"
  "ds_read_b64_tr_b16 v[102:103], v108 offset:4640\n"
  "v_mfma_f32_16x16x32_bf16 v[4:7], v[72:75], a[148:151], v[4:7]\n"
  "v_xor_b32 v108, 0x2000, v108\n"
  "v_mfma_f32_16x16x32_bf16 v[4:7], v[76:79], a[172:175], v[4:7]\n"
  "v_mfma_f32_16x16x32_bf16 v[0:3], v[76:79], a[168:171], v[0:3]\n"
  "v_mfma_f32_16x16x32_bf16 v[0:3], v[72:75], a[144:147], v[0:3]\n"
  "v_mfma_f32_16x16x32_bf16 v[0:3], v[68:71], a[120:123], v[0:3]\n"
  "v_mfma_f32_16x16x32_bf16 v[0:3], v[64:67], a[96:99], v[0:3]\n"
  "s_waitcnt lgkmcnt(4)\n"
  "v_mfma_f32_16x16x32_bf16 v[0:3], v[48:51], a[192:195], v[0:3]\n"
  "ds_read_b128 v[64:67], v105 offset:4096\n"
  "v_mfma_f32_16x16x32_bf16 v[0:3], v[52:55], a[216:219], v[0:3]\n"
  "v_mfma_f32_16x16x32_bf16 v[0:3], v[56:59], a[240:243], v[0:3]\n"
  "ds_read_b128 v[68:71], v105 offset:5120\n"
  "v_mfma_f32_16x16x32_bf16 v[0:3], v[60:63], v[136:139], v[0:3]\n"
  "s_waitcnt lgkmcnt(2)\n"
  "buffer_store_dwordx4 v[100:103], v109, s[48:51], s68 offen offset:256 nt\n"
  "ds_write_b64 v106, v[26:27] offset:576\n"
  "v_mfma_f32_16x16x32_bf16 v[4:7], v[60:63], v[140:143], v[4:7]\n"
  "s_add_u32 s68, s68, s69\n"
  "v_mfma_f32_16x16x32_bf16 v[4:7], v[56:59], a[244:247], v[4:7]\n"
  "s_mov_b32 s69, s70\n"
  "v_mfma_f32_16x16x32_bf16 v[4:7], v[52:55], a[220:223], v[4:7]\n"
  "s_movk_i32 s70, 6144\n"
  "ds_read_b128 v[72:75], v105 offset:6144\n"
  "v_mfma_f32_16x16x32_bf16 v[4:7], v[48:51], a[196:199], v[4:7]\n"
  "v_mfma_f32_16x16x32_bf16 v[8:11], v[48:51], a[200:203], v[8:11]\n"
  "v_mfma_f32_16x16x32_bf16 v[8:11], v[52:55], a[224:227], v[8:11]\n"
  "v_mfma_f32_16x16x32_bf16 v[8:11], v[56:59], a[248:251], v[8:11]\n"
  "ds_read_b128 v[76:79], v105 offset:7168\n"
  "v_xor_b32 v105, 0x4000, v105\n"
  "v_mfma_f32_16x16x32_bf16 v[8:11], v[60:63], v[144:147], v[8:11]\n"
  "v_mfma_f32_16x16x32_bf16 v[12:15], v[60:63], v[148:151], v[12:15]\n"
  "v_mfma_f32_16x16x32_bf16 v[12:15], v[56:59], a[252:255], v[12:15]\n"
  "v_mfma_f32_16x16x32_bf16 v[12:15], v[52:55], a[228:231], v[12:15]\n"
  "v_mfma_f32_16x16x32_bf16 v[12:15], v[48:51], a[204:207], v[12:15]\n"
  "ds_write_b64 v106, v[30:31] offset:864\n"
  "v_mfma_f32_16x16x32_bf16 v[16:19], v[48:51], a[208:211], v[16:19]\n"
  "v_mfma_f32_16x16x32_bf16 v[16:19], v[52:55], a[232:235], v[16:19]\n"
  "v_mfma_f32_16x16x32_bf16 v[16:19], v[56:59], v[128:131], v[16:19]\n"
  "v_mfma_f32_16x16x32_bf16 v[16:19], v[60:63], v[152:155], v[16:19]\n"
  "v_xor_b32 v106, 0x2000, v106\n"
  "v_xor_b32 v107, 0x2000, v107\n"
  "v_mfma_f32_16x16x32_bf16 v[20:23], v[60:63], v[156:159], v[20:23]\n"
  "v_xor_b32 v110, 0x2000, v110\n"
  "v_mfma_f32_16x16x32_bf16 v[20:23], v[56:59], v[132:135], v[20:23]\n"
  "v_mfma_f32_16x16x32_bf16 v[20:23], v[52:55], a[236:239], v[20:23]\n"
  "v_mfma_f32_16x16x32_bf16 v[20:23], v[48:51], a[212:215], v[20:23]\n"
  "s_waitcnt lgkmcnt(0)\n"
  "s_barrier\n"
  "v_mfma_f32_16x16x32_bf16 v[20:23], v[64:67], v[180:183], v[20:23]\n"
  "ds_read_b128 v[48:51], v104 offset:0\n"
  "ds_read_b128 v[84:87], v111\n"
  "ds_read_b128 v[100:103], v111 offset:64\n"
  "v_mfma_f32_16x16x32_bf16 v[20:23], v[68:71], v[204:207], v[20:23]\n"
  "v_xor_b32 v111, 0x2000, v111\n"
  "v_mfma_f32_16x16x32_bf16 v[20:23], v[72:75], v[228:231], v[20:23]\n"
  "ds_read_b128 v[52:55], v104 offset:1024\n"
  "v_mfma_f32_16x16x32_bf16 v[44:47], v[76:79], v[252:255], v[20:23]\n"
  "v_mfma_f32_16x16x32_bf16 v[16:19], v[76:79], v[248:251], v[16:19]\n"
  "v_mfma_f32_16x16x32_bf16 v[16:19], v[72:75], v[224:227], v[16:19]\n"
  "v_mfma_f32_16x16x32_bf16 v[16:19], v[68:71], v[200:203], v[16:19]\n"
  "ds_read_b128 v[56:59], v104 offset:2048\n"
  "v_mfma_f32_16x16x32_bf16 v[40:43], v[64:67], v[176:179], v[16:19]\n"
  "v_mfma_f32_16x16x32_bf16 v[12:15], v[64:67], v[172:175], v[12:15]\n"
  "v_mfma_f32_16x16x32_bf16 v[12:15], v[68:71], v[196:199], v[12:15]\n"
  "v_mfma_f32_16x16x32_bf16 v[12:15], v[72:75], v[220:223], v[12:15]\n"
  "s_waitcnt lgkmcnt(2)\n"
  "ds_read_b128 v[60:63], v104 offset:3072\n"
  "v_add_f32 v84, v84, v100\n"
  "v_add_f32 v85, v85, v101\n"
  "v_mfma_f32_16x16x32_bf16 v[36:39], v[76:79], v[244:247], v[12:15]\n"
  "v_add_f32 v86, v86, v102\n"
  "v_add_f32 v87, v87, v103\n"
  "v_max3_f32 v123, v123, v84, v85\n"
  "v_mfma_f32_16x16x32_bf16 v[8:11], v[76:79], v[240:243], v[8:11]\n"
  "v_max3_f32 v123, v123, v86, v87\n"
  "v_rcp_f32 v84, v84\n"
  "v_mfma_f32_16x16x32_bf16 v[8:11], v[72:75], v[216:219], v[8:11]\n"
  "v_rcp_f32 v85, v85\n"
  "v_mul_f32 v40, v40, v84\n"
  "v_mfma_f32_16x16x32_bf16 v[8:11], v[68:71], v[192:195], v[8:11]\n"
  "v_rcp_f32 v86, v86\n"
  "v_mul_f32 v44, v44, v84\n"
  "v_mfma_f32_16x16x32_bf16 v[32:35], v[64:67], v[168:171], v[8:11]\n"
  "v_rcp_f32 v87, v87\n"
  "v_cvt_pk_bf16_f32 v40, v40, v44\n"
  "v_mfma_f32_16x16x32_bf16 v[4:7], v[64:67], v[164:167], v[4:7]\n"
  "v_mul_f32 v41, v41, v85\n"
  "v_mul_f32 v45, v45, v85\n"
  "v_cvt_pk_bf16_f32 v41, v41, v45\n"
  "v_mfma_f32_16x16x32_bf16 v[4:7], v[68:71], v[188:191], v[4:7]\n"
  "ds_write2_b32 v107, v40, v41 offset0:0 offset1:72\n"
  "v_mfma_f32_16x16x32_bf16 v[4:7], v[72:75], v[212:215], v[4:7]\n"
  "v_mul_f32 v42, v42, v86\n"
  "v_mul_f32 v46, v46, v86\n"
  "v_cvt_pk_bf16_f32 v42, v42, v46\n"
  "v_mfma_f32_16x16x32_bf16 v[28:31], v[76:79], v[236:239], v[4:7]\n"
  "v_mul_f32 v43, v43, v87\n"
  "v_mul_f32 v47, v47, v87\n"
  "v_cvt_pk_bf16_f32 v43, v43, v47\n"
  "v_mfma_f32_16x16x32_bf16 v[0:3], v[76:79], v[232:235], v[0:3]\n"
  "ds_write2_b32 v107, v42, v43 offset0:144 offset1:216\n"
  "v_mfma_f32_16x16x32_bf16 v[0:3], v[72:75], v[208:211], v[0:3]\n"
  "v_mul_f32 v32, v32, v84\n"
  "v_mul_f32 v33, v33, v85\n"
  "v_mul_f32 v36, v36, v84\n"
  "v_mfma_f32_16x16x32_bf16 v[0:3], v[68:71], v[184:187], v[0:3]\n"
  "v_mul_f32 v28, v28, v84\n"
  "v_mul_f32 v29, v29, v85\n"
  "v_mul_f32 v37, v37, v85\n"
  "v_mfma_f32_16x16x32_bf16 v[24:27], v[64:67], v[160:163], v[0:3]\n"
  "s_waitcnt lgkmcnt(2)\n"
  "v_mul_f32 v30, v30, v86\n"
  "v_mul_f32 v31, v31, v87\n"
  "v_mul_f32 v34, v34, v86\n"
  "v_mfma_f32_16x16x32_bf16 v[0:3], v[48:51], a[0:3], 0\n"
  "ds_read_b128 v[64:67], v104 offset:4096\n"
  "v_mul_f32 v35, v35, v87\n"
  "v_mul_f32 v38, v38, v86\n"
  "v_mfma_f32_16x16x32_bf16 v[0:3], v[52:55], a[24:27], v[0:3]\n"
  "v_mul_f32 v39, v39, v87\n"
  "v_max3_f32 v123, v123, v84, v85\n"
  "v_max3_f32 v123, v123, v86, v87\n"
  "v_mfma_f32_16x16x32_bf16 v[4:7], v[52:55], a[28:31], 0\n"
  "ds_read_b128 v[68:71], v104 offset:5120\n"
  "v_mul_f32 v24, v24, v84\n"
  "v_mul_f32 v25, v25, v85\n"
  "v_mfma_f32_16x16x32_bf16 v[4:7], v[48:51], a[4:7], v[4:7]\n"
  "ds_read_b64_tr_b16 v[92:93], v108 offset:0\n"
  "ds_read_b64_tr_b16 v[94:95], v108 offset:32\n"
  "v_cvt_pk_bf16_f32 v24, v24, v28\n"
  "v_mfma_f32_16x16x32_bf16 v[8:11], v[48:51], a[8:11], 0\n"
  "v_cvt_pk_bf16_f32 v28, v25, v29\n"
  "v_cvt_pk_bf16_f32 v25, v32, v36\n"
  "v_cvt_pk_bf16_f32 v29, v33, v37\n"
  "v_mfma_f32_16x16x32_bf16 v[8:11], v[52:55], a[32:35], v[8:11]\n"
  "ds_write_b64 v106, v[24:25] offset:0\n"
  "v_smfmac_f32_16x16x64_bf16 v[80:83], v[88:91], v[48:55], v124\n"
  "ds_read_b128 v[72:75], v104 offset:6144\n"
  "v_mul_f32 v26, v26, v86\n"
  "v_mfma_f32_16x16x32_bf16 v[12:15], v[52:55], a[36:39], 0\n"
  "ds_write_b64 v106, v[28:29] offset:288\n"
  "v_mfma_f32_16x16x32_bf16 v[12:15], v[48:51], a[12:15], v[12:15]\n"
  "v_mul_f32 v27, v27, v87\n"
  "v_cvt_pk_bf16_f32 v26, v26, v30\n"
  "v_cvt_pk_bf16_f32 v30, v27, v31\n"
  "v_mfma_f32_16x16x32_bf16 v[16:19], v[48:51], a[16:19], 0\n"
  "v_cvt_pk_bf16_f32 v27, v34, v38\n"
  "v_cvt_pk_bf16_f32 v31, v35, v39\n"
  "v_mfma_f32_16x16x32_bf16 v[16:19], v[52:55], a[40:43], v[16:19]\n"
  "ds_read_b128 v[76:79], v104 offset:7168\n"
  "ds_write_b64 v106, v[26:27] offset:576\n"
  "v_mfma_f32_16x16x32_bf16 v[20:23], v[52:55], a[44:47], 0\n"
  "ds_write_b64 v106, v[30:31] offset:864\n"
  "v_mfma_f32_16x16x32_bf16 v[20:23], v[48:51], a[20:23], v[20:23]\n"
  "v_xor_b32 v104, 0x4000, v104\n"
  "v_mfma_f32_16x16x32_bf16 v[20:23], v[56:59], a[68:71], v[20:23]\n"
  "s_waitcnt lgkmcnt(6)\n"
  "buffer_store_dwordx4 v[92:95], v109, s[48:51], s68 offen offset:0 nt\n"
  "s_waitcnt vmcnt(31)\n"
  "ds_read_b64_tr_b16 a[0:1], v117 offset:0\n"
  "ds_read_b64_tr_b16 a[4:5], v118 offset:0\n"
  "v_mfma_f32_16x16x32_bf16 v[20:23], v[60:63], a[92:95], v[20:23]\n"
  "v_mfma_f32_16x16x32_bf16 v[16:19], v[60:63], a[88:91], v[16:19]\n"
  "ds_read_b64_tr_b16 a[8:9], v119 offset:0\n"
  "ds_read_b64_tr_b16 a[12:13], v120 offset:0\n"
  "v_mfma_f32_16x16x32_bf16 v[16:19], v[56:59], a[64:67], v[16:19]\n"
  "ds_read_b64_tr_b16 a[16:17], v121 offset:2048\n"
  "ds_read_b64_tr_b16 a[20:21], v122 offset:2048\n"
  "v_mfma_f32_16x16x32_bf16 v[12:15], v[56:59], a[60:63], v[12:15]\n"
  "v_mfma_f32_16x16x32_bf16 v[12:15], v[60:63], a[84:87], v[12:15]\n"
  "s_waitcnt vmcnt(25)\n"
  "ds_read_b64_tr_b16 a[2:3], v117 offset:3072\n"
  "ds_read_b64_tr_b16 a[6:7], v118 offset:3072\n"
  "v_smfmac_f32_16x16x64_bf16 v[80:83], v[88:91], v[56:63], v124\n"
  "v_mfma_f32_16x16x32_bf16 v[8:11], v[60:63], a[80:83], v[8:11]\n"
  "ds_read_b64_tr_b16 a[10:11], v119 offset:3072\n"
  "ds_read_b64_tr_b16 a[14:15], v120 offset:3072\n"
  "v_mfma_f32_16x16x32_bf16 v[8:11], v[56:59], a[56:59], v[8:11]\n"
  "s_add_u32 m0, s59, 0\n"
  "ds_read_b64_tr_b16 a[18:19], v121 offset:5120\n"
  "ds_read_b64_tr_b16 a[22:23], v122 offset:5120\n"
  "v_mfma_f32_16x16x32_bf16 v[4:7], v[56:59], a[52:55], v[4:7]\n"
  "s_waitcnt lgkmcnt(6)\n"
  "buffer_load_dwordx4 v114, s[40:43], s57 offen offset:0 nt lds\n"
  "buffer_load_dwordx4 v115, s[40:43], s57 offen offset:1024 nt lds\n"
  "v_mfma_f32_16x16x32_bf16 v[4:7], v[60:63], a[76:79], v[4:7]\n"
  "s_waitcnt vmcnt(23)\n"
  "ds_read_b64_tr_b16 a[24:25], v117 offset:6144\n"
  "ds_read_b64_tr_b16 a[28:29], v118 offset:6144\n"
  "v_mfma_f32_16x16x32_bf16 v[0:3], v[60:63], a[72:75], v[0:3]\n"
  "buffer_load_dwordx4 v116, s[40:43], s57 offen offset:2048  lds\n"
  "v_mfma_f32_16x16x32_bf16 v[0:3], v[56:59], a[48:51], v[0:3]\n"
  "s_add_u32 s57, s57, 6144\n"
  "ds_read_b64_tr_b16 a[32:33], v119 offset:6144\n"
  "ds_read_b64_tr_b16 a[36:37], v120 offset:6144\n"
  "v_mfma_f32_16x16x32_bf16 v[0:3], v[64:67], a[96:99], v[0:3]\n"
  "s_add_u32 m0, s59, 3072\n"
  "ds_read_b128 v[48:51], v105 offset:0\n"
  "ds_read_b64_tr_b16 a[40:41], v121 offset:8192\n"
  "ds_read_b64_tr_b16 a[44:45], v122 offset:8192\n"
  "v_mfma_f32_16x16x32_bf16 v[0:3], v[68:71], a[120:123], v[0:3]\n"
  "s_waitcnt lgkmcnt(7)\n"
  "buffer_load_dwordx4 v114, s[40:43], s57 offen offset:0 nt lds\n"
  "buffer_load_dwordx4 v115, s[40:43], s57 offen offset:1024 nt lds\n"
  "v_mfma_f32_16x16x32_bf16 v[4:7], v[68:71], a[124:127], v[4:7]\n"
  "ds_read_b128 v[52:55], v105 offset:1024\n"
  "s_waitcnt vmcnt(21)\n"
  "ds_read_b64_tr_b16 a[26:27], v117 offset:9216\n"
  "ds_read_b64_tr_b16 a[30:31], v118 offset:9216\n"
  "v_mfma_f32_16x16x32_bf16 v[4:7], v[64:67], a[100:103], v[4:7]\n"
  "buffer_load_dwordx4 v116, s[40:43], s57 offen offset:2048  lds\n"
  "ds_read_b64_tr_b16 v[96:97], v108 offset:2304\n"
  "v_mfma_f32_16x16x32_bf16 v[8:11], v[64:67], a[104:107], v[8:11]\n"
  "s_add_u32 s57, s57, 6144\n"
  "ds_read_b64_tr_b16 a[34:35], v119 offset:9216\n"
  "ds_read_b64_tr_b16 a[38:39], v120 offset:9216\n"
  "ds_read_b64_tr_b16 v[98:99], v108 offset:2336\n"
  "v_mfma_f32_16x16x32_bf16 v[8:11], v[68:71], a[128:131], v[8:11]\n"
  "s_add_u32 m0, s59, 6144\n"
  "ds_read_b64_tr_b16 a[42:43], v121 offset:11264\n"
  "ds_read_b64_tr_b16 a[46:47], v122 offset:11264\n"
  "v_smfmac_f32_16x16x64_bf16 v[80:83], v[88:91], v[64:71], v124\n"
  "s_waitcnt lgkmcnt(9)\n"
  "buffer_load_dwordx4 v114, s[40:43], s57 offen offset:0 nt lds\n"
  "buffer_load_dwordx4 v115, s[40:43], s57 offen offset:1024 nt lds\n"
  "ds_read_b128 v[56:59], v105 offset:2048\n"
  "v_mfma_f32_16x16x32_bf16 v[12:15], v[68:71], a[132:135], v[12:15]\n"
  "s_waitcnt vmcnt(18)\n"
  "ds_read_b64_tr_b16 a[48:49], v117 offset:12288\n"
  "ds_read_b64_tr_b16 a[52:53], v118 offset:12288\n"
  "v_mfma_f32_16x16x32_bf16 v[12:15], v[64:67], a[108:111], v[12:15]\n"
  "buffer_load_dwordx4 v116, s[40:43], s57 offen offset:2048  lds\n"
  "v_mfma_f32_16x16x32_bf16 v[16:19], v[64:67], a[112:115], v[16:19]\n"
  "s_add_u32 s57, s57, 6144\n"
  "ds_read_b64_tr_b16 a[56:57], v119 offset:12288\n"
  "ds_read_b64_tr_b16 a[60:61], v120 offset:12288\n"
  "v_mfma_f32_16x16x32_bf16 v[16:19], v[68:71], a[136:139], v[16:19]\n"
  "s_add_u32 m0, s59, 9216\n"
  "ds_read_b128 v[60:63], v105 offset:3072\n"
  "ds_read_b64_tr_b16 a[64:65], v121 offset:14336\n"
  "ds_read_b64_tr_b16 a[68:69], v122 offset:14336\n"
  "v_mfma_f32_16x16x32_bf16 v[20:23], v[68:71], a[140:143], v[20:23]\n"
  "s_waitcnt lgkmcnt(8)\n"
  "buffer_load_dwordx4 v114, s[40:43], s57 offen offset:0 nt lds\n"
  "buffer_load_dwordx4 v115, s[40:43], s57 offen offset:1024 nt lds\n"
  "v_mfma_f32_16x16x32_bf16 v[20:23], v[64:67], a[116:119], v[20:23]\n"
  "s_waitcnt vmcnt(18)\n"
  "ds_read_b64_tr_b16 a[50:51], v117 offset:15360\n"
  "ds_read_b64_tr_b16 a[54:55], v118 offset:15360\n"
  "v_mfma_f32_16x16x32_bf16 v[20:23], v[72:75], a[164:167], v[20:23]\n"
  "buffer_load_dwordx4 v116, s[40:43], s57 offen offset:2048  lds\n"
  "v_mfma_f32_16x16x32_bf16 v[20:23], v[76:79], a[188:191], v[20:23]\n"
  "s_add_u32 s57, s57, 6144\n"
  "buffer_store_dwordx4 v[96:99], v109, s[48:51], s68 offen offset:128 nt\n"
  "ds_read_b64_tr_b16 a[58:59], v119 offset:15360\n"
  "ds_read_b64_tr_b16 a[62:63], v120 offset:15360\n"
  "v_mfma_f32_16x16x32_bf16 v[16:19], v[76:79], a[184:187], v[16:19]\n"
  "s_add_u32 m0, s59, 12288\n"
  "ds_read_b64_tr_b16 a[66:67], v121 offset:17408\n"
  "ds_read_b64_tr_b16 a[70:71], v122 offset:17408\n"
  "v_mfma_f32_16x16x32_bf16 v[16:19], v[72:75], a[160:163], v[16:19]\n"
  "s_waitcnt lgkmcnt(6)\n"
  "buffer_load_dwordx4 v114, s[40:43], s57 offen offset:0 nt lds\n"
  "buffer_load_dwordx4 v115, s[40:43], s57 offen offset:1024 nt lds\n"
  "v_mfma_f32_16x16x32_bf16 v[12:15], v[72:75], a[156:159], v[12:15]\n"
  "s_waitcnt vmcnt(12)\n"
  "ds_read_b64_tr_b16 a[72:73], v117 offset:0\n"
  "ds_read_b64_tr_b16 a[76:77], v118 offset:0\n"
  "v_mfma_f32_16x16x32_bf16 v[12:15], v[76:79], a[180:183], v[12:15]\n"
  "buffer_load_dwordx4 v116, s[40:43], s57 offen offset:2048  lds\n"
  "v_smfmac_f32_16x16x64_bf16 v[80:83], v[88:91], v[72:79], v124\n"
  "s_add_u32 s57, s57, 6144\n"
  "ds_read_b64_tr_b16 a[80:81], v119 offset:0\n"
  "ds_read_b64_tr_b16 a[84:85], v120 offset:0\n"
  "v_mfma_f32_16x16x32_bf16 v[8:11], v[76:79], a[176:179], v[8:11]\n"
  "s_add_u32 m0, s59, 15360\n"
  "ds_read_b64_tr_b16 a[88:89], v121 offset:2048\n"
  "ds_read_b64_tr_b16 a[92:93], v122 offset:2048\n"
  "ds_read_b64_tr_b16 v[100:101], v108 offset:4608\n"
  "v_mfma_f32_16x16x32_bf16 v[8:11], v[72:75], a[152:155], v[8:11]\n"
  "s_waitcnt lgkmcnt(7)\n"
  "buffer_load_dwordx4 v114, s[40:43], s57 offen offset:0 nt lds\n"
  "buffer_load_dwordx4 v115, s[40:43], s57 offen offset:1024 nt lds\n"
  "v_mfma_f32_16x16x32_bf16 v[4:7], v[72:75], a[148:151], v[4:7]\n"
  "s_waitcnt vmcnt(12)\n"
  "ds_read_b64_tr_b16 a[74:75], v117 offset:3072\n"
  "ds_read_b64_tr_b16 a[78:79], v118 offset:3072\n"
  "ds_read_b64_tr_b16 v[102:103], v108 offset:4640\n"
  "v_mfma_f32_16x16x32_bf16 v[4:7], v[76:79], a[172:175], v[4:7]\n"
  "buffer_load_dwordx4 v116, s[40:43], s57 offen offset:2048  lds\n"
  "v_add_f32 v44, v80, v81\n"
  "v_mfma_f32_16x16x32_bf16 v[0:3], v[76:79], a[168:171], v[0:3]\n"
  "s_add_u32 s57, s57, 6144\n"
  "ds_read_b64_tr_b16 a[82:83], v119 offset:3072\n"
  "ds_read_b64_tr_b16 a[86:87], v120 offset:3072\n"
  "v_xor_b32 v108, 0x2000, v108\n"
  "v_mfma_f32_16x16x32_bf16 v[0:3], v[72:75], a[144:147], v[0:3]\n"
  "s_add_u32 m0, s59, 0\n"
  "ds_read_b64_tr_b16 a[90:91], v121 offset:5120\n"
  "ds_read_b64_tr_b16 a[94:95], v122 offset:5120\n"
  "v_mov_b32 v80, 0\n"
  "v_mfma_f32_16x16x32_bf16 v[0:3], v[48:51], a[192:195], v[0:3]\n"
  "s_waitcnt lgkmcnt(8)\n"
  "buffer_load_dwordx4 v114, s[40:43], s57 offen offset:0 nt lds\n"
  "buffer_load_dwordx4 v115, s[40:43], s57 offen offset:1024 nt lds\n"
  "ds_read_b128 v[64:67], v105 offset:4096\n"
  "v_mfma_f32_16x16x32_bf16 v[0:3], v[52:55], a[216:219], v[0:3]\n"
  "s_waitcnt vmcnt(12)\n"
  "ds_read_b64_tr_b16 a[96:97], v117 offset:6144\n"
  "ds_read_b64_tr_b16 a[100:101], v118 offset:6144\n"
  "v_mov_b32 v81, 0\n"
  "v_mfma_f32_16x16x32_bf16 v[4:7], v[52:55], a[220:223], v[4:7]\n"
  "buffer_load_dwordx4 v116, s[40:43], s57 offen offset:2048  lds\n"
  "ds_read_b128 v[68:71], v105 offset:5120\n"
  "v_mfma_f32_16x16x32_bf16 v[4:7], v[48:51], a[196:199], v[4:7]\n"
  "s_add_u32 s57, s57, 6144\n"
  "s_waitcnt lgkmcnt(8)\n"
  "buffer_store_dwordx4 v[100:103], v109, s[48:51], s68 offen offset:256 nt\n"
  "ds_read_b64_tr_b16 a[104:105], v119 offset:6144\n"
  "ds_read_b64_tr_b16 a[108:109], v120 offset:6144\n"
  "v_mfma_f32_16x16x32_bf16 v[8:11], v[48:51], a[200:203], v[8:11]\n"
  "s_add_u32 m0, s59, 3072\n"
  "ds_read_b64_tr_b16 a[112:113], v121 offset:8192\n"
  "ds_read_b64_tr_b16 a[116:117], v122 offset:8192\n"
  "v_mfma_f32_16x16x32_bf16 v[8:11], v[52:55], a[224:227], v[8:11]\n"
  "s_waitcnt lgkmcnt(8)\n"
  "buffer_load_dwordx4 v114, s[40:43], s57 offen offset:0 nt lds\n"
  "buffer_load_dwordx4 v115, s[40:43], s57 offen offset:1024 nt lds\n"
  "v_mfma_f32_16x16x32_bf16 v[12:15], v[52:55], a[228:231], v[12:15]\n"
  "ds_read_b128 v[72:75], v105 offset:6144\n"
  "s_waitcnt vmcnt(13)\n"
  "ds_read_b64_tr_b16 a[98:99], v117 offset:9216\n"
  "ds_read_b64_tr_b16 a[102:103], v118 offset:9216\n"
  "v_mfma_f32_16x16x32_bf16 v[12:15], v[48:51], a[204:207], v[12:15]\n"
  "s_add_u32 s68, s68, s69\n"
  "buffer_load_dwordx4 v116, s[40:43], s57 offen offset:2048  lds\n"
  "v_mfma_f32_16x16x32_bf16 v[16:19], v[48:51], a[208:211], v[16:19]\n"
  "s_add_u32 s57, s57, 6144\n"
  "ds_read_b64_tr_b16 a[106:107], v119 offset:9216\n"
  "ds_read_b64_tr_b16 a[110:111], v120 offset:9216\n"
  "v_mfma_f32_16x16x32_bf16 v[16:19], v[52:55], a[232:235], v[16:19]\n"
  "s_add_u32 m0, s59, 6144\n"
  "ds_read_b64_tr_b16 a[114:115], v121 offset:11264\n"
  "ds_read_b64_tr_b16 a[118:119], v122 offset:11264\n"
  "v_mfma_f32_16x16x32_bf16 v[20:23], v[52:55], a[236:239], v[20:23]\n"
  "s_waitcnt lgkmcnt(7)\n"
  "buffer_load_dwordx4 v114, s[40:43], s57 offen offset:0 nt lds\n"
  "buffer_load_dwordx4 v115, s[40:43], s57 offen offset:1024 nt lds\n"
  "ds_read_b128 v[76:79], v105 offset:7168\n"
  "v_mfma_f32_16x16x32_bf16 v[20:23], v[48:51], a[212:215], v[20:23]\n"
  "s_waitcnt vmcnt(12)\n"
  "ds_read_b64_tr_b16 a[120:121], v117 offset:12288\n"
  "ds_read_b64_tr_b16 a[124:125], v118 offset:12288\n"
  "v_xor_b32 v105, 0x4000, v105\n"
  "v_mfma_f32_16x16x32_bf16 v[20:23], v[56:59], v[132:135], v[20:23]\n"
  "s_mov_b32 s69, s70\n"
  "buffer_load_dwordx4 v116, s[40:43], s57 offen offset:2048  lds\n"
  "v_mfma_f32_16x16x32_bf16 v[20:23], v[60:63], v[156:159], v[20:23]\n"
  "s_add_u32 s57, s57, 6144\n"
  "ds_read_b64_tr_b16 a[128:129], v119 offset:12288\n"
  "ds_read_b64_tr_b16 a[132:133], v120 offset:12288\n"
  "v_mfma_f32_16x16x32_bf16 v[16:19], v[60:63], v[152:155], v[16:19]\n"
  "s_add_u32 m0, s59, 9216\n"
  "ds_read_b64_tr_b16 a[136:137], v121 offset:14336\n"
  "ds_read_b64_tr_b16 a[140:141], v122 offset:14336\n"
  "v_mfma_f32_16x16x32_bf16 v[16:19], v[56:59], v[128:131], v[16:19]\n"
  "s_waitcnt lgkmcnt(7)\n"
  "buffer_load_dwordx4 v114, s[40:43], s57 offen offset:0 nt lds\n"
  "buffer_load_dwordx4 v115, s[40:43], s57 offen offset:1024 nt lds\n"
  "v_mfma_f32_16x16x32_bf16 v[12:15], v[56:59], a[252:255], v[12:15]\n"
  "s_waitcnt vmcnt(12)\n"
  "ds_read_b64_tr_b16 a[122:123], v117 offset:15360\n"
  "ds_read_b64_tr_b16 a[126:127], v118 offset:15360\n"
  "ds_write_b32 v110, v44\n"
  "v_mfma_f32_16x16x32_bf16 v[12:15], v[60:63], v[148:151], v[12:15]\n"
  "s_mov_b32 s70, s96\n"
  "buffer_load_dwordx4 v116, s[40:43], s57 offen offset:2048  lds\n"
  "v_mfma_f32_16x16x32_bf16 v[8:11], v[60:63], v[144:147], v[8:11]\n"
  "s_add_u32 s57, s57, s58\n"
  "ds_read_b64_tr_b16 a[130:131], v119 offset:15360\n"
  "ds_read_b64_tr_b16 a[134:135], v120 offset:15360\n"
  "v_mfma_f32_16x16x32_bf16 v[8:11], v[56:59], a[248:251], v[8:11]\n"
  "s_add_u32 m0, s59, 12288\n"
  "ds_read_b64_tr_b16 a[138:139], v121 offset:17408\n"
  "ds_read_b64_tr_b16 a[142:143], v122 offset:17408\n"
  "v_xor_b32 v106, 0x2000, v106\n"
  "v_mfma_f32_16x16x32_bf16 v[4:7], v[56:59], a[244:247], v[4:7]\n"
  "s_waitcnt lgkmcnt(7)\n"
  "buffer_load_dwordx4 v114, s[40:43], s57 offen offset:0 nt lds\n"
  "buffer_load_dwordx4 v115, s[40:43], s57 offen offset:1024 nt lds\n"
  "v_mfma_f32_16x16x32_bf16 v[4:7], v[60:63], v[140:143], v[4:7]\n"
  "s_waitcnt vmcnt(12)\n"
  "ds_read_b64_tr_b16 a[144:145], v117 offset:0\n"
  "ds_read_b64_tr_b16 a[148:149], v118 offset:0\n"
  "v_xor_b32 v107, 0x2000, v107\n"
  "v_mfma_f32_16x16x32_bf16 v[0:3], v[60:63], v[136:139], v[0:3]\n"
  "s_mov_b32 s96, s97\n"
  "buffer_load_dwordx4 v116, s[40:43], s57 offen offset:2048  lds\n"
  "v_xor_b32 v110, 0x2000, v110\n"
  "v_mfma_f32_16x16x32_bf16 v[0:3], v[56:59], a[240:243], v[0:3]\n"
  "s_add_u32 s57, s57, 6144\n"
  "ds_read_b64_tr_b16 a[152:153], v119 offset:0\n"
  "ds_read_b64_tr_b16 a[156:157], v120 offset:0\n"
  "s_waitcnt lgkmcnt(0)\n"
  "s_barrier\n"
  "v_mfma_f32_16x16x32_bf16 v[0:3], v[64:67], v[160:163], v[0:3]\n"
  "s_add_u32 m0, s59, 15360\n"
  "ds_read_b128 v[48:51], v104 offset:0\n"
  "ds_read_b64_tr_b16 a[160:161], v121 offset:2048\n"
  "ds_read_b64_tr_b16 a[164:165], v122 offset:2048\n"
  "v_mfma_f32_16x16x32_bf16 v[0:3], v[68:71], v[184:187], v[0:3]\n"
  "buffer_load_dwordx4 v114, s[40:43], s57 offen offset:0 nt lds\n"
  "buffer_load_dwordx4 v115, s[40:43], s57 offen offset:1024 nt lds\n"
  "v_mfma_f32_16x16x32_bf16 v[4:7], v[68:71], v[188:191], v[4:7]\n"
  "ds_read_b128 v[52:55], v104 offset:1024\n"
  "s_waitcnt vmcnt(11)\n"
  "ds_read_b64_tr_b16 a[146:147], v117 offset:3072\n"
  "ds_read_b64_tr_b16 a[150:151], v118 offset:3072\n"
  "v_mfma_f32_16x16x32_bf16 v[4:7], v[64:67], v[164:167], v[4:7]\n"
  "s_sub_u32 s90, s90, 1\n"
  "s_cselect_b32 s63, s91, 6144\n"
  "s_cmov_b64 s[90:91], s[92:93]\n"
  "s_cmov_b64 s[92:93], s[94:95]\n"
  "buffer_load_dwordx4 v116, s[40:43], s57 offen offset:2048  lds\n"
  "ds_read_b128 v[84:87], v111\n"
  "v_mfma_f32_16x16x32_bf16 v[8:11], v[64:67], v[168:171], v[8:11]\n"
  "s_add_u32 s57, s57, 6144\n"
  "ds_read_b64_tr_b16 a[154:155], v119 offset:3072\n"
  "ds_read_b64_tr_b16 a[158:159], v120 offset:3072\n"
  "ds_read_b128 v[100:103], v111 offset:64\n"
  "v_mfma_f32_16x16x32_bf16 v[8:11], v[68:71], v[192:195], v[8:11]\n"
  "s_add_u32 m0, s59, 0\n"
  "ds_read_b64_tr_b16 a[162:163], v121 offset:5120\n"
  "ds_read_b64_tr_b16 a[166:167], v122 offset:5120\n"
  "v_xor_b32 v111, 0x2000, v111\n"
  "v_mfma_f32_16x16x32_bf16 v[12:15], v[68:71], v[196:199], v[12:15]\n"
  "s_waitcnt lgkmcnt(9)\n"
  "buffer_load_dwordx4 v114, s[40:43], s57 offen offset:0 nt lds\n"
  "buffer_load_dwordx4 v115, s[40:43], s57 offen offset:1024 nt lds\n"
  "ds_read_b128 v[56:59], v104 offset:2048\n"
  "v_mfma_f32_16x16x32_bf16 v[12:15], v[64:67], v[172:175], v[12:15]\n"
  "s_waitcnt vmcnt(11)\n"
  "ds_read_b64_tr_b16 a[168:169], v117 offset:6144\n"
  "ds_read_b64_tr_b16 a[172:173], v118 offset:6144\n"
  "v_mfma_f32_16x16x32_bf16 v[16:19], v[64:67], v[176:179], v[16:19]\n"
  "s_add_u32 s72, s72, 7\n"
  "buffer_load_dwordx4 v116, s[40:43], s57 offen offset:2048  lds\n"
  "v_mfma_f32_16x16x32_bf16 v[16:19], v[68:71], v[200:203], v[16:19]\n"
  "s_add_u32 s57, s57, 6144\n"
  "ds_read_b64_tr_b16 a[176:177], v119 offset:6144\n"
  "ds_read_b64_tr_b16 a[180:181], v120 offset:6144\n"
  "v_mfma_f32_16x16x32_bf16 v[20:23], v[68:71], v[204:207], v[20:23]\n"
  "s_add_u32 m0, s59, 3072\n"
  "ds_read_b128 v[60:63], v104 offset:3072\n"
  "ds_read_b64_tr_b16 a[184:185], v121 offset:8192\n"
  "ds_read_b64_tr_b16 a[188:189], v122 offset:8192\n"
  "v_mfma_f32_16x16x32_bf16 v[20:23], v[64:67], v[180:183], v[20:23]\n"
  "s_waitcnt lgkmcnt(8)\n"
  "buffer_load_dwordx4 v114, s[40:43], s57 offen offset:0 nt lds\n"
  "buffer_load_dwordx4 v115, s[40:43], s57 offen offset:1024 nt lds\n"
  "v_mfma_f32_16x16x32_bf16 v[20:23], v[72:75], v[228:231], v[20:23]\n"
  "s_waitcnt vmcnt(11)\n"
  "ds_read_b64_tr_b16 a[170:171], v117 offset:9216\n"
  "ds_read_b64_tr_b16 a[174:175], v118 offset:9216\n"
  "v_mfma_f32_16x16x32_bf16 v[44:47], v[76:79], v[252:255], v[20:23]\n"
  "buffer_load_dwordx4 v116, s[40:43], s57 offen offset:2048  lds\n"
  "v_add_f32 v84, v84, v100\n"
  "v_mfma_f32_16x16x32_bf16 v[16:19], v[76:79], v[248:251], v[16:19]\n"
  "s_add_u32 s57, s57, 6144\n"
  "ds_read_b64_tr_b16 a[178:179], v119 offset:9216\n"
  "ds_read_b64_tr_b16 a[182:183], v120 offset:9216\n"
  "v_add_f32 v85, v85, v101\n"
  "v_mfma_f32_16x16x32_bf16 v[40:43], v[72:75], v[224:227], v[16:19]\n"
  "s_add_u32 m0, s59, 6144\n"
  "ds_read_b64_tr_b16 a[186:187], v121 offset:11264\n"
  "ds_read_b64_tr_b16 a[190:191], v122 offset:11264\n"
  "v_add_f32 v86, v86, v102\n"
  "v_mfma_f32_16x16x32_bf16 v[12:15], v[72:75], v[220:223], v[12:15]\n"
  "s_waitcnt lgkmcnt(6)\n"
  "buffer_load_dwordx4 v114, s[40:43], s57 offen offset:0 nt lds\n"
  "buffer_load_dwordx4 v115, s[40:43], s57 offen offset:1024 nt lds\n"
  "v_mfma_f32_16x16x32_bf16 v[36:39], v[76:79], v[244:247], v[12:15]\n"
  "s_waitcnt vmcnt(11)\n"
  "ds_read_b64_tr_b16 a[192:193], v117 offset:12288\n"
  "ds_read_b64_tr_b16 a[196:197], v118 offset:12288\n"
  "v_add_f32 v87, v87, v103\n"
  "v_mfma_f32_16x16x32_bf16 v[8:11], v[76:79], v[240:243], v[8:11]\n"
  "s_sub_u32 s56, s56, 1\n"
  "buffer_load_dwordx4 v116, s[40:43], s57 offen offset:2048  lds\n"
  "v_max3_f32 v123, v123, v84, v85\n"
  "v_mfma_f32_16x16x32_bf16 v[32:35], v[72:75], v[216:219], v[8:11]\n"
  "s_add_u32 s57, s57, 6144\n"
  "ds_read_b64_tr_b16 a[200:201], v119 offset:12288\n"
  "ds_read_b64_tr_b16 a[204:205], v120 offset:12288\n"
  "v_max3_f32 v123, v123, v86, v87\n"
  "v_mfma_f32_16x16x32_bf16 v[4:7], v[72:75], v[212:215], v[4:7]\n"
  "s_add_u32 m0, s59, 9216\n"
  "ds_read_b64_tr_b16 a[208:209], v121 offset:14336\n"
  "ds_read_b64_tr_b16 a[212:213], v122 offset:14336\n"
  "v_mfma_f32_16x16x32_bf16 v[28:31], v[76:79], v[236:239], v[4:7]\n"
  "s_waitcnt lgkmcnt(6)\n"
  "buffer_load_dwordx4 v114, s[40:43], s57 offen offset:0 nt lds\n"
  "buffer_load_dwordx4 v115, s[40:43], s57 offen offset:1024 nt lds\n"
  "v_mfma_f32_16x16x32_bf16 v[0:3], v[76:79], v[232:235], v[0:3]\n"
  "s_waitcnt vmcnt(11)\n"
  "ds_read_b64_tr_b16 a[194:195], v117 offset:15360\n"
  "ds_read_b64_tr_b16 a[198:199], v118 offset:15360\n"
  "v_mfma_f32_16x16x32_bf16 v[24:27], v[72:75], v[208:211], v[0:3]\n"
  "buffer_load_dwordx4 v116, s[40:43], s57 offen offset:2048  lds\n"
  "v_mfma_f32_16x16x32_bf16 v[0:3], v[48:51], a[0:3], 0\n"
  "s_add_u32 s57, s57, 6144\n"
  "ds_read_b128 v[64:67], v104 offset:4096\n"
  "ds_read_b64_tr_b16 a[202:203], v119 offset:15360\n"
  "ds_read_b64_tr_b16 a[206:207], v120 offset:15360\n"
  "v_mfma_f32_16x16x32_bf16 v[0:3], v[52:55], a[24:27], v[0:3]\n"
  "s_add_u32 m0, s59, 12288\n"
  "ds_read_b64_tr_b16 a[210:211], v121 offset:17408\n"
  "ds_read_b64_tr_b16 a[214:215], v122 offset:17408\n"
  "v_mfma_f32_16x16x32_bf16 v[4:7], v[52:55], a[28:31], 0\n"
  "s_waitcnt lgkmcnt(7)\n"
  "buffer_load_dwordx4 v114, s[40:43], s57 offen offset:0 nt lds\n"
  "buffer_load_dwordx4 v115, s[40:43], s57 offen offset:1024 nt lds\n"
  "ds_read_b128 v[68:71], v104 offset:5120\n"
  "v_mfma_f32_16x16x32_bf16 v[4:7], v[48:51], a[4:7], v[4:7]\n"
  "s_waitcnt vmcnt(11)\n"
  "ds_read_b64_tr_b16 a[216:217], v117 offset:0\n"
  "ds_read_b64_tr_b16 a[220:221], v118 offset:0\n"
  "ds_read_b64_tr_b16 v[92:93], v108 offset:0\n"
  "v_mfma_f32_16x16x32_bf16 v[8:11], v[48:51], a[8:11], 0\n"
  "s_sub_i32 s61, s56, 5\n"
  "s_max_i32 s61, s61, 0\n"
  "buffer_load_dwordx4 v116, s[40:43], s57 offen offset:2048  lds\n"
  "ds_read_b64_tr_b16 v[94:95], v108 offset:32\n"
  "v_mfma_f32_16x16x32_bf16 v[8:11], v[52:55], a[32:35], v[8:11]\n"
  "s_add_u32 s57, s57, 6144\n"
  "ds_read_b64_tr_b16 a[224:225], v119 offset:0\n"
  "ds_read_b64_tr_b16 a[228:229], v120 offset:0\n"
  "v_smfmac_f32_16x16x64_bf16 v[80:83], v[88:91], v[48:55], v124\n"
  "s_add_u32 m0, s59, 15360\n"
  "ds_read_b128 v[72:75], v104 offset:6144\n"
  "ds_read_b64_tr_b16 a[232:233], v121 offset:2048\n"
  "ds_read_b64_tr_b16 a[236:237], v122 offset:2048\n"
  "v_mfma_f32_16x16x32_bf16 v[12:15], v[52:55], a[36:39], 0\n"
  "s_waitcnt lgkmcnt(10)\n"
  "buffer_load_dwordx4 v114, s[40:43], s57 offen offset:0 nt lds\n"
  "buffer_load_dwordx4 v115, s[40:43], s57 offen offset:1024 nt lds\n"
  "v_mfma_f32_16x16x32_bf16 v[12:15], v[48:51], a[12:15], v[12:15]\n"
  "s_waitcnt vmcnt(11)\n"
  "ds_read_b64_tr_b16 a[218:219], v117 offset:3072\n"
  "ds_read_b64_tr_b16 a[222:223], v118 offset:3072\n"
  "v_mfma_f32_16x16x32_bf16 v[16:19], v[48:51], a[16:19], 0\n"
  "buffer_load_dwordx4 v116, s[40:43], s57 offen offset:2048  lds\n"
  "v_mfma_f32_16x16x32_bf16 v[16:19], v[52:55], a[40:43], v[16:19]\n"
  "s_add_u32 s57, s57, 6144\n"
  "ds_read_b128 v[76:79], v104 offset:7168\n"
  "ds_read_b64_tr_b16 a[226:227], v119 offset:3072\n"
  "ds_read_b64_tr_b16 a[230:231], v120 offset:3072\n"
  "v_mfma_f32_16x16x32_bf16 v[20:23], v[52:55], a[44:47], 0\n"
  "s_add_u32 m0, s59, 0\n"
  "ds_read_b64_tr_b16 a[234:235], v121 offset:5120\n"
  "ds_read_b64_tr_b16 a[238:239], v122 offset:5120\n"
  "v_xor_b32 v104, 0x4000, v104\n"
  "v_mfma_f32_16x16x32_bf16 v[20:23], v[48:51], a[20:23], v[20:23]\n"
  "s_waitcnt lgkmcnt(7)\n"
  "buffer_load_dwordx4 v114, s[40:43], s57 offen offset:0 nt lds\n"
  "buffer_load_dwordx4 v115, s[40:43], s57 offen offset:1024 nt lds\n"
  "v_mfma_f32_16x16x32_bf16 v[20:23], v[56:59], a[68:71], v[20:23]\n"
  "buffer_store_dwordx4 v[92:95], v109, s[48:51], s68 offen offset:0 nt\n"
  "s_waitcnt vmcnt(12)\n"
  "ds_read_b64_tr_b16 a[240:241], v117 offset:6144\n"
  "ds_read_b64_tr_b16 a[244:245], v118 offset:6144\n"
  "v_mfma_f32_16x16x32_bf16 v[20:23], v[60:63], a[92:95], v[20:23]\n"
  "buffer_load_dwordx4 v116, s[40:43], s57 offen offset:2048  lds\n"
  "v_mfma_f32_16x16x32_bf16 v[16:19], v[60:63], a[88:91], v[16:19]\n"
  "s_add_u32 s57, s57, 6144\n"
  "ds_read_b64_tr_b16 a[248:249], v119 offset:6144\n"
  "ds_read_b64_tr_b16 a[252:253], v120 offset:6144\n"
  "v_mfma_f32_16x16x32_bf16 v[16:19], v[56:59], a[64:67], v[16:19]\n"
  "s_add_u32 m0, s59, 3072\n"
  "ds_read_b64_tr_b16 v[128:129], v121 offset:8192\n"
  "ds_read_b64_tr_b16 v[132:133], v122 offset:8192\n"
  "v_mfma_f32_16x16x32_bf16 v[12:15], v[56:59], a[60:63], v[12:15]\n"
  "s_waitcnt lgkmcnt(6)\n"
  "buffer_load_dwordx4 v114, s[40:43], s57 offen offset:0 nt lds\n"
  "buffer_load_dwordx4 v115, s[40:43], s57 offen offset:1024 nt lds\n"
  "v_mfma_f32_16x16x32_bf16 v[12:15], v[60:63], a[84:87], v[12:15]\n"
  "s_waitcnt vmcnt(12)\n"
  "ds_read_b64_tr_b16 a[242:243], v117 offset:9216\n"
  "ds_read_b64_tr_b16 a[246:247], v118 offset:9216\n"
  "v_smfmac_f32_16x16x64_bf16 v[80:83], v[88:91], v[56:63], v124\n"
  "buffer_load_dwordx4 v116, s[40:43], s57 offen offset:2048  lds\n"
  "v_mfma_f32_16x16x32_bf16 v[8:11], v[60:63], a[80:83], v[8:11]\n"
  "s_add_u32 s57, s57, 6144\n"
  "ds_read_b64_tr_b16 a[250:251], v119 offset:9216\n"
  "ds_read_b64_tr_b16 a[254:255], v120 offset:9216\n"
  "v_mfma_f32_16x16x32_bf16 v[8:11], v[56:59], a[56:59], v[8:11]\n"
  "s_add_u32 m0, s59, 6144\n"
  "ds_read_b64_tr_b16 v[130:131], v121 offset:11264\n"
  "ds_read_b64_tr_b16 v[134:135], v122 offset:11264\n"
  "v_mfma_f32_16x16x32_bf16 v[4:7], v[56:59], a[52:55], v[4:7]\n"
  "s_waitcnt lgkmcnt(6)\n"
  "buffer_load_dwordx4 v114, s[40:43], s57 offen offset:0 nt lds\n"
  "buffer_load_dwordx4 v115, s[40:43], s57 offen offset:1024 nt lds\n"
  "v_mfma_f32_16x16x32_bf16 v[4:7], v[60:63], a[76:79], v[4:7]\n"
  "s_waitcnt vmcnt(12)\n"
  "ds_read_b64_tr_b16 v[136:137], v117 offset:12288\n"
  "ds_read_b64_tr_b16 v[140:141], v118 offset:12288\n"
  "v_mfma_f32_16x16x32_bf16 v[0:3], v[60:63], a[72:75], v[0:3]\n"
  "buffer_load_dwordx4 v116, s[40:43], s57 offen offset:2048  lds\n"
  "v_mfma_f32_16x16x32_bf16 v[0:3], v[56:59], a[48:51], v[0:3]\n"
  "s_add_u32 s57, s57, 6144\n"
  "ds_read_b64_tr_b16 v[144:145], v119 offset:12288\n"
  "ds_read_b64_tr_b16 v[148:149], v120 offset:12288\n"
  "v_mfma_f32_16x16x32_bf16 v[0:3], v[64:67], a[96:99], v[0:3]\n"
  "s_add_u32 m0, s59, 9216\n"
  "ds_read_b128 v[48:51], v105 offset:0\n"
  "ds_read_b64_tr_b16 v[152:153], v121 offset:14336\n"
  "ds_read_b64_tr_b16 v[156:157], v122 offset:14336\n"
  "v_mfma_f32_16x16x32_bf16 v[0:3], v[68:71], a[120:123], v[0:3]\n"
  "s_waitcnt lgkmcnt(7)\n"
  "buffer_load_dwordx4 v114, s[40:43], s57 offen offset:0 nt lds\n"
  "buffer_load_dwordx4 v115, s[40:43], s57 offen offset:1024 nt lds\n"
  "v_mfma_f32_16x16x32_bf16 v[4:7], v[68:71], a[124:127], v[4:7]\n"
  "ds_read_b128 v[52:55], v105 offset:1024\n"
  "s_waitcnt vmcnt(12)\n"
  "ds_read_b64_tr_b16 v[138:139], v117 offset:15360\n"
  "ds_read_b64_tr_b16 v[142:143], v118 offset:15360\n"
  "v_mfma_f32_16x16x32_bf16 v[4:7], v[64:67], a[100:103], v[4:7]\n"
  "buffer_load_dwordx4 v116, s[40:43], s57 offen offset:2048  lds\n"
  "ds_read_b64_tr_b16 v[96:97], v108 offset:2304\n"
  "v_mfma_f32_16x16x32_bf16 v[8:11], v[64:67], a[104:107], v[8:11]\n"
  "s_add_u32 s57, s57, 6144\n"
  "ds_read_b64_tr_b16 v[146:147], v119 offset:15360\n"
  "ds_read_b64_tr_b16 v[150:151], v120 offset:15360\n"
  "ds_read_b64_tr_b16 v[98:99], v108 offset:2336\n"
  "v_mfma_f32_16x16x32_bf16 v[8:11], v[68:71], a[128:131], v[8:11]\n"
  "s_add_u32 m0, s59, 12288\n"
  "ds_read_b64_tr_b16 v[154:155], v121 offset:17408\n"
  "ds_read_b64_tr_b16 v[158:159], v122 offset:17408\n"
  "v_smfmac_f32_16x16x64_bf16 v[80:83], v[88:91], v[64:71], v124\n"
  "s_waitcnt lgkmcnt(9)\n"
  "buffer_load_dwordx4 v114, s[40:43], s57 offen offset:0 nt lds\n"
  "buffer_load_dwordx4 v115, s[40:43], s57 offen offset:1024 nt lds\n"
  "ds_read_b128 v[56:59], v105 offset:2048\n"
  "v_mfma_f32_16x16x32_bf16 v[12:15], v[68:71], a[132:135], v[12:15]\n"
  "s_waitcnt vmcnt(11)\n"
  "ds_read_b64_tr_b16 v[160:161], v117 offset:0\n"
  "ds_read_b64_tr_b16 v[164:165], v118 offset:0\n"
  "v_mfma_f32_16x16x32_bf16 v[12:15], v[64:67], a[108:111], v[12:15]\n"
  "buffer_load_dwordx4 v116, s[40:43], s57 offen offset:2048  lds\n"
  "v_mfma_f32_16x16x32_bf16 v[16:19], v[64:67], a[112:115], v[16:19]\n"
  "s_add_u32 s57, s57, 6144\n"
  "ds_read_b64_tr_b16 v[168:169], v119 offset:0\n"
  "ds_read_b64_tr_b16 v[172:173], v120 offset:0\n"
  "v_mfma_f32_16x16x32_bf16 v[16:19], v[68:71], a[136:139], v[16:19]\n"
  "s_add_u32 m0, s59, 15360\n"
  "ds_read_b128 v[60:63], v105 offset:3072\n"
  "ds_read_b64_tr_b16 v[176:177], v121 offset:2048\n"
  "ds_read_b64_tr_b16 v[180:181], v122 offset:2048\n"
  "v_mfma_f32_16x16x32_bf16 v[20:23], v[68:71], a[140:143], v[20:23]\n"
  "s_waitcnt lgkmcnt(8)\n"
  "buffer_load_dwordx4 v114, s[40:43], s57 offen offset:0 nt lds\n"
  "buffer_load_dwordx4 v115, s[40:43], s57 offen offset:1024 nt lds\n"
  "v_mfma_f32_16x16x32_bf16 v[20:23], v[64:67], a[116:119], v[20:23]\n"
  "s_waitcnt vmcnt(11)\n"
  "ds_read_b64_tr_b16 v[162:163], v117 offset:3072\n"
  "ds_read_b64_tr_b16 v[166:167], v118 offset:3072\n"
  "v_mfma_f32_16x16x32_bf16 v[20:23], v[72:75], a[164:167], v[20:23]\n"
  "buffer_load_dwordx4 v116, s[40:43], s57 offen offset:2048  lds\n"
  "v_mfma_f32_16x16x32_bf16 v[20:23], v[76:79], a[188:191], v[20:23]\n"
  "s_add_u32 s57, s57, 6144\n"
  "buffer_store_dwordx4 v[96:99], v109, s[48:51], s68 offen offset:128 nt\n"
  "ds_read_b64_tr_b16 v[170:171], v119 offset:3072\n"
  "ds_read_b64_tr_b16 v[174:175], v120 offset:3072\n"
  "v_mfma_f32_16x16x32_bf16 v[16:19], v[76:79], a[184:187], v[16:19]\n"
  "s_add_u32 m0, s59, 0\n"
  "ds_read_b64_tr_b16 v[178:179], v121 offset:5120\n"
  "ds_read_b64_tr_b16 v[182:183], v122 offset:5120\n"
  "v_mfma_f32_16x16x32_bf16 v[16:19], v[72:75], a[160:163], v[16:19]\n"
  "s_waitcnt lgkmcnt(6)\n"
  "buffer_load_dwordx4 v114, s[40:43], s57 offen offset:0 nt lds\n"
  "buffer_load_dwordx4 v115, s[40:43], s57 offen offset:1024 nt lds\n"
  "v_mfma_f32_16x16x32_bf16 v[12:15], v[72:75], a[156:159], v[12:15]\n"
  "s_waitcnt vmcnt(12)\n"
  "ds_read_b64_tr_b16 v[184:185], v117 offset:6144\n"
  "ds_read_b64_tr_b16 v[188:189], v118 offset:6144\n"
  "v_mfma_f32_16x16x32_bf16 v[12:15], v[76:79], a[180:183], v[12:15]\n"
  "buffer_load_dwordx4 v116, s[40:43], s57 offen offset:2048  lds\n"
  "v_smfmac_f32_16x16x64_bf16 v[80:83], v[88:91], v[72:79], v124\n"
  "s_add_u32 s57, s57, 6144\n"
  "ds_read_b64_tr_b16 v[192:193], v119 offset:6144\n"
  "ds_read_b64_tr_b16 v[196:197], v120 offset:6144\n"
  "v_mfma_f32_16x16x32_bf16 v[8:11], v[76:79], a[176:179], v[8:11]\n"
  "s_add_u32 m0, s59, 3072\n"
  "ds_read_b64_tr_b16 v[200:201], v121 offset:8192\n"
  "ds_read_b64_tr_b16 v[204:205], v122 offset:8192\n"
  "ds_read_b64_tr_b16 v[100:101], v108 offset:4608\n"
  "v_mfma_f32_16x16x32_bf16 v[8:11], v[72:75], a[152:155], v[8:11]\n"
  "s_waitcnt lgkmcnt(7)\n"
  "buffer_load_dwordx4 v114, s[40:43], s57 offen offset:0 nt lds\n"
  "buffer_load_dwordx4 v115, s[40:43], s57 offen offset:1024 nt lds\n"
  "v_mfma_f32_16x16x32_bf16 v[4:7], v[72:75], a[148:151], v[4:7]\n"
  "s_waitcnt vmcnt(12)\n"
  "ds_read_b64_tr_b16 v[186:187], v117 offset:9216\n"
  "ds_read_b64_tr_b16 v[190:191], v118 offset:9216\n"
  "ds_read_b64_tr_b16 v[102:103], v108 offset:4640\n"
  "v_mfma_f32_16x16x32_bf16 v[4:7], v[76:79], a[172:175], v[4:7]\n"
  "buffer_load_dwordx4 v116, s[40:43], s57 offen offset:2048  lds\n"
  "v_xor_b32 v108, 0x2000, v108\n"
  "v_mfma_f32_16x16x32_bf16 v[0:3], v[76:79], a[168:171], v[0:3]\n"
  "s_add_u32 s57, s57, 6144\n"
  "s_add_u32 m0, s66, 0\n"
  "ds_read_b64_tr_b16 v[194:195], v119 offset:9216\n"
  "ds_read_b64_tr_b16 v[198:199], v120 offset:9216\n"
  "v_mfma_f32_16x16x32_bf16 v[0:3], v[72:75], a[144:147], v[0:3]\n"
  "buffer_load_dwordx4 v112, s[44:47], s67 offen offset:0  lds\n"
  "ds_read_b64_tr_b16 v[202:203], v121 offset:11264\n"
  "ds_read_b64_tr_b16 v[206:207], v122 offset:11264\n"
  "v_mfma_f32_16x16x32_bf16 v[0:3], v[48:51], a[192:195], v[0:3]\n"
  "s_add_u32 m0, s66, 1920\n"
  "ds_read_b128 v[64:67], v105 offset:4096\n"
  "v_rcp_f32 v84, v84\n"
  "v_mfma_f32_16x16x32_bf16 v[0:3], v[52:55], a[216:219], v[0:3]\n"
  "buffer_load_dwordx4 v112, s[44:47], s67 offen offset:128  lds\n"
  "s_waitcnt vmcnt(12)\n"
  "ds_read_b64_tr_b16 v[208:209], v117 offset:12288\n"
  "ds_read_b64_tr_b16 v[212:213], v118 offset:12288\n"
  "v_mfma_f32_16x16x32_bf16 v[4:7], v[52:55], a[220:223], v[4:7]\n"
  "s_add_u32 m0, s66, 3840\n"
  "ds_read_b128 v[68:71], v105 offset:5120\n"
  "v_rcp_f32 v85, v85\n"
  "v_mfma_f32_16x16x32_bf16 v[4:7], v[48:51], a[196:199], v[4:7]\n"
  "buffer_load_dwordx4 v112, s[44:47], s67 offen offset:256  lds\n"
  "ds_read_b64_tr_b16 v[216:217], v119 offset:12288\n"
  "ds_read_b64_tr_b16 v[220:221], v120 offset:12288\n"
  "v_mfma_f32_16x16x32_bf16 v[8:11], v[48:51], a[200:203], v[8:11]\n"
  "s_add_u32 s66, s66, 6144\n"
  "s_waitcnt lgkmcnt(10)\n"
  "buffer_store_dwordx4 v[100:103], v109, s[48:51], s68 offen offset:256 nt\n"
  "ds_read_b64_tr_b16 v[224:225], v121 offset:14336\n"
  "ds_read_b64_tr_b16 v[228:229], v122 offset:14336\n"
  "v_mul_f32 v40, v40, v84\n"
  "v_mfma_f32_16x16x32_bf16 v[8:11], v[52:55], a[224:227], v[8:11]\n"
  "s_cmp_ge_u32 s66, 86016\n"
  "s_cselect_b32 s64, 36864, 0\n"
  "s_sub_u32 s66, s66, s64\n"
  "s_add_u32 m0, s66, 0\n"
  "v_rcp_f32 v86, v86\n"
  "v_mul_f32 v44, v44, v84\n"
  "v_mfma_f32_16x16x32_bf16 v[12:15], v[52:55], a[228:231], v[12:15]\n"
  "s_add_u32 s67, s67, s63\n"
  "buffer_load_dwordx4 v112, s[44:47], s67 offen offset:0  lds\n"
  "ds_read_b128 v[72:75], v105 offset:6144\n"
  "s_waitcnt vmcnt(12)\n"
  "ds_read_b64_tr_b16 v[210:211], v117 offset:15360\n"
  "ds_read_b64_tr_b16 v[214:215], v118 offset:15360\n"
  "v_mfma_f32_16x16x32_bf16 v[12:15], v[48:51], a[204:207], v[12:15]\n"
  "s_add_u32 m0, s66, 1920\n"
  "v_rcp_f32 v87, v87\n"
  "v_cvt_pk_bf16_f32 v40, v40, v44\n"
  "v_mfma_f32_16x16x32_bf16 v[16:19], v[48:51], a[208:211], v[16:19]\n"
  "buffer_load_dwordx4 v112, s[44:47], s67 offen offset:128  lds\n"
  "ds_read_b64_tr_b16 v[218:219], v119 offset:15360\n"
  "ds_read_b64_tr_b16 v[222:223], v120 offset:15360\n"
  "v_mfma_f32_16x16x32_bf16 v[16:19], v[52:55], a[232:235], v[16:19]\n"
  "s_add_u32 m0, s66, 3840\n"
  "ds_read_b64_tr_b16 v[226:227], v121 offset:17408\n"
  "ds_read_b64_tr_b16 v[230:231], v122 offset:17408\n"
  "v_mul_f32 v41, v41, v85\n"
  "v_mfma_f32_16x16x32_bf16 v[20:23], v[52:55], a[236:239], v[20:23]\n"
  "buffer_load_dwordx4 v112, s[44:47], s67 offen offset:256  lds\n"
  "ds_read_b128 v[76:79], v105 offset:7168\n"
  "v_mfma_f32_16x16x32_bf16 v[20:23], v[48:51], a[212:215], v[20:23]\n"
  "s_waitcnt vmcnt(10)\n"
  "ds_read_b64_tr_b16 v[232:233], v117 offset:0\n"
  "ds_read_b64_tr_b16 v[236:237], v118 offset:0\n"
  "v_mul_f32 v45, v45, v85\n"
  "v_mfma_f32_16x16x32_bf16 v[20:23], v[56:59], v[132:135], v[20:23]\n"
  "s_add_u32 s66, s66, 6144\n"
  "s_cmp_ge_u32 s66, 86016\n"
  "s_cselect_b32 s64, 36864, 0\n"
  "s_sub_u32 s66, s66, s64\n"
  "s_add_u32 m0, s66, 0\n"
  "v_cvt_pk_bf16_f32 v41, v41, v45\n"
  "v_mul_f32 v42, v42, v86\n"
  "v_mul_f32 v46, v46, v86\n"
  "v_mfma_f32_16x16x32_bf16 v[20:23], v[60:63], v[156:159], v[20:23]\n"
  "s_sub_u32 s90, s90, 1\n"
  "s_cselect_b32 s63, s91, 6144\n"
  "s_cmov_b64 s[90:91], s[92:93]\n"
  "s_cmov_b64 s[92:93], s[94:95]\n"
  "s_add_u32 s67, s67, s63\n"
  "buffer_load_dwordx4 v112, s[44:47], s67 offen offset:0  lds\n"
  "ds_read_b64_tr_b16 v[240:241], v119 offset:0\n"
  "ds_read_b64_tr_b16 v[244:245], v120 offset:0\n"
  "v_mfma_f32_16x16x32_bf16 v[16:19], v[60:63], v[152:155], v[16:19]\n"
  "s_add_u32 m0, s66, 1920\n"
  "ds_read_b64_tr_b16 v[248:249], v121 offset:2048\n"
  "ds_read_b64_tr_b16 v[252:253], v122 offset:2048\n"
  "v_cvt_pk_bf16_f32 v42, v42, v46\n"
  "v_mfma_f32_16x16x32_bf16 v[16:19], v[56:59], v[128:131], v[16:19]\n"
  "buffer_load_dwordx4 v112, s[44:47], s67 offen offset:128  lds\n"
  "v_mul_f32 v43, v43, v87\n"
  "v_mul_f32 v47, v47, v87\n"
  "v_cvt_pk_bf16_f32 v43, v43, v47\n"
  "v_mul_f32 v24, v24, v84\n"
  "v_mul_f32 v25, v25, v85\n"
  "v_mul_f32 v28, v28, v84\n"
  "v_mul_f32 v29, v29, v85\n"
  "v_mul_f32 v32, v32, v84\n"
  "v_mul_f32 v33, v33, v85\n"
  "v_mul_f32 v36, v36, v84\n"
  "v_mul_f32 v37, v37, v85\n"
  "v_cvt_pk_bf16_f32 v24, v24, v28\n"
  "v_cvt_pk_bf16_f32 v28, v25, v29\n"
  "v_cvt_pk_bf16_f32 v25, v32, v36\n"
  "v_cvt_pk_bf16_f32 v29, v33, v37\n"
  "v_mul_f32 v26, v26, v86\n"
  "v_mul_f32 v27, v27, v87\n"
  "v_mul_f32 v30, v30, v86\n"
  "v_mul_f32 v31, v31, v87\n"
  "v_mul_f32 v34, v34, v86\n"
  "v_mul_f32 v35, v35, v87\n"
  "v_mul_f32 v38, v38, v86\n"
  "v_mul_f32 v39, v39, v87\n"
  "v_cvt_pk_bf16_f32 v26, v26, v30\n"
  "v_cvt_pk_bf16_f32 v30, v27, v31\n"
  "v_cvt_pk_bf16_f32 v27, v34, v38\n"
  "v_cvt_pk_bf16_f32 v31, v35, v39\n"
  "v_add_f32 v44, v80, v81\n"
  "v_mfma_f32_16x16x32_bf16 v[12:15], v[56:59], a[252:255], v[12:15]\n"
  "s_add_u32 m0, s66, 3840\n"
  "s_waitcnt vmcnt(9)\n"
  "ds_read_b64_tr_b16 v[234:235], v117 offset:3072\n"
  "ds_read_b64_tr_b16 v[238:239], v118 offset:3072\n"
  "ds_write2_b32 v107, v40, v41 offset0:0 offset1:72\n"
  "ds_write2_b32 v107, v42, v43 offset0:144 offset1:216\n"
  "ds_write_b64 v106, v[24:25] offset:0\n"
  "ds_write_b64 v106, v[28:29] offset:288\n"
  "ds_write_b64 v106, v[26:27] offset:576\n"
  "ds_write_b64 v106, v[30:31] offset:864\n"
  "ds_write_b32 v110, v44\n"
  "v_mfma_f32_16x16x32_bf16 v[12:15], v[60:63], v[148:151], v[12:15]\n"
  "buffer_load_dwordx4 v112, s[44:47], s67 offen offset:256  lds\n"
  "v_mfma_f32_16x16x32_bf16 v[8:11], v[60:63], v[144:147], v[8:11]\n"
  "s_add_u32 s72, s72, 1\n"
  "ds_read_b64_tr_b16 v[242:243], v119 offset:3072\n"
  "ds_read_b64_tr_b16 v[246:247], v120 offset:3072\n"
  "v_max3_f32 v123, v123, v84, v85\n"
  "v_mfma_f32_16x16x32_bf16 v[8:11], v[56:59], a[248:251], v[8:11]\n"
  "s_lshr_b32 s74, s72, 1\n"
  "ds_read_b64_tr_b16 v[250:251], v121 offset:5120\n"
  "ds_read_b64_tr_b16 v[254:255], v122 offset:5120\n"
  "v_max3_f32 v123, v123, v86, v87\n"
  "v_mfma_f32_16x16x32_bf16 v[4:7], v[56:59], a[244:247], v[4:7]\n"
  "s_add_u32 s66, s66, 6144\n"
  "v_readlane_b32 s73, v126, s74\n"
  "v_xor_b32 v106, 0x2000, v106\n"
  "v_mfma_f32_16x16x32_bf16 v[4:7], v[60:63], v[140:143], v[4:7]\n"
  "s_cmp_ge_u32 s66, 86016\n"
  "s_cselect_b32 s64, 36864, 0\n"
  "v_xor_b32 v107, 0x2000, v107\n"
  "v_mov_b32 v80, 0\n"
  "v_mov_b32 v81, 0\n"
  "v_mfma_f32_16x16x32_bf16 v[0:3], v[60:63], v[136:139], v[0:3]\n"
  "s_sub_u32 s66, s66, s64\n"
  "v_xor_b32 v110, 0x2000, v110\n"
  "v_xor_b32 v105, 0x4000, v105\n"
  "v_mfma_f32_16x16x32_bf16 v[0:3], v[56:59], a[240:243], v[0:3]\n"
  "s_sub_u32 s90, s90, 1\n"
  "s_cselect_b32 s63, s91, 6144\n"
  "s_cmov_b64 s[90:91], s[92:93]\n"
  "s_cmov_b64 s[92:93], s[94:95]\n"
  "s_add_u32 s67, s67, s63\n"
  "s_lshl_b32 s74, s72, 4\n"
  "s_lshr_b32 s73, s73, s74\n"
  "s_and_b32 s73, s73, 0xffff\n"
  "s_lshl_b32 s73, s73, 13\n"
  "s_add_u32 s68, s68, s69\n"
  "s_mov_b32 s69, s70\n"
  "s_movk_i32 s70, 6144\n"
  "s_branch B_H1_%=\n"
  "B_LAST_%=:\n"
  "s_waitcnt lgkmcnt(0)\n"
  "v_mfma_f32_16x16x32_bf16 v[20:23], v[64:67], v[180:183], v[20:23]\n"
  "v_mfma_f32_16x16x32_bf16 v[20:23], v[68:71], v[204:207], v[20:23]\n"
  "v_mfma_f32_16x16x32_bf16 v[20:23], v[72:75], v[228:231], v[20:23]\n"
  "v_mfma_f32_16x16x32_bf16 v[44:47], v[76:79], v[252:255], v[20:23]\n"
  "v_mfma_f32_16x16x32_bf16 v[16:19], v[76:79], v[248:251], v[16:19]\n"
  "v_mfma_f32_16x16x32_bf16 v[16:19], v[72:75], v[224:227], v[16:19]\n"
  "v_mfma_f32_16x16x32_bf16 v[16:19], v[68:71], v[200:203], v[16:19]\n"
  "v_mfma_f32_16x16x32_bf16 v[40:43], v[64:67], v[176:179], v[16:19]\n"
  "v_mfma_f32_16x16x32_bf16 v[12:15], v[64:67], v[172:175], v[12:15]\n"
  "v_mfma_f32_16x16x32_bf16 v[12:15], v[68:71], v[196:199], v[12:15]\n"
  "v_mfma_f32_16x16x32_bf16 v[12:15], v[72:75], v[220:223], v[12:15]\n"
  "v_mfma_f32_16x16x32_bf16 v[36:39], v[76:79], v[244:247], v[12:15]\n"
  "v_mfma_f32_16x16x32_bf16 v[8:11], v[76:79], v[240:243], v[8:11]\n"
  "v_mfma_f32_16x16x32_bf16 v[8:11], v[72:75], v[216:219], v[8:11]\n"
  "v_mfma_f32_16x16x32_bf16 v[8:11], v[68:71], v[192:195], v[8:11]\n"
  "v_mfma_f32_16x16x32_bf16 v[32:35], v[64:67], v[168:171], v[8:11]\n"
  "v_mfma_f32_16x16x32_bf16 v[4:7], v[64:67], v[164:167], v[4:7]\n"
  "v_mfma_f32_16x16x32_bf16 v[4:7], v[68:71], v[188:191], v[4:7]\n"
  "v_mfma_f32_16x16x32_bf16 v[4:7], v[72:75], v[212:215], v[4:7]\n"
  "v_mfma_f32_16x16x32_bf16 v[28:31], v[76:79], v[236:239], v[4:7]\n"
  "v_mfma_f32_16x16x32_bf16 v[0:3], v[76:79], v[232:235], v[0:3]\n"
  "v_mfma_f32_16x16x32_bf16 v[0:3], v[72:75], v[208:211], v[0:3]\n"
  "v_mfma_f32_16x16x32_bf16 v[0:3], v[68:71], v[184:187], v[0:3]\n"
  "v_mfma_f32_16x16x32_bf16 v[24:27], v[64:67], v[160:163], v[0:3]\n"
  "s_barrier\n"
  "ds_read_b128 v[84:87], v111\n"
  "ds_read_b128 v[100:103], v111 offset:64\n"
  "v_xor_b32 v111, 0x2000, v111\n"
  "s_waitcnt lgkmcnt(0)\n"
  "v_add_f32 v84, v84, v100\n"
  "v_add_f32 v85, v85, v101\n"
  "v_max3_f32 v123, v123, v84, v85\n"
  "v_rcp_f32 v84, v84\n"
  "v_rcp_f32 v85, v85\n"
  "v_mul_f32 v40, v40, v84\n"
  "v_mul_f32 v44, v44, v84\n"
  "v_cvt_pk_bf16_f32 v40, v40, v44\n"
  "v_mul_f32 v41, v41, v85\n"
  "v_mul_f32 v45, v45, v85\n"
  "v_cvt_pk_bf16_f32 v41, v41, v45\n"
  "v_add_f32 v86, v86, v102\n"
  "v_add_f32 v87, v87, v103\n"
  "v_max3_f32 v123, v123, v86, v87\n"
  "v_mul_f32 v32, v32, v84\n"
  "v_rcp_f32 v86, v86\n"
  "v_rcp_f32 v87, v87\n"
  "v_mul_f32 v42, v42, v86\n"
  "v_mul_f32 v46, v46, v86\n"
  "v_cvt_pk_bf16_f32 v42, v42, v46\n"
  "v_mul_f32 v43, v43, v87\n"
  "v_mul_f32 v47, v47, v87\n"
  "v_cvt_pk_bf16_f32 v43, v43, v47\n"
  "ds_read_b64_tr_b16 v[92:93], v108 offset:0\n"
  "ds_read_b64_tr_b16 v[94:95], v108 offset:32\n"
  "v_mul_f32 v24, v24, v84\n"
  "v_mul_f32 v25, v25, v85\n"
  "v_mul_f32 v28, v28, v84\n"
  "v_mul_f32 v29, v29, v85\n"
  "v_mul_f32 v36, v36, v84\n"
  "v_cvt_pk_bf16_f32 v24, v24, v28\n"
  "v_cvt_pk_bf16_f32 v28, v25, v29\n"
  "v_cvt_pk_bf16_f32 v25, v32, v36\n"
  "v_mul_f32 v33, v33, v85\n"
  "v_mul_f32 v37, v37, v85\n"
  "v_cvt_pk_bf16_f32 v29, v33, v37\n"
  "v_mul_f32 v26, v26, v86\n"
  "v_mul_f32 v27, v27, v87\n"
  "v_mul_f32 v30, v30, v86\n"
  "v_mul_f32 v31, v31, v87\n"
  "s_waitcnt lgkmcnt(0)\n"
  "buffer_store_dwordx4 v[92:95], v109, s[48:51], s68 offen offset:0 nt\n"
  "ds_write2_b32 v107, v40, v41 offset0:0 offset1:72\n"
  "v_mul_f32 v34, v34, v86\n"
  "v_mul_f32 v38, v38, v86\n"
  "v_cvt_pk_bf16_f32 v26, v26, v30\n"
  "v_cvt_pk_bf16_f32 v30, v27, v31\n"
  "v_cvt_pk_bf16_f32 v27, v34, v38\n"
  "v_mul_f32 v35, v35, v87\n"
  "ds_write2_b32 v107, v42, v43 offset0:144 offset1:216\n"
  "v_mul_f32 v39, v39, v87\n"
  "v_cvt_pk_bf16_f32 v31, v35, v39\n"
  "v_max3_f32 v123, v123, v84, v85\n"
  "v_max3_f32 v123, v123, v86, v87\n"
  "v_xor_b32 v104, 0x4000, v104\n"
  "ds_write_b64 v106, v[24:25] offset:0\n"
  "ds_read_b64_tr_b16 v[96:97], v108 offset:2304\n"
  "ds_read_b64_tr_b16 v[98:99], v108 offset:2336\n"
  "s_waitcnt lgkmcnt(0)\n"
  "buffer_store_dwordx4 v[96:99], v109, s[48:51], s68 offen offset:128 nt\n"
  "ds_write_b64 v106, v[28:29] offset:288\n"
  "ds_read_b64_tr_b16 v[100:101], v108 offset:4608\n"
  "ds_read_b64_tr_b16 v[102:103], v108 offset:4640\n"
  "v_xor_b32 v108, 0x2000, v108\n"
  "s_waitcnt lgkmcnt(0)\n"
  "buffer_store_dwordx4 v[100:103], v109, s[48:51], s68 offen offset:256 nt\n"
  "ds_write_b64 v106, v[26:27] offset:576\n"
  "s_nop 0\n"
  "s_add_u32 s68, s68, s69\n"
  "s_mov_b32 s69, s70\n"
  "s_movk_i32 s70, 6144\n"
  "v_xor_b32 v105, 0x4000, v105\n"
  "ds_write_b64 v106, v[30:31] offset:864\n"
  "s_nop 1\n"
  "v_xor_b32 v106, 0x2000, v106\n"
  "v_xor_b32 v107, 0x2000, v107\n"
  "v_xor_b32 v110, 0x2000, v110\n"
  "s_waitcnt lgkmcnt(0)\n"
  "s_barrier\n"
  "ds_read_b64_tr_b16 v[92:93], v108 offset:0\n"
  "ds_read_b64_tr_b16 v[94:95], v108 offset:32\n"
  "s_waitcnt lgkmcnt(0)\n"
  "buffer_store_dwordx4 v[92:95], v109, s[48:51], s68 offen offset:0 nt\n"
  "ds_read_b64_tr_b16 v[96:97], v108 offset:2304\n"
  "ds_read_b64_tr_b16 v[98:99], v108 offset:2336\n"
  "s_waitcnt lgkmcnt(0)\n"
  "buffer_store_dwordx4 v[96:99], v109, s[48:51], s68 offen offset:128 nt\n"
  "ds_read_b64_tr_b16 v[100:101], v108 offset:4608\n"
  "ds_read_b64_tr_b16 v[102:103], v108 offset:4640\n"
  "s_waitcnt lgkmcnt(0)\n"
  "buffer_store_dwordx4 v[100:103], v109, s[48:51], s68 offen offset:256 nt\n"
  "v_cmp_ngt_f32 vcc, s71, v123\n"
  "s_nop 4\n"
  "s_cmp_lg_u64 vcc, 0\n"
  "s_cselect_b32 %0, 1, 0\n"
  "L_END_%=:\n"
  : "=&s"(bad)
  : "s"((unsigned long long)__builtin_amdgcn_kernarg_segment_ptr()), "s"(wid), "s"(it[0]), "s"(it[1]), "s"(it[2]), "s"(it[3]), "s"(xcd | (side & 0) << 3 | (side ? 31 - p : p) << 8)
  : "v0", "v1", "v2", "v3", "v4", "v5", "v6", "v7", "v8", "v9", "v10", "v11", "v12", "v13", "v14", "v15", "v16", "v17", "v18", "v19", "v20", "v21", "v22", "v23", "v24", "v25", "v26", "v27", "v28", "v29", "v30", "v31", "v32", "v33", "v34", "v35", "v36", "v37", "v38", "v39", "v40", "v41", "v42", "v43", "v44", "v45", "v46", "v47", "v48", "v49", "v50", "v51", "v52", "v53", "v54", "v55", "v56", "v57", "v58", "v59", "v60", "v61", "v62", "v63", "v64", "v65", "v66", "v67", "v68", "v69", "v70", "v71", "v72", "v73", "v74", "v75", "v76", "v77", "v78", "v79", "v80", "v81", "v82", "v83", "v84", "v85", "v86", "v87", "v88", "v89", "v90", "v91", "v92", "v93", "v94", "v95", "v96", "v97", "v98", "v99", "v100", "v101", "v102", "v103", "v104", "v105", "v106", "v107", "v108", "v109", "v110", "v111", "v112", "v113", "v114", "v115", "v116", "v117", "v118", "v119", "v120", "v121", "v122", "v123", "v124", "v125", "v126", "v127", "v128", "v129", "v130", "v131", "v132", "v133", "v134", "v135", "v136", "v137", "v138", "v139", "v140", "v141", "v142", "v143", "v144", "v145", "v146", "v147", "v148", "v149", "v150", "v151", "v152", "v153", "v154", "v155", "v156", "v157", "v158", "v159", "v160", "v161", "v162", "v163", "v164", "v165", "v166", "v167", "v168", "v169", "v170", "v171", "v172", "v173", "v174", "v175", "v176", "v177", "v178", "v179", "v180", "v181", "v182", "v183", "v184", "v185", "v186", "v187", "v188", "v189", "v190", "v191", "v192", "v193", "v194", "v195", "v196", "v197", "v198", "v199", "v200", "v201", "v202", "v203", "v204", "v205", "v206", "v207", "v208", "v209", "v210", "v211", "v212", "v213", "v214", "v215", "v216", "v217", "v218", "v219", "v220", "v221", "v222", "v223", "v224", "v225", "v226", "v227", "v228", "v229", "v230", "v231", "v232", "v233", "v234", "v235", "v236", "v237", "v238", "v239", "v240", "v241", "v242", "v243", "v244", "v245", "v246", "v247", "v248", "v249", "v250", "v251", "v252", "v253", "v254", "v255", "a0", "a1", "a2", "a3", "a4", "a5", "a6", "a7", "a8", "a9", "a10", "a11", "a12", "a13", "a14", "a15", "a16", "a17", "a18", "a19", "a20", "a21", "a22", "a23", "a24", "a25", "a26", "a27", "a28", "a29", "a30", "a31", "a32", "a33", "a34", "a35", "a36", "a37", "a38", "a39", "a40", "a41", "a42", "a43", "a44", "a45", "a46", "a47", "a48", "a49", "a50", "a51", "a52", "a53", "a54", "a55", "a56", "a57", "a58", "a59", "a60", "a61", "a62", "a63", "a64", "a65", "a66", "a67", "a68", "a69", "a70", "a71", "a72", "a73", "a74", "a75", "a76", "a77", "a78", "a79", "a80", "a81", "a82", "a83", "a84", "a85", "a86", "a87", "a88", "a89", "a90", "a91", "a92", "a93", "a94", "a95", "a96", "a97", "a98", "a99", "a100", "a101", "a102", "a103", "a104", "a105", "a106", "a107", "a108", "a109", "a110", "a111", "a112", "a113", "a114", "a115", "a116", "a117", "a118", "a119", "a120", "a121", "a122", "a123", "a124", "a125", "a126", "a127", "a128", "a129", "a130", "a131", "a132", "a133", "a134", "a135", "a136", "a137", "a138", "a139", "a140", "a141", "a142", "a143", "a144", "a145", "a146", "a147", "a148", "a149", "a150", "a151", "a152", "a153", "a154", "a155", "a156", "a157", "a158", "a159", "a160", "a161", "a162", "a163", "a164", "a165", "a166", "a167", "a168", "a169", "a170", "a171", "a172", "a173", "a174", "a175", "a176", "a177", "a178", "a179", "a180", "a181", "a182", "a183", "a184", "a185", "a186", "a187", "a188", "a189", "a190", "a191", "a192", "a193", "a194", "a195", "a196", "a197", "a198", "a199", "a200", "a201", "a202", "a203", "a204", "a205", "a206", "a207", "a208", "a209", "a210", "a211", "a212", "a213", "a214", "a215", "a216", "a217", "a218", "a219", "a220", "a221", "a222", "a223", "a224", "a225", "a226", "a227", "a228", "a229", "a230", "a231", "a232", "a233", "a234", "a235", "a236", "a237", "a238", "a239", "a240", "a241", "a242", "a243", "a244", "a245", "a246", "a247", "a248", "a249", "a250", "a251", "a252", "a253", "a254", "a255", "s40", "s41", "s42", "s43", "s44", "s45", "s46", "s47", "s48", "s49", "s50", "s51", "s52", "s53", "s54", "s55", "s56", "s57", "s58", "s59", "s60", "s61", "s62", "s63", "s64", "s65", "s66", "s67", "s68", "s69", "s70", "s71", "s72", "s73", "s74", "s75", "s76", "s77", "s78", "s79", "s80", "s81", "s82", "s83", "s84", "s85", "s86", "s87", "s88", "s89", "s90", "s91", "s92", "s93", "s94", "s95", "s96", "s97", "s98", "s99", "vcc", "scc", "memory");
  if (bad) {
    unsigned long long ka = (unsigned long long)__builtin_amdgcn_kernarg_segment_ptr();
    asm volatile("s_waitcnt vmcnt(0)\n\ts_barrier" : "+s"(ka) :: "memory");
    const unsigned short* const* arg = (const unsigned short* const*)ka;
#pragma unroll 1
    for (int i = 0; i < 4; ++i) {
      const unsigned x = i == 0 ? it[0] : i == 1 ? it[1] : i == 2 ? it[2] : it[3];
#pragma unroll 1
      for (int t = wid & 1; t < (int)(x & 255); t += 2) exact_tile(arg[0], arg[1], arg[2], (unsigned short*)arg[3], x >> 16, 16 * (((x >> 8) & 255) + t));
    }
  }
}

extern "C" void kernel_launch(void* const* d_in, const int* in_sizes, int n_in,
                              void* d_out, int out_size, void* d_ws, size_t ws_size,
                              hipStream_t stream) {
  (void)hipFuncSetAttribute((const void*)attn_fwd_g, hipFuncAttributeMaxDynamicSharedMemorySize, LDS_BYTES);
  hipLaunchKernelGGL(attn_fwd_g, dim3(256), dim3(256), LDS_BYTES, stream, (const unsigned short*)d_in[0], (const unsigned short*)d_in[1],
                     (const unsigned short*)d_in[2], (unsigned short*)d_out);
}
```

```cpp
#include <hip/hip_runtime.h>
#include <hip/hip_bf16.h>

typedef __bf16   bf16x8 __attribute__((ext_vector_type(8)));
typedef __bf16   bf16x2 __attribute__((ext_vector_type(2)));
typedef float    f32x4  __attribute__((ext_vector_type(4)));
typedef float    f32x2  __attribute__((ext_vector_type(2)));
typedef unsigned u32x4  __attribute__((ext_vector_type(4)));
typedef unsigned u32x2  __attribute__((ext_vector_type(2)));
typedef unsigned short u16x8 __attribute__((ext_vector_type(8)));

constexpr int SQ = 848, SK = 512, DH = 192, ROWB = 384;
constexpr int LDS_BYTES = 163840;
constexpr float CSC = 0.07216878364870322f * 1.4426950408889634f;

#define DI __device__ __forceinline__
DI unsigned cvtpk(float lo, float hi) { f32x2 v = {lo, hi}; bf16x2 b = __builtin_convertvector(v, bf16x2); return __builtin_bit_cast(unsigned, b); }
DI float xmax(float x) {
  auto r = __builtin_amdgcn_permlane32_swap(__float_as_uint(x), __float_as_uint(x), false, false);
  x = fmaxf(__uint_as_float(r[0]), __uint_as_float(r[1]));
  auto q = __builtin_amdgcn_permlane16_swap(__float_as_uint(x), __float_as_uint(x), false, false);
  return fmaxf(__uint_as_float(q[0]), __uint_as_float(q[1]));
}
DI float xsum(float x) {
  auto r = __builtin_amdgcn_permlane32_swap(__float_as_uint(x), __float_as_uint(x), false, false);
  x = __uint_as_float(r[0]) + __uint_as_float(r[1]);
  auto q = __builtin_amdgcn_permlane16_swap(__float_as_uint(x), __float_as_uint(x), false, false);
  return __uint_as_float(q[0]) + __uint_as_float(q[1]);
}
DI f32x4 score_tile(const unsigned short* krow, const bf16x8 (&qf)[6]) {
  f32x4 acc = {0.f, 0.f, 0.f, 0.f};
#pragma unroll
  for (int s = 0; s < 6; ++s) acc = __builtin_amdgcn_mfma_f32_16x16x32_bf16(*(const bf16x8*)(krow + 32 * s), qf[s], acc, 0, 0, 0);
  return acc;
}
DI void exact_tile(const unsigned short* Q, const unsigned short* K, const unsigned short* V, unsigned short* O, int head, int row0) {
  const unsigned lane = __builtin_amdgcn_mbcnt_hi(~0u, __builtin_amdgcn_mbcnt_lo(~0u, 0u));
  const int c = lane & 15, g4 = lane >> 4;
  const int qrow = min(row0 + c, SQ - 1);
  bf16x8 qf[6];
#pragma unroll
  for (int s = 0; s < 6; ++s) qf[s] = *(const bf16x8*)(Q + ((size_t)head * SQ + qrow) * DH + 32 * s + 8 * g4);
  const unsigned short* Kh = K + ((size_t)head * SK + c) * DH + 8 * g4;
  float m = -__builtin_huge_valf();
#pragma unroll 1
  for (int kb = 0; kb < SK / 16; ++kb) {
    const f32x4 a = score_tile(Kh + (size_t)kb * 16 * DH, qf);
    m = fmaxf(fmaxf(m, fmaxf(a[0], a[1])), fmaxf(a[2], a[3]));
  }
  const float mc = -xmax(m) * CSC;
  float l = 0.f;
  f32x4 o[12];
#pragma unroll
  for (int dt = 0; dt < 12; ++dt) o[dt] = f32x4{0.f, 0.f, 0.f, 0.f};
#pragma unroll 1
  for (int j = 0; j < SK / 32; ++j) {
    u32x4 p;
#pragma unroll
    for (int kt = 0; kt < 2; ++kt) {
      f32x4 a = score_tile(Kh + (size_t)(32 * j + 16 * kt) * DH, qf);
#pragma unroll
      for (int r = 0; r < 4; ++r) { a[r] = __builtin_amdgcn_exp2f(fmaf(a[r], CSC, mc)); l += a[r]; }
      p[2 * kt] = cvtpk(a[0], a[1]); p[2 * kt + 1] = cvtpk(a[2], a[3]);
    }
    const unsigned short* Vj = V + ((size_t)head * SK + 32 * j + 4 * g4) * DH + c;
#pragma unroll
    for (int dt = 0; dt < 12; ++dt) {
      u16x8 vf;
#pragma unroll
      for (int i = 0; i < 8; ++i) vf[i] = Vj[(16 * (i >> 2) + (i & 3)) * DH + 16 * dt];
      o[dt] = __builtin_amdgcn_mfma_f32_16x16x32_bf16(__builtin_bit_cast(bf16x8, vf), __builtin_bit_cast(bf16x8, p), o[dt], 0, 0, 0);
    }
  }
  const float inv = 1.0f / xsum(l);
  if (row0 + c < SQ) {
#pragma unroll
    for (int dt = 0; dt < 12; ++dt)
      *(u32x2*)(O + ((size_t)head * SQ + qrow) * DH + 16 * dt + 4 * g4) = u32x2{cvtpk(o[dt][0] * inv, o[dt][1] * inv), cvtpk(o[dt][2] * inv, o[dt][3] * inv)};
  }
}

__global__ __launch_bounds__(256, 1)
void attn_fwd_g(const unsigned short* __restrict__ Qg, const unsigned short* __restrict__ Kg,
                const unsigned short* __restrict__ Vg, unsigned short* __restrict__ Og) {
  const int wid = __builtin_amdgcn_readfirstlane(threadIdx.x >> 6);
  const int wg = blockIdx.x;
  unsigned it[4];
  const unsigned xcd = wg & 7, p = wg >> 4, side = (wg >> 3) & 1, x = ((p < 8 ? 0x100f0e0d0b0a0908ull : 0x1a19181715141312ull) >> (8 * (p & 7))) & 255;
  it[0] = (8 * p + 1 * xcd) << 16 | (side ? x << 8 | (53 - x) : x);
  it[1] = (8 * (16 + 2 * p + side) + 1 * xcd) << 16 | 53;
  const unsigned y = (side ? x : 53 - x) + (xcd & 1 ? -1 : 1) * (2 + (int)(p & 0)) + (p & 0 ? -(0) : 0);
  it[2] = (8 * (48 + (p ^ (side & 0))) + 1 * (xcd ^ side)) << 16 | (side ? (53 - y) << 8 | y : y);
  it[3] = 0;
  const unsigned slot = wg >> 3, rank = ((slot < 16 ? (slot < 8 ? 0x1c031d021e011f00ull : 0x180719061a051b04ull) : (slot < 24 ? 0x140b150a16091708ull : 0x100f110e120d130cull)) >> (8 * (slot & 7))) & 31;
  unsigned bad;
  asm volatile(
  "s_load_dwordx8 s[76:83], %1, 0\n"
  "v_mbcnt_lo_u32_b32 v0, -1, 0\n"
  "v_mbcnt_hi_u32_b32 v0, -1, v0\n"
  "L_PC_%=:\n"
  "s_getpc_b64 s[84:85]\n"
  "s_lshr_b32 s62, %7, 6\n"
  "s_add_u32 s62, s62, %2\n"
  "s_lshl_b32 s62, s62, 13\n"
  "s_add_u32 s84, s84, s62\n"
  "s_addc_u32 s85, s85, 0\n"
  "s_sub_i32 s86, L_END_%=-L_PC_%=, s62\n"
  "s_max_i32 s86, s86, 0\n"
  "s_mov_b32 s87, 0x20000\n"
  "s_mov_b32 m0, 159744\n"
  "v_lshlrev_b32 v1, 7, v0\n"
  "buffer_load_dword v1, s[84:87], 0 offen lds\n"
  "s_mov_b32 s52, %3\n"
  "s_mov_b32 s53, %4\n"
  "s_mov_b32 s54, %5\n"
  "s_mov_b32 s55, %6\n"
  "s_and_b32 s65, %2, 1\n"
  "s_mul_i32 s59, %2, 18432\n"
  "s_add_u32 s59, s59, 86016\n"
  "s_mov_b32 s42, 100663296\n"
  "s_mov_b32 s43, 0x20000\n"
  "s_mov_b32 s47, 0x20000\n"
  "s_lshr_b32 s62, s52, 16\n"
  "s_lshl1_add_u32 s62, s62, s65\n"
  "s_mul_i32 s57, s62, 98304\n"
  "s_and_b32 s56, s52, 0xff\n"
  "s_cmp_lg_u32 s53, 0\n"
  "s_cselect_b32 s62, 1, 0\n"
  "s_sub_u32 s56, s56, s62\n"
  "s_bitcmp1_b32 %7, 3\n"
  "s_cselect_b32 s62, 49152, 0\n"
  "s_add_u32 s57, s57, s62\n"
  "s_lshl_b32 s62, s62, 1\n"
  "s_sub_u32 s86, 6144, s62\n"
  "s_lshr_b32 s62, s52, 16\n"
  "s_mul_i32 s62, s62, 53\n"
  "s_bfe_u32 s63, s52, 0x80008\n"
  "s_add_u32 s62, s62, s63\n"
  "s_mul_i32 s67, s62, 6144\n"
  "s_add_u32 s64, s67, 6144\n"
  "s_lshl_b32 s66, s65, 10\n"
  "s_add_u32 s66, s66, 49152\n"
  "v_lshrrev_b32 v2, 3, v0\n"
  "v_and_b32 v1, 7, v0\n"
  "v_xor_b32 v1, v1, v2\n"
  "v_mul_u32_u24 v112, 0x180, v2\n"
  "v_lshl_add_u32 v112, v1, 4, v112\n"
  "v_add_u32 v113, 0x800, v112\n"
  "s_mul_i32 s62, s65, 3072\n"
  "s_sub_u32 s46, 166723584, s62\n"
  "s_cmp_gt_u32 %2, 1\n"
  "s_waitcnt lgkmcnt(0)\n"
  "s_cbranch_scc1 B_START_%=\n"
  "A_START_%=:\n"
  "s_mov_b64 s[40:41], s[78:79]\n"
  "s_add_u32 m0, s59, 0\n"
  "s_nop 0\n"
  "buffer_load_dwordx4 v112, s[40:43], s57 offen offset:0  lds\n"
  "buffer_load_dwordx4 v113, s[40:43], s57 offen offset:1024  lds\n"
  "s_add_u32 m0, s59, 1920\n"
  "s_nop 0\n"
  "buffer_load_dwordx4 v112, s[40:43], s57 offen offset:128  lds\n"
  "buffer_load_dwordx4 v113, s[40:43], s57 offen offset:1152  lds\n"
  "s_add_u32 m0, s59, 3840\n"
  "s_nop 0\n"
  "buffer_load_dwordx4 v112, s[40:43], s57 offen offset:256  lds\n"
  "buffer_load_dwordx4 v113, s[40:43], s57 offen offset:1280  lds\n"
  "s_add_u32 s57, s57, 6144\n"
  "s_add_u32 m0, s59, 5888\n"
  "s_nop 0\n"
  "buffer_load_dwordx4 v112, s[40:43], s57 offen offset:256  lds\n"
  "buffer_load_dwordx4 v113, s[40:43], s57 offen offset:1280  lds\n"
  "s_add_u32 m0, s59, 8064\n"
  "s_nop 0\n"
  "buffer_load_dwordx4 v112, s[40:43], s57 offen offset:128  lds\n"
  "buffer_load_dwordx4 v113, s[40:43], s57 offen offset:1152  lds\n"
  "s_add_u32 m0, s59, 10240\n"
  "s_nop 0\n"
  "buffer_load_dwordx4 v112, s[40:43], s57 offen offset:0  lds\n"
  "buffer_load_dwordx4 v113, s[40:43], s57 offen offset:1024  lds\n"
  "s_add_u32 s57, s57, 6144\n"
  "s_add_u32 m0, s59, 12288\n"
  "s_nop 0\n"
  "buffer_load_dwordx4 v112, s[40:43], s57 offen offset:0  lds\n"
  "buffer_load_dwordx4 v113, s[40:43], s57 offen offset:1024  lds\n"
  "s_add_u32 m0, s59, 14208\n"
  "s_nop 0\n"
  "buffer_load_dwordx4 v112, s[40:43], s57 offen offset:128  lds\n"
  "buffer_load_dwordx4 v113, s[40:43], s57 offen offset:1152  lds\n"
  "s_add_u32 m0, s59, 16128\n"
  "s_nop 0\n"
  "buffer_load_dwordx4 v112, s[40:43], s57 offen offset:256  lds\n"
  "buffer_load_dwordx4 v113, s[40:43], s57 offen offset:1280  lds\n"
  "s_add_u32 s57, s57, 6144\n"
  "v_and_b32 v2, 7, v0\n"
  "v_lshrrev_b32 v83, 4, v0\n"
  "v_xor_b32 v83, v83, v2\n"
  "v_lshlrev_b32 v83, 4, v83\n"
  "v_lshl_add_u32 v83, v2, 7, v83\n"
  "v_bfe_u32 v1, v0, 3, 1\n"
  "v_lshl_add_u32 v83, v1, 10, v83\n"
  "v_xor_b32 v84, 64, v83\n"
  "v_add_u32 v87, s59, v83\n"
  "v_add_u32 v88, s59, v84\n"
  "s_mov_b32 s66, 49152\n"
  "v_add_u32 v81, s66, v83\n"
  "v_add_u32 v82, s66, v84\n"
  "s_lshl_b32 s62, s65, 13\n"
  "v_lshlrev_b32 v80, 4, v0\n"
  "v_add_u32 v80, s62, v80\n"
  "s_mov_b32 s60, 0x3dd53b94\n"
  "s_mov_b32 s61, 0x409fecaf\n"
  "v_mov_b32 v92, 0xc2400000\n"
  "v_mov_b32 v93, 0xc2400000\n"
  "v_mov_b32 v94, 0xc2400000\n"
  "v_mov_b32 v95, 0xc2400000\n"
  "s_waitcnt vmcnt(16)\n"
  "ds_read_b128 a[0:3], v87 offset:0\n"
  "ds_read_b128 a[4:7], v88 offset:0\n"
  "s_waitcnt vmcnt(14)\n"
  "ds_read_b128 a[8:11], v87 offset:2048\n"
  "ds_read_b128 a[12:15], v88 offset:2048\n"
  "s_add_u32 m0, s59, -256\n"
  "s_waitcnt lgkmcnt(2)\n"
  "buffer_load_dwordx4 v112, s[40:43], s57 offen offset:256  lds\n"
  "buffer_load_dwordx4 v113, s[40:43], s57 offen offset:1280  lds\n"
  "s_waitcnt vmcnt(14)\n"
  "ds_read_b128 a[16:19], v87 offset:4096\n"
  "ds_read_b128 a[20:23], v88 offset:4096\n"
  "s_add_u32 m0, s59, 1920\n"
  "s_waitcnt lgkmcnt(2)\n"
  "buffer_load_dwordx4 v112, s[40:43], s57 offen offset:128  lds\n"
  "buffer_load_dwordx4 v113, s[40:43], s57 offen offset:1152  lds\n"
  "s_waitcnt vmcnt(14)\n"
  "ds_read_b128 a[40:43], v87 offset:6144\n"
  "ds_read_b128 a[44:47], v88 offset:6144\n"
  "s_add_u32 m0, s59, 4096\n"
  "s_waitcnt lgkmcnt(2)\n"
  "buffer_load_dwordx4 v112, s[40:43], s57 offen offset:0  lds\n"
  "buffer_load_dwordx4 v113, s[40:43], s57 offen offset:1024  lds\n"
  "s_add_u32 s57, s57, 6144\n"
  "s_waitcnt vmcnt(14)\n"
  "ds_read_b128 a[32:35], v87 offset:8192\n"
  "ds_read_b128 a[36:39], v88 offset:8192\n"
  "s_add_u32 m0, s59, 6144\n"
  "s_waitcnt lgkmcnt(2)\n"
  "buffer_load_dwordx4 v112, s[40:43], s57 offen offset:0  lds\n"
  "buffer_load_dwordx4 v113, s[40:43], s57 offen offset:1024  lds\n"
  "s_waitcnt vmcnt(14)\n"
  "ds_read_b128 a[24:27], v87 offset:10240\n"
  "ds_read_b128 a[28:31], v88 offset:10240\n"
  "s_add_u32 m0, s59, 8064\n"
  "s_waitcnt lgkmcnt(2)\n"
  "buffer_load_dwordx4 v112, s[40:43], s57 offen offset:128  lds\n"
  "buffer_load_dwordx4 v113, s[40:43], s57 offen offset:1152  lds\n"
  "s_waitcnt vmcnt(14)\n"
  "ds_read_b128 a[48:51], v87 offset:12288\n"
  "ds_read_b128 a[52:55], v88 offset:12288\n"
  "s_add_u32 m0, s59, 9984\n"
  "s_waitcnt lgkmcnt(2)\n"
  "buffer_load_dwordx4 v112, s[40:43], s57 offen offset:256  lds\n"
  "buffer_load_dwordx4 v113, s[40:43], s57 offen offset:1280  lds\n"
  "s_add_u32 s57, s57, 6144\n"
  "s_waitcnt vmcnt(14)\n"
  "ds_read_b128 a[56:59], v87 offset:14336\n"
  "ds_read_b128 a[60:63], v88 offset:14336\n"
  "s_add_u32 m0, s59, 12032\n"
  "s_waitcnt lgkmcnt(2)\n"
  "buffer_load_dwordx4 v112, s[40:43], s57 offen offset:256  lds\n"
  "buffer_load_dwordx4 v113, s[40:43], s57 offen offset:1280  lds\n"
  "s_waitcnt vmcnt(14)\n"
  "ds_read_b128 a[64:67], v87 offset:16384\n"
  "ds_read_b128 a[68:71], v88 offset:16384\n"
  "s_add_u32 m0, s59, 14208\n"
  "s_waitcnt lgkmcnt(2)\n"
  "buffer_load_dwordx4 v112, s[40:43], s57 offen offset:128  lds\n"
  "buffer_load_dwordx4 v113, s[40:43], s57 offen offset:1152  lds\n"
  "s_waitcnt vmcnt(14)\n"
  "ds_read_b128 a[88:91], v87 offset:0\n"
  "ds_read_b128 a[92:95], v88 offset:0\n"
  "s_add_u32 m0, s59, 16384\n"
  "s_barrier\n"
  "ds_read_b128 v[32:35], v81 offset:0\n"
  "ds_read_b128 v[36:39], v82 offset:0\n"
  "ds_read_b128 v[40:43], v81 offset:2048\n"
  "ds_read_b128 v[44:47], v82 offset:2048\n"
  "ds_read_b128 v[48:51], v81 offset:4096\n"
  "ds_read_b128 v[52:55], v82 offset:4096\n"
  "s_waitcnt lgkmcnt(0)\n"
  "v_mfma_f32_16x16x32_bf16 v[0:3], a[0:3], v[32:35], v[92:95]\n"
  "s_add_u32 s66, s66, 6144\n"
  "s_cmp_eq_u32 s66, 86016\n"
  "s_cselect_b32 s66, 49152, s66\n"
  "buffer_load_dwordx4 v112, s[40:43], s57 offen offset:0  lds\n"
  "buffer_load_dwordx4 v113, s[40:43], s57 offen offset:1024  lds\n"
  "s_add_u32 s57, s57, 6144\n"
  "v_mfma_f32_16x16x32_bf16 v[0:3], a[4:7], v[36:39], v[0:3]\n"
  "s_waitcnt vmcnt(14)\n"
  "ds_read_b128 a[80:83], v87 offset:2048\n"
  "ds_read_b128 a[84:87], v88 offset:2048\n"
  "s_add_u32 m0, s59, 0\n"
  "v_mfma_f32_16x16x32_bf16 v[0:3], a[8:11], v[40:43], v[0:3]\n"
  "buffer_load_dwordx4 v112, s[40:43], s57 offen offset:0  lds\n"
  "buffer_load_dwordx4 v113, s[40:43], s57 offen offset:1024  lds\n"
  "v_mfma_f32_16x16x32_bf16 v[0:3], a[12:15], v[44:47], v[0:3]\n"
  "s_waitcnt vmcnt(14)\n"
  "ds_read_b128 a[72:75], v87 offset:4096\n"
  "ds_read_b128 a[76:79], v88 offset:4096\n"
  "s_add_u32 m0, s59, 1920\n"
  "v_mfma_f32_16x16x32_bf16 v[0:3], a[16:19], v[48:51], v[0:3]\n"
  "s_waitcnt lgkmcnt(2)\n"
  "buffer_load_dwordx4 v112, s[40:43], s57 offen offset:128  lds\n"
  "buffer_load_dwordx4 v113, s[40:43], s57 offen offset:1152  lds\n"
  "v_mfma_f32_16x16x32_bf16 v[0:3], a[20:23], v[52:55], v[0:3]\n"
  "s_waitcnt vmcnt(14)\n"
  "ds_read_b128 a[96:99], v87 offset:6144\n"
  "ds_read_b128 a[100:103], v88 offset:6144\n"
  "s_add_u32 m0, s59, 3840\n"
  "v_mfma_f32_16x16x32_bf16 v[4:7], a[44:47], v[52:55], v[92:95]\n"
  "s_waitcnt lgkmcnt(2)\n"
  "buffer_load_dwordx4 v112, s[40:43], s57 offen offset:256  lds\n"
  "buffer_load_dwordx4 v113, s[40:43], s57 offen offset:1280  lds\n"
  "s_add_u32 s57, s57, 6144\n"
  "v_mfma_f32_16x16x32_bf16 v[4:7], a[40:43], v[48:51], v[4:7]\n"
  "s_waitcnt vmcnt(14)\n"
  "ds_read_b128 a[104:107], v87 offset:8192\n"
  "ds_read_b128 a[108:111], v88 offset:8192\n"
  "s_add_u32 m0, s59, 5888\n"
  "v_mfma_f32_16x16x32_bf16 v[4:7], a[36:39], v[44:47], v[4:7]\n"
  "s_waitcnt lgkmcnt(2)\n"
  "buffer_load_dwordx4 v112, s[40:43], s57 offen offset:256  lds\n"
  "buffer_load_dwordx4 v113, s[40:43], s57 offen offset:1280  lds\n"
  "v_mfma_f32_16x16x32_bf16 v[4:7], a[32:35], v[40:43], v[4:7]\n"
  "s_waitcnt vmcnt(14)\n"
  "ds_read_b128 a[112:115], v87 offset:10240\n"
  "ds_read_b128 a[116:119], v88 offset:10240\n"
  "s_add_u32 m0, s59, 8064\n"
  "v_mfma_f32_16x16x32_bf16 v[4:7], a[28:31], v[36:39], v[4:7]\n"
  "s_waitcnt lgkmcnt(2)\n"
  "buffer_load_dwordx4 v112, s[40:43], s57 offen offset:128  lds\n"
  "buffer_load_dwordx4 v113, s[40:43], s57 offen offset:1152  lds\n"
  "v_mfma_f32_16x16x32_bf16 v[4:7], a[24:27], v[32:35], v[4:7]\n"
  "s_waitcnt vmcnt(14)\n"
  "ds_read_b128 a[136:139], v87 offset:12288\n"
  "ds_read_b128 a[140:143], v88 offset:12288\n"
  "s_add_u32 m0, s59, 10240\n"
  "v_add_u32 v81, s66, v83\n"
  "v_add_u32 v82, s66, v84\n"
  "v_mfma_f32_16x16x32_bf16 v[8:11], a[48:51], v[32:35], v[92:95]\n"
  "s_waitcnt lgkmcnt(2)\n"
  "buffer_load_dwordx4 v112, s[40:43], s57 offen offset:0  lds\n"
  "buffer_load_dwordx4 v113, s[40:43], s57 offen offset:1024  lds\n"
  "s_add_u32 s57, s57, s86\n"
  "v_mfma_f32_16x16x32_bf16 v[8:11], a[52:55], v[36:39], v[8:11]\n"
  "s_waitcnt vmcnt(14)\n"
  "ds_read_b128 a[128:131], v87 offset:14336\n"
  "ds_read_b128 a[132:135], v88 offset:14336\n"
  "s_add_u32 m0, s59, 12288\n"
  "v_mfma_f32_16x16x32_bf16 v[8:11], a[56:59], v[40:43], v[8:11]\n"
  "s_waitcnt lgkmcnt(2)\n"
  "buffer_load_dwordx4 v112, s[40:43], s57 offen offset:0  lds\n"
  "buffer_load_dwordx4 v113, s[40:43], s57 offen offset:1024  lds\n"
  "v_mfma_f32_16x16x32_bf16 v[8:11], a[60:63], v[44:47], v[8:11]\n"
  "s_waitcnt vmcnt(14)\n"
  "ds_read_b128 a[120:123], v87 offset:16384\n"
  "ds_read_b128 a[124:127], v88 offset:16384\n"
  "s_add_u32 m0, s59, 14208\n"
  "v_mfma_f32_16x16x32_bf16 v[8:11], a[64:67], v[48:51], v[8:11]\n"
  "s_waitcnt lgkmcnt(2)\n"
  "buffer_load_dwordx4 v112, s[40:43], s57 offen offset:128  lds\n"
  "buffer_load_dwordx4 v113, s[40:43], s57 offen offset:1152  lds\n"
  "v_mfma_f32_16x16x32_bf16 v[8:11], a[68:71], v[52:55], v[8:11]\n"
  "s_waitcnt vmcnt(14)\n"
  "ds_read_b128 a[144:147], v87 offset:0\n"
  "ds_read_b128 a[148:151], v88 offset:0\n"
  "s_add_u32 m0, s59, 16128\n"
  "v_pk_mul_f32 v[0:1], v[0:1], s[60:61] op_sel_hi:[1,0]\n"
  "v_pk_mul_f32 v[2:3], v[2:3], s[60:61] op_sel_hi:[1,0]\n"
  "v_pk_mul_f32 v[4:5], v[4:5], s[60:61] op_sel_hi:[1,0]\n"
  "v_pk_mul_f32 v[6:7], v[6:7], s[60:61] op_sel_hi:[1,0]\n"
  "v_mfma_f32_16x16x32_bf16 v[12:15], a[92:95], v[52:55], v[92:95]\n"
  "s_waitcnt lgkmcnt(2)\n"
  "buffer_load_dwordx4 v112, s[40:43], s57 offen offset:256  lds\n"
  "buffer_load_dwordx4 v113, s[40:43], s57 offen offset:1280  lds\n"
  "s_add_u32 s57, s57, 6144\n"
  "v_exp_f32 v0, v0\n"
  "v_mfma_f32_16x16x32_bf16 v[12:15], a[88:91], v[48:51], v[12:15]\n"
  "s_waitcnt vmcnt(14)\n"
  "ds_read_b128 a[152:155], v87 offset:2048\n"
  "ds_read_b128 a[156:159], v88 offset:2048\n"
  "s_add_u32 m0, s59, -256\n"
  "v_exp_f32 v1, v1\n"
  "v_mfma_f32_16x16x32_bf16 v[12:15], a[84:87], v[44:47], v[12:15]\n"
  "s_waitcnt lgkmcnt(2)\n"
  "buffer_load_dwordx4 v112, s[40:43], s57 offen offset:256  lds\n"
  "buffer_load_dwordx4 v113, s[40:43], s57 offen offset:1280  lds\n"
  "v_exp_f32 v2, v2\n"
  "v_mfma_f32_16x16x32_bf16 v[12:15], a[80:83], v[40:43], v[12:15]\n"
  "s_waitcnt vmcnt(14)\n"
  "ds_read_b128 a[160:163], v87 offset:4096\n"
  "ds_read_b128 a[164:167], v88 offset:4096\n"
  "s_add_u32 m0, s59, 1920\n"
  "v_exp_f32 v3, v3\n"
  "v_mfma_f32_16x16x32_bf16 v[12:15], a[76:79], v[36:39], v[12:15]\n"
  "s_waitcnt lgkmcnt(2)\n"
  "buffer_load_dwordx4 v112, s[40:43], s57 offen offset:128  lds\n"
  "buffer_load_dwordx4 v113, s[40:43], s57 offen offset:1152  lds\n"
  "v_cvt_pk_bf16_f32 v0, v0, v1\n"
  "v_cvt_pk_bf16_f32 v1, v2, v3\n"
  "v_mfma_f32_16x16x32_bf16 v[12:15], a[72:75], v[32:35], v[12:15]\n"
  "s_waitcnt vmcnt(14)\n"
  "ds_read_b128 a[184:187], v87 offset:6144\n"
  "ds_read_b128 a[188:191], v88 offset:6144\n"
  "s_add_u32 m0, s59, 4096\n"
  "v_mfma_f32_16x16x32_bf16 v[16:19], a[96:99], v[32:35], v[92:95]\n"
  "s_waitcnt lgkmcnt(2)\n"
  "buffer_load_dwordx4 v112, s[40:43], s57 offen offset:0  lds\n"
  "buffer_load_dwordx4 v113, s[40:43], s57 offen offset:1024  lds\n"
  "s_add_u32 s57, s57, 6144\n"
  "v_exp_f32 v4, v4\n"
  "v_mfma_f32_16x16x32_bf16 v[16:19], a[100:103], v[36:39], v[16:19]\n"
  "s_waitcnt vmcnt(14)\n"
  "ds_read_b128 a[176:179], v87 offset:8192\n"
  "ds_read_b128 a[180:183], v88 offset:8192\n"
  "s_add_u32 m0, s59, 6144\n"
  "v_exp_f32 v5, v5\n"
  "v_mfma_f32_16x16x32_bf16 v[16:19], a[104:107], v[40:43], v[16:19]\n"
  "s_waitcnt lgkmcnt(2)\n"
  "buffer_load_dwordx4 v112, s[40:43], s57 offen offset:0  lds\n"
  "buffer_load_dwordx4 v113, s[40:43], s57 offen offset:1024  lds\n"
  "v_exp_f32 v6, v6\n"
  "v_mfma_f32_16x16x32_bf16 v[16:19], a[108:111], v[44:47], v[16:19]\n"
  "s_waitcnt vmcnt(14)\n"
  "ds_read_b128 a[168:171], v87 offset:10240\n"
  "ds_read_b128 a[172:175], v88 offset:10240\n"
  "s_add_u32 m0, s59, 8064\n"
  "v_exp_f32 v7, v7\n"
  "v_mfma_f32_16x16x32_bf16 v[16:19], a[112:115], v[48:51], v[16:19]\n"
  "s_waitcnt lgkmcnt(2)\n"
  "buffer_load_dwordx4 v112, s[40:43], s57 offen offset:128  lds\n"
  "buffer_load_dwordx4 v113, s[40:43], s57 offen offset:1152  lds\n"
  "v_cvt_pk_bf16_f32 v2, v4, v5\n"
  "v_cvt_pk_bf16_f32 v3, v6, v7\n"
  "v_mfma_f32_16x16x32_bf16 v[16:19], a[116:119], v[52:55], v[16:19]\n"
  "s_waitcnt vmcnt(14)\n"
  "ds_read_b128 a[192:195], v87 offset:12288\n"
  "ds_read_b128 a[196:199], v88 offset:12288\n"
  "s_add_u32 m0, s59, 9984\n"
  "ds_write_b128 v80, v[0:3] offset:0\n"
  "v_pk_mul_f32 v[8:9], v[8:9], s[60:61] op_sel_hi:[1,0]\n"
  "v_pk_mul_f32 v[10:11], v[10:11], s[60:61] op_sel_hi:[1,0]\n"
  "v_pk_mul_f32 v[12:13], v[12:13], s[60:61] op_sel_hi:[1,0]\n"
  "v_pk_mul_f32 v[14:15], v[14:15], s[60:61] op_sel_hi:[1,0]\n"
  "v_mfma_f32_16x16x32_bf16 v[20:23], a[140:143], v[52:55], v[92:95]\n"
  "s_waitcnt lgkmcnt(3)\n"
  "buffer_load_dwordx4 v112, s[40:43], s57 offen offset:256  lds\n"
  "buffer_load_dwordx4 v113, s[40:43], s57 offen offset:1280  lds\n"
  "s_add_u32 s57, s57, 6144\n"
  "v_exp_f32 v8, v8\n"
  "v_mfma_f32_16x16x32_bf16 v[20:23], a[136:139], v[48:51], v[20:23]\n"
  "s_waitcnt vmcnt(14)\n"
  "ds_read_b128 a[200:203], v87 offset:14336\n"
  "ds_read_b128 a[204:207], v88 offset:14336\n"
  "s_add_u32 m0, s59, 12032\n"
  "v_exp_f32 v9, v9\n"
  "v_mfma_f32_16x16x32_bf16 v[20:23], a[132:135], v[44:47], v[20:23]\n"
  "s_waitcnt lgkmcnt(3)\n"
  "buffer_load_dwordx4 v112, s[40:43], s57 offen offset:256  lds\n"
  "buffer_load_dwordx4 v113, s[40:43], s57 offen offset:1280  lds\n"
  "v_exp_f32 v10, v10\n"
  "v_mfma_f32_16x16x32_bf16 v[20:23], a[128:131], v[40:43], v[20:23]\n"
  "s_waitcnt vmcnt(14)\n"
  "ds_read_b128 a[208:211], v87 offset:16384\n"
  "ds_read_b128 a[212:215], v88 offset:16384\n"
  "s_add_u32 m0, s59, 14208\n"
  "v_exp_f32 v11, v11\n"
  "v_mfma_f32_16x16x32_bf16 v[20:23], a[124:127], v[36:39], v[20:23]\n"
  "s_waitcnt lgkmcnt(2)\n"
  "buffer_load_dwordx4 v112, s[40:43], s57 offen offset:128  lds\n"
  "buffer_load_dwordx4 v113, s[40:43], s57 offen offset:1152  lds\n"
  "v_cvt_pk_bf16_f32 v8, v8, v9\n"
  "v_cvt_pk_bf16_f32 v9, v10, v11\n"
  "v_mfma_f32_16x16x32_bf16 v[20:23], a[120:123], v[32:35], v[20:23]\n"
  "s_waitcnt vmcnt(14)\n"
  "ds_read_b128 a[232:235], v87 offset:0\n"
  "ds_read_b128 a[236:239], v88 offset:0\n"
  "s_add_u32 m0, s59, 16384\n"
  "ds_read_b128 v[56:59], v81 offset:0\n"
  "ds_read_b128 v[60:63], v82 offset:0\n"
  "v_mfma_f32_16x16x32_bf16 v[24:27], a[144:147], v[32:35], v[92:95]\n"
  "s_waitcnt lgkmcnt(4)\n"
  "buffer_load_dwordx4 v112, s[40:43], s57 offen offset:0  lds\n"
  "buffer_load_dwordx4 v113, s[40:43], s57 offen offset:1024  lds\n"
  "s_add_u32 s57, s57, 6144\n"
  "v_exp_f32 v12, v12\n"
  "v_mfma_f32_16x16x32_bf16 v[24:27], a[148:151], v[36:39], v[24:27]\n"
  "s_waitcnt vmcnt(14)\n"
  "ds_read_b128 a[224:227], v87 offset:2048\n"
  "ds_read_b128 a[228:231], v88 offset:2048\n"
  "s_add_u32 m0, s59, 0\n"
  "v_exp_f32 v13, v13\n"
  "v_mfma_f32_16x16x32_bf16 v[24:27], a[152:155], v[40:43], v[24:27]\n"
  "s_waitcnt lgkmcnt(4)\n"
  "buffer_load_dwordx4 v112, s[40:43], s57 offen offset:0  lds\n"
  "buffer_load_dwordx4 v113, s[40:43], s57 offen offset:1024  lds\n"
  "v_exp_f32 v14, v14\n"
  "v_mfma_f32_16x16x32_bf16 v[24:27], a[156:159], v[44:47], v[24:27]\n"
  "s_waitcnt vmcnt(14)\n"
  "ds_read_b128 a[216:219], v87 offset:4096\n"
  "ds_read_b128 a[220:223], v88 offset:4096\n"
  "s_add_u32 m0, s59, 1920\n"
  "v_exp_f32 v15, v15\n"
  "v_mfma_f32_16x16x32_bf16 v[24:27], a[160:163], v[48:51], v[24:27]\n"
  "s_waitcnt lgkmcnt(2)\n"
  "buffer_load_dwordx4 v112, s[40:43], s57 offen offset:128  lds\n"
  "buffer_load_dwordx4 v113, s[40:43], s57 offen offset:1152  lds\n"
  "v_cvt_pk_bf16_f32 v10, v12, v13\n"
  "v_cvt_pk_bf16_f32 v11, v14, v15\n"
  "v_mfma_f32_16x16x32_bf16 v[24:27], a[164:167], v[52:55], v[24:27]\n"
  "s_waitcnt vmcnt(14)\n"
  "ds_read_b128 a[240:243], v87 offset:6144\n"
  "ds_read_b128 a[244:247], v88 offset:6144\n"
  "s_add_u32 m0, s59, 3840\n"
  "ds_write_b128 v80, v[8:11] offset:1024\n"
  "v_pk_mul_f32 v[16:17], v[16:17], s[60:61] op_sel_hi:[1,0]\n"
  "v_pk_mul_f32 v[18:19], v[18:19], s[60:61] op_sel_hi:[1,0]\n"
  "v_pk_mul_f32 v[20:21], v[20:21], s[60:61] op_sel_hi:[1,0]\n"
  "v_pk_mul_f32 v[22:23], v[22:23], s[60:61] op_sel_hi:[1,0]\n"
  "v_mfma_f32_16x16x32_bf16 v[28:31], a[188:191], v[52:55], v[92:95]\n"
  "s_waitcnt lgkmcnt(3)\n"
  "buffer_load_dwordx4 v112, s[40:43], s57 offen offset:256  lds\n"
  "buffer_load_dwordx4 v113, s[40:43], s57 offen offset:1280  lds\n"
  "s_add_u32 s57, s57, 6144\n"
  "v_exp_f32 v16, v16\n"
  "v_mfma_f32_16x16x32_bf16 v[28:31], a[184:187], v[48:51], v[28:31]\n"
  "s_waitcnt vmcnt(14)\n"
  "ds_read_b128 a[248:251], v87 offset:8192\n"
  "ds_read_b128 a[252:255], v88 offset:8192\n"
  "s_add_u32 m0, s59, 5888\n"
  "v_exp_f32 v17, v17\n"
  "v_mfma_f32_16x16x32_bf16 v[28:31], a[180:183], v[44:47], v[28:31]\n"
  "s_waitcnt lgkmcnt(3)\n"
  "buffer_load_dwordx4 v112, s[40:43], s57 offen offset:256  lds\n"
  "buffer_load_dwordx4 v113, s[40:43], s57 offen offset:1280  lds\n"
  "v_exp_f32 v18, v18\n"
  "v_mfma_f32_16x16x32_bf16 v[28:31], a[176:179], v[40:43], v[28:31]\n"
  "s_waitcnt vmcnt(14)\n"
  "ds_read_b128 v[128:131], v87 offset:10240\n"
  "ds_read_b128 v[132:135], v88 offset:10240\n"
  "s_add_u32 m0, s59, 8064\n"
  "v_exp_f32 v19, v19\n"
  "v_mfma_f32_16x16x32_bf16 v[28:31], a[172:175], v[36:39], v[28:31]\n"
  "s_waitcnt lgkmcnt(2)\n"
  "buffer_load_dwordx4 v112, s[40:43], s57 offen offset:128  lds\n"
  "buffer_load_dwordx4 v113, s[40:43], s57 offen offset:1152  lds\n"
  "v_cvt_pk_bf16_f32 v16, v16, v17\n"
  "v_cvt_pk_bf16_f32 v17, v18, v19\n"
  "v_mfma_f32_16x16x32_bf16 v[28:31], a[168:171], v[32:35], v[28:31]\n"
  "s_waitcnt vmcnt(14)\n"
  "ds_read_b128 v[152:155], v87 offset:12288\n"
  "ds_read_b128 v[156:159], v88 offset:12288\n"
  "s_add_u32 m0, s59, 10240\n"
  "ds_read_b128 v[64:67], v81 offset:2048\n"
  "ds_read_b128 v[68:71], v82 offset:2048\n"
  "v_mfma_f32_16x16x32_bf16 v[0:3], a[192:195], v[32:35], v[92:95]\n"
  "s_waitcnt lgkmcnt(4)\n"
  "buffer_load_dwordx4 v112, s[40:43], s57 offen offset:0  lds\n"
  "buffer_load_dwordx4 v113, s[40:43], s57 offen offset:1024  lds\n"
  "s_add_u32 s57, s57, 6144\n"
  "v_exp_f32 v20, v20\n"
  "v_mfma_f32_16x16x32_bf16 v[0:3], a[196:199], v[36:39], v[0:3]\n"
  "s_waitcnt vmcnt(14)\n"
  "ds_read_b128 v[144:147], v87 offset:14336\n"
  "ds_read_b128 v[148:151], v88 offset:14336\n"
  "s_add_u32 m0, s59, 12288\n"
  "v_exp_f32 v21, v21\n"
  "v_mfma_f32_16x16x32_bf16 v[0:3], a[200:203], v[40:43], v[0:3]\n"
  "s_waitcnt lgkmcnt(4)\n"
  "buffer_load_dwordx4 v112, s[40:43], s57 offen offset:0  lds\n"
  "buffer_load_dwordx4 v113, s[40:43], s57 offen offset:1024  lds\n"
  "v_exp_f32 v22, v22\n"
  "v_mfma_f32_16x16x32_bf16 v[0:3], a[204:207], v[44:47], v[0:3]\n"
  "s_waitcnt vmcnt(14)\n"
  "ds_read_b128 v[136:139], v87 offset:16384\n"
  "ds_read_b128 v[140:143], v88 offset:16384\n"
  "s_add_u32 m0, s59, 14208\n"
  "v_exp_f32 v23, v23\n"
  "v_mfma_f32_16x16x32_bf16 v[0:3], a[208:211], v[48:51], v[0:3]\n"
  "s_waitcnt lgkmcnt(2)\n"
  "buffer_load_dwordx4 v112, s[40:43], s57 offen offset:128  lds\n"
  "buffer_load_dwordx4 v113, s[40:43], s57 offen offset:1152  lds\n"
  "v_cvt_pk_bf16_f32 v18, v20, v21\n"
  "v_cvt_pk_bf16_f32 v19, v22, v23\n"
  "v_mfma_f32_16x16x32_bf16 v[0:3], a[212:215], v[52:55], v[0:3]\n"
  "s_waitcnt vmcnt(14)\n"
  "ds_read_b128 v[160:163], v87 offset:0\n"
  "ds_read_b128 v[164:167], v88 offset:0\n"
  "s_add_u32 m0, s59, 16128\n"
  "ds_write_b128 v80, v[16:19] offset:2048\n"
  "v_pk_mul_f32 v[24:25], v[24:25], s[60:61] op_sel_hi:[1,0]\n"
  "v_pk_mul_f32 v[26:27], v[26:27], s[60:61] op_sel_hi:[1,0]\n"
  "v_pk_mul_f32 v[28:29], v[28:29], s[60:61] op_sel_hi:[1,0]\n"
  "v_pk_mul_f32 v[30:31], v[30:31], s[60:61] op_sel_hi:[1,0]\n"
  "v_mfma_f32_16x16x32_bf16 v[4:7], a[236:239], v[52:55], v[92:95]\n"
  "s_waitcnt lgkmcnt(3)\n"
  "buffer_load_dwordx4 v112, s[40:43], s57 offen offset:256  lds\n"
  "buffer_load_dwordx4 v113, s[40:43], s57 offen offset:1280  lds\n"
  "s_add_u32 s57, s57, 6144\n"
  "v_exp_f32 v24, v24\n"
  "v_mfma_f32_16x16x32_bf16 v[4:7], a[232:235], v[48:51], v[4:7]\n"
  "s_waitcnt vmcnt(14)\n"
  "ds_read_b128 v[168:171], v87 offset:2048\n"
  "ds_read_b128 v[172:175], v88 offset:2048\n"
  "s_add_u32 m0, s59, -256\n"
  "v_exp_f32 v25, v25\n"
  "v_mfma_f32_16x16x32_bf16 v[4:7], a[228:231], v[44:47], v[4:7]\n"
  "s_waitcnt lgkmcnt(3)\n"
  "buffer_load_dwordx4 v112, s[40:43], s57 offen offset:256  lds\n"
  "buffer_load_dwordx4 v113, s[40:43], s57 offen offset:1280  lds\n"
  "v_exp_f32 v26, v26\n"
  "v_mfma_f32_16x16x32_bf16 v[4:7], a[224:227], v[40:43], v[4:7]\n"
  "s_waitcnt vmcnt(14)\n"
  "ds_read_b128 v[176:179], v87 offset:4096\n"
  "ds_read_b128 v[180:183], v88 offset:4096\n"
  "s_add_u32 m0, s59, 1920\n"
  "v_exp_f32 v27, v27\n"
  "v_mfma_f32_16x16x32_bf16 v[4:7], a[220:223], v[36:39], v[4:7]\n"
  "s_waitcnt lgkmcnt(2)\n"
  "buffer_load_dwordx4 v112, s[40:43], s57 offen offset:128  lds\n"
  "buffer_load_dwordx4 v113, s[40:43], s57 offen offset:1152  lds\n"
  "v_cvt_pk_bf16_f32 v24, v24, v25\n"
  "v_cvt_pk_bf16_f32 v25, v26, v27\n"
  "v_mfma_f32_16x16x32_bf16 v[4:7], a[216:219], v[32:35], v[4:7]\n"
  "s_waitcnt vmcnt(14)\n"
  "ds_read_b128 v[200:203], v87 offset:6144\n"
  "ds_read_b128 v[204:207], v88 offset:6144\n"
  "s_add_u32 m0, s59, 4096\n"
  "ds_read_b128 v[72:75], v81 offset:4096\n"
  "ds_read_b128 v[76:79], v82 offset:4096\n"
  "v_mfma_f32_16x16x32_bf16 v[8:11], a[240:243], v[32:35], v[92:95]\n"
  "s_waitcnt lgkmcnt(4)\n"
  "buffer_load_dwordx4 v112, s[40:43], s57 offen offset:0  lds\n"
  "buffer_load_dwordx4 v113, s[40:43], s57 offen offset:1024  lds\n"
  "s_add_u32 s57, s57, 6144\n"
  "s_barrier\n"
  "v_exp_f32 v28, v28\n"
  "v_mfma_f32_16x16x32_bf16 v[8:11], a[244:247], v[36:39], v[8:11]\n"
  "s_waitcnt vmcnt(14)\n"
  "ds_read_b128 v[192:195], v87 offset:8192\n"
  "ds_read_b128 v[196:199], v88 offset:8192\n"
  "v_exp_f32 v29, v29\n"
  "v_mfma_f32_16x16x32_bf16 v[8:11], a[248:251], v[40:43], v[8:11]\n"
  "v_exp_f32 v30, v30\n"
  "v_mfma_f32_16x16x32_bf16 v[8:11], a[252:255], v[44:47], v[8:11]\n"
  "s_waitcnt vmcnt(12)\n"
  "ds_read_b128 v[184:187], v87 offset:10240\n"
  "ds_read_b128 v[188:191], v88 offset:10240\n"
  "v_exp_f32 v31, v31\n"
  "v_mfma_f32_16x16x32_bf16 v[8:11], v[128:131], v[48:51], v[8:11]\n"
  "v_cvt_pk_bf16_f32 v26, v28, v29\n"
  "v_cvt_pk_bf16_f32 v27, v30, v31\n"
  "v_mfma_f32_16x16x32_bf16 v[8:11], v[132:135], v[52:55], v[8:11]\n"
  "s_waitcnt vmcnt(10)\n"
  "ds_read_b128 v[208:211], v87 offset:12288\n"
  "ds_read_b128 v[212:215], v88 offset:12288\n"
  "ds_write_b128 v80, v[24:27] offset:3072\n"
  "v_pk_mul_f32 v[0:1], v[0:1], s[60:61] op_sel_hi:[1,0]\n"
  "v_pk_mul_f32 v[2:3], v[2:3], s[60:61] op_sel_hi:[1,0]\n"
  "v_pk_mul_f32 v[4:5], v[4:5], s[60:61] op_sel_hi:[1,0]\n"
  "v_pk_mul_f32 v[6:7], v[6:7], s[60:61] op_sel_hi:[1,0]\n"
  "v_mfma_f32_16x16x32_bf16 v[12:15], v[156:159], v[52:55], v[92:95]\n"
  "v_exp_f32 v0, v0\n"
  "v_mfma_f32_16x16x32_bf16 v[12:15], v[152:155], v[48:51], v[12:15]\n"
  "s_waitcnt vmcnt(8)\n"
  "ds_read_b128 v[216:219], v87 offset:14336\n"
  "ds_read_b128 v[220:223], v88 offset:14336\n"
  "v_exp_f32 v1, v1\n"
  "v_mfma_f32_16x16x32_bf16 v[12:15], v[148:151], v[44:47], v[12:15]\n"
  "v_exp_f32 v2, v2\n"
  "v_mfma_f32_16x16x32_bf16 v[12:15], v[144:147], v[40:43], v[12:15]\n"
  "s_waitcnt vmcnt(6)\n"
  "ds_read_b128 v[224:227], v87 offset:16384\n"
  "ds_read_b128 v[228:231], v88 offset:16384\n"
  "v_exp_f32 v3, v3\n"
  "v_mfma_f32_16x16x32_bf16 v[12:15], v[140:143], v[36:39], v[12:15]\n"
  "v_cvt_pk_bf16_f32 v0, v0, v1\n"
  "v_cvt_pk_bf16_f32 v1, v2, v3\n"
  "v_mfma_f32_16x16x32_bf16 v[12:15], v[136:139], v[32:35], v[12:15]\n"
  "s_waitcnt vmcnt(4)\n"
  "ds_read_b128 v[248:251], v87 offset:0\n"
  "ds_read_b128 v[252:255], v88 offset:0\n"
  "v_mul_f32 v8, s60, v8\n"
  "v_mul_f32 v9, s60, v9\n"
  "v_mfma_f32_16x16x32_bf16 v[16:19], v[160:163], v[32:35], v[92:95]\n"
  "v_exp_f32 v4, v4\n"
  "v_mfma_f32_16x16x32_bf16 v[16:19], v[164:167], v[36:39], v[16:19]\n"
  "s_waitcnt vmcnt(2)\n"
  "ds_read_b128 v[240:243], v87 offset:2048\n"
  "ds_read_b128 v[244:247], v88 offset:2048\n"
  "v_exp_f32 v5, v5\n"
  "v_mfma_f32_16x16x32_bf16 v[16:19], v[168:171], v[40:43], v[16:19]\n"
  "v_exp_f32 v6, v6\n"
  "v_mfma_f32_16x16x32_bf16 v[16:19], v[172:175], v[44:47], v[16:19]\n"
  "s_waitcnt vmcnt(0)\n"
  "ds_read_b128 v[232:235], v87 offset:4096\n"
  "ds_read_b128 v[236:239], v88 offset:4096\n"
  "v_exp_f32 v7, v7\n"
  "v_mfma_f32_16x16x32_bf16 v[16:19], v[176:179], v[48:51], v[16:19]\n"
  "v_cvt_pk_bf16_f32 v2, v4, v5\n"
  "v_cvt_pk_bf16_f32 v3, v6, v7\n"
  "v_mfma_f32_16x16x32_bf16 v[16:19], v[180:183], v[52:55], v[16:19]\n"
  "s_waitcnt lgkmcnt(15)\n"
  "ds_write_b128 v80, v[0:3] offset:4096\n"
  "v_pk_mul_f32 v[10:11], v[10:11], s[60:61] op_sel_hi:[1,0]\n"
  "v_pk_mul_f32 v[12:13], v[12:13], s[60:61] op_sel_hi:[1,0]\n"
  "v_pk_mul_f32 v[14:15], v[14:15], s[60:61] op_sel_hi:[1,0]\n"
  "v_mfma_f32_16x16x32_bf16 v[20:23], v[204:207], v[52:55], v[92:95]\n"
  "v_exp_f32 v8, v8\n"
  "v_mfma_f32_16x16x32_bf16 v[20:23], v[200:203], v[48:51], v[20:23]\n"
  "v_exp_f32 v9, v9\n"
  "v_mfma_f32_16x16x32_bf16 v[20:23], v[196:199], v[44:47], v[20:23]\n"
  "v_exp_f32 v10, v10\n"
  "v_mfma_f32_16x16x32_bf16 v[20:23], v[192:195], v[40:43], v[20:23]\n"
  "s_waitcnt lgkmcnt(14)\n"
  "v_exp_f32 v11, v11\n"
  "v_mfma_f32_16x16x32_bf16 v[20:23], v[188:191], v[36:39], v[20:23]\n"
  "v_cvt_pk_bf16_f32 v8, v8, v9\n"
  "v_cvt_pk_bf16_f32 v9, v10, v11\n"
  "v_mfma_f32_16x16x32_bf16 v[20:23], v[184:187], v[32:35], v[20:23]\n"
  "s_waitcnt lgkmcnt(12)\n"
  "v_mul_f32 v16, s60, v16\n"
  "v_mul_f32 v17, s60, v17\n"
  "v_mfma_f32_16x16x32_bf16 v[24:27], v[208:211], v[32:35], v[92:95]\n"
  "v_exp_f32 v12, v12\n"
  "v_mfma_f32_16x16x32_bf16 v[24:27], v[212:215], v[36:39], v[24:27]\n"
  "s_waitcnt lgkmcnt(9)\n"
  "v_exp_f32 v13, v13\n"
  "v_mfma_f32_16x16x32_bf16 v[24:27], v[216:219], v[40:43], v[24:27]\n"
  "v_exp_f32 v14, v14\n"
  "v_mfma_f32_16x16x32_bf16 v[24:27], v[220:223], v[44:47], v[24:27]\n"
  "s_waitcnt lgkmcnt(7)\n"
  "v_exp_f32 v15, v15\n"
  "v_mfma_f32_16x16x32_bf16 v[24:27], v[224:227], v[48:51], v[24:27]\n"
  "v_cvt_pk_bf16_f32 v10, v12, v13\n"
  "v_cvt_pk_bf16_f32 v11, v14, v15\n"
  "v_mfma_f32_16x16x32_bf16 v[24:27], v[228:231], v[52:55], v[24:27]\n"
  "s_waitcnt lgkmcnt(5)\n"
  "ds_write_b128 v80, v[8:11] offset:5120\n"
  "v_pk_mul_f32 v[18:19], v[18:19], s[60:61] op_sel_hi:[1,0]\n"
  "v_pk_mul_f32 v[20:21], v[20:21], s[60:61] op_sel_hi:[1,0]\n"
  "v_pk_mul_f32 v[22:23], v[22:23], s[60:61] op_sel_hi:[1,0]\n"
  "v_mfma_f32_16x16x32_bf16 v[28:31], v[252:255], v[52:55], v[92:95]\n"
  "v_exp_f32 v16, v16\n"
  "v_mfma_f32_16x16x32_bf16 v[28:31], v[248:251], v[48:51], v[28:31]\n"
  "s_waitcnt lgkmcnt(4)\n"
  "v_exp_f32 v17, v17\n"
  "v_mfma_f32_16x16x32_bf16 v[28:31], v[244:247], v[44:47], v[28:31]\n"
  "v_exp_f32 v18, v18\n"
  "v_mfma_f32_16x16x32_bf16 v[28:31], v[240:243], v[40:43], v[28:31]\n"
  "s_waitcnt lgkmcnt(2)\n"
  "s_sub_u32 s56, s56, 1\n"
  "v_exp_f32 v19, v19\n"
  "v_mfma_f32_16x16x32_bf16 v[28:31], v[236:239], v[36:39], v[28:31]\n"
  "v_cvt_pk_bf16_f32 v16, v16, v17\n"
  "v_cvt_pk_bf16_f32 v17, v18, v19\n"
  "v_mfma_f32_16x16x32_bf16 v[28:31], v[232:235], v[32:35], v[28:31]\n"
  ".p2align 6\n"
  "A_H1_%=:\n"
  "v_mfma_f32_16x16x32_bf16 v[0:3], a[0:3], v[56:59], v[92:95]\n"
  "s_add_u32 s66, s66, 6144\n"
  "v_exp_f32 v20, v20\n"
  "v_mfma_f32_16x16x32_bf16 v[0:3], a[4:7], v[60:63], v[0:3]\n"
  "s_cmp_eq_u32 s66, 86016\n"
  "v_exp_f32 v21, v21\n"
  "v_mfma_f32_16x16x32_bf16 v[0:3], a[8:11], v[64:67], v[0:3]\n"
  "s_cselect_b32 s66, 49152, s66\n"
  "v_exp_f32 v22, v22\n"
  "v_mfma_f32_16x16x32_bf16 v[0:3], a[12:15], v[68:71], v[0:3]\n"
  "v_exp_f32 v23, v23\n"
  "v_mfma_f32_16x16x32_bf16 v[0:3], a[16:19], v[72:75], v[0:3]\n"
  "v_cvt_pk_bf16_f32 v18, v20, v21\n"
  "v_cvt_pk_bf16_f32 v19, v22, v23\n"
  "v_mfma_f32_16x16x32_bf16 v[0:3], a[20:23], v[76:79], v[0:3]\n"
  "ds_write_b128 v80, v[16:19] offset:6144\n"
  "v_pk_mul_f32 v[24:25], v[24:25], s[60:61] op_sel_hi:[1,0]\n"
  "v_pk_mul_f32 v[26:27], v[26:27], s[60:61] op_sel_hi:[1,0]\n"
  "v_pk_mul_f32 v[28:29], v[28:29], s[60:61] op_sel_hi:[1,0]\n"
  "v_pk_mul_f32 v[30:31], v[30:31], s[60:61] op_sel_hi:[1,0]\n"
  "v_mfma_f32_16x16x32_bf16 v[4:7], a[44:47], v[76:79], v[92:95]\n"
  "v_exp_f32 v24, v24\n"
  "v_mfma_f32_16x16x32_bf16 v[4:7], a[40:43], v[72:75], v[4:7]\n"
  "v_exp_f32 v25, v25\n"
  "v_mfma_f32_16x16x32_bf16 v[4:7], a[36:39], v[68:71], v[4:7]\n"
  "v_exp_f32 v26, v26\n"
  "v_mfma_f32_16x16x32_bf16 v[4:7], a[32:35], v[64:67], v[4:7]\n"
  "v_exp_f32 v27, v27\n"
  "v_mfma_f32_16x16x32_bf16 v[4:7], a[28:31], v[60:63], v[4:7]\n"
  "v_cvt_pk_bf16_f32 v24, v24, v25\n"
  "v_cvt_pk_bf16_f32 v25, v26, v27\n"
  "v_mfma_f32_16x16x32_bf16 v[4:7], a[24:27], v[56:59], v[4:7]\n"
  "v_add_u32 v81, s66, v83\n"
  "v_add_u32 v82, s66, v84\n"
  "v_mfma_f32_16x16x32_bf16 v[8:11], a[48:51], v[56:59], v[92:95]\n"
  "v_exp_f32 v28, v28\n"
  "v_mfma_f32_16x16x32_bf16 v[8:11], a[52:55], v[60:63], v[8:11]\n"
  "v_exp_f32 v29, v29\n"
  "v_mfma_f32_16x16x32_bf16 v[8:11], a[56:59], v[64:67], v[8:11]\n"
  "v_exp_f32 v30, v30\n"
  "v_mfma_f32_16x16x32_bf16 v[8:11], a[60:63], v[68:71], v[8:11]\n"
  "v_exp_f32 v31, v31\n"
  "v_mfma_f32_16x16x32_bf16 v[8:11], a[64:67], v[72:75], v[8:11]\n"
  "v_cvt_pk_bf16_f32 v26, v28, v29\n"
  "v_cvt_pk_bf16_f32 v27, v30, v31\n"
  "v_mfma_f32_16x16x32_bf16 v[8:11], a[68:71], v[76:79], v[8:11]\n"
  "ds_write_b128 v80, v[24:27] offset:7168\n"
  "v_pk_mul_f32 v[0:1], v[0:1], s[60:61] op_sel_hi:[1,0]\n"
  "v_pk_mul_f32 v[2:3], v[2:3], s[60:61] op_sel_hi:[1,0]\n"
  "v_pk_mul_f32 v[4:5], v[4:5], s[60:61] op_sel_hi:[1,0]\n"
  "v_pk_mul_f32 v[6:7], v[6:7], s[60:61] op_sel_hi:[1,0]\n"
  "v_mfma_f32_16x16x32_bf16 v[12:15], a[92:95], v[76:79], v[92:95]\n"
  "v_exp_f32 v0, v0\n"
  "v_mfma_f32_16x16x32_bf16 v[12:15], a[88:91], v[72:75], v[12:15]\n"
  "v_exp_f32 v1, v1\n"
  "v_mfma_f32_16x16x32_bf16 v[12:15], a[84:87], v[68:71], v[12:15]\n"
  "v_exp_f32 v2, v2\n"
  "v_mfma_f32_16x16x32_bf16 v[12:15], a[80:83], v[64:67], v[12:15]\n"
  "v_exp_f32 v3, v3\n"
  "v_mfma_f32_16x16x32_bf16 v[12:15], a[76:79], v[60:63], v[12:15]\n"
  "v_cvt_pk_bf16_f32 v0, v0, v1\n"
  "v_cvt_pk_bf16_f32 v1, v2, v3\n"
  "v_mfma_f32_16x16x32_bf16 v[12:15], a[72:75], v[56:59], v[12:15]\n"
  "s_waitcnt lgkmcnt(0)\n"
  "s_barrier\n"
  "v_xor_b32 v80, 0x4000, v80\n"
  "v_mfma_f32_16x16x32_bf16 v[16:19], a[96:99], v[56:59], v[92:95]\n"
  "v_exp_f32 v4, v4\n"
  "v_mfma_f32_16x16x32_bf16 v[16:19], a[100:103], v[60:63], v[16:19]\n"
  "v_exp_f32 v5, v5\n"
  "v_mfma_f32_16x16x32_bf16 v[16:19], a[104:107], v[64:67], v[16:19]\n"
  "v_exp_f32 v6, v6\n"
  "v_mfma_f32_16x16x32_bf16 v[16:19], a[108:111], v[68:71], v[16:19]\n"
  "v_exp_f32 v7, v7\n"
  "v_mfma_f32_16x16x32_bf16 v[16:19], a[112:115], v[72:75], v[16:19]\n"
  "v_cvt_pk_bf16_f32 v2, v4, v5\n"
  "v_cvt_pk_bf16_f32 v3, v6, v7\n"
  "v_mfma_f32_16x16x32_bf16 v[16:19], a[116:119], v[76:79], v[16:19]\n"
  "ds_write_b128 v80, v[0:3] offset:0\n"
  "v_pk_mul_f32 v[8:9], v[8:9], s[60:61] op_sel_hi:[1,0]\n"
  "v_pk_mul_f32 v[10:11], v[10:11], s[60:61] op_sel_hi:[1,0]\n"
  "v_pk_mul_f32 v[12:13], v[12:13], s[60:61] op_sel_hi:[1,0]\n"
  "v_pk_mul_f32 v[14:15], v[14:15], s[60:61] op_sel_hi:[1,0]\n"
  "v_mfma_f32_16x16x32_bf16 v[20:23], a[140:143], v[76:79], v[92:95]\n"
  "v_exp_f32 v8, v8\n"
  "v_mfma_f32_16x16x32_bf16 v[20:23], a[136:139], v[72:75], v[20:23]\n"
  "v_exp_f32 v9, v9\n"
  "v_mfma_f32_16x16x32_bf16 v[20:23], a[132:135], v[68:71], v[20:23]\n"
  "v_exp_f32 v10, v10\n"
  "v_mfma_f32_16x16x32_bf16 v[20:23], a[128:131], v[64:67], v[20:23]\n"
  "v_exp_f32 v11, v11\n"
  "v_mfma_f32_16x16x32_bf16 v[20:23], a[124:127], v[60:63], v[20:23]\n"
  "v_cvt_pk_bf16_f32 v8, v8, v9\n"
  "v_cvt_pk_bf16_f32 v9, v10, v11\n"
  "v_mfma_f32_16x16x32_bf16 v[20:23], a[120:123], v[56:59], v[20:23]\n"
  "ds_read_b128 v[32:35], v81 offset:0\n"
  "ds_read_b128 v[36:39], v82 offset:0\n"
  "v_mfma_f32_16x16x32_bf16 v[24:27], a[144:147], v[56:59], v[92:95]\n"
  "v_exp_f32 v12, v12\n"
  "v_mfma_f32_16x16x32_bf16 v[24:27], a[148:151], v[60:63], v[24:27]\n"
  "v_exp_f32 v13, v13\n"
  "v_mfma_f32_16x16x32_bf16 v[24:27], a[152:155], v[64:67], v[24:27]\n"
  "v_exp_f32 v14, v14\n"
  "v_mfma_f32_16x16x32_bf16 v[24:27], a[156:159], v[68:71], v[24:27]\n"
  "v_exp_f32 v15, v15\n"
  "v_mfma_f32_16x16x32_bf16 v[24:27], a[160:163], v[72:75], v[24:27]\n"
  "v_cvt_pk_bf16_f32 v10, v12, v13\n"
  "v_cvt_pk_bf16_f32 v11, v14, v15\n"
  "v_mfma_f32_16x16x32_bf16 v[24:27], a[164:167], v[76:79], v[24:27]\n"
  "ds_write_b128 v80, v[8:11] offset:1024\n"
  "v_pk_mul_f32 v[16:17], v[16:17], s[60:61] op_sel_hi:[1,0]\n"
  "v_pk_mul_f32 v[18:19], v[18:19], s[60:61] op_sel_hi:[1,0]\n"
  "v_pk_mul_f32 v[20:21], v[20:21], s[60:61] op_sel_hi:[1,0]\n"
  "v_pk_mul_f32 v[22:23], v[22:23], s[60:61] op_sel_hi:[1,0]\n"
  "v_mfma_f32_16x16x32_bf16 v[28:31], a[188:191], v[76:79], v[92:95]\n"
  "v_exp_f32 v16, v16\n"
  "v_mfma_f32_16x16x32_bf16 v[28:31], a[184:187], v[72:75], v[28:31]\n"
  "v_exp_f32 v17, v17\n"
  "v_mfma_f32_16x16x32_bf16 v[28:31], a[180:183], v[68:71], v[28:31]\n"
  "v_exp_f32 v18, v18\n"
  "v_mfma_f32_16x16x32_bf16 v[28:31], a[176:179], v[64:67], v[28:31]\n"
  "v_exp_f32 v19, v19\n"
  "v_mfma_f32_16x16x32_bf16 v[28:31], a[172:175], v[60:63], v[28:31]\n"
  "v_cvt_pk_bf16_f32 v16, v16, v17\n"
  "v_cvt_pk_bf16_f32 v17, v18, v19\n"
  "v_mfma_f32_16x16x32_bf16 v[28:31], a[168:171], v[56:59], v[28:31]\n"
  "ds_read_b128 v[40:43], v81 offset:2048\n"
  "ds_read_b128 v[44:47], v82 offset:2048\n"
  "v_mfma_f32_16x16x32_bf16 v[0:3], a[192:195], v[56:59], v[92:95]\n"
  "v_exp_f32 v20, v20\n"
  "v_mfma_f32_16x16x32_bf16 v[0:3], a[196:199], v[60:63], v[0:3]\n"
  "v_exp_f32 v21, v21\n"
  "v_mfma_f32_16x16x32_bf16 v[0:3], a[200:203], v[64:67], v[0:3]\n"
  "v_exp_f32 v22, v22\n"
  "v_mfma_f32_16x16x32_bf16 v[0:3], a[204:207], v[68:71], v[0:3]\n"
  "v_exp_f32 v23, v23\n"
  "v_mfma_f32_16x16x32_bf16 v[0:3], a[208:211], v[72:75], v[0:3]\n"
  "v_cvt_pk_bf16_f32 v18, v20, v21\n"
  "v_cvt_pk_bf16_f32 v19, v22, v23\n"
  "v_mfma_f32_16x16x32_bf16 v[0:3], a[212:215], v[76:79], v[0:3]\n"
  "ds_write_b128 v80, v[16:19] offset:2048\n"
  "v_pk_mul_f32 v[24:25], v[24:25], s[60:61] op_sel_hi:[1,0]\n"
  "v_pk_mul_f32 v[26:27], v[26:27], s[60:61] op_sel_hi:[1,0]\n"
  "v_pk_mul_f32 v[28:29], v[28:29], s[60:61] op_sel_hi:[1,0]\n"
  "v_pk_mul_f32 v[30:31], v[30:31], s[60:61] op_sel_hi:[1,0]\n"
  "v_mfma_f32_16x16x32_bf16 v[4:7], a[236:239], v[76:79], v[92:95]\n"
  "v_exp_f32 v24, v24\n"
  "v_mfma_f32_16x16x32_bf16 v[4:7], a[232:235], v[72:75], v[4:7]\n"
  "v_exp_f32 v25, v25\n"
  "v_mfma_f32_16x16x32_bf16 v[4:7], a[228:231], v[68:71], v[4:7]\n"
  "v_exp_f32 v26, v26\n"
  "v_mfma_f32_16x16x32_bf16 v[4:7], a[224:227], v[64:67], v[4:7]\n"
  "v_exp_f32 v27, v27\n"
  "v_mfma_f32_16x16x32_bf16 v[4:7], a[220:223], v[60:63], v[4:7]\n"
  "v_cvt_pk_bf16_f32 v24, v24, v25\n"
  "v_cvt_pk_bf16_f32 v25, v26, v27\n"
  "v_mfma_f32_16x16x32_bf16 v[4:7], a[216:219], v[56:59], v[4:7]\n"
  "ds_read_b128 v[48:51], v81 offset:4096\n"
  "ds_read_b128 v[52:55], v82 offset:4096\n"
  "v_mfma_f32_16x16x32_bf16 v[8:11], a[240:243], v[56:59], v[92:95]\n"
  "v_exp_f32 v28, v28\n"
  "v_mfma_f32_16x16x32_bf16 v[8:11], a[244:247], v[60:63], v[8:11]\n"
  "v_exp_f32 v29, v29\n"
  "v_mfma_f32_16x16x32_bf16 v[8:11], a[248:251], v[64:67], v[8:11]\n"
  "v_exp_f32 v30, v30\n"
  "v_mfma_f32_16x16x32_bf16 v[8:11], a[252:255], v[68:71], v[8:11]\n"
  "v_exp_f32 v31, v31\n"
  "v_mfma_f32_16x16x32_bf16 v[8:11], v[128:131], v[72:75], v[8:11]\n"
  "v_cvt_pk_bf16_f32 v26, v28, v29\n"
  "v_cvt_pk_bf16_f32 v27, v30, v31\n"
  "v_mfma_f32_16x16x32_bf16 v[8:11], v[132:135], v[76:79], v[8:11]\n"
  "ds_write_b128 v80, v[24:27] offset:3072\n"
  "v_pk_mul_f32 v[0:1], v[0:1], s[60:61] op_sel_hi:[1,0]\n"
  "v_pk_mul_f32 v[2:3], v[2:3], s[60:61] op_sel_hi:[1,0]\n"
  "v_pk_mul_f32 v[4:5], v[4:5], s[60:61] op_sel_hi:[1,0]\n"
  "v_pk_mul_f32 v[6:7], v[6:7], s[60:61] op_sel_hi:[1,0]\n"
  "v_mfma_f32_16x16x32_bf16 v[12:15], v[156:159], v[76:79], v[92:95]\n"
  "v_exp_f32 v0, v0\n"
  "v_mfma_f32_16x16x32_bf16 v[12:15], v[152:155], v[72:75], v[12:15]\n"
  "v_exp_f32 v1, v1\n"
  "v_mfma_f32_16x16x32_bf16 v[12:15], v[148:151], v[68:71], v[12:15]\n"
  "v_exp_f32 v2, v2\n"
  "v_mfma_f32_16x16x32_bf16 v[12:15], v[144:147], v[64:67], v[12:15]\n"
  "v_exp_f32 v3, v3\n"
  "v_mfma_f32_16x16x32_bf16 v[12:15], v[140:143], v[60:63], v[12:15]\n"
  "v_cvt_pk_bf16_f32 v0, v0, v1\n"
  "v_cvt_pk_bf16_f32 v1, v2, v3\n"
  "v_mfma_f32_16x16x32_bf16 v[12:15], v[136:139], v[56:59], v[12:15]\n"
  "v_mul_f32 v8, s60, v8\n"
  "v_mul_f32 v9, s60, v9\n"
  "v_mfma_f32_16x16x32_bf16 v[16:19], v[160:163], v[56:59], v[92:95]\n"
  "v_exp_f32 v4, v4\n"
  "v_mfma_f32_16x16x32_bf16 v[16:19], v[164:167], v[60:63], v[16:19]\n"
  "v_exp_f32 v5, v5\n"
  "v_mfma_f32_16x16x32_bf16 v[16:19], v[168:171], v[64:67], v[16:19]\n"
  "v_exp_f32 v6, v6\n"
  "v_mfma_f32_16x16x32_bf16 v[16:19], v[172:175], v[68:71], v[16:19]\n"
  "v_exp_f32 v7, v7\n"
  "v_mfma_f32_16x16x32_bf16 v[16:19], v[176:179], v[72:75], v[16:19]\n"
  "v_cvt_pk_bf16_f32 v2, v4, v5\n"
  "v_cvt_pk_bf16_f32 v3, v6, v7\n"
  "v_mfma_f32_16x16x32_bf16 v[16:19], v[180:183], v[76:79], v[16:19]\n"
  "ds_write_b128 v80, v[0:3] offset:4096\n"
  "v_pk_mul_f32 v[10:11], v[10:11], s[60:61] op_sel_hi:[1,0]\n"
  "v_pk_mul_f32 v[12:13], v[12:13], s[60:61] op_sel_hi:[1,0]\n"
  "v_pk_mul_f32 v[14:15], v[14:15], s[60:61] op_sel_hi:[1,0]\n"
  "v_mfma_f32_16x16x32_bf16 v[20:23], v[204:207], v[76:79], v[92:95]\n"
  "v_exp_f32 v8, v8\n"
  "v_mfma_f32_16x16x32_bf16 v[20:23], v[200:203], v[72:75], v[20:23]\n"
  "v_exp_f32 v9, v9\n"
  "v_mfma_f32_16x16x32_bf16 v[20:23], v[196:199], v[68:71], v[20:23]\n"
  "v_exp_f32 v10, v10\n"
  "v_mfma_f32_16x16x32_bf16 v[20:23], v[192:195], v[64:67], v[20:23]\n"
  "v_exp_f32 v11, v11\n"
  "v_mfma_f32_16x16x32_bf16 v[20:23], v[188:191], v[60:63], v[20:23]\n"
  "v_cvt_pk_bf16_f32 v8, v8, v9\n"
  "v_cvt_pk_bf16_f32 v9, v10, v11\n"
  "v_mfma_f32_16x16x32_bf16 v[20:23], v[184:187], v[56:59], v[20:23]\n"
  "v_mul_f32 v16, s60, v16\n"
  "v_mul_f32 v17, s60, v17\n"
  "v_mfma_f32_16x16x32_bf16 v[24:27], v[208:211], v[56:59], v[92:95]\n"
  "v_exp_f32 v12, v12\n"
  "v_mfma_f32_16x16x32_bf16 v[24:27], v[212:215], v[60:63], v[24:27]\n"
  "v_exp_f32 v13, v13\n"
  "v_mfma_f32_16x16x32_bf16 v[24:27], v[216:219], v[64:67], v[24:27]\n"
  "v_exp_f32 v14, v14\n"
  "v_mfma_f32_16x16x32_bf16 v[24:27], v[220:223], v[68:71], v[24:27]\n"
  "v_exp_f32 v15, v15\n"
  "v_mfma_f32_16x16x32_bf16 v[24:27], v[224:227], v[72:75], v[24:27]\n"
  "v_cvt_pk_bf16_f32 v10, v12, v13\n"
  "v_cvt_pk_bf16_f32 v11, v14, v15\n"
  "v_mfma_f32_16x16x32_bf16 v[24:27], v[228:231], v[76:79], v[24:27]\n"
  "ds_write_b128 v80, v[8:11] offset:5120\n"
  "v_pk_mul_f32 v[18:19], v[18:19], s[60:61] op_sel_hi:[1,0]\n"
  "v_pk_mul_f32 v[20:21], v[20:21], s[60:61] op_sel_hi:[1,0]\n"
  "v_pk_mul_f32 v[22:23], v[22:23], s[60:61] op_sel_hi:[1,0]\n"
  "v_mfma_f32_16x16x32_bf16 v[28:31], v[252:255], v[76:79], v[92:95]\n"
  "v_exp_f32 v16, v16\n"
  "v_mfma_f32_16x16x32_bf16 v[28:31], v[248:251], v[72:75], v[28:31]\n"
  "v_exp_f32 v17, v17\n"
  "v_mfma_f32_16x16x32_bf16 v[28:31], v[244:247], v[68:71], v[28:31]\n"
  "v_exp_f32 v18, v18\n"
  "v_mfma_f32_16x16x32_bf16 v[28:31], v[240:243], v[64:67], v[28:31]\n"
  "s_sub_u32 s56, s56, 1\n"
  "v_exp_f32 v19, v19\n"
  "v_mfma_f32_16x16x32_bf16 v[28:31], v[236:239], v[60:63], v[28:31]\n"
  "s_cmp_eq_u32 s56, 0\n"
  "v_cvt_pk_bf16_f32 v16, v16, v17\n"
  "v_cvt_pk_bf16_f32 v17, v18, v19\n"
  "v_mfma_f32_16x16x32_bf16 v[28:31], v[232:235], v[56:59], v[28:31]\n"
  "s_waitcnt lgkmcnt(3)\n"
  "s_cbranch_scc1 A_X0_%=\n"
  "A_H0_%=:\n"
  "v_mfma_f32_16x16x32_bf16 v[0:3], a[0:3], v[32:35], v[92:95]\n"
  "s_add_u32 s66, s66, 6144\n"
  "v_exp_f32 v20, v20\n"
  "v_mfma_f32_16x16x32_bf16 v[0:3], a[4:7], v[36:39], v[0:3]\n"
  "s_cmp_eq_u32 s66, 86016\n"
  "v_exp_f32 v21, v21\n"
  "v_mfma_f32_16x16x32_bf16 v[0:3], a[8:11], v[40:43], v[0:3]\n"
  "s_cselect_b32 s66, 49152, s66\n"
  "v_exp_f32 v22, v22\n"
  "v_mfma_f32_16x16x32_bf16 v[0:3], a[12:15], v[44:47], v[0:3]\n"
  "v_exp_f32 v23, v23\n"
  "v_mfma_f32_16x16x32_bf16 v[0:3], a[16:19], v[48:51], v[0:3]\n"
  "v_cvt_pk_bf16_f32 v18, v20, v21\n"
  "v_cvt_pk_bf16_f32 v19, v22, v23\n"
  "v_mfma_f32_16x16x32_bf16 v[0:3], a[20:23], v[52:55], v[0:3]\n"
  "ds_write_b128 v80, v[16:19] offset:6144\n"
  "v_pk_mul_f32 v[24:25], v[24:25], s[60:61] op_sel_hi:[1,0]\n"
  "v_pk_mul_f32 v[26:27], v[26:27], s[60:61] op_sel_hi:[1,0]\n"
  "v_pk_mul_f32 v[28:29], v[28:29], s[60:61] op_sel_hi:[1,0]\n"
  "v_pk_mul_f32 v[30:31], v[30:31], s[60:61] op_sel_hi:[1,0]\n"
  "v_mfma_f32_16x16x32_bf16 v[4:7], a[44:47], v[52:55], v[92:95]\n"
  "v_exp_f32 v24, v24\n"
  "v_mfma_f32_16x16x32_bf16 v[4:7], a[40:43], v[48:51], v[4:7]\n"
  "v_exp_f32 v25, v25\n"
  "v_mfma_f32_16x16x32_bf16 v[4:7], a[36:39], v[44:47], v[4:7]\n"
  "v_exp_f32 v26, v26\n"
  "v_mfma_f32_16x16x32_bf16 v[4:7], a[32:35], v[40:43], v[4:7]\n"
  "v_exp_f32 v27, v27\n"
  "v_mfma_f32_16x16x32_bf16 v[4:7], a[28:31], v[36:39], v[4:7]\n"
  "v_cvt_pk_bf16_f32 v24, v24, v25\n"
  "v_cvt_pk_bf16_f32 v25, v26, v27\n"
  "v_mfma_f32_16x16x32_bf16 v[4:7], a[24:27], v[32:35], v[4:7]\n"
  "v_add_u32 v81, s66, v83\n"
  "v_add_u32 v82, s66, v84\n"
  "v_mfma_f32_16x16x32_bf16 v[8:11], a[48:51], v[32:35], v[92:95]\n"
  "v_exp_f32 v28, v28\n"
  "v_mfma_f32_16x16x32_bf16 v[8:11], a[52:55], v[36:39], v[8:11]\n"
  "v_exp_f32 v29, v29\n"
  "v_mfma_f32_16x16x32_bf16 v[8:11], a[56:59], v[40:43], v[8:11]\n"
  "v_exp_f32 v30, v30\n"
  "v_mfma_f32_16x16x32_bf16 v[8:11], a[60:63], v[44:47], v[8:11]\n"
  "v_exp_f32 v31, v31\n"
  "v_mfma_f32_16x16x32_bf16 v[8:11], a[64:67], v[48:51], v[8:11]\n"
  "v_cvt_pk_bf16_f32 v26, v28, v29\n"
  "v_cvt_pk_bf16_f32 v27, v30, v31\n"
  "v_mfma_f32_16x16x32_bf16 v[8:11], a[68:71], v[52:55], v[8:11]\n"
  "ds_write_b128 v80, v[24:27] offset:7168\n"
  "v_pk_mul_f32 v[0:1], v[0:1], s[60:61] op_sel_hi:[1,0]\n"
  "v_pk_mul_f32 v[2:3], v[2:3], s[60:61] op_sel_hi:[1,0]\n"
  "v_pk_mul_f32 v[4:5], v[4:5], s[60:61] op_sel_hi:[1,0]\n"
  "v_pk_mul_f32 v[6:7], v[6:7], s[60:61] op_sel_hi:[1,0]\n"
  "v_mfma_f32_16x16x32_bf16 v[12:15], a[92:95], v[52:55], v[92:95]\n"
  "v_exp_f32 v0, v0\n"
  "v_mfma_f32_16x16x32_bf16 v[12:15], a[88:91], v[48:51], v[12:15]\n"
  "v_exp_f32 v1, v1\n"
  "v_mfma_f32_16x16x32_bf16 v[12:15], a[84:87], v[44:47], v[12:15]\n"
  "v_exp_f32 v2, v2\n"
  "v_mfma_f32_16x16x32_bf16 v[12:15], a[80:83], v[40:43], v[12:15]\n"
  "v_exp_f32 v3, v3\n"
  "v_mfma_f32_16x16x32_bf16 v[12:15], a[76:79], v[36:39], v[12:15]\n"
  "v_cvt_pk_bf16_f32 v0, v0, v1\n"
  "v_cvt_pk_bf16_f32 v1, v2, v3\n"
  "v_mfma_f32_16x16x32_bf16 v[12:15], a[72:75], v[32:35], v[12:15]\n"
  "s_waitcnt lgkmcnt(0)\n"
  "s_barrier\n"
  "v_xor_b32 v80, 0x4000, v80\n"
  "v_mfma_f32_16x16x32_bf16 v[16:19], a[96:99], v[32:35], v[92:95]\n"
  "v_exp_f32 v4, v4\n"
  "v_mfma_f32_16x16x32_bf16 v[16:19], a[100:103], v[36:39], v[16:19]\n"
  "v_exp_f32 v5, v5\n"
  "v_mfma_f32_16x16x32_bf16 v[16:19], a[104:107], v[40:43], v[16:19]\n"
  "v_exp_f32 v6, v6\n"
  "v_mfma_f32_16x16x32_bf16 v[16:19], a[108:111], v[44:47], v[16:19]\n"
  "v_exp_f32 v7, v7\n"
  "v_mfma_f32_16x16x32_bf16 v[16:19], a[112:115], v[48:51], v[16:19]\n"
  "v_cvt_pk_bf16_f32 v2, v4, v5\n"
  "v_cvt_pk_bf16_f32 v3, v6, v7\n"
  "v_mfma_f32_16x16x32_bf16 v[16:19], a[116:119], v[52:55], v[16:19]\n"
  "ds_write_b128 v80, v[0:3] offset:0\n"
  "v_pk_mul_f32 v[8:9], v[8:9], s[60:61] op_sel_hi:[1,0]\n"
  "v_pk_mul_f32 v[10:11], v[10:11], s[60:61] op_sel_hi:[1,0]\n"
  "v_pk_mul_f32 v[12:13], v[12:13], s[60:61] op_sel_hi:[1,0]\n"
  "v_pk_mul_f32 v[14:15], v[14:15], s[60:61] op_sel_hi:[1,0]\n"
  "v_mfma_f32_16x16x32_bf16 v[20:23], a[140:143], v[52:55], v[92:95]\n"
  "v_exp_f32 v8, v8\n"
  "v_mfma_f32_16x16x32_bf16 v[20:23], a[136:139], v[48:51], v[20:23]\n"
  "v_exp_f32 v9, v9\n"
  "v_mfma_f32_16x16x32_bf16 v[20:23], a[132:135], v[44:47], v[20:23]\n"
  "v_exp_f32 v10, v10\n"
  "v_mfma_f32_16x16x32_bf16 v[20:23], a[128:131], v[40:43], v[20:23]\n"
  "v_exp_f32 v11, v11\n"
  "v_mfma_f32_16x16x32_bf16 v[20:23], a[124:127], v[36:39], v[20:23]\n"
  "v_cvt_pk_bf16_f32 v8, v8, v9\n"
  "v_cvt_pk_bf16_f32 v9, v10, v11\n"
  "v_mfma_f32_16x16x32_bf16 v[20:23], a[120:123], v[32:35], v[20:23]\n"
  "ds_read_b128 v[56:59], v81 offset:0\n"
  "ds_read_b128 v[60:63], v82 offset:0\n"
  "v_mfma_f32_16x16x32_bf16 v[24:27], a[144:147], v[32:35], v[92:95]\n"
  "v_exp_f32 v12, v12\n"
  "v_mfma_f32_16x16x32_bf16 v[24:27], a[148:151], v[36:39], v[24:27]\n"
  "v_exp_f32 v13, v13\n"
  "v_mfma_f32_16x16x32_bf16 v[24:27], a[152:155], v[40:43], v[24:27]\n"
  "v_exp_f32 v14, v14\n"
  "v_mfma_f32_16x16x32_bf16 v[24:27], a[156:159], v[44:47], v[24:27]\n"
  "v_exp_f32 v15, v15\n"
  "v_mfma_f32_16x16x32_bf16 v[24:27], a[160:163], v[48:51], v[24:27]\n"
  "v_cvt_pk_bf16_f32 v10, v12, v13\n"
  "v_cvt_pk_bf16_f32 v11, v14, v15\n"
  "v_mfma_f32_16x16x32_bf16 v[24:27], a[164:167], v[52:55], v[24:27]\n"
  "ds_write_b128 v80, v[8:11] offset:1024\n"
  "v_pk_mul_f32 v[16:17], v[16:17], s[60:61] op_sel_hi:[1,0]\n"
  "v_pk_mul_f32 v[18:19], v[18:19], s[60:61] op_sel_hi:[1,0]\n"
  "v_pk_mul_f32 v[20:21], v[20:21], s[60:61] op_sel_hi:[1,0]\n"
  "v_pk_mul_f32 v[22:23], v[22:23], s[60:61] op_sel_hi:[1,0]\n"
  "v_mfma_f32_16x16x32_bf16 v[28:31], a[188:191], v[52:55], v[92:95]\n"
  "v_exp_f32 v16, v16\n"
  "v_mfma_f32_16x16x32_bf16 v[28:31], a[184:187], v[48:51], v[28:31]\n"
  "v_exp_f32 v17, v17\n"
  "v_mfma_f32_16x16x32_bf16 v[28:31], a[180:183], v[44:47], v[28:31]\n"
  "v_exp_f32 v18, v18\n"
  "v_mfma_f32_16x16x32_bf16 v[28:31], a[176:179], v[40:43], v[28:31]\n"
  "v_exp_f32 v19, v19\n"
  "v_mfma_f32_16x16x32_bf16 v[28:31], a[172:175], v[36:39], v[28:31]\n"
  "v_cvt_pk_bf16_f32 v16, v16, v17\n"
  "v_cvt_pk_bf16_f32 v17, v18, v19\n"
  "v_mfma_f32_16x16x32_bf16 v[28:31], a[168:171], v[32:35], v[28:31]\n"
  "ds_read_b128 v[64:67], v81 offset:2048\n"
  "ds_read_b128 v[68:71], v82 offset:2048\n"
  "v_mfma_f32_16x16x32_bf16 v[0:3], a[192:195], v[32:35], v[92:95]\n"
  "v_exp_f32 v20, v20\n"
  "v_mfma_f32_16x16x32_bf16 v[0:3], a[196:199], v[36:39], v[0:3]\n"
  "v_exp_f32 v21, v21\n"
  "v_mfma_f32_16x16x32_bf16 v[0:3], a[200:203], v[40:43], v[0:3]\n"
  "v_exp_f32 v22, v22\n"
  "v_mfma_f32_16x16x32_bf16 v[0:3], a[204:207], v[44:47], v[0:3]\n"
  "v_exp_f32 v23, v23\n"
  "v_mfma_f32_16x16x32_bf16 v[0:3], a[208:211], v[48:51], v[0:3]\n"
  "v_cvt_pk_bf16_f32 v18, v20, v21\n"
  "v_cvt_pk_bf16_f32 v19, v22, v23\n"
  "v_mfma_f32_16x16x32_bf16 v[0:3], a[212:215], v[52:55], v[0:3]\n"
  "ds_write_b128 v80, v[16:19] offset:2048\n"
  "v_pk_mul_f32 v[24:25], v[24:25], s[60:61] op_sel_hi:[1,0]\n"
  "v_pk_mul_f32 v[26:27], v[26:27], s[60:61] op_sel_hi:[1,0]\n"
  "v_pk_mul_f32 v[28:29], v[28:29], s[60:61] op_sel_hi:[1,0]\n"
  "v_pk_mul_f32 v[30:31], v[30:31], s[60:61] op_sel_hi:[1,0]\n"
  "v_mfma_f32_16x16x32_bf16 v[4:7], a[236:239], v[52:55], v[92:95]\n"
  "v_exp_f32 v24, v24\n"
  "v_mfma_f32_16x16x32_bf16 v[4:7], a[232:235], v[48:51], v[4:7]\n"
  "v_exp_f32 v25, v25\n"
  "v_mfma_f32_16x16x32_bf16 v[4:7], a[228:231], v[44:47], v[4:7]\n"
  "v_exp_f32 v26, v26\n"
  "v_mfma_f32_16x16x32_bf16 v[4:7], a[224:227], v[40:43], v[4:7]\n"
  "v_exp_f32 v27, v27\n"
  "v_mfma_f32_16x16x32_bf16 v[4:7], a[220:223], v[36:39], v[4:7]\n"
  "v_cvt_pk_bf16_f32 v24, v24, v25\n"
  "v_cvt_pk_bf16_f32 v25, v26, v27\n"
  "v_mfma_f32_16x16x32_bf16 v[4:7], a[216:219], v[32:35], v[4:7]\n"
  "ds_read_b128 v[72:75], v81 offset:4096\n"
  "ds_read_b128 v[76:79], v82 offset:4096\n"
  "v_mfma_f32_16x16x32_bf16 v[8:11], a[240:243], v[32:35], v[92:95]\n"
  "v_exp_f32 v28, v28\n"
  "v_mfma_f32_16x16x32_bf16 v[8:11], a[244:247], v[36:39], v[8:11]\n"
  "v_exp_f32 v29, v29\n"
  "v_mfma_f32_16x16x32_bf16 v[8:11], a[248:251], v[40:43], v[8:11]\n"
  "v_exp_f32 v30, v30\n"
  "v_mfma_f32_16x16x32_bf16 v[8:11], a[252:255], v[44:47], v[8:11]\n"
  "v_exp_f32 v31, v31\n"
  "v_mfma_f32_16x16x32_bf16 v[8:11], v[128:131], v[48:51], v[8:11]\n"
  "v_cvt_pk_bf16_f32 v26, v28, v29\n"
  "v_cvt_pk_bf16_f32 v27, v30, v31\n"
  "v_mfma_f32_16x16x32_bf16 v[8:11], v[132:135], v[52:55], v[8:11]\n"
  "ds_write_b128 v80, v[24:27] offset:3072\n"
  "v_pk_mul_f32 v[0:1], v[0:1], s[60:61] op_sel_hi:[1,0]\n"
  "v_pk_mul_f32 v[2:3], v[2:3], s[60:61] op_sel_hi:[1,0]\n"
  "v_pk_mul_f32 v[4:5], v[4:5], s[60:61] op_sel_hi:[1,0]\n"
  "v_pk_mul_f32 v[6:7], v[6:7], s[60:61] op_sel_hi:[1,0]\n"
  "v_mfma_f32_16x16x32_bf16 v[12:15], v[156:159], v[52:55], v[92:95]\n"
  "v_exp_f32 v0, v0\n"
  "v_mfma_f32_16x16x32_bf16 v[12:15], v[152:155], v[48:51], v[12:15]\n"
  "v_exp_f32 v1, v1\n"
  "v_mfma_f32_16x16x32_bf16 v[12:15], v[148:151], v[44:47], v[12:15]\n"
  "v_exp_f32 v2, v2\n"
  "v_mfma_f32_16x16x32_bf16 v[12:15], v[144:147], v[40:43], v[12:15]\n"
  "v_exp_f32 v3, v3\n"
  "v_mfma_f32_16x16x32_bf16 v[12:15], v[140:143], v[36:39], v[12:15]\n"
  "v_cvt_pk_bf16_f32 v0, v0, v1\n"
  "v_cvt_pk_bf16_f32 v1, v2, v3\n"
  "v_mfma_f32_16x16x32_bf16 v[12:15], v[136:139], v[32:35], v[12:15]\n"
  "v_mul_f32 v8, s60, v8\n"
  "v_mul_f32 v9, s60, v9\n"
  "v_mfma_f32_16x16x32_bf16 v[16:19], v[160:163], v[32:35], v[92:95]\n"
  "v_exp_f32 v4, v4\n"
  "v_mfma_f32_16x16x32_bf16 v[16:19], v[164:167], v[36:39], v[16:19]\n"
  "v_exp_f32 v5, v5\n"
  "v_mfma_f32_16x16x32_bf16 v[16:19], v[168:171], v[40:43], v[16:19]\n"
  "v_exp_f32 v6, v6\n"
  "v_mfma_f32_16x16x32_bf16 v[16:19], v[172:175], v[44:47], v[16:19]\n"
  "v_exp_f32 v7, v7\n"
  "v_mfma_f32_16x16x32_bf16 v[16:19], v[176:179], v[48:51], v[16:19]\n"
  "v_cvt_pk_bf16_f32 v2, v4, v5\n"
  "v_cvt_pk_bf16_f32 v3, v6, v7\n"
  "v_mfma_f32_16x16x32_bf16 v[16:19], v[180:183], v[52:55], v[16:19]\n"
  "ds_write_b128 v80, v[0:3] offset:4096\n"
  "v_pk_mul_f32 v[10:11], v[10:11], s[60:61] op_sel_hi:[1,0]\n"
  "v_pk_mul_f32 v[12:13], v[12:13], s[60:61] op_sel_hi:[1,0]\n"
  "v_pk_mul_f32 v[14:15], v[14:15], s[60:61] op_sel_hi:[1,0]\n"
  "v_mfma_f32_16x16x32_bf16 v[20:23], v[204:207], v[52:55], v[92:95]\n"
  "v_exp_f32 v8, v8\n"
  "v_mfma_f32_16x16x32_bf16 v[20:23], v[200:203], v[48:51], v[20:23]\n"
  "v_exp_f32 v9, v9\n"
  "v_mfma_f32_16x16x32_bf16 v[20:23], v[196:199], v[44:47], v[20:23]\n"
  "v_exp_f32 v10, v10\n"
  "v_mfma_f32_16x16x32_bf16 v[20:23], v[192:195], v[40:43], v[20:23]\n"
  "v_exp_f32 v11, v11\n"
  "v_mfma_f32_16x16x32_bf16 v[20:23], v[188:191], v[36:39], v[20:23]\n"
  "v_cvt_pk_bf16_f32 v8, v8, v9\n"
  "v_cvt_pk_bf16_f32 v9, v10, v11\n"
  "v_mfma_f32_16x16x32_bf16 v[20:23], v[184:187], v[32:35], v[20:23]\n"
  "v_mul_f32 v16, s60, v16\n"
  "v_mul_f32 v17, s60, v17\n"
  "v_mfma_f32_16x16x32_bf16 v[24:27], v[208:211], v[32:35], v[92:95]\n"
  "v_exp_f32 v12, v12\n"
  "v_mfma_f32_16x16x32_bf16 v[24:27], v[212:215], v[36:39], v[24:27]\n"
  "v_exp_f32 v13, v13\n"
  "v_mfma_f32_16x16x32_bf16 v[24:27], v[216:219], v[40:43], v[24:27]\n"
  "v_exp_f32 v14, v14\n"
  "v_mfma_f32_16x16x32_bf16 v[24:27], v[220:223], v[44:47], v[24:27]\n"
  "v_exp_f32 v15, v15\n"
  "v_mfma_f32_16x16x32_bf16 v[24:27], v[224:227], v[48:51], v[24:27]\n"
  "v_cvt_pk_bf16_f32 v10, v12, v13\n"
  "v_cvt_pk_bf16_f32 v11, v14, v15\n"
  "v_mfma_f32_16x16x32_bf16 v[24:27], v[228:231], v[52:55], v[24:27]\n"
  "ds_write_b128 v80, v[8:11] offset:5120\n"
  "v_pk_mul_f32 v[18:19], v[18:19], s[60:61] op_sel_hi:[1,0]\n"
  "v_pk_mul_f32 v[20:21], v[20:21], s[60:61] op_sel_hi:[1,0]\n"
  "v_pk_mul_f32 v[22:23], v[22:23], s[60:61] op_sel_hi:[1,0]\n"
  "v_mfma_f32_16x16x32_bf16 v[28:31], v[252:255], v[52:55], v[92:95]\n"
  "v_exp_f32 v16, v16\n"
  "v_mfma_f32_16x16x32_bf16 v[28:31], v[248:251], v[48:51], v[28:31]\n"
  "v_exp_f32 v17, v17\n"
  "v_mfma_f32_16x16x32_bf16 v[28:31], v[244:247], v[44:47], v[28:31]\n"
  "v_exp_f32 v18, v18\n"
  "v_mfma_f32_16x16x32_bf16 v[28:31], v[240:243], v[40:43], v[28:31]\n"
  "s_sub_u32 s56, s56, 1\n"
  "v_exp_f32 v19, v19\n"
  "v_mfma_f32_16x16x32_bf16 v[28:31], v[236:239], v[36:39], v[28:31]\n"
  "s_cmp_eq_u32 s56, 0\n"
  "v_cvt_pk_bf16_f32 v16, v16, v17\n"
  "v_cvt_pk_bf16_f32 v17, v18, v19\n"
  "v_mfma_f32_16x16x32_bf16 v[28:31], v[232:235], v[32:35], v[28:31]\n"
  "s_waitcnt lgkmcnt(3)\n"
  "s_cbranch_scc0 A_H1_%=\n"
  "A_X1_%=:\n"
  "s_mov_b32 s52, s53\n"
  "s_mov_b32 s53, s54\n"
  "s_mov_b32 s54, s55\n"
  "s_cmp_eq_u32 s52, 0\n"
  "s_cbranch_scc1 A_LAST_%=\n"
  "s_lshr_b32 s62, s52, 16\n"
  "s_lshl1_add_u32 s62, s62, s65\n"
  "s_mul_i32 s57, s62, 98304\n"
  "s_and_b32 s56, s52, 0xff\n"
  "s_cmp_lg_u32 s53, 0\n"
  "s_cselect_b32 s62, 1, 0\n"
  "s_sub_u32 s56, s56, s62\n"
  "s_add_u32 s57, s57, 18432\n"
  "v_mfma_f32_16x16x32_bf16 v[0:3], a[0:3], v[56:59], v[92:95]\n"
  "s_add_u32 s66, s66, 6144\n"
  "s_cmp_eq_u32 s66, 86016\n"
  "s_cselect_b32 s66, 49152, s66\n"
  "v_exp_f32 v20, v20\n"
  "v_mfma_f32_16x16x32_bf16 v[0:3], a[4:7], v[60:63], v[0:3]\n"
  "ds_read_b128 a[0:3], v87 offset:0\n"
  "ds_read_b128 a[4:7], v88 offset:0\n"
  "v_exp_f32 v21, v21\n"
  "v_mfma_f32_16x16x32_bf16 v[0:3], a[8:11], v[64:67], v[0:3]\n"
  "v_exp_f32 v22, v22\n"
  "v_mfma_f32_16x16x32_bf16 v[0:3], a[12:15], v[68:71], v[0:3]\n"
  "v_exp_f32 v23, v23\n"
  "v_mfma_f32_16x16x32_bf16 v[0:3], a[16:19], v[72:75], v[0:3]\n"
  "v_cvt_pk_bf16_f32 v18, v20, v21\n"
  "v_cvt_pk_bf16_f32 v19, v22, v23\n"
  "v_mfma_f32_16x16x32_bf16 v[0:3], a[20:23], v[76:79], v[0:3]\n"
  "ds_read_b128 a[8:11], v87 offset:2048\n"
  "ds_read_b128 a[12:15], v88 offset:2048\n"
  "ds_write_b128 v80, v[16:19] offset:6144\n"
  "v_pk_mul_f32 v[24:25], v[24:25], s[60:61] op_sel_hi:[1,0]\n"
  "v_pk_mul_f32 v[26:27], v[26:27], s[60:61] op_sel_hi:[1,0]\n"
  "v_pk_mul_f32 v[28:29], v[28:29], s[60:61] op_sel_hi:[1,0]\n"
  "v_pk_mul_f32 v[30:31], v[30:31], s[60:61] op_sel_hi:[1,0]\n"
  "v_mfma_f32_16x16x32_bf16 v[4:7], a[44:47], v[76:79], v[92:95]\n"
  "v_exp_f32 v24, v24\n"
  "v_mfma_f32_16x16x32_bf16 v[4:7], a[40:43], v[72:75], v[4:7]\n"
  "s_add_u32 m0, s59, -256\n"
  "v_exp_f32 v25, v25\n"
  "v_mfma_f32_16x16x32_bf16 v[4:7], a[36:39], v[68:71], v[4:7]\n"
  "s_waitcnt lgkmcnt(3)\n"
  "buffer_load_dwordx4 v112, s[40:43], s57 offen offset:256 nt lds\n"
  "buffer_load_dwordx4 v113, s[40:43], s57 offen offset:1280 nt lds\n"
  "v_exp_f32 v26, v26\n"
  "v_mfma_f32_16x16x32_bf16 v[4:7], a[32:35], v[64:67], v[4:7]\n"
  "ds_read_b128 a[16:19], v87 offset:4096\n"
  "ds_read_b128 a[20:23], v88 offset:4096\n"
  "v_exp_f32 v27, v27\n"
  "v_mfma_f32_16x16x32_bf16 v[4:7], a[28:31], v[60:63], v[4:7]\n"
  "s_add_u32 m0, s59, 1920\n"
  "v_cvt_pk_bf16_f32 v24, v24, v25\n"
  "v_cvt_pk_bf16_f32 v25, v26, v27\n"
  "v_mfma_f32_16x16x32_bf16 v[4:7], a[24:27], v[56:59], v[4:7]\n"
  "s_waitcnt lgkmcnt(3)\n"
  "buffer_load_dwordx4 v112, s[40:43], s57 offen offset:128 nt lds\n"
  "buffer_load_dwordx4 v113, s[40:43], s57 offen offset:1152 nt lds\n"
  "v_add_u32 v81, s66, v83\n"
  "v_add_u32 v82, s66, v84\n"
  "v_mfma_f32_16x16x32_bf16 v[8:11], a[48:51], v[56:59], v[92:95]\n"
  "ds_read_b128 a[40:43], v87 offset:6144\n"
  "ds_read_b128 a[44:47], v88 offset:6144\n"
  "v_exp_f32 v28, v28\n"
  "v_mfma_f32_16x16x32_bf16 v[8:11], a[52:55], v[60:63], v[8:11]\n"
  "v_exp_f32 v29, v29\n"
  "v_mfma_f32_16x16x32_bf16 v[8:11], a[56:59], v[64:67], v[8:11]\n"
  "s_add_u32 m0, s59, 4096\n"
  "v_exp_f32 v30, v30\n"
  "v_mfma_f32_16x16x32_bf16 v[8:11], a[60:63], v[68:71], v[8:11]\n"
  "s_waitcnt lgkmcnt(2)\n"
  "buffer_load_dwordx4 v112, s[40:43], s57 offen offset:0 nt lds\n"
  "buffer_load_dwordx4 v113, s[40:43], s57 offen offset:1024 nt lds\n"
  "s_add_u32 s57, s57, 6144\n"
  "v_exp_f32 v31, v31\n"
  "v_mfma_f32_16x16x32_bf16 v[8:11], a[64:67], v[72:75], v[8:11]\n"
  "ds_read_b128 a[32:35], v87 offset:8192\n"
  "ds_read_b128 a[36:39], v88 offset:8192\n"
  "v_cvt_pk_bf16_f32 v26, v28, v29\n"
  "v_cvt_pk_bf16_f32 v27, v30, v31\n"
  "v_mfma_f32_16x16x32_bf16 v[8:11], a[68:71], v[76:79], v[8:11]\n"
  "s_add_u32 m0, s59, 6144\n"
  "ds_write_b128 v80, v[24:27] offset:7168\n"
  "v_pk_mul_f32 v[0:1], v[0:1], s[60:61] op_sel_hi:[1,0]\n"
  "v_pk_mul_f32 v[2:3], v[2:3], s[60:61] op_sel_hi:[1,0]\n"
  "v_pk_mul_f32 v[4:5], v[4:5], s[60:61] op_sel_hi:[1,0]\n"
  "v_pk_mul_f32 v[6:7], v[6:7], s[60:61] op_sel_hi:[1,0]\n"
  "v_mfma_f32_16x16x32_bf16 v[12:15], a[92:95], v[76:79], v[92:95]\n"
  "s_waitcnt lgkmcnt(3)\n"
  "buffer_load_dwordx4 v112, s[40:43], s57 offen offset:0 nt lds\n"
  "buffer_load_dwordx4 v113, s[40:43], s57 offen offset:1024 nt lds\n"
  "v_exp_f32 v0, v0\n"
  "v_mfma_f32_16x16x32_bf16 v[12:15], a[88:91], v[72:75], v[12:15]\n"
  "ds_read_b128 a[24:27], v87 offset:10240\n"
  "ds_read_b128 a[28:31], v88 offset:10240\n"
  "v_exp_f32 v1, v1\n"
  "v_mfma_f32_16x16x32_bf16 v[12:15], a[84:87], v[68:71], v[12:15]\n"
  "v_exp_f32 v2, v2\n"
  "v_mfma_f32_16x16x32_bf16 v[12:15], a[80:83], v[64:67], v[12:15]\n"
  "s_add_u32 m0, s59, 8064\n"
  "v_exp_f32 v3, v3\n"
  "v_mfma_f32_16x16x32_bf16 v[12:15], a[76:79], v[60:63], v[12:15]\n"
  "s_waitcnt lgkmcnt(3)\n"
  "buffer_load_dwordx4 v112, s[40:43], s57 offen offset:128 nt lds\n"
  "buffer_load_dwordx4 v113, s[40:43], s57 offen offset:1152 nt lds\n"
  "v_cvt_pk_bf16_f32 v0, v0, v1\n"
  "v_cvt_pk_bf16_f32 v1, v2, v3\n"
  "v_mfma_f32_16x16x32_bf16 v[12:15], a[72:75], v[56:59], v[12:15]\n"
  "ds_read_b128 a[48:51], v87 offset:12288\n"
  "ds_read_b128 a[52:55], v88 offset:12288\n"
  "s_waitcnt lgkmcnt(4)\n"
  "s_barrier\n"
  "v_xor_b32 v80, 0x4000, v80\n"
  "v_mfma_f32_16x16x32_bf16 v[16:19], a[96:99], v[56:59], v[92:95]\n"
  "v_exp_f32 v4, v4\n"
  "v_mfma_f32_16x16x32_bf16 v[16:19], a[100:103], v[60:63], v[16:19]\n"
  "s_add_u32 m0, s59, 9984\n"
  "v_exp_f32 v5, v5\n"
  "v_mfma_f32_16x16x32_bf16 v[16:19], a[104:107], v[64:67], v[16:19]\n"
  "s_waitcnt lgkmcnt(2)\n"
  "buffer_load_dwordx4 v112, s[40:43], s57 offen offset:256 nt lds\n"
  "buffer_load_dwordx4 v113, s[40:43], s57 offen offset:1280 nt lds\n"
  "s_add_u32 s57, s57, 6144\n"
  "v_exp_f32 v6, v6\n"
  "v_mfma_f32_16x16x32_bf16 v[16:19], a[108:111], v[68:71], v[16:19]\n"
  "ds_read_b128 a[56:59], v87 offset:14336\n"
  "ds_read_b128 a[60:63], v88 offset:14336\n"
  "v_exp_f32 v7, v7\n"
  "v_mfma_f32_16x16x32_bf16 v[16:19], a[112:115], v[72:75], v[16:19]\n"
  "s_add_u32 m0, s59, 12032\n"
  "v_cvt_pk_bf16_f32 v2, v4, v5\n"
  "v_cvt_pk_bf16_f32 v3, v6, v7\n"
  "v_mfma_f32_16x16x32_bf16 v[16:19], a[116:119], v[76:79], v[16:19]\n"
  "s_waitcnt lgkmcnt(2)\n"
  "buffer_load_dwordx4 v112, s[40:43], s57 offen offset:256 nt lds\n"
  "buffer_load_dwordx4 v113, s[40:43], s57 offen offset:1280 nt lds\n"
  "ds_write_b128 v80, v[0:3] offset:0\n"
  "v_pk_mul_f32 v[8:9], v[8:9], s[60:61] op_sel_hi:[1,0]\n"
  "v_pk_mul_f32 v[10:11], v[10:11], s[60:61] op_sel_hi:[1,0]\n"
  "v_pk_mul_f32 v[12:13], v[12:13], s[60:61] op_sel_hi:[1,0]\n"
  "v_pk_mul_f32 v[14:15], v[14:15], s[60:61] op_sel_hi:[1,0]\n"
  "v_mfma_f32_16x16x32_bf16 v[20:23], a[140:143], v[76:79], v[92:95]\n"
  "ds_read_b128 a[64:67], v87 offset:16384\n"
  "ds_read_b128 a[68:71], v88 offset:16384\n"
  "v_exp_f32 v8, v8\n"
  "v_mfma_f32_16x16x32_bf16 v[20:23], a[136:139], v[72:75], v[20:23]\n"
  "v_exp_f32 v9, v9\n"
  "v_mfma_f32_16x16x32_bf16 v[20:23], a[132:135], v[68:71], v[20:23]\n"
  "s_add_u32 m0, s59, 14208\n"
  "v_exp_f32 v10, v10\n"
  "v_mfma_f32_16x16x32_bf16 v[20:23], a[128:131], v[64:67], v[20:23]\n"
  "s_waitcnt lgkmcnt(3)\n"
  "buffer_load_dwordx4 v112, s[40:43], s57 offen offset:128 nt lds\n"
  "buffer_load_dwordx4 v113, s[40:43], s57 offen offset:1152 nt lds\n"
  "v_exp_f32 v11, v11\n"
  "v_mfma_f32_16x16x32_bf16 v[20:23], a[124:127], v[60:63], v[20:23]\n"
  "s_waitcnt vmcnt(14)\n"
  "ds_read_b128 a[88:91], v87 offset:0\n"
  "ds_read_b128 a[92:95], v88 offset:0\n"
  "v_cvt_pk_bf16_f32 v8, v8, v9\n"
  "v_cvt_pk_bf16_f32 v9, v10, v11\n"
  "v_mfma_f32_16x16x32_bf16 v[20:23], a[120:123], v[56:59], v[20:23]\n"
  "s_add_u32 m0, s59, 16384\n"
  "ds_read_b128 v[32:35], v81 offset:0\n"
  "ds_read_b128 v[36:39], v82 offset:0\n"
  "v_mfma_f32_16x16x32_bf16 v[24:27], a[144:147], v[56:59], v[92:95]\n"
  "s_waitcnt lgkmcnt(4)\n"
  "buffer_load_dwordx4 v112, s[40:43], s57 offen offset:0 nt lds\n"
  "buffer_load_dwordx4 v113, s[40:43], s57 offen offset:1024 nt lds\n"
  "s_add_u32 s57, s57, 6144\n"
  "v_exp_f32 v12, v12\n"
  "v_mfma_f32_16x16x32_bf16 v[24:27], a[148:151], v[60:63], v[24:27]\n"
  "s_waitcnt vmcnt(14)\n"
  "ds_read_b128 a[80:83], v87 offset:2048\n"
  "ds_read_b128 a[84:87], v88 offset:2048\n"
  "v_exp_f32 v13, v13\n"
  "v_mfma_f32_16x16x32_bf16 v[24:27], a[152:155], v[64:67], v[24:27]\n"
  "v_exp_f32 v14, v14\n"
  "v_mfma_f32_16x16x32_bf16 v[24:27], a[156:159], v[68:71], v[24:27]\n"
  "s_add_u32 m0, s59, 0\n"
  "v_exp_f32 v15, v15\n"
  "v_mfma_f32_16x16x32_bf16 v[24:27], a[160:163], v[72:75], v[24:27]\n"
  "s_waitcnt lgkmcnt(4)\n"
  "buffer_load_dwordx4 v112, s[40:43], s57 offen offset:0 nt lds\n"
  "buffer_load_dwordx4 v113, s[40:43], s57 offen offset:1024 nt lds\n"
  "v_cvt_pk_bf16_f32 v10, v12, v13\n"
  "v_cvt_pk_bf16_f32 v11, v14, v15\n"
  "v_mfma_f32_16x16x32_bf16 v[24:27], a[164:167], v[76:79], v[24:27]\n"
  "s_waitcnt vmcnt(14)\n"
  "ds_read_b128 a[72:75], v87 offset:4096\n"
  "ds_read_b128 a[76:79], v88 offset:4096\n"
  "ds_write_b128 v80, v[8:11] offset:1024\n"
  "v_pk_mul_f32 v[16:17], v[16:17], s[60:61] op_sel_hi:[1,0]\n"
  "v_pk_mul_f32 v[18:19], v[18:19], s[60:61] op_sel_hi:[1,0]\n"
  "v_pk_mul_f32 v[20:21], v[20:21], s[60:61] op_sel_hi:[1,0]\n"
  "v_pk_mul_f32 v[22:23], v[22:23], s[60:61] op_sel_hi:[1,0]\n"
  "v_mfma_f32_16x16x32_bf16 v[28:31], a[188:191], v[76:79], v[92:95]\n"
  "v_exp_f32 v16, v16\n"
  "v_mfma_f32_16x16x32_bf16 v[28:31], a[184:187], v[72:75], v[28:31]\n"
  "s_add_u32 m0, s59, 1920\n"
  "v_exp_f32 v17, v17\n"
  "v_mfma_f32_16x16x32_bf16 v[28:31], a[180:183], v[68:71], v[28:31]\n"
  "s_waitcnt lgkmcnt(3)\n"
  "buffer_load_dwordx4 v112, s[40:43], s57 offen offset:128 nt lds\n"
  "buffer_load_dwordx4 v113, s[40:43], s57 offen offset:1152 nt lds\n"
  "v_exp_f32 v18, v18\n"
  "v_mfma_f32_16x16x32_bf16 v[28:31], a[176:179], v[64:67], v[28:31]\n"
  "s_waitcnt vmcnt(14)\n"
  "ds_read_b128 a[96:99], v87 offset:6144\n"
  "ds_read_b128 a[100:103], v88 offset:6144\n"
  "v_exp_f32 v19, v19\n"
  "v_mfma_f32_16x16x32_bf16 v[28:31], a[172:175], v[60:63], v[28:31]\n"
  "s_add_u32 m0, s59, 3840\n"
  "v_cvt_pk_bf16_f32 v16, v16, v17\n"
  "v_cvt_pk_bf16_f32 v17, v18, v19\n"
  "v_mfma_f32_16x16x32_bf16 v[28:31], a[168:171], v[56:59], v[28:31]\n"
  "s_waitcnt lgkmcnt(3)\n"
  "buffer_load_dwordx4 v112, s[40:43], s57 offen offset:256 nt lds\n"
  "buffer_load_dwordx4 v113, s[40:43], s57 offen offset:1280 nt lds\n"
  "s_add_u32 s57, s57, 6144\n"
  "ds_read_b128 v[40:43], v81 offset:2048\n"
  "ds_read_b128 v[44:47], v82 offset:2048\n"
  "v_mfma_f32_16x16x32_bf16 v[0:3], a[192:195], v[56:59], v[92:95]\n"
  "s_waitcnt vmcnt(14)\n"
  "ds_read_b128 a[104:107], v87 offset:8192\n"
  "ds_read_b128 a[108:111], v88 offset:8192\n"
  "v_exp_f32 v20, v20\n"
  "v_mfma_f32_16x16x32_bf16 v[0:3], a[196:199], v[60:63], v[0:3]\n"
  "v_exp_f32 v21, v21\n"
  "v_mfma_f32_16x16x32_bf16 v[0:3], a[200:203], v[64:67], v[0:3]\n"
  "s_add_u32 m0, s59, 5888\n"
  "v_exp_f32 v22, v22\n"
  "v_mfma_f32_16x16x32_bf16 v[0:3], a[204:207], v[68:71], v[0:3]\n"
  "s_waitcnt lgkmcnt(4)\n"
  "buffer_load_dwordx4 v112, s[40:43], s57 offen offset:256 nt lds\n"
  "buffer_load_dwordx4 v113, s[40:43], s57 offen offset:1280 nt lds\n"
  "v_exp_f32 v23, v23\n"
  "v_mfma_f32_16x16x32_bf16 v[0:3], a[208:211], v[72:75], v[0:3]\n"
  "s_waitcnt vmcnt(14)\n"
  "ds_read_b128 a[112:115], v87 offset:10240\n"
  "ds_read_b128 a[116:119], v88 offset:10240\n"
  "v_cvt_pk_bf16_f32 v18, v20, v21\n"
  "v_cvt_pk_bf16_f32 v19, v22, v23\n"
  "v_mfma_f32_16x16x32_bf16 v[0:3], a[212:215], v[76:79], v[0:3]\n"
  "s_add_u32 m0, s59, 8064\n"
  "ds_write_b128 v80, v[16:19] offset:2048\n"
  "v_pk_mul_f32 v[24:25], v[24:25], s[60:61] op_sel_hi:[1,0]\n"
  "v_pk_mul_f32 v[26:27], v[26:27], s[60:61] op_sel_hi:[1,0]\n"
  "v_pk_mul_f32 v[28:29], v[28:29], s[60:61] op_sel_hi:[1,0]\n"
  "v_pk_mul_f32 v[30:31], v[30:31], s[60:61] op_sel_hi:[1,0]\n"
  "v_mfma_f32_16x16x32_bf16 v[4:7], a[236:239], v[76:79], v[92:95]\n"
  "s_waitcnt lgkmcnt(3)\n"
  "buffer_load_dwordx4 v112, s[40:43], s57 offen offset:128 nt lds\n"
  "buffer_load_dwordx4 v113, s[40:43], s57 offen offset:1152 nt lds\n"
  "v_exp_f32 v24, v24\n"
  "v_mfma_f32_16x16x32_bf16 v[4:7], a[232:235], v[72:75], v[4:7]\n"
  "s_waitcnt vmcnt(14)\n"
  "ds_read_b128 a[136:139], v87 offset:12288\n"
  "ds_read_b128 a[140:143], v88 offset:12288\n"
  "v_exp_f32 v25, v25\n"
  "v_mfma_f32_16x16x32_bf16 v[4:7], a[228:231], v[68:71], v[4:7]\n"
  "v_exp_f32 v26, v26\n"
  "v_mfma_f32_16x16x32_bf16 v[4:7], a[224:227], v[64:67], v[4:7]\n"
  "s_add_u32 m0, s59, 10240\n"
  "v_exp_f32 v27, v27\n"
  "v_mfma_f32_16x16x32_bf16 v[4:7], a[220:223], v[60:63], v[4:7]\n"
  "s_waitcnt lgkmcnt(3)\n"
  "buffer_load_dwordx4 v112, s[40:43], s57 offen offset:0 nt lds\n"
  "buffer_load_dwordx4 v113, s[40:43], s57 offen offset:1024 nt lds\n"
  "s_add_u32 s57, s57, 6144\n"
  "v_cvt_pk_bf16_f32 v24, v24, v25\n"
  "v_cvt_pk_bf16_f32 v25, v26, v27\n"
  "v_mfma_f32_16x16x32_bf16 v[4:7], a[216:219], v[56:59], v[4:7]\n"
  "s_waitcnt vmcnt(14)\n"
  "ds_read_b128 a[128:131], v87 offset:14336\n"
  "ds_read_b128 a[132:135], v88 offset:14336\n"
  "ds_read_b128 v[48:51], v81 offset:4096\n"
  "ds_read_b128 v[52:55], v82 offset:4096\n"
  "v_mfma_f32_16x16x32_bf16 v[8:11], a[240:243], v[56:59], v[92:95]\n"
  "v_exp_f32 v28, v28\n"
  "v_mfma_f32_16x16x32_bf16 v[8:11], a[244:247], v[60:63], v[8:11]\n"
  "s_add_u32 m0, s59, 12288\n"
  "v_exp_f32 v29, v29\n"
  "v_mfma_f32_16x16x32_bf16 v[8:11], a[248:251], v[64:67], v[8:11]\n"
  "s_waitcnt lgkmcnt(4)\n"
  "buffer_load_dwordx4 v112, s[40:43], s57 offen offset:0 nt lds\n"
  "buffer_load_dwordx4 v113, s[40:43], s57 offen offset:1024 nt lds\n"
  "v_exp_f32 v30, v30\n"
  "v_mfma_f32_16x16x32_bf16 v[8:11], a[252:255], v[68:71], v[8:11]\n"
  "s_waitcnt vmcnt(14)\n"
  "ds_read_b128 a[120:123], v87 offset:16384\n"
  "ds_read_b128 a[124:127], v88 offset:16384\n"
  "v_exp_f32 v31, v31\n"
  "v_mfma_f32_16x16x32_bf16 v[8:11], v[128:131], v[72:75], v[8:11]\n"
  "s_add_u32 m0, s59, 14208\n"
  "v_cvt_pk_bf16_f32 v26, v28, v29\n"
  "v_cvt_pk_bf16_f32 v27, v30, v31\n"
  "v_mfma_f32_16x16x32_bf16 v[8:11], v[132:135], v[76:79], v[8:11]\n"
  "s_waitcnt lgkmcnt(4)\n"
  "buffer_load_dwordx4 v112, s[40:43], s57 offen offset:128 nt lds\n"
  "buffer_load_dwordx4 v113, s[40:43], s57 offen offset:1152 nt lds\n"
  "ds_write_b128 v80, v[24:27] offset:3072\n"
  "v_pk_mul_f32 v[0:1], v[0:1], s[60:61] op_sel_hi:[1,0]\n"
  "v_pk_mul_f32 v[2:3], v[2:3], s[60:61] op_sel_hi:[1,0]\n"
  "v_pk_mul_f32 v[4:5], v[4:5], s[60:61] op_sel_hi:[1,0]\n"
  "v_pk_mul_f32 v[6:7], v[6:7], s[60:61] op_sel_hi:[1,0]\n"
  "v_mfma_f32_16x16x32_bf16 v[12:15], v[156:159], v[76:79], v[92:95]\n"
  "s_waitcnt vmcnt(14)\n"
  "ds_read_b128 a[144:147], v87 offset:0\n"
  "ds_read_b128 a[148:151], v88 offset:0\n"
  "v_exp_f32 v0, v0\n"
  "v_mfma_f32_16x16x32_bf16 v[12:15], v[152:155], v[72:75], v[12:15]\n"
  "v_exp_f32 v1, v1\n"
  "v_mfma_f32_16x16x32_bf16 v[12:15], v[148:151], v[68:71], v[12:15]\n"
  "s_add_u32 m0, s59, 16128\n"
  "v_exp_f32 v2, v2\n"
  "v_mfma_f32_16x16x32_bf16 v[12:15], v[144:147], v[64:67], v[12:15]\n"
  "s_waitcnt lgkmcnt(3)\n"
  "buffer_load_dwordx4 v112, s[40:43], s57 offen offset:256 nt lds\n"
  "buffer_load_dwordx4 v113, s[40:43], s57 offen offset:1280 nt lds\n"
  "s_add_u32 s57, s57, 6144\n"
  "v_exp_f32 v3, v3\n"
  "v_mfma_f32_16x16x32_bf16 v[12:15], v[140:143], v[60:63], v[12:15]\n"
  "s_waitcnt vmcnt(14)\n"
  "ds_read_b128 a[152:155], v87 offset:2048\n"
  "ds_read_b128 a[156:159], v88 offset:2048\n"
  "v_cvt_pk_bf16_f32 v0, v0, v1\n"
  "v_cvt_pk_bf16_f32 v1, v2, v3\n"
  "v_mfma_f32_16x16x32_bf16 v[12:15], v[136:139], v[56:59], v[12:15]\n"
  "s_add_u32 m0, s59, -256\n"
  "v_mul_f32 v8, s60, v8\n"
  "v_mul_f32 v9, s60, v9\n"
  "v_mfma_f32_16x16x32_bf16 v[16:19], v[160:163], v[56:59], v[92:95]\n"
  "s_waitcnt lgkmcnt(2)\n"
  "buffer_load_dwordx4 v112, s[40:43], s57 offen offset:256 nt lds\n"
  "buffer_load_dwordx4 v113, s[40:43], s57 offen offset:1280 nt lds\n"
  "v_exp_f32 v4, v4\n"
  "v_mfma_f32_16x16x32_bf16 v[16:19], v[164:167], v[60:63], v[16:19]\n"
  "s_waitcnt vmcnt(14)\n"
  "ds_read_b128 a[160:163], v87 offset:4096\n"
  "ds_read_b128 a[164:167], v88 offset:4096\n"
  "v_exp_f32 v5, v5\n"
  "v_mfma_f32_16x16x32_bf16 v[16:19], v[168:171], v[64:67], v[16:19]\n"
  "v_exp_f32 v6, v6\n"
  "v_mfma_f32_16x16x32_bf16 v[16:19], v[172:175], v[68:71], v[16:19]\n"
  "s_add_u32 m0, s59, 1920\n"
  "v_exp_f32 v7, v7\n"
  "v_mfma_f32_16x16x32_bf16 v[16:19], v[176:179], v[72:75], v[16:19]\n"
  "s_waitcnt lgkmcnt(2)\n"
  "buffer_load_dwordx4 v112, s[40:43], s57 offen offset:128 nt lds\n"
  "buffer_load_dwordx4 v113, s[40:43], s57 offen offset:1152 nt lds\n"
  "v_cvt_pk_bf16_f32 v2, v4, v5\n"
  "v_cvt_pk_bf16_f32 v3, v6, v7\n"
  "v_mfma_f32_16x16x32_bf16 v[16:19], v[180:183], v[76:79], v[16:19]\n"
  "s_waitcnt vmcnt(14)\n"
  "ds_read_b128 a[184:187], v87 offset:6144\n"
  "ds_read_b128 a[188:191], v88 offset:6144\n"
  "ds_write_b128 v80, v[0:3] offset:4096\n"
  "v_pk_mul_f32 v[10:11], v[10:11], s[60:61] op_sel_hi:[1,0]\n"
  "v_pk_mul_f32 v[12:13], v[12:13], s[60:61] op_sel_hi:[1,0]\n"
  "v_pk_mul_f32 v[14:15], v[14:15], s[60:61] op_sel_hi:[1,0]\n"
  "v_mfma_f32_16x16x32_bf16 v[20:23], v[204:207], v[76:79], v[92:95]\n"
  "v_exp_f32 v8, v8\n"
  "v_mfma_f32_16x16x32_bf16 v[20:23], v[200:203], v[72:75], v[20:23]\n"
  "s_add_u32 m0, s59, 4096\n"
  "v_exp_f32 v9, v9\n"
  "v_mfma_f32_16x16x32_bf16 v[20:23], v[196:199], v[68:71], v[20:23]\n"
  "s_waitcnt lgkmcnt(3)\n"
  "buffer_load_dwordx4 v112, s[40:43], s57 offen offset:0 nt lds\n"
  "buffer_load_dwordx4 v113, s[40:43], s57 offen offset:1024 nt lds\n"
  "s_add_u32 s57, s57, 6144\n"
  "v_exp_f32 v10, v10\n"
  "v_mfma_f32_16x16x32_bf16 v[20:23], v[192:195], v[64:67], v[20:23]\n"
  "s_waitcnt vmcnt(14)\n"
  "ds_read_b128 a[176:179], v87 offset:8192\n"
  "ds_read_b128 a[180:183], v88 offset:8192\n"
  "v_exp_f32 v11, v11\n"
  "v_mfma_f32_16x16x32_bf16 v[20:23], v[188:191], v[60:63], v[20:23]\n"
  "s_add_u32 m0, s59, 6144\n"
  "v_cvt_pk_bf16_f32 v8, v8, v9\n"
  "v_cvt_pk_bf16_f32 v9, v10, v11\n"
  "v_mfma_f32_16x16x32_bf16 v[20:23], v[184:187], v[56:59], v[20:23]\n"
  "s_waitcnt lgkmcnt(3)\n"
  "buffer_load_dwordx4 v112, s[40:43], s57 offen offset:0 nt lds\n"
  "buffer_load_dwordx4 v113, s[40:43], s57 offen offset:1024 nt lds\n"
  "v_mul_f32 v16, s60, v16\n"
  "v_mul_f32 v17, s60, v17\n"
  "v_mfma_f32_16x16x32_bf16 v[24:27], v[208:211], v[56:59], v[92:95]\n"
  "s_waitcnt vmcnt(14)\n"
  "ds_read_b128 a[168:171], v87 offset:10240\n"
  "ds_read_b128 a[172:175], v88 offset:10240\n"
  "v_exp_f32 v12, v12\n"
  "v_mfma_f32_16x16x32_bf16 v[24:27], v[212:215], v[60:63], v[24:27]\n"
  "v_exp_f32 v13, v13\n"
  "v_mfma_f32_16x16x32_bf16 v[24:27], v[216:219], v[64:67], v[24:27]\n"
  "s_add_u32 m0, s59, 8064\n"
  "v_exp_f32 v14, v14\n"
  "v_mfma_f32_16x16x32_bf16 v[24:27], v[220:223], v[68:71], v[24:27]\n"
  "s_waitcnt lgkmcnt(2)\n"
  "buffer_load_dwordx4 v112, s[40:43], s57 offen offset:128 nt lds\n"
  "buffer_load_dwordx4 v113, s[40:43], s57 offen offset:1152 nt lds\n"
  "v_exp_f32 v15, v15\n"
  "v_mfma_f32_16x16x32_bf16 v[24:27], v[224:227], v[72:75], v[24:27]\n"
  "s_waitcnt vmcnt(14)\n"
  "ds_read_b128 a[192:195], v87 offset:12288\n"
  "ds_read_b128 a[196:199], v88 offset:12288\n"
  "v_cvt_pk_bf16_f32 v10, v12, v13\n"
  "v_cvt_pk_bf16_f32 v11, v14, v15\n"
  "v_mfma_f32_16x16x32_bf16 v[24:27], v[228:231], v[76:79], v[24:27]\n"
  "s_add_u32 m0, s59, 9984\n"
  "ds_write_b128 v80, v[8:11] offset:5120\n"
  "v_pk_mul_f32 v[18:19], v[18:19], s[60:61] op_sel_hi:[1,0]\n"
  "v_pk_mul_f32 v[20:21], v[20:21], s[60:61] op_sel_hi:[1,0]\n"
  "v_pk_mul_f32 v[22:23], v[22:23], s[60:61] op_sel_hi:[1,0]\n"
  "v_mfma_f32_16x16x32_bf16 v[28:31], v[252:255], v[76:79], v[92:95]\n"
  "s_waitcnt lgkmcnt(3)\n"
  "buffer_load_dwordx4 v112, s[40:43], s57 offen offset:256 nt lds\n"
  "buffer_load_dwordx4 v113, s[40:43], s57 offen offset:1280 nt lds\n"
  "s_add_u32 s57, s57, 6144\n"
  "v_exp_f32 v16, v16\n"
  "v_mfma_f32_16x16x32_bf16 v[28:31], v[248:251], v[72:75], v[28:31]\n"
  "s_waitcnt vmcnt(14)\n"
  "ds_read_b128 a[200:203], v87 offset:14336\n"
  "ds_read_b128 a[204:207], v88 offset:14336\n"
  "v_exp_f32 v17, v17\n"
  "v_mfma_f32_16x16x32_bf16 v[28:31], v[244:247], v[68:71], v[28:31]\n"
  "v_exp_f32 v18, v18\n"
  "v_mfma_f32_16x16x32_bf16 v[28:31], v[240:243], v[64:67], v[28:31]\n"
  "s_add_u32 m0, s59, 12032\n"
  "v_exp_f32 v19, v19\n"
  "v_mfma_f32_16x16x32_bf16 v[28:31], v[236:239], v[60:63], v[28:31]\n"
  "s_waitcnt lgkmcnt(3)\n"
  "buffer_load_dwordx4 v112, s[40:43], s57 offen offset:256 nt lds\n"
  "buffer_load_dwordx4 v113, s[40:43], s57 offen offset:1280 nt lds\n"
  "v_cvt_pk_bf16_f32 v16, v16, v17\n"
  "v_cvt_pk_bf16_f32 v17, v18, v19\n"
  "v_mfma_f32_16x16x32_bf16 v[28:31], v[232:235], v[56:59], v[28:31]\n"
  "s_waitcnt vmcnt(14)\n"
  "ds_read_b128 a[208:211], v87 offset:16384\n"
  "ds_read_b128 a[212:215], v88 offset:16384\n"
  "v_mfma_f32_16x16x32_bf16 v[0:3], a[0:3], v[32:35], v[92:95]\n"
  "s_add_u32 s66, s66, 6144\n"
  "s_cmp_eq_u32 s66, 86016\n"
  "s_cselect_b32 s66, 49152, s66\n"
  "v_exp_f32 v20, v20\n"
  "v_mfma_f32_16x16x32_bf16 v[0:3], a[4:7], v[36:39], v[0:3]\n"
  "s_add_u32 m0, s59, 14208\n"
  "v_exp_f32 v21, v21\n"
  "v_mfma_f32_16x16x32_bf16 v[0:3], a[8:11], v[40:43], v[0:3]\n"
  "s_waitcnt lgkmcnt(2)\n"
  "buffer_load_dwordx4 v112, s[40:43], s57 offen offset:128 nt lds\n"
  "buffer_load_dwordx4 v113, s[40:43], s57 offen offset:1152 nt lds\n"
  "v_exp_f32 v22, v22\n"
  "v_mfma_f32_16x16x32_bf16 v[0:3], a[12:15], v[44:47], v[0:3]\n"
  "s_waitcnt vmcnt(14)\n"
  "ds_read_b128 a[232:235], v87 offset:0\n"
  "ds_read_b128 a[236:239], v88 offset:0\n"
  "v_exp_f32 v23, v23\n"
  "v_mfma_f32_16x16x32_bf16 v[0:3], a[16:19], v[48:51], v[0:3]\n"
  "s_add_u32 m0, s59, 16384\n"
  "v_cvt_pk_bf16_f32 v18, v20, v21\n"
  "v_cvt_pk_bf16_f32 v19, v22, v23\n"
  "v_mfma_f32_16x16x32_bf16 v[0:3], a[20:23], v[52:55], v[0:3]\n"
  "s_waitcnt lgkmcnt(2)\n"
  "buffer_load_dwordx4 v112, s[40:43], s57 offen offset:0 nt lds\n"
  "buffer_load_dwordx4 v113, s[40:43], s57 offen offset:1024 nt lds\n"
  "s_add_u32 s57, s57, 6144\n"
  "ds_write_b128 v80, v[16:19] offset:6144\n"
  "v_pk_mul_f32 v[24:25], v[24:25], s[60:61] op_sel_hi:[1,0]\n"
  "v_pk_mul_f32 v[26:27], v[26:27], s[60:61] op_sel_hi:[1,0]\n"
  "v_pk_mul_f32 v[28:29], v[28:29], s[60:61] op_sel_hi:[1,0]\n"
  "v_pk_mul_f32 v[30:31], v[30:31], s[60:61] op_sel_hi:[1,0]\n"
  "v_mfma_f32_16x16x32_bf16 v[4:7], a[44:47], v[52:55], v[92:95]\n"
  "s_waitcnt vmcnt(14)\n"
  "ds_read_b128 a[224:227], v87 offset:2048\n"
  "ds_read_b128 a[228:231], v88 offset:2048\n"
  "v_exp_f32 v24, v24\n"
  "v_mfma_f32_16x16x32_bf16 v[4:7], a[40:43], v[48:51], v[4:7]\n"
  "v_exp_f32 v25, v25\n"
  "v_mfma_f32_16x16x32_bf16 v[4:7], a[36:39], v[44:47], v[4:7]\n"
  "s_add_u32 m0, s59, 0\n"
  "v_exp_f32 v26, v26\n"
  "v_mfma_f32_16x16x32_bf16 v[4:7], a[32:35], v[40:43], v[4:7]\n"
  "s_waitcnt lgkmcnt(3)\n"
  "buffer_load_dwordx4 v112, s[40:43], s57 offen offset:0 nt lds\n"
  "buffer_load_dwordx4 v113, s[40:43], s57 offen offset:1024 nt lds\n"
  "v_exp_f32 v27, v27\n"
  "v_mfma_f32_16x16x32_bf16 v[4:7], a[28:31], v[36:39], v[4:7]\n"
  "s_waitcnt vmcnt(14)\n"
  "ds_read_b128 a[216:219], v87 offset:4096\n"
  "ds_read_b128 a[220:223], v88 offset:4096\n"
  "v_cvt_pk_bf16_f32 v24, v24, v25\n"
  "v_cvt_pk_bf16_f32 v25, v26, v27\n"
  "v_mfma_f32_16x16x32_bf16 v[4:7], a[24:27], v[32:35], v[4:7]\n"
  "s_add_u32 m0, s59, 1920\n"
  "v_add_u32 v81, s66, v83\n"
  "v_add_u32 v82, s66, v84\n"
  "v_mfma_f32_16x16x32_bf16 v[8:11], a[48:51], v[32:35], v[92:95]\n"
  "s_waitcnt lgkmcnt(2)\n"
  "buffer_load_dwordx4 v112, s[40:43], s57 offen offset:128 nt lds\n"
  "buffer_load_dwordx4 v113, s[40:43], s57 offen offset:1152 nt lds\n"
  "v_exp_f32 v28, v28\n"
  "v_mfma_f32_16x16x32_bf16 v[8:11], a[52:55], v[36:39], v[8:11]\n"
  "s_waitcnt vmcnt(14)\n"
  "ds_read_b128 a[240:243], v87 offset:6144\n"
  "ds_read_b128 a[244:247], v88 offset:6144\n"
  "v_exp_f32 v29, v29\n"
  "v_mfma_f32_16x16x32_bf16 v[8:11], a[56:59], v[40:43], v[8:11]\n"
  "v_exp_f32 v30, v30\n"
  "v_mfma_f32_16x16x32_bf16 v[8:11], a[60:63], v[44:47], v[8:11]\n"
  "s_add_u32 m0, s59, 3840\n"
  "v_exp_f32 v31, v31\n"
  "v_mfma_f32_16x16x32_bf16 v[8:11], a[64:67], v[48:51], v[8:11]\n"
  "s_waitcnt lgkmcnt(2)\n"
  "buffer_load_dwordx4 v112, s[40:43], s57 offen offset:256 nt lds\n"
  "buffer_load_dwordx4 v113, s[40:43], s57 offen offset:1280 nt lds\n"
  "s_add_u32 s57, s57, 6144\n"
  "v_cvt_pk_bf16_f32 v26, v28, v29\n"
  "v_cvt_pk_bf16_f32 v27, v30, v31\n"
  "v_mfma_f32_16x16x32_bf16 v[8:11], a[68:71], v[52:55], v[8:11]\n"
  "s_waitcnt vmcnt(14)\n"
  "ds_read_b128 a[248:251], v87 offset:8192\n"
  "ds_read_b128 a[252:255], v88 offset:8192\n"
  "ds_write_b128 v80, v[24:27] offset:7168\n"
  "v_pk_mul_f32 v[0:1], v[0:1], s[60:61] op_sel_hi:[1,0]\n"
  "v_pk_mul_f32 v[2:3], v[2:3], s[60:61] op_sel_hi:[1,0]\n"
  "v_pk_mul_f32 v[4:5], v[4:5], s[60:61] op_sel_hi:[1,0]\n"
  "v_pk_mul_f32 v[6:7], v[6:7], s[60:61] op_sel_hi:[1,0]\n"
  "v_mfma_f32_16x16x32_bf16 v[12:15], a[92:95], v[52:55], v[92:95]\n"
  "v_exp_f32 v0, v0\n"
  "v_mfma_f32_16x16x32_bf16 v[12:15], a[88:91], v[48:51], v[12:15]\n"
  "s_add_u32 m0, s59, 5888\n"
  "v_exp_f32 v1, v1\n"
  "v_mfma_f32_16x16x32_bf16 v[12:15], a[84:87], v[44:47], v[12:15]\n"
  "s_waitcnt lgkmcnt(3)\n"
  "buffer_load_dwordx4 v112, s[40:43], s57 offen offset:256 nt lds\n"
  "buffer_load_dwordx4 v113, s[40:43], s57 offen offset:1280 nt lds\n"
  "v_exp_f32 v2, v2\n"
  "v_mfma_f32_16x16x32_bf16 v[12:15], a[80:83], v[40:43], v[12:15]\n"
  "s_waitcnt vmcnt(14)\n"
  "ds_read_b128 v[128:131], v87 offset:10240\n"
  "ds_read_b128 v[132:135], v88 offset:10240\n"
  "v_exp_f32 v3, v3\n"
  "v_mfma_f32_16x16x32_bf16 v[12:15], a[76:79], v[36:39], v[12:15]\n"
  "s_add_u32 m0, s59, 8064\n"
  "v_cvt_pk_bf16_f32 v0, v0, v1\n"
  "v_cvt_pk_bf16_f32 v1, v2, v3\n"
  "v_mfma_f32_16x16x32_bf16 v[12:15], a[72:75], v[32:35], v[12:15]\n"
  "s_waitcnt lgkmcnt(3)\n"
  "buffer_load_dwordx4 v112, s[40:43], s57 offen offset:128 nt lds\n"
  "buffer_load_dwordx4 v113, s[40:43], s57 offen offset:1152 nt lds\n"
  "s_waitcnt lgkmcnt(2)\n"
  "s_barrier\n"
  "v_xor_b32 v80, 0x4000, v80\n"
  "v_mfma_f32_16x16x32_bf16 v[16:19], a[96:99], v[32:35], v[92:95]\n"
  "s_waitcnt vmcnt(14)\n"
  "ds_read_b128 v[152:155], v87 offset:12288\n"
  "ds_read_b128 v[156:159], v88 offset:12288\n"
  "v_exp_f32 v4, v4\n"
  "v_mfma_f32_16x16x32_bf16 v[16:19], a[100:103], v[36:39], v[16:19]\n"
  "v_exp_f32 v5, v5\n"
  "v_mfma_f32_16x16x32_bf16 v[16:19], a[104:107], v[40:43], v[16:19]\n"
  "s_add_u32 m0, s59, 10240\n"
  "v_exp_f32 v6, v6\n"
  "v_mfma_f32_16x16x32_bf16 v[16:19], a[108:111], v[44:47], v[16:19]\n"
  "s_waitcnt lgkmcnt(2)\n"
  "buffer_load_dwordx4 v112, s[40:43], s57 offen offset:0 nt lds\n"
  "buffer_load_dwordx4 v113, s[40:43], s57 offen offset:1024 nt lds\n"
  "s_add_u32 s57, s57, 6144\n"
  "v_exp_f32 v7, v7\n"
  "v_mfma_f32_16x16x32_bf16 v[16:19], a[112:115], v[48:51], v[16:19]\n"
  "s_waitcnt vmcnt(14)\n"
  "ds_read_b128 v[144:147], v87 offset:14336\n"
  "ds_read_b128 v[148:151], v88 offset:14336\n"
  "v_cvt_pk_bf16_f32 v2, v4, v5\n"
  "v_cvt_pk_bf16_f32 v3, v6, v7\n"
  "v_mfma_f32_16x16x32_bf16 v[16:19], a[116:119], v[52:55], v[16:19]\n"
  "s_add_u32 m0, s59, 12288\n"
  "ds_write_b128 v80, v[0:3] offset:0\n"
  "v_pk_mul_f32 v[8:9], v[8:9], s[60:61] op_sel_hi:[1,0]\n"
  "v_pk_mul_f32 v[10:11], v[10:11], s[60:61] op_sel_hi:[1,0]\n"
  "v_pk_mul_f32 v[12:13], v[12:13], s[60:61] op_sel_hi:[1,0]\n"
  "v_pk_mul_f32 v[14:15], v[14:15], s[60:61] op_sel_hi:[1,0]\n"
  "v_mfma_f32_16x16x32_bf16 v[20:23], a[140:143], v[52:55], v[92:95]\n"
  "s_waitcnt lgkmcnt(3)\n"
  "buffer_load_dwordx4 v112, s[40:43], s57 offen offset:0 nt lds\n"
  "buffer_load_dwordx4 v113, s[40:43], s57 offen offset:1024 nt lds\n"
  "v_exp_f32 v8, v8\n"
  "v_mfma_f32_16x16x32_bf16 v[20:23], a[136:139], v[48:51], v[20:23]\n"
  "s_waitcnt vmcnt(14)\n"
  "ds_read_b128 v[136:139], v87 offset:16384\n"
  "ds_read_b128 v[140:143], v88 offset:16384\n"
  "v_exp_f32 v9, v9\n"
  "v_mfma_f32_16x16x32_bf16 v[20:23], a[132:135], v[44:47], v[20:23]\n"
  "v_exp_f32 v10, v10\n"
  "v_mfma_f32_16x16x32_bf16 v[20:23], a[128:131], v[40:43], v[20:23]\n"
  "s_add_u32 m0, s59, 14208\n"
  "v_exp_f32 v11, v11\n"
  "v_mfma_f32_16x16x32_bf16 v[20:23], a[124:127], v[36:39], v[20:23]\n"
  "s_waitcnt lgkmcnt(3)\n"
  "buffer_load_dwordx4 v112, s[40:43], s57 offen offset:128 nt lds\n"
  "buffer_load_dwordx4 v113, s[40:43], s57 offen offset:1152 nt lds\n"
  "v_cvt_pk_bf16_f32 v8, v8, v9\n"
  "v_cvt_pk_bf16_f32 v9, v10, v11\n"
  "v_mfma_f32_16x16x32_bf16 v[20:23], a[120:123], v[32:35], v[20:23]\n"
  "s_waitcnt vmcnt(14)\n"
  "ds_read_b128 v[160:163], v87 offset:0\n"
  "ds_read_b128 v[164:167], v88 offset:0\n"
  "ds_read_b128 v[56:59], v81 offset:0\n"
  "ds_read_b128 v[60:63], v82 offset:0\n"
  "v_mfma_f32_16x16x32_bf16 v[24:27], a[144:147], v[32:35], v[92:95]\n"
  "v_exp_f32 v12, v12\n"
  "v_mfma_f32_16x16x32_bf16 v[24:27], a[148:151], v[36:39], v[24:27]\n"
  "s_add_u32 m0, s59, 16128\n"
  "v_exp_f32 v13, v13\n"
  "v_mfma_f32_16x16x32_bf16 v[24:27], a[152:155], v[40:43], v[24:27]\n"
  "s_waitcnt lgkmcnt(4)\n"
  "buffer_load_dwordx4 v112, s[40:43], s57 offen offset:256 nt lds\n"
  "buffer_load_dwordx4 v113, s[40:43], s57 offen offset:1280 nt lds\n"
  "s_add_u32 s57, s57, 6144\n"
  "v_exp_f32 v14, v14\n"
  "v_mfma_f32_16x16x32_bf16 v[24:27], a[156:159], v[44:47], v[24:27]\n"
  "s_waitcnt vmcnt(14)\n"
  "ds_read_b128 v[168:171], v87 offset:2048\n"
  "ds_read_b128 v[172:175], v88 offset:2048\n"
  "v_exp_f32 v15, v15\n"
  "v_mfma_f32_16x16x32_bf16 v[24:27], a[160:163], v[48:51], v[24:27]\n"
  "s_add_u32 m0, s59, -256\n"
  "v_cvt_pk_bf16_f32 v10, v12, v13\n"
  "v_cvt_pk_bf16_f32 v11, v14, v15\n"
  "v_mfma_f32_16x16x32_bf16 v[24:27], a[164:167], v[52:55], v[24:27]\n"
  "s_waitcnt lgkmcnt(4)\n"
  "buffer_load_dwordx4 v112, s[40:43], s57 offen offset:256 nt lds\n"
  "buffer_load_dwordx4 v113, s[40:43], s57 offen offset:1280 nt lds\n"
  "ds_write_b128 v80, v[8:11] offset:1024\n"
  "v_pk_mul_f32 v[16:17], v[16:17], s[60:61] op_sel_hi:[1,0]\n"
  "v_pk_mul_f32 v[18:19], v[18:19], s[60:61] op_sel_hi:[1,0]\n"
  "v_pk_mul_f32 v[20:21], v[20:21], s[60:61] op_sel_hi:[1,0]\n"
  "v_pk_mul_f32 v[22:23], v[22:23], s[60:61] op_sel_hi:[1,0]\n"
  "v_mfma_f32_16x16x32_bf16 v[28:31], a[188:191], v[52:55], v[92:95]\n"
  "s_waitcnt vmcnt(14)\n"
  "ds_read_b128 v[176:179], v87 offset:4096\n"
  "ds_read_b128 v[180:183], v88 offset:4096\n"
  "v_exp_f32 v16, v16\n"
  "v_mfma_f32_16x16x32_bf16 v[28:31], a[184:187], v[48:51], v[28:31]\n"
  "v_exp_f32 v17, v17\n"
  "v_mfma_f32_16x16x32_bf16 v[28:31], a[180:183], v[44:47], v[28:31]\n"
  "s_add_u32 m0, s59, 1920\n"
  "v_exp_f32 v18, v18\n"
  "v_mfma_f32_16x16x32_bf16 v[28:31], a[176:179], v[40:43], v[28:31]\n"
  "s_waitcnt lgkmcnt(3)\n"
  "buffer_load_dwordx4 v112, s[40:43], s57 offen offset:128 nt lds\n"
  "buffer_load_dwordx4 v113, s[40:43], s57 offen offset:1152 nt lds\n"
  "v_exp_f32 v19, v19\n"
  "v_mfma_f32_16x16x32_bf16 v[28:31], a[172:175], v[36:39], v[28:31]\n"
  "s_waitcnt vmcnt(14)\n"
  "ds_read_b128 v[200:203], v87 offset:6144\n"
  "ds_read_b128 v[204:207], v88 offset:6144\n"
  "v_cvt_pk_bf16_f32 v16, v16, v17\n"
  "v_cvt_pk_bf16_f32 v17, v18, v19\n"
  "v_mfma_f32_16x16x32_bf16 v[28:31], a[168:171], v[32:35], v[28:31]\n"
  "s_add_u32 m0, s59, 4096\n"
  "ds_read_b128 v[64:67], v81 offset:2048\n"
  "ds_read_b128 v[68:71], v82 offset:2048\n"
  "v_mfma_f32_16x16x32_bf16 v[0:3], a[192:195], v[32:35], v[92:95]\n"
  "s_waitcnt lgkmcnt(4)\n"
  "buffer_load_dwordx4 v112, s[40:43], s57 offen offset:0 nt lds\n"
  "buffer_load_dwordx4 v113, s[40:43], s57 offen offset:1024 nt lds\n"
  "s_add_u32 s57, s57, 6144\n"
  "v_exp_f32 v20, v20\n"
  "v_mfma_f32_16x16x32_bf16 v[0:3], a[196:199], v[36:39], v[0:3]\n"
  "s_waitcnt vmcnt(14)\n"
  "ds_read_b128 v[192:195], v87 offset:8192\n"
  "ds_read_b128 v[196:199], v88 offset:8192\n"
  "v_exp_f32 v21, v21\n"
  "v_mfma_f32_16x16x32_bf16 v[0:3], a[200:203], v[40:43], v[0:3]\n"
  "v_exp_f32 v22, v22\n"
  "v_mfma_f32_16x16x32_bf16 v[0:3], a[204:207], v[44:47], v[0:3]\n"
  "v_exp_f32 v23, v23\n"
  "v_mfma_f32_16x16x32_bf16 v[0:3], a[208:211], v[48:51], v[0:3]\n"
  "v_cvt_pk_bf16_f32 v18, v20, v21\n"
  "v_cvt_pk_bf16_f32 v19, v22, v23\n"
  "v_mfma_f32_16x16x32_bf16 v[0:3], a[212:215], v[52:55], v[0:3]\n"
  "s_waitcnt vmcnt(12)\n"
  "ds_read_b128 v[184:187], v87 offset:10240\n"
  "ds_read_b128 v[188:191], v88 offset:10240\n"
  "ds_write_b128 v80, v[16:19] offset:2048\n"
  "v_pk_mul_f32 v[24:25], v[24:25], s[60:61] op_sel_hi:[1,0]\n"
  "v_pk_mul_f32 v[26:27], v[26:27], s[60:61] op_sel_hi:[1,0]\n"
  "v_pk_mul_f32 v[28:29], v[28:29], s[60:61] op_sel_hi:[1,0]\n"
  "v_pk_mul_f32 v[30:31], v[30:31], s[60:61] op_sel_hi:[1,0]\n"
  "v_mfma_f32_16x16x32_bf16 v[4:7], a[236:239], v[52:55], v[92:95]\n"
  "v_exp_f32 v24, v24\n"
  "v_mfma_f32_16x16x32_bf16 v[4:7], a[232:235], v[48:51], v[4:7]\n"
  "v_exp_f32 v25, v25\n"
  "v_mfma_f32_16x16x32_bf16 v[4:7], a[228:231], v[44:47], v[4:7]\n"
  "v_exp_f32 v26, v26\n"
  "v_mfma_f32_16x16x32_bf16 v[4:7], a[224:227], v[40:43], v[4:7]\n"
  "s_waitcnt vmcnt(10)\n"
  "ds_read_b128 v[208:211], v87 offset:12288\n"
  "ds_read_b128 v[212:215], v88 offset:12288\n"
  "v_exp_f32 v27, v27\n"
  "v_mfma_f32_16x16x32_bf16 v[4:7], a[220:223], v[36:39], v[4:7]\n"
  "v_cvt_pk_bf16_f32 v24, v24, v25\n"
  "v_cvt_pk_bf16_f32 v25, v26, v27\n"
  "v_mfma_f32_16x16x32_bf16 v[4:7], a[216:219], v[32:35], v[4:7]\n"
  "ds_read_b128 v[72:75], v81 offset:4096\n"
  "ds_read_b128 v[76:79], v82 offset:4096\n"
  "v_mfma_f32_16x16x32_bf16 v[8:11], a[240:243], v[32:35], v[92:95]\n"
  "s_waitcnt vmcnt(8)\n"
  "ds_read_b128 v[216:219], v87 offset:14336\n"
  "ds_read_b128 v[220:223], v88 offset:14336\n"
  "v_exp_f32 v28, v28\n"
  "v_mfma_f32_16x16x32_bf16 v[8:11], a[244:247], v[36:39], v[8:11]\n"
  "v_exp_f32 v29, v29\n"
  "v_mfma_f32_16x16x32_bf16 v[8:11], a[248:251], v[40:43], v[8:11]\n"
  "v_exp_f32 v30, v30\n"
  "v_mfma_f32_16x16x32_bf16 v[8:11], a[252:255], v[44:47], v[8:11]\n"
  "v_exp_f32 v31, v31\n"
  "v_mfma_f32_16x16x32_bf16 v[8:11], v[128:131], v[48:51], v[8:11]\n"
  "s_waitcnt vmcnt(6)\n"
  "ds_read_b128 v[224:227], v87 offset:16384\n"
  "ds_read_b128 v[228:231], v88 offset:16384\n"
  "v_cvt_pk_bf16_f32 v26, v28, v29\n"
  "v_cvt_pk_bf16_f32 v27, v30, v31\n"
  "v_mfma_f32_16x16x32_bf16 v[8:11], v[132:135], v[52:55], v[8:11]\n"
  "ds_write_b128 v80, v[24:27] offset:3072\n"
  "v_pk_mul_f32 v[0:1], v[0:1], s[60:61] op_sel_hi:[1,0]\n"
  "v_pk_mul_f32 v[2:3], v[2:3], s[60:61] op_sel_hi:[1,0]\n"
  "v_pk_mul_f32 v[4:5], v[4:5], s[60:61] op_sel_hi:[1,0]\n"
  "v_pk_mul_f32 v[6:7], v[6:7], s[60:61] op_sel_hi:[1,0]\n"
  "v_mfma_f32_16x16x32_bf16 v[12:15], v[156:159], v[52:55], v[92:95]\n"
  "v_exp_f32 v0, v0\n"
  "v_mfma_f32_16x16x32_bf16 v[12:15], v[152:155], v[48:51], v[12:15]\n"
  "s_waitcnt vmcnt(4)\n"
  "ds_read_b128 v[248:251], v87 offset:0\n"
  "ds_read_b128 v[252:255], v88 offset:0\n"
  "v_exp_f32 v1, v1\n"
  "v_mfma_f32_16x16x32_bf16 v[12:15], v[148:151], v[44:47], v[12:15]\n"
  "v_exp_f32 v2, v2\n"
  "v_mfma_f32_16x16x32_bf16 v[12:15], v[144:147], v[40:43], v[12:15]\n"
  "v_exp_f32 v3, v3\n"
  "v_mfma_f32_16x16x32_bf16 v[12:15], v[140:143], v[36:39], v[12:15]\n"
  "v_cvt_pk_bf16_f32 v0, v0, v1\n"
  "v_cvt_pk_bf16_f32 v1, v2, v3\n"
  "v_mfma_f32_16x16x32_bf16 v[12:15], v[136:139], v[32:35], v[12:15]\n"
  "s_waitcnt vmcnt(2)\n"
  "ds_read_b128 v[240:243], v87 offset:2048\n"
  "ds_read_b128 v[244:247], v88 offset:2048\n"
  "v_mul_f32 v8, s60, v8\n"
  "v_mul_f32 v9, s60, v9\n"
  "v_mfma_f32_16x16x32_bf16 v[16:19], v[160:163], v[32:35], v[92:95]\n"
  "v_exp_f32 v4, v4\n"
  "v_mfma_f32_16x16x32_bf16 v[16:19], v[164:167], v[36:39], v[16:19]\n"
  "v_exp_f32 v5, v5\n"
  "v_mfma_f32_16x16x32_bf16 v[16:19], v[168:171], v[40:43], v[16:19]\n"
  "v_exp_f32 v6, v6\n"
  "v_mfma_f32_16x16x32_bf16 v[16:19], v[172:175], v[44:47], v[16:19]\n"
  "s_waitcnt vmcnt(0)\n"
  "ds_read_b128 v[232:235], v87 offset:4096\n"
  "ds_read_b128 v[236:239], v88 offset:4096\n"
  "v_exp_f32 v7, v7\n"
  "v_mfma_f32_16x16x32_bf16 v[16:19], v[176:179], v[48:51], v[16:19]\n"
  "v_cvt_pk_bf16_f32 v2, v4, v5\n"
  "v_cvt_pk_bf16_f32 v3, v6, v7\n"
  "v_mfma_f32_16x16x32_bf16 v[16:19], v[180:183], v[52:55], v[16:19]\n"
  "s_waitcnt lgkmcnt(15)\n"
  "ds_write_b128 v80, v[0:3] offset:4096\n"
  "v_pk_mul_f32 v[10:11], v[10:11], s[60:61] op_sel_hi:[1,0]\n"
  "v_pk_mul_f32 v[12:13], v[12:13], s[60:61] op_sel_hi:[1,0]\n"
  "v_pk_mul_f32 v[14:15], v[14:15], s[60:61] op_sel_hi:[1,0]\n"
  "v_mfma_f32_16x16x32_bf16 v[20:23], v[204:207], v[52:55], v[92:95]\n"
  "v_exp_f32 v8, v8\n"
  "v_mfma_f32_16x16x32_bf16 v[20:23], v[200:203], v[48:51], v[20:23]\n"
  "v_exp_f32 v9, v9\n"
  "v_mfma_f32_16x16x32_bf16 v[20:23], v[196:199], v[44:47], v[20:23]\n"
  "v_exp_f32 v10, v10\n"
  "v_mfma_f32_16x16x32_bf16 v[20:23], v[192:195], v[40:43], v[20:23]\n"
  "v_exp_f32 v11, v11\n"
  "v_mfma_f32_16x16x32_bf16 v[20:23], v[188:191], v[36:39], v[20:23]\n"
  "v_cvt_pk_bf16_f32 v8, v8, v9\n"
  "v_cvt_pk_bf16_f32 v9, v10, v11\n"
  "v_mfma_f32_16x16x32_bf16 v[20:23], v[184:187], v[32:35], v[20:23]\n"
  "s_waitcnt lgkmcnt(14)\n"
  "v_mul_f32 v16, s60, v16\n"
  "v_mul_f32 v17, s60, v17\n"
  "v_mfma_f32_16x16x32_bf16 v[24:27], v[208:211], v[32:35], v[92:95]\n"
  "v_exp_f32 v12, v12\n"
  "v_mfma_f32_16x16x32_bf16 v[24:27], v[212:215], v[36:39], v[24:27]\n"
  "s_waitcnt lgkmcnt(10)\n"
  "v_exp_f32 v13, v13\n"
  "v_mfma_f32_16x16x32_bf16 v[24:27], v[216:219], v[40:43], v[24:27]\n"
  "v_exp_f32 v14, v14\n"
  "v_mfma_f32_16x16x32_bf16 v[24:27], v[220:223], v[44:47], v[24:27]\n"
  "s_waitcnt lgkmcnt(8)\n"
  "v_exp_f32 v15, v15\n"
  "v_mfma_f32_16x16x32_bf16 v[24:27], v[224:227], v[48:51], v[24:27]\n"
  "v_cvt_pk_bf16_f32 v10, v12, v13\n"
  "v_cvt_pk_bf16_f32 v11, v14, v15\n"
  "v_mfma_f32_16x16x32_bf16 v[24:27], v[228:231], v[52:55], v[24:27]\n"
  "s_waitcnt lgkmcnt(5)\n"
  "ds_write_b128 v80, v[8:11] offset:5120\n"
  "v_pk_mul_f32 v[18:19], v[18:19], s[60:61] op_sel_hi:[1,0]\n"
  "v_pk_mul_f32 v[20:21], v[20:21], s[60:61] op_sel_hi:[1,0]\n"
  "v_pk_mul_f32 v[22:23], v[22:23], s[60:61] op_sel_hi:[1,0]\n"
  "v_mfma_f32_16x16x32_bf16 v[28:31], v[252:255], v[52:55], v[92:95]\n"
  "v_exp_f32 v16, v16\n"
  "v_mfma_f32_16x16x32_bf16 v[28:31], v[248:251], v[48:51], v[28:31]\n"
  "s_waitcnt lgkmcnt(4)\n"
  "v_exp_f32 v17, v17\n"
  "v_mfma_f32_16x16x32_bf16 v[28:31], v[244:247], v[44:47], v[28:31]\n"
  "v_exp_f32 v18, v18\n"
  "v_mfma_f32_16x16x32_bf16 v[28:31], v[240:243], v[40:43], v[28:31]\n"
  "s_waitcnt lgkmcnt(2)\n"
  "s_sub_u32 s56, s56, 1\n"
  "v_exp_f32 v19, v19\n"
  "v_mfma_f32_16x16x32_bf16 v[28:31], v[236:239], v[36:39], v[28:31]\n"
  "v_cvt_pk_bf16_f32 v16, v16, v17\n"
  "v_cvt_pk_bf16_f32 v17, v18, v19\n"
  "v_mfma_f32_16x16x32_bf16 v[28:31], v[232:235], v[32:35], v[28:31]\n"
  "s_branch A_H1_%=\n"
  "A_X0_%=:\n"
  "s_mov_b32 s52, s53\n"
  "s_mov_b32 s53, s54\n"
  "s_mov_b32 s54, s55\n"
  "s_cmp_eq_u32 s52, 0\n"
  "s_cbranch_scc1 A_LAST_%=\n"
  "s_lshr_b32 s62, s52, 16\n"
  "s_lshl1_add_u32 s62, s62, s65\n"
  "s_mul_i32 s57, s62, 98304\n"
  "s_and_b32 s56, s52, 0xff\n"
  "s_cmp_lg_u32 s53, 0\n"
  "s_cselect_b32 s62, 1, 0\n"
  "s_sub_u32 s56, s56, s62\n"
  "s_add_u32 s57, s57, 18432\n"
  "v_mfma_f32_16x16x32_bf16 v[0:3], a[0:3], v[32:35], v[92:95]\n"
  "s_add_u32 s66, s66, 6144\n"
  "s_cmp_eq_u32 s66, 86016\n"
  "s_cselect_b32 s66, 49152, s66\n"
  "v_exp_f32 v20, v20\n"
  "v_mfma_f32_16x16x32_bf16 v[0:3], a[4:7], v[36:39], v[0:3]\n"
  "ds_read_b128 a[0:3], v87 offset:0\n"
  "ds_read_b128 a[4:7], v88 offset:0\n"
  "v_exp_f32 v21, v21\n"
  "v_mfma_f32_16x16x32_bf16 v[0:3], a[8:11], v[40:43], v[0:3]\n"
  "v_exp_f32 v22, v22\n"
  "v_mfma_f32_16x16x32_bf16 v[0:3], a[12:15], v[44:47], v[0:3]\n"
  "v_exp_f32 v23, v23\n"
  "v_mfma_f32_16x16x32_bf16 v[0:3], a[16:19], v[48:51], v[0:3]\n"
  "v_cvt_pk_bf16_f32 v18, v20, v21\n"
  "v_cvt_pk_bf16_f32 v19, v22, v23\n"
  "v_mfma_f32_16x16x32_bf16 v[0:3], a[20:23], v[52:55], v[0:3]\n"
  "ds_read_b128 a[8:11], v87 offset:2048\n"
  "ds_read_b128 a[12:15], v88 offset:2048\n"
  "ds_write_b128 v80, v[16:19] offset:6144\n"
  "v_pk_mul_f32 v[24:25], v[24:25], s[60:61] op_sel_hi:[1,0]\n"
  "v_pk_mul_f32 v[26:27], v[26:27], s[60:61] op_sel_hi:[1,0]\n"
  "v_pk_mul_f32 v[28:29], v[28:29], s[60:61] op_sel_hi:[1,0]\n"
  "v_pk_mul_f32 v[30:31], v[30:31], s[60:61] op_sel_hi:[1,0]\n"
  "v_mfma_f32_16x16x32_bf16 v[4:7], a[44:47], v[52:55], v[92:95]\n"
  "v_exp_f32 v24, v24\n"
  "v_mfma_f32_16x16x32_bf16 v[4:7], a[40:43], v[48:51], v[4:7]\n"
  "s_add_u32 m0, s59, -256\n"
  "v_exp_f32 v25, v25\n"
  "v_mfma_f32_16x16x32_bf16 v[4:7], a[36:39], v[44:47], v[4:7]\n"
  "s_waitcnt lgkmcnt(3)\n"
  "buffer_load_dwordx4 v112, s[40:43], s57 offen offset:256 nt lds\n"
  "buffer_load_dwordx4 v113, s[40:43], s57 offen offset:1280 nt lds\n"
  "v_exp_f32 v26, v26\n"
  "v_mfma_f32_16x16x32_bf16 v[4:7], a[32:35], v[40:43], v[4:7]\n"
  "ds_read_b128 a[16:19], v87 offset:4096\n"
  "ds_read_b128 a[20:23], v88 offset:4096\n"
  "v_exp_f32 v27, v27\n"
  "v_mfma_f32_16x16x32_bf16 v[4:7], a[28:31], v[36:39], v[4:7]\n"
  "s_add_u32 m0, s59, 1920\n"
  "v_cvt_pk_bf16_f32 v24, v24, v25\n"
  "v_cvt_pk_bf16_f32 v25, v26, v27\n"
  "v_mfma_f32_16x16x32_bf16 v[4:7], a[24:27], v[32:35], v[4:7]\n"
  "s_waitcnt lgkmcnt(3)\n"
  "buffer_load_dwordx4 v112, s[40:43], s57 offen offset:128 nt lds\n"
  "buffer_load_dwordx4 v113, s[40:43], s57 offen offset:1152 nt lds\n"
  "v_add_u32 v81, s66, v83\n"
  "v_add_u32 v82, s66, v84\n"
  "v_mfma_f32_16x16x32_bf16 v[8:11], a[48:51], v[32:35], v[92:95]\n"
  "ds_read_b128 a[40:43], v87 offset:6144\n"
  "ds_read_b128 a[44:47], v88 offset:6144\n"
  "v_exp_f32 v28, v28\n"
  "v_mfma_f32_16x16x32_bf16 v[8:11], a[52:55], v[36:39], v[8:11]\n"
  "v_exp_f32 v29, v29\n"
  "v_mfma_f32_16x16x32_bf16 v[8:11], a[56:59], v[40:43], v[8:11]\n"
  "s_add_u32 m0, s59, 4096\n"
  "v_exp_f32 v30, v30\n"
  "v_mfma_f32_16x16x32_bf16 v[8:11], a[60:63], v[44:47], v[8:11]\n"
  "s_waitcnt lgkmcnt(2)\n"
  "buffer_load_dwordx4 v112, s[40:43], s57 offen offset:0 nt lds\n"
  "buffer_load_dwordx4 v113, s[40:43], s57 offen offset:1024 nt lds\n"
  "s_add_u32 s57, s57, 6144\n"
  "v_exp_f32 v31, v31\n"
  "v_mfma_f32_16x16x32_bf16 v[8:11], a[64:67], v[48:51], v[8:11]\n"
  "ds_read_b128 a[32:35], v87 offset:8192\n"
  "ds_read_b128 a[36:39], v88 offset:8192\n"
  "v_cvt_pk_bf16_f32 v26, v28, v29\n"
  "v_cvt_pk_bf16_f32 v27, v30, v31\n"
  "v_mfma_f32_16x16x32_bf16 v[8:11], a[68:71], v[52:55], v[8:11]\n"
  "s_add_u32 m0, s59, 6144\n"
  "ds_write_b128 v80, v[24:27] offset:7168\n"
  "v_pk_mul_f32 v[0:1], v[0:1], s[60:61] op_sel_hi:[1,0]\n"
  "v_pk_mul_f32 v[2:3], v[2:3], s[60:61] op_sel_hi:[1,0]\n"
  "v_pk_mul_f32 v[4:5], v[4:5], s[60:61] op_sel_hi:[1,0]\n"
  "v_pk_mul_f32 v[6:7], v[6:7], s[60:61] op_sel_hi:[1,0]\n"
  "v_mfma_f32_16x16x32_bf16 v[12:15], a[92:95], v[52:55], v[92:95]\n"
  "s_waitcnt lgkmcnt(3)\n"
  "buffer_load_dwordx4 v112, s[40:43], s57 offen offset:0 nt lds\n"
  "buffer_load_dwordx4 v113, s[40:43], s57 offen offset:1024 nt lds\n"
  "v_exp_f32 v0, v0\n"
  "v_mfma_f32_16x16x32_bf16 v[12:15], a[88:91], v[48:51], v[12:15]\n"
  "ds_read_b128 a[24:27], v87 offset:10240\n"
  "ds_read_b128 a[28:31], v88 offset:10240\n"
  "v_exp_f32 v1, v1\n"
  "v_mfma_f32_16x16x32_bf16 v[12:15], a[84:87], v[44:47], v[12:15]\n"
  "v_exp_f32 v2, v2\n"
  "v_mfma_f32_16x16x32_bf16 v[12:15], a[80:83], v[40:43], v[12:15]\n"
  "s_add_u32 m0, s59, 8064\n"
  "v_exp_f32 v3, v3\n"
  "v_mfma_f32_16x16x32_bf16 v[12:15], a[76:79], v[36:39], v[12:15]\n"
  "s_waitcnt lgkmcnt(3)\n"
  "buffer_load_dwordx4 v112, s[40:43], s57 offen offset:128 nt lds\n"
  "buffer_load_dwordx4 v113, s[40:43], s57 offen offset:1152 nt lds\n"
  "v_cvt_pk_bf16_f32 v0, v0, v1\n"
  "v_cvt_pk_bf16_f32 v1, v2, v3\n"
  "v_mfma_f32_16x16x32_bf16 v[12:15], a[72:75], v[32:35], v[12:15]\n"
  "ds_read_b128 a[48:51], v87 offset:12288\n"
  "ds_read_b128 a[52:55], v88 offset:12288\n"
  "s_waitcnt lgkmcnt(4)\n"
  "s_barrier\n"
  "v_xor_b32 v80, 0x4000, v80\n"
  "v_mfma_f32_16x16x32_bf16 v[16:19], a[96:99], v[32:35], v[92:95]\n"
  "v_exp_f32 v4, v4\n"
  "v_mfma_f32_16x16x32_bf16 v[16:19], a[100:103], v[36:39], v[16:19]\n"
  "s_add_u32 m0, s59, 9984\n"
  "v_exp_f32 v5, v5\n"
  "v_mfma_f32_16x16x32_bf16 v[16:19], a[104:107], v[40:43], v[16:19]\n"
  "s_waitcnt lgkmcnt(2)\n"
  "buffer_load_dwordx4 v112, s[40:43], s57 offen offset:256 nt lds\n"
  "buffer_load_dwordx4 v113, s[40:43], s57 offen offset:1280 nt lds\n"
  "s_add_u32 s57, s57, 6144\n"
  "v_exp_f32 v6, v6\n"
  "v_mfma_f32_16x16x32_bf16 v[16:19], a[108:111], v[44:47], v[16:19]\n"
  "ds_read_b128 a[56:59], v87 offset:14336\n"
  "ds_read_b128 a[60:63], v88 offset:14336\n"
  "v_exp_f32 v7, v7\n"
  "v_mfma_f32_16x16x32_bf16 v[16:19], a[112:115], v[48:51], v[16:19]\n"
  "s_add_u32 m0, s59, 12032\n"
  "v_cvt_pk_bf16_f32 v2, v4, v5\n"
  "v_cvt_pk_bf16_f32 v3, v6, v7\n"
  "v_mfma_f32_16x16x32_bf16 v[16:19], a[116:119], v[52:55], v[16:19]\n"
  "s_waitcnt lgkmcnt(2)\n"
  "buffer_load_dwordx4 v112, s[40:43], s57 offen offset:256 nt lds\n"
  "buffer_load_dwordx4 v113, s[40:43], s57 offen offset:1280 nt lds\n"
  "ds_write_b128 v80, v[0:3] offset:0\n"
  "v_pk_mul_f32 v[8:9], v[8:9], s[60:61] op_sel_hi:[1,0]\n"
  "v_pk_mul_f32 v[10:11], v[10:11], s[60:61] op_sel_hi:[1,0]\n"
  "v_pk_mul_f32 v[12:13], v[12:13], s[60:61] op_sel_hi:[1,0]\n"
  "v_pk_mul_f32 v[14:15], v[14:15], s[60:61] op_sel_hi:[1,0]\n"
  "v_mfma_f32_16x16x32_bf16 v[20:23], a[140:143], v[52:55], v[92:95]\n"
  "ds_read_b128 a[64:67], v87 offset:16384\n"
  "ds_read_b128 a[68:71], v88 offset:16384\n"
  "v_exp_f32 v8, v8\n"
  "v_mfma_f32_16x16x32_bf16 v[20:23], a[136:139], v[48:51], v[20:23]\n"
  "v_exp_f32 v9, v9\n"
  "v_mfma_f32_16x16x32_bf16 v[20:23], a[132:135], v[44:47], v[20:23]\n"
  "s_add_u32 m0, s59, 14208\n"
  "v_exp_f32 v10, v10\n"
  "v_mfma_f32_16x16x32_bf16 v[20:23], a[128:131], v[40:43], v[20:23]\n"
  "s_waitcnt lgkmcnt(3)\n"
  "buffer_load_dwordx4 v112, s[40:43], s57 offen offset:128 nt lds\n"
  "buffer_load_dwordx4 v113, s[40:43], s57 offen offset:1152 nt lds\n"
  "v_exp_f32 v11, v11\n"
  "v_mfma_f32_16x16x32_bf16 v[20:23], a[124:127], v[36:39], v[20:23]\n"
  "s_waitcnt vmcnt(14)\n"
  "ds_read_b128 a[88:91], v87 offset:0\n"
  "ds_read_b128 a[92:95], v88 offset:0\n"
  "v_cvt_pk_bf16_f32 v8, v8, v9\n"
  "v_cvt_pk_bf16_f32 v9, v10, v11\n"
  "v_mfma_f32_16x16x32_bf16 v[20:23], a[120:123], v[32:35], v[20:23]\n"
  "s_add_u32 m0, s59, 16384\n"
  "ds_read_b128 v[56:59], v81 offset:0\n"
  "ds_read_b128 v[60:63], v82 offset:0\n"
  "v_mfma_f32_16x16x32_bf16 v[24:27], a[144:147], v[32:35], v[92:95]\n"
  "s_waitcnt lgkmcnt(4)\n"
  "buffer_load_dwordx4 v112, s[40:43], s57 offen offset:0 nt lds\n"
  "buffer_load_dwordx4 v113, s[40:43], s57 offen offset:1024 nt lds\n"
  "s_add_u32 s57, s57, 6144\n"
  "v_exp_f32 v12, v12\n"
  "v_mfma_f32_16x16x32_bf16 v[24:27], a[148:151], v[36:39], v[24:27]\n"
  "s_waitcnt vmcnt(14)\n"
  "ds_read_b128 a[80:83], v87 offset:2048\n"
  "ds_read_b128 a[84:87], v88 offset:2048\n"
  "v_exp_f32 v13, v13\n"
  "v_mfma_f32_16x16x32_bf16 v[24:27], a[152:155], v[40:43], v[24:27]\n"
  "v_exp_f32 v14, v14\n"
  "v_mfma_f32_16x16x32_bf16 v[24:27], a[156:159], v[44:47], v[24:27]\n"
  "s_add_u32 m0, s59, 0\n"
  "v_exp_f32 v15, v15\n"
  "v_mfma_f32_16x16x32_bf16 v[24:27], a[160:163], v[48:51], v[24:27]\n"
  "s_waitcnt lgkmcnt(4)\n"
  "buffer_load_dwordx4 v112, s[40:43], s57 offen offset:0 nt lds\n"
  "buffer_load_dwordx4 v113, s[40:43], s57 offen offset:1024 nt lds\n"
  "v_cvt_pk_bf16_f32 v10, v12, v13\n"
  "v_cvt_pk_bf16_f32 v11, v14, v15\n"
  "v_mfma_f32_16x16x32_bf16 v[24:27], a[164:167], v[52:55], v[24:27]\n"
  "s_waitcnt vmcnt(14)\n"
  "ds_read_b128 a[72:75], v87 offset:4096\n"
  "ds_read_b128 a[76:79], v88 offset:4096\n"
  "ds_write_b128 v80, v[8:11] offset:1024\n"
  "v_pk_mul_f32 v[16:17], v[16:17], s[60:61] op_sel_hi:[1,0]\n"
  "v_pk_mul_f32 v[18:19], v[18:19], s[60:61] op_sel_hi:[1,0]\n"
  "v_pk_mul_f32 v[20:21], v[20:21], s[60:61] op_sel_hi:[1,0]\n"
  "v_pk_mul_f32 v[22:23], v[22:23], s[60:61] op_sel_hi:[1,0]\n"
  "v_mfma_f32_16x16x32_bf16 v[28:31], a[188:191], v[52:55], v[92:95]\n"
  "v_exp_f32 v16, v16\n"
  "v_mfma_f32_16x16x32_bf16 v[28:31], a[184:187], v[48:51], v[28:31]\n"
  "s_add_u32 m0, s59, 1920\n"
  "v_exp_f32 v17, v17\n"
  "v_mfma_f32_16x16x32_bf16 v[28:31], a[180:183], v[44:47], v[28:31]\n"
  "s_waitcnt lgkmcnt(3)\n"
  "buffer_load_dwordx4 v112, s[40:43], s57 offen offset:128 nt lds\n"
  "buffer_load_dwordx4 v113, s[40:43], s57 offen offset:1152 nt lds\n"
  "v_exp_f32 v18, v18\n"
  "v_mfma_f32_16x16x32_bf16 v[28:31], a[176:179], v[40:43], v[28:31]\n"
  "s_waitcnt vmcnt(14)\n"
  "ds_read_b128 a[96:99], v87 offset:6144\n"
  "ds_read_b128 a[100:103], v88 offset:6144\n"
  "v_exp_f32 v19, v19\n"
  "v_mfma_f32_16x16x32_bf16 v[28:31], a[172:175], v[36:39], v[28:31]\n"
  "s_add_u32 m0, s59, 3840\n"
  "v_cvt_pk_bf16_f32 v16, v16, v17\n"
  "v_cvt_pk_bf16_f32 v17, v18, v19\n"
  "v_mfma_f32_16x16x32_bf16 v[28:31], a[168:171], v[32:35], v[28:31]\n"
  "s_waitcnt lgkmcnt(3)\n"
  "buffer_load_dwordx4 v112, s[40:43], s57 offen offset:256 nt lds\n"
  "buffer_load_dwordx4 v113, s[40:43], s57 offen offset:1280 nt lds\n"
  "s_add_u32 s57, s57, 6144\n"
  "ds_read_b128 v[64:67], v81 offset:2048\n"
  "ds_read_b128 v[68:71], v82 offset:2048\n"
  "v_mfma_f32_16x16x32_bf16 v[0:3], a[192:195], v[32:35], v[92:95]\n"
  "s_waitcnt vmcnt(14)\n"
  "ds_read_b128 a[104:107], v87 offset:8192\n"
  "ds_read_b128 a[108:111], v88 offset:8192\n"
  "v_exp_f32 v20, v20\n"
  "v_mfma_f32_16x16x32_bf16 v[0:3], a[196:199], v[36:39], v[0:3]\n"
  "v_exp_f32 v21, v21\n"
  "v_mfma_f32_16x16x32_bf16 v[0:3], a[200:203], v[40:43], v[0:3]\n"
  "s_add_u32 m0, s59, 5888\n"
  "v_exp_f32 v22, v22\n"
  "v_mfma_f32_16x16x32_bf16 v[0:3], a[204:207], v[44:47], v[0:3]\n"
  "s_waitcnt lgkmcnt(4)\n"
  "buffer_load_dwordx4 v112, s[40:43], s57 offen offset:256 nt lds\n"
  "buffer_load_dwordx4 v113, s[40:43], s57 offen offset:1280 nt lds\n"
  "v_exp_f32 v23, v23\n"
  "v_mfma_f32_16x16x32_bf16 v[0:3], a[208:211], v[48:51], v[0:3]\n"
  "s_waitcnt vmcnt(14)\n"
  "ds_read_b128 a[112:115], v87 offset:10240\n"
  "ds_read_b128 a[116:119], v88 offset:10240\n"
  "v_cvt_pk_bf16_f32 v18, v20, v21\n"
  "v_cvt_pk_bf16_f32 v19, v22, v23\n"
  "v_mfma_f32_16x16x32_bf16 v[0:3], a[212:215], v[52:55], v[0:3]\n"
  "s_add_u32 m0, s59, 8064\n"
  "ds_write_b128 v80, v[16:19] offset:2048\n"
  "v_pk_mul_f32 v[24:25], v[24:25], s[60:61] op_sel_hi:[1,0]\n"
  "v_pk_mul_f32 v[26:27], v[26:27], s[60:61] op_sel_hi:[1,0]\n"
  "v_pk_mul_f32 v[28:29], v[28:29], s[60:61] op_sel_hi:[1,0]\n"
  "v_pk_mul_f32 v[30:31], v[30:31], s[60:61] op_sel_hi:[1,0]\n"
  "v_mfma_f32_16x16x32_bf16 v[4:7], a[236:239], v[52:55], v[92:95]\n"
  "s_waitcnt lgkmcnt(3)\n"
  "buffer_load_dwordx4 v112, s[40:43], s57 offen offset:128 nt lds\n"
  "buffer_load_dwordx4 v113, s[40:43], s57 offen offset:1152 nt lds\n"
  "v_exp_f32 v24, v24\n"
  "v_mfma_f32_16x16x32_bf16 v[4:7], a[232:235], v[48:51], v[4:7]\n"
  "s_waitcnt vmcnt(14)\n"
  "ds_read_b128 a[136:139], v87 offset:12288\n"
  "ds_read_b128 a[140:143], v88 offset:12288\n"
  "v_exp_f32 v25, v25\n"
  "v_mfma_f32_16x16x32_bf16 v[4:7], a[228:231], v[44:47], v[4:7]\n"
  "v_exp_f32 v26, v26\n"
  "v_mfma_f32_16x16x32_bf16 v[4:7], a[224:227], v[40:43], v[4:7]\n"
  "s_add_u32 m0, s59, 10240\n"
  "v_exp_f32 v27, v27\n"
  "v_mfma_f32_16x16x32_bf16 v[4:7], a[220:223], v[36:39], v[4:7]\n"
  "s_waitcnt lgkmcnt(3)\n"
  "buffer_load_dwordx4 v112, s[40:43], s57 offen offset:0 nt lds\n"
  "buffer_load_dwordx4 v113, s[40:43], s57 offen offset:1024 nt lds\n"
  "s_add_u32 s57, s57, 6144\n"
  "v_cvt_pk_bf16_f32 v24, v24, v25\n"
  "v_cvt_pk_bf16_f32 v25, v26, v27\n"
  "v_mfma_f32_16x16x32_bf16 v[4:7], a[216:219], v[32:35], v[4:7]\n"
  "s_waitcnt vmcnt(14)\n"
  "ds_read_b128 a[128:131], v87 offset:14336\n"
  "ds_read_b128 a[132:135], v88 offset:14336\n"
  "ds_read_b128 v[72:75], v81 offset:4096\n"
  "ds_read_b128 v[76:79], v82 offset:4096\n"
  "v_mfma_f32_16x16x32_bf16 v[8:11], a[240:243], v[32:35], v[92:95]\n"
  "v_exp_f32 v28, v28\n"
  "v_mfma_f32_16x16x32_bf16 v[8:11], a[244:247], v[36:39], v[8:11]\n"
  "s_add_u32 m0, s59, 12288\n"
  "v_exp_f32 v29, v29\n"
  "v_mfma_f32_16x16x32_bf16 v[8:11], a[248:251], v[40:43], v[8:11]\n"
  "s_waitcnt lgkmcnt(4)\n"
  "buffer_load_dwordx4 v112, s[40:43], s57 offen offset:0 nt lds\n"
  "buffer_load_dwordx4 v113, s[40:43], s57 offen offset:1024 nt lds\n"
  "v_exp_f32 v30, v30\n"
  "v_mfma_f32_16x16x32_bf16 v[8:11], a[252:255], v[44:47], v[8:11]\n"
  "s_waitcnt vmcnt(14)\n"
  "ds_read_b128 a[120:123], v87 offset:16384\n"
  "ds_read_b128 a[124:127], v88 offset:16384\n"
  "v_exp_f32 v31, v31\n"
  "v_mfma_f32_16x16x32_bf16 v[8:11], v[128:131], v[48:51], v[8:11]\n"
  "s_add_u32 m0, s59, 14208\n"
  "v_cvt_pk_bf16_f32 v26, v28, v29\n"
  "v_cvt_pk_bf16_f32 v27, v30, v31\n"
  "v_mfma_f32_16x16x32_bf16 v[8:11], v[132:135], v[52:55], v[8:11]\n"
  "s_waitcnt lgkmcnt(4)\n"
  "buffer_load_dwordx4 v112, s[40:43], s57 offen offset:128 nt lds\n"
  "buffer_load_dwordx4 v113, s[40:43], s57 offen offset:1152 nt lds\n"
  "ds_write_b128 v80, v[24:27] offset:3072\n"
  "v_pk_mul_f32 v[0:1], v[0:1], s[60:61] op_sel_hi:[1,0]\n"
  "v_pk_mul_f32 v[2:3], v[2:3], s[60:61] op_sel_hi:[1,0]\n"
  "v_pk_mul_f32 v[4:5], v[4:5], s[60:61] op_sel_hi:[1,0]\n"
  "v_pk_mul_f32 v[6:7], v[6:7], s[60:61] op_sel_hi:[1,0]\n"
  "v_mfma_f32_16x16x32_bf16 v[12:15], v[156:159], v[52:55], v[92:95]\n"
  "s_waitcnt vmcnt(14)\n"
  "ds_read_b128 a[144:147], v87 offset:0\n"
  "ds_read_b128 a[148:151], v88 offset:0\n"
  "v_exp_f32 v0, v0\n"
  "v_mfma_f32_16x16x32_bf16 v[12:15], v[152:155], v[48:51], v[12:15]\n"
  "v_exp_f32 v1, v1\n"
  "v_mfma_f32_16x16x32_bf16 v[12:15], v[148:151], v[44:47], v[12:15]\n"
  "s_add_u32 m0, s59, 16128\n"
  "v_exp_f32 v2, v2\n"
  "v_mfma_f32_16x16x32_bf16 v[12:15], v[144:147], v[40:43], v[12:15]\n"
  "s_waitcnt lgkmcnt(3)\n"
  "buffer_load_dwordx4 v112, s[40:43], s57 offen offset:256 nt lds\n"
  "buffer_load_dwordx4 v113, s[40:43], s57 offen offset:1280 nt lds\n"
  "s_add_u32 s57, s57, 6144\n"
  "v_exp_f32 v3, v3\n"
  "v_mfma_f32_16x16x32_bf16 v[12:15], v[140:143], v[36:39], v[12:15]\n"
  "s_waitcnt vmcnt(14)\n"
  "ds_read_b128 a[152:155], v87 offset:2048\n"
  "ds_read_b128 a[156:159], v88 offset:2048\n"
  "v_cvt_pk_bf16_f32 v0, v0, v1\n"
  "v_cvt_pk_bf16_f32 v1, v2, v3\n"
  "v_mfma_f32_16x16x32_bf16 v[12:15], v[136:139], v[32:35], v[12:15]\n"
  "s_add_u32 m0, s59, -256\n"
  "v_mul_f32 v8, s60, v8\n"
  "v_mul_f32 v9, s60, v9\n"
  "v_mfma_f32_16x16x32_bf16 v[16:19], v[160:163], v[32:35], v[92:95]\n"
  "s_waitcnt lgkmcnt(2)\n"
  "buffer_load_dwordx4 v112, s[40:43], s57 offen offset:256 nt lds\n"
  "buffer_load_dwordx4 v113, s[40:43], s57 offen offset:1280 nt lds\n"
  "v_exp_f32 v4, v4\n"
  "v_mfma_f32_16x16x32_bf16 v[16:19], v[164:167], v[36:39], v[16:19]\n"
  "s_waitcnt vmcnt(14)\n"
  "ds_read_b128 a[160:163], v87 offset:4096\n"
  "ds_read_b128 a[164:167], v88 offset:4096\n"
  "v_exp_f32 v5, v5\n"
  "v_mfma_f32_16x16x32_bf16 v[16:19], v[168:171], v[40:43], v[16:19]\n"
  "v_exp_f32 v6, v6\n"
  "v_mfma_f32_16x16x32_bf16 v[16:19], v[172:175], v[44:47], v[16:19]\n"
  "s_add_u32 m0, s59, 1920\n"
  "v_exp_f32 v7, v7\n"
  "v_mfma_f32_16x16x32_bf16 v[16:19], v[176:179], v[48:51], v[16:19]\n"
  "s_waitcnt lgkmcnt(2)\n"
  "buffer_load_dwordx4 v112, s[40:43], s57 offen offset:128 nt lds\n"
  "buffer_load_dwordx4 v113, s[40:43], s57 offen offset:1152 nt lds\n"
  "v_cvt_pk_bf16_f32 v2, v4, v5\n"
  "v_cvt_pk_bf16_f32 v3, v6, v7\n"
  "v_mfma_f32_16x16x32_bf16 v[16:19], v[180:183], v[52:55], v[16:19]\n"
  "s_waitcnt vmcnt(14)\n"
  "ds_read_b128 a[184:187], v87 offset:6144\n"
  "ds_read_b128 a[188:191], v88 offset:6144\n"
  "ds_write_b128 v80, v[0:3] offset:4096\n"
  "v_pk_mul_f32 v[10:11], v[10:11], s[60:61] op_sel_hi:[1,0]\n"
  "v_pk_mul_f32 v[12:13], v[12:13], s[60:61] op_sel_hi:[1,0]\n"
  "v_pk_mul_f32 v[14:15], v[14:15], s[60:61] op_sel_hi:[1,0]\n"
  "v_mfma_f32_16x16x32_bf16 v[20:23], v[204:207], v[52:55], v[92:95]\n"
  "v_exp_f32 v8, v8\n"
  "v_mfma_f32_16x16x32_bf16 v[20:23], v[200:203], v[48:51], v[20:23]\n"
  "s_add_u32 m0, s59, 4096\n"
  "v_exp_f32 v9, v9\n"
  "v_mfma_f32_16x16x32_bf16 v[20:23], v[196:199], v[44:47], v[20:23]\n"
  "s_waitcnt lgkmcnt(3)\n"
  "buffer_load_dwordx4 v112, s[40:43], s57 offen offset:0 nt lds\n"
  "buffer_load_dwordx4 v113, s[40:43], s57 offen offset:1024 nt lds\n"
  "s_add_u32 s57, s57, 6144\n"
  "v_exp_f32 v10, v10\n"
  "v_mfma_f32_16x16x32_bf16 v[20:23], v[192:195], v[40:43], v[20:23]\n"
  "s_waitcnt vmcnt(14)\n"
  "ds_read_b128 a[176:179], v87 offset:8192\n"
  "ds_read_b128 a[180:183], v88 offset:8192\n"
  "v_exp_f32 v11, v11\n"
  "v_mfma_f32_16x16x32_bf16 v[20:23], v[188:191], v[36:39], v[20:23]\n"
  "s_add_u32 m0, s59, 6144\n"
  "v_cvt_pk_bf16_f32 v8, v8, v9\n"
  "v_cvt_pk_bf16_f32 v9, v10, v11\n"
  "v_mfma_f32_16x16x32_bf16 v[20:23], v[184:187], v[32:35], v[20:23]\n"
  "s_waitcnt lgkmcnt(3)\n"
  "buffer_load_dwordx4 v112, s[40:43], s57 offen offset:0 nt lds\n"
  "buffer_load_dwordx4 v113, s[40:43], s57 offen offset:1024 nt lds\n"
  "v_mul_f32 v16, s60, v16\n"
  "v_mul_f32 v17, s60, v17\n"
  "v_mfma_f32_16x16x32_bf16 v[24:27], v[208:211], v[32:35], v[92:95]\n"
  "s_waitcnt vmcnt(14)\n"
  "ds_read_b128 a[168:171], v87 offset:10240\n"
  "ds_read_b128 a[172:175], v88 offset:10240\n"
  "v_exp_f32 v12, v12\n"
  "v_mfma_f32_16x16x32_bf16 v[24:27], v[212:215], v[36:39], v[24:27]\n"
  "v_exp_f32 v13, v13\n"
  "v_mfma_f32_16x16x32_bf16 v[24:27], v[216:219], v[40:43], v[24:27]\n"
  "s_add_u32 m0, s59, 8064\n"
  "v_exp_f32 v14, v14\n"
  "v_mfma_f32_16x16x32_bf16 v[24:27], v[220:223], v[44:47], v[24:27]\n"
  "s_waitcnt lgkmcnt(2)\n"
  "buffer_load_dwordx4 v112, s[40:43], s57 offen offset:128 nt lds\n"
  "buffer_load_dwordx4 v113, s[40:43], s57 offen offset:1152 nt lds\n"
  "v_exp_f32 v15, v15\n"
  "v_mfma_f32_16x16x32_bf16 v[24:27], v[224:227], v[48:51], v[24:27]\n"
  "s_waitcnt vmcnt(14)\n"
  "ds_read_b128 a[192:195], v87 offset:12288\n"
  "ds_read_b128 a[196:199], v88 offset:12288\n"
  "v_cvt_pk_bf16_f32 v10, v12, v13\n"
  "v_cvt_pk_bf16_f32 v11, v14, v15\n"
  "v_mfma_f32_16x16x32_bf16 v[24:27], v[228:231], v[52:55], v[24:27]\n"
  "s_add_u32 m0, s59, 9984\n"
  "ds_write_b128 v80, v[8:11] offset:5120\n"
  "v_pk_mul_f32 v[18:19], v[18:19], s[60:61] op_sel_hi:[1,0]\n"
  "v_pk_mul_f32 v[20:21], v[20:21], s[60:61] op_sel_hi:[1,0]\n"
  "v_pk_mul_f32 v[22:23], v[22:23], s[60:61] op_sel_hi:[1,0]\n"
  "v_mfma_f32_16x16x32_bf16 v[28:31], v[252:255], v[52:55], v[92:95]\n"
  "s_waitcnt lgkmcnt(3)\n"
  "buffer_load_dwordx4 v112, s[40:43], s57 offen offset:256 nt lds\n"
  "buffer_load_dwordx4 v113, s[40:43], s57 offen offset:1280 nt lds\n"
  "s_add_u32 s57, s57, 6144\n"
  "v_exp_f32 v16, v16\n"
  "v_mfma_f32_16x16x32_bf16 v[28:31], v[248:251], v[48:51], v[28:31]\n"
  "s_waitcnt vmcnt(14)\n"
  "ds_read_b128 a[200:203], v87 offset:14336\n"
  "ds_read_b128 a[204:207], v88 offset:14336\n"
  "v_exp_f32 v17, v17\n"
  "v_mfma_f32_16x16x32_bf16 v[28:31], v[244:247], v[44:47], v[28:31]\n"
  "v_exp_f32 v18, v18\n"
  "v_mfma_f32_16x16x32_bf16 v[28:31], v[240:243], v[40:43], v[28:31]\n"
  "s_add_u32 m0, s59, 12032\n"
  "v_exp_f32 v19, v19\n"
  "v_mfma_f32_16x16x32_bf16 v[28:31], v[236:239], v[36:39], v[28:31]\n"
  "s_waitcnt lgkmcnt(3)\n"
  "buffer_load_dwordx4 v112, s[40:43], s57 offen offset:256 nt lds\n"
  "buffer_load_dwordx4 v113, s[40:43], s57 offen offset:1280 nt lds\n"
  "v_cvt_pk_bf16_f32 v16, v16, v17\n"
  "v_cvt_pk_bf16_f32 v17, v18, v19\n"
  "v_mfma_f32_16x16x32_bf16 v[28:31], v[232:235], v[32:35], v[28:31]\n"
  "s_waitcnt vmcnt(14)\n"
  "ds_read_b128 a[208:211], v87 offset:16384\n"
  "ds_read_b128 a[212:215], v88 offset:16384\n"
  "v_mfma_f32_16x16x32_bf16 v[0:3], a[0:3], v[56:59], v[92:95]\n"
  "s_add_u32 s66, s66, 6144\n"
  "s_cmp_eq_u32 s66, 86016\n"
  "s_cselect_b32 s66, 49152, s66\n"
  "v_exp_f32 v20, v20\n"
  "v_mfma_f32_16x16x32_bf16 v[0:3], a[4:7], v[60:63], v[0:3]\n"
  "s_add_u32 m0, s59, 14208\n"
  "v_exp_f32 v21, v21\n"
  "v_mfma_f32_16x16x32_bf16 v[0:3], a[8:11], v[64:67], v[0:3]\n"
  "s_waitcnt lgkmcnt(2)\n"
  "buffer_load_dwordx4 v112, s[40:43], s57 offen offset:128 nt lds\n"
  "buffer_load_dwordx4 v113, s[40:43], s57 offen offset:1152 nt lds\n"
  "v_exp_f32 v22, v22\n"
  "v_mfma_f32_16x16x32_bf16 v[0:3], a[12:15], v[68:71], v[0:3]\n"
  "s_waitcnt vmcnt(14)\n"
  "ds_read_b128 a[232:235], v87 offset:0\n"
  "ds_read_b128 a[236:239], v88 offset:0\n"
  "v_exp_f32 v23, v23\n"
  "v_mfma_f32_16x16x32_bf16 v[0:3], a[16:19], v[72:75], v[0:3]\n"
  "s_add_u32 m0, s59, 16384\n"
  "v_cvt_pk_bf16_f32 v18, v20, v21\n"
  "v_cvt_pk_bf16_f32 v19, v22, v23\n"
  "v_mfma_f32_16x16x32_bf16 v[0:3], a[20:23], v[76:79], v[0:3]\n"
  "s_waitcnt lgkmcnt(2)\n"
  "buffer_load_dwordx4 v112, s[40:43], s57 offen offset:0 nt lds\n"
  "buffer_load_dwordx4 v113, s[40:43], s57 offen offset:1024 nt lds\n"
  "s_add_u32 s57, s57, 6144\n"
  "ds_write_b128 v80, v[16:19] offset:6144\n"
  "v_pk_mul_f32 v[24:25], v[24:25], s[60:61] op_sel_hi:[1,0]\n"
  "v_pk_mul_f32 v[26:27], v[26:27], s[60:61] op_sel_hi:[1,0]\n"
  "v_pk_mul_f32 v[28:29], v[28:29], s[60:61] op_sel_hi:[1,0]\n"
  "v_pk_mul_f32 v[30:31], v[30:31], s[60:61] op_sel_hi:[1,0]\n"
  "v_mfma_f32_16x16x32_bf16 v[4:7], a[44:47], v[76:79], v[92:95]\n"
  "s_waitcnt vmcnt(14)\n"
  "ds_read_b128 a[224:227], v87 offset:2048\n"
  "ds_read_b128 a[228:231], v88 offset:2048\n"
  "v_exp_f32 v24, v24\n"
  "v_mfma_f32_16x16x32_bf16 v[4:7], a[40:43], v[72:75], v[4:7]\n"
  "v_exp_f32 v25, v25\n"
  "v_mfma_f32_16x16x32_bf16 v[4:7], a[36:39], v[68:71], v[4:7]\n"
  "s_add_u32 m0, s59, 0\n"
  "v_exp_f32 v26, v26\n"
  "v_mfma_f32_16x16x32_bf16 v[4:7], a[32:35], v[64:67], v[4:7]\n"
  "s_waitcnt lgkmcnt(3)\n"
  "buffer_load_dwordx4 v112, s[40:43], s57 offen offset:0 nt lds\n"
  "buffer_load_dwordx4 v113, s[40:43], s57 offen offset:1024 nt lds\n"
  "v_exp_f32 v27, v27\n"
  "v_mfma_f32_16x16x32_bf16 v[4:7], a[28:31], v[60:63], v[4:7]\n"
  "s_waitcnt vmcnt(14)\n"
  "ds_read_b128 a[216:219], v87 offset:4096\n"
  "ds_read_b128 a[220:223], v88 offset:4096\n"
  "v_cvt_pk_bf16_f32 v24, v24, v25\n"
  "v_cvt_pk_bf16_f32 v25, v26, v27\n"
  "v_mfma_f32_16x16x32_bf16 v[4:7], a[24:27], v[56:59], v[4:7]\n"
  "s_add_u32 m0, s59, 1920\n"
  "v_add_u32 v81, s66, v83\n"
  "v_add_u32 v82, s66, v84\n"
  "v_mfma_f32_16x16x32_bf16 v[8:11], a[48:51], v[56:59], v[92:95]\n"
  "s_waitcnt lgkmcnt(2)\n"
  "buffer_load_dwordx4 v112, s[40:43], s57 offen offset:128 nt lds\n"
  "buffer_load_dwordx4 v113, s[40:43], s57 offen offset:1152 nt lds\n"
  "v_exp_f32 v28, v28\n"
  "v_mfma_f32_16x16x32_bf16 v[8:11], a[52:55], v[60:63], v[8:11]\n"
  "s_waitcnt vmcnt(14)\n"
  "ds_read_b128 a[240:243], v87 offset:6144\n"
  "ds_read_b128 a[244:247], v88 offset:6144\n"
  "v_exp_f32 v29, v29\n"
  "v_mfma_f32_16x16x32_bf16 v[8:11], a[56:59], v[64:67], v[8:11]\n"
  "v_exp_f32 v30, v30\n"
  "v_mfma_f32_16x16x32_bf16 v[8:11], a[60:63], v[68:71], v[8:11]\n"
  "s_add_u32 m0, s59, 3840\n"
  "v_exp_f32 v31, v31\n"
  "v_mfma_f32_16x16x32_bf16 v[8:11], a[64:67], v[72:75], v[8:11]\n"
  "s_waitcnt lgkmcnt(2)\n"
  "buffer_load_dwordx4 v112, s[40:43], s57 offen offset:256 nt lds\n"
  "buffer_load_dwordx4 v113, s[40:43], s57 offen offset:1280 nt lds\n"
  "s_add_u32 s57, s57, 6144\n"
  "v_cvt_pk_bf16_f32 v26, v28, v29\n"
  "v_cvt_pk_bf16_f32 v27, v30, v31\n"
  "v_mfma_f32_16x16x32_bf16 v[8:11], a[68:71], v[76:79], v[8:11]\n"
  "s_waitcnt vmcnt(14)\n"
  "ds_read_b128 a[248:251], v87 offset:8192\n"
  "ds_read_b128 a[252:255], v88 offset:8192\n"
  "ds_write_b128 v80, v[24:27] offset:7168\n"
  "v_pk_mul_f32 v[0:1], v[0:1], s[60:61] op_sel_hi:[1,0]\n"
  "v_pk_mul_f32 v[2:3], v[2:3], s[60:61] op_sel_hi:[1,0]\n"
  "v_pk_mul_f32 v[4:5], v[4:5], s[60:61] op_sel_hi:[1,0]\n"
  "v_pk_mul_f32 v[6:7], v[6:7], s[60:61] op_sel_hi:[1,0]\n"
  "v_mfma_f32_16x16x32_bf16 v[12:15], a[92:95], v[76:79], v[92:95]\n"
  "v_exp_f32 v0, v0\n"
  "v_mfma_f32_16x16x32_bf16 v[12:15], a[88:91], v[72:75], v[12:15]\n"
  "s_add_u32 m0, s59, 5888\n"
  "v_exp_f32 v1, v1\n"
  "v_mfma_f32_16x16x32_bf16 v[12:15], a[84:87], v[68:71], v[12:15]\n"
  "s_waitcnt lgkmcnt(3)\n"
  "buffer_load_dwordx4 v112, s[40:43], s57 offen offset:256 nt lds\n"
  "buffer_load_dwordx4 v113, s[40:43], s57 offen offset:1280 nt lds\n"
  "v_exp_f32 v2, v2\n"
  "v_mfma_f32_16x16x32_bf16 v[12:15], a[80:83], v[64:67], v[12:15]\n"
  "s_waitcnt vmcnt(14)\n"
  "ds_read_b128 v[128:131], v87 offset:10240\n"
  "ds_read_b128 v[132:135], v88 offset:10240\n"
  "v_exp_f32 v3, v3\n"
  "v_mfma_f32_16x16x32_bf16 v[12:15], a[76:79], v[60:63], v[12:15]\n"
  "s_add_u32 m0, s59, 8064\n"
  "v_cvt_pk_bf16_f32 v0, v0, v1\n"
  "v_cvt_pk_bf16_f32 v1, v2, v3\n"
  "v_mfma_f32_16x16x32_bf16 v[12:15], a[72:75], v[56:59], v[12:15]\n"
  "s_waitcnt lgkmcnt(3)\n"
  "buffer_load_dwordx4 v112, s[40:43], s57 offen offset:128 nt lds\n"
  "buffer_load_dwordx4 v113, s[40:43], s57 offen offset:1152 nt lds\n"
  "s_waitcnt lgkmcnt(2)\n"
  "s_barrier\n"
  "v_xor_b32 v80, 0x4000, v80\n"
  "v_mfma_f32_16x16x32_bf16 v[16:19], a[96:99], v[56:59], v[92:95]\n"
  "s_waitcnt vmcnt(14)\n"
  "ds_read_b128 v[152:155], v87 offset:12288\n"
  "ds_read_b128 v[156:159], v88 offset:12288\n"
  "v_exp_f32 v4, v4\n"
  "v_mfma_f32_16x16x32_bf16 v[16:19], a[100:103], v[60:63], v[16:19]\n"
  "v_exp_f32 v5, v5\n"
  "v_mfma_f32_16x16x32_bf16 v[16:19], a[104:107], v[64:67], v[16:19]\n"
  "s_add_u32 m0, s59, 10240\n"
  "v_exp_f32 v6, v6\n"
  "v_mfma_f32_16x16x32_bf16 v[16:19], a[108:111], v[68:71], v[16:19]\n"
  "s_waitcnt lgkmcnt(2)\n"
  "buffer_load_dwordx4 v112, s[40:43], s57 offen offset:0 nt lds\n"
  "buffer_load_dwordx4 v113, s[40:43], s57 offen offset:1024 nt lds\n"
  "s_add_u32 s57, s57, 6144\n"
  "v_exp_f32 v7, v7\n"
  "v_mfma_f32_16x16x32_bf16 v[16:19], a[112:115], v[72:75], v[16:19]\n"
  "s_waitcnt vmcnt(14)\n"
  "ds_read_b128 v[144:147], v87 offset:14336\n"
  "ds_read_b128 v[148:151], v88 offset:14336\n"
  "v_cvt_pk_bf16_f32 v2, v4, v5\n"
  "v_cvt_pk_bf16_f32 v3, v6, v7\n"
  "v_mfma_f32_16x16x32_bf16 v[16:19], a[116:119], v[76:79], v[16:19]\n"
  "s_add_u32 m0, s59, 12288\n"
  "ds_write_b128 v80, v[0:3] offset:0\n"
  "v_pk_mul_f32 v[8:9], v[8:9], s[60:61] op_sel_hi:[1,0]\n"
  "v_pk_mul_f32 v[10:11], v[10:11], s[60:61] op_sel_hi:[1,0]\n"
  "v_pk_mul_f32 v[12:13], v[12:13], s[60:61] op_sel_hi:[1,0]\n"
  "v_pk_mul_f32 v[14:15], v[14:15], s[60:61] op_sel_hi:[1,0]\n"
  "v_mfma_f32_16x16x32_bf16 v[20:23], a[140:143], v[76:79], v[92:95]\n"
  "s_waitcnt lgkmcnt(3)\n"
  "buffer_load_dwordx4 v112, s[40:43], s57 offen offset:0 nt lds\n"
  "buffer_load_dwordx4 v113, s[40:43], s57 offen offset:1024 nt lds\n"
  "v_exp_f32 v8, v8\n"
  "v_mfma_f32_16x16x32_bf16 v[20:23], a[136:139], v[72:75], v[20:23]\n"
  "s_waitcnt vmcnt(14)\n"
  "ds_read_b128 v[136:139], v87 offset:16384\n"
  "ds_read_b128 v[140:143], v88 offset:16384\n"
  "v_exp_f32 v9, v9\n"
  "v_mfma_f32_16x16x32_bf16 v[20:23], a[132:135], v[68:71], v[20:23]\n"
  "v_exp_f32 v10, v10\n"
  "v_mfma_f32_16x16x32_bf16 v[20:23], a[128:131], v[64:67], v[20:23]\n"
  "s_add_u32 m0, s59, 14208\n"
  "v_exp_f32 v11, v11\n"
  "v_mfma_f32_16x16x32_bf16 v[20:23], a[124:127], v[60:63], v[20:23]\n"
  "s_waitcnt lgkmcnt(3)\n"
  "buffer_load_dwordx4 v112, s[40:43], s57 offen offset:128 nt lds\n"
  "buffer_load_dwordx4 v113, s[40:43], s57 offen offset:1152 nt lds\n"
  "v_cvt_pk_bf16_f32 v8, v8, v9\n"
  "v_cvt_pk_bf16_f32 v9, v10, v11\n"
  "v_mfma_f32_16x16x32_bf16 v[20:23], a[120:123], v[56:59], v[20:23]\n"
  "s_waitcnt vmcnt(14)\n"
  "ds_read_b128 v[160:163], v87 offset:0\n"
  "ds_read_b128 v[164:167], v88 offset:0\n"
  "ds_read_b128 v[32:35], v81 offset:0\n"
  "ds_read_b128 v[36:39], v82 offset:0\n"
  "v_mfma_f32_16x16x32_bf16 v[24:27], a[144:147], v[56:59], v[92:95]\n"
  "v_exp_f32 v12, v12\n"
  "v_mfma_f32_16x16x32_bf16 v[24:27], a[148:151], v[60:63], v[24:27]\n"
  "s_add_u32 m0, s59, 16128\n"
  "v_exp_f32 v13, v13\n"
  "v_mfma_f32_16x16x32_bf16 v[24:27], a[152:155], v[64:67], v[24:27]\n"
  "s_waitcnt lgkmcnt(4)\n"
  "buffer_load_dwordx4 v112, s[40:43], s57 offen offset:256 nt lds\n"
  "buffer_load_dwordx4 v113, s[40:43], s57 offen offset:1280 nt lds\n"
  "s_add_u32 s57, s57, 6144\n"
  "v_exp_f32 v14, v14\n"
  "v_mfma_f32_16x16x32_bf16 v[24:27], a[156:159], v[68:71], v[24:27]\n"
  "s_waitcnt vmcnt(14)\n"
  "ds_read_b128 v[168:171], v87 offset:2048\n"
  "ds_read_b128 v[172:175], v88 offset:2048\n"
  "v_exp_f32 v15, v15\n"
  "v_mfma_f32_16x16x32_bf16 v[24:27], a[160:163], v[72:75], v[24:27]\n"
  "s_add_u32 m0, s59, -256\n"
  "v_cvt_pk_bf16_f32 v10, v12, v13\n"
  "v_cvt_pk_bf16_f32 v11, v14, v15\n"
  "v_mfma_f32_16x16x32_bf16 v[24:27], a[164:167], v[76:79], v[24:27]\n"
  "s_waitcnt lgkmcnt(4)\n"
  "buffer_load_dwordx4 v112, s[40:43], s57 offen offset:256 nt lds\n"
  "buffer_load_dwordx4 v113, s[40:43], s57 offen offset:1280 nt lds\n"
  "ds_write_b128 v80, v[8:11] offset:1024\n"
  "v_pk_mul_f32 v[16:17], v[16:17], s[60:61] op_sel_hi:[1,0]\n"
  "v_pk_mul_f32 v[18:19], v[18:19], s[60:61] op_sel_hi:[1,0]\n"
  "v_pk_mul_f32 v[20:21], v[20:21], s[60:61] op_sel_hi:[1,0]\n"
  "v_pk_mul_f32 v[22:23], v[22:23], s[60:61] op_sel_hi:[1,0]\n"
  "v_mfma_f32_16x16x32_bf16 v[28:31], a[188:191], v[76:79], v[92:95]\n"
  "s_waitcnt vmcnt(14)\n"
  "ds_read_b128 v[176:179], v87 offset:4096\n"
  "ds_read_b128 v[180:183], v88 offset:4096\n"
  "v_exp_f32 v16, v16\n"
  "v_mfma_f32_16x16x32_bf16 v[28:31], a[184:187], v[72:75], v[28:31]\n"
  "v_exp_f32 v17, v17\n"
  "v_mfma_f32_16x16x32_bf16 v[28:31], a[180:183], v[68:71], v[28:31]\n"
  "s_add_u32 m0, s59, 1920\n"
  "v_exp_f32 v18, v18\n"
  "v_mfma_f32_16x16x32_bf16 v[28:31], a[176:179], v[64:67], v[28:31]\n"
  "s_waitcnt lgkmcnt(3)\n"
  "buffer_load_dwordx4 v112, s[40:43], s57 offen offset:128 nt lds\n"
  "buffer_load_dwordx4 v113, s[40:43], s57 offen offset:1152 nt lds\n"
  "v_exp_f32 v19, v19\n"
  "v_mfma_f32_16x16x32_bf16 v[28:31], a[172:175], v[60:63], v[28:31]\n"
  "s_waitcnt vmcnt(14)\n"
  "ds_read_b128 v[200:203], v87 offset:6144\n"
  "ds_read_b128 v[204:207], v88 offset:6144\n"
  "v_cvt_pk_bf16_f32 v16, v16, v17\n"
  "v_cvt_pk_bf16_f32 v17, v18, v19\n"
  "v_mfma_f32_16x16x32_bf16 v[28:31], a[168:171], v[56:59], v[28:31]\n"
  "s_add_u32 m0, s59, 4096\n"
  "ds_read_b128 v[40:43], v81 offset:2048\n"
  "ds_read_b128 v[44:47], v82 offset:2048\n"
  "v_mfma_f32_16x16x32_bf16 v[0:3], a[192:195], v[56:59], v[92:95]\n"
  "s_waitcnt lgkmcnt(4)\n"
  "buffer_load_dwordx4 v112, s[40:43], s57 offen offset:0 nt lds\n"
  "buffer_load_dwordx4 v113, s[40:43], s57 offen offset:1024 nt lds\n"
  "s_add_u32 s57, s57, 6144\n"
  "v_exp_f32 v20, v20\n"
  "v_mfma_f32_16x16x32_bf16 v[0:3], a[196:199], v[60:63], v[0:3]\n"
  "s_waitcnt vmcnt(14)\n"
  "ds_read_b128 v[192:195], v87 offset:8192\n"
  "ds_read_b128 v[196:199], v88 offset:8192\n"
  "v_exp_f32 v21, v21\n"
  "v_mfma_f32_16x16x32_bf16 v[0:3], a[200:203], v[64:67], v[0:3]\n"
  "v_exp_f32 v22, v22\n"
  "v_mfma_f32_16x16x32_bf16 v[0:3], a[204:207], v[68:71], v[0:3]\n"
  "v_exp_f32 v23, v23\n"
  "v_mfma_f32_16x16x32_bf16 v[0:3], a[208:211], v[72:75], v[0:3]\n"
  "v_cvt_pk_bf16_f32 v18, v20, v21\n"
  "v_cvt_pk_bf16_f32 v19, v22, v23\n"
  "v_mfma_f32_16x16x32_bf16 v[0:3], a[212:215], v[76:79], v[0:3]\n"
  "s_waitcnt vmcnt(12)\n"
  "ds_read_b128 v[184:187], v87 offset:10240\n"
  "ds_read_b128 v[188:191], v88 offset:10240\n"
  "ds_write_b128 v80, v[16:19] offset:2048\n"
  "v_pk_mul_f32 v[24:25], v[24:25], s[60:61] op_sel_hi:[1,0]\n"
  "v_pk_mul_f32 v[26:27], v[26:27], s[60:61] op_sel_hi:[1,0]\n"
  "v_pk_mul_f32 v[28:29], v[28:29], s[60:61] op_sel_hi:[1,0]\n"
  "v_pk_mul_f32 v[30:31], v[30:31], s[60:61] op_sel_hi:[1,0]\n"
  "v_mfma_f32_16x16x32_bf16 v[4:7], a[236:239], v[76:79], v[92:95]\n"
  "v_exp_f32 v24, v24\n"
  "v_mfma_f32_16x16x32_bf16 v[4:7], a[232:235], v[72:75], v[4:7]\n"
  "v_exp_f32 v25, v25\n"
  "v_mfma_f32_16x16x32_bf16 v[4:7], a[228:231], v[68:71], v[4:7]\n"
  "v_exp_f32 v26, v26\n"
  "v_mfma_f32_16x16x32_bf16 v[4:7], a[224:227], v[64:67], v[4:7]\n"
  "s_waitcnt vmcnt(10)\n"
  "ds_read_b128 v[208:211], v87 offset:12288\n"
  "ds_read_b128 v[212:215], v88 offset:12288\n"
  "v_exp_f32 v27, v27\n"
  "v_mfma_f32_16x16x32_bf16 v[4:7], a[220:223], v[60:63], v[4:7]\n"
  "v_cvt_pk_bf16_f32 v24, v24, v25\n"
  "v_cvt_pk_bf16_f32 v25, v26, v27\n"
  "v_mfma_f32_16x16x32_bf16 v[4:7], a[216:219], v[56:59], v[4:7]\n"
  "ds_read_b128 v[48:51], v81 offset:4096\n"
  "ds_read_b128 v[52:55], v82 offset:4096\n"
  "v_mfma_f32_16x16x32_bf16 v[8:11], a[240:243], v[56:59], v[92:95]\n"
  "s_waitcnt vmcnt(8)\n"
  "ds_read_b128 v[216:219], v87 offset:14336\n"
  "ds_read_b128 v[220:223], v88 offset:14336\n"
  "v_exp_f32 v28, v28\n"
  "v_mfma_f32_16x16x32_bf16 v[8:11], a[244:247], v[60:63], v[8:11]\n"
  "v_exp_f32 v29, v29\n"
  "v_mfma_f32_16x16x32_bf16 v[8:11], a[248:251], v[64:67], v[8:11]\n"
  "v_exp_f32 v30, v30\n"
  "v_mfma_f32_16x16x32_bf16 v[8:11], a[252:255], v[68:71], v[8:11]\n"
  "v_exp_f32 v31, v31\n"
  "v_mfma_f32_16x16x32_bf16 v[8:11], v[128:131], v[72:75], v[8:11]\n"
  "s_waitcnt vmcnt(6)\n"
  "ds_read_b128 v[224:227], v87 offset:16384\n"
  "ds_read_b128 v[228:231], v88 offset:16384\n"
  "v_cvt_pk_bf16_f32 v26, v28, v29\n"
  "v_cvt_pk_bf16_f32 v27, v30, v31\n"
  "v_mfma_f32_16x16x32_bf16 v[8:11], v[132:135], v[76:79], v[8:11]\n"
  "ds_write_b128 v80, v[24:27] offset:3072\n"
  "v_pk_mul_f32 v[0:1], v[0:1], s[60:61] op_sel_hi:[1,0]\n"
  "v_pk_mul_f32 v[2:3], v[2:3], s[60:61] op_sel_hi:[1,0]\n"
  "v_pk_mul_f32 v[4:5], v[4:5], s[60:61] op_sel_hi:[1,0]\n"
  "v_pk_mul_f32 v[6:7], v[6:7], s[60:61] op_sel_hi:[1,0]\n"
  "v_mfma_f32_16x16x32_bf16 v[12:15], v[156:159], v[76:79], v[92:95]\n"
  "v_exp_f32 v0, v0\n"
  "v_mfma_f32_16x16x32_bf16 v[12:15], v[152:155], v[72:75], v[12:15]\n"
  "s_waitcnt vmcnt(4)\n"
  "ds_read_b128 v[248:251], v87 offset:0\n"
  "ds_read_b128 v[252:255], v88 offset:0\n"
  "v_exp_f32 v1, v1\n"
  "v_mfma_f32_16x16x32_bf16 v[12:15], v[148:151], v[68:71], v[12:15]\n"
  "v_exp_f32 v2, v2\n"
  "v_mfma_f32_16x16x32_bf16 v[12:15], v[144:147], v[64:67], v[12:15]\n"
  "v_exp_f32 v3, v3\n"
  "v_mfma_f32_16x16x32_bf16 v[12:15], v[140:143], v[60:63], v[12:15]\n"
  "v_cvt_pk_bf16_f32 v0, v0, v1\n"
  "v_cvt_pk_bf16_f32 v1, v2, v3\n"
  "v_mfma_f32_16x16x32_bf16 v[12:15], v[136:139], v[56:59], v[12:15]\n"
  "s_waitcnt vmcnt(2)\n"
  "ds_read_b128 v[240:243], v87 offset:2048\n"
  "ds_read_b128 v[244:247], v88 offset:2048\n"
  "v_mul_f32 v8, s60, v8\n"
  "v_mul_f32 v9, s60, v9\n"
  "v_mfma_f32_16x16x32_bf16 v[16:19], v[160:163], v[56:59], v[92:95]\n"
  "v_exp_f32 v4, v4\n"
  "v_mfma_f32_16x16x32_bf16 v[16:19], v[164:167], v[60:63], v[16:19]\n"
  "v_exp_f32 v5, v5\n"
  "v_mfma_f32_16x16x32_bf16 v[16:19], v[168:171], v[64:67], v[16:19]\n"
  "v_exp_f32 v6, v6\n"
  "v_mfma_f32_16x16x32_bf16 v[16:19], v[172:175], v[68:71], v[16:19]\n"
  "s_waitcnt vmcnt(0)\n"
  "ds_read_b128 v[232:235], v87 offset:4096\n"
  "ds_read_b128 v[236:239], v88 offset:4096\n"
  "v_exp_f32 v7, v7\n"
  "v_mfma_f32_16x16x32_bf16 v[16:19], v[176:179], v[72:75], v[16:19]\n"
  "v_cvt_pk_bf16_f32 v2, v4, v5\n"
  "v_cvt_pk_bf16_f32 v3, v6, v7\n"
  "v_mfma_f32_16x16x32_bf16 v[16:19], v[180:183], v[76:79], v[16:19]\n"
  "s_waitcnt lgkmcnt(15)\n"
  "ds_write_b128 v80, v[0:3] offset:4096\n"
  "v_pk_mul_f32 v[10:11], v[10:11], s[60:61] op_sel_hi:[1,0]\n"
  "v_pk_mul_f32 v[12:13], v[12:13], s[60:61] op_sel_hi:[1,0]\n"
  "v_pk_mul_f32 v[14:15], v[14:15], s[60:61] op_sel_hi:[1,0]\n"
  "v_mfma_f32_16x16x32_bf16 v[20:23], v[204:207], v[76:79], v[92:95]\n"
  "v_exp_f32 v8, v8\n"
  "v_mfma_f32_16x16x32_bf16 v[20:23], v[200:203], v[72:75], v[20:23]\n"
  "v_exp_f32 v9, v9\n"
  "v_mfma_f32_16x16x32_bf16 v[20:23], v[196:199], v[68:71], v[20:23]\n"
  "v_exp_f32 v10, v10\n"
  "v_mfma_f32_16x16x32_bf16 v[20:23], v[192:195], v[64:67], v[20:23]\n"
  "v_exp_f32 v11, v11\n"
  "v_mfma_f32_16x16x32_bf16 v[20:23], v[188:191], v[60:63], v[20:23]\n"
  "v_cvt_pk_bf16_f32 v8, v8, v9\n"
  "v_cvt_pk_bf16_f32 v9, v10, v11\n"
  "v_mfma_f32_16x16x32_bf16 v[20:23], v[184:187], v[56:59], v[20:23]\n"
  "s_waitcnt lgkmcnt(14)\n"
  "v_mul_f32 v16, s60, v16\n"
  "v_mul_f32 v17, s60, v17\n"
  "v_mfma_f32_16x16x32_bf16 v[24:27], v[208:211], v[56:59], v[92:95]\n"
  "v_exp_f32 v12, v12\n"
  "v_mfma_f32_16x16x32_bf16 v[24:27], v[212:215], v[60:63], v[24:27]\n"
  "s_waitcnt lgkmcnt(10)\n"
  "v_exp_f32 v13, v13\n"
  "v_mfma_f32_16x16x32_bf16 v[24:27], v[216:219], v[64:67], v[24:27]\n"
  "v_exp_f32 v14, v14\n"
  "v_mfma_f32_16x16x32_bf16 v[24:27], v[220:223], v[68:71], v[24:27]\n"
  "s_waitcnt lgkmcnt(8)\n"
  "v_exp_f32 v15, v15\n"
  "v_mfma_f32_16x16x32_bf16 v[24:27], v[224:227], v[72:75], v[24:27]\n"
  "v_cvt_pk_bf16_f32 v10, v12, v13\n"
  "v_cvt_pk_bf16_f32 v11, v14, v15\n"
  "v_mfma_f32_16x16x32_bf16 v[24:27], v[228:231], v[76:79], v[24:27]\n"
  "s_waitcnt lgkmcnt(5)\n"
  "ds_write_b128 v80, v[8:11] offset:5120\n"
  "v_pk_mul_f32 v[18:19], v[18:19], s[60:61] op_sel_hi:[1,0]\n"
  "v_pk_mul_f32 v[20:21], v[20:21], s[60:61] op_sel_hi:[1,0]\n"
  "v_pk_mul_f32 v[22:23], v[22:23], s[60:61] op_sel_hi:[1,0]\n"
  "v_mfma_f32_16x16x32_bf16 v[28:31], v[252:255], v[76:79], v[92:95]\n"
  "v_exp_f32 v16, v16\n"
  "v_mfma_f32_16x16x32_bf16 v[28:31], v[248:251], v[72:75], v[28:31]\n"
  "s_waitcnt lgkmcnt(4)\n"
  "v_exp_f32 v17, v17\n"
  "v_mfma_f32_16x16x32_bf16 v[28:31], v[244:247], v[68:71], v[28:31]\n"
  "v_exp_f32 v18, v18\n"
  "v_mfma_f32_16x16x32_bf16 v[28:31], v[240:243], v[64:67], v[28:31]\n"
  "s_waitcnt lgkmcnt(2)\n"
  "s_sub_u32 s56, s56, 1\n"
  "v_exp_f32 v19, v19\n"
  "v_mfma_f32_16x16x32_bf16 v[28:31], v[236:239], v[60:63], v[28:31]\n"
  "v_cvt_pk_bf16_f32 v16, v16, v17\n"
  "v_cvt_pk_bf16_f32 v17, v18, v19\n"
  "v_mfma_f32_16x16x32_bf16 v[28:31], v[232:235], v[56:59], v[28:31]\n"
  "s_branch A_H0_%=\n"
  "A_LAST_%=:\n"
  "v_exp_f32 v20, v20\n"
  "v_exp_f32 v21, v21\n"
  "v_exp_f32 v22, v22\n"
  "v_exp_f32 v23, v23\n"
  "v_pk_mul_f32 v[24:25], v[24:25], s[60:61] op_sel_hi:[1,0]\n"
  "v_pk_mul_f32 v[26:27], v[26:27], s[60:61] op_sel_hi:[1,0]\n"
  "v_cvt_pk_bf16_f32 v18, v20, v21\n"
  "v_cvt_pk_bf16_f32 v19, v22, v23\n"
  "ds_write_b128 v80, v[16:19] offset:6144\n"
  "v_pk_mul_f32 v[28:29], v[28:29], s[60:61] op_sel_hi:[1,0]\n"
  "v_pk_mul_f32 v[30:31], v[30:31], s[60:61] op_sel_hi:[1,0]\n"
  "v_exp_f32 v24, v24\n"
  "v_exp_f32 v25, v25\n"
  "v_exp_f32 v26, v26\n"
  "v_exp_f32 v27, v27\n"
  "v_exp_f32 v28, v28\n"
  "v_exp_f32 v29, v29\n"
  "v_cvt_pk_bf16_f32 v24, v24, v25\n"
  "v_cvt_pk_bf16_f32 v25, v26, v27\n"
  "v_exp_f32 v30, v30\n"
  "v_exp_f32 v31, v31\n"
  "v_cvt_pk_bf16_f32 v26, v28, v29\n"
  "v_cvt_pk_bf16_f32 v27, v30, v31\n"
  "ds_write_b128 v80, v[24:27] offset:7168\n"
  "s_waitcnt lgkmcnt(0)\n"
  "s_barrier\n"
  "s_mov_b32 %0, 0\n"
  "s_branch L_END_%=\n"
  "B_START_%=:\n"
  "s_add_u32 s44, s76, s62\n"
  "s_addc_u32 s45, s77, 0\n"
  "s_add_u32 m0, s66, 0\n"
  "s_nop 0\n"
  "buffer_load_dwordx4 v112, s[44:47], s67 offen offset:0 nt lds\n"
  "s_add_u32 m0, s66, 1920\n"
  "s_nop 0\n"
  "buffer_load_dwordx4 v112, s[44:47], s67 offen offset:128 nt lds\n"
  "s_add_u32 m0, s66, 3840\n"
  "s_nop 0\n"
  "buffer_load_dwordx4 v112, s[44:47], s67 offen offset:256 nt lds\n"
  "s_add_u32 m0, s66, 6144\n"
  "s_nop 0\n"
  "buffer_load_dwordx4 v112, s[44:47], s64 offen offset:0 nt lds\n"
  "s_add_u32 m0, s66, 8064\n"
  "s_nop 0\n"
  "buffer_load_dwordx4 v112, s[44:47], s64 offen offset:128 nt lds\n"
  "s_add_u32 m0, s66, 9984\n"
  "s_nop 0\n"
  "buffer_load_dwordx4 v112, s[44:47], s64 offen offset:256 nt lds\n"
  "s_sub_u32 s40, s80, 2048\n"
  "s_subb_u32 s41, s81, 0\n"
  "s_mov_b32 s42, 100665344\n"
  "s_mov_b64 s[48:49], s[82:83]\n"
  "s_mov_b32 s50, 166723584\n"
  "s_mov_b32 s51, 0x20000\n"
  "s_lshr_b32 s62, s52, 16\n"
  "s_lshl1_add_u32 s62, s62, s65\n"
  "s_mul_i32 s57, s62, 98304\n"
  "s_and_b32 s56, s52, 0xff\n"
  "s_cmp_lg_u32 s53, 0\n"
  "s_cselect_b32 s62, 6, 0\n"
  "s_sub_u32 s56, s56, s62\n"
  "s_bitcmp1_b32 %7, 3\n"
  "s_cselect_b32 s62, 49152, 0\n"
  "s_add_u32 s57, s57, s62\n"
  "s_mov_b32 s68, 0xf0000000\n"
  "s_mov_b32 s69, 0\n"
  "s_sub_u32 s70, s67, s68\n"
  "s_mov_b32 s64, s67\n"
  "s_and_b32 s90, s52, 0xff\n"
  "s_sub_u32 s90, s90, 1\n"
  "s_mul_i32 s91, s90, 6144\n"
  "s_add_u32 s91, s91, s64\n"
  "s_lshr_b32 s62, s53, 16\n"
  "s_mul_i32 s62, s62, 53\n"
  "s_bfe_u32 s63, s53, 0x80008\n"
  "s_add_u32 s62, s62, s63\n"
  "s_mul_i32 s64, s62, 6144\n"
  "s_cmp_eq_u32 s53, 0\n"
  "s_cmov_b32 s64, 0xf0000000\n"
  "s_sub_u32 s91, s64, s91\n"
  "s_mov_b32 s96, s91\n"
  "s_and_b32 s92, s53, 0xff\n"
  "s_sub_u32 s92, s92, 1\n"
  "s_mul_i32 s93, s92, 6144\n"
  "s_add_u32 s93, s93, s64\n"
  "s_lshr_b32 s62, s54, 16\n"
  "s_mul_i32 s62, s62, 53\n"
  "s_bfe_u32 s63, s54, 0x80008\n"
  "s_add_u32 s62, s62, s63\n"
  "s_mul_i32 s64, s62, 6144\n"
  "s_cmp_eq_u32 s54, 0\n"
  "s_cmov_b32 s64, 0xf0000000\n"
  "s_sub_u32 s93, s64, s93\n"
  "s_mov_b32 s97, s93\n"
  "s_and_b32 s94, s54, 0xff\n"
  "s_sub_u32 s94, s94, 1\n"
  "s_mul_i32 s95, s94, 6144\n"
  "s_add_u32 s95, s95, s64\n"
  "s_lshr_b32 s62, s55, 16\n"
  "s_mul_i32 s62, s62, 53\n"
  "s_bfe_u32 s63, s55, 0x80008\n"
  "s_add_u32 s62, s62, s63\n"
  "s_mul_i32 s64, s62, 6144\n"
  "s_cmp_eq_u32 s55, 0\n"
  "s_cmov_b32 s64, 0xf0000000\n"
  "s_sub_u32 s95, s64, s95\n"
  "s_add_u32 s66, s66, 6144\n"
  "s_cmp_ge_u32 s66, 86016\n"
  "s_cselect_b32 s64, 36864, 0\n"
  "s_sub_u32 s66, s66, s64\n"
  "s_sub_u32 s90, s90, 1\n"
  "s_cselect_b32 s63, s91, 6144\n"
  "s_cmov_b64 s[90:91], s[92:93]\n"
  "s_cmov_b64 s[92:93], s[94:95]\n"
  "s_add_u32 s67, s67, s63\n"
  "s_add_u32 s66, s66, 6144\n"
  "s_cmp_ge_u32 s66, 86016\n"
  "s_cselect_b32 s64, 36864, 0\n"
  "s_sub_u32 s66, s66, s64\n"
  "s_sub_u32 s90, s90, 1\n"
  "s_cselect_b32 s63, s91, 6144\n"
  "s_cmov_b64 s[90:91], s[92:93]\n"
  "s_cmov_b64 s[92:93], s[94:95]\n"
  "s_add_u32 s67, s67, s63\n"
  "v_lshrrev_b32 v1, 3, v0\n"
  "v_mul_u32_u24 v114, 0x180, v1\n"
  "v_lshrrev_b32 v1, 4, v0\n"
  "v_and_b32 v2, 7, v0\n"
  "v_xor_b32 v2, v2, v1\n"
  "v_lshl_add_u32 v114, v2, 4, v114\n"
  "s_lshl_b32 s62, s65, 8\n"
  "s_add_u32 s62, s62, 2048\n"
  "v_add_u32 v114, s62, v114\n"
  "v_add_u32 v115, 0x800, v114\n"
  "v_lshrrev_b32 v2, 2, v0\n"
  "v_mul_u32_u24 v116, 0x180, v2\n"
  "v_and_b32 v1, 1, v1\n"
  "v_and_b32 v2, 3, v0\n"
  "v_xor_b32 v2, v2, v1\n"
  "v_lshl_add_u32 v116, v2, 4, v116\n"
  "s_lshl_b32 s62, s65, 6\n"
  "s_add_u32 s62, s62, 128\n"
  "v_add_u32 v116, s62, v116\n"
  "s_mov_b32 s58, 0xfffd1800\n"
  "s_cmp_eq_u32 s65, 0\n"
  "s_cmov_b32 s58, 6144\n"
  "s_cselect_b64 s[80:81], s[78:79], s[80:81]\n"
  "s_mov_b32 s82, 100663296\n"
  "s_mov_b32 s83, 0x20000\n"
  "s_mov_b64 s[76:77], s[78:79]\n"
  "s_mov_b32 s78, 100663296\n"
  "s_mov_b32 s79, 0x20000\n"
  "s_add_u32 s87, s58, 6144\n"
  "s_sub_u32 s87, s87, s86\n"
  "v_lshrrev_b32 v1, 2, v0\n"
  "v_lshlrev_b32 v117, 7, v1\n"
  "v_lshlrev_b32 v121, 6, v1\n"
  "v_and_b32 v2, 1, v0\n"
  "v_lshl_add_u32 v117, v2, 3, v117\n"
  "v_lshl_add_u32 v121, v2, 3, v121\n"
  "v_bfe_u32 v2, v0, 1, 1\n"
  "v_lshl_add_u32 v117, v2, 6, v117\n"
  "v_lshl_add_u32 v121, v2, 5, v121\n"
  "v_bfe_u32 v2, v0, 3, 2\n"
  "v_lshl_add_u32 v117, v2, 4, v117\n"
  "v_bfe_u32 v2, v0, 4, 1\n"
  "v_lshl_add_u32 v121, v2, 4, v121\n"
  "v_add_u32 v117, s59, v117\n"
  "v_add_u32 v121, s59, v121\n"
  "v_xor_b32 v118, 16, v117\n"
  "v_xor_b32 v119, 32, v117\n"
  "v_xor_b32 v120, 48, v117\n"
  "v_xor_b32 v122, 16, v121\n"
  "s_lshl_b32 s62, s65, 13\n"
  "v_lshlrev_b32 v104, 4, v0\n"
  "v_add_u32 v104, s62, v104\n"
  "v_xor_b32 v105, 0x2000, v104\n"
  "v_lshrrev_b32 v1, 5, v0\n"
  "v_mul_u32_u24 v1, 0x480, v1\n"
  "v_and_b32 v2, 31, v0\n"
  "v_lshl_add_u32 v106, v2, 3, v1\n"
  "s_mul_i32 s62, s65, 4608\n"
  "s_add_u32 s62, s62, 32768\n"
  "v_add_u32 v106, s62, v106\n"
  "v_and_b32 v2, 7, v0\n"
  "v_lshl_add_u32 v107, v2, 3, v1\n"
  "v_bfe_u32 v2, v0, 3, 1\n"
  "v_lshl_add_u32 v107, v2, 2, v107\n"
  "v_bfe_u32 v2, v0, 4, 1\n"
  "v_lshl_add_u32 v107, v2, 7, v107\n"
  "s_lshl_b32 s62, s65, 6\n"
  "s_add_u32 s62, s62, 35072\n"
  "v_add_u32 v107, s62, v107\n"
  "v_lshrrev_b32 v1, 4, v0\n"
  "v_mul_u32_u24 v108, 0x120, v1\n"
  "v_and_b32 v2, 3, v0\n"
  "v_lshl_add_u32 v108, v2, 6, v108\n"
  "v_bfe_u32 v2, v0, 2, 2\n"
  "v_lshl_add_u32 v108, v2, 3, v108\n"
  "s_mul_i32 s62, s65, 1152\n"
  "s_add_u32 s62, s62, 40960\n"
  "v_add_u32 v108, s62, v108\n"
  "v_bfe_u32 v2, v0, 3, 1\n"
  "v_lshl_add_u32 v2, v2, 2, v1\n"
  "v_mul_u32_u24 v109, 0x180, v2\n"
  "v_and_b32 v2, 7, v0\n"
  "v_lshl_add_u32 v109, v2, 4, v109\n"
  "s_mul_i32 s62, s65, 3072\n"
  "v_add_u32 v109, s62, v109\n"
  "s_mov_b32 s62, 47872\n"
  "v_lshl_add_u32 v111, v1, 4, s62\n"
  "s_lshl_b32 s62, s65, 6\n"
  "s_add_u32 s62, s62, 39680\n"
  "v_lshl_add_u32 v110, v0, 2, s62\n"
  "v_mov_b32 v3, 0x40000\n"
  "v_cmp_gt_u32 vcc, 16, v0\n"
  "s_nop 1\n"
  "v_cndmask_b32 v110, v3, v110, vcc\n"
  "v_mov_b32 v44, 1.0\n"
  "ds_write_b32 v110, v44 offset:8192\n"
  "v_and_b32 v2, 15, v0\n"
  "v_mov_b32 v124, 0x44444444\n"
  "v_mov_b32 v3, 0xeeeeeeee\n"
  "v_cmp_eq_u32 vcc, 1, v2\n"
  "s_nop 1\n"
  "v_cndmask_b32 v124, v124, v3, vcc\n"
  "v_mov_b32 v3, 0x3f803f80\n"
  "v_cmp_gt_u32 vcc, 2, v2\n"
  "v_mov_b32 v80, 0\n"
  "v_mov_b32 v81, 0\n"
  "v_mov_b32 v82, 0\n"
  "v_mov_b32 v83, 0\n"
  "v_cndmask_b32 v88, 0, v3, vcc\n"
  "v_cndmask_b32 v89, 0, v3, vcc\n"
  "v_cndmask_b32 v90, 0, v3, vcc\n"
  "v_cndmask_b32 v91, 0, v3, vcc\n"
  "s_mov_b32 s99, 0\n"
  "s_waitcnt vmcnt(0)\n"
  "s_barrier\n"
  "v_lshlrev_b32 v125, 7, v0\n"
  "v_mov_b32 v126, 0\n"
  "s_mov_b32 s62, 0x20d00000\n"
  "v_writelane_b32 v126, s62, 2\n"
  "s_mov_b32 s62, 0x31142912\n"
  "v_writelane_b32 v126, s62, 3\n"
  "s_mov_b32 s62, 0x3956\n"
  "v_writelane_b32 v126, s62, 4\n"
  "s_mov_b32 s62, 0x4a1a41d8\n"
  "v_writelane_b32 v126, s62, 5\n"
  "s_mov_b32 s62, 0x5a5e521c\n"
  "v_writelane_b32 v126, s62, 6\n"
  "s_mov_b32 s62, 0x62e00000\n"
  "v_writelane_b32 v126, s62, 7\n"
  "s_mov_b32 s62, 0x73246b22\n"
  "v_writelane_b32 v126, s62, 8\n"
  "s_mov_b32 s62, 0x7b66\n"
  "v_writelane_b32 v126, s62, 9\n"
  "s_mov_b32 s62, 0x942a8be8\n"
  "v_writelane_b32 v126, s62, 10\n"
  "s_mov_b32 s62, 0xa4ae9c6c\n"
  "v_writelane_b32 v126, s62, 11\n"
  "s_mov_b32 s62, 0xad2da4ef\n"
  "v_writelane_b32 v126, s62, 12\n"
  "s_mov_b32 s62, 0xbd69b52b\n"
  "v_writelane_b32 v126, s62, 13\n"
  "s_mov_b32 s62, 0xc5e70000\n"
  "v_writelane_b32 v126, s62, 14\n"
  "s_mov_b32 s62, 0xd623ce25\n"
  "v_writelane_b32 v126, s62, 15\n"
  "s_mov_b32 s62, 0xde61\n"
  "v_writelane_b32 v126, s62, 16\n"
  "s_mov_b32 s62, 0xef1de6df\n"
  "v_writelane_b32 v126, s62, 17\n"
  "s_mov_b32 s62, 0xff59f71b\n"
  "v_writelane_b32 v126, s62, 18\n"
  "s_mov_b32 s62, 0x7d70000\n"
  "v_writelane_b32 v126, s62, 19\n"
  "s_mov_b32 s62, 0x130015\n"
  "v_writelane_b32 v126, s62, 20\n"
  "s_mov_b32 s62, 0x11\n"
  "v_writelane_b32 v126, s62, 21\n"
  "s_mov_b32 s62, 0x20f00000\n"
  "v_writelane_b32 v126, s62, 29\n"
  "s_mov_b32 s62, 0x31322931\n"
  "v_writelane_b32 v126, s62, 30\n"
  "s_mov_b32 s62, 0x3973\n"
  "v_writelane_b32 v126, s62, 31\n"
  "s_mov_b32 s62, 0x4a3541f4\n"
  "v_writelane_b32 v126, s62, 32\n"
  "s_mov_b32 s62, 0x5a775236\n"
  "v_writelane_b32 v126, s62, 33\n"
  "s_mov_b32 s62, 0x62f80000\n"
  "v_writelane_b32 v126, s62, 34\n"
  "s_mov_b32 s62, 0x733a6b39\n"
  "v_writelane_b32 v126, s62, 35\n"
  "s_mov_b32 s62, 0x7b7b\n"
  "v_writelane_b32 v126, s62, 36\n"
  "s_mov_b32 s62, 0x943d8bfc\n"
  "v_writelane_b32 v126, s62, 37\n"
  "s_mov_b32 s62, 0xa4bf9c7e\n"
  "v_writelane_b32 v126, s62, 38\n"
  "s_mov_b32 s62, 0xad0ea4cf\n"
  "v_writelane_b32 v126, s62, 39\n"
  "s_mov_b32 s62, 0xbd4cb50d\n"
  "v_writelane_b32 v126, s62, 40\n"
  "s_mov_b32 s62, 0xc5cb0000\n"
  "v_writelane_b32 v126, s62, 41\n"
  "s_mov_b32 s62, 0xd609ce0a\n"
  "v_writelane_b32 v126, s62, 42\n"
  "s_mov_b32 s62, 0xde48\n"
  "v_writelane_b32 v126, s62, 43\n"
  "s_mov_b32 s62, 0xef06e6c7\n"
  "v_writelane_b32 v126, s62, 44\n"
  "s_mov_b32 s62, 0xff44f705\n"
  "v_writelane_b32 v126, s62, 45\n"
  "s_mov_b32 s62, 0x7c30000\n"
  "v_writelane_b32 v126, s62, 46\n"
  "s_mov_b32 s62, 0x10002\n"
  "v_writelane_b32 v126, s62, 47\n"
  "s_mov_b32 s62, 0x800\n"
  "v_writelane_b32 v126, s62, 48\n"
  "s_lshr_b32 s62, %7, 8\n"
  "s_and_b32 s63, %7, 7\n"
  "s_mul_i32 s63, s63, 24\n"
  "v_mov_b32 v3, 0xffff\n"
  "s_mul_i32 s64, s65, 5\n"
  "v_mov_b32 v6, s63\n"
  "s_and_b32 s63, %7, 7\n"
  "s_xor_b32 s63, s63, 1\n"
  "s_mul_i32 s63, s63, 24\n"
  "s_add_u32 s63, s63, 9216\n"
  "v_mov_b32 v7, s63\n"
  "s_add_u32 s64, s64, 6\n"
  "v_bfe_u32 v2, v126, 0, 6\n"
  "v_mul_u32_u24 v4, 192, v2\n"
  "v_lshrrev_b32 v1, s64, v126\n"
  "v_cmp_gt_u32 vcc, 16, v2\n"
  "s_nop 1\n"
  "v_cndmask_b32 v8, v6, v7, vcc\n"
  "v_bfe_u32 v2, v126, 0, 16\n"
  "v_sub_u32 v1, s62, v1\n"
  "v_and_b32 v1, 31, v1\n"
  "v_cmp_gt_u32 vcc, 24, v1\n"
  "v_add3_u32 v4, v4, v1, v8\n"
  "s_nop 0\n"
  "v_cndmask_b32 v4, v3, v4, vcc\n"
  "v_cmp_ne_u32 vcc, 0, v2\n"
  "s_nop 1\n"
  "v_cndmask_b32 v4, v3, v4, vcc\n"
  "s_add_u32 s64, s64, 16\n"
  "v_bfe_u32 v2, v126, 16, 6\n"
  "v_mul_u32_u24 v5, 192, v2\n"
  "v_lshrrev_b32 v1, s64, v126\n"
  "v_cmp_gt_u32 vcc, 16, v2\n"
  "s_nop 1\n"
  "v_cndmask_b32 v8, v6, v7, vcc\n"
  "v_bfe_u32 v2, v126, 16, 16\n"
  "v_sub_u32 v1, s62, v1\n"
  "v_and_b32 v1, 31, v1\n"
  "v_cmp_gt_u32 vcc, 24, v1\n"
  "v_add3_u32 v5, v5, v1, v8\n"
  "s_nop 0\n"
  "v_cndmask_b32 v5, v3, v5, vcc\n"
  "v_cmp_ne_u32 vcc, 0, v2\n"
  "s_nop 1\n"
  "v_cndmask_b32 v5, v3, v5, vcc\n"
  "v_lshl_or_b32 v126, v5, 16, v4\n"
  "s_mul_i32 s72, s65, -2\n"
  "s_add_u32 s72, s72, 1\n"
  "s_bitcmp1_b32 %7, 4\n"
  "s_cselect_b32 s62, 0, 0\n"
  "s_add_u32 s72, s72, s62\n"
  "s_mov_b32 s71, 0x53800000\n"
  "s_sub_u32 s75, s59, 36864\n"
  "s_barrier\n"
  "s_add_u32 m0, s66, 0\n"
  "s_nop 0\n"
  "buffer_load_dwordx4 v112, s[44:47], s67 offen offset:0 nt lds\n"
  "s_add_u32 m0, s66, 1920\n"
  "s_nop 0\n"
  "buffer_load_dwordx4 v112, s[44:47], s67 offen offset:128 nt lds\n"
  "s_add_u32 m0, s66, 3840\n"
  "s_nop 0\n"
  "buffer_load_dwordx4 v112, s[44:47], s67 offen offset:256 nt lds\n"
  "s_add_u32 s66, s66, 6144\n"
  "s_cmp_ge_u32 s66, 86016\n"
  "s_cselect_b32 s64, 36864, 0\n"
  "s_sub_u32 s66, s66, s64\n"
  "s_sub_u32 s90, s90, 1\n"
  "s_cselect_b32 s63, s91, 6144\n"
  "s_cmov_b64 s[90:91], s[92:93]\n"
  "s_cmov_b64 s[92:93], s[94:95]\n"
  "s_add_u32 s67, s67, s63\n"
  "s_add_u32 m0, s66, 0\n"
  "s_nop 0\n"
  "buffer_load_dwordx4 v112, s[44:47], s67 offen offset:0 nt lds\n"
  "s_add_u32 m0, s66, 1920\n"
  "s_nop 0\n"
  "buffer_load_dwordx4 v112, s[44:47], s67 offen offset:128 nt lds\n"
  "s_add_u32 m0, s66, 3840\n"
  "s_nop 0\n"
  "buffer_load_dwordx4 v112, s[44:47], s67 offen offset:256 nt lds\n"
  "s_add_u32 s66, s66, 6144\n"
  "s_cmp_ge_u32 s66, 86016\n"
  "s_cselect_b32 s64, 36864, 0\n"
  "s_sub_u32 s66, s66, s64\n"
  "s_sub_u32 s90, s90, 1\n"
  "s_cselect_b32 s63, s91, 6144\n"
  "s_cmov_b64 s[90:91], s[92:93]\n"
  "s_cmov_b64 s[92:93], s[94:95]\n"
  "s_add_u32 s67, s67, s63\n"
  "s_add_u32 m0, s59, 0\n"
  "s_nop 0\n"
  "buffer_load_dwordx4 v114, s[40:43], s57 offen offset:0  lds\n"
  "buffer_load_dwordx4 v115, s[40:43], s57 offen offset:1024  lds\n"
  "buffer_load_dwordx4 v116, s[40:43], s57 offen offset:2048  lds\n"
  "s_add_u32 s57, s57, 6144\n"
  "s_add_u32 m0, s59, 3072\n"
  "s_nop 0\n"
  "buffer_load_dwordx4 v114, s[40:43], s57 offen offset:0  lds\n"
  "buffer_load_dwordx4 v115, s[40:43], s57 offen offset:1024  lds\n"
  "buffer_load_dwordx4 v116, s[40:43], s57 offen offset:2048  lds\n"
  "s_add_u32 s57, s57, 6144\n"
  "s_add_u32 m0, s59, 6144\n"
  "s_nop 0\n"
  "buffer_load_dwordx4 v114, s[40:43], s57 offen offset:0  lds\n"
  "buffer_load_dwordx4 v115, s[40:43], s57 offen offset:1024  lds\n"
  "buffer_load_dwordx4 v116, s[40:43], s57 offen offset:2048  lds\n"
  "s_add_u32 s57, s57, 6144\n"
  "s_add_u32 m0, s59, 9216\n"
  "s_nop 0\n"
  "buffer_load_dwordx4 v114, s[40:43], s57 offen offset:0  lds\n"
  "buffer_load_dwordx4 v115, s[40:43], s57 offen offset:1024  lds\n"
  "buffer_load_dwordx4 v116, s[40:43], s57 offen offset:2048  lds\n"
  "s_add_u32 s57, s57, 6144\n"
  "s_add_u32 m0, s59, 12288\n"
  "s_nop 0\n"
  "buffer_load_dwordx4 v114, s[40:43], s57 offen offset:0  lds\n"
  "buffer_load_dwordx4 v115, s[40:43], s57 offen offset:1024  lds\n"
  "buffer_load_dwordx4 v116, s[40:43], s57 offen offset:2048  lds\n"
  "s_add_u32 s57, s57, 6144\n"
  "s_add_u32 m0, s59, 15360\n"
  "s_nop 0\n"
  "buffer_load_dwordx4 v114, s[40:43], s57 offen offset:0  lds\n"
  "buffer_load_dwordx4 v115, s[40:43], s57 offen offset:1024  lds\n"
  "buffer_load_dwordx4 v116, s[40:43], s57 offen offset:2048  lds\n"
  "s_add_u32 s57, s57, 6144\n"
  "s_waitcnt vmcnt(15)\n"
  "ds_read_b64 v[32:33], v117 offset:0\n"
  "ds_read_b64 v[34:35], v118 offset:0\n"
  "s_waitcnt lgkmcnt(0)\n"
  "v_max3_f32 v32, |v32|, |v33|, |v34|\n"
  "v_max3_f32 v32, v32, |v35|, 0\n"
  "v_max_f32 v32, 0, v32\n"
  "v_mul_f32 v33, 0x40000000, v32\n"
  "v_mul_f32 v32, 0x2b800000, v32\n"
  "v_max_f32 v82, v82, v32\n"
  "s_nop 0\n"
  "v_readlane_b32 s98, v33, 0\n"
  "v_readlane_b32 s62, v33, 21\n"
  "v_readlane_b32 s63, v33, 42\n"
  "v_readlane_b32 s64, v33, 63\n"
  "s_nop 1\n"
  "s_max_u32 s98, s98, s62\n"
  "s_max_u32 s63, s63, s64\n"
  "s_max_u32 s98, s98, s63\n"
  "s_and_b32 s98, s98, 0x7f800000\n"
  "s_cselect_b32 s62, 0x400000, 0\n"
  "s_or_b32 s98, s98, s62\n"
  "s_nop 0\n"
  "v_mov_b32 v100, s98\n"
  "v_mov_b32 v101, s98\n"
  "v_mov_b32 v102, s98\n"
  "v_mov_b32 v103, s98\n"
  "ds_read_b64_tr_b16 a[0:1], v117 offset:0\n"
  "ds_read_b64_tr_b16 a[4:5], v118 offset:0\n"
  "ds_read_b64_tr_b16 a[8:9], v119 offset:0\n"
  "ds_read_b64_tr_b16 a[12:13], v120 offset:0\n"
  "ds_read_b64_tr_b16 a[16:17], v121 offset:2048\n"
  "ds_read_b64_tr_b16 a[20:21], v122 offset:2048\n"
  "s_waitcnt vmcnt(12)\n"
  "ds_read_b64_tr_b16 a[2:3], v117 offset:3072\n"
  "ds_read_b64_tr_b16 a[6:7], v118 offset:3072\n"
  "s_add_u32 m0, s59, 0\n"
  "ds_read_b64_tr_b16 a[10:11], v119 offset:3072\n"
  "ds_read_b64_tr_b16 a[14:15], v120 offset:3072\n"
  "s_waitcnt lgkmcnt(4)\n"
  "buffer_load_dwordx4 v114, s[40:43], s57 offen offset:0  lds\n"
  "buffer_load_dwordx4 v115, s[40:43], s57 offen offset:1024  lds\n"
  "ds_read_b64_tr_b16 a[18:19], v121 offset:5120\n"
  "ds_read_b64_tr_b16 a[22:23], v122 offset:5120\n"
  "s_waitcnt vmcnt(11)\n"
  "ds_read_b64_tr_b16 a[24:25], v117 offset:6144\n"
  "ds_read_b64_tr_b16 a[28:29], v118 offset:6144\n"
  "buffer_load_dwordx4 v116, s[40:43], s57 offen offset:2048  lds\n"
  "s_add_u32 s57, s57, 6144\n"
  "s_add_u32 m0, s59, 3072\n"
  "ds_read_b64_tr_b16 a[32:33], v119 offset:6144\n"
  "ds_read_b64_tr_b16 a[36:37], v120 offset:6144\n"
  "s_waitcnt lgkmcnt(4)\n"
  "buffer_load_dwordx4 v114, s[40:43], s57 offen offset:0  lds\n"
  "buffer_load_dwordx4 v115, s[40:43], s57 offen offset:1024  lds\n"
  "ds_read_b64_tr_b16 a[40:41], v121 offset:8192\n"
  "ds_read_b64_tr_b16 a[44:45], v122 offset:8192\n"
  "s_waitcnt vmcnt(11)\n"
  "ds_read_b64_tr_b16 a[26:27], v117 offset:9216\n"
  "ds_read_b64_tr_b16 a[30:31], v118 offset:9216\n"
  "buffer_load_dwordx4 v116, s[40:43], s57 offen offset:2048  lds\n"
  "s_add_u32 s57, s57, s86\n"
  "s_add_u32 m0, s59, 6144\n"
  "ds_read_b64_tr_b16 a[34:35], v119 offset:9216\n"
  "ds_read_b64_tr_b16 a[38:39], v120 offset:9216\n"
  "s_waitcnt lgkmcnt(4)\n"
  "buffer_load_dwordx4 v114, s[40:43], s57 offen offset:0  lds\n"
  "buffer_load_dwordx4 v115, s[40:43], s57 offen offset:1024  lds\n"
  "ds_read_b64_tr_b16 a[42:43], v121 offset:11264\n"
  "ds_read_b64_tr_b16 a[46:47], v122 offset:11264\n"
  "s_waitcnt vmcnt(11)\n"
  "ds_read_b64_tr_b16 a[48:49], v117 offset:12288\n"
  "ds_read_b64_tr_b16 a[52:53], v118 offset:12288\n"
  "buffer_load_dwordx4 v116, s[40:43], s57 offen offset:2048  lds\n"
  "s_add_u32 s57, s57, 6144\n"
  "s_add_u32 m0, s59, 9216\n"
  "ds_read_b64_tr_b16 a[56:57], v119 offset:12288\n"
  "ds_read_b64_tr_b16 a[60:61], v120 offset:12288\n"
  "s_waitcnt lgkmcnt(4)\n"
  "buffer_load_dwordx4 v114, s[40:43], s57 offen offset:0  lds\n"
  "buffer_load_dwordx4 v115, s[40:43], s57 offen offset:1024  lds\n"
  "ds_read_b64_tr_b16 a[64:65], v121 offset:14336\n"
  "ds_read_b64_tr_b16 a[68:69], v122 offset:14336\n"
  "s_waitcnt vmcnt(11)\n"
  "ds_read_b64_tr_b16 a[50:51], v117 offset:15360\n"
  "ds_read_b64_tr_b16 a[54:55], v118 offset:15360\n"
  "buffer_load_dwordx4 v116, s[40:43], s57 offen offset:2048  lds\n"
  "s_add_u32 s57, s57, 6144\n"
  "s_add_u32 m0, s59, 12288\n"
  "ds_read_b64_tr_b16 a[58:59], v119 offset:15360\n"
  "ds_read_b64_tr_b16 a[62:63], v120 offset:15360\n"
  "s_waitcnt lgkmcnt(4)\n"
  "buffer_load_dwordx4 v114, s[40:43], s57 offen offset:0  lds\n"
  "buffer_load_dwordx4 v115, s[40:43], s57 offen offset:1024  lds\n"
  "ds_read_b64_tr_b16 a[66:67], v121 offset:17408\n"
  "ds_read_b64_tr_b16 a[70:71], v122 offset:17408\n"
  "s_waitcnt vmcnt(11)\n"
  "ds_read_b64_tr_b16 a[72:73], v117 offset:0\n"
  "ds_read_b64_tr_b16 a[76:77], v118 offset:0\n"
  "buffer_load_dwordx4 v116, s[40:43], s57 offen offset:2048  lds\n"
  "s_add_u32 s57, s57, 6144\n"
  "s_add_u32 m0, s59, 15360\n"
  "ds_read_b64_tr_b16 a[80:81], v119 offset:0\n"
  "ds_read_b64_tr_b16 a[84:85], v120 offset:0\n"
  "s_waitcnt lgkmcnt(4)\n"
  "buffer_load_dwordx4 v114, s[40:43], s57 offen offset:0  lds\n"
  "buffer_load_dwordx4 v115, s[40:43], s57 offen offset:1024  lds\n"
  "ds_read_b64_tr_b16 a[88:89], v121 offset:2048\n"
  "ds_read_b64_tr_b16 a[92:93], v122 offset:2048\n"
  "s_waitcnt vmcnt(11)\n"
  "ds_read_b64_tr_b16 a[74:75], v117 offset:3072\n"
  "ds_read_b64_tr_b16 a[78:79], v118 offset:3072\n"
  "buffer_load_dwordx4 v116, s[40:43], s57 offen offset:2048  lds\n"
  "s_add_u32 s57, s57, 6144\n"
  "s_add_u32 m0, s59, 0\n"
  "ds_read_b64_tr_b16 a[82:83], v119 offset:3072\n"
  "ds_read_b64_tr_b16 a[86:87], v120 offset:3072\n"
  "s_waitcnt lgkmcnt(0)\n"
  "s_barrier\n"
  "buffer_load_dwordx4 v114, s[40:43], s57 offen offset:0  lds\n"
  "buffer_load_dwordx4 v115, s[40:43], s57 offen offset:1024  lds\n"
  "ds_read_b128 v[48:51], v104 offset:0\n"
  "ds_read_b64_tr_b16 a[90:91], v121 offset:5120\n"
  "ds_read_b64_tr_b16 a[94:95], v122 offset:5120\n"
  "s_waitcnt vmcnt(11)\n"
  "ds_read_b64_tr_b16 a[96:97], v117 offset:6144\n"
  "ds_read_b64_tr_b16 a[100:101], v118 offset:6144\n"
  "ds_read_b128 v[84:87], v111\n"
  "ds_read_b128 v[96:99], v111 offset:64\n"
  "s_sub_u32 s56, s56, 1\n"
  "buffer_load_dwordx4 v116, s[40:43], s57 offen offset:2048  lds\n"
  "ds_read_b128 v[52:55], v104 offset:1024\n"
  "v_xor_b32 v111, 0x2000, v111\n"
  "s_add_u32 s57, s57, 6144\n"
  "s_add_u32 m0, s59, 3072\n"
  "ds_read_b64_tr_b16 a[104:105], v119 offset:6144\n"
  "ds_read_b64_tr_b16 a[108:109], v120 offset:6144\n"
  "s_waitcnt lgkmcnt(7)\n"
  "buffer_load_dwordx4 v114, s[40:43], s57 offen offset:0  lds\n"
  "buffer_load_dwordx4 v115, s[40:43], s57 offen offset:1024  lds\n"
  "ds_read_b64_tr_b16 a[112:113], v121 offset:8192\n"
  "ds_read_b64_tr_b16 a[116:117], v122 offset:8192\n"
  "s_waitcnt vmcnt(11)\n"
  "ds_read_b64_tr_b16 a[98:99], v117 offset:9216\n"
  "ds_read_b64_tr_b16 a[102:103], v118 offset:9216\n"
  "s_sub_i32 s61, s56, 5\n"
  "s_max_i32 s61, s61, 0\n"
  "buffer_load_dwordx4 v116, s[40:43], s57 offen offset:2048  lds\n"
  "ds_read_b128 v[56:59], v104 offset:2048\n"
  "s_add_u32 s57, s57, 6144\n"
  "s_add_u32 m0, s59, 6144\n"
  "ds_read_b64_tr_b16 a[106:107], v119 offset:9216\n"
  "ds_read_b64_tr_b16 a[110:111], v120 offset:9216\n"
  "s_waitcnt lgkmcnt(5)\n"
  "buffer_load_dwordx4 v114, s[40:43], s57 offen offset:0  lds\n"
  "buffer_load_dwordx4 v115, s[40:43], s57 offen offset:1024  lds\n"
  "ds_read_b64_tr_b16 a[114:115], v121 offset:11264\n"
  "ds_read_b64_tr_b16 a[118:119], v122 offset:11264\n"
  "s_waitcnt vmcnt(11)\n"
  "ds_read_b64_tr_b16 a[120:121], v117 offset:12288\n"
  "ds_read_b64_tr_b16 a[124:125], v118 offset:12288\n"
  "buffer_load_dwordx4 v116, s[40:43], s57 offen offset:2048  lds\n"
  "ds_read_b128 v[60:63], v104 offset:3072\n"
  "v_add_f32 v84, v84, v96\n"
  "s_add_u32 s57, s57, 6144\n"
  "s_add_u32 m0, s59, 9216\n"
  "ds_read_b64_tr_b16 a[128:129], v119 offset:12288\n"
  "ds_read_b64_tr_b16 a[132:133], v120 offset:12288\n"
  "v_add_f32 v85, v85, v97\n"
  "v_add_f32 v86, v86, v98\n"
  "s_waitcnt lgkmcnt(5)\n"
  "buffer_load_dwordx4 v114, s[40:43], s57 offen offset:0  lds\n"
  "buffer_load_dwordx4 v115, s[40:43], s57 offen offset:1024  lds\n"
  "ds_read_b64_tr_b16 a[136:137], v121 offset:14336\n"
  "ds_read_b64_tr_b16 a[140:141], v122 offset:14336\n"
  "s_waitcnt vmcnt(11)\n"
  "ds_read_b64_tr_b16 a[122:123], v117 offset:15360\n"
  "ds_read_b64_tr_b16 a[126:127], v118 offset:15360\n"
  "v_add_f32 v87, v87, v99\n"
  "v_max3_f32 v82, v82, v84, v85\n"
  "buffer_load_dwordx4 v116, s[40:43], s57 offen offset:2048  lds\n"
  "v_rcp_f32 v84, v84\n"
  "s_add_u32 s57, s57, s87\n"
  "s_add_u32 m0, s59, 12288\n"
  "ds_read_b64_tr_b16 a[130:131], v119 offset:15360\n"
  "ds_read_b64_tr_b16 a[134:135], v120 offset:15360\n"
  "v_rcp_f32 v85, v85\n"
  "s_waitcnt lgkmcnt(4)\n"
  "buffer_load_dwordx4 v114, s[40:43], s57 offen offset:0  lds\n"
  "buffer_load_dwordx4 v115, s[40:43], s57 offen offset:1024  lds\n"
  "ds_read_b64_tr_b16 a[138:139], v121 offset:17408\n"
  "ds_read_b64_tr_b16 a[142:143], v122 offset:17408\n"
  "s_waitcnt vmcnt(11)\n"
  "ds_read_b64_tr_b16 a[144:145], v117 offset:0\n"
  "ds_read_b64_tr_b16 a[148:149], v118 offset:0\n"
  "v_max3_f32 v82, v82, v86, v87\n"
  "v_max3_f32 v83, v83, v84, v85\n"
  "buffer_load_dwordx4 v116, s[40:43], s57 offen offset:2048  lds\n"
  "v_rcp_f32 v86, v86\n"
  "s_add_u32 s57, s57, 6144\n"
  "s_add_u32 m0, s59, 15360\n"
  "ds_read_b64_tr_b16 a[152:153], v119 offset:0\n"
  "ds_read_b64_tr_b16 a[156:157], v120 offset:0\n"
  "v_rcp_f32 v87, v87\n"
  "s_waitcnt lgkmcnt(4)\n"
  "buffer_load_dwordx4 v114, s[40:43], s57 offen offset:0  lds\n"
  "buffer_load_dwordx4 v115, s[40:43], s57 offen offset:1024  lds\n"
  "ds_read_b64_tr_b16 a[160:161], v121 offset:2048\n"
  "ds_read_b64_tr_b16 a[164:165], v122 offset:2048\n"
  "s_waitcnt vmcnt(11)\n"
  "ds_read_b64_tr_b16 a[146:147], v117 offset:3072\n"
  "ds_read_b64_tr_b16 a[150:151], v118 offset:3072\n"
  "v_max3_f32 v83, v83, v86, v87\n"
  "v_mul_f32 v96, s99, v84\n"
  "buffer_load_dwordx4 v116, s[40:43], s57 offen offset:2048  lds\n"
  "v_mul_f32 v97, s99, v85\n"
  "v_mul_f32 v98, s99, v86\n"
  "s_add_u32 s57, s57, 6144\n"
  "s_add_u32 m0, s59, 0\n"
  "ds_read_b64_tr_b16 a[154:155], v119 offset:3072\n"
  "ds_read_b64_tr_b16 a[158:159], v120 offset:3072\n"
  "v_mul_f32 v99, s99, v87\n"
  "v_fma_f32 v40, v40, v84, v96\n"
  "v_mfma_f32_16x16x32_bf16 v[0:3], v[48:51], a[0:3], v[100:103]\n"
  "s_waitcnt lgkmcnt(4)\n"
  "buffer_load_dwordx4 v114, s[40:43], s57 offen offset:0  lds\n"
  "buffer_load_dwordx4 v115, s[40:43], s57 offen offset:1024  lds\n"
  "ds_read_b128 v[64:67], v104 offset:4096\n"
  "ds_read_b64_tr_b16 a[162:163], v121 offset:5120\n"
  "ds_read_b64_tr_b16 a[166:167], v122 offset:5120\n"
  "v_mfma_f32_16x16x32_bf16 v[0:3], v[52:55], a[24:27], v[0:3]\n"
  "s_waitcnt vmcnt(11)\n"
  "ds_read_b64_tr_b16 a[168:169], v117 offset:6144\n"
  "ds_read_b64_tr_b16 a[172:173], v118 offset:6144\n"
  "v_fma_f32 v44, v44, v84, v96\n"
  "v_fma_f32 v41, v41, v85, v97\n"
  "v_mfma_f32_16x16x32_bf16 v[4:7], v[52:55], a[28:31], v[100:103]\n"
  "s_xor_b32 s99, 0x80000000, s98\n"
  "buffer_load_dwordx4 v116, s[40:43], s57 offen offset:2048  lds\n"
  "ds_read_b128 v[68:71], v104 offset:5120\n"
  "v_fma_f32 v45, v45, v85, v97\n"
  "v_mfma_f32_16x16x32_bf16 v[4:7], v[48:51], a[4:7], v[4:7]\n"
  "s_add_u32 s57, s57, 6144\n"
  "s_add_u32 m0, s59, 3072\n"
  "ds_read_b64_tr_b16 a[176:177], v119 offset:6144\n"
  "ds_read_b64_tr_b16 a[180:181], v120 offset:6144\n"
  "ds_read_b64_tr_b16 v[92:93], v108 offset:0\n"
  "ds_read_b64_tr_b16 v[94:95], v108 offset:32\n"
  "v_mfma_f32_16x16x32_bf16 v[8:11], v[48:51], a[8:11], v[100:103]\n"
  "s_waitcnt lgkmcnt(7)\n"
  "buffer_load_dwordx4 v114, s[40:43], s57 offen offset:0  lds\n"
  "buffer_load_dwordx4 v115, s[40:43], s57 offen offset:1024  lds\n"
  "ds_read_b64_tr_b16 a[184:185], v121 offset:8192\n"
  "ds_read_b64_tr_b16 a[188:189], v122 offset:8192\n"
  "v_mfma_f32_16x16x32_bf16 v[8:11], v[52:55], a[32:35], v[8:11]\n"
  "s_waitcnt vmcnt(11)\n"
  "ds_read_b64_tr_b16 a[170:171], v117 offset:9216\n"
  "ds_read_b64_tr_b16 a[174:175], v118 offset:9216\n"
  "v_fma_f32 v42, v42, v86, v98\n"
  "v_fma_f32 v46, v46, v86, v98\n"
  "v_smfmac_f32_16x16x64_bf16 v[80:83], v[88:91], v[48:55], v124\n"
  "buffer_load_dwordx4 v116, s[40:43], s57 offen offset:2048  lds\n"
  "ds_read_b128 v[72:75], v104 offset:6144\n"
  "v_mfma_f32_16x16x32_bf16 v[12:15], v[52:55], a[36:39], v[100:103]\n"
  "s_add_u32 s57, s57, 6144\n"
  "s_add_u32 m0, s59, 6144\n"
  "ds_read_b64_tr_b16 a[178:179], v119 offset:9216\n"
  "ds_read_b64_tr_b16 a[182:183], v120 offset:9216\n"
  "v_fma_f32 v43, v43, v87, v99\n"
  "v_fma_f32 v47, v47, v87, v99\n"
  "v_mfma_f32_16x16x32_bf16 v[12:15], v[48:51], a[12:15], v[12:15]\n"
  "s_waitcnt lgkmcnt(5)\n"
  "buffer_load_dwordx4 v114, s[40:43], s57 offen offset:0  lds\n"
  "buffer_load_dwordx4 v115, s[40:43], s57 offen offset:1024  lds\n"
  "ds_read_b64_tr_b16 a[186:187], v121 offset:11264\n"
  "ds_read_b64_tr_b16 a[190:191], v122 offset:11264\n"
  "v_mfma_f32_16x16x32_bf16 v[16:19], v[48:51], a[16:19], v[100:103]\n"
  "s_waitcnt vmcnt(11)\n"
  "ds_read_b64_tr_b16 a[192:193], v117 offset:12288\n"
  "ds_read_b64_tr_b16 a[196:197], v118 offset:12288\n"
  "v_fma_f32 v24, v24, v84, v96\n"
  "v_fma_f32 v25, v25, v85, v97\n"
  "v_mfma_f32_16x16x32_bf16 v[16:19], v[52:55], a[40:43], v[16:19]\n"
  "buffer_load_dwordx4 v116, s[40:43], s57 offen offset:2048  lds\n"
  "ds_read_b128 v[76:79], v104 offset:7168\n"
  "v_fma_f32 v28, v28, v84, v96\n"
  "v_mfma_f32_16x16x32_bf16 v[20:23], v[52:55], a[44:47], v[100:103]\n"
  "s_add_u32 s57, s57, 6144\n"
  "s_add_u32 m0, s59, 9216\n"
  "ds_read_b64_tr_b16 a[200:201], v119 offset:12288\n"
  "ds_read_b64_tr_b16 a[204:205], v120 offset:12288\n"
  "v_fma_f32 v29, v29, v85, v97\n"
  "v_fma_f32 v32, v32, v84, v96\n"
  "v_mfma_f32_16x16x32_bf16 v[20:23], v[48:51], a[20:23], v[20:23]\n"
  "s_waitcnt lgkmcnt(5)\n"
  "buffer_load_dwordx4 v114, s[40:43], s57 offen offset:0  lds\n"
  "buffer_load_dwordx4 v115, s[40:43], s57 offen offset:1024  lds\n"
  "ds_read_b64_tr_b16 a[208:209], v121 offset:14336\n"
  "ds_read_b64_tr_b16 a[212:213], v122 offset:14336\n"
  "v_mfma_f32_16x16x32_bf16 v[20:23], v[56:59], a[68:71], v[20:23]\n"
  "buffer_store_dwordx4 v[92:95], v109, s[48:51], s68 offen offset:0 nt\n"
  "s_waitcnt vmcnt(12)\n"
  "ds_read_b64_tr_b16 a[194:195], v117 offset:15360\n"
  "ds_read_b64_tr_b16 a[198:199], v118 offset:15360\n"
  "v_fma_f32 v33, v33, v85, v97\n"
  "v_mfma_f32_16x16x32_bf16 v[20:23], v[60:63], a[92:95], v[20:23]\n"
  "buffer_load_dwordx4 v116, s[40:43], s57 offen offset:2048  lds\n"
  "v_fma_f32 v36, v36, v84, v96\n"
  "v_fma_f32 v37, v37, v85, v97\n"
  "v_mfma_f32_16x16x32_bf16 v[16:19], v[60:63], a[88:91], v[16:19]\n"
  "s_add_u32 s57, s57, 6144\n"
  "s_add_u32 m0, s59, 12288\n"
  "ds_read_b64_tr_b16 a[202:203], v119 offset:15360\n"
  "ds_read_b64_tr_b16 a[206:207], v120 offset:15360\n"
  "v_fma_f32 v26, v26, v86, v98\n"
  "v_fma_f32 v27, v27, v87, v99\n"
  "v_mfma_f32_16x16x32_bf16 v[16:19], v[56:59], a[64:67], v[16:19]\n"
  "s_waitcnt lgkmcnt(4)\n"
  "buffer_load_dwordx4 v114, s[40:43], s57 offen offset:0  lds\n"
  "buffer_load_dwordx4 v115, s[40:43], s57 offen offset:1024  lds\n"
  "ds_read_b64_tr_b16 a[210:211], v121 offset:17408\n"
  "ds_read_b64_tr_b16 a[214:215], v122 offset:17408\n"
  "v_mfma_f32_16x16x32_bf16 v[12:15], v[56:59], a[60:63], v[12:15]\n"
  "s_waitcnt vmcnt(12)\n"
  "ds_read_b64_tr_b16 a[216:217], v117 offset:0\n"
  "ds_read_b64_tr_b16 a[220:221], v118 offset:0\n"
  "v_fma_f32 v30, v30, v86, v98\n"
  "v_fma_f32 v31, v31, v87, v99\n"
  "v_mfma_f32_16x16x32_bf16 v[12:15], v[60:63], a[84:87], v[12:15]\n"
  "buffer_load_dwordx4 v116, s[40:43], s57 offen offset:2048  lds\n"
  "v_fma_f32 v34, v34, v86, v98\n"
  "v_fma_f32 v35, v35, v87, v99\n"
  "v_smfmac_f32_16x16x64_bf16 v[80:83], v[88:91], v[56:63], v124\n"
  "s_add_u32 s57, s57, 6144\n"
  "s_add_u32 m0, s59, 15360\n"
  "ds_read_b64_tr_b16 a[224:225], v119 offset:0\n"
  "ds_read_b64_tr_b16 a[228:229], v120 offset:0\n"
  "v_fma_f32 v38, v38, v86, v98\n"
  "v_mfma_f32_16x16x32_bf16 v[8:11], v[60:63], a[80:83], v[8:11]\n"
  "s_waitcnt lgkmcnt(4)\n"
  "buffer_load_dwordx4 v114, s[40:43], s57 offen offset:0  lds\n"
  "buffer_load_dwordx4 v115, s[40:43], s57 offen offset:1024  lds\n"
  "ds_read_b64_tr_b16 a[232:233], v121 offset:2048\n"
  "ds_read_b64_tr_b16 a[236:237], v122 offset:2048\n"
  "v_mfma_f32_16x16x32_bf16 v[8:11], v[56:59], a[56:59], v[8:11]\n"
  "s_waitcnt vmcnt(12)\n"
  "ds_read_b64_tr_b16 a[218:219], v117 offset:3072\n"
  "ds_read_b64_tr_b16 a[222:223], v118 offset:3072\n"
  "v_fma_f32 v39, v39, v87, v99\n"
  "v_cvt_pk_bf16_f32 v40, v40, v44\n"
  "v_mfma_f32_16x16x32_bf16 v[4:7], v[56:59], a[52:55], v[4:7]\n"
  "buffer_load_dwordx4 v116, s[40:43], s57 offen offset:2048  lds\n"
  "v_cvt_pk_bf16_f32 v41, v41, v45\n"
  "v_cvt_pk_bf16_f32 v42, v42, v46\n"
  "v_mfma_f32_16x16x32_bf16 v[4:7], v[60:63], a[76:79], v[4:7]\n"
  "s_add_u32 s57, s57, s86\n"
  "s_add_u32 m0, s59, 0\n"
  "ds_read_b64_tr_b16 a[226:227], v119 offset:3072\n"
  "ds_read_b64_tr_b16 a[230:231], v120 offset:3072\n"
  "v_cvt_pk_bf16_f32 v43, v43, v47\n"
  "v_cvt_pk_bf16_f32 v24, v24, v28\n"
  "v_mfma_f32_16x16x32_bf16 v[0:3], v[60:63], a[72:75], v[0:3]\n"
  "s_waitcnt lgkmcnt(4)\n"
  "buffer_load_dwordx4 v114, s[40:43], s57 offen offset:0  lds\n"
  "buffer_load_dwordx4 v115, s[40:43], s57 offen offset:1024  lds\n"
  "ds_read_b64_tr_b16 a[234:235], v121 offset:5120\n"
  "ds_read_b64_tr_b16 a[238:239], v122 offset:5120\n"
  "v_mfma_f32_16x16x32_bf16 v[0:3], v[56:59], a[48:51], v[0:3]\n"
  "s_waitcnt vmcnt(12)\n"
  "ds_read_b64_tr_b16 a[240:241], v117 offset:6144\n"
  "ds_read_b64_tr_b16 a[244:245], v118 offset:6144\n"
  "v_cvt_pk_bf16_f32 v28, v25, v29\n"
  "v_cvt_pk_bf16_f32 v25, v32, v36\n"
  "v_mfma_f32_16x16x32_bf16 v[0:3], v[64:67], a[96:99], v[0:3]\n"
  "buffer_load_dwordx4 v116, s[40:43], s57 offen offset:2048  lds\n"
  "ds_read_b128 v[48:51], v105 offset:0\n"
  "v_cvt_pk_bf16_f32 v29, v33, v37\n"
  "v_mfma_f32_16x16x32_bf16 v[0:3], v[68:71], a[120:123], v[0:3]\n"
  "s_add_u32 s57, s57, 6144\n"
  "s_add_u32 m0, s59, 3072\n"
  "ds_read_b64_tr_b16 a[248:249], v119 offset:6144\n"
  "ds_read_b64_tr_b16 a[252:253], v120 offset:6144\n"
  "v_cvt_pk_bf16_f32 v26, v26, v30\n"
  "v_cvt_pk_bf16_f32 v30, v27, v31\n"
  "v_mfma_f32_16x16x32_bf16 v[4:7], v[68:71], a[124:127], v[4:7]\n"
  "s_waitcnt lgkmcnt(5)\n"
  "buffer_load_dwordx4 v114, s[40:43], s57 offen offset:0  lds\n"
  "buffer_load_dwordx4 v115, s[40:43], s57 offen offset:1024  lds\n"
  "ds_read_b128 v[52:55], v105 offset:1024\n"
  "ds_read_b64_tr_b16 v[128:129], v121 offset:8192\n"
  "ds_read_b64_tr_b16 v[132:133], v122 offset:8192\n"
  "v_mfma_f32_16x16x32_bf16 v[4:7], v[64:67], a[100:103], v[4:7]\n"
  "s_waitcnt vmcnt(11)\n"
  "ds_read_b64_tr_b16 a[242:243], v117 offset:9216\n"
  "ds_read_b64_tr_b16 a[246:247], v118 offset:9216\n"
  "ds_read_b64_tr_b16 v[96:97], v108 offset:2304\n"
  "ds_read_b64_tr_b16 v[98:99], v108 offset:2336\n"
  "v_mfma_f32_16x16x32_bf16 v[8:11], v[64:67], a[104:107], v[8:11]\n"
  "buffer_load_dwordx4 v116, s[40:43], s57 offen offset:2048  lds\n"
  "v_cvt_pk_bf16_f32 v27, v34, v38\n"
  "v_cvt_pk_bf16_f32 v31, v35, v39\n"
  "v_mfma_f32_16x16x32_bf16 v[8:11], v[68:71], a[128:131], v[8:11]\n"
  "s_add_u32 s57, s57, 6144\n"
  "s_add_u32 m0, s59, 6144\n"
  "ds_read_b64_tr_b16 a[250:251], v119 offset:9216\n"
  "ds_read_b64_tr_b16 a[254:255], v120 offset:9216\n"
  "ds_write2_b32 v107, v40, v41 offset0:0 offset1:72\n"
  "v_smfmac_f32_16x16x64_bf16 v[80:83], v[88:91], v[64:71], v124\n"
  "s_waitcnt lgkmcnt(7)\n"
  "buffer_load_dwordx4 v114, s[40:43], s57 offen offset:0  lds\n"
  "buffer_load_dwordx4 v115, s[40:43], s57 offen offset:1024  lds\n"
  "ds_read_b128 v[56:59], v105 offset:2048\n"
  "ds_read_b64_tr_b16 v[130:131], v121 offset:11264\n"
  "ds_read_b64_tr_b16 v[134:135], v122 offset:11264\n"
  "v_mfma_f32_16x16x32_bf16 v[12:15], v[68:71], a[132:135], v[12:15]\n"
  "s_waitcnt vmcnt(11)\n"
  "ds_read_b64_tr_b16 v[136:137], v117 offset:12288\n"
  "ds_read_b64_tr_b16 v[140:141], v118 offset:12288\n"
  "ds_write2_b32 v107, v42, v43 offset0:144 offset1:216\n"
  "v_mfma_f32_16x16x32_bf16 v[12:15], v[64:67], a[108:111], v[12:15]\n"
  "buffer_load_dwordx4 v116, s[40:43], s57 offen offset:2048  lds\n"
  "ds_write_b64 v106, v[24:25] offset:0\n"
  "v_mfma_f32_16x16x32_bf16 v[16:19], v[64:67], a[112:115], v[16:19]\n"
  "s_add_u32 s57, s57, 6144\n"
  "s_add_u32 m0, s59, 9216\n"
  "ds_read_b64_tr_b16 v[144:145], v119 offset:12288\n"
  "ds_read_b64_tr_b16 v[148:149], v120 offset:12288\n"
  "ds_write_b64 v106, v[28:29] offset:288\n"
  "v_mfma_f32_16x16x32_bf16 v[16:19], v[68:71], a[136:139], v[16:19]\n"
  "s_waitcnt lgkmcnt(7)\n"
  "buffer_load_dwordx4 v114, s[40:43], s57 offen offset:0  lds\n"
  "buffer_load_dwordx4 v115, s[40:43], s57 offen offset:1024  lds\n"
  "ds_read_b128 v[60:63], v105 offset:3072\n"
  "ds_read_b64_tr_b16 v[152:153], v121 offset:14336\n"
  "ds_read_b64_tr_b16 v[156:157], v122 offset:14336\n"
  "v_mfma_f32_16x16x32_bf16 v[20:23], v[68:71], a[140:143], v[20:23]\n"
  "s_waitcnt vmcnt(11)\n"
  "ds_read_b64_tr_b16 v[138:139], v117 offset:15360\n"
  "ds_read_b64_tr_b16 v[142:143], v118 offset:15360\n"
  "ds_write_b64 v106, v[26:27] offset:576\n"
  "v_mfma_f32_16x16x32_bf16 v[20:23], v[64:67], a[116:119], v[20:23]\n"
  "buffer_load_dwordx4 v116, s[40:43], s57 offen offset:2048  lds\n"
  "ds_write_b64 v106, v[30:31] offset:864\n"
  "v_mfma_f32_16x16x32_bf16 v[20:23], v[72:75], a[164:167], v[20:23]\n"
  "s_add_u32 s57, s57, 6144\n"
  "s_add_u32 m0, s59, 12288\n"
  "buffer_store_dwordx4 v[96:99], v109, s[48:51], s68 offen offset:128 nt\n"
  "ds_read_b64_tr_b16 v[146:147], v119 offset:15360\n"
  "ds_read_b64_tr_b16 v[150:151], v120 offset:15360\n"
  "v_xor_b32 v104, 0x4000, v104\n"
  "v_mfma_f32_16x16x32_bf16 v[20:23], v[76:79], a[188:191], v[20:23]\n"
  "s_waitcnt lgkmcnt(6)\n"
  "buffer_load_dwordx4 v114, s[40:43], s57 offen offset:0  lds\n"
  "buffer_load_dwordx4 v115, s[40:43], s57 offen offset:1024  lds\n"
  "ds_read_b64_tr_b16 v[154:155], v121 offset:17408\n"
  "ds_read_b64_tr_b16 v[158:159], v122 offset:17408\n"
  "v_mfma_f32_16x16x32_bf16 v[16:19], v[76:79], a[184:187], v[16:19]\n"
  "s_waitcnt vmcnt(12)\n"
  "ds_read_b64_tr_b16 v[160:161], v117 offset:0\n"
  "ds_read_b64_tr_b16 v[164:165], v118 offset:0\n"
  "v_mfma_f32_16x16x32_bf16 v[16:19], v[72:75], a[160:163], v[16:19]\n"
  "buffer_load_dwordx4 v116, s[40:43], s57 offen offset:2048  lds\n"
  "v_mfma_f32_16x16x32_bf16 v[12:15], v[72:75], a[156:159], v[12:15]\n"
  "s_add_u32 s57, s57, 6144\n"
  "s_add_u32 m0, s59, 15360\n"
  "ds_read_b64_tr_b16 v[168:169], v119 offset:0\n"
  "ds_read_b64_tr_b16 v[172:173], v120 offset:0\n"
  "v_mfma_f32_16x16x32_bf16 v[12:15], v[76:79], a[180:183], v[12:15]\n"
  "s_waitcnt lgkmcnt(4)\n"
  "buffer_load_dwordx4 v114, s[40:43], s57 offen offset:0  lds\n"
  "buffer_load_dwordx4 v115, s[40:43], s57 offen offset:1024  lds\n"
  "ds_read_b64_tr_b16 v[176:177], v121 offset:2048\n"
  "ds_read_b64_tr_b16 v[180:181], v122 offset:2048\n"
  "v_smfmac_f32_16x16x64_bf16 v[80:83], v[88:91], v[72:79], v124\n"
  "s_waitcnt vmcnt(12)\n"
  "ds_read_b64_tr_b16 v[162:163], v117 offset:3072\n"
  "ds_read_b64_tr_b16 v[166:167], v118 offset:3072\n"
  "ds_read_b64_tr_b16 v[92:93], v108 offset:4608\n"
  "v_mfma_f32_16x16x32_bf16 v[8:11], v[76:79], a[176:179], v[8:11]\n"
  "buffer_load_dwordx4 v116, s[40:43], s57 offen offset:2048  lds\n"
  "ds_read_b64_tr_b16 v[94:95], v108 offset:4640\n"
  "v_xor_b32 v108, 0x2000, v108\n"
  "v_mfma_f32_16x16x32_bf16 v[8:11], v[72:75], a[152:155], v[8:11]\n"
  "s_add_u32 s57, s57, 6144\n"
  "s_add_u32 m0, s59, 0\n"
  "ds_read_b64_tr_b16 v[170:171], v119 offset:3072\n"
  "ds_read_b64_tr_b16 v[174:175], v120 offset:3072\n"
  "v_mfma_f32_16x16x32_bf16 v[4:7], v[72:75], a[148:151], v[4:7]\n"
  "s_waitcnt lgkmcnt(6)\n"
  "buffer_load_dwordx4 v114, s[40:43], s57 offen offset:0  lds\n"
  "buffer_load_dwordx4 v115, s[40:43], s57 offen offset:1024  lds\n"
  "ds_read_b64_tr_b16 v[178:179], v121 offset:5120\n"
  "ds_read_b64_tr_b16 v[182:183], v122 offset:5120\n"
  "v_mfma_f32_16x16x32_bf16 v[4:7], v[76:79], a[172:175], v[4:7]\n"
  "s_waitcnt vmcnt(12)\n"
  "ds_read_b64_tr_b16 v[184:185], v117 offset:6144\n"
  "ds_read_b64_tr_b16 v[188:189], v118 offset:6144\n"
  "v_add_f32 v44, v80, v81\n"
  "v_mov_b32 v80, 0\n"
  "v_mfma_f32_16x16x32_bf16 v[0:3], v[76:79], a[168:171], v[0:3]\n"
  "buffer_load_dwordx4 v116, s[40:43], s57 offen offset:2048  lds\n"
  "ds_write_b32 v110, v44\n"
  "v_mfma_f32_16x16x32_bf16 v[0:3], v[72:75], a[144:147], v[0:3]\n"
  "s_add_u32 s57, s57, 6144\n"
  "s_add_u32 m0, s59, 3072\n"
  "ds_read_b64_tr_b16 v[192:193], v119 offset:6144\n"
  "ds_read_b64_tr_b16 v[196:197], v120 offset:6144\n"
  "v_mov_b32 v81, 0\n"
  "v_mfma_f32_16x16x32_bf16 v[0:3], v[48:51], a[192:195], v[0:3]\n"
  "s_waitcnt lgkmcnt(5)\n"
  "buffer_load_dwordx4 v114, s[40:43], s57 offen offset:0  lds\n"
  "buffer_load_dwordx4 v115, s[40:43], s57 offen offset:1024  lds\n"
  "ds_read_b128 v[64:67], v105 offset:4096\n"
  "ds_read_b64_tr_b16 v[200:201], v121 offset:8192\n"
  "ds_read_b64_tr_b16 v[204:205], v122 offset:8192\n"
  "v_mfma_f32_16x16x32_bf16 v[0:3], v[52:55], a[216:219], v[0:3]\n"
  "s_waitcnt vmcnt(12)\n"
  "ds_read_b64_tr_b16 v[186:187], v117 offset:9216\n"
  "ds_read_b64_tr_b16 v[190:191], v118 offset:9216\n"
  "v_mfma_f32_16x16x32_bf16 v[4:7], v[52:55], a[220:223], v[4:7]\n"
  "s_add_u32 s72, s72, 1\n"
  "buffer_load_dwordx4 v116, s[40:43], s57 offen offset:2048  lds\n"
  "ds_read_b128 v[68:71], v105 offset:5120\n"
  "v_mfma_f32_16x16x32_bf16 v[4:7], v[48:51], a[196:199], v[4:7]\n"
  "s_add_u32 s57, s57, 6144\n"
  "ds_read_b64_tr_b16 v[194:195], v119 offset:9216\n"
  "ds_read_b64_tr_b16 v[198:199], v120 offset:9216\n"
  "v_mfma_f32_16x16x32_bf16 v[8:11], v[48:51], a[200:203], v[8:11]\n"
  "s_lshr_b32 s74, s72, 1\n"
  "buffer_store_dwordx4 v[92:95], v109, s[48:51], s68 offen offset:256 nt\n"
  "ds_read_b64_tr_b16 v[202:203], v121 offset:11264\n"
  "ds_read_b64_tr_b16 v[206:207], v122 offset:11264\n"
  "v_mfma_f32_16x16x32_bf16 v[8:11], v[52:55], a[224:227], v[8:11]\n"
  "s_waitcnt vmcnt(10)\n"
  "ds_read_b64_tr_b16 v[208:209], v117 offset:12288\n"
  "ds_read_b64_tr_b16 v[212:213], v118 offset:12288\n"
  "v_readlane_b32 s73, v126, s74\n"
  "v_mfma_f32_16x16x32_bf16 v[12:15], v[52:55], a[228:231], v[12:15]\n"
  "s_lshl_b32 s74, s72, 4\n"
  "ds_read_b128 v[72:75], v105 offset:6144\n"
  "v_mfma_f32_16x16x32_bf16 v[12:15], v[48:51], a[204:207], v[12:15]\n"
  "s_lshr_b32 s73, s73, s74\n"
  "ds_read_b64_tr_b16 v[216:217], v119 offset:12288\n"
  "ds_read_b64_tr_b16 v[220:221], v120 offset:12288\n"
  "v_mfma_f32_16x16x32_bf16 v[16:19], v[48:51], a[208:211], v[16:19]\n"
  "s_and_b32 s73, s73, 0xffff\n"
  "ds_read_b64_tr_b16 v[224:225], v121 offset:14336\n"
  "ds_read_b64_tr_b16 v[228:229], v122 offset:14336\n"
  "v_mfma_f32_16x16x32_bf16 v[16:19], v[52:55], a[232:235], v[16:19]\n"
  "s_waitcnt vmcnt(7)\n"
  "ds_read_b64_tr_b16 v[210:211], v117 offset:15360\n"
  "ds_read_b64_tr_b16 v[214:215], v118 offset:15360\n"
  "v_mfma_f32_16x16x32_bf16 v[20:23], v[52:55], a[236:239], v[20:23]\n"
  "s_lshl_b32 s73, s73, 13\n"
  "ds_read_b128 v[76:79], v105 offset:7168\n"
  "v_xor_b32 v105, 0x4000, v105\n"
  "v_mfma_f32_16x16x32_bf16 v[20:23], v[48:51], a[212:215], v[20:23]\n"
  "ds_read_b64_tr_b16 v[218:219], v119 offset:15360\n"
  "ds_read_b64_tr_b16 v[222:223], v120 offset:15360\n"
  "v_mfma_f32_16x16x32_bf16 v[20:23], v[56:59], v[132:135], v[20:23]\n"
  "s_add_u32 s68, s68, s69\n"
  "ds_read_b64_tr_b16 v[226:227], v121 offset:17408\n"
  "ds_read_b64_tr_b16 v[230:231], v122 offset:17408\n"
  "v_mfma_f32_16x16x32_bf16 v[20:23], v[60:63], v[156:159], v[20:23]\n"
  "s_waitcnt vmcnt(4)\n"
  "ds_read_b64_tr_b16 v[232:233], v117 offset:0\n"
  "ds_read_b64_tr_b16 v[236:237], v118 offset:0\n"
  "v_mfma_f32_16x16x32_bf16 v[16:19], v[60:63], v[152:155], v[16:19]\n"
  "s_mov_b32 s69, s70\n"
  "v_mfma_f32_16x16x32_bf16 v[16:19], v[56:59], v[128:131], v[16:19]\n"
  "s_movk_i32 s70, 6144\n"
  "ds_read_b64_tr_b16 v[240:241], v119 offset:0\n"
  "ds_read_b64_tr_b16 v[244:245], v120 offset:0\n"
  "v_mfma_f32_16x16x32_bf16 v[12:15], v[56:59], a[252:255], v[12:15]\n"
  "ds_read_b64_tr_b16 v[248:249], v121 offset:2048\n"
  "ds_read_b64_tr_b16 v[252:253], v122 offset:2048\n"
  "v_mfma_f32_16x16x32_bf16 v[12:15], v[60:63], v[148:151], v[12:15]\n"
  "s_waitcnt vmcnt(1)\n"
  "ds_read_b64_tr_b16 v[234:235], v117 offset:3072\n"
  "ds_read_b64_tr_b16 v[238:239], v118 offset:3072\n"
  "v_mfma_f32_16x16x32_bf16 v[8:11], v[60:63], v[144:147], v[8:11]\n"
  "v_mfma_f32_16x16x32_bf16 v[8:11], v[56:59], a[248:251], v[8:11]\n"
  "ds_read_b64_tr_b16 v[242:243], v119 offset:3072\n"
  "ds_read_b64_tr_b16 v[246:247], v120 offset:3072\n"
  "v_xor_b32 v106, 0x2000, v106\n"
  "v_xor_b32 v107, 0x2000, v107\n"
  "v_mfma_f32_16x16x32_bf16 v[4:7], v[56:59], a[244:247], v[4:7]\n"
  "ds_read_b64_tr_b16 v[250:251], v121 offset:5120\n"
  "ds_read_b64_tr_b16 v[254:255], v122 offset:5120\n"
  "v_xor_b32 v110, 0x2000, v110\n"
  "v_mfma_f32_16x16x32_bf16 v[4:7], v[60:63], v[140:143], v[4:7]\n"
  "v_mfma_f32_16x16x32_bf16 v[0:3], v[60:63], v[136:139], v[0:3]\n"
  "v_mfma_f32_16x16x32_bf16 v[0:3], v[56:59], a[240:243], v[0:3]\n"
  "s_add_u32 m0, s66, 0\n"
  "s_nop 0\n"
  "buffer_load_dwordx4 v112, s[44:47], s67 offen offset:0 nt lds\n"
  "s_add_u32 m0, s66, 1920\n"
  "s_nop 0\n"
  "buffer_load_dwordx4 v112, s[44:47], s67 offen offset:128 nt lds\n"
  "s_add_u32 m0, s66, 3840\n"
  "s_nop 0\n"
  "buffer_load_dwordx4 v112, s[44:47], s67 offen offset:256 nt lds\n"
  "s_add_u32 s66, s66, 6144\n"
  "s_cmp_ge_u32 s66, 86016\n"
  "s_cselect_b32 s64, 36864, 0\n"
  "s_sub_u32 s66, s66, s64\n"
  "s_sub_u32 s90, s90, 1\n"
  "s_cselect_b32 s63, s91, 6144\n"
  "s_cmov_b64 s[90:91], s[92:93]\n"
  "s_cmov_b64 s[92:93], s[94:95]\n"
  "s_add_u32 s67, s67, s63\n"
  "s_add_u32 m0, s66, 0\n"
  "s_nop 0\n"
  "buffer_load_dwordx4 v112, s[44:47], s67 offen offset:0 nt lds\n"
  "s_add_u32 m0, s66, 1920\n"
  "s_nop 0\n"
  "buffer_load_dwordx4 v112, s[44:47], s67 offen offset:128 nt lds\n"
  "s_add_u32 m0, s66, 3840\n"
  "s_nop 0\n"
  "buffer_load_dwordx4 v112, s[44:47], s67 offen offset:256 nt lds\n"
  "s_add_u32 s66, s66, 6144\n"
  "s_cmp_ge_u32 s66, 86016\n"
  "s_cselect_b32 s64, 36864, 0\n"
  "s_sub_u32 s66, s66, s64\n"
  "s_sub_u32 s90, s90, 1\n"
  "s_cselect_b32 s63, s91, 6144\n"
  "s_cmov_b64 s[90:91], s[92:93]\n"
  "s_cmov_b64 s[92:93], s[94:95]\n"
  "s_add_u32 s67, s67, s63\n"
  "s_add_u32 m0, s66, 0\n"
  "s_nop 0\n"
  "buffer_load_dwordx4 v112, s[44:47], s67 offen offset:0 nt lds\n"
  "s_add_u32 m0, s66, 1920\n"
  "s_nop 0\n"
  "buffer_load_dwordx4 v112, s[44:47], s67 offen offset:128 nt lds\n"
  "s_add_u32 m0, s66, 3840\n"
  "s_nop 0\n"
  "buffer_load_dwordx4 v112, s[44:47], s67 offen offset:256 nt lds\n"
  "s_add_u32 s66, s66, 6144\n"
  "s_cmp_ge_u32 s66, 86016\n"
  "s_cselect_b32 s64, 36864, 0\n"
  "s_sub_u32 s66, s66, s64\n"
  "s_sub_u32 s90, s90, 1\n"
  "s_cselect_b32 s63, s91, 6144\n"
  "s_cmov_b64 s[90:91], s[92:93]\n"
  "s_cmov_b64 s[92:93], s[94:95]\n"
  "s_add_u32 s67, s67, s63\n"
  "s_add_u32 m0, s66, 0\n"
  "s_nop 0\n"
  "buffer_load_dwordx4 v112, s[44:47], s67 offen offset:0 nt lds\n"
  "s_add_u32 m0, s66, 1920\n"
  "s_nop 0\n"
  "buffer_load_dwordx4 v112, s[44:47], s67 offen offset:128 nt lds\n"
  "s_add_u32 m0, s66, 3840\n"
  "s_nop 0\n"
  "buffer_load_dwordx4 v112, s[44:47], s67 offen offset:256 nt lds\n"
  "s_add_u32 s66, s66, 6144\n"
  "s_cmp_ge_u32 s66, 86016\n"
  "s_cselect_b32 s64, 36864, 0\n"
  "s_sub_u32 s66, s66, s64\n"
  "s_sub_u32 s90, s90, 1\n"
  "s_cselect_b32 s63, s91, 6144\n"
  "s_cmov_b64 s[90:91], s[92:93]\n"
  "s_cmov_b64 s[92:93], s[94:95]\n"
  "s_add_u32 s67, s67, s63\n"
  ".p2align 6\n"
  "s_nop 0\n"
  "B_H1_%=:\n"
  "s_waitcnt vmcnt(16)\n"
  "B_H_%=:\n"
  "s_waitcnt vmcnt(32) lgkmcnt(0)\n"
  "s_barrier\n"
  "v_mfma_f32_16x16x32_bf16 v[20:23], v[64:67], v[180:183], v[20:23]\n"
  "s_add_u32 m0, s66, 0\n"
  "ds_read_b128 v[48:51], v104 offset:0\n"
  "ds_read_b128 v[84:87], v111\n"
  "v_mfma_f32_16x16x32_bf16 v[20:23], v[68:71], v[204:207], v[20:23]\n"
  "s_sub_u32 s56, s56, 1\n"
  "ds_read_b128 v[96:99], v111 offset:64\n"
  "v_xor_b32 v111, 0x2000, v111\n"
  "v_mfma_f32_16x16x32_bf16 v[20:23], v[72:75], v[228:231], v[20:23]\n"
  "ds_read_b128 v[52:55], v104 offset:1024\n"
  "v_mfma_f32_16x16x32_bf16 v[44:47], v[76:79], v[252:255], v[20:23]\n"
  "buffer_load_dwordx4 v112, s[44:47], s67 offen offset:0 nt lds\n"
  "v_mfma_f32_16x16x32_bf16 v[16:19], v[76:79], v[248:251], v[16:19]\n"
  "s_add_u32 m0, s66, 1920\n"
  "v_mfma_f32_16x16x32_bf16 v[16:19], v[72:75], v[224:227], v[16:19]\n"
  "v_mfma_f32_16x16x32_bf16 v[16:19], v[68:71], v[200:203], v[16:19]\n"
  "ds_read_b128 v[56:59], v104 offset:2048\n"
  "v_mfma_f32_16x16x32_bf16 v[40:43], v[64:67], v[176:179], v[16:19]\n"
  "buffer_load_dwordx4 v112, s[44:47], s67 offen offset:128 nt lds\n"
  "v_mfma_f32_16x16x32_bf16 v[12:15], v[64:67], v[172:175], v[12:15]\n"
  "s_add_u32 m0, s66, 3840\n"
  "v_mfma_f32_16x16x32_bf16 v[12:15], v[68:71], v[196:199], v[12:15]\n"
  "v_mfma_f32_16x16x32_bf16 v[12:15], v[72:75], v[220:223], v[12:15]\n"
  "s_waitcnt lgkmcnt(2)\n"
  "ds_read_b128 v[60:63], v104 offset:3072\n"
  "v_add_f32 v84, v84, v96\n"
  "v_mfma_f32_16x16x32_bf16 v[36:39], v[76:79], v[244:247], v[12:15]\n"
  "buffer_load_dwordx4 v112, s[44:47], s67 offen offset:256 nt lds\n"
  "v_mfma_f32_16x16x32_bf16 v[8:11], v[76:79], v[240:243], v[8:11]\n"
  "s_mov_b32 m0, 159744\n"
  "v_add_f32 v85, v85, v97\n"
  "v_add_f32 v86, v86, v98\n"
  "v_mfma_f32_16x16x32_bf16 v[8:11], v[72:75], v[216:219], v[8:11]\n"
  "v_add_f32 v87, v87, v99\n"
  "v_max3_f32 v82, v82, v84, v85\n"
  "v_mfma_f32_16x16x32_bf16 v[8:11], v[68:71], v[192:195], v[8:11]\n"
  "v_rcp_f32 v84, v84\n"
  "v_mfma_f32_16x16x32_bf16 v[32:35], v[64:67], v[168:171], v[8:11]\n"
  "buffer_load_dword v125, s[80:83], s73 offen  lds\n"
  "v_mfma_f32_16x16x32_bf16 v[4:7], v[64:67], v[164:167], v[4:7]\n"
  "v_rcp_f32 v85, v85\n"
  "v_mfma_f32_16x16x32_bf16 v[4:7], v[68:71], v[188:191], v[4:7]\n"
  "v_max3_f32 v82, v82, v86, v87\n"
  "v_max3_f32 v83, v83, v84, v85\n"
  "v_mfma_f32_16x16x32_bf16 v[4:7], v[72:75], v[212:215], v[4:7]\n"
  "v_rcp_f32 v86, v86\n"
  "v_mfma_f32_16x16x32_bf16 v[28:31], v[76:79], v[236:239], v[4:7]\n"
  "v_rcp_f32 v87, v87\n"
  "v_mfma_f32_16x16x32_bf16 v[0:3], v[76:79], v[232:235], v[0:3]\n"
  "v_max3_f32 v83, v83, v86, v87\n"
  "v_mul_f32 v96, s99, v84\n"
  "v_mfma_f32_16x16x32_bf16 v[0:3], v[72:75], v[208:211], v[0:3]\n"
  "v_mul_f32 v97, s99, v85\n"
  "v_fma_f32 v40, v40, v84, v96\n"
  "v_mfma_f32_16x16x32_bf16 v[0:3], v[68:71], v[184:187], v[0:3]\n"
  "v_fma_f32 v44, v44, v84, v96\n"
  "v_cvt_pk_bf16_f32 v40, v40, v44\n"
  "v_mfma_f32_16x16x32_bf16 v[24:27], v[64:67], v[160:163], v[0:3]\n"
  "s_waitcnt lgkmcnt(0)\n"
  "v_fma_f32 v41, v41, v85, v97\n"
  "v_fma_f32 v45, v45, v85, v97\n"
  "v_mfma_f32_16x16x32_bf16 v[0:3], v[48:51], a[0:3], v[100:103]\n"
  "ds_read_b128 v[64:67], v104 offset:4096\n"
  "v_cvt_pk_bf16_f32 v41, v41, v45\n"
  "v_mfma_f32_16x16x32_bf16 v[0:3], v[52:55], a[24:27], v[0:3]\n"
  "v_mul_f32 v98, s99, v86\n"
  "v_mul_f32 v99, s99, v87\n"
  "v_mfma_f32_16x16x32_bf16 v[0:3], v[56:59], a[48:51], v[0:3]\n"
  "ds_read_b128 v[68:71], v104 offset:5120\n"
  "v_fma_f32 v42, v42, v86, v98\n"
  "v_mfma_f32_16x16x32_bf16 v[0:3], v[60:63], a[72:75], v[0:3]\n"
  "ds_read_b64_tr_b16 v[92:93], v108 offset:0\n"
  "ds_read_b64_tr_b16 v[94:95], v108 offset:32\n"
  "v_mfma_f32_16x16x32_bf16 v[4:7], v[60:63], a[76:79], v[100:103]\n"
  "v_fma_f32 v46, v46, v86, v98\n"
  "v_cvt_pk_bf16_f32 v42, v42, v46\n"
  "v_mfma_f32_16x16x32_bf16 v[4:7], v[56:59], a[52:55], v[4:7]\n"
  "v_fma_f32 v43, v43, v87, v99\n"
  "v_fma_f32 v47, v47, v87, v99\n"
  "v_mfma_f32_16x16x32_bf16 v[4:7], v[52:55], a[28:31], v[4:7]\n"
  "ds_read_b128 v[72:75], v104 offset:6144\n"
  "v_cvt_pk_bf16_f32 v43, v43, v47\n"
  "v_mfma_f32_16x16x32_bf16 v[4:7], v[48:51], a[4:7], v[4:7]\n"
  "v_fma_f32 v24, v24, v84, v96\n"
  "v_fma_f32 v25, v25, v85, v97\n"
  "v_mfma_f32_16x16x32_bf16 v[8:11], v[48:51], a[8:11], v[100:103]\n"
  "v_fma_f32 v28, v28, v84, v96\n"
  "v_fma_f32 v29, v29, v85, v97\n"
  "v_mfma_f32_16x16x32_bf16 v[8:11], v[52:55], a[32:35], v[8:11]\n"
  "v_fma_f32 v32, v32, v84, v96\n"
  "v_fma_f32 v36, v36, v84, v96\n"
  "v_mfma_f32_16x16x32_bf16 v[8:11], v[56:59], a[56:59], v[8:11]\n"
  "ds_read_b128 v[76:79], v104 offset:7168\n"
  "v_cvt_pk_bf16_f32 v24, v24, v28\n"
  "v_mfma_f32_16x16x32_bf16 v[8:11], v[60:63], a[80:83], v[8:11]\n"
  "v_cvt_pk_bf16_f32 v28, v25, v29\n"
  "v_cvt_pk_bf16_f32 v25, v32, v36\n"
  "v_smfmac_f32_16x16x64_bf16 v[80:83], v[88:91], v[48:55], v124\n"
  "v_fma_f32 v33, v33, v85, v97\n"
  "v_smfmac_f32_16x16x64_bf16 v[80:83], v[88:91], v[56:63], v124\n"
  "s_waitcnt lgkmcnt(2)\n"
  "buffer_store_dwordx4 v[92:95], v109, s[48:51], s68 offen offset:0 nt\n"
  "ds_write2_b32 v107, v40, v41 offset0:0 offset1:72\n"
  "v_mfma_f32_16x16x32_bf16 v[12:15], v[60:63], a[84:87], v[100:103]\n"
  "v_fma_f32 v37, v37, v85, v97\n"
  "v_fma_f32 v26, v26, v86, v98\n"
  "v_mfma_f32_16x16x32_bf16 v[12:15], v[56:59], a[60:63], v[12:15]\n"
  "v_fma_f32 v27, v27, v87, v99\n"
  "v_fma_f32 v30, v30, v86, v98\n"
  "v_mfma_f32_16x16x32_bf16 v[12:15], v[52:55], a[36:39], v[12:15]\n"
  "v_fma_f32 v31, v31, v87, v99\n"
  "v_fma_f32 v34, v34, v86, v98\n"
  "v_mfma_f32_16x16x32_bf16 v[12:15], v[48:51], a[12:15], v[12:15]\n"
  "ds_write2_b32 v107, v42, v43 offset0:144 offset1:216\n"
  "v_mfma_f32_16x16x32_bf16 v[16:19], v[48:51], a[16:19], v[100:103]\n"
  "v_fma_f32 v35, v35, v87, v99\n"
  "v_fma_f32 v38, v38, v86, v98\n"
  "v_mfma_f32_16x16x32_bf16 v[16:19], v[52:55], a[40:43], v[16:19]\n"
  "v_fma_f32 v39, v39, v87, v99\n"
  "v_cvt_pk_bf16_f32 v29, v33, v37\n"
  "v_mfma_f32_16x16x32_bf16 v[16:19], v[56:59], a[64:67], v[16:19]\n"
  "v_cvt_pk_bf16_f32 v26, v26, v30\n"
  "v_cvt_pk_bf16_f32 v30, v27, v31\n"
  "v_mfma_f32_16x16x32_bf16 v[16:19], v[60:63], a[88:91], v[16:19]\n"
  "v_cvt_pk_bf16_f32 v27, v34, v38\n"
  "v_cvt_pk_bf16_f32 v31, v35, v39\n"
  "v_mfma_f32_16x16x32_bf16 v[20:23], v[60:63], a[92:95], v[100:103]\n"
  "v_xor_b32 v104, 0x4000, v104\n"
  "v_mfma_f32_16x16x32_bf16 v[20:23], v[56:59], a[68:71], v[20:23]\n"
  "v_mfma_f32_16x16x32_bf16 v[20:23], v[52:55], a[44:47], v[20:23]\n"
  "v_mfma_f32_16x16x32_bf16 v[20:23], v[48:51], a[20:23], v[20:23]\n"
  "s_waitcnt lgkmcnt(2)\n"
  "v_mfma_f32_16x16x32_bf16 v[20:23], v[64:67], a[116:119], v[20:23]\n"
  "s_add_u32 s66, s66, 6144\n"
  "ds_read_b128 v[48:51], v105 offset:0\n"
  "v_mfma_f32_16x16x32_bf16 v[20:23], v[68:71], a[140:143], v[20:23]\n"
  "s_cmp_ge_u32 s66, 86016\n"
  "s_cselect_b32 s64, 36864, 0\n"
  "v_mfma_f32_16x16x32_bf16 v[20:23], v[72:75], a[164:167], v[20:23]\n"
  "s_sub_u32 s66, s66, s64\n"
  "ds_read_b128 v[52:55], v105 offset:1024\n"
  "v_mfma_f32_16x16x32_bf16 v[20:23], v[76:79], a[188:191], v[20:23]\n"
  "ds_write_b64 v106, v[24:25] offset:0\n"
  "v_mfma_f32_16x16x32_bf16 v[16:19], v[76:79], a[184:187], v[16:19]\n"
  "s_sub_u32 s90, s90, 1\n"
  "s_cselect_b32 s63, s91, 6144\n"
  "s_cmov_b64 s[90:91], s[92:93]\n"
  "s_cmov_b64 s[92:93], s[94:95]\n"
  "ds_read_b64_tr_b16 v[96:97], v108 offset:2304\n"
  "ds_read_b64_tr_b16 v[98:99], v108 offset:2336\n"
  "v_mfma_f32_16x16x32_bf16 v[16:19], v[72:75], a[160:163], v[16:19]\n"
  "s_add_u32 s67, s67, s63\n"
  "v_mfma_f32_16x16x32_bf16 v[16:19], v[68:71], a[136:139], v[16:19]\n"
  "ds_read_b128 v[56:59], v105 offset:2048\n"
  "v_mfma_f32_16x16x32_bf16 v[16:19], v[64:67], a[112:115], v[16:19]\n"
  "v_mfma_f32_16x16x32_bf16 v[12:15], v[64:67], a[108:111], v[12:15]\n"
  "v_mfma_f32_16x16x32_bf16 v[12:15], v[68:71], a[132:135], v[12:15]\n"
  "v_mfma_f32_16x16x32_bf16 v[12:15], v[72:75], a[156:159], v[12:15]\n"
  "ds_read_b128 v[60:63], v105 offset:3072\n"
  "v_mfma_f32_16x16x32_bf16 v[12:15], v[76:79], a[180:183], v[12:15]\n"
  "v_smfmac_f32_16x16x64_bf16 v[80:83], v[88:91], v[64:71], v124\n"
  "v_smfmac_f32_16x16x64_bf16 v[80:83], v[88:91], v[72:79], v124\n"
  "s_waitcnt lgkmcnt(2)\n"
  "buffer_store_dwordx4 v[96:99], v109, s[48:51], s68 offen offset:128 nt\n"
  "ds_write_b64 v106, v[28:29] offset:288\n"
  "v_mfma_f32_16x16x32_bf16 v[8:11], v[76:79], a[176:179], v[8:11]\n"
  "v_mfma_f32_16x16x32_bf16 v[8:11], v[72:75], a[152:155], v[8:11]\n"
  "v_mfma_f32_16x16x32_bf16 v[8:11], v[68:71], a[128:131], v[8:11]\n"
  "v_add_f32 v44, v80, v81\n"
  "v_mov_b32 v80, 0\n"
  "v_mfma_f32_16x16x32_bf16 v[8:11], v[64:67], a[104:107], v[8:11]\n"
  "ds_write_b32 v110, v44\n"
  "v_mfma_f32_16x16x32_bf16 v[4:7], v[64:67], a[100:103], v[4:7]\n"
  "v_mov_b32 v81, 0\n"
  "v_mfma_f32_16x16x32_bf16 v[4:7], v[68:71], a[124:127], v[4:7]\n"
  "ds_read_b64_tr_b16 v[92:93], v108 offset:4608\n"
  "ds_read_b64_tr_b16 v[94:95], v108 offset:4640\n"
  "v_mfma_f32_16x16x32_bf16 v[4:7], v[72:75], a[148:151], v[4:7]\n"
  "v_xor_b32 v108, 0x2000, v108\n"
  "v_mfma_f32_16x16x32_bf16 v[4:7], v[76:79], a[172:175], v[4:7]\n"
  "v_mfma_f32_16x16x32_bf16 v[0:3], v[76:79], a[168:171], v[0:3]\n"
  "v_mfma_f32_16x16x32_bf16 v[0:3], v[72:75], a[144:147], v[0:3]\n"
  "v_mfma_f32_16x16x32_bf16 v[0:3], v[68:71], a[120:123], v[0:3]\n"
  "v_mfma_f32_16x16x32_bf16 v[0:3], v[64:67], a[96:99], v[0:3]\n"
  "s_waitcnt lgkmcnt(4)\n"
  "v_mfma_f32_16x16x32_bf16 v[0:3], v[48:51], a[192:195], v[0:3]\n"
  "s_add_u32 s72, s72, 1\n"
  "ds_read_b128 v[64:67], v105 offset:4096\n"
  "v_mfma_f32_16x16x32_bf16 v[0:3], v[52:55], a[216:219], v[0:3]\n"
  "s_lshr_b32 s74, s72, 1\n"
  "v_readlane_b32 s73, v126, s74\n"
  "v_mfma_f32_16x16x32_bf16 v[0:3], v[56:59], a[240:243], v[0:3]\n"
  "s_lshl_b32 s74, s72, 4\n"
  "ds_read_b128 v[68:71], v105 offset:5120\n"
  "v_mfma_f32_16x16x32_bf16 v[0:3], v[60:63], v[136:139], v[0:3]\n"
  "s_lshr_b32 s73, s73, s74\n"
  "s_waitcnt lgkmcnt(2)\n"
  "buffer_store_dwordx4 v[92:95], v109, s[48:51], s68 offen offset:256 nt\n"
  "ds_write_b64 v106, v[26:27] offset:576\n"
  "v_mfma_f32_16x16x32_bf16 v[4:7], v[60:63], v[140:143], v[4:7]\n"
  "s_and_b32 s73, s73, 0xffff\n"
  "v_mfma_f32_16x16x32_bf16 v[4:7], v[56:59], a[244:247], v[4:7]\n"
  "s_lshl_b32 s73, s73, 13\n"
  "v_mfma_f32_16x16x32_bf16 v[4:7], v[52:55], a[220:223], v[4:7]\n"
  "s_add_u32 s68, s68, s69\n"
  "ds_read_b128 v[72:75], v105 offset:6144\n"
  "v_mfma_f32_16x16x32_bf16 v[4:7], v[48:51], a[196:199], v[4:7]\n"
  "s_mov_b32 s69, s70\n"
  "v_mfma_f32_16x16x32_bf16 v[8:11], v[48:51], a[200:203], v[8:11]\n"
  "s_movk_i32 s70, 6144\n"
  "v_mfma_f32_16x16x32_bf16 v[8:11], v[52:55], a[224:227], v[8:11]\n"
  "v_mfma_f32_16x16x32_bf16 v[8:11], v[56:59], a[248:251], v[8:11]\n"
  "ds_read_b128 v[76:79], v105 offset:7168\n"
  "v_xor_b32 v105, 0x4000, v105\n"
  "v_mfma_f32_16x16x32_bf16 v[8:11], v[60:63], v[144:147], v[8:11]\n"
  "v_mfma_f32_16x16x32_bf16 v[12:15], v[60:63], v[148:151], v[12:15]\n"
  "v_mfma_f32_16x16x32_bf16 v[12:15], v[56:59], a[252:255], v[12:15]\n"
  "v_mfma_f32_16x16x32_bf16 v[12:15], v[52:55], a[228:231], v[12:15]\n"
  "v_mfma_f32_16x16x32_bf16 v[12:15], v[48:51], a[204:207], v[12:15]\n"
  "ds_write_b64 v106, v[30:31] offset:864\n"
  "v_mfma_f32_16x16x32_bf16 v[16:19], v[48:51], a[208:211], v[16:19]\n"
  "v_mfma_f32_16x16x32_bf16 v[16:19], v[52:55], a[232:235], v[16:19]\n"
  "v_mfma_f32_16x16x32_bf16 v[16:19], v[56:59], v[128:131], v[16:19]\n"
  "v_mfma_f32_16x16x32_bf16 v[16:19], v[60:63], v[152:155], v[16:19]\n"
  "v_xor_b32 v106, 0x2000, v106\n"
  "v_xor_b32 v107, 0x2000, v107\n"
  "v_mfma_f32_16x16x32_bf16 v[20:23], v[60:63], v[156:159], v[20:23]\n"
  "v_xor_b32 v110, 0x2000, v110\n"
  "v_mfma_f32_16x16x32_bf16 v[20:23], v[56:59], v[132:135], v[20:23]\n"
  "v_mfma_f32_16x16x32_bf16 v[20:23], v[52:55], a[236:239], v[20:23]\n"
  "s_sub_u32 s62, s56, 1\n"
  "s_cmp_lt_u32 s62, s61\n"
  "v_mfma_f32_16x16x32_bf16 v[20:23], v[48:51], a[212:215], v[20:23]\n"
  "s_cbranch_scc1 B_H_%=\n"
  "s_cmp_lg_u32 s56, 0\n"
  "s_cbranch_scc1 B_H1_%=\n"
  "s_mov_b32 s52, s53\n"
  "s_mov_b32 s53, s54\n"
  "s_mov_b32 s54, s55\n"
  "s_cmp_eq_u32 s52, 0\n"
  "s_cbranch_scc1 B_LAST_%=\n"
  "s_lshr_b32 s62, s52, 16\n"
  "s_lshl1_add_u32 s62, s62, s65\n"
  "s_mul_i32 s57, s62, 98304\n"
  "s_and_b32 s56, s52, 0xff\n"
  "s_cmp_lg_u32 s53, 0\n"
  "s_cselect_b32 s62, 6, 0\n"
  "s_sub_u32 s56, s56, s62\n"
  "s_waitcnt vmcnt(16) lgkmcnt(0)\n"
  "s_barrier\n"
  "v_mfma_f32_16x16x32_bf16 v[20:23], v[64:67], v[180:183], v[20:23]\n"
  "s_add_u32 m0, s66, 0\n"
  "ds_read_b128 v[48:51], v104 offset:0\n"
  "ds_read_b128 v[84:87], v111\n"
  "v_mfma_f32_16x16x32_bf16 v[20:23], v[68:71], v[204:207], v[20:23]\n"
  "ds_read_b128 v[96:99], v111 offset:64\n"
  "v_xor_b32 v111, 0x2000, v111\n"
  "v_mfma_f32_16x16x32_bf16 v[20:23], v[72:75], v[228:231], v[20:23]\n"
  "ds_read_b128 v[52:55], v104 offset:1024\n"
  "v_mfma_f32_16x16x32_bf16 v[44:47], v[76:79], v[252:255], v[20:23]\n"
  "buffer_load_dwordx4 v112, s[44:47], s67 offen offset:0  lds\n"
  "v_mfma_f32_16x16x32_bf16 v[16:19], v[76:79], v[248:251], v[16:19]\n"
  "s_add_u32 m0, s66, 1920\n"
  "v_mfma_f32_16x16x32_bf16 v[16:19], v[72:75], v[224:227], v[16:19]\n"
  "v_mfma_f32_16x16x32_bf16 v[16:19], v[68:71], v[200:203], v[16:19]\n"
  "ds_read_b128 v[56:59], v104 offset:2048\n"
  "v_mfma_f32_16x16x32_bf16 v[40:43], v[64:67], v[176:179], v[16:19]\n"
  "buffer_load_dwordx4 v112, s[44:47], s67 offen offset:128  lds\n"
  "v_mfma_f32_16x16x32_bf16 v[12:15], v[64:67], v[172:175], v[12:15]\n"
  "s_add_u32 m0, s66, 3840\n"
  "v_mfma_f32_16x16x32_bf16 v[12:15], v[68:71], v[196:199], v[12:15]\n"
  "v_mfma_f32_16x16x32_bf16 v[12:15], v[72:75], v[220:223], v[12:15]\n"
  "s_waitcnt lgkmcnt(2)\n"
  "ds_read_b128 v[60:63], v104 offset:3072\n"
  "v_add_f32 v84, v84, v96\n"
  "v_mfma_f32_16x16x32_bf16 v[36:39], v[76:79], v[244:247], v[12:15]\n"
  "buffer_load_dwordx4 v112, s[44:47], s67 offen offset:256  lds\n"
  "v_mfma_f32_16x16x32_bf16 v[8:11], v[76:79], v[240:243], v[8:11]\n"
  "v_add_f32 v85, v85, v97\n"
  "v_add_f32 v86, v86, v98\n"
  "v_mfma_f32_16x16x32_bf16 v[8:11], v[72:75], v[216:219], v[8:11]\n"
  "v_add_f32 v87, v87, v99\n"
  "v_max3_f32 v82, v82, v84, v85\n"
  "v_mfma_f32_16x16x32_bf16 v[8:11], v[68:71], v[192:195], v[8:11]\n"
  "v_rcp_f32 v84, v84\n"
  "v_mfma_f32_16x16x32_bf16 v[32:35], v[64:67], v[168:171], v[8:11]\n"
  "v_rcp_f32 v85, v85\n"
  "v_mfma_f32_16x16x32_bf16 v[4:7], v[64:67], v[164:167], v[4:7]\n"
  "v_max3_f32 v82, v82, v86, v87\n"
  "v_max3_f32 v83, v83, v84, v85\n"
  "v_mfma_f32_16x16x32_bf16 v[4:7], v[68:71], v[188:191], v[4:7]\n"
  "v_rcp_f32 v86, v86\n"
  "v_mfma_f32_16x16x32_bf16 v[4:7], v[72:75], v[212:215], v[4:7]\n"
  "v_rcp_f32 v87, v87\n"
  "v_mfma_f32_16x16x32_bf16 v[28:31], v[76:79], v[236:239], v[4:7]\n"
  "v_max3_f32 v83, v83, v86, v87\n"
  "v_mul_f32 v96, s99, v84\n"
  "v_mfma_f32_16x16x32_bf16 v[0:3], v[76:79], v[232:235], v[0:3]\n"
  "s_add_u32 m0, s75, 0\n"
  "v_mul_f32 v97, s99, v85\n"
  "v_mul_f32 v98, s99, v86\n"
  "v_mfma_f32_16x16x32_bf16 v[0:3], v[72:75], v[208:211], v[0:3]\n"
  "buffer_load_dwordx4 v112, s[76:79], s57 offen offset:0  lds\n"
  "v_mfma_f32_16x16x32_bf16 v[0:3], v[68:71], v[184:187], v[0:3]\n"
  "v_mul_f32 v99, s99, v87\n"
  "v_fma_f32 v40, v40, v84, v96\n"
  "v_mfma_f32_16x16x32_bf16 v[24:27], v[64:67], v[160:163], v[0:3]\n"
  "s_waitcnt lgkmcnt(0)\n"
  "v_fma_f32 v44, v44, v84, v96\n"
  "v_fma_f32 v41, v41, v85, v97\n"
  "v_mfma_f32_16x16x32_bf16 v[0:3], v[48:51], a[0:3], v[100:103]\n"
  "ds_read_b128 v[64:67], v104 offset:4096\n"
  "v_fma_f32 v45, v45, v85, v97\n"
  "v_mfma_f32_16x16x32_bf16 v[0:3], v[52:55], a[24:27], v[0:3]\n"
  "v_fma_f32 v42, v42, v86, v98\n"
  "v_fma_f32 v46, v46, v86, v98\n"
  "v_mfma_f32_16x16x32_bf16 v[0:3], v[56:59], a[48:51], v[0:3]\n"
  "ds_read_b128 v[68:71], v104 offset:5120\n"
  "v_fma_f32 v43, v43, v87, v99\n"
  "v_mfma_f32_16x16x32_bf16 v[0:3], v[60:63], a[72:75], v[0:3]\n"
  "ds_read_b64_tr_b16 v[92:93], v108 offset:0\n"
  "ds_read_b64_tr_b16 v[94:95], v108 offset:32\n"
  "v_mfma_f32_16x16x32_bf16 v[4:7], v[60:63], a[76:79], v[100:103]\n"
  "buffer_load_dwordx4 v113, s[76:79], s57 offen offset:1024  lds\n"
  "v_mfma_f32_16x16x32_bf16 v[4:7], v[56:59], a[52:55], v[4:7]\n"
  "v_fma_f32 v47, v47, v87, v99\n"
  "v_fma_f32 v24, v24, v84, v96\n"
  "v_mfma_f32_16x16x32_bf16 v[4:7], v[52:55], a[28:31], v[4:7]\n"
  "ds_read_b128 v[72:75], v104 offset:6144\n"
  "v_fma_f32 v25, v25, v85, v97\n"
  "v_mfma_f32_16x16x32_bf16 v[4:7], v[48:51], a[4:7], v[4:7]\n"
  "v_fma_f32 v28, v28, v84, v96\n"
  "v_fma_f32 v29, v29, v85, v97\n"
  "v_mfma_f32_16x16x32_bf16 v[8:11], v[48:51], a[8:11], v[100:103]\n"
  "v_fma_f32 v32, v32, v84, v96\n"
  "v_fma_f32 v33, v33, v85, v97\n"
  "v_mfma_f32_16x16x32_bf16 v[8:11], v[52:55], a[32:35], v[8:11]\n"
  "v_fma_f32 v36, v36, v84, v96\n"
  "v_fma_f32 v37, v37, v85, v97\n"
  "v_mfma_f32_16x16x32_bf16 v[8:11], v[56:59], a[56:59], v[8:11]\n"
  "s_add_u32 m0, s75, 1920\n"
  "ds_read_b128 v[76:79], v104 offset:7168\n"
  "v_fma_f32 v26, v26, v86, v98\n"
  "v_mfma_f32_16x16x32_bf16 v[8:11], v[60:63], a[80:83], v[8:11]\n"
  "buffer_load_dwordx4 v112, s[76:79], s57 offen offset:128  lds\n"
  "v_smfmac_f32_16x16x64_bf16 v[80:83], v[88:91], v[48:55], v124\n"
  "v_fma_f32 v27, v27, v87, v99\n"
  "v_smfmac_f32_16x16x64_bf16 v[80:83], v[88:91], v[56:63], v124\n"
  "s_waitcnt lgkmcnt(2)\n"
  "buffer_store_dwordx4 v[92:95], v109, s[48:51], s68 offen offset:0 nt\n"
  "v_fma_f32 v30, v30, v86, v98\n"
  "v_fma_f32 v31, v31, v87, v99\n"
  "v_mfma_f32_16x16x32_bf16 v[12:15], v[60:63], a[84:87], v[100:103]\n"
  "v_fma_f32 v34, v34, v86, v98\n"
  "v_fma_f32 v35, v35, v87, v99\n"
  "v_mfma_f32_16x16x32_bf16 v[12:15], v[56:59], a[60:63], v[12:15]\n"
  "v_fma_f32 v38, v38, v86, v98\n"
  "v_fma_f32 v39, v39, v87, v99\n"
  "v_mfma_f32_16x16x32_bf16 v[12:15], v[52:55], a[36:39], v[12:15]\n"
  "v_cvt_pk_bf16_f32 v40, v40, v44\n"
  "v_cvt_pk_bf16_f32 v41, v41, v45\n"
  "v_mfma_f32_16x16x32_bf16 v[12:15], v[48:51], a[12:15], v[12:15]\n"
  "v_cvt_pk_bf16_f32 v42, v42, v46\n"
  "v_cvt_pk_bf16_f32 v43, v43, v47\n"
  "v_mfma_f32_16x16x32_bf16 v[16:19], v[48:51], a[16:19], v[100:103]\n"
  "buffer_load_dwordx4 v113, s[76:79], s57 offen offset:1152  lds\n"
  "v_mfma_f32_16x16x32_bf16 v[16:19], v[52:55], a[40:43], v[16:19]\n"
  "v_cvt_pk_bf16_f32 v24, v24, v28\n"
  "v_cvt_pk_bf16_f32 v28, v25, v29\n"
  "v_mfma_f32_16x16x32_bf16 v[16:19], v[56:59], a[64:67], v[16:19]\n"
  "v_cvt_pk_bf16_f32 v25, v32, v36\n"
  "v_cvt_pk_bf16_f32 v29, v33, v37\n"
  "v_mfma_f32_16x16x32_bf16 v[16:19], v[60:63], a[88:91], v[16:19]\n"
  "v_cvt_pk_bf16_f32 v26, v26, v30\n"
  "v_cvt_pk_bf16_f32 v30, v27, v31\n"
  "v_mfma_f32_16x16x32_bf16 v[20:23], v[60:63], a[92:95], v[100:103]\n"
  "v_cvt_pk_bf16_f32 v27, v34, v38\n"
  "v_cvt_pk_bf16_f32 v31, v35, v39\n"
  "v_mfma_f32_16x16x32_bf16 v[20:23], v[56:59], a[68:71], v[20:23]\n"
  "v_xor_b32 v104, 0x4000, v104\n"
  "v_mfma_f32_16x16x32_bf16 v[20:23], v[52:55], a[44:47], v[20:23]\n"
  "s_add_u32 m0, s75, 3840\n"
  "v_mfma_f32_16x16x32_bf16 v[20:23], v[48:51], a[20:23], v[20:23]\n"
  "s_waitcnt lgkmcnt(0)\n"
  "buffer_load_dwordx4 v112, s[76:79], s57 offen offset:256  lds\n"
  "v_mfma_f32_16x16x32_bf16 v[20:23], v[64:67], a[116:119], v[20:23]\n"
  "s_add_u32 s66, s66, 6144\n"
  "ds_read_b128 v[48:51], v105 offset:0\n"
  "v_mfma_f32_16x16x32_bf16 v[20:23], v[68:71], a[140:143], v[20:23]\n"
  "s_cmp_ge_u32 s66, 86016\n"
  "s_cselect_b32 s64, 36864, 0\n"
  "v_mfma_f32_16x16x32_bf16 v[20:23], v[72:75], a[164:167], v[20:23]\n"
  "s_sub_u32 s66, s66, s64\n"
  "ds_read_b128 v[52:55], v105 offset:1024\n"
  "v_mfma_f32_16x16x32_bf16 v[20:23], v[76:79], a[188:191], v[20:23]\n"
  "s_sub_u32 s90, s90, 1\n"
  "s_cselect_b32 s63, s91, 6144\n"
  "s_cmov_b64 s[90:91], s[92:93]\n"
  "s_cmov_b64 s[92:93], s[94:95]\n"
  "ds_read_b64_tr_b16 v[96:97], v108 offset:2304\n"
  "ds_read_b64_tr_b16 v[98:99], v108 offset:2336\n"
  "v_mfma_f32_16x16x32_bf16 v[16:19], v[76:79], a[184:187], v[16:19]\n"
  "s_add_u32 s67, s67, s63\n"
  "v_mfma_f32_16x16x32_bf16 v[16:19], v[72:75], a[160:163], v[16:19]\n"
  "v_mfma_f32_16x16x32_bf16 v[16:19], v[68:71], a[136:139], v[16:19]\n"
  "buffer_load_dwordx4 v113, s[76:79], s57 offen offset:1280  lds\n"
  "ds_read_b128 v[56:59], v105 offset:2048\n"
  "v_mfma_f32_16x16x32_bf16 v[16:19], v[64:67], a[112:115], v[16:19]\n"
  "s_add_u32 s57, s57, 6144\n"
  "ds_write2_b32 v107, v40, v41 offset0:0 offset1:72\n"
  "v_mfma_f32_16x16x32_bf16 v[12:15], v[64:67], a[108:111], v[12:15]\n"
  "v_mfma_f32_16x16x32_bf16 v[12:15], v[68:71], a[132:135], v[12:15]\n"
  "v_mfma_f32_16x16x32_bf16 v[12:15], v[72:75], a[156:159], v[12:15]\n"
  "ds_read_b128 v[60:63], v105 offset:3072\n"
  "v_mfma_f32_16x16x32_bf16 v[12:15], v[76:79], a[180:183], v[12:15]\n"
  "ds_write2_b32 v107, v42, v43 offset0:144 offset1:216\n"
  "v_smfmac_f32_16x16x64_bf16 v[80:83], v[88:91], v[64:71], v124\n"
  "s_add_u32 m0, s75, 5888\n"
  "v_smfmac_f32_16x16x64_bf16 v[80:83], v[88:91], v[72:79], v124\n"
  "buffer_load_dwordx4 v112, s[76:79], s57 offen offset:256  lds\n"
  "v_mfma_f32_16x16x32_bf16 v[8:11], v[76:79], a[176:179], v[8:11]\n"
  "v_mfma_f32_16x16x32_bf16 v[8:11], v[72:75], a[152:155], v[8:11]\n"
  "v_mfma_f32_16x16x32_bf16 v[8:11], v[68:71], a[128:131], v[8:11]\n"
  "v_add_f32 v44, v80, v81\n"
  "v_mov_b32 v80, 0\n"
  "v_mfma_f32_16x16x32_bf16 v[8:11], v[64:67], a[104:107], v[8:11]\n"
  "s_waitcnt lgkmcnt(4)\n"
  "buffer_store_dwordx4 v[96:99], v109, s[48:51], s68 offen offset:128 nt\n"
  "ds_write_b64 v106, v[24:25] offset:0\n"
  "v_mfma_f32_16x16x32_bf16 v[4:7], v[64:67], a[100:103], v[4:7]\n"
  "v_mov_b32 v81, 0\n"
  "v_mfma_f32_16x16x32_bf16 v[4:7], v[68:71], a[124:127], v[4:7]\n"
  "ds_read_b64_tr_b16 v[92:93], v108 offset:4608\n"
  "ds_read_b64_tr_b16 v[94:95], v108 offset:4640\n"
  "v_mfma_f32_16x16x32_bf16 v[4:7], v[72:75], a[148:151], v[4:7]\n"
  "buffer_load_dwordx4 v113, s[76:79], s57 offen offset:1280  lds\n"
  "v_mfma_f32_16x16x32_bf16 v[4:7], v[76:79], a[172:175], v[4:7]\n"
  "ds_write_b64 v106, v[28:29] offset:288\n"
  "v_mfma_f32_16x16x32_bf16 v[0:3], v[76:79], a[168:171], v[0:3]\n"
  "v_xor_b32 v108, 0x2000, v108\n"
  "v_mfma_f32_16x16x32_bf16 v[0:3], v[72:75], a[144:147], v[0:3]\n"
  "v_mfma_f32_16x16x32_bf16 v[0:3], v[68:71], a[120:123], v[0:3]\n"
  "v_mfma_f32_16x16x32_bf16 v[0:3], v[64:67], a[96:99], v[0:3]\n"
  "s_waitcnt lgkmcnt(5)\n"
  "v_mfma_f32_16x16x32_bf16 v[0:3], v[48:51], a[192:195], v[0:3]\n"
  "s_add_u32 m0, s75, 8064\n"
  "ds_read_b128 v[64:67], v105 offset:4096\n"
  "v_mfma_f32_16x16x32_bf16 v[0:3], v[52:55], a[216:219], v[0:3]\n"
  "buffer_load_dwordx4 v112, s[76:79], s57 offen offset:128  lds\n"
  "v_mfma_f32_16x16x32_bf16 v[0:3], v[56:59], a[240:243], v[0:3]\n"
  "ds_read_b128 v[68:71], v105 offset:5120\n"
  "v_mfma_f32_16x16x32_bf16 v[0:3], v[60:63], v[136:139], v[0:3]\n"
  "s_waitcnt lgkmcnt(3)\n"
  "buffer_store_dwordx4 v[92:95], v109, s[48:51], s68 offen offset:256 nt\n"
  "ds_write_b64 v106, v[26:27] offset:576\n"
  "v_mfma_f32_16x16x32_bf16 v[4:7], v[60:63], v[140:143], v[4:7]\n"
  "s_add_u32 s68, s68, s69\n"
  "v_mfma_f32_16x16x32_bf16 v[4:7], v[56:59], a[244:247], v[4:7]\n"
  "s_mov_b32 s69, s70\n"
  "v_mfma_f32_16x16x32_bf16 v[4:7], v[52:55], a[220:223], v[4:7]\n"
  "s_movk_i32 s70, 6144\n"
  "ds_read_b128 v[72:75], v105 offset:6144\n"
  "v_mfma_f32_16x16x32_bf16 v[4:7], v[48:51], a[196:199], v[4:7]\n"
  "ds_write_b64 v106, v[30:31] offset:864\n"
  "v_mfma_f32_16x16x32_bf16 v[8:11], v[48:51], a[200:203], v[8:11]\n"
  "buffer_load_dwordx4 v113, s[76:79], s57 offen offset:1152  lds\n"
  "v_mfma_f32_16x16x32_bf16 v[8:11], v[52:55], a[224:227], v[8:11]\n"
  "v_mfma_f32_16x16x32_bf16 v[8:11], v[56:59], a[248:251], v[8:11]\n"
  "ds_read_b128 v[76:79], v105 offset:7168\n"
  "v_xor_b32 v105, 0x4000, v105\n"
  "v_mfma_f32_16x16x32_bf16 v[8:11], v[60:63], v[144:147], v[8:11]\n"
  "ds_write_b32 v110, v44\n"
  "v_mfma_f32_16x16x32_bf16 v[12:15], v[60:63], v[148:151], v[12:15]\n"
  "v_mfma_f32_16x16x32_bf16 v[12:15], v[56:59], a[252:255], v[12:15]\n"
  "v_mfma_f32_16x16x32_bf16 v[12:15], v[52:55], a[228:231], v[12:15]\n"
  "v_mfma_f32_16x16x32_bf16 v[12:15], v[48:51], a[204:207], v[12:15]\n"
  "v_mfma_f32_16x16x32_bf16 v[16:19], v[48:51], a[208:211], v[16:19]\n"
  "v_mfma_f32_16x16x32_bf16 v[16:19], v[52:55], a[232:235], v[16:19]\n"
  "v_mfma_f32_16x16x32_bf16 v[16:19], v[56:59], v[128:131], v[16:19]\n"
  "v_mfma_f32_16x16x32_bf16 v[16:19], v[60:63], v[152:155], v[16:19]\n"
  "v_xor_b32 v106, 0x2000, v106\n"
  "v_xor_b32 v107, 0x2000, v107\n"
  "v_mfma_f32_16x16x32_bf16 v[20:23], v[60:63], v[156:159], v[20:23]\n"
  "v_xor_b32 v110, 0x2000, v110\n"
  "v_mfma_f32_16x16x32_bf16 v[20:23], v[56:59], v[132:135], v[20:23]\n"
  "v_mfma_f32_16x16x32_bf16 v[20:23], v[52:55], a[236:239], v[20:23]\n"
  "v_mfma_f32_16x16x32_bf16 v[20:23], v[48:51], a[212:215], v[20:23]\n"
  "s_waitcnt vmcnt(29) lgkmcnt(0)\n"
  "s_barrier\n"
  "v_mfma_f32_16x16x32_bf16 v[20:23], v[64:67], v[180:183], v[20:23]\n"
  "s_add_u32 m0, s66, 0\n"
  "ds_read_b128 v[48:51], v104 offset:0\n"
  "ds_read_b128 v[84:87], v111\n"
  "v_mfma_f32_16x16x32_bf16 v[20:23], v[68:71], v[204:207], v[20:23]\n"
  "ds_read_b128 v[96:99], v111 offset:64\n"
  "v_xor_b32 v111, 0x2000, v111\n"
  "v_mfma_f32_16x16x32_bf16 v[20:23], v[72:75], v[228:231], v[20:23]\n"
  "ds_read_b128 v[52:55], v104 offset:1024\n"
  "v_mfma_f32_16x16x32_bf16 v[44:47], v[76:79], v[252:255], v[20:23]\n"
  "buffer_load_dwordx4 v112, s[44:47], s67 offen offset:0  lds\n"
  "v_mfma_f32_16x16x32_bf16 v[16:19], v[76:79], v[248:251], v[16:19]\n"
  "s_add_u32 m0, s66, 1920\n"
  "v_mfma_f32_16x16x32_bf16 v[16:19], v[72:75], v[224:227], v[16:19]\n"
  "v_mfma_f32_16x16x32_bf16 v[16:19], v[68:71], v[200:203], v[16:19]\n"
  "ds_read_b128 v[56:59], v104 offset:2048\n"
  "v_mfma_f32_16x16x32_bf16 v[40:43], v[64:67], v[176:179], v[16:19]\n"
  "buffer_load_dwordx4 v112, s[44:47], s67 offen offset:128  lds\n"
  "v_mfma_f32_16x16x32_bf16 v[12:15], v[64:67], v[172:175], v[12:15]\n"
  "s_add_u32 m0, s66, 3840\n"
  "v_mfma_f32_16x16x32_bf16 v[12:15], v[68:71], v[196:199], v[12:15]\n"
  "v_mfma_f32_16x16x32_bf16 v[12:15], v[72:75], v[220:223], v[12:15]\n"
  "s_waitcnt lgkmcnt(2)\n"
  "ds_read_b128 v[60:63], v104 offset:3072\n"
  "v_add_f32 v84, v84, v96\n"
  "v_mfma_f32_16x16x32_bf16 v[36:39], v[76:79], v[244:247], v[12:15]\n"
  "buffer_load_dwordx4 v112, s[44:47], s67 offen offset:256  lds\n"
  "v_mfma_f32_16x16x32_bf16 v[8:11], v[76:79], v[240:243], v[8:11]\n"
  "v_add_f32 v85, v85, v97\n"
  "v_add_f32 v86, v86, v98\n"
  "v_mfma_f32_16x16x32_bf16 v[8:11], v[72:75], v[216:219], v[8:11]\n"
  "v_add_f32 v87, v87, v99\n"
  "v_max3_f32 v82, v82, v84, v85\n"
  "v_mfma_f32_16x16x32_bf16 v[8:11], v[68:71], v[192:195], v[8:11]\n"
  "v_rcp_f32 v84, v84\n"
  "v_mfma_f32_16x16x32_bf16 v[32:35], v[64:67], v[168:171], v[8:11]\n"
  "v_rcp_f32 v85, v85\n"
  "v_mfma_f32_16x16x32_bf16 v[4:7], v[64:67], v[164:167], v[4:7]\n"
  "v_max3_f32 v82, v82, v86, v87\n"
  "v_max3_f32 v83, v83, v84, v85\n"
  "v_mfma_f32_16x16x32_bf16 v[4:7], v[68:71], v[188:191], v[4:7]\n"
  "v_rcp_f32 v86, v86\n"
  "v_mfma_f32_16x16x32_bf16 v[4:7], v[72:75], v[212:215], v[4:7]\n"
  "v_rcp_f32 v87, v87\n"
  "v_mfma_f32_16x16x32_bf16 v[28:31], v[76:79], v[236:239], v[4:7]\n"
  "v_max3_f32 v83, v83, v86, v87\n"
  "v_mul_f32 v96, s99, v84\n"
  "v_mfma_f32_16x16x32_bf16 v[0:3], v[76:79], v[232:235], v[0:3]\n"
  "s_add_u32 m0, s75, 10240\n"
  "v_mul_f32 v97, s99, v85\n"
  "v_mul_f32 v98, s99, v86\n"
  "v_mfma_f32_16x16x32_bf16 v[0:3], v[72:75], v[208:211], v[0:3]\n"
  "buffer_load_dwordx4 v112, s[76:79], s57 offen offset:0  lds\n"
  "v_mfma_f32_16x16x32_bf16 v[0:3], v[68:71], v[184:187], v[0:3]\n"
  "v_mul_f32 v99, s99, v87\n"
  "v_fma_f32 v40, v40, v84, v96\n"
  "v_mfma_f32_16x16x32_bf16 v[24:27], v[64:67], v[160:163], v[0:3]\n"
  "s_waitcnt lgkmcnt(0)\n"
  "v_fma_f32 v44, v44, v84, v96\n"
  "v_fma_f32 v41, v41, v85, v97\n"
  "v_mfma_f32_16x16x32_bf16 v[0:3], v[48:51], a[0:3], v[100:103]\n"
  "ds_read_b128 v[64:67], v104 offset:4096\n"
  "v_fma_f32 v45, v45, v85, v97\n"
  "v_mfma_f32_16x16x32_bf16 v[0:3], v[52:55], a[24:27], v[0:3]\n"
  "v_fma_f32 v42, v42, v86, v98\n"
  "v_fma_f32 v46, v46, v86, v98\n"
  "v_mfma_f32_16x16x32_bf16 v[0:3], v[56:59], a[48:51], v[0:3]\n"
  "ds_read_b128 v[68:71], v104 offset:5120\n"
  "v_fma_f32 v43, v43, v87, v99\n"
  "v_mfma_f32_16x16x32_bf16 v[0:3], v[60:63], a[72:75], v[0:3]\n"
  "buffer_load_dwordx4 v113, s[76:79], s57 offen offset:1024  lds\n"
  "v_mfma_f32_16x16x32_bf16 v[4:7], v[60:63], a[76:79], v[100:103]\n"
  "s_add_u32 s57, s57, 6144\n"
  "ds_read_b64_tr_b16 v[92:93], v108 offset:0\n"
  "ds_read_b64_tr_b16 v[94:95], v108 offset:32\n"
  "v_mfma_f32_16x16x32_bf16 v[4:7], v[56:59], a[52:55], v[4:7]\n"
  "v_fma_f32 v47, v47, v87, v99\n"
  "v_fma_f32 v24, v24, v84, v96\n"
  "v_mfma_f32_16x16x32_bf16 v[4:7], v[52:55], a[28:31], v[4:7]\n"
  "ds_read_b128 v[72:75], v104 offset:6144\n"
  "v_fma_f32 v25, v25, v85, v97\n"
  "v_mfma_f32_16x16x32_bf16 v[4:7], v[48:51], a[4:7], v[4:7]\n"
  "v_fma_f32 v28, v28, v84, v96\n"
  "v_fma_f32 v29, v29, v85, v97\n"
  "v_mfma_f32_16x16x32_bf16 v[8:11], v[48:51], a[8:11], v[100:103]\n"
  "s_add_u32 m0, s75, 12288\n"
  "v_fma_f32 v32, v32, v84, v96\n"
  "v_fma_f32 v33, v33, v85, v97\n"
  "v_mfma_f32_16x16x32_bf16 v[8:11], v[52:55], a[32:35], v[8:11]\n"
  "buffer_load_dwordx4 v112, s[76:79], s57 offen offset:0  lds\n"
  "v_mfma_f32_16x16x32_bf16 v[8:11], v[56:59], a[56:59], v[8:11]\n"
  "ds_read_b128 v[76:79], v104 offset:7168\n"
  "v_fma_f32 v36, v36, v84, v96\n"
  "v_mfma_f32_16x16x32_bf16 v[8:11], v[60:63], a[80:83], v[8:11]\n"
  "v_fma_f32 v37, v37, v85, v97\n"
  "v_fma_f32 v26, v26, v86, v98\n"
  "v_smfmac_f32_16x16x64_bf16 v[80:83], v[88:91], v[48:55], v124\n"
  "v_fma_f32 v27, v27, v87, v99\n"
  "v_smfmac_f32_16x16x64_bf16 v[80:83], v[88:91], v[56:63], v124\n"
  "s_waitcnt lgkmcnt(2)\n"
  "buffer_store_dwordx4 v[92:95], v109, s[48:51], s68 offen offset:0 nt\n"
  "v_fma_f32 v30, v30, v86, v98\n"
  "v_fma_f32 v31, v31, v87, v99\n"
  "v_mfma_f32_16x16x32_bf16 v[12:15], v[60:63], a[84:87], v[100:103]\n"
  "v_fma_f32 v34, v34, v86, v98\n"
  "v_fma_f32 v35, v35, v87, v99\n"
  "v_mfma_f32_16x16x32_bf16 v[12:15], v[56:59], a[60:63], v[12:15]\n"
  "buffer_load_dwordx4 v113, s[76:79], s57 offen offset:1024  lds\n"
  "v_mfma_f32_16x16x32_bf16 v[12:15], v[52:55], a[36:39], v[12:15]\n"
  "v_fma_f32 v38, v38, v86, v98\n"
  "v_fma_f32 v39, v39, v87, v99\n"
  "v_mfma_f32_16x16x32_bf16 v[12:15], v[48:51], a[12:15], v[12:15]\n"
  "v_cvt_pk_bf16_f32 v40, v40, v44\n"
  "v_cvt_pk_bf16_f32 v41, v41, v45\n"
  "v_mfma_f32_16x16x32_bf16 v[16:19], v[48:51], a[16:19], v[100:103]\n"
  "v_cvt_pk_bf16_f32 v42, v42, v46\n"
  "v_cvt_pk_bf16_f32 v43, v43, v47\n"
  "v_mfma_f32_16x16x32_bf16 v[16:19], v[52:55], a[40:43], v[16:19]\n"
  "v_cvt_pk_bf16_f32 v24, v24, v28\n"
  "v_cvt_pk_bf16_f32 v28, v25, v29\n"
  "v_mfma_f32_16x16x32_bf16 v[16:19], v[56:59], a[64:67], v[16:19]\n"
  "s_add_u32 m0, s75, 14208\n"
  "v_cvt_pk_bf16_f32 v25, v32, v36\n"
  "v_cvt_pk_bf16_f32 v29, v33, v37\n"
  "v_mfma_f32_16x16x32_bf16 v[16:19], v[60:63], a[88:91], v[16:19]\n"
  "buffer_load_dwordx4 v112, s[76:79], s57 offen offset:128  lds\n"
  "v_mfma_f32_16x16x32_bf16 v[20:23], v[60:63], a[92:95], v[100:103]\n"
  "v_cvt_pk_bf16_f32 v26, v26, v30\n"
  "v_cvt_pk_bf16_f32 v30, v27, v31\n"
  "v_mfma_f32_16x16x32_bf16 v[20:23], v[56:59], a[68:71], v[20:23]\n"
  "v_cvt_pk_bf16_f32 v27, v34, v38\n"
  "v_cvt_pk_bf16_f32 v31, v35, v39\n"
  "v_mfma_f32_16x16x32_bf16 v[20:23], v[52:55], a[44:47], v[20:23]\n"
  "v_xor_b32 v104, 0x4000, v104\n"
  "v_mfma_f32_16x16x32_bf16 v[20:23], v[48:51], a[20:23], v[20:23]\n"
  "s_waitcnt lgkmcnt(0)\n"
  "v_mfma_f32_16x16x32_bf16 v[20:23], v[64:67], a[116:119], v[20:23]\n"
  "s_add_u32 s66, s66, 6144\n"
  "ds_read_b128 v[48:51], v105 offset:0\n"
  "v_mfma_f32_16x16x32_bf16 v[20:23], v[68:71], a[140:143], v[20:23]\n"
  "s_cmp_ge_u32 s66, 86016\n"
  "s_cselect_b32 s64, 36864, 0\n"
  "buffer_load_dwordx4 v113, s[76:79], s57 offen offset:1152  lds\n"
  "v_mfma_f32_16x16x32_bf16 v[20:23], v[72:75], a[164:167], v[20:23]\n"
  "s_sub_u32 s66, s66, s64\n"
  "ds_read_b128 v[52:55], v105 offset:1024\n"
  "v_mfma_f32_16x16x32_bf16 v[20:23], v[76:79], a[188:191], v[20:23]\n"
  "s_sub_u32 s90, s90, 1\n"
  "s_cselect_b32 s63, s91, 6144\n"
  "s_cmov_b64 s[90:91], s[92:93]\n"
  "s_cmov_b64 s[92:93], s[94:95]\n"
  "ds_read_b64_tr_b16 v[96:97], v108 offset:2304\n"
  "ds_read_b64_tr_b16 v[98:99], v108 offset:2336\n"
  "v_mfma_f32_16x16x32_bf16 v[16:19], v[76:79], a[184:187], v[16:19]\n"
  "s_add_u32 s67, s67, s63\n"
  "v_mfma_f32_16x16x32_bf16 v[16:19], v[72:75], a[160:163], v[16:19]\n"
  "v_mfma_f32_16x16x32_bf16 v[16:19], v[68:71], a[136:139], v[16:19]\n"
  "s_add_u32 m0, s75, 16128\n"
  "ds_read_b128 v[56:59], v105 offset:2048\n"
  "v_mfma_f32_16x16x32_bf16 v[16:19], v[64:67], a[112:115], v[16:19]\n"
  "buffer_load_dwordx4 v112, s[76:79], s57 offen offset:256  lds\n"
  "v_mfma_f32_16x16x32_bf16 v[12:15], v[64:67], a[108:111], v[12:15]\n"
  "v_mfma_f32_16x16x32_bf16 v[12:15], v[68:71], a[132:135], v[12:15]\n"
  "v_mfma_f32_16x16x32_bf16 v[12:15], v[72:75], a[156:159], v[12:15]\n"
  "ds_read_b128 v[60:63], v105 offset:3072\n"
  "v_mfma_f32_16x16x32_bf16 v[12:15], v[76:79], a[180:183], v[12:15]\n"
  "ds_write2_b32 v107, v40, v41 offset0:0 offset1:72\n"
  "v_smfmac_f32_16x16x64_bf16 v[80:83], v[88:91], v[64:71], v124\n"
  "v_smfmac_f32_16x16x64_bf16 v[80:83], v[88:91], v[72:79], v124\n"
  "buffer_load_dwordx4 v113, s[76:79], s57 offen offset:1280  lds\n"
  "v_mfma_f32_16x16x32_bf16 v[8:11], v[76:79], a[176:179], v[8:11]\n"
  "s_sub_u32 s57, s57, 12288\n"
  "v_mfma_f32_16x16x32_bf16 v[8:11], v[72:75], a[152:155], v[8:11]\n"
  "v_mfma_f32_16x16x32_bf16 v[8:11], v[68:71], a[128:131], v[8:11]\n"
  "v_add_f32 v44, v80, v81\n"
  "v_mov_b32 v80, 0\n"
  "v_mfma_f32_16x16x32_bf16 v[8:11], v[64:67], a[104:107], v[8:11]\n"
  "s_waitcnt lgkmcnt(3)\n"
  "buffer_store_dwordx4 v[96:99], v109, s[48:51], s68 offen offset:128 nt\n"
  "ds_write2_b32 v107, v42, v43 offset0:144 offset1:216\n"
  "v_mfma_f32_16x16x32_bf16 v[4:7], v[64:67], a[100:103], v[4:7]\n"
  "s_add_u32 m0, s59, 0\n"
  "v_mov_b32 v81, 0\n"
  "v_mfma_f32_16x16x32_bf16 v[4:7], v[68:71], a[124:127], v[4:7]\n"
  "buffer_load_dwordx4 v114, s[40:43], s57 offen offset:0  lds\n"
  "v_mfma_f32_16x16x32_bf16 v[4:7], v[72:75], a[148:151], v[4:7]\n"
  "ds_read_b64_tr_b16 v[92:93], v108 offset:4608\n"
  "ds_read_b64_tr_b16 v[94:95], v108 offset:4640\n"
  "v_mfma_f32_16x16x32_bf16 v[4:7], v[76:79], a[172:175], v[4:7]\n"
  "ds_write_b64 v106, v[24:25] offset:0\n"
  "v_mfma_f32_16x16x32_bf16 v[0:3], v[76:79], a[168:171], v[0:3]\n"
  "v_xor_b32 v108, 0x2000, v108\n"
  "v_mfma_f32_16x16x32_bf16 v[0:3], v[72:75], a[144:147], v[0:3]\n"
  "v_mfma_f32_16x16x32_bf16 v[0:3], v[68:71], a[120:123], v[0:3]\n"
  "v_mfma_f32_16x16x32_bf16 v[0:3], v[64:67], a[96:99], v[0:3]\n"
  "s_waitcnt lgkmcnt(5)\n"
  "buffer_load_dwordx4 v115, s[40:43], s57 offen offset:1024  lds\n"
  "v_mfma_f32_16x16x32_bf16 v[0:3], v[48:51], a[192:195], v[0:3]\n"
  "ds_read_b128 v[64:67], v105 offset:4096\n"
  "v_mfma_f32_16x16x32_bf16 v[0:3], v[52:55], a[216:219], v[0:3]\n"
  "v_mfma_f32_16x16x32_bf16 v[0:3], v[56:59], a[240:243], v[0:3]\n"
  "ds_read_b128 v[68:71], v105 offset:5120\n"
  "v_mfma_f32_16x16x32_bf16 v[0:3], v[60:63], v[136:139], v[0:3]\n"
  "s_waitcnt lgkmcnt(3)\n"
  "buffer_store_dwordx4 v[92:95], v109, s[48:51], s68 offen offset:256 nt\n"
  "ds_write_b64 v106, v[28:29] offset:288\n"
  "v_mfma_f32_16x16x32_bf16 v[4:7], v[60:63], v[140:143], v[4:7]\n"
  "s_add_u32 s68, s68, s69\n"
  "v_mfma_f32_16x16x32_bf16 v[4:7], v[56:59], a[244:247], v[4:7]\n"
  "s_mov_b32 s69, s70\n"
  "buffer_load_dwordx4 v116, s[40:43], s57 offen offset:2048  lds\n"
  "v_mfma_f32_16x16x32_bf16 v[4:7], v[52:55], a[220:223], v[4:7]\n"
  "s_add_u32 s57, s57, 6144\n"
  "ds_read_b128 v[72:75], v105 offset:6144\n"
  "v_mfma_f32_16x16x32_bf16 v[4:7], v[48:51], a[196:199], v[4:7]\n"
  "ds_write_b64 v106, v[26:27] offset:576\n"
  "v_mfma_f32_16x16x32_bf16 v[8:11], v[48:51], a[200:203], v[8:11]\n"
  "s_movk_i32 s70, 6144\n"
  "v_mfma_f32_16x16x32_bf16 v[8:11], v[52:55], a[224:227], v[8:11]\n"
  "v_mfma_f32_16x16x32_bf16 v[8:11], v[56:59], a[248:251], v[8:11]\n"
  "ds_read_b128 v[76:79], v105 offset:7168\n"
  "v_xor_b32 v105, 0x4000, v105\n"
  "v_mfma_f32_16x16x32_bf16 v[8:11], v[60:63], v[144:147], v[8:11]\n"
  "ds_write_b64 v106, v[30:31] offset:864\n"
  "v_mfma_f32_16x16x32_bf16 v[12:15], v[60:63], v[148:151], v[12:15]\n"
  "v_mfma_f32_16x16x32_bf16 v[12:15], v[56:59], a[252:255], v[12:15]\n"
  "v_mfma_f32_16x16x32_bf16 v[12:15], v[52:55], a[228:231], v[12:15]\n"
  "v_mfma_f32_16x16x32_bf16 v[12:15], v[48:51], a[204:207], v[12:15]\n"
  "ds_write_b32 v110, v44\n"
  "v_mfma_f32_16x16x32_bf16 v[16:19], v[48:51], a[208:211], v[16:19]\n"
  "v_mfma_f32_16x16x32_bf16 v[16:19], v[52:55], a[232:235], v[16:19]\n"
  "v_mfma_f32_16x16x32_bf16 v[16:19], v[56:59], v[128:131], v[16:19]\n"
  "v_mfma_f32_16x16x32_bf16 v[16:19], v[60:63], v[152:155], v[16:19]\n"
  "v_xor_b32 v106, 0x2000, v106\n"
  "v_xor_b32 v107, 0x2000, v107\n"
  "v_mfma_f32_16x16x32_bf16 v[20:23], v[60:63], v[156:159], v[20:23]\n"
  "v_xor_b32 v110, 0x2000, v110\n"
  "v_mfma_f32_16x16x32_bf16 v[20:23], v[56:59], v[132:135], v[20:23]\n"
  "v_mfma_f32_16x16x32_bf16 v[20:23], v[52:55], a[236:239], v[20:23]\n"
  "v_mfma_f32_16x16x32_bf16 v[20:23], v[48:51], a[212:215], v[20:23]\n"
  "s_waitcnt vmcnt(43) lgkmcnt(0)\n"
  "s_barrier\n"
  "v_mfma_f32_16x16x32_bf16 v[20:23], v[64:67], v[180:183], v[20:23]\n"
  "s_add_u32 m0, s66, 0\n"
  "ds_read_b128 v[48:51], v104 offset:0\n"
  "ds_read_b128 v[84:87], v111\n"
  "v_mfma_f32_16x16x32_bf16 v[20:23], v[68:71], v[204:207], v[20:23]\n"
  "ds_read_b128 v[96:99], v111 offset:64\n"
  "v_xor_b32 v111, 0x2000, v111\n"
  "v_mfma_f32_16x16x32_bf16 v[20:23], v[72:75], v[228:231], v[20:23]\n"
  "ds_read_b128 v[52:55], v104 offset:1024\n"
  "v_mfma_f32_16x16x32_bf16 v[44:47], v[76:79], v[252:255], v[20:23]\n"
  "buffer_load_dwordx4 v112, s[44:47], s67 offen offset:0  lds\n"
  "v_mfma_f32_16x16x32_bf16 v[16:19], v[76:79], v[248:251], v[16:19]\n"
  "s_add_u32 m0, s66, 1920\n"
  "v_mfma_f32_16x16x32_bf16 v[16:19], v[72:75], v[224:227], v[16:19]\n"
  "v_mfma_f32_16x16x32_bf16 v[16:19], v[68:71], v[200:203], v[16:19]\n"
  "ds_read_b128 v[56:59], v104 offset:2048\n"
  "v_mfma_f32_16x16x32_bf16 v[40:43], v[64:67], v[176:179], v[16:19]\n"
  "buffer_load_dwordx4 v112, s[44:47], s67 offen offset:128  lds\n"
  "v_mfma_f32_16x16x32_bf16 v[12:15], v[64:67], v[172:175], v[12:15]\n"
  "s_add_u32 m0, s66, 3840\n"
  "v_mfma_f32_16x16x32_bf16 v[12:15], v[68:71], v[196:199], v[12:15]\n"
  "v_mfma_f32_16x16x32_bf16 v[12:15], v[72:75], v[220:223], v[12:15]\n"
  "s_waitcnt lgkmcnt(2)\n"
  "ds_read_b128 v[60:63], v104 offset:3072\n"
  "v_add_f32 v84, v84, v96\n"
  "v_mfma_f32_16x16x32_bf16 v[36:39], v[76:79], v[244:247], v[12:15]\n"
  "buffer_load_dwordx4 v112, s[44:47], s67 offen offset:256  lds\n"
  "v_mfma_f32_16x16x32_bf16 v[8:11], v[76:79], v[240:243], v[8:11]\n"
  "v_add_f32 v85, v85, v97\n"
  "v_add_f32 v86, v86, v98\n"
  "v_mfma_f32_16x16x32_bf16 v[8:11], v[72:75], v[216:219], v[8:11]\n"
  "v_add_f32 v87, v87, v99\n"
  "v_max3_f32 v82, v82, v84, v85\n"
  "v_mfma_f32_16x16x32_bf16 v[8:11], v[68:71], v[192:195], v[8:11]\n"
  "v_rcp_f32 v84, v84\n"
  "v_mfma_f32_16x16x32_bf16 v[32:35], v[64:67], v[168:171], v[8:11]\n"
  "v_rcp_f32 v85, v85\n"
  "v_mfma_f32_16x16x32_bf16 v[4:7], v[64:67], v[164:167], v[4:7]\n"
  "v_max3_f32 v82, v82, v86, v87\n"
  "v_max3_f32 v83, v83, v84, v85\n"
  "v_mfma_f32_16x16x32_bf16 v[4:7], v[68:71], v[188:191], v[4:7]\n"
  "v_rcp_f32 v86, v86\n"
  "v_mfma_f32_16x16x32_bf16 v[4:7], v[72:75], v[212:215], v[4:7]\n"
  "v_rcp_f32 v87, v87\n"
  "v_mfma_f32_16x16x32_bf16 v[28:31], v[76:79], v[236:239], v[4:7]\n"
  "v_max3_f32 v83, v83, v86, v87\n"
  "v_mul_f32 v96, s99, v84\n"
  "v_mfma_f32_16x16x32_bf16 v[0:3], v[76:79], v[232:235], v[0:3]\n"
  "s_add_u32 m0, s59, 3072\n"
  "v_mul_f32 v97, s99, v85\n"
  "v_mul_f32 v98, s99, v86\n"
  "v_mfma_f32_16x16x32_bf16 v[0:3], v[72:75], v[208:211], v[0:3]\n"
  "buffer_load_dwordx4 v114, s[40:43], s57 offen offset:0  lds\n"
  "v_mfma_f32_16x16x32_bf16 v[0:3], v[68:71], v[184:187], v[0:3]\n"
  "v_mul_f32 v99, s99, v87\n"
  "v_fma_f32 v40, v40, v84, v96\n"
  "v_mfma_f32_16x16x32_bf16 v[24:27], v[64:67], v[160:163], v[0:3]\n"
  "s_waitcnt lgkmcnt(0)\n"
  "v_fma_f32 v44, v44, v84, v96\n"
  "v_fma_f32 v41, v41, v85, v97\n"
  "v_mfma_f32_16x16x32_bf16 v[0:3], v[48:51], a[0:3], v[100:103]\n"
  "ds_read_b128 v[64:67], v104 offset:4096\n"
  "v_fma_f32 v45, v45, v85, v97\n"
  "v_mfma_f32_16x16x32_bf16 v[0:3], v[52:55], a[24:27], v[0:3]\n"
  "v_fma_f32 v42, v42, v86, v98\n"
  "v_fma_f32 v46, v46, v86, v98\n"
  "v_mfma_f32_16x16x32_bf16 v[0:3], v[56:59], a[48:51], v[0:3]\n"
  "ds_read_b128 v[68:71], v104 offset:5120\n"
  "v_fma_f32 v43, v43, v87, v99\n"
  "v_mfma_f32_16x16x32_bf16 v[0:3], v[60:63], a[72:75], v[0:3]\n"
  "ds_read_b64_tr_b16 v[92:93], v108 offset:0\n"
  "ds_read_b64_tr_b16 v[94:95], v108 offset:32\n"
  "v_mfma_f32_16x16x32_bf16 v[4:7], v[60:63], a[76:79], v[100:103]\n"
  "v_fma_f32 v47, v47, v87, v99\n"
  "v_fma_f32 v24, v24, v84, v96\n"
  "v_mfma_f32_16x16x32_bf16 v[4:7], v[56:59], a[52:55], v[4:7]\n"
  "buffer_load_dwordx4 v115, s[40:43], s57 offen offset:1024  lds\n"
  "v_mfma_f32_16x16x32_bf16 v[4:7], v[52:55], a[28:31], v[4:7]\n"
  "ds_read_b128 v[72:75], v104 offset:6144\n"
  "v_fma_f32 v25, v25, v85, v97\n"
  "v_mfma_f32_16x16x32_bf16 v[4:7], v[48:51], a[4:7], v[4:7]\n"
  "v_fma_f32 v28, v28, v84, v96\n"
  "v_fma_f32 v29, v29, v85, v97\n"
  "v_mfma_f32_16x16x32_bf16 v[8:11], v[48:51], a[8:11], v[100:103]\n"
  "v_fma_f32 v32, v32, v84, v96\n"
  "v_fma_f32 v33, v33, v85, v97\n"
  "v_mfma_f32_16x16x32_bf16 v[8:11], v[52:55], a[32:35], v[8:11]\n"
  "v_fma_f32 v36, v36, v84, v96\n"
  "v_fma_f32 v37, v37, v85, v97\n"
  "v_mfma_f32_16x16x32_bf16 v[8:11], v[56:59], a[56:59], v[8:11]\n"
  "ds_read_b128 v[76:79], v104 offset:7168\n"
  "v_fma_f32 v26, v26, v86, v98\n"
  "v_mfma_f32_16x16x32_bf16 v[8:11], v[60:63], a[80:83], v[8:11]\n"
  "v_fma_f32 v27, v27, v87, v99\n"
  "v_fma_f32 v30, v30, v86, v98\n"
  "v_smfmac_f32_16x16x64_bf16 v[80:83], v[88:91], v[48:55], v124\n"
  "v_fma_f32 v31, v31, v87, v99\n"
  "v_smfmac_f32_16x16x64_bf16 v[80:83], v[88:91], v[56:63], v124\n"
  "buffer_load_dwordx4 v116, s[40:43], s57 offen offset:2048  lds\n"
  "v_mfma_f32_16x16x32_bf16 v[12:15], v[60:63], a[84:87], v[100:103]\n"
  "s_add_u32 s57, s57, 6144\n"
  "v_fma_f32 v34, v34, v86, v98\n"
  "v_fma_f32 v35, v35, v87, v99\n"
  "v_mfma_f32_16x16x32_bf16 v[12:15], v[56:59], a[60:63], v[12:15]\n"
  "v_fma_f32 v38, v38, v86, v98\n"
  "v_fma_f32 v39, v39, v87, v99\n"
  "v_mfma_f32_16x16x32_bf16 v[12:15], v[52:55], a[36:39], v[12:15]\n"
  "v_cvt_pk_bf16_f32 v40, v40, v44\n"
  "v_cvt_pk_bf16_f32 v41, v41, v45\n"
  "v_mfma_f32_16x16x32_bf16 v[12:15], v[48:51], a[12:15], v[12:15]\n"
  "s_waitcnt lgkmcnt(2)\n"
  "buffer_store_dwordx4 v[92:95], v109, s[48:51], s68 offen offset:0 nt\n"
  "ds_write2_b32 v107, v40, v41 offset0:0 offset1:72\n"
  "v_mfma_f32_16x16x32_bf16 v[16:19], v[48:51], a[16:19], v[100:103]\n"
  "v_cvt_pk_bf16_f32 v42, v42, v46\n"
  "v_cvt_pk_bf16_f32 v43, v43, v47\n"
  "v_mfma_f32_16x16x32_bf16 v[16:19], v[52:55], a[40:43], v[16:19]\n"
  "v_cvt_pk_bf16_f32 v24, v24, v28\n"
  "v_cvt_pk_bf16_f32 v28, v25, v29\n"
  "v_mfma_f32_16x16x32_bf16 v[16:19], v[56:59], a[64:67], v[16:19]\n"
  "s_add_u32 m0, s59, 6144\n"
  "v_cvt_pk_bf16_f32 v25, v32, v36\n"
  "v_cvt_pk_bf16_f32 v29, v33, v37\n"
  "v_mfma_f32_16x16x32_bf16 v[16:19], v[60:63], a[88:91], v[16:19]\n"
  "buffer_load_dwordx4 v114, s[40:43], s57 offen offset:0  lds\n"
  "v_mfma_f32_16x16x32_bf16 v[20:23], v[60:63], a[92:95], v[100:103]\n"
  "v_cvt_pk_bf16_f32 v26, v26, v30\n"
  "v_cvt_pk_bf16_f32 v30, v27, v31\n"
  "v_mfma_f32_16x16x32_bf16 v[20:23], v[56:59], a[68:71], v[20:23]\n"
  "v_cvt_pk_bf16_f32 v27, v34, v38\n"
  "v_cvt_pk_bf16_f32 v31, v35, v39\n"
  "v_mfma_f32_16x16x32_bf16 v[20:23], v[52:55], a[44:47], v[20:23]\n"
  "v_xor_b32 v104, 0x4000, v104\n"
  "v_mfma_f32_16x16x32_bf16 v[20:23], v[48:51], a[20:23], v[20:23]\n"
  "s_waitcnt lgkmcnt(1)\n"
  "v_mfma_f32_16x16x32_bf16 v[20:23], v[64:67], a[116:119], v[20:23]\n"
  "s_add_u32 s66, s66, 6144\n"
  "ds_read_b128 v[48:51], v105 offset:0\n"
  "v_mfma_f32_16x16x32_bf16 v[20:23], v[68:71], a[140:143], v[20:23]\n"
  "s_cmp_ge_u32 s66, 86016\n"
  "s_cselect_b32 s64, 36864, 0\n"
  "v_mfma_f32_16x16x32_bf16 v[20:23], v[72:75], a[164:167], v[20:23]\n"
  "s_sub_u32 s66, s66, s64\n"
  "ds_read_b128 v[52:55], v105 offset:1024\n"
  "v_mfma_f32_16x16x32_bf16 v[20:23], v[76:79], a[188:191], v[20:23]\n"
  "s_sub_u32 s90, s90, 1\n"
  "s_cselect_b32 s63, s91, 6144\n"
  "s_cmov_b64 s[90:91], s[92:93]\n"
  "s_cmov_b64 s[92:93], s[94:95]\n"
  "buffer_load_dwordx4 v115, s[40:43], s57 offen offset:1024  lds\n"
  "v_mfma_f32_16x16x32_bf16 v[16:19], v[76:79], a[184:187], v[16:19]\n"
  "s_add_u32 s67, s67, s63\n"
  "ds_read_b64_tr_b16 v[96:97], v108 offset:2304\n"
  "ds_read_b64_tr_b16 v[98:99], v108 offset:2336\n"
  "v_mfma_f32_16x16x32_bf16 v[16:19], v[72:75], a[160:163], v[16:19]\n"
  "v_mfma_f32_16x16x32_bf16 v[16:19], v[68:71], a[136:139], v[16:19]\n"
  "ds_read_b128 v[56:59], v105 offset:2048\n"
  "v_mfma_f32_16x16x32_bf16 v[16:19], v[64:67], a[112:115], v[16:19]\n"
  "ds_write2_b32 v107, v42, v43 offset0:144 offset1:216\n"
  "v_mfma_f32_16x16x32_bf16 v[12:15], v[64:67], a[108:111], v[12:15]\n"
  "v_mfma_f32_16x16x32_bf16 v[12:15], v[68:71], a[132:135], v[12:15]\n"
  "v_mfma_f32_16x16x32_bf16 v[12:15], v[72:75], a[156:159], v[12:15]\n"
  "ds_read_b128 v[60:63], v105 offset:3072\n"
  "v_mfma_f32_16x16x32_bf16 v[12:15], v[76:79], a[180:183], v[12:15]\n"
  "buffer_load_dwordx4 v116, s[40:43], s57 offen offset:2048  lds\n"
  "v_smfmac_f32_16x16x64_bf16 v[80:83], v[88:91], v[64:71], v124\n"
  "s_add_u32 s57, s57, 6144\n"
  "v_smfmac_f32_16x16x64_bf16 v[80:83], v[88:91], v[72:79], v124\n"
  "s_waitcnt lgkmcnt(3)\n"
  "buffer_store_dwordx4 v[96:99], v109, s[48:51], s68 offen offset:128 nt\n"
  "ds_write_b64 v106, v[24:25] offset:0\n"
  "v_mfma_f32_16x16x32_bf16 v[8:11], v[76:79], a[176:179], v[8:11]\n"
  "v_mfma_f32_16x16x32_bf16 v[8:11], v[72:75], a[152:155], v[8:11]\n"
  "v_mfma_f32_16x16x32_bf16 v[8:11], v[68:71], a[128:131], v[8:11]\n"
  "v_add_f32 v44, v80, v81\n"
  "v_mov_b32 v80, 0\n"
  "v_mfma_f32_16x16x32_bf16 v[8:11], v[64:67], a[104:107], v[8:11]\n"
  "ds_write_b64 v106, v[28:29] offset:288\n"
  "v_mfma_f32_16x16x32_bf16 v[4:7], v[64:67], a[100:103], v[4:7]\n"
  "s_add_u32 m0, s59, 9216\n"
  "v_mov_b32 v81, 0\n"
  "v_mfma_f32_16x16x32_bf16 v[4:7], v[68:71], a[124:127], v[4:7]\n"
  "buffer_load_dwordx4 v114, s[40:43], s57 offen offset:0  lds\n"
  "v_mfma_f32_16x16x32_bf16 v[4:7], v[72:75], a[148:151], v[4:7]\n"
  "ds_read_b64_tr_b16 v[92:93], v108 offset:4608\n"
  "ds_read_b64_tr_b16 v[94:95], v108 offset:4640\n"
  "v_mfma_f32_16x16x32_bf16 v[4:7], v[76:79], a[172:175], v[4:7]\n"
  "ds_write_b64 v106, v[26:27] offset:576\n"
  "v_mfma_f32_16x16x32_bf16 v[0:3], v[76:79], a[168:171], v[0:3]\n"
  "v_xor_b32 v108, 0x2000, v108\n"
  "v_mfma_f32_16x16x32_bf16 v[0:3], v[72:75], a[144:147], v[0:3]\n"
  "v_mfma_f32_16x16x32_bf16 v[0:3], v[68:71], a[120:123], v[0:3]\n"
  "v_mfma_f32_16x16x32_bf16 v[0:3], v[64:67], a[96:99], v[0:3]\n"
  "s_waitcnt lgkmcnt(5)\n"
  "v_mfma_f32_16x16x32_bf16 v[0:3], v[48:51], a[192:195], v[0:3]\n"
  "ds_read_b128 v[64:67], v105 offset:4096\n"
  "v_mfma_f32_16x16x32_bf16 v[0:3], v[52:55], a[216:219], v[0:3]\n"
  "buffer_load_dwordx4 v115, s[40:43], s57 offen offset:1024  lds\n"
  "v_mfma_f32_16x16x32_bf16 v[0:3], v[56:59], a[240:243], v[0:3]\n"
  "ds_read_b128 v[68:71], v105 offset:5120\n"
  "v_mfma_f32_16x16x32_bf16 v[0:3], v[60:63], v[136:139], v[0:3]\n"
  "s_waitcnt lgkmcnt(3)\n"
  "buffer_store_dwordx4 v[92:95], v109, s[48:51], s68 offen offset:256 nt\n"
  "ds_write_b64 v106, v[30:31] offset:864\n"
  "v_mfma_f32_16x16x32_bf16 v[4:7], v[60:63], v[140:143], v[4:7]\n"
  "s_add_u32 s68, s68, s69\n"
  "v_mfma_f32_16x16x32_bf16 v[4:7], v[56:59], a[244:247], v[4:7]\n"
  "s_mov_b32 s69, s70\n"
  "v_mfma_f32_16x16x32_bf16 v[4:7], v[52:55], a[220:223], v[4:7]\n"
  "s_movk_i32 s70, 6144\n"
  "ds_read_b128 v[72:75], v105 offset:6144\n"
  "v_mfma_f32_16x16x32_bf16 v[4:7], v[48:51], a[196:199], v[4:7]\n"
  "ds_write_b32 v110, v44\n"
  "v_mfma_f32_16x16x32_bf16 v[8:11], v[48:51], a[200:203], v[8:11]\n"
  "v_mfma_f32_16x16x32_bf16 v[8:11], v[52:55], a[224:227], v[8:11]\n"
  "buffer_load_dwordx4 v116, s[40:43], s57 offen offset:2048  lds\n"
  "v_mfma_f32_16x16x32_bf16 v[8:11], v[56:59], a[248:251], v[8:11]\n"
  "s_add_u32 s57, s57, 6144\n"
  "ds_read_b128 v[76:79], v105 offset:7168\n"
  "v_xor_b32 v105, 0x4000, v105\n"
  "v_mfma_f32_16x16x32_bf16 v[8:11], v[60:63], v[144:147], v[8:11]\n"
  "v_mfma_f32_16x16x32_bf16 v[12:15], v[60:63], v[148:151], v[12:15]\n"
  "v_mfma_f32_16x16x32_bf16 v[12:15], v[56:59], a[252:255], v[12:15]\n"
  "v_mfma_f32_16x16x32_bf16 v[12:15], v[52:55], a[228:231], v[12:15]\n"
  "v_mfma_f32_16x16x32_bf16 v[12:15], v[48:51], a[204:207], v[12:15]\n"
  "v_mfma_f32_16x16x32_bf16 v[16:19], v[48:51], a[208:211], v[16:19]\n"
  "v_mfma_f32_16x16x32_bf16 v[16:19], v[52:55], a[232:235], v[16:19]\n"
  "v_mfma_f32_16x16x32_bf16 v[16:19], v[56:59], v[128:131], v[16:19]\n"
  "v_mfma_f32_16x16x32_bf16 v[16:19], v[60:63], v[152:155], v[16:19]\n"
  "v_xor_b32 v106, 0x2000, v106\n"
  "v_xor_b32 v107, 0x2000, v107\n"
  "v_mfma_f32_16x16x32_bf16 v[20:23], v[60:63], v[156:159], v[20:23]\n"
  "v_xor_b32 v110, 0x2000, v110\n"
  "v_mfma_f32_16x16x32_bf16 v[20:23], v[56:59], v[132:135], v[20:23]\n"
  "v_mfma_f32_16x16x32_bf16 v[20:23], v[52:55], a[236:239], v[20:23]\n"
  "v_mfma_f32_16x16x32_bf16 v[20:23], v[48:51], a[212:215], v[20:23]\n"
  "s_waitcnt vmcnt(20) lgkmcnt(0)\n"
  "s_barrier\n"
  "v_mfma_f32_16x16x32_bf16 v[20:23], v[64:67], v[180:183], v[20:23]\n"
  "s_add_u32 m0, s66, 0\n"
  "ds_read_b128 v[48:51], v104 offset:0\n"
  "ds_read_b128 v[84:87], v111\n"
  "v_mfma_f32_16x16x32_bf16 v[20:23], v[68:71], v[204:207], v[20:23]\n"
  "ds_read_b128 v[96:99], v111 offset:64\n"
  "v_xor_b32 v111, 0x2000, v111\n"
  "v_mfma_f32_16x16x32_bf16 v[20:23], v[72:75], v[228:231], v[20:23]\n"
  "ds_read_b128 v[52:55], v104 offset:1024\n"
  "v_mfma_f32_16x16x32_bf16 v[44:47], v[76:79], v[252:255], v[20:23]\n"
  "buffer_load_dwordx4 v112, s[44:47], s67 offen offset:0  lds\n"
  "v_mfma_f32_16x16x32_bf16 v[16:19], v[76:79], v[248:251], v[16:19]\n"
  "s_add_u32 m0, s66, 1920\n"
  "v_mfma_f32_16x16x32_bf16 v[16:19], v[72:75], v[224:227], v[16:19]\n"
  "v_mfma_f32_16x16x32_bf16 v[16:19], v[68:71], v[200:203], v[16:19]\n"
  "ds_read_b128 v[56:59], v104 offset:2048\n"
  "v_mfma_f32_16x16x32_bf16 v[40:43], v[64:67], v[176:179], v[16:19]\n"
  "buffer_load_dwordx4 v112, s[44:47], s67 offen offset:128  lds\n"
  "v_mfma_f32_16x16x32_bf16 v[12:15], v[64:67], v[172:175], v[12:15]\n"
  "s_add_u32 m0, s66, 3840\n"
  "v_mfma_f32_16x16x32_bf16 v[12:15], v[68:71], v[196:199], v[12:15]\n"
  "v_mfma_f32_16x16x32_bf16 v[12:15], v[72:75], v[220:223], v[12:15]\n"
  "s_waitcnt lgkmcnt(2)\n"
  "ds_read_b128 v[60:63], v104 offset:3072\n"
  "v_add_f32 v84, v84, v96\n"
  "v_mfma_f32_16x16x32_bf16 v[36:39], v[76:79], v[244:247], v[12:15]\n"
  "buffer_load_dwordx4 v112, s[44:47], s67 offen offset:256  lds\n"
  "v_mfma_f32_16x16x32_bf16 v[8:11], v[76:79], v[240:243], v[8:11]\n"
  "v_add_f32 v85, v85, v97\n"
  "v_add_f32 v86, v86, v98\n"
  "v_mfma_f32_16x16x32_bf16 v[8:11], v[72:75], v[216:219], v[8:11]\n"
  "v_add_f32 v87, v87, v99\n"
  "v_max3_f32 v82, v82, v84, v85\n"
  "v_mfma_f32_16x16x32_bf16 v[8:11], v[68:71], v[192:195], v[8:11]\n"
  "s_add_u32 m0, s59, 12288\n"
  "v_rcp_f32 v84, v84\n"
  "v_mfma_f32_16x16x32_bf16 v[32:35], v[64:67], v[168:171], v[8:11]\n"
  "buffer_load_dwordx4 v114, s[40:43], s57 offen offset:0  lds\n"
  "v_mfma_f32_16x16x32_bf16 v[4:7], v[64:67], v[164:167], v[4:7]\n"
  "v_rcp_f32 v85, v85\n"
  "v_mfma_f32_16x16x32_bf16 v[4:7], v[68:71], v[188:191], v[4:7]\n"
  "v_max3_f32 v82, v82, v86, v87\n"
  "v_max3_f32 v83, v83, v84, v85\n"
  "v_mfma_f32_16x16x32_bf16 v[4:7], v[72:75], v[212:215], v[4:7]\n"
  "v_rcp_f32 v86, v86\n"
  "v_mfma_f32_16x16x32_bf16 v[28:31], v[76:79], v[236:239], v[4:7]\n"
  "buffer_load_dwordx4 v115, s[40:43], s57 offen offset:1024  lds\n"
  "v_mfma_f32_16x16x32_bf16 v[0:3], v[76:79], v[232:235], v[0:3]\n"
  "v_rcp_f32 v87, v87\n"
  "v_mfma_f32_16x16x32_bf16 v[0:3], v[72:75], v[208:211], v[0:3]\n"
  "v_max3_f32 v83, v83, v86, v87\n"
  "v_mul_f32 v96, s99, v84\n"
  "v_mfma_f32_16x16x32_bf16 v[0:3], v[68:71], v[184:187], v[0:3]\n"
  "v_mul_f32 v97, s99, v85\n"
  "v_mul_f32 v98, s99, v86\n"
  "v_mfma_f32_16x16x32_bf16 v[24:27], v[64:67], v[160:163], v[0:3]\n"
  "s_waitcnt lgkmcnt(0)\n"
  "buffer_load_dwordx4 v116, s[40:43], s57 offen offset:2048  lds\n"
  "v_mfma_f32_16x16x32_bf16 v[0:3], v[48:51], a[0:3], v[100:103]\n"
  "s_add_u32 s57, s57, 6144\n"
  "ds_read_b128 v[64:67], v104 offset:4096\n"
  "v_mul_f32 v99, s99, v87\n"
  "v_mfma_f32_16x16x32_bf16 v[0:3], v[52:55], a[24:27], v[0:3]\n"
  "v_fma_f32 v40, v40, v84, v96\n"
  "v_fma_f32 v44, v44, v84, v96\n"
  "v_mfma_f32_16x16x32_bf16 v[0:3], v[56:59], a[48:51], v[0:3]\n"
  "s_add_u32 m0, s59, 15360\n"
  "ds_read_b128 v[68:71], v104 offset:5120\n"
  "v_fma_f32 v41, v41, v85, v97\n"
  "v_mfma_f32_16x16x32_bf16 v[0:3], v[60:63], a[72:75], v[0:3]\n"
  "buffer_load_dwordx4 v114, s[40:43], s57 offen offset:0  lds\n"
  "v_mfma_f32_16x16x32_bf16 v[4:7], v[60:63], a[76:79], v[100:103]\n"
  "ds_read_b64_tr_b16 v[92:93], v108 offset:0\n"
  "ds_read_b64_tr_b16 v[94:95], v108 offset:32\n"
  "v_mfma_f32_16x16x32_bf16 v[4:7], v[56:59], a[52:55], v[4:7]\n"
  "v_fma_f32 v45, v45, v85, v97\n"
  "v_fma_f32 v42, v42, v86, v98\n"
  "v_mfma_f32_16x16x32_bf16 v[4:7], v[52:55], a[28:31], v[4:7]\n"
  "ds_read_b128 v[72:75], v104 offset:6144\n"
  "v_fma_f32 v46, v46, v86, v98\n"
  "v_mfma_f32_16x16x32_bf16 v[4:7], v[48:51], a[4:7], v[4:7]\n"
  "buffer_load_dwordx4 v115, s[40:43], s57 offen offset:1024  lds\n"
  "v_mfma_f32_16x16x32_bf16 v[8:11], v[48:51], a[8:11], v[100:103]\n"
  "v_fma_f32 v43, v43, v87, v99\n"
  "v_fma_f32 v47, v47, v87, v99\n"
  "v_mfma_f32_16x16x32_bf16 v[8:11], v[52:55], a[32:35], v[8:11]\n"
  "v_fma_f32 v24, v24, v84, v96\n"
  "v_fma_f32 v25, v25, v85, v97\n"
  "v_mfma_f32_16x16x32_bf16 v[8:11], v[56:59], a[56:59], v[8:11]\n"
  "ds_read_b128 v[76:79], v104 offset:7168\n"
  "v_fma_f32 v28, v28, v84, v96\n"
  "v_mfma_f32_16x16x32_bf16 v[8:11], v[60:63], a[80:83], v[8:11]\n"
  "buffer_load_dwordx4 v116, s[40:43], s57 offen offset:2048  lds\n"
  "v_smfmac_f32_16x16x64_bf16 v[80:83], v[88:91], v[48:55], v124\n"
  "s_add_u32 s57, s57, 6144\n"
  "v_fma_f32 v29, v29, v85, v97\n"
  "v_smfmac_f32_16x16x64_bf16 v[80:83], v[88:91], v[56:63], v124\n"
  "s_waitcnt lgkmcnt(2)\n"
  "buffer_store_dwordx4 v[92:95], v109, s[48:51], s68 offen offset:0 nt\n"
  "v_fma_f32 v32, v32, v84, v96\n"
  "v_fma_f32 v33, v33, v85, v97\n"
  "v_mfma_f32_16x16x32_bf16 v[12:15], v[60:63], a[84:87], v[100:103]\n"
  "v_fma_f32 v36, v36, v84, v96\n"
  "v_fma_f32 v37, v37, v85, v97\n"
  "v_mfma_f32_16x16x32_bf16 v[12:15], v[56:59], a[60:63], v[12:15]\n"
  "v_fma_f32 v26, v26, v86, v98\n"
  "v_fma_f32 v27, v27, v87, v99\n"
  "v_mfma_f32_16x16x32_bf16 v[12:15], v[52:55], a[36:39], v[12:15]\n"
  "v_fma_f32 v30, v30, v86, v98\n"
  "v_fma_f32 v31, v31, v87, v99\n"
  "v_mfma_f32_16x16x32_bf16 v[12:15], v[48:51], a[12:15], v[12:15]\n"
  "v_fma_f32 v34, v34, v86, v98\n"
  "v_fma_f32 v35, v35, v87, v99\n"
  "v_mfma_f32_16x16x32_bf16 v[16:19], v[48:51], a[16:19], v[100:103]\n"
  "v_fma_f32 v38, v38, v86, v98\n"
  "v_fma_f32 v39, v39, v87, v99\n"
  "v_mfma_f32_16x16x32_bf16 v[16:19], v[52:55], a[40:43], v[16:19]\n"
  "v_cvt_pk_bf16_f32 v40, v40, v44\n"
  "v_cvt_pk_bf16_f32 v41, v41, v45\n"
  "v_mfma_f32_16x16x32_bf16 v[16:19], v[56:59], a[64:67], v[16:19]\n"
  "v_cvt_pk_bf16_f32 v42, v42, v46\n"
  "v_cvt_pk_bf16_f32 v43, v43, v47\n"
  "v_mfma_f32_16x16x32_bf16 v[16:19], v[60:63], a[88:91], v[16:19]\n"
  "v_cvt_pk_bf16_f32 v24, v24, v28\n"
  "v_cvt_pk_bf16_f32 v28, v25, v29\n"
  "v_mfma_f32_16x16x32_bf16 v[20:23], v[60:63], a[92:95], v[100:103]\n"
  "v_cvt_pk_bf16_f32 v25, v32, v36\n"
  "v_cvt_pk_bf16_f32 v29, v33, v37\n"
  "v_mfma_f32_16x16x32_bf16 v[20:23], v[56:59], a[68:71], v[20:23]\n"
  "v_cvt_pk_bf16_f32 v26, v26, v30\n"
  "v_cvt_pk_bf16_f32 v30, v27, v31\n"
  "v_mfma_f32_16x16x32_bf16 v[20:23], v[52:55], a[44:47], v[20:23]\n"
  "v_cvt_pk_bf16_f32 v27, v34, v38\n"
  "v_cvt_pk_bf16_f32 v31, v35, v39\n"
  "v_mfma_f32_16x16x32_bf16 v[20:23], v[48:51], a[20:23], v[20:23]\n"
  "s_waitcnt lgkmcnt(0)\n"
  "v_xor_b32 v104, 0x4000, v104\n"
  "v_mfma_f32_16x16x32_bf16 v[20:23], v[64:67], a[116:119], v[20:23]\n"
  "s_add_u32 s66, s66, 6144\n"
  "ds_read_b128 v[48:51], v105 offset:0\n"
  "v_mfma_f32_16x16x32_bf16 v[20:23], v[68:71], a[140:143], v[20:23]\n"
  "s_cmp_ge_u32 s66, 86016\n"
  "s_cselect_b32 s64, 36864, 0\n"
  "v_mfma_f32_16x16x32_bf16 v[20:23], v[72:75], a[164:167], v[20:23]\n"
  "s_sub_u32 s66, s66, s64\n"
  "ds_read_b128 v[52:55], v105 offset:1024\n"
  "v_mfma_f32_16x16x32_bf16 v[20:23], v[76:79], a[188:191], v[20:23]\n"
  "s_sub_u32 s90, s90, 1\n"
  "s_cselect_b32 s63, s91, 6144\n"
  "s_cmov_b64 s[90:91], s[92:93]\n"
  "s_cmov_b64 s[92:93], s[94:95]\n"
  "ds_read_b64_tr_b16 v[96:97], v108 offset:2304\n"
  "ds_read_b64_tr_b16 v[98:99], v108 offset:2336\n"
  "v_mfma_f32_16x16x32_bf16 v[16:19], v[76:79], a[184:187], v[16:19]\n"
  "s_add_u32 s67, s67, s63\n"
  "v_mfma_f32_16x16x32_bf16 v[16:19], v[72:75], a[160:163], v[16:19]\n"
  "v_mfma_f32_16x16x32_bf16 v[16:19], v[68:71], a[136:139], v[16:19]\n"
  "ds_read_b128 v[56:59], v105 offset:2048\n"
  "v_mfma_f32_16x16x32_bf16 v[16:19], v[64:67], a[112:115], v[16:19]\n"
  "ds_write2_b32 v107, v40, v41 offset0:0 offset1:72\n"
  "v_mfma_f32_16x16x32_bf16 v[12:15], v[64:67], a[108:111], v[12:15]\n"
  "v_mfma_f32_16x16x32_bf16 v[12:15], v[68:71], a[132:135], v[12:15]\n"
  "v_mfma_f32_16x16x32_bf16 v[12:15], v[72:75], a[156:159], v[12:15]\n"
  "ds_read_b128 v[60:63], v105 offset:3072\n"
  "v_mfma_f32_16x16x32_bf16 v[12:15], v[76:79], a[180:183], v[12:15]\n"
  "ds_write2_b32 v107, v42, v43 offset0:144 offset1:216\n"
  "v_smfmac_f32_16x16x64_bf16 v[80:83], v[88:91], v[64:71], v124\n"
  "v_smfmac_f32_16x16x64_bf16 v[80:83], v[88:91], v[72:79], v124\n"
  "s_waitcnt lgkmcnt(4)\n"
  "buffer_store_dwordx4 v[96:99], v109, s[48:51], s68 offen offset:128 nt\n"
  "ds_write_b64 v106, v[24:25] offset:0\n"
  "v_mfma_f32_16x16x32_bf16 v[8:11], v[76:79], a[176:179], v[8:11]\n"
  "v_mfma_f32_16x16x32_bf16 v[8:11], v[72:75], a[152:155], v[8:11]\n"
  "v_mfma_f32_16x16x32_bf16 v[8:11], v[68:71], a[128:131], v[8:11]\n"
  "v_add_f32 v44, v80, v81\n"
  "v_mov_b32 v80, 0\n"
  "v_mfma_f32_16x16x32_bf16 v[8:11], v[64:67], a[104:107], v[8:11]\n"
  "ds_write_b64 v106, v[28:29] offset:288\n"
  "v_mfma_f32_16x16x32_bf16 v[4:7], v[64:67], a[100:103], v[4:7]\n"
  "v_mov_b32 v81, 0\n"
  "v_mfma_f32_16x16x32_bf16 v[4:7], v[68:71], a[124:127], v[4:7]\n"
  "ds_read_b64_tr_b16 v[92:93], v108 offset:4608\n"
  "ds_read_b64_tr_b16 v[94:95], v108 offset:4640\n"
  "v_mfma_f32_16x16x32_bf16 v[4:7], v[72:75], a[148:151], v[4:7]\n"
  "v_xor_b32 v108, 0x2000, v108\n"
  "v_mfma_f32_16x16x32_bf16 v[4:7], v[76:79], a[172:175], v[4:7]\n"
  "ds_write_b64 v106, v[26:27] offset:576\n"
  "v_mfma_f32_16x16x32_bf16 v[0:3], v[76:79], a[168:171], v[0:3]\n"
  "v_mfma_f32_16x16x32_bf16 v[0:3], v[72:75], a[144:147], v[0:3]\n"
  "v_mfma_f32_16x16x32_bf16 v[0:3], v[68:71], a[120:123], v[0:3]\n"
  "v_mfma_f32_16x16x32_bf16 v[0:3], v[64:67], a[96:99], v[0:3]\n"
  "s_waitcnt lgkmcnt(6)\n"
  "v_mfma_f32_16x16x32_bf16 v[0:3], v[48:51], a[192:195], v[0:3]\n"
  "ds_read_b128 v[64:67], v105 offset:4096\n"
  "v_mfma_f32_16x16x32_bf16 v[0:3], v[52:55], a[216:219], v[0:3]\n"
  "v_mfma_f32_16x16x32_bf16 v[0:3], v[56:59], a[240:243], v[0:3]\n"
  "ds_read_b128 v[68:71], v105 offset:5120\n"
  "v_mfma_f32_16x16x32_bf16 v[0:3], v[60:63], v[136:139], v[0:3]\n"
  "s_waitcnt lgkmcnt(3)\n"
  "buffer_store_dwordx4 v[92:95], v109, s[48:51], s68 offen offset:256 nt\n"
  "ds_write_b64 v106, v[30:31] offset:864\n"
  "v_mfma_f32_16x16x32_bf16 v[4:7], v[60:63], v[140:143], v[4:7]\n"
  "s_add_u32 s68, s68, s69\n"
  "v_mfma_f32_16x16x32_bf16 v[4:7], v[56:59], a[244:247], v[4:7]\n"
  "s_mov_b32 s69, s70\n"
  "v_mfma_f32_16x16x32_bf16 v[4:7], v[52:55], a[220:223], v[4:7]\n"
  "s_movk_i32 s70, 6144\n"
  "ds_read_b128 v[72:75], v105 offset:6144\n"
  "v_mfma_f32_16x16x32_bf16 v[4:7], v[48:51], a[196:199], v[4:7]\n"
  "ds_write_b32 v110, v44\n"
  "v_mfma_f32_16x16x32_bf16 v[8:11], v[48:51], a[200:203], v[8:11]\n"
  "v_mfma_f32_16x16x32_bf16 v[8:11], v[52:55], a[224:227], v[8:11]\n"
  "v_mfma_f32_16x16x32_bf16 v[8:11], v[56:59], a[248:251], v[8:11]\n"
  "ds_read_b128 v[76:79], v105 offset:7168\n"
  "v_xor_b32 v105, 0x4000, v105\n"
  "v_mfma_f32_16x16x32_bf16 v[8:11], v[60:63], v[144:147], v[8:11]\n"
  "v_mfma_f32_16x16x32_bf16 v[12:15], v[60:63], v[148:151], v[12:15]\n"
  "v_mfma_f32_16x16x32_bf16 v[12:15], v[56:59], a[252:255], v[12:15]\n"
  "v_mfma_f32_16x16x32_bf16 v[12:15], v[52:55], a[228:231], v[12:15]\n"
  "v_mfma_f32_16x16x32_bf16 v[12:15], v[48:51], a[204:207], v[12:15]\n"
  "v_mfma_f32_16x16x32_bf16 v[16:19], v[48:51], a[208:211], v[16:19]\n"
  "v_mfma_f32_16x16x32_bf16 v[16:19], v[52:55], a[232:235], v[16:19]\n"
  "v_mfma_f32_16x16x32_bf16 v[16:19], v[56:59], v[128:131], v[16:19]\n"
  "v_mfma_f32_16x16x32_bf16 v[16:19], v[60:63], v[152:155], v[16:19]\n"
  "v_xor_b32 v106, 0x2000, v106\n"
  "v_xor_b32 v107, 0x2000, v107\n"
  "v_mfma_f32_16x16x32_bf16 v[20:23], v[60:63], v[156:159], v[20:23]\n"
  "v_xor_b32 v110, 0x2000, v110\n"
  "v_mfma_f32_16x16x32_bf16 v[20:23], v[56:59], v[132:135], v[20:23]\n"
  "v_mfma_f32_16x16x32_bf16 v[20:23], v[52:55], a[236:239], v[20:23]\n"
  "v_mfma_f32_16x16x32_bf16 v[20:23], v[48:51], a[212:215], v[20:23]\n"
  "s_waitcnt lgkmcnt(0)\n"
  "s_barrier\n"
  "v_mfma_f32_16x16x32_bf16 v[20:23], v[64:67], v[180:183], v[20:23]\n"
  "ds_read_b128 v[48:51], v104 offset:0\n"
  "ds_read_b128 v[84:87], v111\n"
  "v_mfma_f32_16x16x32_bf16 v[20:23], v[68:71], v[204:207], v[20:23]\n"
  "ds_read_b128 v[96:99], v111 offset:64\n"
  "v_xor_b32 v111, 0x2000, v111\n"
  "v_mfma_f32_16x16x32_bf16 v[20:23], v[72:75], v[228:231], v[20:23]\n"
  "ds_read_b128 v[52:55], v104 offset:1024\n"
  "v_mfma_f32_16x16x32_bf16 v[44:47], v[76:79], v[252:255], v[20:23]\n"
  "v_mfma_f32_16x16x32_bf16 v[16:19], v[76:79], v[248:251], v[16:19]\n"
  "v_mfma_f32_16x16x32_bf16 v[16:19], v[72:75], v[224:227], v[16:19]\n"
  "v_mfma_f32_16x16x32_bf16 v[16:19], v[68:71], v[200:203], v[16:19]\n"
  "ds_read_b128 v[56:59], v104 offset:2048\n"
  "v_mfma_f32_16x16x32_bf16 v[40:43], v[64:67], v[176:179], v[16:19]\n"
  "v_mfma_f32_16x16x32_bf16 v[12:15], v[64:67], v[172:175], v[12:15]\n"
  "v_mfma_f32_16x16x32_bf16 v[12:15], v[68:71], v[196:199], v[12:15]\n"
  "v_mfma_f32_16x16x32_bf16 v[12:15], v[72:75], v[220:223], v[12:15]\n"
  "s_waitcnt lgkmcnt(2)\n"
  "ds_read_b128 v[60:63], v104 offset:3072\n"
  "v_add_f32 v84, v84, v96\n"
  "v_mfma_f32_16x16x32_bf16 v[36:39], v[76:79], v[244:247], v[12:15]\n"
  "v_add_f32 v85, v85, v97\n"
  "v_add_f32 v86, v86, v98\n"
  "v_mfma_f32_16x16x32_bf16 v[8:11], v[76:79], v[240:243], v[8:11]\n"
  "v_add_f32 v87, v87, v99\n"
  "v_max3_f32 v82, v82, v84, v85\n"
  "v_mfma_f32_16x16x32_bf16 v[8:11], v[72:75], v[216:219], v[8:11]\n"
  "v_rcp_f32 v84, v84\n"
  "v_mfma_f32_16x16x32_bf16 v[8:11], v[68:71], v[192:195], v[8:11]\n"
  "v_rcp_f32 v85, v85\n"
  "v_mfma_f32_16x16x32_bf16 v[32:35], v[64:67], v[168:171], v[8:11]\n"
  "v_max3_f32 v82, v82, v86, v87\n"
  "v_max3_f32 v83, v83, v84, v85\n"
  "v_mfma_f32_16x16x32_bf16 v[4:7], v[64:67], v[164:167], v[4:7]\n"
  "v_rcp_f32 v86, v86\n"
  "v_mfma_f32_16x16x32_bf16 v[4:7], v[68:71], v[188:191], v[4:7]\n"
  "v_rcp_f32 v87, v87\n"
  "v_mfma_f32_16x16x32_bf16 v[4:7], v[72:75], v[212:215], v[4:7]\n"
  "v_max3_f32 v83, v83, v86, v87\n"
  "v_mul_f32 v96, s99, v84\n"
  "v_mfma_f32_16x16x32_bf16 v[28:31], v[76:79], v[236:239], v[4:7]\n"
  "v_mul_f32 v97, s99, v85\n"
  "v_fma_f32 v40, v40, v84, v96\n"
  "v_mfma_f32_16x16x32_bf16 v[0:3], v[76:79], v[232:235], v[0:3]\n"
  "v_fma_f32 v44, v44, v84, v96\n"
  "v_cvt_pk_bf16_f32 v40, v40, v44\n"
  "v_mfma_f32_16x16x32_bf16 v[0:3], v[72:75], v[208:211], v[0:3]\n"
  "v_fma_f32 v41, v41, v85, v97\n"
  "v_fma_f32 v45, v45, v85, v97\n"
  "v_mfma_f32_16x16x32_bf16 v[0:3], v[68:71], v[184:187], v[0:3]\n"
  "v_cvt_pk_bf16_f32 v41, v41, v45\n"
  "v_mul_f32 v98, s99, v86\n"
  "v_mfma_f32_16x16x32_bf16 v[24:27], v[64:67], v[160:163], v[0:3]\n"
  "s_waitcnt lgkmcnt(0)\n"
  "v_mul_f32 v99, s99, v87\n"
  "v_fma_f32 v42, v42, v86, v98\n"
  "v_mfma_f32_16x16x32_bf16 v[0:3], v[48:51], a[0:3], v[100:103]\n"
  "ds_read_b128 v[64:67], v104 offset:4096\n"
  "v_fma_f32 v46, v46, v86, v98\n"
  "v_mfma_f32_16x16x32_bf16 v[0:3], v[52:55], a[24:27], v[0:3]\n"
  "v_cvt_pk_bf16_f32 v42, v42, v46\n"
  "v_fma_f32 v43, v43, v87, v99\n"
  "v_mfma_f32_16x16x32_bf16 v[0:3], v[56:59], a[48:51], v[0:3]\n"
  "ds_read_b128 v[68:71], v104 offset:5120\n"
  "v_fma_f32 v47, v47, v87, v99\n"
  "v_mfma_f32_16x16x32_bf16 v[0:3], v[60:63], a[72:75], v[0:3]\n"
  "ds_read_b64_tr_b16 v[92:93], v108 offset:0\n"
  "ds_read_b64_tr_b16 v[94:95], v108 offset:32\n"
  "v_mfma_f32_16x16x32_bf16 v[4:7], v[60:63], a[76:79], v[100:103]\n"
  "v_cvt_pk_bf16_f32 v43, v43, v47\n"
  "v_fma_f32 v24, v24, v84, v96\n"
  "v_mfma_f32_16x16x32_bf16 v[4:7], v[56:59], a[52:55], v[4:7]\n"
  "v_fma_f32 v25, v25, v85, v97\n"
  "v_fma_f32 v28, v28, v84, v96\n"
  "v_mfma_f32_16x16x32_bf16 v[4:7], v[52:55], a[28:31], v[4:7]\n"
  "ds_read_b128 v[72:75], v104 offset:6144\n"
  "v_fma_f32 v29, v29, v85, v97\n"
  "v_mfma_f32_16x16x32_bf16 v[4:7], v[48:51], a[4:7], v[4:7]\n"
  "v_fma_f32 v32, v32, v84, v96\n"
  "v_fma_f32 v36, v36, v84, v96\n"
  "v_mfma_f32_16x16x32_bf16 v[8:11], v[48:51], a[8:11], v[100:103]\n"
  "v_cvt_pk_bf16_f32 v24, v24, v28\n"
  "v_cvt_pk_bf16_f32 v28, v25, v29\n"
  "v_mfma_f32_16x16x32_bf16 v[8:11], v[52:55], a[32:35], v[8:11]\n"
  "v_cvt_pk_bf16_f32 v25, v32, v36\n"
  "v_fma_f32 v33, v33, v85, v97\n"
  "v_mfma_f32_16x16x32_bf16 v[8:11], v[56:59], a[56:59], v[8:11]\n"
  "ds_read_b128 v[76:79], v104 offset:7168\n"
  "v_fma_f32 v37, v37, v85, v97\n"
  "v_mfma_f32_16x16x32_bf16 v[8:11], v[60:63], a[80:83], v[8:11]\n"
  "v_fma_f32 v26, v26, v86, v98\n"
  "v_fma_f32 v27, v27, v87, v99\n"
  "v_smfmac_f32_16x16x64_bf16 v[80:83], v[88:91], v[48:55], v124\n"
  "v_fma_f32 v30, v30, v86, v98\n"
  "v_smfmac_f32_16x16x64_bf16 v[80:83], v[88:91], v[56:63], v124\n"
  "s_waitcnt lgkmcnt(2)\n"
  "buffer_store_dwordx4 v[92:95], v109, s[48:51], s68 offen offset:0 nt\n"
  "ds_write2_b32 v107, v40, v41 offset0:0 offset1:72\n"
  "v_mfma_f32_16x16x32_bf16 v[12:15], v[60:63], a[84:87], v[100:103]\n"
  "v_fma_f32 v31, v31, v87, v99\n"
  "v_fma_f32 v34, v34, v86, v98\n"
  "v_mfma_f32_16x16x32_bf16 v[12:15], v[56:59], a[60:63], v[12:15]\n"
  "v_fma_f32 v35, v35, v87, v99\n"
  "v_fma_f32 v38, v38, v86, v98\n"
  "v_mfma_f32_16x16x32_bf16 v[12:15], v[52:55], a[36:39], v[12:15]\n"
  "v_fma_f32 v39, v39, v87, v99\n"
  "v_cvt_pk_bf16_f32 v29, v33, v37\n"
  "v_mfma_f32_16x16x32_bf16 v[12:15], v[48:51], a[12:15], v[12:15]\n"
  "ds_write2_b32 v107, v42, v43 offset0:144 offset1:216\n"
  "v_mfma_f32_16x16x32_bf16 v[16:19], v[48:51], a[16:19], v[100:103]\n"
  "v_cvt_pk_bf16_f32 v26, v26, v30\n"
  "v_cvt_pk_bf16_f32 v30, v27, v31\n"
  "v_mfma_f32_16x16x32_bf16 v[16:19], v[52:55], a[40:43], v[16:19]\n"
  "v_cvt_pk_bf16_f32 v27, v34, v38\n"
  "v_cvt_pk_bf16_f32 v31, v35, v39\n"
  "v_mfma_f32_16x16x32_bf16 v[16:19], v[56:59], a[64:67], v[16:19]\n"
  "v_xor_b32 v104, 0x4000, v104\n"
  "v_mfma_f32_16x16x32_bf16 v[16:19], v[60:63], a[88:91], v[16:19]\n"
  "v_mfma_f32_16x16x32_bf16 v[20:23], v[60:63], a[92:95], v[100:103]\n"
  "v_mfma_f32_16x16x32_bf16 v[20:23], v[56:59], a[68:71], v[20:23]\n"
  "v_mfma_f32_16x16x32_bf16 v[20:23], v[52:55], a[44:47], v[20:23]\n"
  "v_mfma_f32_16x16x32_bf16 v[20:23], v[48:51], a[20:23], v[20:23]\n"
  "s_waitcnt lgkmcnt(2)\n"
  "v_mfma_f32_16x16x32_bf16 v[20:23], v[64:67], a[116:119], v[20:23]\n"
  "ds_read_b128 v[48:51], v105 offset:0\n"
  "v_mfma_f32_16x16x32_bf16 v[20:23], v[68:71], a[140:143], v[20:23]\n"
  "v_mfma_f32_16x16x32_bf16 v[20:23], v[72:75], a[164:167], v[20:23]\n"
  "ds_read_b128 v[52:55], v105 offset:1024\n"
  "v_mfma_f32_16x16x32_bf16 v[20:23], v[76:79], a[188:191], v[20:23]\n"
  "ds_write_b64 v106, v[24:25] offset:0\n"
  "v_mfma_f32_16x16x32_bf16 v[16:19], v[76:79], a[184:187], v[16:19]\n"
  "ds_read_b64_tr_b16 v[96:97], v108 offset:2304\n"
  "ds_read_b64_tr_b16 v[98:99], v108 offset:2336\n"
  "v_mfma_f32_16x16x32_bf16 v[16:19], v[72:75], a[160:163], v[16:19]\n"
  "v_mfma_f32_16x16x32_bf16 v[16:19], v[68:71], a[136:139], v[16:19]\n"
  "ds_read_b128 v[56:59], v105 offset:2048\n"
  "v_mfma_f32_16x16x32_bf16 v[16:19], v[64:67], a[112:115], v[16:19]\n"
  "v_mfma_f32_16x16x32_bf16 v[12:15], v[64:67], a[108:111], v[12:15]\n"
  "v_mfma_f32_16x16x32_bf16 v[12:15], v[68:71], a[132:135], v[12:15]\n"
  "v_mfma_f32_16x16x32_bf16 v[12:15], v[72:75], a[156:159], v[12:15]\n"
  "ds_read_b128 v[60:63], v105 offset:3072\n"
  "v_mfma_f32_16x16x32_bf16 v[12:15], v[76:79], a[180:183], v[12:15]\n"
  "v_smfmac_f32_16x16x64_bf16 v[80:83], v[88:91], v[64:71], v124\n"
  "v_smfmac_f32_16x16x64_bf16 v[80:83], v[88:91], v[72:79], v124\n"
  "s_waitcnt lgkmcnt(2)\n"
  "buffer_store_dwordx4 v[96:99], v109, s[48:51], s68 offen offset:128 nt\n"
  "ds_write_b64 v106, v[28:29] offset:288\n"
  "v_mfma_f32_16x16x32_bf16 v[8:11], v[76:79], a[176:179], v[8:11]\n"
  "v_mfma_f32_16x16x32_bf16 v[8:11], v[72:75], a[152:155], v[8:11]\n"
  "v_mfma_f32_16x16x32_bf16 v[8:11], v[68:71], a[128:131], v[8:11]\n"
  "v_add_f32 v44, v80, v81\n"
  "v_mov_b32 v80, 0\n"
  "v_mfma_f32_16x16x32_bf16 v[8:11], v[64:67], a[104:107], v[8:11]\n"
  "ds_write_b32 v110, v44\n"
  "v_mfma_f32_16x16x32_bf16 v[4:7], v[64:67], a[100:103], v[4:7]\n"
  "v_mov_b32 v81, 0\n"
  "v_mfma_f32_16x16x32_bf16 v[4:7], v[68:71], a[124:127], v[4:7]\n"
  "ds_read_b64_tr_b16 v[92:93], v108 offset:4608\n"
  "ds_read_b64_tr_b16 v[94:95], v108 offset:4640\n"
  "v_mfma_f32_16x16x32_bf16 v[4:7], v[72:75], a[148:151], v[4:7]\n"
  "v_xor_b32 v108, 0x2000, v108\n"
  "v_mfma_f32_16x16x32_bf16 v[4:7], v[76:79], a[172:175], v[4:7]\n"
  "v_mfma_f32_16x16x32_bf16 v[0:3], v[76:79], a[168:171], v[0:3]\n"
  "v_mfma_f32_16x16x32_bf16 v[0:3], v[72:75], a[144:147], v[0:3]\n"
  "v_mfma_f32_16x16x32_bf16 v[0:3], v[68:71], a[120:123], v[0:3]\n"
  "v_mfma_f32_16x16x32_bf16 v[0:3], v[64:67], a[96:99], v[0:3]\n"
  "s_waitcnt lgkmcnt(4)\n"
  "v_mfma_f32_16x16x32_bf16 v[0:3], v[48:51], a[192:195], v[0:3]\n"
  "ds_read_b128 v[64:67], v105 offset:4096\n"
  "v_mfma_f32_16x16x32_bf16 v[0:3], v[52:55], a[216:219], v[0:3]\n"
  "v_mfma_f32_16x16x32_bf16 v[0:3], v[56:59], a[240:243], v[0:3]\n"
  "ds_read_b128 v[68:71], v105 offset:5120\n"
  "v_mfma_f32_16x16x32_bf16 v[0:3], v[60:63], v[136:139], v[0:3]\n"
  "s_waitcnt lgkmcnt(2)\n"
  "buffer_store_dwordx4 v[92:95], v109, s[48:51], s68 offen offset:256 nt\n"
  "ds_write_b64 v106, v[26:27] offset:576\n"
  "v_mfma_f32_16x16x32_bf16 v[4:7], v[60:63], v[140:143], v[4:7]\n"
  "s_add_u32 s68, s68, s69\n"
  "v_mfma_f32_16x16x32_bf16 v[4:7], v[56:59], a[244:247], v[4:7]\n"
  "s_mov_b32 s69, s70\n"
  "v_mfma_f32_16x16x32_bf16 v[4:7], v[52:55], a[220:223], v[4:7]\n"
  "s_movk_i32 s70, 6144\n"
  "ds_read_b128 v[72:75], v105 offset:6144\n"
  "v_mfma_f32_16x16x32_bf16 v[4:7], v[48:51], a[196:199], v[4:7]\n"
  "v_mfma_f32_16x16x32_bf16 v[8:11], v[48:51], a[200:203], v[8:11]\n"
  "v_mfma_f32_16x16x32_bf16 v[8:11], v[52:55], a[224:227], v[8:11]\n"
  "v_mfma_f32_16x16x32_bf16 v[8:11], v[56:59], a[248:251], v[8:11]\n"
  "ds_read_b128 v[76:79], v105 offset:7168\n"
  "v_xor_b32 v105, 0x4000, v105\n"
  "v_mfma_f32_16x16x32_bf16 v[8:11], v[60:63], v[144:147], v[8:11]\n"
  "v_mfma_f32_16x16x32_bf16 v[12:15], v[60:63], v[148:151], v[12:15]\n"
  "v_mfma_f32_16x16x32_bf16 v[12:15], v[56:59], a[252:255], v[12:15]\n"
  "v_mfma_f32_16x16x32_bf16 v[12:15], v[52:55], a[228:231], v[12:15]\n"
  "v_mfma_f32_16x16x32_bf16 v[12:15], v[48:51], a[204:207], v[12:15]\n"
  "ds_write_b64 v106, v[30:31] offset:864\n"
  "v_mfma_f32_16x16x32_bf16 v[16:19], v[48:51], a[208:211], v[16:19]\n"
  "v_mfma_f32_16x16x32_bf16 v[16:19], v[52:55], a[232:235], v[16:19]\n"
  "v_mfma_f32_16x16x32_bf16 v[16:19], v[56:59], v[128:131], v[16:19]\n"
  "v_mfma_f32_16x16x32_bf16 v[16:19], v[60:63], v[152:155], v[16:19]\n"
  "v_xor_b32 v106, 0x2000, v106\n"
  "v_xor_b32 v107, 0x2000, v107\n"
  "v_mfma_f32_16x16x32_bf16 v[20:23], v[60:63], v[156:159], v[20:23]\n"
  "v_xor_b32 v110, 0x2000, v110\n"
  "v_mfma_f32_16x16x32_bf16 v[20:23], v[56:59], v[132:135], v[20:23]\n"
  "v_mfma_f32_16x16x32_bf16 v[20:23], v[52:55], a[236:239], v[20:23]\n"
  "v_mfma_f32_16x16x32_bf16 v[20:23], v[48:51], a[212:215], v[20:23]\n"
  "s_waitcnt lgkmcnt(0)\n"
  "s_barrier\n"
  "v_mfma_f32_16x16x32_bf16 v[20:23], v[64:67], v[180:183], v[20:23]\n"
  "ds_read_b128 v[48:51], v104 offset:0\n"
  "ds_read_b128 v[84:87], v111\n"
  "ds_read_b128 v[96:99], v111 offset:64\n"
  "v_xor_b32 v111, 0x2000, v111\n"
  "v_mfma_f32_16x16x32_bf16 v[20:23], v[68:71], v[204:207], v[20:23]\n"
  "v_mfma_f32_16x16x32_bf16 v[20:23], v[72:75], v[228:231], v[20:23]\n"
  "ds_read_b128 v[52:55], v104 offset:1024\n"
  "v_mfma_f32_16x16x32_bf16 v[44:47], v[76:79], v[252:255], v[20:23]\n"
  "v_mfma_f32_16x16x32_bf16 v[16:19], v[76:79], v[248:251], v[16:19]\n"
  "v_mfma_f32_16x16x32_bf16 v[16:19], v[72:75], v[224:227], v[16:19]\n"
  "v_mfma_f32_16x16x32_bf16 v[16:19], v[68:71], v[200:203], v[16:19]\n"
  "ds_read_b128 v[56:59], v104 offset:2048\n"
  "v_mfma_f32_16x16x32_bf16 v[40:43], v[64:67], v[176:179], v[16:19]\n"
  "v_mfma_f32_16x16x32_bf16 v[12:15], v[64:67], v[172:175], v[12:15]\n"
  "v_mfma_f32_16x16x32_bf16 v[12:15], v[68:71], v[196:199], v[12:15]\n"
  "v_mfma_f32_16x16x32_bf16 v[12:15], v[72:75], v[220:223], v[12:15]\n"
  "s_waitcnt lgkmcnt(2)\n"
  "ds_read_b128 v[60:63], v104 offset:3072\n"
  "v_add_f32 v84, v84, v96\n"
  "v_add_f32 v85, v85, v97\n"
  "v_add_f32 v86, v86, v98\n"
  "v_mfma_f32_16x16x32_bf16 v[36:39], v[76:79], v[244:247], v[12:15]\n"
  "v_add_f32 v87, v87, v99\n"
  "v_max3_f32 v82, v82, v84, v85\n"
  "v_rcp_f32 v84, v84\n"
  "v_mfma_f32_16x16x32_bf16 v[8:11], v[76:79], v[240:243], v[8:11]\n"
  "v_rcp_f32 v85, v85\n"
  "v_max3_f32 v82, v82, v86, v87\n"
  "v_mul_f32 v96, s99, v84\n"
  "v_mfma_f32_16x16x32_bf16 v[8:11], v[72:75], v[216:219], v[8:11]\n"
  "v_rcp_f32 v86, v86\n"
  "v_rcp_f32 v87, v87\n"
  "v_mfma_f32_16x16x32_bf16 v[8:11], v[68:71], v[192:195], v[8:11]\n"
  "v_max3_f32 v83, v83, v84, v85\n"
  "v_max3_f32 v83, v83, v86, v87\n"
  "v_mul_f32 v97, s99, v85\n"
  "v_mul_f32 v98, s99, v86\n"
  "v_mfma_f32_16x16x32_bf16 v[32:35], v[64:67], v[168:171], v[8:11]\n"
  "v_mul_f32 v99, s99, v87\n"
  "v_fma_f32 v36, v36, v84, v96\n"
  "v_fma_f32 v37, v37, v85, v97\n"
  "v_fma_f32 v38, v38, v86, v98\n"
  "v_mfma_f32_16x16x32_bf16 v[4:7], v[64:67], v[164:167], v[4:7]\n"
  "v_fma_f32 v39, v39, v87, v99\n"
  "v_fma_f32 v40, v40, v84, v96\n"
  "v_fma_f32 v44, v44, v84, v96\n"
  "v_fma_f32 v41, v41, v85, v97\n"
  "v_mfma_f32_16x16x32_bf16 v[4:7], v[68:71], v[188:191], v[4:7]\n"
  "v_fma_f32 v45, v45, v85, v97\n"
  "v_fma_f32 v42, v42, v86, v98\n"
  "v_fma_f32 v46, v46, v86, v98\n"
  "v_fma_f32 v43, v43, v87, v99\n"
  "v_mfma_f32_16x16x32_bf16 v[4:7], v[72:75], v[212:215], v[4:7]\n"
  "v_fma_f32 v32, v32, v84, v96\n"
  "v_fma_f32 v33, v33, v85, v97\n"
  "v_fma_f32 v34, v34, v86, v98\n"
  "v_fma_f32 v35, v35, v87, v99\n"
  "v_mfma_f32_16x16x32_bf16 v[28:31], v[76:79], v[236:239], v[4:7]\n"
  "v_fma_f32 v47, v47, v87, v99\n"
  "v_cvt_pk_bf16_f32 v40, v40, v44\n"
  "v_cvt_pk_bf16_f32 v41, v41, v45\n"
  "v_cvt_pk_bf16_f32 v42, v42, v46\n"
  "v_mfma_f32_16x16x32_bf16 v[0:3], v[76:79], v[232:235], v[0:3]\n"
  "ds_write2_b32 v107, v40, v41 offset0:0 offset1:72\n"
  "v_cvt_pk_bf16_f32 v43, v43, v47\n"
  "v_mfma_f32_16x16x32_bf16 v[0:3], v[72:75], v[208:211], v[0:3]\n"
  "ds_write2_b32 v107, v42, v43 offset0:144 offset1:216\n"
  "v_mfma_f32_16x16x32_bf16 v[0:3], v[68:71], v[184:187], v[0:3]\n"
  "v_fma_f32 v28, v28, v84, v96\n"
  "v_fma_f32 v29, v29, v85, v97\n"
  "v_fma_f32 v30, v30, v86, v98\n"
  "v_fma_f32 v31, v31, v87, v99\n"
  "v_mfma_f32_16x16x32_bf16 v[24:27], v[64:67], v[160:163], v[0:3]\n"
  "s_waitcnt lgkmcnt(2)\n"
  "v_mfma_f32_16x16x32_bf16 v[0:3], v[48:51], a[0:3], v[100:103]\n"
  "ds_read_b128 v[64:67], v104 offset:4096\n"
  "v_mfma_f32_16x16x32_bf16 v[0:3], v[52:55], a[24:27], v[0:3]\n"
  "v_mfma_f32_16x16x32_bf16 v[4:7], v[52:55], a[28:31], v[100:103]\n"
  "ds_read_b128 v[68:71], v104 offset:5120\n"
  "v_fma_f32 v24, v24, v84, v96\n"
  "v_fma_f32 v25, v25, v85, v97\n"
  "v_cvt_pk_bf16_f32 v24, v24, v28\n"
  "v_mfma_f32_16x16x32_bf16 v[4:7], v[48:51], a[4:7], v[4:7]\n"
  "ds_read_b64_tr_b16 v[92:93], v108 offset:0\n"
  "ds_read_b64_tr_b16 v[94:95], v108 offset:32\n"
  "v_cvt_pk_bf16_f32 v28, v25, v29\n"
  "v_cvt_pk_bf16_f32 v25, v32, v36\n"
  "v_mfma_f32_16x16x32_bf16 v[8:11], v[48:51], a[8:11], v[100:103]\n"
  "ds_write_b64 v106, v[24:25] offset:0\n"
  "v_cvt_pk_bf16_f32 v29, v33, v37\n"
  "v_mfma_f32_16x16x32_bf16 v[8:11], v[52:55], a[32:35], v[8:11]\n"
  "ds_write_b64 v106, v[28:29] offset:288\n"
  "v_fma_f32 v26, v26, v86, v98\n"
  "v_smfmac_f32_16x16x64_bf16 v[80:83], v[88:91], v[48:55], v124\n"
  "ds_read_b128 v[72:75], v104 offset:6144\n"
  "v_fma_f32 v27, v27, v87, v99\n"
  "v_cvt_pk_bf16_f32 v26, v26, v30\n"
  "v_mfma_f32_16x16x32_bf16 v[12:15], v[52:55], a[36:39], v[100:103]\n"
  "v_cvt_pk_bf16_f32 v30, v27, v31\n"
  "v_cvt_pk_bf16_f32 v27, v34, v38\n"
  "v_cvt_pk_bf16_f32 v31, v35, v39\n"
  "v_mfma_f32_16x16x32_bf16 v[12:15], v[48:51], a[12:15], v[12:15]\n"
  "ds_write_b64 v106, v[26:27] offset:576\n"
  "v_mfma_f32_16x16x32_bf16 v[16:19], v[48:51], a[16:19], v[100:103]\n"
  "ds_write_b64 v106, v[30:31] offset:864\n"
  "v_mfma_f32_16x16x32_bf16 v[16:19], v[52:55], a[40:43], v[16:19]\n"
  "ds_read_b128 v[76:79], v104 offset:7168\n"
  "v_xor_b32 v104, 0x4000, v104\n"
  "v_mfma_f32_16x16x32_bf16 v[20:23], v[52:55], a[44:47], v[100:103]\n"
  "v_mfma_f32_16x16x32_bf16 v[20:23], v[48:51], a[20:23], v[20:23]\n"
  "v_mfma_f32_16x16x32_bf16 v[20:23], v[56:59], a[68:71], v[20:23]\n"
  "s_waitcnt lgkmcnt(6)\n"
  "buffer_store_dwordx4 v[92:95], v109, s[48:51], s68 offen offset:0 nt\n"
  "s_waitcnt vmcnt(31)\n"
  "ds_read_b64_tr_b16 a[0:1], v117 offset:0\n"
  "ds_read_b64_tr_b16 a[4:5], v118 offset:0\n"
  "v_mfma_f32_16x16x32_bf16 v[20:23], v[60:63], a[92:95], v[20:23]\n"
  "v_mfma_f32_16x16x32_bf16 v[16:19], v[60:63], a[88:91], v[16:19]\n"
  "ds_read_b64_tr_b16 a[8:9], v119 offset:0\n"
  "ds_read_b64_tr_b16 a[12:13], v120 offset:0\n"
  "v_mfma_f32_16x16x32_bf16 v[16:19], v[56:59], a[64:67], v[16:19]\n"
  "ds_read_b64_tr_b16 a[16:17], v121 offset:2048\n"
  "ds_read_b64_tr_b16 a[20:21], v122 offset:2048\n"
  "v_mfma_f32_16x16x32_bf16 v[12:15], v[56:59], a[60:63], v[12:15]\n"
  "v_mfma_f32_16x16x32_bf16 v[12:15], v[60:63], a[84:87], v[12:15]\n"
  "s_waitcnt vmcnt(25)\n"
  "ds_read_b64_tr_b16 a[2:3], v117 offset:3072\n"
  "ds_read_b64_tr_b16 a[6:7], v118 offset:3072\n"
  "v_smfmac_f32_16x16x64_bf16 v[80:83], v[88:91], v[56:63], v124\n"
  "v_mfma_f32_16x16x32_bf16 v[8:11], v[60:63], a[80:83], v[8:11]\n"
  "ds_read_b64_tr_b16 a[10:11], v119 offset:3072\n"
  "ds_read_b64_tr_b16 a[14:15], v120 offset:3072\n"
  "v_mfma_f32_16x16x32_bf16 v[8:11], v[56:59], a[56:59], v[8:11]\n"
  "s_add_u32 m0, s59, 0\n"
  "ds_read_b64_tr_b16 a[18:19], v121 offset:5120\n"
  "ds_read_b64_tr_b16 a[22:23], v122 offset:5120\n"
  "v_mfma_f32_16x16x32_bf16 v[4:7], v[56:59], a[52:55], v[4:7]\n"
  "s_waitcnt lgkmcnt(6)\n"
  "buffer_load_dwordx4 v114, s[40:43], s57 offen offset:0 nt lds\n"
  "buffer_load_dwordx4 v115, s[40:43], s57 offen offset:1024 nt lds\n"
  "v_mfma_f32_16x16x32_bf16 v[4:7], v[60:63], a[76:79], v[4:7]\n"
  "s_waitcnt vmcnt(23)\n"
  "ds_read_b64_tr_b16 a[24:25], v117 offset:6144\n"
  "ds_read_b64_tr_b16 a[28:29], v118 offset:6144\n"
  "v_mfma_f32_16x16x32_bf16 v[0:3], v[60:63], a[72:75], v[0:3]\n"
  "buffer_load_dwordx4 v116, s[40:43], s57 offen offset:2048  lds\n"
  "v_mfma_f32_16x16x32_bf16 v[0:3], v[56:59], a[48:51], v[0:3]\n"
  "s_add_u32 s57, s57, 6144\n"
  "ds_read_b64_tr_b16 a[32:33], v119 offset:6144\n"
  "ds_read_b64_tr_b16 a[36:37], v120 offset:6144\n"
  "v_mfma_f32_16x16x32_bf16 v[0:3], v[64:67], a[96:99], v[0:3]\n"
  "s_add_u32 m0, s59, 3072\n"
  "ds_read_b128 v[48:51], v105 offset:0\n"
  "ds_read_b64_tr_b16 a[40:41], v121 offset:8192\n"
  "ds_read_b64_tr_b16 a[44:45], v122 offset:8192\n"
  "v_mfma_f32_16x16x32_bf16 v[0:3], v[68:71], a[120:123], v[0:3]\n"
  "s_waitcnt lgkmcnt(7)\n"
  "buffer_load_dwordx4 v114, s[40:43], s57 offen offset:0 nt lds\n"
  "buffer_load_dwordx4 v115, s[40:43], s57 offen offset:1024 nt lds\n"
  "v_mfma_f32_16x16x32_bf16 v[4:7], v[68:71], a[124:127], v[4:7]\n"
  "ds_read_b128 v[52:55], v105 offset:1024\n"
  "s_waitcnt vmcnt(21)\n"
  "ds_read_b64_tr_b16 a[26:27], v117 offset:9216\n"
  "ds_read_b64_tr_b16 a[30:31], v118 offset:9216\n"
  "s_waitcnt vmcnt(12)\n"
  "ds_read_b64 v[32:33], v117 offset:15360\n"
  "v_mfma_f32_16x16x32_bf16 v[4:7], v[64:67], a[100:103], v[4:7]\n"
  "buffer_load_dwordx4 v116, s[40:43], s57 offen offset:2048  lds\n"
  "ds_read_b64 v[34:35], v118 offset:15360\n"
  "ds_read_b64_tr_b16 v[96:97], v108 offset:2304\n"
  "v_mfma_f32_16x16x32_bf16 v[8:11], v[64:67], a[104:107], v[8:11]\n"
  "s_add_u32 s57, s57, 6144\n"
  "ds_read_b64_tr_b16 a[34:35], v119 offset:9216\n"
  "ds_read_b64_tr_b16 a[38:39], v120 offset:9216\n"
  "ds_read_b64_tr_b16 v[98:99], v108 offset:2336\n"
  "v_mfma_f32_16x16x32_bf16 v[8:11], v[68:71], a[128:131], v[8:11]\n"
  "s_add_u32 m0, s59, 6144\n"
  "ds_read_b64_tr_b16 a[42:43], v121 offset:11264\n"
  "ds_read_b64_tr_b16 a[46:47], v122 offset:11264\n"
  "v_smfmac_f32_16x16x64_bf16 v[80:83], v[88:91], v[64:71], v124\n"
  "s_waitcnt lgkmcnt(11)\n"
  "buffer_load_dwordx4 v114, s[40:43], s57 offen offset:0 nt lds\n"
  "buffer_load_dwordx4 v115, s[40:43], s57 offen offset:1024 nt lds\n"
  "ds_read_b128 v[56:59], v105 offset:2048\n"
  "v_mfma_f32_16x16x32_bf16 v[12:15], v[68:71], a[132:135], v[12:15]\n"
  "ds_read_b64_tr_b16 a[48:49], v117 offset:12288\n"
  "ds_read_b64_tr_b16 a[52:53], v118 offset:12288\n"
  "v_mfma_f32_16x16x32_bf16 v[12:15], v[64:67], a[108:111], v[12:15]\n"
  "buffer_load_dwordx4 v116, s[40:43], s57 offen offset:2048  lds\n"
  "v_mfma_f32_16x16x32_bf16 v[16:19], v[64:67], a[112:115], v[16:19]\n"
  "s_add_u32 s57, s57, 6144\n"
  "ds_read_b64_tr_b16 a[56:57], v119 offset:12288\n"
  "ds_read_b64_tr_b16 a[60:61], v120 offset:12288\n"
  "s_waitcnt lgkmcnt(11)\n"
  "v_max3_f32 v32, |v32|, |v33|, |v34|\n"
  "v_max3_f32 v32, v32, |v35|, 0\n"
  "v_mfma_f32_16x16x32_bf16 v[16:19], v[68:71], a[136:139], v[16:19]\n"
  "s_add_u32 m0, s59, 9216\n"
  "ds_read_b128 v[60:63], v105 offset:3072\n"
  "ds_read_b64_tr_b16 a[64:65], v121 offset:14336\n"
  "ds_read_b64_tr_b16 a[68:69], v122 offset:14336\n"
  "v_max_f32 v32, 0, v32\n"
  "v_mfma_f32_16x16x32_bf16 v[20:23], v[68:71], a[140:143], v[20:23]\n"
  "s_waitcnt lgkmcnt(8)\n"
  "buffer_load_dwordx4 v114, s[40:43], s57 offen offset:0 nt lds\n"
  "buffer_load_dwordx4 v115, s[40:43], s57 offen offset:1024 nt lds\n"
  "v_mfma_f32_16x16x32_bf16 v[20:23], v[64:67], a[116:119], v[20:23]\n"
  "ds_read_b64_tr_b16 a[50:51], v117 offset:15360\n"
  "ds_read_b64_tr_b16 a[54:55], v118 offset:15360\n"
  "v_mul_f32 v33, 0x40000000, v32\n"
  "v_mul_f32 v32, 0x2b800000, v32\n"
  "v_mfma_f32_16x16x32_bf16 v[20:23], v[72:75], a[164:167], v[20:23]\n"
  "buffer_load_dwordx4 v116, s[40:43], s57 offen offset:2048  lds\n"
  "v_mfma_f32_16x16x32_bf16 v[20:23], v[76:79], a[188:191], v[20:23]\n"
  "s_add_u32 s57, s57, 6144\n"
  "buffer_store_dwordx4 v[96:99], v109, s[48:51], s68 offen offset:128 nt\n"
  "ds_read_b64_tr_b16 a[58:59], v119 offset:15360\n"
  "ds_read_b64_tr_b16 a[62:63], v120 offset:15360\n"
  "v_mfma_f32_16x16x32_bf16 v[16:19], v[76:79], a[184:187], v[16:19]\n"
  "s_add_u32 m0, s59, 12288\n"
  "ds_read_b64_tr_b16 a[66:67], v121 offset:17408\n"
  "ds_read_b64_tr_b16 a[70:71], v122 offset:17408\n"
  "v_mfma_f32_16x16x32_bf16 v[16:19], v[72:75], a[160:163], v[16:19]\n"
  "s_waitcnt lgkmcnt(6)\n"
  "buffer_load_dwordx4 v114, s[40:43], s57 offen offset:0 nt lds\n"
  "buffer_load_dwordx4 v115, s[40:43], s57 offen offset:1024 nt lds\n"
  "v_mfma_f32_16x16x32_bf16 v[12:15], v[72:75], a[156:159], v[12:15]\n"
  "s_waitcnt vmcnt(12)\n"
  "ds_read_b64_tr_b16 a[72:73], v117 offset:0\n"
  "ds_read_b64_tr_b16 a[76:77], v118 offset:0\n"
  "v_mfma_f32_16x16x32_bf16 v[12:15], v[76:79], a[180:183], v[12:15]\n"
  "buffer_load_dwordx4 v116, s[40:43], s57 offen offset:2048  lds\n"
  "v_smfmac_f32_16x16x64_bf16 v[80:83], v[88:91], v[72:79], v124\n"
  "s_add_u32 s57, s57, 6144\n"
  "ds_read_b64_tr_b16 a[80:81], v119 offset:0\n"
  "ds_read_b64_tr_b16 a[84:85], v120 offset:0\n"
  "ds_read_b64_tr_b16 v[92:93], v108 offset:4608\n"
  "v_mfma_f32_16x16x32_bf16 v[8:11], v[76:79], a[176:179], v[8:11]\n"
  "s_add_u32 m0, s59, 15360\n"
  "ds_read_b64_tr_b16 a[88:89], v121 offset:2048\n"
  "ds_read_b64_tr_b16 a[92:93], v122 offset:2048\n"
  "ds_read_b64_tr_b16 v[94:95], v108 offset:4640\n"
  "v_xor_b32 v108, 0x2000, v108\n"
  "v_mfma_f32_16x16x32_bf16 v[8:11], v[72:75], a[152:155], v[8:11]\n"
  "s_waitcnt lgkmcnt(8)\n"
  "buffer_load_dwordx4 v114, s[40:43], s57 offen offset:0 nt lds\n"
  "buffer_load_dwordx4 v115, s[40:43], s57 offen offset:1024 nt lds\n"
  "v_mfma_f32_16x16x32_bf16 v[4:7], v[72:75], a[148:151], v[4:7]\n"
  "s_waitcnt vmcnt(12)\n"
  "ds_read_b64_tr_b16 a[74:75], v117 offset:3072\n"
  "ds_read_b64_tr_b16 a[78:79], v118 offset:3072\n"
  "v_add_f32 v44, v80, v81\n"
  "v_max_f32 v82, v82, v32\n"
  "v_mfma_f32_16x16x32_bf16 v[4:7], v[76:79], a[172:175], v[4:7]\n"
  "buffer_load_dwordx4 v116, s[40:43], s57 offen offset:2048  lds\n"
  "ds_write_b32 v110, v44\n"
  "v_mfma_f32_16x16x32_bf16 v[0:3], v[76:79], a[168:171], v[0:3]\n"
  "s_add_u32 s57, s57, 6144\n"
  "ds_read_b64_tr_b16 a[82:83], v119 offset:3072\n"
  "ds_read_b64_tr_b16 a[86:87], v120 offset:3072\n"
  "v_readlane_b32 s98, v33, 0\n"
  "v_mfma_f32_16x16x32_bf16 v[0:3], v[72:75], a[144:147], v[0:3]\n"
  "s_add_u32 m0, s59, 0\n"
  "ds_read_b64_tr_b16 a[90:91], v121 offset:5120\n"
  "ds_read_b64_tr_b16 a[94:95], v122 offset:5120\n"
  "v_readlane_b32 s62, v33, 21\n"
  "v_mfma_f32_16x16x32_bf16 v[0:3], v[48:51], a[192:195], v[0:3]\n"
  "s_waitcnt lgkmcnt(8)\n"
  "buffer_load_dwordx4 v114, s[40:43], s57 offen offset:0 nt lds\n"
  "buffer_load_dwordx4 v115, s[40:43], s57 offen offset:1024 nt lds\n"
  "ds_read_b128 v[64:67], v105 offset:4096\n"
  "v_mfma_f32_16x16x32_bf16 v[0:3], v[52:55], a[216:219], v[0:3]\n"
  "s_waitcnt vmcnt(12)\n"
  "ds_read_b64_tr_b16 a[96:97], v117 offset:6144\n"
  "ds_read_b64_tr_b16 a[100:101], v118 offset:6144\n"
  "v_readlane_b32 s63, v33, 42\n"
  "v_mfma_f32_16x16x32_bf16 v[4:7], v[52:55], a[220:223], v[4:7]\n"
  "buffer_load_dwordx4 v116, s[40:43], s57 offen offset:2048  lds\n"
  "ds_read_b128 v[68:71], v105 offset:5120\n"
  "v_mov_b32 v80, 0\n"
  "v_mfma_f32_16x16x32_bf16 v[4:7], v[48:51], a[196:199], v[4:7]\n"
  "s_add_u32 s57, s57, 6144\n"
  "s_waitcnt lgkmcnt(11)\n"
  "buffer_store_dwordx4 v[92:95], v109, s[48:51], s68 offen offset:256 nt\n"
  "ds_read_b64_tr_b16 a[104:105], v119 offset:6144\n"
  "ds_read_b64_tr_b16 a[108:109], v120 offset:6144\n"
  "v_mov_b32 v81, 0\n"
  "v_mfma_f32_16x16x32_bf16 v[8:11], v[48:51], a[200:203], v[8:11]\n"
  "s_add_u32 m0, s59, 3072\n"
  "ds_read_b64_tr_b16 a[112:113], v121 offset:8192\n"
  "ds_read_b64_tr_b16 a[116:117], v122 offset:8192\n"
  "v_readlane_b32 s64, v33, 63\n"
  "v_mfma_f32_16x16x32_bf16 v[8:11], v[52:55], a[224:227], v[8:11]\n"
  "s_waitcnt lgkmcnt(8)\n"
  "buffer_load_dwordx4 v114, s[40:43], s57 offen offset:0 nt lds\n"
  "buffer_load_dwordx4 v115, s[40:43], s57 offen offset:1024 nt lds\n"
  "v_mfma_f32_16x16x32_bf16 v[12:15], v[52:55], a[228:231], v[12:15]\n"
  "ds_read_b128 v[72:75], v105 offset:6144\n"
  "s_waitcnt vmcnt(13)\n"
  "ds_read_b64_tr_b16 a[98:99], v117 offset:9216\n"
  "ds_read_b64_tr_b16 a[102:103], v118 offset:9216\n"
  "v_mfma_f32_16x16x32_bf16 v[12:15], v[48:51], a[204:207], v[12:15]\n"
  "s_add_u32 s68, s68, s69\n"
  "buffer_load_dwordx4 v116, s[40:43], s57 offen offset:2048  lds\n"
  "v_mfma_f32_16x16x32_bf16 v[16:19], v[48:51], a[208:211], v[16:19]\n"
  "s_add_u32 s57, s57, 6144\n"
  "ds_read_b64_tr_b16 a[106:107], v119 offset:9216\n"
  "ds_read_b64_tr_b16 a[110:111], v120 offset:9216\n"
  "v_mfma_f32_16x16x32_bf16 v[16:19], v[52:55], a[232:235], v[16:19]\n"
  "s_add_u32 m0, s59, 6144\n"
  "ds_read_b64_tr_b16 a[114:115], v121 offset:11264\n"
  "ds_read_b64_tr_b16 a[118:119], v122 offset:11264\n"
  "v_mfma_f32_16x16x32_bf16 v[20:23], v[52:55], a[236:239], v[20:23]\n"
  "s_waitcnt lgkmcnt(7)\n"
  "buffer_load_dwordx4 v114, s[40:43], s57 offen offset:0 nt lds\n"
  "buffer_load_dwordx4 v115, s[40:43], s57 offen offset:1024 nt lds\n"
  "ds_read_b128 v[76:79], v105 offset:7168\n"
  "v_mfma_f32_16x16x32_bf16 v[20:23], v[48:51], a[212:215], v[20:23]\n"
  "s_waitcnt vmcnt(12)\n"
  "ds_read_b64_tr_b16 a[120:121], v117 offset:12288\n"
  "ds_read_b64_tr_b16 a[124:125], v118 offset:12288\n"
  "v_xor_b32 v105, 0x4000, v105\n"
  "v_mfma_f32_16x16x32_bf16 v[20:23], v[56:59], v[132:135], v[20:23]\n"
  "s_mov_b32 s69, s70\n"
  "buffer_load_dwordx4 v116, s[40:43], s57 offen offset:2048  lds\n"
  "v_mfma_f32_16x16x32_bf16 v[20:23], v[60:63], v[156:159], v[20:23]\n"
  "s_add_u32 s57, s57, 6144\n"
  "ds_read_b64_tr_b16 a[128:129], v119 offset:12288\n"
  "ds_read_b64_tr_b16 a[132:133], v120 offset:12288\n"
  "v_mfma_f32_16x16x32_bf16 v[16:19], v[60:63], v[152:155], v[16:19]\n"
  "s_add_u32 m0, s59, 9216\n"
  "ds_read_b64_tr_b16 a[136:137], v121 offset:14336\n"
  "ds_read_b64_tr_b16 a[140:141], v122 offset:14336\n"
  "v_mfma_f32_16x16x32_bf16 v[16:19], v[56:59], v[128:131], v[16:19]\n"
  "s_waitcnt lgkmcnt(7)\n"
  "buffer_load_dwordx4 v114, s[40:43], s57 offen offset:0 nt lds\n"
  "buffer_load_dwordx4 v115, s[40:43], s57 offen offset:1024 nt lds\n"
  "v_mfma_f32_16x16x32_bf16 v[12:15], v[56:59], a[252:255], v[12:15]\n"
  "s_waitcnt vmcnt(12)\n"
  "ds_read_b64_tr_b16 a[122:123], v117 offset:15360\n"
  "ds_read_b64_tr_b16 a[126:127], v118 offset:15360\n"
  "v_mfma_f32_16x16x32_bf16 v[12:15], v[60:63], v[148:151], v[12:15]\n"
  "s_mov_b32 s70, s96\n"
  "buffer_load_dwordx4 v116, s[40:43], s57 offen offset:2048  lds\n"
  "v_mfma_f32_16x16x32_bf16 v[8:11], v[60:63], v[144:147], v[8:11]\n"
  "s_add_u32 s57, s57, s58\n"
  "ds_read_b64_tr_b16 a[130:131], v119 offset:15360\n"
  "ds_read_b64_tr_b16 a[134:135], v120 offset:15360\n"
  "v_mfma_f32_16x16x32_bf16 v[8:11], v[56:59], a[248:251], v[8:11]\n"
  "s_add_u32 m0, s59, 12288\n"
  "ds_read_b64_tr_b16 a[138:139], v121 offset:17408\n"
  "ds_read_b64_tr_b16 a[142:143], v122 offset:17408\n"
  "v_xor_b32 v106, 0x2000, v106\n"
  "v_xor_b32 v107, 0x2000, v107\n"
  "v_mfma_f32_16x16x32_bf16 v[4:7], v[56:59], a[244:247], v[4:7]\n"
  "s_waitcnt lgkmcnt(6)\n"
  "buffer_load_dwordx4 v114, s[40:43], s57 offen offset:0 nt lds\n"
  "buffer_load_dwordx4 v115, s[40:43], s57 offen offset:1024 nt lds\n"
  "v_mfma_f32_16x16x32_bf16 v[4:7], v[60:63], v[140:143], v[4:7]\n"
  "s_waitcnt vmcnt(12)\n"
  "ds_read_b64_tr_b16 a[144:145], v117 offset:0\n"
  "ds_read_b64_tr_b16 a[148:149], v118 offset:0\n"
  "v_xor_b32 v110, 0x2000, v110\n"
  "v_mfma_f32_16x16x32_bf16 v[0:3], v[60:63], v[136:139], v[0:3]\n"
  "s_mov_b32 s96, s97\n"
  "buffer_load_dwordx4 v116, s[40:43], s57 offen offset:2048  lds\n"
  "v_mfma_f32_16x16x32_bf16 v[0:3], v[56:59], a[240:243], v[0:3]\n"
  "s_add_u32 s57, s57, 6144\n"
  "s_nop 1\n"
  "s_max_u32 s98, s98, s62\n"
  "s_max_u32 s63, s63, s64\n"
  "s_max_u32 s98, s98, s63\n"
  "s_and_b32 s98, s98, 0x7f800000\n"
  "s_cselect_b32 s62, 0x400000, 0\n"
  "s_or_b32 s98, s98, s62\n"
  "s_nop 0\n"
  "ds_read_b64_tr_b16 a[152:153], v119 offset:0\n"
  "ds_read_b64_tr_b16 a[156:157], v120 offset:0\n"
  "v_mov_b32 v100, s98\n"
  "v_mov_b32 v101, s98\n"
  "v_mov_b32 v102, s98\n"
  "v_mov_b32 v103, s98\n"
  "s_waitcnt lgkmcnt(0)\n"
  "s_barrier\n"
  "v_mfma_f32_16x16x32_bf16 v[0:3], v[64:67], v[160:163], v[0:3]\n"
  "s_add_u32 m0, s59, 15360\n"
  "ds_read_b128 v[48:51], v104 offset:0\n"
  "ds_read_b64_tr_b16 a[160:161], v121 offset:2048\n"
  "ds_read_b64_tr_b16 a[164:165], v122 offset:2048\n"
  "ds_read_b128 v[84:87], v111\n"
  "v_mfma_f32_16x16x32_bf16 v[0:3], v[68:71], v[184:187], v[0:3]\n"
  "buffer_load_dwordx4 v114, s[40:43], s57 offen offset:0 nt lds\n"
  "buffer_load_dwordx4 v115, s[40:43], s57 offen offset:1024 nt lds\n"
  "v_mfma_f32_16x16x32_bf16 v[4:7], v[68:71], v[188:191], v[4:7]\n"
  "ds_read_b128 v[52:55], v104 offset:1024\n"
  "s_waitcnt vmcnt(11)\n"
  "ds_read_b64_tr_b16 a[146:147], v117 offset:3072\n"
  "ds_read_b64_tr_b16 a[150:151], v118 offset:3072\n"
  "ds_read_b128 v[96:99], v111 offset:64\n"
  "v_mfma_f32_16x16x32_bf16 v[4:7], v[64:67], v[164:167], v[4:7]\n"
  "s_sub_u32 s90, s90, 1\n"
  "s_cselect_b32 s63, s91, 6144\n"
  "s_cmov_b64 s[90:91], s[92:93]\n"
  "s_cmov_b64 s[92:93], s[94:95]\n"
  "buffer_load_dwordx4 v116, s[40:43], s57 offen offset:2048  lds\n"
  "v_xor_b32 v111, 0x2000, v111\n"
  "v_mfma_f32_16x16x32_bf16 v[8:11], v[64:67], v[168:171], v[8:11]\n"
  "s_add_u32 s57, s57, 6144\n"
  "ds_read_b64_tr_b16 a[154:155], v119 offset:3072\n"
  "ds_read_b64_tr_b16 a[158:159], v120 offset:3072\n"
  "v_mfma_f32_16x16x32_bf16 v[8:11], v[68:71], v[192:195], v[8:11]\n"
  "s_add_u32 m0, s59, 0\n"
  "ds_read_b64_tr_b16 a[162:163], v121 offset:5120\n"
  "ds_read_b64_tr_b16 a[166:167], v122 offset:5120\n"
  "v_mfma_f32_16x16x32_bf16 v[12:15], v[68:71], v[196:199], v[12:15]\n"
  "s_waitcnt lgkmcnt(9)\n"
  "buffer_load_dwordx4 v114, s[40:43], s57 offen offset:0 nt lds\n"
  "buffer_load_dwordx4 v115, s[40:43], s57 offen offset:1024 nt lds\n"
  "ds_read_b128 v[56:59], v104 offset:2048\n"
  "v_mfma_f32_16x16x32_bf16 v[12:15], v[64:67], v[172:175], v[12:15]\n"
  "s_waitcnt vmcnt(11)\n"
  "ds_read_b64_tr_b16 a[168:169], v117 offset:6144\n"
  "ds_read_b64_tr_b16 a[172:173], v118 offset:6144\n"
  "v_mfma_f32_16x16x32_bf16 v[16:19], v[64:67], v[176:179], v[16:19]\n"
  "s_add_u32 s72, s72, 7\n"
  "buffer_load_dwordx4 v116, s[40:43], s57 offen offset:2048  lds\n"
  "v_mfma_f32_16x16x32_bf16 v[16:19], v[68:71], v[200:203], v[16:19]\n"
  "s_add_u32 s57, s57, 6144\n"
  "ds_read_b64_tr_b16 a[176:177], v119 offset:6144\n"
  "ds_read_b64_tr_b16 a[180:181], v120 offset:6144\n"
  "v_mfma_f32_16x16x32_bf16 v[20:23], v[68:71], v[204:207], v[20:23]\n"
  "s_add_u32 m0, s59, 3072\n"
  "ds_read_b128 v[60:63], v104 offset:3072\n"
  "ds_read_b64_tr_b16 a[184:185], v121 offset:8192\n"
  "ds_read_b64_tr_b16 a[188:189], v122 offset:8192\n"
  "v_mfma_f32_16x16x32_bf16 v[20:23], v[64:67], v[180:183], v[20:23]\n"
  "s_waitcnt lgkmcnt(8)\n"
  "buffer_load_dwordx4 v114, s[40:43], s57 offen offset:0 nt lds\n"
  "buffer_load_dwordx4 v115, s[40:43], s57 offen offset:1024 nt lds\n"
  "v_mfma_f32_16x16x32_bf16 v[20:23], v[72:75], v[228:231], v[20:23]\n"
  "s_waitcnt vmcnt(11)\n"
  "ds_read_b64_tr_b16 a[170:171], v117 offset:9216\n"
  "ds_read_b64_tr_b16 a[174:175], v118 offset:9216\n"
  "v_mfma_f32_16x16x32_bf16 v[44:47], v[76:79], v[252:255], v[20:23]\n"
  "buffer_load_dwordx4 v116, s[40:43], s57 offen offset:2048  lds\n"
  "v_add_f32 v84, v84, v96\n"
  "v_add_f32 v85, v85, v97\n"
  "v_mfma_f32_16x16x32_bf16 v[16:19], v[76:79], v[248:251], v[16:19]\n"
  "s_add_u32 s57, s57, 6144\n"
  "ds_read_b64_tr_b16 a[178:179], v119 offset:9216\n"
  "ds_read_b64_tr_b16 a[182:183], v120 offset:9216\n"
  "v_add_f32 v86, v86, v98\n"
  "v_add_f32 v87, v87, v99\n"
  "v_mfma_f32_16x16x32_bf16 v[40:43], v[72:75], v[224:227], v[16:19]\n"
  "s_add_u32 m0, s59, 6144\n"
  "ds_read_b64_tr_b16 a[186:187], v121 offset:11264\n"
  "ds_read_b64_tr_b16 a[190:191], v122 offset:11264\n"
  "v_max3_f32 v82, v82, v84, v85\n"
  "v_max3_f32 v82, v82, v86, v87\n"
  "v_mfma_f32_16x16x32_bf16 v[12:15], v[72:75], v[220:223], v[12:15]\n"
  "s_waitcnt lgkmcnt(6)\n"
  "buffer_load_dwordx4 v114, s[40:43], s57 offen offset:0 nt lds\n"
  "buffer_load_dwordx4 v115, s[40:43], s57 offen offset:1024 nt lds\n"
  "v_mfma_f32_16x16x32_bf16 v[36:39], v[76:79], v[244:247], v[12:15]\n"
  "s_waitcnt vmcnt(11)\n"
  "ds_read_b64_tr_b16 a[192:193], v117 offset:12288\n"
  "ds_read_b64_tr_b16 a[196:197], v118 offset:12288\n"
  "v_rcp_f32 v84, v84\n"
  "v_mfma_f32_16x16x32_bf16 v[8:11], v[76:79], v[240:243], v[8:11]\n"
  "s_sub_u32 s56, s56, 1\n"
  "buffer_load_dwordx4 v116, s[40:43], s57 offen offset:2048  lds\n"
  "v_rcp_f32 v85, v85\n"
  "v_mfma_f32_16x16x32_bf16 v[32:35], v[72:75], v[216:219], v[8:11]\n"
  "s_add_u32 s57, s57, 6144\n"
  "ds_read_b64_tr_b16 a[200:201], v119 offset:12288\n"
  "ds_read_b64_tr_b16 a[204:205], v120 offset:12288\n"
  "v_rcp_f32 v86, v86\n"
  "v_mfma_f32_16x16x32_bf16 v[4:7], v[72:75], v[212:215], v[4:7]\n"
  "s_add_u32 m0, s59, 9216\n"
  "ds_read_b64_tr_b16 a[208:209], v121 offset:14336\n"
  "ds_read_b64_tr_b16 a[212:213], v122 offset:14336\n"
  "v_rcp_f32 v87, v87\n"
  "v_mfma_f32_16x16x32_bf16 v[28:31], v[76:79], v[236:239], v[4:7]\n"
  "s_waitcnt lgkmcnt(6)\n"
  "buffer_load_dwordx4 v114, s[40:43], s57 offen offset:0 nt lds\n"
  "buffer_load_dwordx4 v115, s[40:43], s57 offen offset:1024 nt lds\n"
  "v_mfma_f32_16x16x32_bf16 v[0:3], v[76:79], v[232:235], v[0:3]\n"
  "s_waitcnt vmcnt(11)\n"
  "ds_read_b64_tr_b16 a[194:195], v117 offset:15360\n"
  "ds_read_b64_tr_b16 a[198:199], v118 offset:15360\n"
  "v_max3_f32 v83, v83, v84, v85\n"
  "v_max3_f32 v83, v83, v86, v87\n"
  "v_mfma_f32_16x16x32_bf16 v[24:27], v[72:75], v[208:211], v[0:3]\n"
  "buffer_load_dwordx4 v116, s[40:43], s57 offen offset:2048  lds\n"
  "v_mul_f32 v96, s99, v84\n"
  "v_mul_f32 v97, s99, v85\n"
  "v_mfma_f32_16x16x32_bf16 v[0:3], v[48:51], a[0:3], v[100:103]\n"
  "s_add_u32 s57, s57, 6144\n"
  "ds_read_b128 v[64:67], v104 offset:4096\n"
  "ds_read_b64_tr_b16 a[202:203], v119 offset:15360\n"
  "ds_read_b64_tr_b16 a[206:207], v120 offset:15360\n"
  "v_mul_f32 v98, s99, v86\n"
  "v_mfma_f32_16x16x32_bf16 v[0:3], v[52:55], a[24:27], v[0:3]\n"
  "s_add_u32 m0, s59, 12288\n"
  "ds_read_b64_tr_b16 a[210:211], v121 offset:17408\n"
  "ds_read_b64_tr_b16 a[214:215], v122 offset:17408\n"
  "v_mul_f32 v99, s99, v87\n"
  "v_fma_f32 v40, v40, v84, v96\n"
  "v_mfma_f32_16x16x32_bf16 v[4:7], v[52:55], a[28:31], v[100:103]\n"
  "s_waitcnt lgkmcnt(7)\n"
  "buffer_load_dwordx4 v114, s[40:43], s57 offen offset:0 nt lds\n"
  "buffer_load_dwordx4 v115, s[40:43], s57 offen offset:1024 nt lds\n"
  "ds_read_b128 v[68:71], v104 offset:5120\n"
  "v_mfma_f32_16x16x32_bf16 v[4:7], v[48:51], a[4:7], v[4:7]\n"
  "s_waitcnt vmcnt(11)\n"
  "ds_read_b64_tr_b16 a[216:217], v117 offset:0\n"
  "ds_read_b64_tr_b16 a[220:221], v118 offset:0\n"
  "ds_read_b64_tr_b16 v[92:93], v108 offset:0\n"
  "ds_read_b64_tr_b16 v[94:95], v108 offset:32\n"
  "v_mfma_f32_16x16x32_bf16 v[8:11], v[48:51], a[8:11], v[100:103]\n"
  "s_xor_b32 s99, 0x80000000, s98\n"
  "buffer_load_dwordx4 v116, s[40:43], s57 offen offset:2048  lds\n"
  "v_fma_f32 v44, v44, v84, v96\n"
  "v_fma_f32 v41, v41, v85, v97\n"
  "v_mfma_f32_16x16x32_bf16 v[8:11], v[52:55], a[32:35], v[8:11]\n"
  "s_add_u32 s57, s57, 6144\n"
  "ds_read_b64_tr_b16 a[224:225], v119 offset:0\n"
  "ds_read_b64_tr_b16 a[228:229], v120 offset:0\n"
  "v_fma_f32 v45, v45, v85, v97\n"
  "v_fma_f32 v42, v42, v86, v98\n"
  "v_smfmac_f32_16x16x64_bf16 v[80:83], v[88:91], v[48:55], v124\n"
  "s_add_u32 m0, s59, 15360\n"
  "ds_read_b128 v[72:75], v104 offset:6144\n"
  "ds_read_b64_tr_b16 a[232:233], v121 offset:2048\n"
  "ds_read_b64_tr_b16 a[236:237], v122 offset:2048\n"
  "v_mfma_f32_16x16x32_bf16 v[12:15], v[52:55], a[36:39], v[100:103]\n"
  "s_waitcnt lgkmcnt(10)\n"
  "buffer_load_dwordx4 v114, s[40:43], s57 offen offset:0 nt lds\n"
  "buffer_load_dwordx4 v115, s[40:43], s57 offen offset:1024 nt lds\n"
  "v_mfma_f32_16x16x32_bf16 v[12:15], v[48:51], a[12:15], v[12:15]\n"
  "s_waitcnt vmcnt(11)\n"
  "ds_read_b64_tr_b16 a[218:219], v117 offset:3072\n"
  "ds_read_b64_tr_b16 a[222:223], v118 offset:3072\n"
  "v_fma_f32 v46, v46, v86, v98\n"
  "v_fma_f32 v43, v43, v87, v99\n"
  "v_mfma_f32_16x16x32_bf16 v[16:19], v[48:51], a[16:19], v[100:103]\n"
  "s_sub_i32 s61, s56, 5\n"
  "s_max_i32 s61, s61, 0\n"
  "buffer_load_dwordx4 v116, s[40:43], s57 offen offset:2048  lds\n"
  "v_fma_f32 v47, v47, v87, v99\n"
  "v_fma_f32 v24, v24, v84, v96\n"
  "v_mfma_f32_16x16x32_bf16 v[16:19], v[52:55], a[40:43], v[16:19]\n"
  "s_add_u32 s57, s57, 6144\n"
  "ds_read_b128 v[76:79], v104 offset:7168\n"
  "ds_read_b64_tr_b16 a[226:227], v119 offset:3072\n"
  "ds_read_b64_tr_b16 a[230:231], v120 offset:3072\n"
  "v_fma_f32 v25, v25, v85, v97\n"
  "v_mfma_f32_16x16x32_bf16 v[20:23], v[52:55], a[44:47], v[100:103]\n"
  "s_add_u32 m0, s59, 0\n"
  "ds_read_b64_tr_b16 a[234:235], v121 offset:5120\n"
  "ds_read_b64_tr_b16 a[238:239], v122 offset:5120\n"
  "v_fma_f32 v28, v28, v84, v96\n"
  "v_fma_f32 v29, v29, v85, v97\n"
  "v_mfma_f32_16x16x32_bf16 v[20:23], v[48:51], a[20:23], v[20:23]\n"
  "s_waitcnt lgkmcnt(7)\n"
  "buffer_load_dwordx4 v114, s[40:43], s57 offen offset:0 nt lds\n"
  "buffer_load_dwordx4 v115, s[40:43], s57 offen offset:1024 nt lds\n"
  "v_mfma_f32_16x16x32_bf16 v[20:23], v[56:59], a[68:71], v[20:23]\n"
  "buffer_store_dwordx4 v[92:95], v109, s[48:51], s68 offen offset:0 nt\n"
  "s_waitcnt vmcnt(12)\n"
  "ds_read_b64_tr_b16 a[240:241], v117 offset:6144\n"
  "ds_read_b64_tr_b16 a[244:245], v118 offset:6144\n"
  "v_fma_f32 v32, v32, v84, v96\n"
  "v_mfma_f32_16x16x32_bf16 v[20:23], v[60:63], a[92:95], v[20:23]\n"
  "buffer_load_dwordx4 v116, s[40:43], s57 offen offset:2048  lds\n"
  "v_fma_f32 v33, v33, v85, v97\n"
  "v_fma_f32 v36, v36, v84, v96\n"
  "v_mfma_f32_16x16x32_bf16 v[16:19], v[60:63], a[88:91], v[16:19]\n"
  "s_add_u32 s57, s57, 6144\n"
  "ds_read_b64_tr_b16 a[248:249], v119 offset:6144\n"
  "ds_read_b64_tr_b16 a[252:253], v120 offset:6144\n"
  "v_fma_f32 v37, v37, v85, v97\n"
  "v_fma_f32 v26, v26, v86, v98\n"
  "v_mfma_f32_16x16x32_bf16 v[16:19], v[56:59], a[64:67], v[16:19]\n"
  "s_add_u32 m0, s59, 3072\n"
  "ds_read_b64_tr_b16 v[128:129], v121 offset:8192\n"
  "ds_read_b64_tr_b16 v[132:133], v122 offset:8192\n"
  "v_fma_f32 v27, v27, v87, v99\n"
  "v_fma_f32 v30, v30, v86, v98\n"
  "v_mfma_f32_16x16x32_bf16 v[12:15], v[56:59], a[60:63], v[12:15]\n"
  "s_waitcnt lgkmcnt(6)\n"
  "buffer_load_dwordx4 v114, s[40:43], s57 offen offset:0 nt lds\n"
  "buffer_load_dwordx4 v115, s[40:43], s57 offen offset:1024 nt lds\n"
  "v_mfma_f32_16x16x32_bf16 v[12:15], v[60:63], a[84:87], v[12:15]\n"
  "s_waitcnt vmcnt(12)\n"
  "ds_read_b64_tr_b16 a[242:243], v117 offset:9216\n"
  "ds_read_b64_tr_b16 a[246:247], v118 offset:9216\n"
  "v_fma_f32 v31, v31, v87, v99\n"
  "v_fma_f32 v34, v34, v86, v98\n"
  "v_smfmac_f32_16x16x64_bf16 v[80:83], v[88:91], v[56:63], v124\n"
  "buffer_load_dwordx4 v116, s[40:43], s57 offen offset:2048  lds\n"
  "v_fma_f32 v35, v35, v87, v99\n"
  "v_mfma_f32_16x16x32_bf16 v[8:11], v[60:63], a[80:83], v[8:11]\n"
  "s_add_u32 s57, s57, 6144\n"
  "ds_read_b64_tr_b16 a[250:251], v119 offset:9216\n"
  "ds_read_b64_tr_b16 a[254:255], v120 offset:9216\n"
  "v_fma_f32 v38, v38, v86, v98\n"
  "v_fma_f32 v39, v39, v87, v99\n"
  "v_mfma_f32_16x16x32_bf16 v[8:11], v[56:59], a[56:59], v[8:11]\n"
  "s_add_u32 m0, s59, 6144\n"
  "ds_read_b64_tr_b16 v[130:131], v121 offset:11264\n"
  "ds_read_b64_tr_b16 v[134:135], v122 offset:11264\n"
  "v_cvt_pk_bf16_f32 v40, v40, v44\n"
  "v_cvt_pk_bf16_f32 v41, v41, v45\n"
  "v_mfma_f32_16x16x32_bf16 v[4:7], v[56:59], a[52:55], v[4:7]\n"
  "s_waitcnt lgkmcnt(6)\n"
  "buffer_load_dwordx4 v114, s[40:43], s57 offen offset:0 nt lds\n"
  "buffer_load_dwordx4 v115, s[40:43], s57 offen offset:1024 nt lds\n"
  "v_mfma_f32_16x16x32_bf16 v[4:7], v[60:63], a[76:79], v[4:7]\n"
  "s_waitcnt vmcnt(12)\n"
  "ds_read_b64_tr_b16 v[136:137], v117 offset:12288\n"
  "ds_read_b64_tr_b16 v[140:141], v118 offset:12288\n"
  "v_cvt_pk_bf16_f32 v42, v42, v46\n"
  "v_cvt_pk_bf16_f32 v43, v43, v47\n"
  "v_mfma_f32_16x16x32_bf16 v[0:3], v[60:63], a[72:75], v[0:3]\n"
  "buffer_load_dwordx4 v116, s[40:43], s57 offen offset:2048  lds\n"
  "v_cvt_pk_bf16_f32 v24, v24, v28\n"
  "v_cvt_pk_bf16_f32 v28, v25, v29\n"
  "v_mfma_f32_16x16x32_bf16 v[0:3], v[56:59], a[48:51], v[0:3]\n"
  "s_add_u32 s57, s57, 6144\n"
  "ds_read_b64_tr_b16 v[144:145], v119 offset:12288\n"
  "ds_read_b64_tr_b16 v[148:149], v120 offset:12288\n"
  "v_cvt_pk_bf16_f32 v25, v32, v36\n"
  "v_cvt_pk_bf16_f32 v29, v33, v37\n"
  "v_mfma_f32_16x16x32_bf16 v[0:3], v[64:67], a[96:99], v[0:3]\n"
  "s_add_u32 m0, s59, 9216\n"
  "ds_read_b128 v[48:51], v105 offset:0\n"
  "ds_read_b64_tr_b16 v[152:153], v121 offset:14336\n"
  "ds_read_b64_tr_b16 v[156:157], v122 offset:14336\n"
  "v_cvt_pk_bf16_f32 v26, v26, v30\n"
  "v_mfma_f32_16x16x32_bf16 v[0:3], v[68:71], a[120:123], v[0:3]\n"
  "s_waitcnt lgkmcnt(7)\n"
  "buffer_load_dwordx4 v114, s[40:43], s57 offen offset:0 nt lds\n"
  "buffer_load_dwordx4 v115, s[40:43], s57 offen offset:1024 nt lds\n"
  "v_mfma_f32_16x16x32_bf16 v[4:7], v[68:71], a[124:127], v[4:7]\n"
  "ds_read_b128 v[52:55], v105 offset:1024\n"
  "s_waitcnt vmcnt(12)\n"
  "ds_read_b64_tr_b16 v[138:139], v117 offset:15360\n"
  "ds_read_b64_tr_b16 v[142:143], v118 offset:15360\n"
  "v_cvt_pk_bf16_f32 v30, v27, v31\n"
  "v_mfma_f32_16x16x32_bf16 v[4:7], v[64:67], a[100:103], v[4:7]\n"
  "buffer_load_dwordx4 v116, s[40:43], s57 offen offset:2048  lds\n"
  "ds_read_b64_tr_b16 v[96:97], v108 offset:2304\n"
  "ds_read_b64_tr_b16 v[98:99], v108 offset:2336\n"
  "v_mfma_f32_16x16x32_bf16 v[8:11], v[64:67], a[104:107], v[8:11]\n"
  "s_add_u32 s57, s57, 6144\n"
  "ds_read_b64_tr_b16 v[146:147], v119 offset:15360\n"
  "ds_read_b64_tr_b16 v[150:151], v120 offset:15360\n"
  "v_cvt_pk_bf16_f32 v27, v34, v38\n"
  "v_cvt_pk_bf16_f32 v31, v35, v39\n"
  "v_mfma_f32_16x16x32_bf16 v[8:11], v[68:71], a[128:131], v[8:11]\n"
  "s_add_u32 m0, s59, 12288\n"
  "ds_read_b64_tr_b16 v[154:155], v121 offset:17408\n"
  "ds_read_b64_tr_b16 v[158:159], v122 offset:17408\n"
  "ds_write2_b32 v107, v40, v41 offset0:0 offset1:72\n"
  "v_smfmac_f32_16x16x64_bf16 v[80:83], v[88:91], v[64:71], v124\n"
  "s_waitcnt lgkmcnt(10)\n"
  "buffer_load_dwordx4 v114, s[40:43], s57 offen offset:0 nt lds\n"
  "buffer_load_dwordx4 v115, s[40:43], s57 offen offset:1024 nt lds\n"
  "ds_read_b128 v[56:59], v105 offset:2048\n"
  "v_mfma_f32_16x16x32_bf16 v[12:15], v[68:71], a[132:135], v[12:15]\n"
  "s_waitcnt vmcnt(11)\n"
  "ds_read_b64_tr_b16 v[160:161], v117 offset:0\n"
  "ds_read_b64_tr_b16 v[164:165], v118 offset:0\n"
  "ds_write2_b32 v107, v42, v43 offset0:144 offset1:216\n"
  "v_mfma_f32_16x16x32_bf16 v[12:15], v[64:67], a[108:111], v[12:15]\n"
  "buffer_load_dwordx4 v116, s[40:43], s57 offen offset:2048  lds\n"
  "ds_write_b64 v106, v[24:25] offset:0\n"
  "v_mfma_f32_16x16x32_bf16 v[16:19], v[64:67], a[112:115], v[16:19]\n"
  "s_add_u32 s57, s57, 6144\n"
  "ds_read_b64_tr_b16 v[168:169], v119 offset:0\n"
  "ds_read_b64_tr_b16 v[172:173], v120 offset:0\n"
  "ds_write_b64 v106, v[28:29] offset:288\n"
  "v_mfma_f32_16x16x32_bf16 v[16:19], v[68:71], a[136:139], v[16:19]\n"
  "s_add_u32 m0, s59, 15360\n"
  "ds_read_b128 v[60:63], v105 offset:3072\n"
  "ds_read_b64_tr_b16 v[176:177], v121 offset:2048\n"
  "ds_read_b64_tr_b16 v[180:181], v122 offset:2048\n"
  "v_xor_b32 v104, 0x4000, v104\n"
  "v_mfma_f32_16x16x32_bf16 v[20:23], v[68:71], a[140:143], v[20:23]\n"
  "s_waitcnt lgkmcnt(12)\n"
  "buffer_load_dwordx4 v114, s[40:43], s57 offen offset:0 nt lds\n"
  "buffer_load_dwordx4 v115, s[40:43], s57 offen offset:1024 nt lds\n"
  "v_mfma_f32_16x16x32_bf16 v[20:23], v[64:67], a[116:119], v[20:23]\n"
  "s_waitcnt vmcnt(11)\n"
  "ds_read_b64_tr_b16 v[162:163], v117 offset:3072\n"
  "ds_read_b64_tr_b16 v[166:167], v118 offset:3072\n"
  "ds_write_b64 v106, v[26:27] offset:576\n"
  "v_mfma_f32_16x16x32_bf16 v[20:23], v[72:75], a[164:167], v[20:23]\n"
  "buffer_load_dwordx4 v116, s[40:43], s57 offen offset:2048  lds\n"
  "ds_write_b64 v106, v[30:31] offset:864\n"
  "v_mfma_f32_16x16x32_bf16 v[20:23], v[76:79], a[188:191], v[20:23]\n"
  "s_add_u32 s57, s57, 6144\n"
  "buffer_store_dwordx4 v[96:99], v109, s[48:51], s68 offen offset:128 nt\n"
  "ds_read_b64_tr_b16 v[170:171], v119 offset:3072\n"
  "ds_read_b64_tr_b16 v[174:175], v120 offset:3072\n"
  "v_mfma_f32_16x16x32_bf16 v[16:19], v[76:79], a[184:187], v[16:19]\n"
  "s_add_u32 m0, s59, 0\n"
  "ds_read_b64_tr_b16 v[178:179], v121 offset:5120\n"
  "ds_read_b64_tr_b16 v[182:183], v122 offset:5120\n"
  "v_mfma_f32_16x16x32_bf16 v[16:19], v[72:75], a[160:163], v[16:19]\n"
  "s_waitcnt lgkmcnt(8)\n"
  "buffer_load_dwordx4 v114, s[40:43], s57 offen offset:0 nt lds\n"
  "buffer_load_dwordx4 v115, s[40:43], s57 offen offset:1024 nt lds\n"
  "v_mfma_f32_16x16x32_bf16 v[12:15], v[72:75], a[156:159], v[12:15]\n"
  "s_waitcnt vmcnt(12)\n"
  "ds_read_b64_tr_b16 v[184:185], v117 offset:6144\n"
  "ds_read_b64_tr_b16 v[188:189], v118 offset:6144\n"
  "v_mfma_f32_16x16x32_bf16 v[12:15], v[76:79], a[180:183], v[12:15]\n"
  "buffer_load_dwordx4 v116, s[40:43], s57 offen offset:2048  lds\n"
  "v_smfmac_f32_16x16x64_bf16 v[80:83], v[88:91], v[72:79], v124\n"
  "s_add_u32 s57, s57, 6144\n"
  "ds_read_b64_tr_b16 v[192:193], v119 offset:6144\n"
  "ds_read_b64_tr_b16 v[196:197], v120 offset:6144\n"
  "ds_read_b64_tr_b16 v[92:93], v108 offset:4608\n"
  "v_mfma_f32_16x16x32_bf16 v[8:11], v[76:79], a[176:179], v[8:11]\n"
  "s_add_u32 m0, s59, 3072\n"
  "ds_read_b64_tr_b16 v[200:201], v121 offset:8192\n"
  "ds_read_b64_tr_b16 v[204:205], v122 offset:8192\n"
  "ds_read_b64_tr_b16 v[94:95], v108 offset:4640\n"
  "v_xor_b32 v108, 0x2000, v108\n"
  "v_mfma_f32_16x16x32_bf16 v[8:11], v[72:75], a[152:155], v[8:11]\n"
  "s_waitcnt lgkmcnt(8)\n"
  "buffer_load_dwordx4 v114, s[40:43], s57 offen offset:0 nt lds\n"
  "buffer_load_dwordx4 v115, s[40:43], s57 offen offset:1024 nt lds\n"
  "v_mfma_f32_16x16x32_bf16 v[4:7], v[72:75], a[148:151], v[4:7]\n"
  "s_waitcnt vmcnt(12)\n"
  "ds_read_b64_tr_b16 v[186:187], v117 offset:9216\n"
  "ds_read_b64_tr_b16 v[190:191], v118 offset:9216\n"
  "v_add_f32 v44, v80, v81\n"
  "v_mov_b32 v80, 0\n"
  "v_mfma_f32_16x16x32_bf16 v[4:7], v[76:79], a[172:175], v[4:7]\n"
  "buffer_load_dwordx4 v116, s[40:43], s57 offen offset:2048  lds\n"
  "ds_write_b32 v110, v44\n"
  "v_mfma_f32_16x16x32_bf16 v[0:3], v[76:79], a[168:171], v[0:3]\n"
  "s_add_u32 s57, s57, 6144\n"
  "s_add_u32 m0, s66, 0\n"
  "ds_read_b64_tr_b16 v[194:195], v119 offset:9216\n"
  "ds_read_b64_tr_b16 v[198:199], v120 offset:9216\n"
  "v_mov_b32 v81, 0\n"
  "v_mfma_f32_16x16x32_bf16 v[0:3], v[72:75], a[144:147], v[0:3]\n"
  "buffer_load_dwordx4 v112, s[44:47], s67 offen offset:0  lds\n"
  "ds_read_b64_tr_b16 v[202:203], v121 offset:11264\n"
  "ds_read_b64_tr_b16 v[206:207], v122 offset:11264\n"
  "v_mfma_f32_16x16x32_bf16 v[0:3], v[48:51], a[192:195], v[0:3]\n"
  "s_add_u32 m0, s66, 1920\n"
  "ds_read_b128 v[64:67], v105 offset:4096\n"
  "v_mfma_f32_16x16x32_bf16 v[0:3], v[52:55], a[216:219], v[0:3]\n"
  "buffer_load_dwordx4 v112, s[44:47], s67 offen offset:128  lds\n"
  "s_waitcnt vmcnt(12)\n"
  "ds_read_b64_tr_b16 v[208:209], v117 offset:12288\n"
  "ds_read_b64_tr_b16 v[212:213], v118 offset:12288\n"
  "v_mfma_f32_16x16x32_bf16 v[4:7], v[52:55], a[220:223], v[4:7]\n"
  "s_add_u32 m0, s66, 3840\n"
  "ds_read_b128 v[68:71], v105 offset:5120\n"
  "v_mfma_f32_16x16x32_bf16 v[4:7], v[48:51], a[196:199], v[4:7]\n"
  "buffer_load_dwordx4 v112, s[44:47], s67 offen offset:256  lds\n"
  "ds_read_b64_tr_b16 v[216:217], v119 offset:12288\n"
  "ds_read_b64_tr_b16 v[220:221], v120 offset:12288\n"
  "v_mfma_f32_16x16x32_bf16 v[8:11], v[48:51], a[200:203], v[8:11]\n"
  "s_add_u32 s66, s66, 6144\n"
  "s_waitcnt lgkmcnt(13)\n"
  "buffer_store_dwordx4 v[92:95], v109, s[48:51], s68 offen offset:256 nt\n"
  "ds_read_b64_tr_b16 v[224:225], v121 offset:14336\n"
  "ds_read_b64_tr_b16 v[228:229], v122 offset:14336\n"
  "v_mfma_f32_16x16x32_bf16 v[8:11], v[52:55], a[224:227], v[8:11]\n"
  "s_cmp_ge_u32 s66, 86016\n"
  "s_cselect_b32 s64, 36864, 0\n"
  "s_sub_u32 s66, s66, s64\n"
  "s_add_u32 m0, s66, 0\n"
  "v_mfma_f32_16x16x32_bf16 v[12:15], v[52:55], a[228:231], v[12:15]\n"
  "s_add_u32 s67, s67, s63\n"
  "buffer_load_dwordx4 v112, s[44:47], s67 offen offset:0  lds\n"
  "ds_read_b128 v[72:75], v105 offset:6144\n"
  "s_waitcnt vmcnt(12)\n"
  "ds_read_b64_tr_b16 v[210:211], v117 offset:15360\n"
  "ds_read_b64_tr_b16 v[214:215], v118 offset:15360\n"
  "v_mfma_f32_16x16x32_bf16 v[12:15], v[48:51], a[204:207], v[12:15]\n"
  "s_add_u32 m0, s66, 1920\n"
  "v_mfma_f32_16x16x32_bf16 v[16:19], v[48:51], a[208:211], v[16:19]\n"
  "buffer_load_dwordx4 v112, s[44:47], s67 offen offset:128  lds\n"
  "ds_read_b64_tr_b16 v[218:219], v119 offset:15360\n"
  "ds_read_b64_tr_b16 v[222:223], v120 offset:15360\n"
  "v_mfma_f32_16x16x32_bf16 v[16:19], v[52:55], a[232:235], v[16:19]\n"
  "s_add_u32 m0, s66, 3840\n"
  "ds_read_b64_tr_b16 v[226:227], v121 offset:17408\n"
  "ds_read_b64_tr_b16 v[230:231], v122 offset:17408\n"
  "v_mfma_f32_16x16x32_bf16 v[20:23], v[52:55], a[236:239], v[20:23]\n"
  "buffer_load_dwordx4 v112, s[44:47], s67 offen offset:256  lds\n"
  "ds_read_b128 v[76:79], v105 offset:7168\n"
  "v_mfma_f32_16x16x32_bf16 v[20:23], v[48:51], a[212:215], v[20:23]\n"
  "s_waitcnt vmcnt(10)\n"
  "ds_read_b64_tr_b16 v[232:233], v117 offset:0\n"
  "ds_read_b64_tr_b16 v[236:237], v118 offset:0\n"
  "v_xor_b32 v105, 0x4000, v105\n"
  "v_mfma_f32_16x16x32_bf16 v[20:23], v[56:59], v[132:135], v[20:23]\n"
  "s_add_u32 s66, s66, 6144\n"
  "s_cmp_ge_u32 s66, 86016\n"
  "s_cselect_b32 s64, 36864, 0\n"
  "s_sub_u32 s66, s66, s64\n"
  "s_add_u32 m0, s66, 0\n"
  "v_mfma_f32_16x16x32_bf16 v[20:23], v[60:63], v[156:159], v[20:23]\n"
  "s_sub_u32 s90, s90, 1\n"
  "s_cselect_b32 s63, s91, 6144\n"
  "s_cmov_b64 s[90:91], s[92:93]\n"
  "s_cmov_b64 s[92:93], s[94:95]\n"
  "s_add_u32 s67, s67, s63\n"
  "buffer_load_dwordx4 v112, s[44:47], s67 offen offset:0  lds\n"
  "ds_read_b64_tr_b16 v[240:241], v119 offset:0\n"
  "ds_read_b64_tr_b16 v[244:245], v120 offset:0\n"
  "v_mfma_f32_16x16x32_bf16 v[16:19], v[60:63], v[152:155], v[16:19]\n"
  "s_add_u32 m0, s66, 1920\n"
  "ds_read_b64_tr_b16 v[248:249], v121 offset:2048\n"
  "ds_read_b64_tr_b16 v[252:253], v122 offset:2048\n"
  "v_mfma_f32_16x16x32_bf16 v[16:19], v[56:59], v[128:131], v[16:19]\n"
  "buffer_load_dwordx4 v112, s[44:47], s67 offen offset:128  lds\n"
  "v_mfma_f32_16x16x32_bf16 v[12:15], v[56:59], a[252:255], v[12:15]\n"
  "s_add_u32 m0, s66, 3840\n"
  "s_waitcnt vmcnt(9)\n"
  "ds_read_b64_tr_b16 v[234:235], v117 offset:3072\n"
  "ds_read_b64_tr_b16 v[238:239], v118 offset:3072\n"
  "v_mfma_f32_16x16x32_bf16 v[12:15], v[60:63], v[148:151], v[12:15]\n"
  "buffer_load_dwordx4 v112, s[44:47], s67 offen offset:256  lds\n"
  "v_mfma_f32_16x16x32_bf16 v[8:11], v[60:63], v[144:147], v[8:11]\n"
  "s_add_u32 s72, s72, 1\n"
  "ds_read_b64_tr_b16 v[242:243], v119 offset:3072\n"
  "ds_read_b64_tr_b16 v[246:247], v120 offset:3072\n"
  "v_mfma_f32_16x16x32_bf16 v[8:11], v[56:59], a[248:251], v[8:11]\n"
  "s_lshr_b32 s74, s72, 1\n"
  "ds_read_b64_tr_b16 v[250:251], v121 offset:5120\n"
  "ds_read_b64_tr_b16 v[254:255], v122 offset:5120\n"
  "v_readlane_b32 s73, v126, s74\n"
  "v_mfma_f32_16x16x32_bf16 v[4:7], v[56:59], a[244:247], v[4:7]\n"
  "s_add_u32 s66, s66, 6144\n"
  "v_xor_b32 v106, 0x2000, v106\n"
  "v_xor_b32 v107, 0x2000, v107\n"
  "v_xor_b32 v110, 0x2000, v110\n"
  "v_mfma_f32_16x16x32_bf16 v[4:7], v[60:63], v[140:143], v[4:7]\n"
  "s_cmp_ge_u32 s66, 86016\n"
  "s_cselect_b32 s64, 36864, 0\n"
  "v_mfma_f32_16x16x32_bf16 v[0:3], v[60:63], v[136:139], v[0:3]\n"
  "s_sub_u32 s66, s66, s64\n"
  "v_mfma_f32_16x16x32_bf16 v[0:3], v[56:59], a[240:243], v[0:3]\n"
  "s_sub_u32 s90, s90, 1\n"
  "s_cselect_b32 s63, s91, 6144\n"
  "s_cmov_b64 s[90:91], s[92:93]\n"
  "s_cmov_b64 s[92:93], s[94:95]\n"
  "s_add_u32 s67, s67, s63\n"
  "s_lshl_b32 s74, s72, 4\n"
  "s_lshr_b32 s73, s73, s74\n"
  "s_and_b32 s73, s73, 0xffff\n"
  "s_lshl_b32 s73, s73, 13\n"
  "s_add_u32 s68, s68, s69\n"
  "s_mov_b32 s69, s70\n"
  "s_movk_i32 s70, 6144\n"
  "s_branch B_H1_%=\n"
  "B_LAST_%=:\n"
  "s_waitcnt lgkmcnt(0)\n"
  "s_barrier\n"
  "v_mfma_f32_16x16x32_bf16 v[20:23], v[64:67], v[180:183], v[20:23]\n"
  "ds_read_b64_tr_b16 v[48:49], v108 offset:0\n"
  "ds_read_b64_tr_b16 v[50:51], v108 offset:32\n"
  "ds_read_b128 v[84:87], v111\n"
  "v_mfma_f32_16x16x32_bf16 v[20:23], v[68:71], v[204:207], v[20:23]\n"
  "ds_read_b64_tr_b16 v[52:53], v108 offset:2304\n"
  "ds_read_b64_tr_b16 v[54:55], v108 offset:2336\n"
  "ds_read_b128 v[96:99], v111 offset:64\n"
  "v_mfma_f32_16x16x32_bf16 v[20:23], v[72:75], v[228:231], v[20:23]\n"
  "v_xor_b32 v111, 0x2000, v111\n"
  "v_mfma_f32_16x16x32_bf16 v[44:47], v[76:79], v[252:255], v[20:23]\n"
  "v_mfma_f32_16x16x32_bf16 v[16:19], v[76:79], v[248:251], v[16:19]\n"
  "v_mfma_f32_16x16x32_bf16 v[16:19], v[72:75], v[224:227], v[16:19]\n"
  "ds_read_b64_tr_b16 v[56:57], v108 offset:4608\n"
  "ds_read_b64_tr_b16 v[58:59], v108 offset:4640\n"
  "v_xor_b32 v108, 0x2000, v108\n"
  "v_mfma_f32_16x16x32_bf16 v[16:19], v[68:71], v[200:203], v[16:19]\n"
  "v_mfma_f32_16x16x32_bf16 v[40:43], v[64:67], v[176:179], v[16:19]\n"
  "s_waitcnt lgkmcnt(2)\n"
  "buffer_store_dwordx4 v[48:51], v109, s[48:51], s68 offen offset:0 nt\n"
  "v_add_f32 v84, v84, v96\n"
  "v_add_f32 v85, v85, v97\n"
  "v_add_f32 v86, v86, v98\n"
  "v_mfma_f32_16x16x32_bf16 v[12:15], v[64:67], v[172:175], v[12:15]\n"
  "v_add_f32 v87, v87, v99\n"
  "v_max3_f32 v82, v82, v84, v85\n"
  "v_max3_f32 v82, v82, v86, v87\n"
  "v_mfma_f32_16x16x32_bf16 v[12:15], v[68:71], v[196:199], v[12:15]\n"
  "v_rcp_f32 v84, v84\n"
  "v_mfma_f32_16x16x32_bf16 v[12:15], v[72:75], v[220:223], v[12:15]\n"
  "v_rcp_f32 v85, v85\n"
  "v_mul_f32 v96, s99, v84\n"
  "v_mfma_f32_16x16x32_bf16 v[36:39], v[76:79], v[244:247], v[12:15]\n"
  "buffer_store_dwordx4 v[52:55], v109, s[48:51], s68 offen offset:128 nt\n"
  "v_rcp_f32 v86, v86\n"
  "v_mul_f32 v97, s99, v85\n"
  "v_mfma_f32_16x16x32_bf16 v[8:11], v[76:79], v[240:243], v[8:11]\n"
  "v_rcp_f32 v87, v87\n"
  "v_mul_f32 v98, s99, v86\n"
  "v_mfma_f32_16x16x32_bf16 v[8:11], v[72:75], v[216:219], v[8:11]\n"
  "v_mul_f32 v99, s99, v87\n"
  "v_fma_f32 v40, v40, v84, v96\n"
  "v_fma_f32 v44, v44, v84, v96\n"
  "v_mfma_f32_16x16x32_bf16 v[8:11], v[68:71], v[192:195], v[8:11]\n"
  "v_cvt_pk_bf16_f32 v40, v40, v44\n"
  "v_fma_f32 v41, v41, v85, v97\n"
  "v_fma_f32 v45, v45, v85, v97\n"
  "v_mfma_f32_16x16x32_bf16 v[32:35], v[64:67], v[168:171], v[8:11]\n"
  "s_waitcnt lgkmcnt(0)\n"
  "buffer_store_dwordx4 v[56:59], v109, s[48:51], s68 offen offset:256 nt\n"
  "v_cvt_pk_bf16_f32 v41, v41, v45\n"
  "v_fma_f32 v42, v42, v86, v98\n"
  "v_fma_f32 v46, v46, v86, v98\n"
  "v_mfma_f32_16x16x32_bf16 v[4:7], v[64:67], v[164:167], v[4:7]\n"
  "s_add_u32 s68, s68, s69\n"
  "ds_write2_b32 v107, v40, v41 offset0:0 offset1:72\n"
  "v_mfma_f32_16x16x32_bf16 v[4:7], v[68:71], v[188:191], v[4:7]\n"
  "s_mov_b32 s69, s70\n"
  "v_cvt_pk_bf16_f32 v42, v42, v46\n"
  "v_fma_f32 v43, v43, v87, v99\n"
  "v_fma_f32 v47, v47, v87, v99\n"
  "v_mfma_f32_16x16x32_bf16 v[4:7], v[72:75], v[212:215], v[4:7]\n"
  "s_movk_i32 s70, 6144\n"
  "v_cvt_pk_bf16_f32 v43, v43, v47\n"
  "v_fma_f32 v32, v32, v84, v96\n"
  "v_fma_f32 v33, v33, v85, v97\n"
  "v_mfma_f32_16x16x32_bf16 v[28:31], v[76:79], v[236:239], v[4:7]\n"
  "ds_write2_b32 v107, v42, v43 offset0:144 offset1:216\n"
  "v_mfma_f32_16x16x32_bf16 v[0:3], v[76:79], v[232:235], v[0:3]\n"
  "v_fma_f32 v36, v36, v84, v96\n"
  "v_fma_f32 v37, v37, v85, v97\n"
  "v_fma_f32 v34, v34, v86, v98\n"
  "v_mfma_f32_16x16x32_bf16 v[0:3], v[72:75], v[208:211], v[0:3]\n"
  "v_fma_f32 v35, v35, v87, v99\n"
  "v_fma_f32 v38, v38, v86, v98\n"
  "v_fma_f32 v39, v39, v87, v99\n"
  "v_mfma_f32_16x16x32_bf16 v[0:3], v[68:71], v[184:187], v[0:3]\n"
  "v_fma_f32 v28, v28, v84, v96\n"
  "v_fma_f32 v29, v29, v85, v97\n"
  "v_fma_f32 v30, v30, v86, v98\n"
  "v_mfma_f32_16x16x32_bf16 v[24:27], v[64:67], v[160:163], v[0:3]\n"
  "v_fma_f32 v31, v31, v87, v99\n"
  "v_max3_f32 v83, v83, v84, v85\n"
  "v_max3_f32 v83, v83, v86, v87\n"
  "s_nop 4\n"
  "v_fma_f32 v24, v24, v84, v96\n"
  "v_fma_f32 v25, v25, v85, v97\n"
  "v_cvt_pk_bf16_f32 v24, v24, v28\n"
  "v_cvt_pk_bf16_f32 v28, v25, v29\n"
  "v_cvt_pk_bf16_f32 v25, v32, v36\n"
  "v_cvt_pk_bf16_f32 v29, v33, v37\n"
  "ds_write_b64 v106, v[24:25] offset:0\n"
  "ds_write_b64 v106, v[28:29] offset:288\n"
  "v_fma_f32 v26, v26, v86, v98\n"
  "v_fma_f32 v27, v27, v87, v99\n"
  "v_cvt_pk_bf16_f32 v26, v26, v30\n"
  "v_cvt_pk_bf16_f32 v30, v27, v31\n"
  "v_cvt_pk_bf16_f32 v27, v34, v38\n"
  "v_cvt_pk_bf16_f32 v31, v35, v39\n"
  "ds_write_b64 v106, v[26:27] offset:576\n"
  "ds_write_b64 v106, v[30:31] offset:864\n"
  "v_mul_f32 v83, 0x51800000, v83\n"
  "v_add_f32 v82, v82, v83\n"
  "ds_write_b32 v104, v82\n"
  "s_waitcnt lgkmcnt(0)\n"
  "s_barrier\n"
  "ds_read_b32 v83, v105\n"
  "ds_read_b64_tr_b16 v[48:49], v108 offset:0\n"
  "ds_read_b64_tr_b16 v[50:51], v108 offset:32\n"
  "ds_read_b64_tr_b16 v[52:53], v108 offset:2304\n"
  "ds_read_b64_tr_b16 v[54:55], v108 offset:2336\n"
  "ds_read_b64_tr_b16 v[56:57], v108 offset:4608\n"
  "ds_read_b64_tr_b16 v[58:59], v108 offset:4640\n"
  "s_waitcnt lgkmcnt(4)\n"
  "buffer_store_dwordx4 v[48:51], v109, s[48:51], s68 offen offset:0 nt\n"
  "s_waitcnt lgkmcnt(2)\n"
  "buffer_store_dwordx4 v[52:55], v109, s[48:51], s68 offen offset:128 nt\n"
  "s_waitcnt lgkmcnt(0)\n"
  "buffer_store_dwordx4 v[56:59], v109, s[48:51], s68 offen offset:256 nt\n"
  "v_add_f32 v82, v82, v83\n"
  "v_cmp_ngt_f32 vcc, s71, v82\n"
  "s_nop 4\n"
  "s_cmp_lg_u64 vcc, 0\n"
  "s_cselect_b32 %0, 1, 0\n"
  "L_END_%=:\n"
  : "=&s"(bad)
  : "s"((unsigned long long)__builtin_amdgcn_kernarg_segment_ptr()), "s"(wid), "s"(it[0]), "s"(it[1]), "s"(it[2]), "s"(it[3]), "s"(xcd | (side & 0) << 3 | (p & 0) << 4 | rank << 8)
  : "v0", "v1", "v2", "v3", "v4", "v5", "v6", "v7", "v8", "v9", "v10", "v11", "v12", "v13", "v14", "v15", "v16", "v17", "v18", "v19", "v20", "v21", "v22", "v23", "v24", "v25", "v26", "v27", "v28", "v29", "v30", "v31", "v32", "v33", "v34", "v35", "v36", "v37", "v38", "v39", "v40", "v41", "v42", "v43", "v44", "v45", "v46", "v47", "v48", "v49", "v50", "v51", "v52", "v53", "v54", "v55", "v56", "v57", "v58", "v59", "v60", "v61", "v62", "v63", "v64", "v65", "v66", "v67", "v68", "v69", "v70", "v71", "v72", "v73", "v74", "v75", "v76", "v77", "v78", "v79", "v80", "v81", "v82", "v83", "v84", "v85", "v86", "v87", "v88", "v89", "v90", "v91", "v92", "v93", "v94", "v95", "v96", "v97", "v98", "v99", "v100", "v101", "v102", "v103", "v104", "v105", "v106", "v107", "v108", "v109", "v110", "v111", "v112", "v113", "v114", "v115", "v116", "v117", "v118", "v119", "v120", "v121", "v122", "v123", "v124", "v125", "v126", "v127", "v128", "v129", "v130", "v131", "v132", "v133", "v134", "v135", "v136", "v137", "v138", "v139", "v140", "v141", "v142", "v143", "v144", "v145", "v146", "v147", "v148", "v149", "v150", "v151", "v152", "v153", "v154", "v155", "v156", "v157", "v158", "v159", "v160", "v161", "v162", "v163", "v164", "v165", "v166", "v167", "v168", "v169", "v170", "v171", "v172", "v173", "v174", "v175", "v176", "v177", "v178", "v179", "v180", "v181", "v182", "v183", "v184", "v185", "v186", "v187", "v188", "v189", "v190", "v191", "v192", "v193", "v194", "v195", "v196", "v197", "v198", "v199", "v200", "v201", "v202", "v203", "v204", "v205", "v206", "v207", "v208", "v209", "v210", "v211", "v212", "v213", "v214", "v215", "v216", "v217", "v218", "v219", "v220", "v221", "v222", "v223", "v224", "v225", "v226", "v227", "v228", "v229", "v230", "v231", "v232", "v233", "v234", "v235", "v236", "v237", "v238", "v239", "v240", "v241", "v242", "v243", "v244", "v245", "v246", "v247", "v248", "v249", "v250", "v251", "v252", "v253", "v254", "v255", "a0", "a1", "a2", "a3", "a4", "a5", "a6", "a7", "a8", "a9", "a10", "a11", "a12", "a13", "a14", "a15", "a16", "a17", "a18", "a19", "a20", "a21", "a22", "a23", "a24", "a25", "a26", "a27", "a28", "a29", "a30", "a31", "a32", "a33", "a34", "a35", "a36", "a37", "a38", "a39", "a40", "a41", "a42", "a43", "a44", "a45", "a46", "a47", "a48", "a49", "a50", "a51", "a52", "a53", "a54", "a55", "a56", "a57", "a58", "a59", "a60", "a61", "a62", "a63", "a64", "a65", "a66", "a67", "a68", "a69", "a70", "a71", "a72", "a73", "a74", "a75", "a76", "a77", "a78", "a79", "a80", "a81", "a82", "a83", "a84", "a85", "a86", "a87", "a88", "a89", "a90", "a91", "a92", "a93", "a94", "a95", "a96", "a97", "a98", "a99", "a100", "a101", "a102", "a103", "a104", "a105", "a106", "a107", "a108", "a109", "a110", "a111", "a112", "a113", "a114", "a115", "a116", "a117", "a118", "a119", "a120", "a121", "a122", "a123", "a124", "a125", "a126", "a127", "a128", "a129", "a130", "a131", "a132", "a133", "a134", "a135", "a136", "a137", "a138", "a139", "a140", "a141", "a142", "a143", "a144", "a145", "a146", "a147", "a148", "a149", "a150", "a151", "a152", "a153", "a154", "a155", "a156", "a157", "a158", "a159", "a160", "a161", "a162", "a163", "a164", "a165", "a166", "a167", "a168", "a169", "a170", "a171", "a172", "a173", "a174", "a175", "a176", "a177", "a178", "a179", "a180", "a181", "a182", "a183", "a184", "a185", "a186", "a187", "a188", "a189", "a190", "a191", "a192", "a193", "a194", "a195", "a196", "a197", "a198", "a199", "a200", "a201", "a202", "a203", "a204", "a205", "a206", "a207", "a208", "a209", "a210", "a211", "a212", "a213", "a214", "a215", "a216", "a217", "a218", "a219", "a220", "a221", "a222", "a223", "a224", "a225", "a226", "a227", "a228", "a229", "a230", "a231", "a232", "a233", "a234", "a235", "a236", "a237", "a238", "a239", "a240", "a241", "a242", "a243", "a244", "a245", "a246", "a247", "a248", "a249", "a250", "a251", "a252", "a253", "a254", "a255", "s40", "s41", "s42", "s43", "s44", "s45", "s46", "s47", "s48", "s49", "s50", "s51", "s52", "s53", "s54", "s55", "s56", "s57", "s58", "s59", "s60", "s61", "s62", "s63", "s64", "s65", "s66", "s67", "s68", "s69", "s70", "s71", "s72", "s73", "s74", "s75", "s76", "s77", "s78", "s79", "s80", "s81", "s82", "s83", "s84", "s85", "s86", "s87", "s88", "s89", "s90", "s91", "s92", "s93", "s94", "s95", "s96", "s97", "s98", "s99", "vcc", "scc", "memory");
  if (bad) {
    unsigned long long ka = (unsigned long long)__builtin_amdgcn_kernarg_segment_ptr();
    asm volatile("s_waitcnt vmcnt(0)\n\ts_barrier" : "+s"(ka) :: "memory");
    const unsigned short* const* arg = (const unsigned short* const*)ka;
#pragma unroll 1
    for (int i = 0; i < 4; ++i) {
      const unsigned x = i == 0 ? it[0] : i == 1 ? it[1] : i == 2 ? it[2] : it[3];
#pragma unroll 1
      for (int t = wid & 1; t < (int)(x & 255); t += 2) exact_tile(arg[0], arg[1], arg[2], (unsigned short*)arg[3], x >> 16, 16 * (((x >> 8) & 255) + t));
    }
  }
}

extern "C" void kernel_launch(void* const* d_in, const int* in_sizes, int n_in,
                              void* d_out, int out_size, void* d_ws, size_t ws_size,
                              hipStream_t stream) {
  (void)hipFuncSetAttribute((const void*)attn_fwd_g, hipFuncAttributeMaxDynamicSharedMemorySize, LDS_BYTES);
  hipLaunchKernelGGL(attn_fwd_g, dim3(256), dim3(256), LDS_BYTES, stream, (const unsigned short*)d_in[0], (const unsigned short*)d_in[1],
                     (const unsigned short*)d_in[2], (unsigned short*)d_out);
}
```

```cpp
#include <hip/hip_runtime.h>
#include <hip/hip_bf16.h>

typedef __bf16   bf16x8 __attribute__((ext_vector_type(8)));
typedef __bf16   bf16x2 __attribute__((ext_vector_type(2)));
typedef float    f32x4  __attribute__((ext_vector_type(4)));
typedef float    f32x2  __attribute__((ext_vector_type(2)));
typedef unsigned u32x4  __attribute__((ext_vector_type(4)));
typedef unsigned u32x2  __attribute__((ext_vector_type(2)));
typedef unsigned short u16x8 __attribute__((ext_vector_type(8)));

constexpr int SQ = 848, SK = 512, DH = 192, ROWB = 384;
constexpr int LDS_BYTES = 163840;
constexpr float CSC = 0.07216878364870322f * 1.4426950408889634f;

#define DI __device__ __forceinline__
DI unsigned cvtpk(float lo, float hi) { f32x2 v = {lo, hi}; bf16x2 b = __builtin_convertvector(v, bf16x2); return __builtin_bit_cast(unsigned, b); }
DI float xmax(float x) {
  auto r = __builtin_amdgcn_permlane32_swap(__float_as_uint(x), __float_as_uint(x), false, false);
  x = fmaxf(__uint_as_float(r[0]), __uint_as_float(r[1]));
  auto q = __builtin_amdgcn_permlane16_swap(__float_as_uint(x), __float_as_uint(x), false, false);
  return fmaxf(__uint_as_float(q[0]), __uint_as_float(q[1]));
}
DI float xsum(float x) {
  auto r = __builtin_amdgcn_permlane32_swap(__float_as_uint(x), __float_as_uint(x), false, false);
  x = __uint_as_float(r[0]) + __uint_as_float(r[1]);
  auto q = __builtin_amdgcn_permlane16_swap(__float_as_uint(x), __float_as_uint(x), false, false);
  return __uint_as_float(q[0]) + __uint_as_float(q[1]);
}
DI f32x4 score_tile(const unsigned short* krow, const bf16x8 (&qf)[6]) {
  f32x4 acc = {0.f, 0.f, 0.f, 0.f};
#pragma unroll
  for (int s = 0; s < 6; ++s) acc = __builtin_amdgcn_mfma_f32_16x16x32_bf16(*(const bf16x8*)(krow + 32 * s), qf[s], acc, 0, 0, 0);
  return acc;
}
DI void exact_tile(const unsigned short* Q, const unsigned short* K, const unsigned short* V, unsigned short* O, int head, int row0) {
  const unsigned lane = __builtin_amdgcn_mbcnt_hi(~0u, __builtin_amdgcn_mbcnt_lo(~0u, 0u));
  const int c = lane & 15, g4 = lane >> 4;
  const int qrow = min(row0 + c, SQ - 1);
  bf16x8 qf[6];
#pragma unroll
  for (int s = 0; s < 6; ++s) qf[s] = *(const bf16x8*)(Q + ((size_t)head * SQ + qrow) * DH + 32 * s + 8 * g4);
  const unsigned short* Kh = K + ((size_t)head * SK + c) * DH + 8 * g4;
  float m = -__builtin_huge_valf();
#pragma unroll 1
  for (int kb = 0; kb < SK / 16; ++kb) {
    const f32x4 a = score_tile(Kh + (size_t)kb * 16 * DH, qf);
    m = fmaxf(fmaxf(m, fmaxf(a[0], a[1])), fmaxf(a[2], a[3]));
  }
  const float mc = -xmax(m) * CSC;
  float l = 0.f;
  f32x4 o[12];
#pragma unroll
  for (int dt = 0; dt < 12; ++dt) o[dt] = f32x4{0.f, 0.f, 0.f, 0.f};
#pragma unroll 1
  for (int j = 0; j < SK / 32; ++j) {
    u32x4 p;
#pragma unroll
    for (int kt = 0; kt < 2; ++kt) {
      f32x4 a = score_tile(Kh + (size_t)(32 * j + 16 * kt) * DH, qf);
#pragma unroll
      for (int r = 0; r < 4; ++r) { a[r] = __builtin_amdgcn_exp2f(fmaf(a[r], CSC, mc)); l += a[r]; }
      p[2 * kt] = cvtpk(a[0], a[1]); p[2 * kt + 1] = cvtpk(a[2], a[3]);
    }
    const unsigned short* Vj = V + ((size_t)head * SK + 32 * j + 4 * g4) * DH + c;
#pragma unroll
    for (int dt = 0; dt < 12; ++dt) {
      u16x8 vf;
#pragma unroll
      for (int i = 0; i < 8; ++i) vf[i] = Vj[(16 * (i >> 2) + (i & 3)) * DH + 16 * dt];
      o[dt] = __builtin_amdgcn_mfma_f32_16x16x32_bf16(__builtin_bit_cast(bf16x8, vf), __builtin_bit_cast(bf16x8, p), o[dt], 0, 0, 0);
    }
  }
  const float inv = 1.0f / xsum(l);
  if (row0 + c < SQ) {
#pragma unroll
    for (int dt = 0; dt < 12; ++dt)
      *(u32x2*)(O + ((size_t)head * SQ + qrow) * DH + 16 * dt + 4 * g4) = u32x2{cvtpk(o[dt][0] * inv, o[dt][1] * inv), cvtpk(o[dt][2] * inv, o[dt][3] * inv)};
  }
}

__global__ __launch_bounds__(256, 1)
void attn_fwd_g(const unsigned short* __restrict__ Qg, const unsigned short* __restrict__ Kg,
                const unsigned short* __restrict__ Vg, unsigned short* __restrict__ Og) {
  const int wid = __builtin_amdgcn_readfirstlane(threadIdx.x >> 6);
  const int wg = blockIdx.x;
  unsigned bad;
  asm volatile(
  "s_load_dwordx8 s[76:83], %1, 0\n"
  "L_PC_%=:\n"
  "s_getpc_b64 s[84:85]\n"
  "v_mbcnt_lo_u32_b32 v0, -1, 0\n"
  "v_mbcnt_hi_u32_b32 v0, -1, v0\n"
  "v_lshrrev_b32 v2, 3, v0\n"
  "v_and_b32 v1, 7, v0\n"
  "v_xor_b32 v1, v1, v2\n"
  "v_mul_u32_u24 v112, 0x180, v2\n"
  "v_lshl_add_u32 v112, v1, 4, v112\n"
  "s_lshr_b32 s87, %3, 4\n"
  "s_and_b32 s63, %3, 7\n"
  "s_lshl3_add_u32 s86, s87, s63\n"
  "s_and_b32 s65, %2, 1\n"
  "s_mul_i32 s59, %2, 18432\n"
  "s_add_u32 s59, s59, 86016\n"
  "s_cmp_gt_u32 %2, 1\n"
  "s_cbranch_scc0 A_START_%=\n"
  "s_lshr_b32 s62, %3, 3\n"
  "s_lshl1_add_u32 s62, s62, s65\n"
  "s_lshl_b32 s62, s62, 13\n"
  "s_add_u32 s48, s84, s62\n"
  "s_addc_u32 s49, s85, 0\n"
  "s_sub_i32 s50, L_END_%=-L_PC_%=, s62\n"
  "s_max_i32 s50, s50, 0\n"
  "s_mov_b32 s51, 0x20000\n"
  "s_mov_b32 m0, 159744\n"
  "v_lshlrev_b32 v125, 7, v0\n"
  "buffer_load_dword v125, s[48:51], 0 offen lds\n"
  "s_mul_i32 s75, s87, 20\n"
  "s_lshr_b32 s75, s75, 4\n"
  "s_add_u32 s75, s75, 8\n"
  "s_bitcmp1_b32 %3, 3\n"
  "s_cselect_b32 s62, s75, 0\n"
  "s_mul_i32 s63, s86, 53\n"
  "s_add_u32 s62, s62, s63\n"
  "s_mul_i32 s67, s62, 6144\n"
  "s_add_u32 s64, s67, 6144\n"
  "s_lshl_b32 s66, s65, 10\n"
  "s_add_u32 s66, s66, 49152\n"
  "s_mul_i32 s62, s65, 3072\n"
  "s_sub_u32 s46, 166723584, s62\n"
  "s_mov_b32 s47, 0x20000\n"
  "s_waitcnt lgkmcnt(0)\n"
  "s_add_u32 s44, s76, s62\n"
  "s_addc_u32 s45, s77, 0\n"
  "s_add_u32 m0, s66, 0\n"
  "s_nop 0\n"
  "buffer_load_dwordx4 v112, s[44:47], s67 offen offset:0 nt lds\n"
  "s_add_u32 m0, s66, 1920\n"
  "s_nop 0\n"
  "buffer_load_dwordx4 v112, s[44:47], s67 offen offset:128 nt lds\n"
  "s_add_u32 m0, s66, 3840\n"
  "s_nop 0\n"
  "buffer_load_dwordx4 v112, s[44:47], s67 offen offset:256 nt lds\n"
  "s_add_u32 m0, s66, 6144\n"
  "s_nop 0\n"
  "buffer_load_dwordx4 v112, s[44:47], s64 offen offset:0 nt lds\n"
  "s_add_u32 m0, s66, 8064\n"
  "s_nop 0\n"
  "buffer_load_dwordx4 v112, s[44:47], s64 offen offset:128 nt lds\n"
  "s_add_u32 m0, s66, 9984\n"
  "s_nop 0\n"
  "buffer_load_dwordx4 v112, s[44:47], s64 offen offset:256 nt lds\n"
  "s_branch B_START_%=\n"
  "A_START_%=:\n"
  "s_lshl1_add_u32 s62, s86, s65\n"
  "s_mul_i32 s57, s62, 98304\n"
  "v_add_u32 v113, 0x800, v112\n"
  "s_mov_b32 s42, 100663296\n"
  "s_mov_b32 s43, 0x20000\n"
  "s_waitcnt lgkmcnt(0)\n"
  "s_mov_b64 s[40:41], s[78:79]\n"
  "s_add_u32 m0, s59, 0\n"
  "s_nop 0\n"
  "buffer_load_dwordx4 v112, s[40:43], s57 offen offset:0  lds\n"
  "buffer_load_dwordx4 v113, s[40:43], s57 offen offset:1024  lds\n"
  "s_add_u32 m0, s59, 1920\n"
  "s_nop 0\n"
  "buffer_load_dwordx4 v112, s[40:43], s57 offen offset:128  lds\n"
  "buffer_load_dwordx4 v113, s[40:43], s57 offen offset:1152  lds\n"
  "s_add_u32 m0, s59, 3840\n"
  "s_nop 0\n"
  "buffer_load_dwordx4 v112, s[40:43], s57 offen offset:256  lds\n"
  "buffer_load_dwordx4 v113, s[40:43], s57 offen offset:1280  lds\n"
  "s_add_u32 s57, s57, 6144\n"
  "s_add_u32 m0, s59, 5888\n"
  "s_nop 0\n"
  "buffer_load_dwordx4 v112, s[40:43], s57 offen offset:256  lds\n"
  "buffer_load_dwordx4 v113, s[40:43], s57 offen offset:1280  lds\n"
  "s_add_u32 m0, s59, 8064\n"
  "s_nop 0\n"
  "buffer_load_dwordx4 v112, s[40:43], s57 offen offset:128  lds\n"
  "buffer_load_dwordx4 v113, s[40:43], s57 offen offset:1152  lds\n"
  "s_add_u32 m0, s59, 10240\n"
  "s_nop 0\n"
  "buffer_load_dwordx4 v112, s[40:43], s57 offen offset:0  lds\n"
  "buffer_load_dwordx4 v113, s[40:43], s57 offen offset:1024  lds\n"
  "s_add_u32 s57, s57, 6144\n"
  "s_add_u32 m0, s59, 12288\n"
  "s_nop 0\n"
  "buffer_load_dwordx4 v112, s[40:43], s57 offen offset:0  lds\n"
  "buffer_load_dwordx4 v113, s[40:43], s57 offen offset:1024  lds\n"
  "s_add_u32 m0, s59, 14208\n"
  "s_nop 0\n"
  "buffer_load_dwordx4 v112, s[40:43], s57 offen offset:128  lds\n"
  "buffer_load_dwordx4 v113, s[40:43], s57 offen offset:1152  lds\n"
  "s_add_u32 m0, s59, 16128\n"
  "s_nop 0\n"
  "buffer_load_dwordx4 v112, s[40:43], s57 offen offset:256  lds\n"
  "buffer_load_dwordx4 v113, s[40:43], s57 offen offset:1280  lds\n"
  "s_add_u32 s57, s57, 6144\n"
  "s_mul_i32 s75, s87, 20\n"
  "s_lshr_b32 s75, s75, 4\n"
  "s_add_u32 s75, s75, 8\n"
  "s_sub_u32 s62, 53, s75\n"
  "s_bitcmp1_b32 %3, 3\n"
  "s_cselect_b32 s52, s62, s75\n"
  "s_cselect_b32 s54, s75, s62\n"
  "s_cselect_b32 s62, s75, 0\n"
  "s_lshl_b32 s62, s62, 8\n"
  "s_or_b32 s52, s52, s62\n"
  "s_pack_ll_b32_b16 s52, s52, s86\n"
  "s_add_u32 s62, %3, 128\n"
  "s_pack_ll_b32_b16 s53, 53, s62\n"
  "s_bitcmp1_b32 %3, 0\n"
  "s_cselect_b32 s62, -2, 2\n"
  "s_add_u32 s54, s54, s62\n"
  "s_sub_u32 s62, 53, s54\n"
  "s_lshl_b32 s62, s62, 8\n"
  "s_bfe_u32 s63, %3, 0x10003\n"
  "s_cselect_b32 s62, s62, 0\n"
  "s_or_b32 s54, s54, s62\n"
  "s_xor_b32 s63, s63, s86\n"
  "s_add_u32 s63, s63, 384\n"
  "s_pack_ll_b32_b16 s54, s54, s63\n"
  "s_mov_b32 s55, 0\n"
  "s_and_b32 s56, s52, 0xff\n"
  "s_cmp_lg_u32 s53, 0\n"
  "s_cselect_b32 s62, 1, 0\n"
  "s_sub_u32 s56, s56, s62\n"
  "v_and_b32 v2, 7, v0\n"
  "v_lshrrev_b32 v83, 4, v0\n"
  "v_xor_b32 v83, v83, v2\n"
  "v_lshlrev_b32 v83, 4, v83\n"
  "v_lshl_add_u32 v83, v2, 7, v83\n"
  "v_bfe_u32 v1, v0, 3, 1\n"
  "v_lshl_add_u32 v83, v1, 10, v83\n"
  "v_xor_b32 v84, 64, v83\n"
  "v_add_u32 v87, s59, v83\n"
  "v_add_u32 v88, s59, v84\n"
  "s_mov_b32 s66, 49152\n"
  "v_add_u32 v81, s66, v83\n"
  "v_add_u32 v82, s66, v84\n"
  "s_lshl_b32 s62, s65, 13\n"
  "v_lshlrev_b32 v80, 4, v0\n"
  "v_add_u32 v80, s62, v80\n"
  "s_mov_b32 s60, 0x3dd53b94\n"
  "s_mov_b32 s61, 0x409fecaf\n"
  "v_mov_b32 v92, 0xc2400000\n"
  "v_mov_b32 v93, 0xc2400000\n"
  "v_mov_b32 v94, 0xc2400000\n"
  "v_mov_b32 v95, 0xc2400000\n"
  "s_waitcnt vmcnt(16)\n"
  "ds_read_b128 a[0:3], v87 offset:0\n"
  "ds_read_b128 a[4:7], v88 offset:0\n"
  "s_waitcnt vmcnt(14)\n"
  "ds_read_b128 a[8:11], v87 offset:2048\n"
  "ds_read_b128 a[12:15], v88 offset:2048\n"
  "s_add_u32 m0, s59, -256\n"
  "s_waitcnt lgkmcnt(2)\n"
  "buffer_load_dwordx4 v112, s[40:43], s57 offen offset:256  lds\n"
  "buffer_load_dwordx4 v113, s[40:43], s57 offen offset:1280  lds\n"
  "s_waitcnt vmcnt(14)\n"
  "ds_read_b128 a[16:19], v87 offset:4096\n"
  "ds_read_b128 a[20:23], v88 offset:4096\n"
  "s_add_u32 m0, s59, 1920\n"
  "s_waitcnt lgkmcnt(2)\n"
  "buffer_load_dwordx4 v112, s[40:43], s57 offen offset:128  lds\n"
  "buffer_load_dwordx4 v113, s[40:43], s57 offen offset:1152  lds\n"
  "s_waitcnt vmcnt(14)\n"
  "ds_read_b128 a[40:43], v87 offset:6144\n"
  "ds_read_b128 a[44:47], v88 offset:6144\n"
  "s_add_u32 m0, s59, 4096\n"
  "s_waitcnt lgkmcnt(2)\n"
  "buffer_load_dwordx4 v112, s[40:43], s57 offen offset:0  lds\n"
  "buffer_load_dwordx4 v113, s[40:43], s57 offen offset:1024  lds\n"
  "s_add_u32 s57, s57, 6144\n"
  "s_waitcnt vmcnt(14)\n"
  "ds_read_b128 a[32:35], v87 offset:8192\n"
  "ds_read_b128 a[36:39], v88 offset:8192\n"
  "s_add_u32 m0, s59, 6144\n"
  "s_waitcnt lgkmcnt(2)\n"
  "buffer_load_dwordx4 v112, s[40:43], s57 offen offset:0  lds\n"
  "buffer_load_dwordx4 v113, s[40:43], s57 offen offset:1024  lds\n"
  "s_waitcnt vmcnt(14)\n"
  "ds_read_b128 a[24:27], v87 offset:10240\n"
  "ds_read_b128 a[28:31], v88 offset:10240\n"
  "s_add_u32 m0, s59, 8064\n"
  "s_waitcnt lgkmcnt(2)\n"
  "buffer_load_dwordx4 v112, s[40:43], s57 offen offset:128  lds\n"
  "buffer_load_dwordx4 v113, s[40:43], s57 offen offset:1152  lds\n"
  "s_waitcnt vmcnt(14)\n"
  "ds_read_b128 a[48:51], v87 offset:12288\n"
  "ds_read_b128 a[52:55], v88 offset:12288\n"
  "s_add_u32 m0, s59, 9984\n"
  "s_waitcnt lgkmcnt(2)\n"
  "buffer_load_dwordx4 v112, s[40:43], s57 offen offset:256  lds\n"
  "buffer_load_dwordx4 v113, s[40:43], s57 offen offset:1280  lds\n"
  "s_add_u32 s57, s57, 6144\n"
  "s_waitcnt vmcnt(14)\n"
  "ds_read_b128 a[56:59], v87 offset:14336\n"
  "ds_read_b128 a[60:63], v88 offset:14336\n"
  "s_add_u32 m0, s59, 12032\n"
  "s_waitcnt lgkmcnt(2)\n"
  "buffer_load_dwordx4 v112, s[40:43], s57 offen offset:256  lds\n"
  "buffer_load_dwordx4 v113, s[40:43], s57 offen offset:1280  lds\n"
  "s_waitcnt vmcnt(14)\n"
  "ds_read_b128 a[64:67], v87 offset:16384\n"
  "ds_read_b128 a[68:71], v88 offset:16384\n"
  "s_add_u32 m0, s59, 14208\n"
  "s_waitcnt lgkmcnt(2)\n"
  "buffer_load_dwordx4 v112, s[40:43], s57 offen offset:128  lds\n"
  "buffer_load_dwordx4 v113, s[40:43], s57 offen offset:1152  lds\n"
  "s_waitcnt vmcnt(14)\n"
  "ds_read_b128 a[88:91], v87 offset:0\n"
  "ds_read_b128 a[92:95], v88 offset:0\n"
  "s_add_u32 m0, s59, 16384\n"
  "s_barrier\n"
  "ds_read_b128 v[32:35], v81 offset:0\n"
  "ds_read_b128 v[36:39], v82 offset:0\n"
  "ds_read_b128 v[40:43], v81 offset:2048\n"
  "ds_read_b128 v[44:47], v82 offset:2048\n"
  "ds_read_b128 v[48:51], v81 offset:4096\n"
  "ds_read_b128 v[52:55], v82 offset:4096\n"
  "s_waitcnt lgkmcnt(0)\n"
  "v_mfma_f32_16x16x32_bf16 v[0:3], a[0:3], v[32:35], v[92:95]\n"
  "s_add_u32 s66, s66, 6144\n"
  "s_cmp_eq_u32 s66, 86016\n"
  "s_cselect_b32 s66, 49152, s66\n"
  "buffer_load_dwordx4 v112, s[40:43], s57 offen offset:0  lds\n"
  "buffer_load_dwordx4 v113, s[40:43], s57 offen offset:1024  lds\n"
  "s_add_u32 s57, s57, 6144\n"
  "v_mfma_f32_16x16x32_bf16 v[0:3], a[4:7], v[36:39], v[0:3]\n"
  "s_waitcnt vmcnt(14)\n"
  "ds_read_b128 a[80:83], v87 offset:2048\n"
  "ds_read_b128 a[84:87], v88 offset:2048\n"
  "s_add_u32 m0, s59, 0\n"
  "v_mfma_f32_16x16x32_bf16 v[0:3], a[8:11], v[40:43], v[0:3]\n"
  "buffer_load_dwordx4 v112, s[40:43], s57 offen offset:0  lds\n"
  "buffer_load_dwordx4 v113, s[40:43], s57 offen offset:1024  lds\n"
  "v_mfma_f32_16x16x32_bf16 v[0:3], a[12:15], v[44:47], v[0:3]\n"
  "s_waitcnt vmcnt(14)\n"
  "ds_read_b128 a[72:75], v87 offset:4096\n"
  "ds_read_b128 a[76:79], v88 offset:4096\n"
  "s_add_u32 m0, s59, 1920\n"
  "v_mfma_f32_16x16x32_bf16 v[0:3], a[16:19], v[48:51], v[0:3]\n"
  "s_waitcnt lgkmcnt(2)\n"
  "buffer_load_dwordx4 v112, s[40:43], s57 offen offset:128  lds\n"
  "buffer_load_dwordx4 v113, s[40:43], s57 offen offset:1152  lds\n"
  "v_mfma_f32_16x16x32_bf16 v[0:3], a[20:23], v[52:55], v[0:3]\n"
  "s_waitcnt vmcnt(14)\n"
  "ds_read_b128 a[96:99], v87 offset:6144\n"
  "ds_read_b128 a[100:103], v88 offset:6144\n"
  "s_add_u32 m0, s59, 3840\n"
  "v_mfma_f32_16x16x32_bf16 v[4:7], a[44:47], v[52:55], v[92:95]\n"
  "s_waitcnt lgkmcnt(2)\n"
  "buffer_load_dwordx4 v112, s[40:43], s57 offen offset:256  lds\n"
  "buffer_load_dwordx4 v113, s[40:43], s57 offen offset:1280  lds\n"
  "s_add_u32 s57, s57, 6144\n"
  "v_mfma_f32_16x16x32_bf16 v[4:7], a[40:43], v[48:51], v[4:7]\n"
  "s_waitcnt vmcnt(14)\n"
  "ds_read_b128 a[104:107], v87 offset:8192\n"
  "ds_read_b128 a[108:111], v88 offset:8192\n"
  "s_add_u32 m0, s59, 5888\n"
  "v_mfma_f32_16x16x32_bf16 v[4:7], a[36:39], v[44:47], v[4:7]\n"
  "s_waitcnt lgkmcnt(2)\n"
  "buffer_load_dwordx4 v112, s[40:43], s57 offen offset:256  lds\n"
  "buffer_load_dwordx4 v113, s[40:43], s57 offen offset:1280  lds\n"
  "v_mfma_f32_16x16x32_bf16 v[4:7], a[32:35], v[40:43], v[4:7]\n"
  "s_waitcnt vmcnt(14)\n"
  "ds_read_b128 a[112:115], v87 offset:10240\n"
  "ds_read_b128 a[116:119], v88 offset:10240\n"
  "s_add_u32 m0, s59, 8064\n"
  "v_mfma_f32_16x16x32_bf16 v[4:7], a[28:31], v[36:39], v[4:7]\n"
  "s_waitcnt lgkmcnt(2)\n"
  "buffer_load_dwordx4 v112, s[40:43], s57 offen offset:128  lds\n"
  "buffer_load_dwordx4 v113, s[40:43], s57 offen offset:1152  lds\n"
  "v_mfma_f32_16x16x32_bf16 v[4:7], a[24:27], v[32:35], v[4:7]\n"
  "s_waitcnt vmcnt(14)\n"
  "ds_read_b128 a[136:139], v87 offset:12288\n"
  "ds_read_b128 a[140:143], v88 offset:12288\n"
  "s_add_u32 m0, s59, 10240\n"
  "v_add_u32 v81, s66, v83\n"
  "v_add_u32 v82, s66, v84\n"
  "v_mfma_f32_16x16x32_bf16 v[8:11], a[48:51], v[32:35], v[92:95]\n"
  "s_waitcnt lgkmcnt(2)\n"
  "buffer_load_dwordx4 v112, s[40:43], s57 offen offset:0  lds\n"
  "buffer_load_dwordx4 v113, s[40:43], s57 offen offset:1024  lds\n"
  "s_add_u32 s57, s57, 6144\n"
  "v_mfma_f32_16x16x32_bf16 v[8:11], a[52:55], v[36:39], v[8:11]\n"
  "s_waitcnt vmcnt(14)\n"
  "ds_read_b128 a[128:131], v87 offset:14336\n"
  "ds_read_b128 a[132:135], v88 offset:14336\n"
  "s_add_u32 m0, s59, 12288\n"
  "v_mfma_f32_16x16x32_bf16 v[8:11], a[56:59], v[40:43], v[8:11]\n"
  "s_waitcnt lgkmcnt(2)\n"
  "buffer_load_dwordx4 v112, s[40:43], s57 offen offset:0  lds\n"
  "buffer_load_dwordx4 v113, s[40:43], s57 offen offset:1024  lds\n"
  "v_mfma_f32_16x16x32_bf16 v[8:11], a[60:63], v[44:47], v[8:11]\n"
  "s_waitcnt vmcnt(14)\n"
  "ds_read_b128 a[120:123], v87 offset:16384\n"
  "ds_read_b128 a[124:127], v88 offset:16384\n"
  "s_add_u32 m0, s59, 14208\n"
  "v_mfma_f32_16x16x32_bf16 v[8:11], a[64:67], v[48:51], v[8:11]\n"
  "s_waitcnt lgkmcnt(2)\n"
  "buffer_load_dwordx4 v112, s[40:43], s57 offen offset:128  lds\n"
  "buffer_load_dwordx4 v113, s[40:43], s57 offen offset:1152  lds\n"
  "v_mfma_f32_16x16x32_bf16 v[8:11], a[68:71], v[52:55], v[8:11]\n"
  "s_waitcnt vmcnt(14)\n"
  "ds_read_b128 a[144:147], v87 offset:0\n"
  "ds_read_b128 a[148:151], v88 offset:0\n"
  "s_add_u32 m0, s59, 16128\n"
  "v_pk_mul_f32 v[0:1], v[0:1], s[60:61] op_sel_hi:[1,0]\n"
  "v_pk_mul_f32 v[2:3], v[2:3], s[60:61] op_sel_hi:[1,0]\n"
  "v_pk_mul_f32 v[4:5], v[4:5], s[60:61] op_sel_hi:[1,0]\n"
  "v_pk_mul_f32 v[6:7], v[6:7], s[60:61] op_sel_hi:[1,0]\n"
  "v_mfma_f32_16x16x32_bf16 v[12:15], a[92:95], v[52:55], v[92:95]\n"
  "s_waitcnt lgkmcnt(2)\n"
  "buffer_load_dwordx4 v112, s[40:43], s57 offen offset:256  lds\n"
  "buffer_load_dwordx4 v113, s[40:43], s57 offen offset:1280  lds\n"
  "s_add_u32 s57, s57, 6144\n"
  "v_exp_f32 v0, v0\n"
  "v_mfma_f32_16x16x32_bf16 v[12:15], a[88:91], v[48:51], v[12:15]\n"
  "s_waitcnt vmcnt(14)\n"
  "ds_read_b128 a[152:155], v87 offset:2048\n"
  "ds_read_b128 a[156:159], v88 offset:2048\n"
  "s_add_u32 m0, s59, -256\n"
  "v_exp_f32 v1, v1\n"
  "v_mfma_f32_16x16x32_bf16 v[12:15], a[84:87], v[44:47], v[12:15]\n"
  "s_waitcnt lgkmcnt(2)\n"
  "buffer_load_dwordx4 v112, s[40:43], s57 offen offset:256  lds\n"
  "buffer_load_dwordx4 v113, s[40:43], s57 offen offset:1280  lds\n"
  "v_exp_f32 v2, v2\n"
  "v_mfma_f32_16x16x32_bf16 v[12:15], a[80:83], v[40:43], v[12:15]\n"
  "s_waitcnt vmcnt(14)\n"
  "ds_read_b128 a[160:163], v87 offset:4096\n"
  "ds_read_b128 a[164:167], v88 offset:4096\n"
  "s_add_u32 m0, s59, 1920\n"
  "v_exp_f32 v3, v3\n"
  "v_mfma_f32_16x16x32_bf16 v[12:15], a[76:79], v[36:39], v[12:15]\n"
  "s_waitcnt lgkmcnt(2)\n"
  "buffer_load_dwordx4 v112, s[40:43], s57 offen offset:128  lds\n"
  "buffer_load_dwordx4 v113, s[40:43], s57 offen offset:1152  lds\n"
  "v_cvt_pk_bf16_f32 v0, v0, v1\n"
  "v_cvt_pk_bf16_f32 v1, v2, v3\n"
  "v_mfma_f32_16x16x32_bf16 v[12:15], a[72:75], v[32:35], v[12:15]\n"
  "s_waitcnt vmcnt(14)\n"
  "ds_read_b128 a[184:187], v87 offset:6144\n"
  "ds_read_b128 a[188:191], v88 offset:6144\n"
  "s_add_u32 m0, s59, 4096\n"
  "v_mfma_f32_16x16x32_bf16 v[16:19], a[96:99], v[32:35], v[92:95]\n"
  "s_waitcnt lgkmcnt(2)\n"
  "buffer_load_dwordx4 v112, s[40:43], s57 offen offset:0  lds\n"
  "buffer_load_dwordx4 v113, s[40:43], s57 offen offset:1024  lds\n"
  "s_add_u32 s57, s57, 6144\n"
  "v_exp_f32 v4, v4\n"
  "v_mfma_f32_16x16x32_bf16 v[16:19], a[100:103], v[36:39], v[16:19]\n"
  "s_waitcnt vmcnt(14)\n"
  "ds_read_b128 a[176:179], v87 offset:8192\n"
  "ds_read_b128 a[180:183], v88 offset:8192\n"
  "s_add_u32 m0, s59, 6144\n"
  "v_exp_f32 v5, v5\n"
  "v_mfma_f32_16x16x32_bf16 v[16:19], a[104:107], v[40:43], v[16:19]\n"
  "s_waitcnt lgkmcnt(2)\n"
  "buffer_load_dwordx4 v112, s[40:43], s57 offen offset:0  lds\n"
  "buffer_load_dwordx4 v113, s[40:43], s57 offen offset:1024  lds\n"
  "v_exp_f32 v6, v6\n"
  "v_mfma_f32_16x16x32_bf16 v[16:19], a[108:111], v[44:47], v[16:19]\n"
  "s_waitcnt vmcnt(14)\n"
  "ds_read_b128 a[168:171], v87 offset:10240\n"
  "ds_read_b128 a[172:175], v88 offset:10240\n"
  "s_add_u32 m0, s59, 8064\n"
  "v_exp_f32 v7, v7\n"
  "v_mfma_f32_16x16x32_bf16 v[16:19], a[112:115], v[48:51], v[16:19]\n"
  "s_waitcnt lgkmcnt(2)\n"
  "buffer_load_dwordx4 v112, s[40:43], s57 offen offset:128  lds\n"
  "buffer_load_dwordx4 v113, s[40:43], s57 offen offset:1152  lds\n"
  "v_cvt_pk_bf16_f32 v2, v4, v5\n"
  "v_cvt_pk_bf16_f32 v3, v6, v7\n"
  "v_mfma_f32_16x16x32_bf16 v[16:19], a[116:119], v[52:55], v[16:19]\n"
  "s_waitcnt vmcnt(14)\n"
  "ds_read_b128 a[192:195], v87 offset:12288\n"
  "ds_read_b128 a[196:199], v88 offset:12288\n"
  "s_add_u32 m0, s59, 9984\n"
  "ds_write_b128 v80, v[0:3] offset:0\n"
  "v_pk_mul_f32 v[8:9], v[8:9], s[60:61] op_sel_hi:[1,0]\n"
  "v_pk_mul_f32 v[10:11], v[10:11], s[60:61] op_sel_hi:[1,0]\n"
  "v_pk_mul_f32 v[12:13], v[12:13], s[60:61] op_sel_hi:[1,0]\n"
  "v_pk_mul_f32 v[14:15], v[14:15], s[60:61] op_sel_hi:[1,0]\n"
  "v_mfma_f32_16x16x32_bf16 v[20:23], a[140:143], v[52:55], v[92:95]\n"
  "s_waitcnt lgkmcnt(3)\n"
  "buffer_load_dwordx4 v112, s[40:43], s57 offen offset:256  lds\n"
  "buffer_load_dwordx4 v113, s[40:43], s57 offen offset:1280  lds\n"
  "s_add_u32 s57, s57, 6144\n"
  "v_exp_f32 v8, v8\n"
  "v_mfma_f32_16x16x32_bf16 v[20:23], a[136:139], v[48:51], v[20:23]\n"
  "s_waitcnt vmcnt(14)\n"
  "ds_read_b128 a[200:203], v87 offset:14336\n"
  "ds_read_b128 a[204:207], v88 offset:14336\n"
  "s_add_u32 m0, s59, 12032\n"
  "v_exp_f32 v9, v9\n"
  "v_mfma_f32_16x16x32_bf16 v[20:23], a[132:135], v[44:47], v[20:23]\n"
  "s_waitcnt lgkmcnt(3)\n"
  "buffer_load_dwordx4 v112, s[40:43], s57 offen offset:256  lds\n"
  "buffer_load_dwordx4 v113, s[40:43], s57 offen offset:1280  lds\n"
  "v_exp_f32 v10, v10\n"
  "v_mfma_f32_16x16x32_bf16 v[20:23], a[128:131], v[40:43], v[20:23]\n"
  "s_waitcnt vmcnt(14)\n"
  "ds_read_b128 a[208:211], v87 offset:16384\n"
  "ds_read_b128 a[212:215], v88 offset:16384\n"
  "s_add_u32 m0, s59, 14208\n"
  "v_exp_f32 v11, v11\n"
  "v_mfma_f32_16x16x32_bf16 v[20:23], a[124:127], v[36:39], v[20:23]\n"
  "s_waitcnt lgkmcnt(2)\n"
  "buffer_load_dwordx4 v112, s[40:43], s57 offen offset:128  lds\n"
  "buffer_load_dwordx4 v113, s[40:43], s57 offen offset:1152  lds\n"
  "v_cvt_pk_bf16_f32 v8, v8, v9\n"
  "v_cvt_pk_bf16_f32 v9, v10, v11\n"
  "v_mfma_f32_16x16x32_bf16 v[20:23], a[120:123], v[32:35], v[20:23]\n"
  "s_waitcnt vmcnt(14)\n"
  "ds_read_b128 a[232:235], v87 offset:0\n"
  "ds_read_b128 a[236:239], v88 offset:0\n"
  "s_add_u32 m0, s59, 16384\n"
  "ds_read_b128 v[56:59], v81 offset:0\n"
  "ds_read_b128 v[60:63], v82 offset:0\n"
  "v_mfma_f32_16x16x32_bf16 v[24:27], a[144:147], v[32:35], v[92:95]\n"
  "s_waitcnt lgkmcnt(4)\n"
  "buffer_load_dwordx4 v112, s[40:43], s57 offen offset:0  lds\n"
  "buffer_load_dwordx4 v113, s[40:43], s57 offen offset:1024  lds\n"
  "s_add_u32 s57, s57, 6144\n"
  "v_exp_f32 v12, v12\n"
  "v_mfma_f32_16x16x32_bf16 v[24:27], a[148:151], v[36:39], v[24:27]\n"
  "s_waitcnt vmcnt(14)\n"
  "ds_read_b128 a[224:227], v87 offset:2048\n"
  "ds_read_b128 a[228:231], v88 offset:2048\n"
  "s_add_u32 m0, s59, 0\n"
  "v_exp_f32 v13, v13\n"
  "v_mfma_f32_16x16x32_bf16 v[24:27], a[152:155], v[40:43], v[24:27]\n"
  "s_waitcnt lgkmcnt(4)\n"
  "buffer_load_dwordx4 v112, s[40:43], s57 offen offset:0  lds\n"
  "buffer_load_dwordx4 v113, s[40:43], s57 offen offset:1024  lds\n"
  "v_exp_f32 v14, v14\n"
  "v_mfma_f32_16x16x32_bf16 v[24:27], a[156:159], v[44:47], v[24:27]\n"
  "s_waitcnt vmcnt(14)\n"
  "ds_read_b128 a[216:219], v87 offset:4096\n"
  "ds_read_b128 a[220:223], v88 offset:4096\n"
  "s_add_u32 m0, s59, 1920\n"
  "v_exp_f32 v15, v15\n"
  "v_mfma_f32_16x16x32_bf16 v[24:27], a[160:163], v[48:51], v[24:27]\n"
  "s_waitcnt lgkmcnt(2)\n"
  "buffer_load_dwordx4 v112, s[40:43], s57 offen offset:128  lds\n"
  "buffer_load_dwordx4 v113, s[40:43], s57 offen offset:1152  lds\n"
  "v_cvt_pk_bf16_f32 v10, v12, v13\n"
  "v_cvt_pk_bf16_f32 v11, v14, v15\n"
  "v_mfma_f32_16x16x32_bf16 v[24:27], a[164:167], v[52:55], v[24:27]\n"
  "s_waitcnt vmcnt(14)\n"
  "ds_read_b128 a[240:243], v87 offset:6144\n"
  "ds_read_b128 a[244:247], v88 offset:6144\n"
  "s_add_u32 m0, s59, 3840\n"
  "ds_write_b128 v80, v[8:11] offset:1024\n"
  "v_pk_mul_f32 v[16:17], v[16:17], s[60:61] op_sel_hi:[1,0]\n"
  "v_pk_mul_f32 v[18:19], v[18:19], s[60:61] op_sel_hi:[1,0]\n"
  "v_pk_mul_f32 v[20:21], v[20:21], s[60:61] op_sel_hi:[1,0]\n"
  "v_pk_mul_f32 v[22:23], v[22:23], s[60:61] op_sel_hi:[1,0]\n"
  "v_mfma_f32_16x16x32_bf16 v[28:31], a[188:191], v[52:55], v[92:95]\n"
  "s_waitcnt lgkmcnt(3)\n"
  "buffer_load_dwordx4 v112, s[40:43], s57 offen offset:256  lds\n"
  "buffer_load_dwordx4 v113, s[40:43], s57 offen offset:1280  lds\n"
  "s_add_u32 s57, s57, 6144\n"
  "v_exp_f32 v16, v16\n"
  "v_mfma_f32_16x16x32_bf16 v[28:31], a[184:187], v[48:51], v[28:31]\n"
  "s_waitcnt vmcnt(14)\n"
  "ds_read_b128 a[248:251], v87 offset:8192\n"
  "ds_read_b128 a[252:255], v88 offset:8192\n"
  "s_add_u32 m0, s59, 5888\n"
  "v_exp_f32 v17, v17\n"
  "v_mfma_f32_16x16x32_bf16 v[28:31], a[180:183], v[44:47], v[28:31]\n"
  "s_waitcnt lgkmcnt(3)\n"
  "buffer_load_dwordx4 v112, s[40:43], s57 offen offset:256  lds\n"
  "buffer_load_dwordx4 v113, s[40:43], s57 offen offset:1280  lds\n"
  "v_exp_f32 v18, v18\n"
  "v_mfma_f32_16x16x32_bf16 v[28:31], a[176:179], v[40:43], v[28:31]\n"
  "s_waitcnt vmcnt(14)\n"
  "ds_read_b128 v[128:131], v87 offset:10240\n"
  "ds_read_b128 v[132:135], v88 offset:10240\n"
  "s_add_u32 m0, s59, 8064\n"
  "v_exp_f32 v19, v19\n"
  "v_mfma_f32_16x16x32_bf16 v[28:31], a[172:175], v[36:39], v[28:31]\n"
  "s_waitcnt lgkmcnt(2)\n"
  "buffer_load_dwordx4 v112, s[40:43], s57 offen offset:128  lds\n"
  "buffer_load_dwordx4 v113, s[40:43], s57 offen offset:1152  lds\n"
  "v_cvt_pk_bf16_f32 v16, v16, v17\n"
  "v_cvt_pk_bf16_f32 v17, v18, v19\n"
  "v_mfma_f32_16x16x32_bf16 v[28:31], a[168:171], v[32:35], v[28:31]\n"
  "s_waitcnt vmcnt(14)\n"
  "ds_read_b128 v[152:155], v87 offset:12288\n"
  "ds_read_b128 v[156:159], v88 offset:12288\n"
  "s_add_u32 m0, s59, 10240\n"
  "ds_read_b128 v[64:67], v81 offset:2048\n"
  "ds_read_b128 v[68:71], v82 offset:2048\n"
  "v_mfma_f32_16x16x32_bf16 v[0:3], a[192:195], v[32:35], v[92:95]\n"
  "s_waitcnt lgkmcnt(4)\n"
  "buffer_load_dwordx4 v112, s[40:43], s57 offen offset:0  lds\n"
  "buffer_load_dwordx4 v113, s[40:43], s57 offen offset:1024  lds\n"
  "s_add_u32 s57, s57, 6144\n"
  "v_exp_f32 v20, v20\n"
  "v_mfma_f32_16x16x32_bf16 v[0:3], a[196:199], v[36:39], v[0:3]\n"
  "s_waitcnt vmcnt(14)\n"
  "ds_read_b128 v[144:147], v87 offset:14336\n"
  "ds_read_b128 v[148:151], v88 offset:14336\n"
  "s_add_u32 m0, s59, 12288\n"
  "v_exp_f32 v21, v21\n"
  "v_mfma_f32_16x16x32_bf16 v[0:3], a[200:203], v[40:43], v[0:3]\n"
  "s_waitcnt lgkmcnt(4)\n"
  "buffer_load_dwordx4 v112, s[40:43], s57 offen offset:0  lds\n"
  "buffer_load_dwordx4 v113, s[40:43], s57 offen offset:1024  lds\n"
  "v_exp_f32 v22, v22\n"
  "v_mfma_f32_16x16x32_bf16 v[0:3], a[204:207], v[44:47], v[0:3]\n"
  "s_waitcnt vmcnt(14)\n"
  "ds_read_b128 v[136:139], v87 offset:16384\n"
  "ds_read_b128 v[140:143], v88 offset:16384\n"
  "s_add_u32 m0, s59, 14208\n"
  "v_exp_f32 v23, v23\n"
  "v_mfma_f32_16x16x32_bf16 v[0:3], a[208:211], v[48:51], v[0:3]\n"
  "s_waitcnt lgkmcnt(2)\n"
  "buffer_load_dwordx4 v112, s[40:43], s57 offen offset:128  lds\n"
  "buffer_load_dwordx4 v113, s[40:43], s57 offen offset:1152  lds\n"
  "v_cvt_pk_bf16_f32 v18, v20, v21\n"
  "v_cvt_pk_bf16_f32 v19, v22, v23\n"
  "v_mfma_f32_16x16x32_bf16 v[0:3], a[212:215], v[52:55], v[0:3]\n"
  "s_waitcnt vmcnt(14)\n"
  "ds_read_b128 v[160:163], v87 offset:0\n"
  "ds_read_b128 v[164:167], v88 offset:0\n"
  "s_add_u32 m0, s59, 16128\n"
  "ds_write_b128 v80, v[16:19] offset:2048\n"
  "v_pk_mul_f32 v[24:25], v[24:25], s[60:61] op_sel_hi:[1,0]\n"
  "v_pk_mul_f32 v[26:27], v[26:27], s[60:61] op_sel_hi:[1,0]\n"
  "v_pk_mul_f32 v[28:29], v[28:29], s[60:61] op_sel_hi:[1,0]\n"
  "v_pk_mul_f32 v[30:31], v[30:31], s[60:61] op_sel_hi:[1,0]\n"
  "v_mfma_f32_16x16x32_bf16 v[4:7], a[236:239], v[52:55], v[92:95]\n"
  "s_waitcnt lgkmcnt(3)\n"
  "buffer_load_dwordx4 v112, s[40:43], s57 offen offset:256  lds\n"
  "buffer_load_dwordx4 v113, s[40:43], s57 offen offset:1280  lds\n"
  "s_add_u32 s57, s57, 6144\n"
  "v_exp_f32 v24, v24\n"
  "v_mfma_f32_16x16x32_bf16 v[4:7], a[232:235], v[48:51], v[4:7]\n"
  "s_waitcnt vmcnt(14)\n"
  "ds_read_b128 v[168:171], v87 offset:2048\n"
  "ds_read_b128 v[172:175], v88 offset:2048\n"
  "s_add_u32 m0, s59, -256\n"
  "v_exp_f32 v25, v25\n"
  "v_mfma_f32_16x16x32_bf16 v[4:7], a[228:231], v[44:47], v[4:7]\n"
  "s_waitcnt lgkmcnt(3)\n"
  "buffer_load_dwordx4 v112, s[40:43], s57 offen offset:256  lds\n"
  "buffer_load_dwordx4 v113, s[40:43], s57 offen offset:1280  lds\n"
  "v_exp_f32 v26, v26\n"
  "v_mfma_f32_16x16x32_bf16 v[4:7], a[224:227], v[40:43], v[4:7]\n"
  "s_waitcnt vmcnt(14)\n"
  "ds_read_b128 v[176:179], v87 offset:4096\n"
  "ds_read_b128 v[180:183], v88 offset:4096\n"
  "s_add_u32 m0, s59, 1920\n"
  "v_exp_f32 v27, v27\n"
  "v_mfma_f32_16x16x32_bf16 v[4:7], a[220:223], v[36:39], v[4:7]\n"
  "s_waitcnt lgkmcnt(2)\n"
  "buffer_load_dwordx4 v112, s[40:43], s57 offen offset:128  lds\n"
  "buffer_load_dwordx4 v113, s[40:43], s57 offen offset:1152  lds\n"
  "v_cvt_pk_bf16_f32 v24, v24, v25\n"
  "v_cvt_pk_bf16_f32 v25, v26, v27\n"
  "v_mfma_f32_16x16x32_bf16 v[4:7], a[216:219], v[32:35], v[4:7]\n"
  "s_waitcnt vmcnt(14)\n"
  "ds_read_b128 v[200:203], v87 offset:6144\n"
  "ds_read_b128 v[204:207], v88 offset:6144\n"
  "s_add_u32 m0, s59, 4096\n"
  "ds_read_b128 v[72:75], v81 offset:4096\n"
  "ds_read_b128 v[76:79], v82 offset:4096\n"
  "v_mfma_f32_16x16x32_bf16 v[8:11], a[240:243], v[32:35], v[92:95]\n"
  "s_waitcnt lgkmcnt(4)\n"
  "buffer_load_dwordx4 v112, s[40:43], s57 offen offset:0  lds\n"
  "buffer_load_dwordx4 v113, s[40:43], s57 offen offset:1024  lds\n"
  "s_add_u32 s57, s57, 6144\n"
  "s_barrier\n"
  "v_exp_f32 v28, v28\n"
  "v_mfma_f32_16x16x32_bf16 v[8:11], a[244:247], v[36:39], v[8:11]\n"
  "s_waitcnt vmcnt(14)\n"
  "ds_read_b128 v[192:195], v87 offset:8192\n"
  "ds_read_b128 v[196:199], v88 offset:8192\n"
  "v_exp_f32 v29, v29\n"
  "v_mfma_f32_16x16x32_bf16 v[8:11], a[248:251], v[40:43], v[8:11]\n"
  "v_exp_f32 v30, v30\n"
  "v_mfma_f32_16x16x32_bf16 v[8:11], a[252:255], v[44:47], v[8:11]\n"
  "s_waitcnt vmcnt(12)\n"
  "ds_read_b128 v[184:187], v87 offset:10240\n"
  "ds_read_b128 v[188:191], v88 offset:10240\n"
  "v_exp_f32 v31, v31\n"
  "v_mfma_f32_16x16x32_bf16 v[8:11], v[128:131], v[48:51], v[8:11]\n"
  "v_cvt_pk_bf16_f32 v26, v28, v29\n"
  "v_cvt_pk_bf16_f32 v27, v30, v31\n"
  "v_mfma_f32_16x16x32_bf16 v[8:11], v[132:135], v[52:55], v[8:11]\n"
  "s_waitcnt vmcnt(10)\n"
  "ds_read_b128 v[208:211], v87 offset:12288\n"
  "ds_read_b128 v[212:215], v88 offset:12288\n"
  "ds_write_b128 v80, v[24:27] offset:3072\n"
  "v_pk_mul_f32 v[0:1], v[0:1], s[60:61] op_sel_hi:[1,0]\n"
  "v_pk_mul_f32 v[2:3], v[2:3], s[60:61] op_sel_hi:[1,0]\n"
  "v_pk_mul_f32 v[4:5], v[4:5], s[60:61] op_sel_hi:[1,0]\n"
  "v_pk_mul_f32 v[6:7], v[6:7], s[60:61] op_sel_hi:[1,0]\n"
  "v_mfma_f32_16x16x32_bf16 v[12:15], v[156:159], v[52:55], v[92:95]\n"
  "v_exp_f32 v0, v0\n"
  "v_mfma_f32_16x16x32_bf16 v[12:15], v[152:155], v[48:51], v[12:15]\n"
  "s_waitcnt vmcnt(8)\n"
  "ds_read_b128 v[216:219], v87 offset:14336\n"
  "ds_read_b128 v[220:223], v88 offset:14336\n"
  "v_exp_f32 v1, v1\n"
  "v_mfma_f32_16x16x32_bf16 v[12:15], v[148:151], v[44:47], v[12:15]\n"
  "v_exp_f32 v2, v2\n"
  "v_mfma_f32_16x16x32_bf16 v[12:15], v[144:147], v[40:43], v[12:15]\n"
  "s_waitcnt vmcnt(6)\n"
  "ds_read_b128 v[224:227], v87 offset:16384\n"
  "ds_read_b128 v[228:231], v88 offset:16384\n"
  "v_exp_f32 v3, v3\n"
  "v_mfma_f32_16x16x32_bf16 v[12:15], v[140:143], v[36:39], v[12:15]\n"
  "v_cvt_pk_bf16_f32 v0, v0, v1\n"
  "v_cvt_pk_bf16_f32 v1, v2, v3\n"
  "v_mfma_f32_16x16x32_bf16 v[12:15], v[136:139], v[32:35], v[12:15]\n"
  "s_waitcnt vmcnt(4)\n"
  "ds_read_b128 v[248:251], v87 offset:0\n"
  "ds_read_b128 v[252:255], v88 offset:0\n"
  "v_mul_f32 v8, s60, v8\n"
  "v_mul_f32 v9, s60, v9\n"
  "v_mfma_f32_16x16x32_bf16 v[16:19], v[160:163], v[32:35], v[92:95]\n"
  "v_exp_f32 v4, v4\n"
  "v_mfma_f32_16x16x32_bf16 v[16:19], v[164:167], v[36:39], v[16:19]\n"
  "s_waitcnt vmcnt(2)\n"
  "ds_read_b128 v[240:243], v87 offset:2048\n"
  "ds_read_b128 v[244:247], v88 offset:2048\n"
  "v_exp_f32 v5, v5\n"
  "v_mfma_f32_16x16x32_bf16 v[16:19], v[168:171], v[40:43], v[16:19]\n"
  "v_exp_f32 v6, v6\n"
  "v_mfma_f32_16x16x32_bf16 v[16:19], v[172:175], v[44:47], v[16:19]\n"
  "s_waitcnt vmcnt(0)\n"
  "ds_read_b128 v[232:235], v87 offset:4096\n"
  "ds_read_b128 v[236:239], v88 offset:4096\n"
  "v_exp_f32 v7, v7\n"
  "v_mfma_f32_16x16x32_bf16 v[16:19], v[176:179], v[48:51], v[16:19]\n"
  "v_cvt_pk_bf16_f32 v2, v4, v5\n"
  "v_cvt_pk_bf16_f32 v3, v6, v7\n"
  "v_mfma_f32_16x16x32_bf16 v[16:19], v[180:183], v[52:55], v[16:19]\n"
  "s_waitcnt lgkmcnt(15)\n"
  "ds_write_b128 v80, v[0:3] offset:4096\n"
  "v_pk_mul_f32 v[10:11], v[10:11], s[60:61] op_sel_hi:[1,0]\n"
  "v_pk_mul_f32 v[12:13], v[12:13], s[60:61] op_sel_hi:[1,0]\n"
  "v_pk_mul_f32 v[14:15], v[14:15], s[60:61] op_sel_hi:[1,0]\n"
  "v_mfma_f32_16x16x32_bf16 v[20:23], v[204:207], v[52:55], v[92:95]\n"
  "v_exp_f32 v8, v8\n"
  "v_mfma_f32_16x16x32_bf16 v[20:23], v[200:203], v[48:51], v[20:23]\n"
  "v_exp_f32 v9, v9\n"
  "v_mfma_f32_16x16x32_bf16 v[20:23], v[196:199], v[44:47], v[20:23]\n"
  "v_exp_f32 v10, v10\n"
  "v_mfma_f32_16x16x32_bf16 v[20:23], v[192:195], v[40:43], v[20:23]\n"
  "s_waitcnt lgkmcnt(14)\n"
  "v_exp_f32 v11, v11\n"
  "v_mfma_f32_16x16x32_bf16 v[20:23], v[188:191], v[36:39], v[20:23]\n"
  "v_cvt_pk_bf16_f32 v8, v8, v9\n"
  "v_cvt_pk_bf16_f32 v9, v10, v11\n"
  "v_mfma_f32_16x16x32_bf16 v[20:23], v[184:187], v[32:35], v[20:23]\n"
  "s_waitcnt lgkmcnt(12)\n"
  "v_mul_f32 v16, s60, v16\n"
  "v_mul_f32 v17, s60, v17\n"
  "v_mfma_f32_16x16x32_bf16 v[24:27], v[208:211], v[32:35], v[92:95]\n"
  "v_exp_f32 v12, v12\n"
  "v_mfma_f32_16x16x32_bf16 v[24:27], v[212:215], v[36:39], v[24:27]\n"
  "s_waitcnt lgkmcnt(9)\n"
  "v_exp_f32 v13, v13\n"
  "v_mfma_f32_16x16x32_bf16 v[24:27], v[216:219], v[40:43], v[24:27]\n"
  "v_exp_f32 v14, v14\n"
  "v_mfma_f32_16x16x32_bf16 v[24:27], v[220:223], v[44:47], v[24:27]\n"
  "s_waitcnt lgkmcnt(7)\n"
  "v_exp_f32 v15, v15\n"
  "v_mfma_f32_16x16x32_bf16 v[24:27], v[224:227], v[48:51], v[24:27]\n"
  "v_cvt_pk_bf16_f32 v10, v12, v13\n"
  "v_cvt_pk_bf16_f32 v11, v14, v15\n"
  "v_mfma_f32_16x16x32_bf16 v[24:27], v[228:231], v[52:55], v[24:27]\n"
  "s_waitcnt lgkmcnt(5)\n"
  "ds_write_b128 v80, v[8:11] offset:5120\n"
  "v_pk_mul_f32 v[18:19], v[18:19], s[60:61] op_sel_hi:[1,0]\n"
  "v_pk_mul_f32 v[20:21], v[20:21], s[60:61] op_sel_hi:[1,0]\n"
  "v_pk_mul_f32 v[22:23], v[22:23], s[60:61] op_sel_hi:[1,0]\n"
  "v_mfma_f32_16x16x32_bf16 v[28:31], v[252:255], v[52:55], v[92:95]\n"
  "v_exp_f32 v16, v16\n"
  "v_mfma_f32_16x16x32_bf16 v[28:31], v[248:251], v[48:51], v[28:31]\n"
  "s_waitcnt lgkmcnt(4)\n"
  "v_exp_f32 v17, v17\n"
  "v_mfma_f32_16x16x32_bf16 v[28:31], v[244:247], v[44:47], v[28:31]\n"
  "v_exp_f32 v18, v18\n"
  "v_mfma_f32_16x16x32_bf16 v[28:31], v[240:243], v[40:43], v[28:31]\n"
  "s_waitcnt lgkmcnt(2)\n"
  "s_sub_u32 s56, s56, 1\n"
  "v_exp_f32 v19, v19\n"
  "v_mfma_f32_16x16x32_bf16 v[28:31], v[236:239], v[36:39], v[28:31]\n"
  "v_cvt_pk_bf16_f32 v16, v16, v17\n"
  "v_cvt_pk_bf16_f32 v17, v18, v19\n"
  "v_mfma_f32_16x16x32_bf16 v[28:31], v[232:235], v[32:35], v[28:31]\n"
  ".p2align 6\n"
  "A_H1_%=:\n"
  "v_mfma_f32_16x16x32_bf16 v[0:3], a[0:3], v[56:59], v[92:95]\n"
  "s_add_u32 s66, s66, 6144\n"
  "v_exp_f32 v20, v20\n"
  "v_mfma_f32_16x16x32_bf16 v[0:3], a[4:7], v[60:63], v[0:3]\n"
  "s_cmp_eq_u32 s66, 86016\n"
  "v_exp_f32 v21, v21\n"
  "v_mfma_f32_16x16x32_bf16 v[0:3], a[8:11], v[64:67], v[0:3]\n"
  "s_cselect_b32 s66, 49152, s66\n"
  "v_exp_f32 v22, v22\n"
  "v_mfma_f32_16x16x32_bf16 v[0:3], a[12:15], v[68:71], v[0:3]\n"
  "v_exp_f32 v23, v23\n"
  "v_mfma_f32_16x16x32_bf16 v[0:3], a[16:19], v[72:75], v[0:3]\n"
  "v_cvt_pk_bf16_f32 v18, v20, v21\n"
  "v_cvt_pk_bf16_f32 v19, v22, v23\n"
  "v_mfma_f32_16x16x32_bf16 v[0:3], a[20:23], v[76:79], v[0:3]\n"
  "ds_write_b128 v80, v[16:19] offset:6144\n"
  "v_pk_mul_f32 v[24:25], v[24:25], s[60:61] op_sel_hi:[1,0]\n"
  "v_pk_mul_f32 v[26:27], v[26:27], s[60:61] op_sel_hi:[1,0]\n"
  "v_pk_mul_f32 v[28:29], v[28:29], s[60:61] op_sel_hi:[1,0]\n"
  "v_pk_mul_f32 v[30:31], v[30:31], s[60:61] op_sel_hi:[1,0]\n"
  "v_mfma_f32_16x16x32_bf16 v[4:7], a[44:47], v[76:79], v[92:95]\n"
  "v_exp_f32 v24, v24\n"
  "v_mfma_f32_16x16x32_bf16 v[4:7], a[40:43], v[72:75], v[4:7]\n"
  "v_exp_f32 v25, v25\n"
  "v_mfma_f32_16x16x32_bf16 v[4:7], a[36:39], v[68:71], v[4:7]\n"
  "v_exp_f32 v26, v26\n"
  "v_mfma_f32_16x16x32_bf16 v[4:7], a[32:35], v[64:67], v[4:7]\n"
  "v_exp_f32 v27, v27\n"
  "v_mfma_f32_16x16x32_bf16 v[4:7], a[28:31], v[60:63], v[4:7]\n"
  "v_cvt_pk_bf16_f32 v24, v24, v25\n"
  "v_cvt_pk_bf16_f32 v25, v26, v27\n"
  "v_mfma_f32_16x16x32_bf16 v[4:7], a[24:27], v[56:59], v[4:7]\n"
  "v_add_u32 v81, s66, v83\n"
  "v_add_u32 v82, s66, v84\n"
  "v_mfma_f32_16x16x32_bf16 v[8:11], a[48:51], v[56:59], v[92:95]\n"
  "v_exp_f32 v28, v28\n"
  "v_mfma_f32_16x16x32_bf16 v[8:11], a[52:55], v[60:63], v[8:11]\n"
  "v_exp_f32 v29, v29\n"
  "v_mfma_f32_16x16x32_bf16 v[8:11], a[56:59], v[64:67], v[8:11]\n"
  "v_exp_f32 v30, v30\n"
  "v_mfma_f32_16x16x32_bf16 v[8:11], a[60:63], v[68:71], v[8:11]\n"
  "v_exp_f32 v31, v31\n"
  "v_mfma_f32_16x16x32_bf16 v[8:11], a[64:67], v[72:75], v[8:11]\n"
  "v_cvt_pk_bf16_f32 v26, v28, v29\n"
  "v_cvt_pk_bf16_f32 v27, v30, v31\n"
  "v_mfma_f32_16x16x32_bf16 v[8:11], a[68:71], v[76:79], v[8:11]\n"
  "ds_write_b128 v80, v[24:27] offset:7168\n"
  "v_pk_mul_f32 v[0:1], v[0:1], s[60:61] op_sel_hi:[1,0]\n"
  "v_pk_mul_f32 v[2:3], v[2:3], s[60:61] op_sel_hi:[1,0]\n"
  "v_pk_mul_f32 v[4:5], v[4:5], s[60:61] op_sel_hi:[1,0]\n"
  "v_pk_mul_f32 v[6:7], v[6:7], s[60:61] op_sel_hi:[1,0]\n"
  "v_mfma_f32_16x16x32_bf16 v[12:15], a[92:95], v[76:79], v[92:95]\n"
  "v_exp_f32 v0, v0\n"
  "v_mfma_f32_16x16x32_bf16 v[12:15], a[88:91], v[72:75], v[12:15]\n"
  "v_exp_f32 v1, v1\n"
  "v_mfma_f32_16x16x32_bf16 v[12:15], a[84:87], v[68:71], v[12:15]\n"
  "v_exp_f32 v2, v2\n"
  "v_mfma_f32_16x16x32_bf16 v[12:15], a[80:83], v[64:67], v[12:15]\n"
  "v_exp_f32 v3, v3\n"
  "v_mfma_f32_16x16x32_bf16 v[12:15], a[76:79], v[60:63], v[12:15]\n"
  "v_cvt_pk_bf16_f32 v0, v0, v1\n"
  "v_cvt_pk_bf16_f32 v1, v2, v3\n"
  "v_mfma_f32_16x16x32_bf16 v[12:15], a[72:75], v[56:59], v[12:15]\n"
  "s_waitcnt lgkmcnt(0)\n"
  "s_barrier\n"
  "v_xor_b32 v80, 0x4000, v80\n"
  "v_mfma_f32_16x16x32_bf16 v[16:19], a[96:99], v[56:59], v[92:95]\n"
  "v_exp_f32 v4, v4\n"
  "v_mfma_f32_16x16x32_bf16 v[16:19], a[100:103], v[60:63], v[16:19]\n"
  "v_exp_f32 v5, v5\n"
  "v_mfma_f32_16x16x32_bf16 v[16:19], a[104:107], v[64:67], v[16:19]\n"
  "v_exp_f32 v6, v6\n"
  "v_mfma_f32_16x16x32_bf16 v[16:19], a[108:111], v[68:71], v[16:19]\n"
  "v_exp_f32 v7, v7\n"
  "v_mfma_f32_16x16x32_bf16 v[16:19], a[112:115], v[72:75], v[16:19]\n"
  "v_cvt_pk_bf16_f32 v2, v4, v5\n"
  "v_cvt_pk_bf16_f32 v3, v6, v7\n"
  "v_mfma_f32_16x16x32_bf16 v[16:19], a[116:119], v[76:79], v[16:19]\n"
  "ds_write_b128 v80, v[0:3] offset:0\n"
  "v_pk_mul_f32 v[8:9], v[8:9], s[60:61] op_sel_hi:[1,0]\n"
  "v_pk_mul_f32 v[10:11], v[10:11], s[60:61] op_sel_hi:[1,0]\n"
  "v_pk_mul_f32 v[12:13], v[12:13], s[60:61] op_sel_hi:[1,0]\n"
  "v_pk_mul_f32 v[14:15], v[14:15], s[60:61] op_sel_hi:[1,0]\n"
  "v_mfma_f32_16x16x32_bf16 v[20:23], a[140:143], v[76:79], v[92:95]\n"
  "v_exp_f32 v8, v8\n"
  "v_mfma_f32_16x16x32_bf16 v[20:23], a[136:139], v[72:75], v[20:23]\n"
  "v_exp_f32 v9, v9\n"
  "v_mfma_f32_16x16x32_bf16 v[20:23], a[132:135], v[68:71], v[20:23]\n"
  "v_exp_f32 v10, v10\n"
  "v_mfma_f32_16x16x32_bf16 v[20:23], a[128:131], v[64:67], v[20:23]\n"
  "v_exp_f32 v11, v11\n"
  "v_mfma_f32_16x16x32_bf16 v[20:23], a[124:127], v[60:63], v[20:23]\n"
  "v_cvt_pk_bf16_f32 v8, v8, v9\n"
  "v_cvt_pk_bf16_f32 v9, v10, v11\n"
  "v_mfma_f32_16x16x32_bf16 v[20:23], a[120:123], v[56:59], v[20:23]\n"
  "ds_read_b128 v[32:35], v81 offset:0\n"
  "ds_read_b128 v[36:39], v82 offset:0\n"
  "v_mfma_f32_16x16x32_bf16 v[24:27], a[144:147], v[56:59], v[92:95]\n"
  "v_exp_f32 v12, v12\n"
  "v_mfma_f32_16x16x32_bf16 v[24:27], a[148:151], v[60:63], v[24:27]\n"
  "v_exp_f32 v13, v13\n"
  "v_mfma_f32_16x16x32_bf16 v[24:27], a[152:155], v[64:67], v[24:27]\n"
  "v_exp_f32 v14, v14\n"
  "v_mfma_f32_16x16x32_bf16 v[24:27], a[156:159], v[68:71], v[24:27]\n"
  "v_exp_f32 v15, v15\n"
  "v_mfma_f32_16x16x32_bf16 v[24:27], a[160:163], v[72:75], v[24:27]\n"
  "v_cvt_pk_bf16_f32 v10, v12, v13\n"
  "v_cvt_pk_bf16_f32 v11, v14, v15\n"
  "v_mfma_f32_16x16x32_bf16 v[24:27], a[164:167], v[76:79], v[24:27]\n"
  "ds_write_b128 v80, v[8:11] offset:1024\n"
  "v_pk_mul_f32 v[16:17], v[16:17], s[60:61] op_sel_hi:[1,0]\n"
  "v_pk_mul_f32 v[18:19], v[18:19], s[60:61] op_sel_hi:[1,0]\n"
  "v_pk_mul_f32 v[20:21], v[20:21], s[60:61] op_sel_hi:[1,0]\n"
  "v_pk_mul_f32 v[22:23], v[22:23], s[60:61] op_sel_hi:[1,0]\n"
  "v_mfma_f32_16x16x32_bf16 v[28:31], a[188:191], v[76:79], v[92:95]\n"
  "v_exp_f32 v16, v16\n"
  "v_mfma_f32_16x16x32_bf16 v[28:31], a[184:187], v[72:75], v[28:31]\n"
  "v_exp_f32 v17, v17\n"
  "v_mfma_f32_16x16x32_bf16 v[28:31], a[180:183], v[68:71], v[28:31]\n"
  "v_exp_f32 v18, v18\n"
  "v_mfma_f32_16x16x32_bf16 v[28:31], a[176:179], v[64:67], v[28:31]\n"
  "v_exp_f32 v19, v19\n"
  "v_mfma_f32_16x16x32_bf16 v[28:31], a[172:175], v[60:63], v[28:31]\n"
  "v_cvt_pk_bf16_f32 v16, v16, v17\n"
  "v_cvt_pk_bf16_f32 v17, v18, v19\n"
  "v_mfma_f32_16x16x32_bf16 v[28:31], a[168:171], v[56:59], v[28:31]\n"
  "ds_read_b128 v[40:43], v81 offset:2048\n"
  "ds_read_b128 v[44:47], v82 offset:2048\n"
  "v_mfma_f32_16x16x32_bf16 v[0:3], a[192:195], v[56:59], v[92:95]\n"
  "v_exp_f32 v20, v20\n"
  "v_mfma_f32_16x16x32_bf16 v[0:3], a[196:199], v[60:63], v[0:3]\n"
  "v_exp_f32 v21, v21\n"
  "v_mfma_f32_16x16x32_bf16 v[0:3], a[200:203], v[64:67], v[0:3]\n"
  "v_exp_f32 v22, v22\n"
  "v_mfma_f32_16x16x32_bf16 v[0:3], a[204:207], v[68:71], v[0:3]\n"
  "v_exp_f32 v23, v23\n"
  "v_mfma_f32_16x16x32_bf16 v[0:3], a[208:211], v[72:75], v[0:3]\n"
  "v_cvt_pk_bf16_f32 v18, v20, v21\n"
  "v_cvt_pk_bf16_f32 v19, v22, v23\n"
  "v_mfma_f32_16x16x32_bf16 v[0:3], a[212:215], v[76:79], v[0:3]\n"
  "ds_write_b128 v80, v[16:19] offset:2048\n"
  "v_pk_mul_f32 v[24:25], v[24:25], s[60:61] op_sel_hi:[1,0]\n"
  "v_pk_mul_f32 v[26:27], v[26:27], s[60:61] op_sel_hi:[1,0]\n"
  "v_pk_mul_f32 v[28:29], v[28:29], s[60:61] op_sel_hi:[1,0]\n"
  "v_pk_mul_f32 v[30:31], v[30:31], s[60:61] op_sel_hi:[1,0]\n"
  "v_mfma_f32_16x16x32_bf16 v[4:7], a[236:239], v[76:79], v[92:95]\n"
  "v_exp_f32 v24, v24\n"
  "v_mfma_f32_16x16x32_bf16 v[4:7], a[232:235], v[72:75], v[4:7]\n"
  "v_exp_f32 v25, v25\n"
  "v_mfma_f32_16x16x32_bf16 v[4:7], a[228:231], v[68:71], v[4:7]\n"
  "v_exp_f32 v26, v26\n"
  "v_mfma_f32_16x16x32_bf16 v[4:7], a[224:227], v[64:67], v[4:7]\n"
  "v_exp_f32 v27, v27\n"
  "v_mfma_f32_16x16x32_bf16 v[4:7], a[220:223], v[60:63], v[4:7]\n"
  "v_cvt_pk_bf16_f32 v24, v24, v25\n"
  "v_cvt_pk_bf16_f32 v25, v26, v27\n"
  "v_mfma_f32_16x16x32_bf16 v[4:7], a[216:219], v[56:59], v[4:7]\n"
  "ds_read_b128 v[48:51], v81 offset:4096\n"
  "ds_read_b128 v[52:55], v82 offset:4096\n"
  "v_mfma_f32_16x16x32_bf16 v[8:11], a[240:243], v[56:59], v[92:95]\n"
  "v_exp_f32 v28, v28\n"
  "v_mfma_f32_16x16x32_bf16 v[8:11], a[244:247], v[60:63], v[8:11]\n"
  "v_exp_f32 v29, v29\n"
  "v_mfma_f32_16x16x32_bf16 v[8:11], a[248:251], v[64:67], v[8:11]\n"
  "v_exp_f32 v30, v30\n"
  "v_mfma_f32_16x16x32_bf16 v[8:11], a[252:255], v[68:71], v[8:11]\n"
  "v_exp_f32 v31, v31\n"
  "v_mfma_f32_16x16x32_bf16 v[8:11], v[128:131], v[72:75], v[8:11]\n"
  "v_cvt_pk_bf16_f32 v26, v28, v29\n"
  "v_cvt_pk_bf16_f32 v27, v30, v31\n"
  "v_mfma_f32_16x16x32_bf16 v[8:11], v[132:135], v[76:79], v[8:11]\n"
  "ds_write_b128 v80, v[24:27] offset:3072\n"
  "v_pk_mul_f32 v[0:1], v[0:1], s[60:61] op_sel_hi:[1,0]\n"
  "v_pk_mul_f32 v[2:3], v[2:3], s[60:61] op_sel_hi:[1,0]\n"
  "v_pk_mul_f32 v[4:5], v[4:5], s[60:61] op_sel_hi:[1,0]\n"
  "v_pk_mul_f32 v[6:7], v[6:7], s[60:61] op_sel_hi:[1,0]\n"
  "v_mfma_f32_16x16x32_bf16 v[12:15], v[156:159], v[76:79], v[92:95]\n"
  "v_exp_f32 v0, v0\n"
  "v_mfma_f32_16x16x32_bf16 v[12:15], v[152:155], v[72:75], v[12:15]\n"
  "v_exp_f32 v1, v1\n"
  "v_mfma_f32_16x16x32_bf16 v[12:15], v[148:151], v[68:71], v[12:15]\n"
  "v_exp_f32 v2, v2\n"
  "v_mfma_f32_16x16x32_bf16 v[12:15], v[144:147], v[64:67], v[12:15]\n"
  "v_exp_f32 v3, v3\n"
  "v_mfma_f32_16x16x32_bf16 v[12:15], v[140:143], v[60:63], v[12:15]\n"
  "v_cvt_pk_bf16_f32 v0, v0, v1\n"
  "v_cvt_pk_bf16_f32 v1, v2, v3\n"
  "v_mfma_f32_16x16x32_bf16 v[12:15], v[136:139], v[56:59], v[12:15]\n"
  "v_mul_f32 v8, s60, v8\n"
  "v_mul_f32 v9, s60, v9\n"
  "v_mfma_f32_16x16x32_bf16 v[16:19], v[160:163], v[56:59], v[92:95]\n"
  "v_exp_f32 v4, v4\n"
  "v_mfma_f32_16x16x32_bf16 v[16:19], v[164:167], v[60:63], v[16:19]\n"
  "v_exp_f32 v5, v5\n"
  "v_mfma_f32_16x16x32_bf16 v[16:19], v[168:171], v[64:67], v[16:19]\n"
  "v_exp_f32 v6, v6\n"
  "v_mfma_f32_16x16x32_bf16 v[16:19], v[172:175], v[68:71], v[16:19]\n"
  "v_exp_f32 v7, v7\n"
  "v_mfma_f32_16x16x32_bf16 v[16:19], v[176:179], v[72:75], v[16:19]\n"
  "v_cvt_pk_bf16_f32 v2, v4, v5\n"
  "v_cvt_pk_bf16_f32 v3, v6, v7\n"
  "v_mfma_f32_16x16x32_bf16 v[16:19], v[180:183], v[76:79], v[16:19]\n"
  "ds_write_b128 v80, v[0:3] offset:4096\n"
  "v_pk_mul_f32 v[10:11], v[10:11], s[60:61] op_sel_hi:[1,0]\n"
  "v_pk_mul_f32 v[12:13], v[12:13], s[60:61] op_sel_hi:[1,0]\n"
  "v_pk_mul_f32 v[14:15], v[14:15], s[60:61] op_sel_hi:[1,0]\n"
  "v_mfma_f32_16x16x32_bf16 v[20:23], v[204:207], v[76:79], v[92:95]\n"
  "v_exp_f32 v8, v8\n"
  "v_mfma_f32_16x16x32_bf16 v[20:23], v[200:203], v[72:75], v[20:23]\n"
  "v_exp_f32 v9, v9\n"
  "v_mfma_f32_16x16x32_bf16 v[20:23], v[196:199], v[68:71], v[20:23]\n"
  "v_exp_f32 v10, v10\n"
  "v_mfma_f32_16x16x32_bf16 v[20:23], v[192:195], v[64:67], v[20:23]\n"
  "v_exp_f32 v11, v11\n"
  "v_mfma_f32_16x16x32_bf16 v[20:23], v[188:191], v[60:63], v[20:23]\n"
  "v_cvt_pk_bf16_f32 v8, v8, v9\n"
  "v_cvt_pk_bf16_f32 v9, v10, v11\n"
  "v_mfma_f32_16x16x32_bf16 v[20:23], v[184:187], v[56:59], v[20:23]\n"
  "v_mul_f32 v16, s60, v16\n"
  "v_mul_f32 v17, s60, v17\n"
  "v_mfma_f32_16x16x32_bf16 v[24:27], v[208:211], v[56:59], v[92:95]\n"
  "v_exp_f32 v12, v12\n"
  "v_mfma_f32_16x16x32_bf16 v[24:27], v[212:215], v[60:63], v[24:27]\n"
  "v_exp_f32 v13, v13\n"
  "v_mfma_f32_16x16x32_bf16 v[24:27], v[216:219], v[64:67], v[24:27]\n"
  "v_exp_f32 v14, v14\n"
  "v_mfma_f32_16x16x32_bf16 v[24:27], v[220:223], v[68:71], v[24:27]\n"
  "v_exp_f32 v15, v15\n"
  "v_mfma_f32_16x16x32_bf16 v[24:27], v[224:227], v[72:75], v[24:27]\n"
  "v_cvt_pk_bf16_f32 v10, v12, v13\n"
  "v_cvt_pk_bf16_f32 v11, v14, v15\n"
  "v_mfma_f32_16x16x32_bf16 v[24:27], v[228:231], v[76:79], v[24:27]\n"
  "ds_write_b128 v80, v[8:11] offset:5120\n"
  "v_pk_mul_f32 v[18:19], v[18:19], s[60:61] op_sel_hi:[1,0]\n"
  "v_pk_mul_f32 v[20:21], v[20:21], s[60:61] op_sel_hi:[1,0]\n"
  "v_pk_mul_f32 v[22:23], v[22:23], s[60:61] op_sel_hi:[1,0]\n"
  "v_mfma_f32_16x16x32_bf16 v[28:31], v[252:255], v[76:79], v[92:95]\n"
  "v_exp_f32 v16, v16\n"
  "v_mfma_f32_16x16x32_bf16 v[28:31], v[248:251], v[72:75], v[28:31]\n"
  "v_exp_f32 v17, v17\n"
  "v_mfma_f32_16x16x32_bf16 v[28:31], v[244:247], v[68:71], v[28:31]\n"
  "v_exp_f32 v18, v18\n"
  "v_mfma_f32_16x16x32_bf16 v[28:31], v[240:243], v[64:67], v[28:31]\n"
  "s_sub_u32 s56, s56, 1\n"
  "v_exp_f32 v19, v19\n"
  "v_mfma_f32_16x16x32_bf16 v[28:31], v[236:239], v[60:63], v[28:31]\n"
  "s_cmp_eq_u32 s56, 0\n"
  "v_cvt_pk_bf16_f32 v16, v16, v17\n"
  "v_cvt_pk_bf16_f32 v17, v18, v19\n"
  "v_mfma_f32_16x16x32_bf16 v[28:31], v[232:235], v[56:59], v[28:31]\n"
  "s_waitcnt lgkmcnt(3)\n"
  "s_cbranch_scc1 A_X0_%=\n"
  "A_H0_%=:\n"
  "v_mfma_f32_16x16x32_bf16 v[0:3], a[0:3], v[32:35], v[92:95]\n"
  "s_add_u32 s66, s66, 6144\n"
  "v_exp_f32 v20, v20\n"
  "v_mfma_f32_16x16x32_bf16 v[0:3], a[4:7], v[36:39], v[0:3]\n"
  "s_cmp_eq_u32 s66, 86016\n"
  "v_exp_f32 v21, v21\n"
  "v_mfma_f32_16x16x32_bf16 v[0:3], a[8:11], v[40:43], v[0:3]\n"
  "s_cselect_b32 s66, 49152, s66\n"
  "v_exp_f32 v22, v22\n"
  "v_mfma_f32_16x16x32_bf16 v[0:3], a[12:15], v[44:47], v[0:3]\n"
  "v_exp_f32 v23, v23\n"
  "v_mfma_f32_16x16x32_bf16 v[0:3], a[16:19], v[48:51], v[0:3]\n"
  "v_cvt_pk_bf16_f32 v18, v20, v21\n"
  "v_cvt_pk_bf16_f32 v19, v22, v23\n"
  "v_mfma_f32_16x16x32_bf16 v[0:3], a[20:23], v[52:55], v[0:3]\n"
  "ds_write_b128 v80, v[16:19] offset:6144\n"
  "v_pk_mul_f32 v[24:25], v[24:25], s[60:61] op_sel_hi:[1,0]\n"
  "v_pk_mul_f32 v[26:27], v[26:27], s[60:61] op_sel_hi:[1,0]\n"
  "v_pk_mul_f32 v[28:29], v[28:29], s[60:61] op_sel_hi:[1,0]\n"
  "v_pk_mul_f32 v[30:31], v[30:31], s[60:61] op_sel_hi:[1,0]\n"
  "v_mfma_f32_16x16x32_bf16 v[4:7], a[44:47], v[52:55], v[92:95]\n"
  "v_exp_f32 v24, v24\n"
  "v_mfma_f32_16x16x32_bf16 v[4:7], a[40:43], v[48:51], v[4:7]\n"
  "v_exp_f32 v25, v25\n"
  "v_mfma_f32_16x16x32_bf16 v[4:7], a[36:39], v[44:47], v[4:7]\n"
  "v_exp_f32 v26, v26\n"
  "v_mfma_f32_16x16x32_bf16 v[4:7], a[32:35], v[40:43], v[4:7]\n"
  "v_exp_f32 v27, v27\n"
  "v_mfma_f32_16x16x32_bf16 v[4:7], a[28:31], v[36:39], v[4:7]\n"
  "v_cvt_pk_bf16_f32 v24, v24, v25\n"
  "v_cvt_pk_bf16_f32 v25, v26, v27\n"
  "v_mfma_f32_16x16x32_bf16 v[4:7], a[24:27], v[32:35], v[4:7]\n"
  "v_add_u32 v81, s66, v83\n"
  "v_add_u32 v82, s66, v84\n"
  "v_mfma_f32_16x16x32_bf16 v[8:11], a[48:51], v[32:35], v[92:95]\n"
  "v_exp_f32 v28, v28\n"
  "v_mfma_f32_16x16x32_bf16 v[8:11], a[52:55], v[36:39], v[8:11]\n"
  "v_exp_f32 v29, v29\n"
  "v_mfma_f32_16x16x32_bf16 v[8:11], a[56:59], v[40:43], v[8:11]\n"
  "v_exp_f32 v30, v30\n"
  "v_mfma_f32_16x16x32_bf16 v[8:11], a[60:63], v[44:47], v[8:11]\n"
  "v_exp_f32 v31, v31\n"
  "v_mfma_f32_16x16x32_bf16 v[8:11], a[64:67], v[48:51], v[8:11]\n"
  "v_cvt_pk_bf16_f32 v26, v28, v29\n"
  "v_cvt_pk_bf16_f32 v27, v30, v31\n"
  "v_mfma_f32_16x16x32_bf16 v[8:11], a[68:71], v[52:55], v[8:11]\n"
  "ds_write_b128 v80, v[24:27] offset:7168\n"
  "v_pk_mul_f32 v[0:1], v[0:1], s[60:61] op_sel_hi:[1,0]\n"
  "v_pk_mul_f32 v[2:3], v[2:3], s[60:61] op_sel_hi:[1,0]\n"
  "v_pk_mul_f32 v[4:5], v[4:5], s[60:61] op_sel_hi:[1,0]\n"
  "v_pk_mul_f32 v[6:7], v[6:7], s[60:61] op_sel_hi:[1,0]\n"
  "v_mfma_f32_16x16x32_bf16 v[12:15], a[92:95], v[52:55], v[92:95]\n"
  "v_exp_f32 v0, v0\n"
  "v_mfma_f32_16x16x32_bf16 v[12:15], a[88:91], v[48:51], v[12:15]\n"
  "v_exp_f32 v1, v1\n"
  "v_mfma_f32_16x16x32_bf16 v[12:15], a[84:87], v[44:47], v[12:15]\n"
  "v_exp_f32 v2, v2\n"
  "v_mfma_f32_16x16x32_bf16 v[12:15], a[80:83], v[40:43], v[12:15]\n"
  "v_exp_f32 v3, v3\n"
  "v_mfma_f32_16x16x32_bf16 v[12:15], a[76:79], v[36:39], v[12:15]\n"
  "v_cvt_pk_bf16_f32 v0, v0, v1\n"
  "v_cvt_pk_bf16_f32 v1, v2, v3\n"
  "v_mfma_f32_16x16x32_bf16 v[12:15], a[72:75], v[32:35], v[12:15]\n"
  "s_waitcnt lgkmcnt(0)\n"
  "s_barrier\n"
  "v_xor_b32 v80, 0x4000, v80\n"
  "v_mfma_f32_16x16x32_bf16 v[16:19], a[96:99], v[32:35], v[92:95]\n"
  "v_exp_f32 v4, v4\n"
  "v_mfma_f32_16x16x32_bf16 v[16:19], a[100:103], v[36:39], v[16:19]\n"
  "v_exp_f32 v5, v5\n"
  "v_mfma_f32_16x16x32_bf16 v[16:19], a[104:107], v[40:43], v[16:19]\n"
  "v_exp_f32 v6, v6\n"
  "v_mfma_f32_16x16x32_bf16 v[16:19], a[108:111], v[44:47], v[16:19]\n"
  "v_exp_f32 v7, v7\n"
  "v_mfma_f32_16x16x32_bf16 v[16:19], a[112:115], v[48:51], v[16:19]\n"
  "v_cvt_pk_bf16_f32 v2, v4, v5\n"
  "v_cvt_pk_bf16_f32 v3, v6, v7\n"
  "v_mfma_f32_16x16x32_bf16 v[16:19], a[116:119], v[52:55], v[16:19]\n"
  "ds_write_b128 v80, v[0:3] offset:0\n"
  "v_pk_mul_f32 v[8:9], v[8:9], s[60:61] op_sel_hi:[1,0]\n"
  "v_pk_mul_f32 v[10:11], v[10:11], s[60:61] op_sel_hi:[1,0]\n"
  "v_pk_mul_f32 v[12:13], v[12:13], s[60:61] op_sel_hi:[1,0]\n"
  "v_pk_mul_f32 v[14:15], v[14:15], s[60:61] op_sel_hi:[1,0]\n"
  "v_mfma_f32_16x16x32_bf16 v[20:23], a[140:143], v[52:55], v[92:95]\n"
  "v_exp_f32 v8, v8\n"
  "v_mfma_f32_16x16x32_bf16 v[20:23], a[136:139], v[48:51], v[20:23]\n"
  "v_exp_f32 v9, v9\n"
  "v_mfma_f32_16x16x32_bf16 v[20:23], a[132:135], v[44:47], v[20:23]\n"
  "v_exp_f32 v10, v10\n"
  "v_mfma_f32_16x16x32_bf16 v[20:23], a[128:131], v[40:43], v[20:23]\n"
  "v_exp_f32 v11, v11\n"
  "v_mfma_f32_16x16x32_bf16 v[20:23], a[124:127], v[36:39], v[20:23]\n"
  "v_cvt_pk_bf16_f32 v8, v8, v9\n"
  "v_cvt_pk_bf16_f32 v9, v10, v11\n"
  "v_mfma_f32_16x16x32_bf16 v[20:23], a[120:123], v[32:35], v[20:23]\n"
  "ds_read_b128 v[56:59], v81 offset:0\n"
  "ds_read_b128 v[60:63], v82 offset:0\n"
  "v_mfma_f32_16x16x32_bf16 v[24:27], a[144:147], v[32:35], v[92:95]\n"
  "v_exp_f32 v12, v12\n"
  "v_mfma_f32_16x16x32_bf16 v[24:27], a[148:151], v[36:39], v[24:27]\n"
  "v_exp_f32 v13, v13\n"
  "v_mfma_f32_16x16x32_bf16 v[24:27], a[152:155], v[40:43], v[24:27]\n"
  "v_exp_f32 v14, v14\n"
  "v_mfma_f32_16x16x32_bf16 v[24:27], a[156:159], v[44:47], v[24:27]\n"
  "v_exp_f32 v15, v15\n"
  "v_mfma_f32_16x16x32_bf16 v[24:27], a[160:163], v[48:51], v[24:27]\n"
  "v_cvt_pk_bf16_f32 v10, v12, v13\n"
  "v_cvt_pk_bf16_f32 v11, v14, v15\n"
  "v_mfma_f32_16x16x32_bf16 v[24:27], a[164:167], v[52:55], v[24:27]\n"
  "ds_write_b128 v80, v[8:11] offset:1024\n"
  "v_pk_mul_f32 v[16:17], v[16:17], s[60:61] op_sel_hi:[1,0]\n"
  "v_pk_mul_f32 v[18:19], v[18:19], s[60:61] op_sel_hi:[1,0]\n"
  "v_pk_mul_f32 v[20:21], v[20:21], s[60:61] op_sel_hi:[1,0]\n"
  "v_pk_mul_f32 v[22:23], v[22:23], s[60:61] op_sel_hi:[1,0]\n"
  "v_mfma_f32_16x16x32_bf16 v[28:31], a[188:191], v[52:55], v[92:95]\n"
  "v_exp_f32 v16, v16\n"
  "v_mfma_f32_16x16x32_bf16 v[28:31], a[184:187], v[48:51], v[28:31]\n"
  "v_exp_f32 v17, v17\n"
  "v_mfma_f32_16x16x32_bf16 v[28:31], a[180:183], v[44:47], v[28:31]\n"
  "v_exp_f32 v18, v18\n"
  "v_mfma_f32_16x16x32_bf16 v[28:31], a[176:179], v[40:43], v[28:31]\n"
  "v_exp_f32 v19, v19\n"
  "v_mfma_f32_16x16x32_bf16 v[28:31], a[172:175], v[36:39], v[28:31]\n"
  "v_cvt_pk_bf16_f32 v16, v16, v17\n"
  "v_cvt_pk_bf16_f32 v17, v18, v19\n"
  "v_mfma_f32_16x16x32_bf16 v[28:31], a[168:171], v[32:35], v[28:31]\n"
  "ds_read_b128 v[64:67], v81 offset:2048\n"
  "ds_read_b128 v[68:71], v82 offset:2048\n"
  "v_mfma_f32_16x16x32_bf16 v[0:3], a[192:195], v[32:35], v[92:95]\n"
  "v_exp_f32 v20, v20\n"
  "v_mfma_f32_16x16x32_bf16 v[0:3], a[196:199], v[36:39], v[0:3]\n"
  "v_exp_f32 v21, v21\n"
  "v_mfma_f32_16x16x32_bf16 v[0:3], a[200:203], v[40:43], v[0:3]\n"
  "v_exp_f32 v22, v22\n"
  "v_mfma_f32_16x16x32_bf16 v[0:3], a[204:207], v[44:47], v[0:3]\n"
  "v_exp_f32 v23, v23\n"
  "v_mfma_f32_16x16x32_bf16 v[0:3], a[208:211], v[48:51], v[0:3]\n"
  "v_cvt_pk_bf16_f32 v18, v20, v21\n"
  "v_cvt_pk_bf16_f32 v19, v22, v23\n"
  "v_mfma_f32_16x16x32_bf16 v[0:3], a[212:215], v[52:55], v[0:3]\n"
  "ds_write_b128 v80, v[16:19] offset:2048\n"
  "v_pk_mul_f32 v[24:25], v[24:25], s[60:61] op_sel_hi:[1,0]\n"
  "v_pk_mul_f32 v[26:27], v[26:27], s[60:61] op_sel_hi:[1,0]\n"
  "v_pk_mul_f32 v[28:29], v[28:29], s[60:61] op_sel_hi:[1,0]\n"
  "v_pk_mul_f32 v[30:31], v[30:31], s[60:61] op_sel_hi:[1,0]\n"
  "v_mfma_f32_16x16x32_bf16 v[4:7], a[236:239], v[52:55], v[92:95]\n"
  "v_exp_f32 v24, v24\n"
  "v_mfma_f32_16x16x32_bf16 v[4:7], a[232:235], v[48:51], v[4:7]\n"
  "v_exp_f32 v25, v25\n"
  "v_mfma_f32_16x16x32_bf16 v[4:7], a[228:231], v[44:47], v[4:7]\n"
  "v_exp_f32 v26, v26\n"
  "v_mfma_f32_16x16x32_bf16 v[4:7], a[224:227], v[40:43], v[4:7]\n"
  "v_exp_f32 v27, v27\n"
  "v_mfma_f32_16x16x32_bf16 v[4:7], a[220:223], v[36:39], v[4:7]\n"
  "v_cvt_pk_bf16_f32 v24, v24, v25\n"
  "v_cvt_pk_bf16_f32 v25, v26, v27\n"
  "v_mfma_f32_16x16x32_bf16 v[4:7], a[216:219], v[32:35], v[4:7]\n"
  "ds_read_b128 v[72:75], v81 offset:4096\n"
  "ds_read_b128 v[76:79], v82 offset:4096\n"
  "v_mfma_f32_16x16x32_bf16 v[8:11], a[240:243], v[32:35], v[92:95]\n"
  "v_exp_f32 v28, v28\n"
  "v_mfma_f32_16x16x32_bf16 v[8:11], a[244:247], v[36:39], v[8:11]\n"
  "v_exp_f32 v29, v29\n"
  "v_mfma_f32_16x16x32_bf16 v[8:11], a[248:251], v[40:43], v[8:11]\n"
  "v_exp_f32 v30, v30\n"
  "v_mfma_f32_16x16x32_bf16 v[8:11], a[252:255], v[44:47], v[8:11]\n"
  "v_exp_f32 v31, v31\n"
  "v_mfma_f32_16x16x32_bf16 v[8:11], v[128:131], v[48:51], v[8:11]\n"
  "v_cvt_pk_bf16_f32 v26, v28, v29\n"
  "v_cvt_pk_bf16_f32 v27, v30, v31\n"
  "v_mfma_f32_16x16x32_bf16 v[8:11], v[132:135], v[52:55], v[8:11]\n"
  "ds_write_b128 v80, v[24:27] offset:3072\n"
  "v_pk_mul_f32 v[0:1], v[0:1], s[60:61] op_sel_hi:[1,0]\n"
  "v_pk_mul_f32 v[2:3], v[2:3], s[60:61] op_sel_hi:[1,0]\n"
  "v_pk_mul_f32 v[4:5], v[4:5], s[60:61] op_sel_hi:[1,0]\n"
  "v_pk_mul_f32 v[6:7], v[6:7], s[60:61] op_sel_hi:[1,0]\n"
  "v_mfma_f32_16x16x32_bf16 v[12:15], v[156:159], v[52:55], v[92:95]\n"
  "v_exp_f32 v0, v0\n"
  "v_mfma_f32_16x16x32_bf16 v[12:15], v[152:155], v[48:51], v[12:15]\n"
  "v_exp_f32 v1, v1\n"
  "v_mfma_f32_16x16x32_bf16 v[12:15], v[148:151], v[44:47], v[12:15]\n"
  "v_exp_f32 v2, v2\n"
  "v_mfma_f32_16x16x32_bf16 v[12:15], v[144:147], v[40:43], v[12:15]\n"
  "v_exp_f32 v3, v3\n"
  "v_mfma_f32_16x16x32_bf16 v[12:15], v[140:143], v[36:39], v[12:15]\n"
  "v_cvt_pk_bf16_f32 v0, v0, v1\n"
  "v_cvt_pk_bf16_f32 v1, v2, v3\n"
  "v_mfma_f32_16x16x32_bf16 v[12:15], v[136:139], v[32:35], v[12:15]\n"
  "v_mul_f32 v8, s60, v8\n"
  "v_mul_f32 v9, s60, v9\n"
  "v_mfma_f32_16x16x32_bf16 v[16:19], v[160:163], v[32:35], v[92:95]\n"
  "v_exp_f32 v4, v4\n"
  "v_mfma_f32_16x16x32_bf16 v[16:19], v[164:167], v[36:39], v[16:19]\n"
  "v_exp_f32 v5, v5\n"
  "v_mfma_f32_16x16x32_bf16 v[16:19], v[168:171], v[40:43], v[16:19]\n"
  "v_exp_f32 v6, v6\n"
  "v_mfma_f32_16x16x32_bf16 v[16:19], v[172:175], v[44:47], v[16:19]\n"
  "v_exp_f32 v7, v7\n"
  "v_mfma_f32_16x16x32_bf16 v[16:19], v[176:179], v[48:51], v[16:19]\n"
  "v_cvt_pk_bf16_f32 v2, v4, v5\n"
  "v_cvt_pk_bf16_f32 v3, v6, v7\n"
  "v_mfma_f32_16x16x32_bf16 v[16:19], v[180:183], v[52:55], v[16:19]\n"
  "ds_write_b128 v80, v[0:3] offset:4096\n"
  "v_pk_mul_f32 v[10:11], v[10:11], s[60:61] op_sel_hi:[1,0]\n"
  "v_pk_mul_f32 v[12:13], v[12:13], s[60:61] op_sel_hi:[1,0]\n"
  "v_pk_mul_f32 v[14:15], v[14:15], s[60:61] op_sel_hi:[1,0]\n"
  "v_mfma_f32_16x16x32_bf16 v[20:23], v[204:207], v[52:55], v[92:95]\n"
  "v_exp_f32 v8, v8\n"
  "v_mfma_f32_16x16x32_bf16 v[20:23], v[200:203], v[48:51], v[20:23]\n"
  "v_exp_f32 v9, v9\n"
  "v_mfma_f32_16x16x32_bf16 v[20:23], v[196:199], v[44:47], v[20:23]\n"
  "v_exp_f32 v10, v10\n"
  "v_mfma_f32_16x16x32_bf16 v[20:23], v[192:195], v[40:43], v[20:23]\n"
  "v_exp_f32 v11, v11\n"
  "v_mfma_f32_16x16x32_bf16 v[20:23], v[188:191], v[36:39], v[20:23]\n"
  "v_cvt_pk_bf16_f32 v8, v8, v9\n"
  "v_cvt_pk_bf16_f32 v9, v10, v11\n"
  "v_mfma_f32_16x16x32_bf16 v[20:23], v[184:187], v[32:35], v[20:23]\n"
  "v_mul_f32 v16, s60, v16\n"
  "v_mul_f32 v17, s60, v17\n"
  "v_mfma_f32_16x16x32_bf16 v[24:27], v[208:211], v[32:35], v[92:95]\n"
  "v_exp_f32 v12, v12\n"
  "v_mfma_f32_16x16x32_bf16 v[24:27], v[212:215], v[36:39], v[24:27]\n"
  "v_exp_f32 v13, v13\n"
  "v_mfma_f32_16x16x32_bf16 v[24:27], v[216:219], v[40:43], v[24:27]\n"
  "v_exp_f32 v14, v14\n"
  "v_mfma_f32_16x16x32_bf16 v[24:27], v[220:223], v[44:47], v[24:27]\n"
  "v_exp_f32 v15, v15\n"
  "v_mfma_f32_16x16x32_bf16 v[24:27], v[224:227], v[48:51], v[24:27]\n"
  "v_cvt_pk_bf16_f32 v10, v12, v13\n"
  "v_cvt_pk_bf16_f32 v11, v14, v15\n"
  "v_mfma_f32_16x16x32_bf16 v[24:27], v[228:231], v[52:55], v[24:27]\n"
  "ds_write_b128 v80, v[8:11] offset:5120\n"
  "v_pk_mul_f32 v[18:19], v[18:19], s[60:61] op_sel_hi:[1,0]\n"
  "v_pk_mul_f32 v[20:21], v[20:21], s[60:61] op_sel_hi:[1,0]\n"
  "v_pk_mul_f32 v[22:23], v[22:23], s[60:61] op_sel_hi:[1,0]\n"
  "v_mfma_f32_16x16x32_bf16 v[28:31], v[252:255], v[52:55], v[92:95]\n"
  "v_exp_f32 v16, v16\n"
  "v_mfma_f32_16x16x32_bf16 v[28:31], v[248:251], v[48:51], v[28:31]\n"
  "v_exp_f32 v17, v17\n"
  "v_mfma_f32_16x16x32_bf16 v[28:31], v[244:247], v[44:47], v[28:31]\n"
  "v_exp_f32 v18, v18\n"
  "v_mfma_f32_16x16x32_bf16 v[28:31], v[240:243], v[40:43], v[28:31]\n"
  "s_sub_u32 s56, s56, 1\n"
  "v_exp_f32 v19, v19\n"
  "v_mfma_f32_16x16x32_bf16 v[28:31], v[236:239], v[36:39], v[28:31]\n"
  "s_cmp_eq_u32 s56, 0\n"
  "v_cvt_pk_bf16_f32 v16, v16, v17\n"
  "v_cvt_pk_bf16_f32 v17, v18, v19\n"
  "v_mfma_f32_16x16x32_bf16 v[28:31], v[232:235], v[32:35], v[28:31]\n"
  "s_waitcnt lgkmcnt(3)\n"
  "s_cbranch_scc0 A_H1_%=\n"
  "A_X1_%=:\n"
  "s_mov_b32 s52, s53\n"
  "s_mov_b32 s53, s54\n"
  "s_mov_b32 s54, s55\n"
  "s_cmp_eq_u32 s52, 0\n"
  "s_cbranch_scc1 A_LAST_%=\n"
  "s_lshr_b32 s62, s52, 16\n"
  "s_lshl1_add_u32 s62, s62, s65\n"
  "s_mul_i32 s57, s62, 98304\n"
  "s_and_b32 s56, s52, 0xff\n"
  "s_cmp_lg_u32 s53, 0\n"
  "s_cselect_b32 s62, 1, 0\n"
  "s_sub_u32 s56, s56, s62\n"
  "s_add_u32 s57, s57, 18432\n"
  "v_mfma_f32_16x16x32_bf16 v[0:3], a[0:3], v[56:59], v[92:95]\n"
  "s_add_u32 s66, s66, 6144\n"
  "s_cmp_eq_u32 s66, 86016\n"
  "s_cselect_b32 s66, 49152, s66\n"
  "v_exp_f32 v20, v20\n"
  "v_mfma_f32_16x16x32_bf16 v[0:3], a[4:7], v[60:63], v[0:3]\n"
  "ds_read_b128 a[0:3], v87 offset:0\n"
  "ds_read_b128 a[4:7], v88 offset:0\n"
  "v_exp_f32 v21, v21\n"
  "v_mfma_f32_16x16x32_bf16 v[0:3], a[8:11], v[64:67], v[0:3]\n"
  "v_exp_f32 v22, v22\n"
  "v_mfma_f32_16x16x32_bf16 v[0:3], a[12:15], v[68:71], v[0:3]\n"
  "v_exp_f32 v23, v23\n"
  "v_mfma_f32_16x16x32_bf16 v[0:3], a[16:19], v[72:75], v[0:3]\n"
  "v_cvt_pk_bf16_f32 v18, v20, v21\n"
  "v_cvt_pk_bf16_f32 v19, v22, v23\n"
  "v_mfma_f32_16x16x32_bf16 v[0:3], a[20:23], v[76:79], v[0:3]\n"
  "ds_read_b128 a[8:11], v87 offset:2048\n"
  "ds_read_b128 a[12:15], v88 offset:2048\n"
  "ds_write_b128 v80, v[16:19] offset:6144\n"
  "v_pk_mul_f32 v[24:25], v[24:25], s[60:61] op_sel_hi:[1,0]\n"
  "v_pk_mul_f32 v[26:27], v[26:27], s[60:61] op_sel_hi:[1,0]\n"
  "v_pk_mul_f32 v[28:29], v[28:29], s[60:61] op_sel_hi:[1,0]\n"
  "v_pk_mul_f32 v[30:31], v[30:31], s[60:61] op_sel_hi:[1,0]\n"
  "v_mfma_f32_16x16x32_bf16 v[4:7], a[44:47], v[76:79], v[92:95]\n"
  "v_exp_f32 v24, v24\n"
  "v_mfma_f32_16x16x32_bf16 v[4:7], a[40:43], v[72:75], v[4:7]\n"
  "s_add_u32 m0, s59, -256\n"
  "v_exp_f32 v25, v25\n"
  "v_mfma_f32_16x16x32_bf16 v[4:7], a[36:39], v[68:71], v[4:7]\n"
  "s_waitcnt lgkmcnt(3)\n"
  "buffer_load_dwordx4 v112, s[40:43], s57 offen offset:256 nt lds\n"
  "buffer_load_dwordx4 v113, s[40:43], s57 offen offset:1280 nt lds\n"
  "v_exp_f32 v26, v26\n"
  "v_mfma_f32_16x16x32_bf16 v[4:7], a[32:35], v[64:67], v[4:7]\n"
  "ds_read_b128 a[16:19], v87 offset:4096\n"
  "ds_read_b128 a[20:23], v88 offset:4096\n"
  "v_exp_f32 v27, v27\n"
  "v_mfma_f32_16x16x32_bf16 v[4:7], a[28:31], v[60:63], v[4:7]\n"
  "s_add_u32 m0, s59, 1920\n"
  "v_cvt_pk_bf16_f32 v24, v24, v25\n"
  "v_cvt_pk_bf16_f32 v25, v26, v27\n"
  "v_mfma_f32_16x16x32_bf16 v[4:7], a[24:27], v[56:59], v[4:7]\n"
  "s_waitcnt lgkmcnt(3)\n"
  "buffer_load_dwordx4 v112, s[40:43], s57 offen offset:128 nt lds\n"
  "buffer_load_dwordx4 v113, s[40:43], s57 offen offset:1152 nt lds\n"
  "v_add_u32 v81, s66, v83\n"
  "v_add_u32 v82, s66, v84\n"
  "v_mfma_f32_16x16x32_bf16 v[8:11], a[48:51], v[56:59], v[92:95]\n"
  "ds_read_b128 a[40:43], v87 offset:6144\n"
  "ds_read_b128 a[44:47], v88 offset:6144\n"
  "v_exp_f32 v28, v28\n"
  "v_mfma_f32_16x16x32_bf16 v[8:11], a[52:55], v[60:63], v[8:11]\n"
  "v_exp_f32 v29, v29\n"
  "v_mfma_f32_16x16x32_bf16 v[8:11], a[56:59], v[64:67], v[8:11]\n"
  "s_add_u32 m0, s59, 4096\n"
  "v_exp_f32 v30, v30\n"
  "v_mfma_f32_16x16x32_bf16 v[8:11], a[60:63], v[68:71], v[8:11]\n"
  "s_waitcnt lgkmcnt(2)\n"
  "buffer_load_dwordx4 v112, s[40:43], s57 offen offset:0 nt lds\n"
  "buffer_load_dwordx4 v113, s[40:43], s57 offen offset:1024 nt lds\n"
  "s_add_u32 s57, s57, 6144\n"
  "v_exp_f32 v31, v31\n"
  "v_mfma_f32_16x16x32_bf16 v[8:11], a[64:67], v[72:75], v[8:11]\n"
  "ds_read_b128 a[32:35], v87 offset:8192\n"
  "ds_read_b128 a[36:39], v88 offset:8192\n"
  "v_cvt_pk_bf16_f32 v26, v28, v29\n"
  "v_cvt_pk_bf16_f32 v27, v30, v31\n"
  "v_mfma_f32_16x16x32_bf16 v[8:11], a[68:71], v[76:79], v[8:11]\n"
  "s_add_u32 m0, s59, 6144\n"
  "ds_write_b128 v80, v[24:27] offset:7168\n"
  "v_pk_mul_f32 v[0:1], v[0:1], s[60:61] op_sel_hi:[1,0]\n"
  "v_pk_mul_f32 v[2:3], v[2:3], s[60:61] op_sel_hi:[1,0]\n"
  "v_pk_mul_f32 v[4:5], v[4:5], s[60:61] op_sel_hi:[1,0]\n"
  "v_pk_mul_f32 v[6:7], v[6:7], s[60:61] op_sel_hi:[1,0]\n"
  "v_mfma_f32_16x16x32_bf16 v[12:15], a[92:95], v[76:79], v[92:95]\n"
  "s_waitcnt lgkmcnt(3)\n"
  "buffer_load_dwordx4 v112, s[40:43], s57 offen offset:0 nt lds\n"
  "buffer_load_dwordx4 v113, s[40:43], s57 offen offset:1024 nt lds\n"
  "v_exp_f32 v0, v0\n"
  "v_mfma_f32_16x16x32_bf16 v[12:15], a[88:91], v[72:75], v[12:15]\n"
  "ds_read_b128 a[24:27], v87 offset:10240\n"
  "ds_read_b128 a[28:31], v88 offset:10240\n"
  "v_exp_f32 v1, v1\n"
  "v_mfma_f32_16x16x32_bf16 v[12:15], a[84:87], v[68:71], v[12:15]\n"
  "v_exp_f32 v2, v2\n"
  "v_mfma_f32_16x16x32_bf16 v[12:15], a[80:83], v[64:67], v[12:15]\n"
  "s_add_u32 m0, s59, 8064\n"
  "v_exp_f32 v3, v3\n"
  "v_mfma_f32_16x16x32_bf16 v[12:15], a[76:79], v[60:63], v[12:15]\n"
  "s_waitcnt lgkmcnt(3)\n"
  "buffer_load_dwordx4 v112, s[40:43], s57 offen offset:128 nt lds\n"
  "buffer_load_dwordx4 v113, s[40:43], s57 offen offset:1152 nt lds\n"
  "v_cvt_pk_bf16_f32 v0, v0, v1\n"
  "v_cvt_pk_bf16_f32 v1, v2, v3\n"
  "v_mfma_f32_16x16x32_bf16 v[12:15], a[72:75], v[56:59], v[12:15]\n"
  "ds_read_b128 a[48:51], v87 offset:12288\n"
  "ds_read_b128 a[52:55], v88 offset:12288\n"
  "s_waitcnt lgkmcnt(4)\n"
  "s_barrier\n"
  "v_xor_b32 v80, 0x4000, v80\n"
  "v_mfma_f32_16x16x32_bf16 v[16:19], a[96:99], v[56:59], v[92:95]\n"
  "v_exp_f32 v4, v4\n"
  "v_mfma_f32_16x16x32_bf16 v[16:19], a[100:103], v[60:63], v[16:19]\n"
  "s_add_u32 m0, s59, 9984\n"
  "v_exp_f32 v5, v5\n"
  "v_mfma_f32_16x16x32_bf16 v[16:19], a[104:107], v[64:67], v[16:19]\n"
  "s_waitcnt lgkmcnt(2)\n"
  "buffer_load_dwordx4 v112, s[40:43], s57 offen offset:256 nt lds\n"
  "buffer_load_dwordx4 v113, s[40:43], s57 offen offset:1280 nt lds\n"
  "s_add_u32 s57, s57, 6144\n"
  "v_exp_f32 v6, v6\n"
  "v_mfma_f32_16x16x32_bf16 v[16:19], a[108:111], v[68:71], v[16:19]\n"
  "ds_read_b128 a[56:59], v87 offset:14336\n"
  "ds_read_b128 a[60:63], v88 offset:14336\n"
  "v_exp_f32 v7, v7\n"
  "v_mfma_f32_16x16x32_bf16 v[16:19], a[112:115], v[72:75], v[16:19]\n"
  "s_add_u32 m0, s59, 12032\n"
  "v_cvt_pk_bf16_f32 v2, v4, v5\n"
  "v_cvt_pk_bf16_f32 v3, v6, v7\n"
  "v_mfma_f32_16x16x32_bf16 v[16:19], a[116:119], v[76:79], v[16:19]\n"
  "s_waitcnt lgkmcnt(2)\n"
  "buffer_load_dwordx4 v112, s[40:43], s57 offen offset:256 nt lds\n"
  "buffer_load_dwordx4 v113, s[40:43], s57 offen offset:1280 nt lds\n"
  "ds_write_b128 v80, v[0:3] offset:0\n"
  "v_pk_mul_f32 v[8:9], v[8:9], s[60:61] op_sel_hi:[1,0]\n"
  "v_pk_mul_f32 v[10:11], v[10:11], s[60:61] op_sel_hi:[1,0]\n"
  "v_pk_mul_f32 v[12:13], v[12:13], s[60:61] op_sel_hi:[1,0]\n"
  "v_pk_mul_f32 v[14:15], v[14:15], s[60:61] op_sel_hi:[1,0]\n"
  "v_mfma_f32_16x16x32_bf16 v[20:23], a[140:143], v[76:79], v[92:95]\n"
  "ds_read_b128 a[64:67], v87 offset:16384\n"
  "ds_read_b128 a[68:71], v88 offset:16384\n"
  "v_exp_f32 v8, v8\n"
  "v_mfma_f32_16x16x32_bf16 v[20:23], a[136:139], v[72:75], v[20:23]\n"
  "v_exp_f32 v9, v9\n"
  "v_mfma_f32_16x16x32_bf16 v[20:23], a[132:135], v[68:71], v[20:23]\n"
  "s_add_u32 m0, s59, 14208\n"
  "v_exp_f32 v10, v10\n"
  "v_mfma_f32_16x16x32_bf16 v[20:23], a[128:131], v[64:67], v[20:23]\n"
  "s_waitcnt lgkmcnt(3)\n"
  "buffer_load_dwordx4 v112, s[40:43], s57 offen offset:128 nt lds\n"
  "buffer_load_dwordx4 v113, s[40:43], s57 offen offset:1152 nt lds\n"
  "v_exp_f32 v11, v11\n"
  "v_mfma_f32_16x16x32_bf16 v[20:23], a[124:127], v[60:63], v[20:23]\n"
  "s_waitcnt vmcnt(14)\n"
  "ds_read_b128 a[88:91], v87 offset:0\n"
  "ds_read_b128 a[92:95], v88 offset:0\n"
  "v_cvt_pk_bf16_f32 v8, v8, v9\n"
  "v_cvt_pk_bf16_f32 v9, v10, v11\n"
  "v_mfma_f32_16x16x32_bf16 v[20:23], a[120:123], v[56:59], v[20:23]\n"
  "s_add_u32 m0, s59, 16384\n"
  "ds_read_b128 v[32:35], v81 offset:0\n"
  "ds_read_b128 v[36:39], v82 offset:0\n"
  "v_mfma_f32_16x16x32_bf16 v[24:27], a[144:147], v[56:59], v[92:95]\n"
  "s_waitcnt lgkmcnt(4)\n"
  "buffer_load_dwordx4 v112, s[40:43], s57 offen offset:0 nt lds\n"
  "buffer_load_dwordx4 v113, s[40:43], s57 offen offset:1024 nt lds\n"
  "s_add_u32 s57, s57, 6144\n"
  "v_exp_f32 v12, v12\n"
  "v_mfma_f32_16x16x32_bf16 v[24:27], a[148:151], v[60:63], v[24:27]\n"
  "s_waitcnt vmcnt(14)\n"
  "ds_read_b128 a[80:83], v87 offset:2048\n"
  "ds_read_b128 a[84:87], v88 offset:2048\n"
  "v_exp_f32 v13, v13\n"
  "v_mfma_f32_16x16x32_bf16 v[24:27], a[152:155], v[64:67], v[24:27]\n"
  "v_exp_f32 v14, v14\n"
  "v_mfma_f32_16x16x32_bf16 v[24:27], a[156:159], v[68:71], v[24:27]\n"
  "s_add_u32 m0, s59, 0\n"
  "v_exp_f32 v15, v15\n"
  "v_mfma_f32_16x16x32_bf16 v[24:27], a[160:163], v[72:75], v[24:27]\n"
  "s_waitcnt lgkmcnt(4)\n"
  "buffer_load_dwordx4 v112, s[40:43], s57 offen offset:0 nt lds\n"
  "buffer_load_dwordx4 v113, s[40:43], s57 offen offset:1024 nt lds\n"
  "v_cvt_pk_bf16_f32 v10, v12, v13\n"
  "v_cvt_pk_bf16_f32 v11, v14, v15\n"
  "v_mfma_f32_16x16x32_bf16 v[24:27], a[164:167], v[76:79], v[24:27]\n"
  "s_waitcnt vmcnt(14)\n"
  "ds_read_b128 a[72:75], v87 offset:4096\n"
  "ds_read_b128 a[76:79], v88 offset:4096\n"
  "ds_write_b128 v80, v[8:11] offset:1024\n"
  "v_pk_mul_f32 v[16:17], v[16:17], s[60:61] op_sel_hi:[1,0]\n"
  "v_pk_mul_f32 v[18:19], v[18:19], s[60:61] op_sel_hi:[1,0]\n"
  "v_pk_mul_f32 v[20:21], v[20:21], s[60:61] op_sel_hi:[1,0]\n"
  "v_pk_mul_f32 v[22:23], v[22:23], s[60:61] op_sel_hi:[1,0]\n"
  "v_mfma_f32_16x16x32_bf16 v[28:31], a[188:191], v[76:79], v[92:95]\n"
  "v_exp_f32 v16, v16\n"
  "v_mfma_f32_16x16x32_bf16 v[28:31], a[184:187], v[72:75], v[28:31]\n"
  "s_add_u32 m0, s59, 1920\n"
  "v_exp_f32 v17, v17\n"
  "v_mfma_f32_16x16x32_bf16 v[28:31], a[180:183], v[68:71], v[28:31]\n"
  "s_waitcnt lgkmcnt(3)\n"
  "buffer_load_dwordx4 v112, s[40:43], s57 offen offset:128 nt lds\n"
  "buffer_load_dwordx4 v113, s[40:43], s57 offen offset:1152 nt lds\n"
  "v_exp_f32 v18, v18\n"
  "v_mfma_f32_16x16x32_bf16 v[28:31], a[176:179], v[64:67], v[28:31]\n"
  "s_waitcnt vmcnt(14)\n"
  "ds_read_b128 a[96:99], v87 offset:6144\n"
  "ds_read_b128 a[100:103], v88 offset:6144\n"
  "v_exp_f32 v19, v19\n"
  "v_mfma_f32_16x16x32_bf16 v[28:31], a[172:175], v[60:63], v[28:31]\n"
  "s_add_u32 m0, s59, 3840\n"
  "v_cvt_pk_bf16_f32 v16, v16, v17\n"
  "v_cvt_pk_bf16_f32 v17, v18, v19\n"
  "v_mfma_f32_16x16x32_bf16 v[28:31], a[168:171], v[56:59], v[28:31]\n"
  "s_waitcnt lgkmcnt(3)\n"
  "buffer_load_dwordx4 v112, s[40:43], s57 offen offset:256 nt lds\n"
  "buffer_load_dwordx4 v113, s[40:43], s57 offen offset:1280 nt lds\n"
  "s_add_u32 s57, s57, 6144\n"
  "ds_read_b128 v[40:43], v81 offset:2048\n"
  "ds_read_b128 v[44:47], v82 offset:2048\n"
  "v_mfma_f32_16x16x32_bf16 v[0:3], a[192:195], v[56:59], v[92:95]\n"
  "s_waitcnt vmcnt(14)\n"
  "ds_read_b128 a[104:107], v87 offset:8192\n"
  "ds_read_b128 a[108:111], v88 offset:8192\n"
  "v_exp_f32 v20, v20\n"
  "v_mfma_f32_16x16x32_bf16 v[0:3], a[196:199], v[60:63], v[0:3]\n"
  "v_exp_f32 v21, v21\n"
  "v_mfma_f32_16x16x32_bf16 v[0:3], a[200:203], v[64:67], v[0:3]\n"
  "s_add_u32 m0, s59, 5888\n"
  "v_exp_f32 v22, v22\n"
  "v_mfma_f32_16x16x32_bf16 v[0:3], a[204:207], v[68:71], v[0:3]\n"
  "s_waitcnt lgkmcnt(4)\n"
  "buffer_load_dwordx4 v112, s[40:43], s57 offen offset:256 nt lds\n"
  "buffer_load_dwordx4 v113, s[40:43], s57 offen offset:1280 nt lds\n"
  "v_exp_f32 v23, v23\n"
  "v_mfma_f32_16x16x32_bf16 v[0:3], a[208:211], v[72:75], v[0:3]\n"
  "s_waitcnt vmcnt(14)\n"
  "ds_read_b128 a[112:115], v87 offset:10240\n"
  "ds_read_b128 a[116:119], v88 offset:10240\n"
  "v_cvt_pk_bf16_f32 v18, v20, v21\n"
  "v_cvt_pk_bf16_f32 v19, v22, v23\n"
  "v_mfma_f32_16x16x32_bf16 v[0:3], a[212:215], v[76:79], v[0:3]\n"
  "s_add_u32 m0, s59, 8064\n"
  "ds_write_b128 v80, v[16:19] offset:2048\n"
  "v_pk_mul_f32 v[24:25], v[24:25], s[60:61] op_sel_hi:[1,0]\n"
  "v_pk_mul_f32 v[26:27], v[26:27], s[60:61] op_sel_hi:[1,0]\n"
  "v_pk_mul_f32 v[28:29], v[28:29], s[60:61] op_sel_hi:[1,0]\n"
  "v_pk_mul_f32 v[30:31], v[30:31], s[60:61] op_sel_hi:[1,0]\n"
  "v_mfma_f32_16x16x32_bf16 v[4:7], a[236:239], v[76:79], v[92:95]\n"
  "s_waitcnt lgkmcnt(3)\n"
  "buffer_load_dwordx4 v112, s[40:43], s57 offen offset:128 nt lds\n"
  "buffer_load_dwordx4 v113, s[40:43], s57 offen offset:1152 nt lds\n"
  "v_exp_f32 v24, v24\n"
  "v_mfma_f32_16x16x32_bf16 v[4:7], a[232:235], v[72:75], v[4:7]\n"
  "s_waitcnt vmcnt(14)\n"
  "ds_read_b128 a[136:139], v87 offset:12288\n"
  "ds_read_b128 a[140:143], v88 offset:12288\n"
  "v_exp_f32 v25, v25\n"
  "v_mfma_f32_16x16x32_bf16 v[4:7], a[228:231], v[68:71], v[4:7]\n"
  "v_exp_f32 v26, v26\n"
  "v_mfma_f32_16x16x32_bf16 v[4:7], a[224:227], v[64:67], v[4:7]\n"
  "s_add_u32 m0, s59, 10240\n"
  "v_exp_f32 v27, v27\n"
  "v_mfma_f32_16x16x32_bf16 v[4:7], a[220:223], v[60:63], v[4:7]\n"
  "s_waitcnt lgkmcnt(3)\n"
  "buffer_load_dwordx4 v112, s[40:43], s57 offen offset:0 nt lds\n"
  "buffer_load_dwordx4 v113, s[40:43], s57 offen offset:1024 nt lds\n"
  "s_add_u32 s57, s57, 6144\n"
  "v_cvt_pk_bf16_f32 v24, v24, v25\n"
  "v_cvt_pk_bf16_f32 v25, v26, v27\n"
  "v_mfma_f32_16x16x32_bf16 v[4:7], a[216:219], v[56:59], v[4:7]\n"
  "s_waitcnt vmcnt(14)\n"
  "ds_read_b128 a[128:131], v87 offset:14336\n"
  "ds_read_b128 a[132:135], v88 offset:14336\n"
  "ds_read_b128 v[48:51], v81 offset:4096\n"
  "ds_read_b128 v[52:55], v82 offset:4096\n"
  "v_mfma_f32_16x16x32_bf16 v[8:11], a[240:243], v[56:59], v[92:95]\n"
  "v_exp_f32 v28, v28\n"
  "v_mfma_f32_16x16x32_bf16 v[8:11], a[244:247], v[60:63], v[8:11]\n"
  "s_add_u32 m0, s59, 12288\n"
  "v_exp_f32 v29, v29\n"
  "v_mfma_f32_16x16x32_bf16 v[8:11], a[248:251], v[64:67], v[8:11]\n"
  "s_waitcnt lgkmcnt(4)\n"
  "buffer_load_dwordx4 v112, s[40:43], s57 offen offset:0 nt lds\n"
  "buffer_load_dwordx4 v113, s[40:43], s57 offen offset:1024 nt lds\n"
  "v_exp_f32 v30, v30\n"
  "v_mfma_f32_16x16x32_bf16 v[8:11], a[252:255], v[68:71], v[8:11]\n"
  "s_waitcnt vmcnt(14)\n"
  "ds_read_b128 a[120:123], v87 offset:16384\n"
  "ds_read_b128 a[124:127], v88 offset:16384\n"
  "v_exp_f32 v31, v31\n"
  "v_mfma_f32_16x16x32_bf16 v[8:11], v[128:131], v[72:75], v[8:11]\n"
  "s_add_u32 m0, s59, 14208\n"
  "v_cvt_pk_bf16_f32 v26, v28, v29\n"
  "v_cvt_pk_bf16_f32 v27, v30, v31\n"
  "v_mfma_f32_16x16x32_bf16 v[8:11], v[132:135], v[76:79], v[8:11]\n"
  "s_waitcnt lgkmcnt(4)\n"
  "buffer_load_dwordx4 v112, s[40:43], s57 offen offset:128 nt lds\n"
  "buffer_load_dwordx4 v113, s[40:43], s57 offen offset:1152 nt lds\n"
  "ds_write_b128 v80, v[24:27] offset:3072\n"
  "v_pk_mul_f32 v[0:1], v[0:1], s[60:61] op_sel_hi:[1,0]\n"
  "v_pk_mul_f32 v[2:3], v[2:3], s[60:61] op_sel_hi:[1,0]\n"
  "v_pk_mul_f32 v[4:5], v[4:5], s[60:61] op_sel_hi:[1,0]\n"
  "v_pk_mul_f32 v[6:7], v[6:7], s[60:61] op_sel_hi:[1,0]\n"
  "v_mfma_f32_16x16x32_bf16 v[12:15], v[156:159], v[76:79], v[92:95]\n"
  "s_waitcnt vmcnt(14)\n"
  "ds_read_b128 a[144:147], v87 offset:0\n"
  "ds_read_b128 a[148:151], v88 offset:0\n"
  "v_exp_f32 v0, v0\n"
  "v_mfma_f32_16x16x32_bf16 v[12:15], v[152:155], v[72:75], v[12:15]\n"
  "v_exp_f32 v1, v1\n"
  "v_mfma_f32_16x16x32_bf16 v[12:15], v[148:151], v[68:71], v[12:15]\n"
  "s_add_u32 m0, s59, 16128\n"
  "v_exp_f32 v2, v2\n"
  "v_mfma_f32_16x16x32_bf16 v[12:15], v[144:147], v[64:67], v[12:15]\n"
  "s_waitcnt lgkmcnt(3)\n"
  "buffer_load_dwordx4 v112, s[40:43], s57 offen offset:256 nt lds\n"
  "buffer_load_dwordx4 v113, s[40:43], s57 offen offset:1280 nt lds\n"
  "s_add_u32 s57, s57, 6144\n"
  "v_exp_f32 v3, v3\n"
  "v_mfma_f32_16x16x32_bf16 v[12:15], v[140:143], v[60:63], v[12:15]\n"
  "s_waitcnt vmcnt(14)\n"
  "ds_read_b128 a[152:155], v87 offset:2048\n"
  "ds_read_b128 a[156:159], v88 offset:2048\n"
  "v_cvt_pk_bf16_f32 v0, v0, v1\n"
  "v_cvt_pk_bf16_f32 v1, v2, v3\n"
  "v_mfma_f32_16x16x32_bf16 v[12:15], v[136:139], v[56:59], v[12:15]\n"
  "s_add_u32 m0, s59, -256\n"
  "v_mul_f32 v8, s60, v8\n"
  "v_mul_f32 v9, s60, v9\n"
  "v_mfma_f32_16x16x32_bf16 v[16:19], v[160:163], v[56:59], v[92:95]\n"
  "s_waitcnt lgkmcnt(2)\n"
  "buffer_load_dwordx4 v112, s[40:43], s57 offen offset:256 nt lds\n"
  "buffer_load_dwordx4 v113, s[40:43], s57 offen offset:1280 nt lds\n"
  "v_exp_f32 v4, v4\n"
  "v_mfma_f32_16x16x32_bf16 v[16:19], v[164:167], v[60:63], v[16:19]\n"
  "s_waitcnt vmcnt(14)\n"
  "ds_read_b128 a[160:163], v87 offset:4096\n"
  "ds_read_b128 a[164:167], v88 offset:4096\n"
  "v_exp_f32 v5, v5\n"
  "v_mfma_f32_16x16x32_bf16 v[16:19], v[168:171], v[64:67], v[16:19]\n"
  "v_exp_f32 v6, v6\n"
  "v_mfma_f32_16x16x32_bf16 v[16:19], v[172:175], v[68:71], v[16:19]\n"
  "s_add_u32 m0, s59, 1920\n"
  "v_exp_f32 v7, v7\n"
  "v_mfma_f32_16x16x32_bf16 v[16:19], v[176:179], v[72:75], v[16:19]\n"
  "s_waitcnt lgkmcnt(2)\n"
  "buffer_load_dwordx4 v112, s[40:43], s57 offen offset:128 nt lds\n"
  "buffer_load_dwordx4 v113, s[40:43], s57 offen offset:1152 nt lds\n"
  "v_cvt_pk_bf16_f32 v2, v4, v5\n"
  "v_cvt_pk_bf16_f32 v3, v6, v7\n"
  "v_mfma_f32_16x16x32_bf16 v[16:19], v[180:183], v[76:79], v[16:19]\n"
  "s_waitcnt vmcnt(14)\n"
  "ds_read_b128 a[184:187], v87 offset:6144\n"
  "ds_read_b128 a[188:191], v88 offset:6144\n"
  "ds_write_b128 v80, v[0:3] offset:4096\n"
  "v_pk_mul_f32 v[10:11], v[10:11], s[60:61] op_sel_hi:[1,0]\n"
  "v_pk_mul_f32 v[12:13], v[12:13], s[60:61] op_sel_hi:[1,0]\n"
  "v_pk_mul_f32 v[14:15], v[14:15], s[60:61] op_sel_hi:[1,0]\n"
  "v_mfma_f32_16x16x32_bf16 v[20:23], v[204:207], v[76:79], v[92:95]\n"
  "v_exp_f32 v8, v8\n"
  "v_mfma_f32_16x16x32_bf16 v[20:23], v[200:203], v[72:75], v[20:23]\n"
  "s_add_u32 m0, s59, 4096\n"
  "v_exp_f32 v9, v9\n"
  "v_mfma_f32_16x16x32_bf16 v[20:23], v[196:199], v[68:71], v[20:23]\n"
  "s_waitcnt lgkmcnt(3)\n"
  "buffer_load_dwordx4 v112, s[40:43], s57 offen offset:0 nt lds\n"
  "buffer_load_dwordx4 v113, s[40:43], s57 offen offset:1024 nt lds\n"
  "s_add_u32 s57, s57, 6144\n"
  "v_exp_f32 v10, v10\n"
  "v_mfma_f32_16x16x32_bf16 v[20:23], v[192:195], v[64:67], v[20:23]\n"
  "s_waitcnt vmcnt(14)\n"
  "ds_read_b128 a[176:179], v87 offset:8192\n"
  "ds_read_b128 a[180:183], v88 offset:8192\n"
  "v_exp_f32 v11, v11\n"
  "v_mfma_f32_16x16x32_bf16 v[20:23], v[188:191], v[60:63], v[20:23]\n"
  "s_add_u32 m0, s59, 6144\n"
  "v_cvt_pk_bf16_f32 v8, v8, v9\n"
  "v_cvt_pk_bf16_f32 v9, v10, v11\n"
  "v_mfma_f32_16x16x32_bf16 v[20:23], v[184:187], v[56:59], v[20:23]\n"
  "s_waitcnt lgkmcnt(3)\n"
  "buffer_load_dwordx4 v112, s[40:43], s57 offen offset:0 nt lds\n"
  "buffer_load_dwordx4 v113, s[40:43], s57 offen offset:1024 nt lds\n"
  "v_mul_f32 v16, s60, v16\n"
  "v_mul_f32 v17, s60, v17\n"
  "v_mfma_f32_16x16x32_bf16 v[24:27], v[208:211], v[56:59], v[92:95]\n"
  "s_waitcnt vmcnt(14)\n"
  "ds_read_b128 a[168:171], v87 offset:10240\n"
  "ds_read_b128 a[172:175], v88 offset:10240\n"
  "v_exp_f32 v12, v12\n"
  "v_mfma_f32_16x16x32_bf16 v[24:27], v[212:215], v[60:63], v[24:27]\n"
  "v_exp_f32 v13, v13\n"
  "v_mfma_f32_16x16x32_bf16 v[24:27], v[216:219], v[64:67], v[24:27]\n"
  "s_add_u32 m0, s59, 8064\n"
  "v_exp_f32 v14, v14\n"
  "v_mfma_f32_16x16x32_bf16 v[24:27], v[220:223], v[68:71], v[24:27]\n"
  "s_waitcnt lgkmcnt(2)\n"
  "buffer_load_dwordx4 v112, s[40:43], s57 offen offset:128 nt lds\n"
  "buffer_load_dwordx4 v113, s[40:43], s57 offen offset:1152 nt lds\n"
  "v_exp_f32 v15, v15\n"
  "v_mfma_f32_16x16x32_bf16 v[24:27], v[224:227], v[72:75], v[24:27]\n"
  "s_waitcnt vmcnt(14)\n"
  "ds_read_b128 a[192:195], v87 offset:12288\n"
  "ds_read_b128 a[196:199], v88 offset:12288\n"
  "v_cvt_pk_bf16_f32 v10, v12, v13\n"
  "v_cvt_pk_bf16_f32 v11, v14, v15\n"
  "v_mfma_f32_16x16x32_bf16 v[24:27], v[228:231], v[76:79], v[24:27]\n"
  "s_add_u32 m0, s59, 9984\n"
  "ds_write_b128 v80, v[8:11] offset:5120\n"
  "v_pk_mul_f32 v[18:19], v[18:19], s[60:61] op_sel_hi:[1,0]\n"
  "v_pk_mul_f32 v[20:21], v[20:21], s[60:61] op_sel_hi:[1,0]\n"
  "v_pk_mul_f32 v[22:23], v[22:23], s[60:61] op_sel_hi:[1,0]\n"
  "v_mfma_f32_16x16x32_bf16 v[28:31], v[252:255], v[76:79], v[92:95]\n"
  "s_waitcnt lgkmcnt(3)\n"
  "buffer_load_dwordx4 v112, s[40:43], s57 offen offset:256 nt lds\n"
  "buffer_load_dwordx4 v113, s[40:43], s57 offen offset:1280 nt lds\n"
  "s_add_u32 s57, s57, 6144\n"
  "v_exp_f32 v16, v16\n"
  "v_mfma_f32_16x16x32_bf16 v[28:31], v[248:251], v[72:75], v[28:31]\n"
  "s_waitcnt vmcnt(14)\n"
  "ds_read_b128 a[200:203], v87 offset:14336\n"
  "ds_read_b128 a[204:207], v88 offset:14336\n"
  "v_exp_f32 v17, v17\n"
  "v_mfma_f32_16x16x32_bf16 v[28:31], v[244:247], v[68:71], v[28:31]\n"
  "v_exp_f32 v18, v18\n"
  "v_mfma_f32_16x16x32_bf16 v[28:31], v[240:243], v[64:67], v[28:31]\n"
  "s_add_u32 m0, s59, 12032\n"
  "v_exp_f32 v19, v19\n"
  "v_mfma_f32_16x16x32_bf16 v[28:31], v[236:239], v[60:63], v[28:31]\n"
  "s_waitcnt lgkmcnt(3)\n"
  "buffer_load_dwordx4 v112, s[40:43], s57 offen offset:256 nt lds\n"
  "buffer_load_dwordx4 v113, s[40:43], s57 offen offset:1280 nt lds\n"
  "v_cvt_pk_bf16_f32 v16, v16, v17\n"
  "v_cvt_pk_bf16_f32 v17, v18, v19\n"
  "v_mfma_f32_16x16x32_bf16 v[28:31], v[232:235], v[56:59], v[28:31]\n"
  "s_waitcnt vmcnt(14)\n"
  "ds_read_b128 a[208:211], v87 offset:16384\n"
  "ds_read_b128 a[212:215], v88 offset:16384\n"
  "v_mfma_f32_16x16x32_bf16 v[0:3], a[0:3], v[32:35], v[92:95]\n"
  "s_add_u32 s66, s66, 6144\n"
  "s_cmp_eq_u32 s66, 86016\n"
  "s_cselect_b32 s66, 49152, s66\n"
  "v_exp_f32 v20, v20\n"
  "v_mfma_f32_16x16x32_bf16 v[0:3], a[4:7], v[36:39], v[0:3]\n"
  "s_add_u32 m0, s59, 14208\n"
  "v_exp_f32 v21, v21\n"
  "v_mfma_f32_16x16x32_bf16 v[0:3], a[8:11], v[40:43], v[0:3]\n"
  "s_waitcnt lgkmcnt(2)\n"
  "buffer_load_dwordx4 v112, s[40:43], s57 offen offset:128 nt lds\n"
  "buffer_load_dwordx4 v113, s[40:43], s57 offen offset:1152 nt lds\n"
  "v_exp_f32 v22, v22\n"
  "v_mfma_f32_16x16x32_bf16 v[0:3], a[12:15], v[44:47], v[0:3]\n"
  "s_waitcnt vmcnt(14)\n"
  "ds_read_b128 a[232:235], v87 offset:0\n"
  "ds_read_b128 a[236:239], v88 offset:0\n"
  "v_exp_f32 v23, v23\n"
  "v_mfma_f32_16x16x32_bf16 v[0:3], a[16:19], v[48:51], v[0:3]\n"
  "s_add_u32 m0, s59, 16384\n"
  "v_cvt_pk_bf16_f32 v18, v20, v21\n"
  "v_cvt_pk_bf16_f32 v19, v22, v23\n"
  "v_mfma_f32_16x16x32_bf16 v[0:3], a[20:23], v[52:55], v[0:3]\n"
  "s_waitcnt lgkmcnt(2)\n"
  "buffer_load_dwordx4 v112, s[40:43], s57 offen offset:0 nt lds\n"
  "buffer_load_dwordx4 v113, s[40:43], s57 offen offset:1024 nt lds\n"
  "s_add_u32 s57, s57, 6144\n"
  "ds_write_b128 v80, v[16:19] offset:6144\n"
  "v_pk_mul_f32 v[24:25], v[24:25], s[60:61] op_sel_hi:[1,0]\n"
  "v_pk_mul_f32 v[26:27], v[26:27], s[60:61] op_sel_hi:[1,0]\n"
  "v_pk_mul_f32 v[28:29], v[28:29], s[60:61] op_sel_hi:[1,0]\n"
  "v_pk_mul_f32 v[30:31], v[30:31], s[60:61] op_sel_hi:[1,0]\n"
  "v_mfma_f32_16x16x32_bf16 v[4:7], a[44:47], v[52:55], v[92:95]\n"
  "s_waitcnt vmcnt(14)\n"
  "ds_read_b128 a[224:227], v87 offset:2048\n"
  "ds_read_b128 a[228:231], v88 offset:2048\n"
  "v_exp_f32 v24, v24\n"
  "v_mfma_f32_16x16x32_bf16 v[4:7], a[40:43], v[48:51], v[4:7]\n"
  "v_exp_f32 v25, v25\n"
  "v_mfma_f32_16x16x32_bf16 v[4:7], a[36:39], v[44:47], v[4:7]\n"
  "s_add_u32 m0, s59, 0\n"
  "v_exp_f32 v26, v26\n"
  "v_mfma_f32_16x16x32_bf16 v[4:7], a[32:35], v[40:43], v[4:7]\n"
  "s_waitcnt lgkmcnt(3)\n"
  "buffer_load_dwordx4 v112, s[40:43], s57 offen offset:0 nt lds\n"
  "buffer_load_dwordx4 v113, s[40:43], s57 offen offset:1024 nt lds\n"
  "v_exp_f32 v27, v27\n"
  "v_mfma_f32_16x16x32_bf16 v[4:7], a[28:31], v[36:39], v[4:7]\n"
  "s_waitcnt vmcnt(14)\n"
  "ds_read_b128 a[216:219], v87 offset:4096\n"
  "ds_read_b128 a[220:223], v88 offset:4096\n"
  "v_cvt_pk_bf16_f32 v24, v24, v25\n"
  "v_cvt_pk_bf16_f32 v25, v26, v27\n"
  "v_mfma_f32_16x16x32_bf16 v[4:7], a[24:27], v[32:35], v[4:7]\n"
  "s_add_u32 m0, s59, 1920\n"
  "v_add_u32 v81, s66, v83\n"
  "v_add_u32 v82, s66, v84\n"
  "v_mfma_f32_16x16x32_bf16 v[8:11], a[48:51], v[32:35], v[92:95]\n"
  "s_waitcnt lgkmcnt(2)\n"
  "buffer_load_dwordx4 v112, s[40:43], s57 offen offset:128 nt lds\n"
  "buffer_load_dwordx4 v113, s[40:43], s57 offen offset:1152 nt lds\n"
  "v_exp_f32 v28, v28\n"
  "v_mfma_f32_16x16x32_bf16 v[8:11], a[52:55], v[36:39], v[8:11]\n"
  "s_waitcnt vmcnt(14)\n"
  "ds_read_b128 a[240:243], v87 offset:6144\n"
  "ds_read_b128 a[244:247], v88 offset:6144\n"
  "v_exp_f32 v29, v29\n"
  "v_mfma_f32_16x16x32_bf16 v[8:11], a[56:59], v[40:43], v[8:11]\n"
  "v_exp_f32 v30, v30\n"
  "v_mfma_f32_16x16x32_bf16 v[8:11], a[60:63], v[44:47], v[8:11]\n"
  "s_add_u32 m0, s59, 3840\n"
  "v_exp_f32 v31, v31\n"
  "v_mfma_f32_16x16x32_bf16 v[8:11], a[64:67], v[48:51], v[8:11]\n"
  "s_waitcnt lgkmcnt(2)\n"
  "buffer_load_dwordx4 v112, s[40:43], s57 offen offset:256 nt lds\n"
  "buffer_load_dwordx4 v113, s[40:43], s57 offen offset:1280 nt lds\n"
  "s_add_u32 s57, s57, 6144\n"
  "v_cvt_pk_bf16_f32 v26, v28, v29\n"
  "v_cvt_pk_bf16_f32 v27, v30, v31\n"
  "v_mfma_f32_16x16x32_bf16 v[8:11], a[68:71], v[52:55], v[8:11]\n"
  "s_waitcnt vmcnt(14)\n"
  "ds_read_b128 a[248:251], v87 offset:8192\n"
  "ds_read_b128 a[252:255], v88 offset:8192\n"
  "ds_write_b128 v80, v[24:27] offset:7168\n"
  "v_pk_mul_f32 v[0:1], v[0:1], s[60:61] op_sel_hi:[1,0]\n"
  "v_pk_mul_f32 v[2:3], v[2:3], s[60:61] op_sel_hi:[1,0]\n"
  "v_pk_mul_f32 v[4:5], v[4:5], s[60:61] op_sel_hi:[1,0]\n"
  "v_pk_mul_f32 v[6:7], v[6:7], s[60:61] op_sel_hi:[1,0]\n"
  "v_mfma_f32_16x16x32_bf16 v[12:15], a[92:95], v[52:55], v[92:95]\n"
  "v_exp_f32 v0, v0\n"
  "v_mfma_f32_16x16x32_bf16 v[12:15], a[88:91], v[48:51], v[12:15]\n"
  "s_add_u32 m0, s59, 5888\n"
  "v_exp_f32 v1, v1\n"
  "v_mfma_f32_16x16x32_bf16 v[12:15], a[84:87], v[44:47], v[12:15]\n"
  "s_waitcnt lgkmcnt(3)\n"
  "buffer_load_dwordx4 v112, s[40:43], s57 offen offset:256 nt lds\n"
  "buffer_load_dwordx4 v113, s[40:43], s57 offen offset:1280 nt lds\n"
  "v_exp_f32 v2, v2\n"
  "v_mfma_f32_16x16x32_bf16 v[12:15], a[80:83], v[40:43], v[12:15]\n"
  "s_waitcnt vmcnt(14)\n"
  "ds_read_b128 v[128:131], v87 offset:10240\n"
  "ds_read_b128 v[132:135], v88 offset:10240\n"
  "v_exp_f32 v3, v3\n"
  "v_mfma_f32_16x16x32_bf16 v[12:15], a[76:79], v[36:39], v[12:15]\n"
  "s_add_u32 m0, s59, 8064\n"
  "v_cvt_pk_bf16_f32 v0, v0, v1\n"
  "v_cvt_pk_bf16_f32 v1, v2, v3\n"
  "v_mfma_f32_16x16x32_bf16 v[12:15], a[72:75], v[32:35], v[12:15]\n"
  "s_waitcnt lgkmcnt(3)\n"
  "buffer_load_dwordx4 v112, s[40:43], s57 offen offset:128 nt lds\n"
  "buffer_load_dwordx4 v113, s[40:43], s57 offen offset:1152 nt lds\n"
  "s_waitcnt lgkmcnt(2)\n"
  "s_barrier\n"
  "v_xor_b32 v80, 0x4000, v80\n"
  "v_mfma_f32_16x16x32_bf16 v[16:19], a[96:99], v[32:35], v[92:95]\n"
  "s_waitcnt vmcnt(14)\n"
  "ds_read_b128 v[152:155], v87 offset:12288\n"
  "ds_read_b128 v[156:159], v88 offset:12288\n"
  "v_exp_f32 v4, v4\n"
  "v_mfma_f32_16x16x32_bf16 v[16:19], a[100:103], v[36:39], v[16:19]\n"
  "v_exp_f32 v5, v5\n"
  "v_mfma_f32_16x16x32_bf16 v[16:19], a[104:107], v[40:43], v[16:19]\n"
  "s_add_u32 m0, s59, 10240\n"
  "v_exp_f32 v6, v6\n"
  "v_mfma_f32_16x16x32_bf16 v[16:19], a[108:111], v[44:47], v[16:19]\n"
  "s_waitcnt lgkmcnt(2)\n"
  "buffer_load_dwordx4 v112, s[40:43], s57 offen offset:0 nt lds\n"
  "buffer_load_dwordx4 v113, s[40:43], s57 offen offset:1024 nt lds\n"
  "s_add_u32 s57, s57, 6144\n"
  "v_exp_f32 v7, v7\n"
  "v_mfma_f32_16x16x32_bf16 v[16:19], a[112:115], v[48:51], v[16:19]\n"
  "s_waitcnt vmcnt(14)\n"
  "ds_read_b128 v[144:147], v87 offset:14336\n"
  "ds_read_b128 v[148:151], v88 offset:14336\n"
  "v_cvt_pk_bf16_f32 v2, v4, v5\n"
  "v_cvt_pk_bf16_f32 v3, v6, v7\n"
  "v_mfma_f32_16x16x32_bf16 v[16:19], a[116:119], v[52:55], v[16:19]\n"
  "s_add_u32 m0, s59, 12288\n"
  "ds_write_b128 v80, v[0:3] offset:0\n"
  "v_pk_mul_f32 v[8:9], v[8:9], s[60:61] op_sel_hi:[1,0]\n"
  "v_pk_mul_f32 v[10:11], v[10:11], s[60:61] op_sel_hi:[1,0]\n"
  "v_pk_mul_f32 v[12:13], v[12:13], s[60:61] op_sel_hi:[1,0]\n"
  "v_pk_mul_f32 v[14:15], v[14:15], s[60:61] op_sel_hi:[1,0]\n"
  "v_mfma_f32_16x16x32_bf16 v[20:23], a[140:143], v[52:55], v[92:95]\n"
  "s_waitcnt lgkmcnt(3)\n"
  "buffer_load_dwordx4 v112, s[40:43], s57 offen offset:0 nt lds\n"
  "buffer_load_dwordx4 v113, s[40:43], s57 offen offset:1024 nt lds\n"
  "v_exp_f32 v8, v8\n"
  "v_mfma_f32_16x16x32_bf16 v[20:23], a[136:139], v[48:51], v[20:23]\n"
  "s_waitcnt vmcnt(14)\n"
  "ds_read_b128 v[136:139], v87 offset:16384\n"
  "ds_read_b128 v[140:143], v88 offset:16384\n"
  "v_exp_f32 v9, v9\n"
  "v_mfma_f32_16x16x32_bf16 v[20:23], a[132:135], v[44:47], v[20:23]\n"
  "v_exp_f32 v10, v10\n"
  "v_mfma_f32_16x16x32_bf16 v[20:23], a[128:131], v[40:43], v[20:23]\n"
  "s_add_u32 m0, s59, 14208\n"
  "v_exp_f32 v11, v11\n"
  "v_mfma_f32_16x16x32_bf16 v[20:23], a[124:127], v[36:39], v[20:23]\n"
  "s_waitcnt lgkmcnt(3)\n"
  "buffer_load_dwordx4 v112, s[40:43], s57 offen offset:128 nt lds\n"
  "buffer_load_dwordx4 v113, s[40:43], s57 offen offset:1152 nt lds\n"
  "v_cvt_pk_bf16_f32 v8, v8, v9\n"
  "v_cvt_pk_bf16_f32 v9, v10, v11\n"
  "v_mfma_f32_16x16x32_bf16 v[20:23], a[120:123], v[32:35], v[20:23]\n"
  "s_waitcnt vmcnt(14)\n"
  "ds_read_b128 v[160:163], v87 offset:0\n"
  "ds_read_b128 v[164:167], v88 offset:0\n"
  "ds_read_b128 v[56:59], v81 offset:0\n"
  "ds_read_b128 v[60:63], v82 offset:0\n"
  "v_mfma_f32_16x16x32_bf16 v[24:27], a[144:147], v[32:35], v[92:95]\n"
  "v_exp_f32 v12, v12\n"
  "v_mfma_f32_16x16x32_bf16 v[24:27], a[148:151], v[36:39], v[24:27]\n"
  "s_add_u32 m0, s59, 16128\n"
  "v_exp_f32 v13, v13\n"
  "v_mfma_f32_16x16x32_bf16 v[24:27], a[152:155], v[40:43], v[24:27]\n"
  "s_waitcnt lgkmcnt(4)\n"
  "buffer_load_dwordx4 v112, s[40:43], s57 offen offset:256 nt lds\n"
  "buffer_load_dwordx4 v113, s[40:43], s57 offen offset:1280 nt lds\n"
  "s_add_u32 s57, s57, 6144\n"
  "v_exp_f32 v14, v14\n"
  "v_mfma_f32_16x16x32_bf16 v[24:27], a[156:159], v[44:47], v[24:27]\n"
  "s_waitcnt vmcnt(14)\n"
  "ds_read_b128 v[168:171], v87 offset:2048\n"
  "ds_read_b128 v[172:175], v88 offset:2048\n"
  "v_exp_f32 v15, v15\n"
  "v_mfma_f32_16x16x32_bf16 v[24:27], a[160:163], v[48:51], v[24:27]\n"
  "s_add_u32 m0, s59, -256\n"
  "v_cvt_pk_bf16_f32 v10, v12, v13\n"
  "v_cvt_pk_bf16_f32 v11, v14, v15\n"
  "v_mfma_f32_16x16x32_bf16 v[24:27], a[164:167], v[52:55], v[24:27]\n"
  "s_waitcnt lgkmcnt(4)\n"
  "buffer_load_dwordx4 v112, s[40:43], s57 offen offset:256 nt lds\n"
  "buffer_load_dwordx4 v113, s[40:43], s57 offen offset:1280 nt lds\n"
  "ds_write_b128 v80, v[8:11] offset:1024\n"
  "v_pk_mul_f32 v[16:17], v[16:17], s[60:61] op_sel_hi:[1,0]\n"
  "v_pk_mul_f32 v[18:19], v[18:19], s[60:61] op_sel_hi:[1,0]\n"
  "v_pk_mul_f32 v[20:21], v[20:21], s[60:61] op_sel_hi:[1,0]\n"
  "v_pk_mul_f32 v[22:23], v[22:23], s[60:61] op_sel_hi:[1,0]\n"
  "v_mfma_f32_16x16x32_bf16 v[28:31], a[188:191], v[52:55], v[92:95]\n"
  "s_waitcnt vmcnt(14)\n"
  "ds_read_b128 v[176:179], v87 offset:4096\n"
  "ds_read_b128 v[180:183], v88 offset:4096\n"
  "v_exp_f32 v16, v16\n"
  "v_mfma_f32_16x16x32_bf16 v[28:31], a[184:187], v[48:51], v[28:31]\n"
  "v_exp_f32 v17, v17\n"
  "v_mfma_f32_16x16x32_bf16 v[28:31], a[180:183], v[44:47], v[28:31]\n"
  "s_add_u32 m0, s59, 1920\n"
  "v_exp_f32 v18, v18\n"
  "v_mfma_f32_16x16x32_bf16 v[28:31], a[176:179], v[40:43], v[28:31]\n"
  "s_waitcnt lgkmcnt(3)\n"
  "buffer_load_dwordx4 v112, s[40:43], s57 offen offset:128 nt lds\n"
  "buffer_load_dwordx4 v113, s[40:43], s57 offen offset:1152 nt lds\n"
  "v_exp_f32 v19, v19\n"
  "v_mfma_f32_16x16x32_bf16 v[28:31], a[172:175], v[36:39], v[28:31]\n"
  "s_waitcnt vmcnt(14)\n"
  "ds_read_b128 v[200:203], v87 offset:6144\n"
  "ds_read_b128 v[204:207], v88 offset:6144\n"
  "v_cvt_pk_bf16_f32 v16, v16, v17\n"
  "v_cvt_pk_bf16_f32 v17, v18, v19\n"
  "v_mfma_f32_16x16x32_bf16 v[28:31], a[168:171], v[32:35], v[28:31]\n"
  "s_add_u32 m0, s59, 4096\n"
  "ds_read_b128 v[64:67], v81 offset:2048\n"
  "ds_read_b128 v[68:71], v82 offset:2048\n"
  "v_mfma_f32_16x16x32_bf16 v[0:3], a[192:195], v[32:35], v[92:95]\n"
  "s_waitcnt lgkmcnt(4)\n"
  "buffer_load_dwordx4 v112, s[40:43], s57 offen offset:0 nt lds\n"
  "buffer_load_dwordx4 v113, s[40:43], s57 offen offset:1024 nt lds\n"
  "s_add_u32 s57, s57, 6144\n"
  "v_exp_f32 v20, v20\n"
  "v_mfma_f32_16x16x32_bf16 v[0:3], a[196:199], v[36:39], v[0:3]\n"
  "s_waitcnt vmcnt(14)\n"
  "ds_read_b128 v[192:195], v87 offset:8192\n"
  "ds_read_b128 v[196:199], v88 offset:8192\n"
  "v_exp_f32 v21, v21\n"
  "v_mfma_f32_16x16x32_bf16 v[0:3], a[200:203], v[40:43], v[0:3]\n"
  "v_exp_f32 v22, v22\n"
  "v_mfma_f32_16x16x32_bf16 v[0:3], a[204:207], v[44:47], v[0:3]\n"
  "v_exp_f32 v23, v23\n"
  "v_mfma_f32_16x16x32_bf16 v[0:3], a[208:211], v[48:51], v[0:3]\n"
  "v_cvt_pk_bf16_f32 v18, v20, v21\n"
  "v_cvt_pk_bf16_f32 v19, v22, v23\n"
  "v_mfma_f32_16x16x32_bf16 v[0:3], a[212:215], v[52:55], v[0:3]\n"
  "s_waitcnt vmcnt(12)\n"
  "ds_read_b128 v[184:187], v87 offset:10240\n"
  "ds_read_b128 v[188:191], v88 offset:10240\n"
  "ds_write_b128 v80, v[16:19] offset:2048\n"
  "v_pk_mul_f32 v[24:25], v[24:25], s[60:61] op_sel_hi:[1,0]\n"
  "v_pk_mul_f32 v[26:27], v[26:27], s[60:61] op_sel_hi:[1,0]\n"
  "v_pk_mul_f32 v[28:29], v[28:29], s[60:61] op_sel_hi:[1,0]\n"
  "v_pk_mul_f32 v[30:31], v[30:31], s[60:61] op_sel_hi:[1,0]\n"
  "v_mfma_f32_16x16x32_bf16 v[4:7], a[236:239], v[52:55], v[92:95]\n"
  "v_exp_f32 v24, v24\n"
  "v_mfma_f32_16x16x32_bf16 v[4:7], a[232:235], v[48:51], v[4:7]\n"
  "v_exp_f32 v25, v25\n"
  "v_mfma_f32_16x16x32_bf16 v[4:7], a[228:231], v[44:47], v[4:7]\n"
  "v_exp_f32 v26, v26\n"
  "v_mfma_f32_16x16x32_bf16 v[4:7], a[224:227], v[40:43], v[4:7]\n"
  "s_waitcnt vmcnt(10)\n"
  "ds_read_b128 v[208:211], v87 offset:12288\n"
  "ds_read_b128 v[212:215], v88 offset:12288\n"
  "v_exp_f32 v27, v27\n"
  "v_mfma_f32_16x16x32_bf16 v[4:7], a[220:223], v[36:39], v[4:7]\n"
  "v_cvt_pk_bf16_f32 v24, v24, v25\n"
  "v_cvt_pk_bf16_f32 v25, v26, v27\n"
  "v_mfma_f32_16x16x32_bf16 v[4:7], a[216:219], v[32:35], v[4:7]\n"
  "ds_read_b128 v[72:75], v81 offset:4096\n"
  "ds_read_b128 v[76:79], v82 offset:4096\n"
  "v_mfma_f32_16x16x32_bf16 v[8:11], a[240:243], v[32:35], v[92:95]\n"
  "s_waitcnt vmcnt(8)\n"
  "ds_read_b128 v[216:219], v87 offset:14336\n"
  "ds_read_b128 v[220:223], v88 offset:14336\n"
  "v_exp_f32 v28, v28\n"
  "v_mfma_f32_16x16x32_bf16 v[8:11], a[244:247], v[36:39], v[8:11]\n"
  "v_exp_f32 v29, v29\n"
  "v_mfma_f32_16x16x32_bf16 v[8:11], a[248:251], v[40:43], v[8:11]\n"
  "v_exp_f32 v30, v30\n"
  "v_mfma_f32_16x16x32_bf16 v[8:11], a[252:255], v[44:47], v[8:11]\n"
  "v_exp_f32 v31, v31\n"
  "v_mfma_f32_16x16x32_bf16 v[8:11], v[128:131], v[48:51], v[8:11]\n"
  "s_waitcnt vmcnt(6)\n"
  "ds_read_b128 v[224:227], v87 offset:16384\n"
  "ds_read_b128 v[228:231], v88 offset:16384\n"
  "v_cvt_pk_bf16_f32 v26, v28, v29\n"
  "v_cvt_pk_bf16_f32 v27, v30, v31\n"
  "v_mfma_f32_16x16x32_bf16 v[8:11], v[132:135], v[52:55], v[8:11]\n"
  "ds_write_b128 v80, v[24:27] offset:3072\n"
  "v_pk_mul_f32 v[0:1], v[0:1], s[60:61] op_sel_hi:[1,0]\n"
  "v_pk_mul_f32 v[2:3], v[2:3], s[60:61] op_sel_hi:[1,0]\n"
  "v_pk_mul_f32 v[4:5], v[4:5], s[60:61] op_sel_hi:[1,0]\n"
  "v_pk_mul_f32 v[6:7], v[6:7], s[60:61] op_sel_hi:[1,0]\n"
  "v_mfma_f32_16x16x32_bf16 v[12:15], v[156:159], v[52:55], v[92:95]\n"
  "v_exp_f32 v0, v0\n"
  "v_mfma_f32_16x16x32_bf16 v[12:15], v[152:155], v[48:51], v[12:15]\n"
  "s_waitcnt vmcnt(4)\n"
  "ds_read_b128 v[248:251], v87 offset:0\n"
  "ds_read_b128 v[252:255], v88 offset:0\n"
  "v_exp_f32 v1, v1\n"
  "v_mfma_f32_16x16x32_bf16 v[12:15], v[148:151], v[44:47], v[12:15]\n"
  "v_exp_f32 v2, v2\n"
  "v_mfma_f32_16x16x32_bf16 v[12:15], v[144:147], v[40:43], v[12:15]\n"
  "v_exp_f32 v3, v3\n"
  "v_mfma_f32_16x16x32_bf16 v[12:15], v[140:143], v[36:39], v[12:15]\n"
  "v_cvt_pk_bf16_f32 v0, v0, v1\n"
  "v_cvt_pk_bf16_f32 v1, v2, v3\n"
  "v_mfma_f32_16x16x32_bf16 v[12:15], v[136:139], v[32:35], v[12:15]\n"
  "s_waitcnt vmcnt(2)\n"
  "ds_read_b128 v[240:243], v87 offset:2048\n"
  "ds_read_b128 v[244:247], v88 offset:2048\n"
  "v_mul_f32 v8, s60, v8\n"
  "v_mul_f32 v9, s60, v9\n"
  "v_mfma_f32_16x16x32_bf16 v[16:19], v[160:163], v[32:35], v[92:95]\n"
  "v_exp_f32 v4, v4\n"
  "v_mfma_f32_16x16x32_bf16 v[16:19], v[164:167], v[36:39], v[16:19]\n"
  "v_exp_f32 v5, v5\n"
  "v_mfma_f32_16x16x32_bf16 v[16:19], v[168:171], v[40:43], v[16:19]\n"
  "v_exp_f32 v6, v6\n"
  "v_mfma_f32_16x16x32_bf16 v[16:19], v[172:175], v[44:47], v[16:19]\n"
  "s_waitcnt vmcnt(0)\n"
  "ds_read_b128 v[232:235], v87 offset:4096\n"
  "ds_read_b128 v[236:239], v88 offset:4096\n"
  "v_exp_f32 v7, v7\n"
  "v_mfma_f32_16x16x32_bf16 v[16:19], v[176:179], v[48:51], v[16:19]\n"
  "v_cvt_pk_bf16_f32 v2, v4, v5\n"
  "v_cvt_pk_bf16_f32 v3, v6, v7\n"
  "v_mfma_f32_16x16x32_bf16 v[16:19], v[180:183], v[52:55], v[16:19]\n"
  "s_waitcnt lgkmcnt(15)\n"
  "ds_write_b128 v80, v[0:3] offset:4096\n"
  "v_pk_mul_f32 v[10:11], v[10:11], s[60:61] op_sel_hi:[1,0]\n"
  "v_pk_mul_f32 v[12:13], v[12:13], s[60:61] op_sel_hi:[1,0]\n"
  "v_pk_mul_f32 v[14:15], v[14:15], s[60:61] op_sel_hi:[1,0]\n"
  "v_mfma_f32_16x16x32_bf16 v[20:23], v[204:207], v[52:55], v[92:95]\n"
  "v_exp_f32 v8, v8\n"
  "v_mfma_f32_16x16x32_bf16 v[20:23], v[200:203], v[48:51], v[20:23]\n"
  "v_exp_f32 v9, v9\n"
  "v_mfma_f32_16x16x32_bf16 v[20:23], v[196:199], v[44:47], v[20:23]\n"
  "v_exp_f32 v10, v10\n"
  "v_mfma_f32_16x16x32_bf16 v[20:23], v[192:195], v[40:43], v[20:23]\n"
  "v_exp_f32 v11, v11\n"
  "v_mfma_f32_16x16x32_bf16 v[20:23], v[188:191], v[36:39], v[20:23]\n"
  "v_cvt_pk_bf16_f32 v8, v8, v9\n"
  "v_cvt_pk_bf16_f32 v9, v10, v11\n"
  "v_mfma_f32_16x16x32_bf16 v[20:23], v[184:187], v[32:35], v[20:23]\n"
  "s_waitcnt lgkmcnt(14)\n"
  "v_mul_f32 v16, s60, v16\n"
  "v_mul_f32 v17, s60, v17\n"
  "v_mfma_f32_16x16x32_bf16 v[24:27], v[208:211], v[32:35], v[92:95]\n"
  "v_exp_f32 v12, v12\n"
  "v_mfma_f32_16x16x32_bf16 v[24:27], v[212:215], v[36:39], v[24:27]\n"
  "s_waitcnt lgkmcnt(10)\n"
  "v_exp_f32 v13, v13\n"
  "v_mfma_f32_16x16x32_bf16 v[24:27], v[216:219], v[40:43], v[24:27]\n"
  "v_exp_f32 v14, v14\n"
  "v_mfma_f32_16x16x32_bf16 v[24:27], v[220:223], v[44:47], v[24:27]\n"
  "s_waitcnt lgkmcnt(8)\n"
  "v_exp_f32 v15, v15\n"
  "v_mfma_f32_16x16x32_bf16 v[24:27], v[224:227], v[48:51], v[24:27]\n"
  "v_cvt_pk_bf16_f32 v10, v12, v13\n"
  "v_cvt_pk_bf16_f32 v11, v14, v15\n"
  "v_mfma_f32_16x16x32_bf16 v[24:27], v[228:231], v[52:55], v[24:27]\n"
  "s_waitcnt lgkmcnt(5)\n"
  "ds_write_b128 v80, v[8:11] offset:5120\n"
  "v_pk_mul_f32 v[18:19], v[18:19], s[60:61] op_sel_hi:[1,0]\n"
  "v_pk_mul_f32 v[20:21], v[20:21], s[60:61] op_sel_hi:[1,0]\n"
  "v_pk_mul_f32 v[22:23], v[22:23], s[60:61] op_sel_hi:[1,0]\n"
  "v_mfma_f32_16x16x32_bf16 v[28:31], v[252:255], v[52:55], v[92:95]\n"
  "v_exp_f32 v16, v16\n"
  "v_mfma_f32_16x16x32_bf16 v[28:31], v[248:251], v[48:51], v[28:31]\n"
  "s_waitcnt lgkmcnt(4)\n"
  "v_exp_f32 v17, v17\n"
  "v_mfma_f32_16x16x32_bf16 v[28:31], v[244:247], v[44:47], v[28:31]\n"
  "v_exp_f32 v18, v18\n"
  "v_mfma_f32_16x16x32_bf16 v[28:31], v[240:243], v[40:43], v[28:31]\n"
  "s_waitcnt lgkmcnt(2)\n"
  "s_sub_u32 s56, s56, 1\n"
  "v_exp_f32 v19, v19\n"
  "v_mfma_f32_16x16x32_bf16 v[28:31], v[236:239], v[36:39], v[28:31]\n"
  "v_cvt_pk_bf16_f32 v16, v16, v17\n"
  "v_cvt_pk_bf16_f32 v17, v18, v19\n"
  "v_mfma_f32_16x16x32_bf16 v[28:31], v[232:235], v[32:35], v[28:31]\n"
  "s_branch A_H1_%=\n"
  "A_X0_%=:\n"
  "s_mov_b32 s52, s53\n"
  "s_mov_b32 s53, s54\n"
  "s_mov_b32 s54, s55\n"
  "s_cmp_eq_u32 s52, 0\n"
  "s_cbranch_scc1 A_LAST_%=\n"
  "s_lshr_b32 s62, s52, 16\n"
  "s_lshl1_add_u32 s62, s62, s65\n"
  "s_mul_i32 s57, s62, 98304\n"
  "s_and_b32 s56, s52, 0xff\n"
  "s_cmp_lg_u32 s53, 0\n"
  "s_cselect_b32 s62, 1, 0\n"
  "s_sub_u32 s56, s56, s62\n"
  "s_add_u32 s57, s57, 18432\n"
  "v_mfma_f32_16x16x32_bf16 v[0:3], a[0:3], v[32:35], v[92:95]\n"
  "s_add_u32 s66, s66, 6144\n"
  "s_cmp_eq_u32 s66, 86016\n"
  "s_cselect_b32 s66, 49152, s66\n"
  "v_exp_f32 v20, v20\n"
  "v_mfma_f32_16x16x32_bf16 v[0:3], a[4:7], v[36:39], v[0:3]\n"
  "ds_read_b128 a[0:3], v87 offset:0\n"
  "ds_read_b128 a[4:7], v88 offset:0\n"
  "v_exp_f32 v21, v21\n"
  "v_mfma_f32_16x16x32_bf16 v[0:3], a[8:11], v[40:43], v[0:3]\n"
  "v_exp_f32 v22, v22\n"
  "v_mfma_f32_16x16x32_bf16 v[0:3], a[12:15], v[44:47], v[0:3]\n"
  "v_exp_f32 v23, v23\n"
  "v_mfma_f32_16x16x32_bf16 v[0:3], a[16:19], v[48:51], v[0:3]\n"
  "v_cvt_pk_bf16_f32 v18, v20, v21\n"
  "v_cvt_pk_bf16_f32 v19, v22, v23\n"
  "v_mfma_f32_16x16x32_bf16 v[0:3], a[20:23], v[52:55], v[0:3]\n"
  "ds_read_b128 a[8:11], v87 offset:2048\n"
  "ds_read_b128 a[12:15], v88 offset:2048\n"
  "ds_write_b128 v80, v[16:19] offset:6144\n"
  "v_pk_mul_f32 v[24:25], v[24:25], s[60:61] op_sel_hi:[1,0]\n"
  "v_pk_mul_f32 v[26:27], v[26:27], s[60:61] op_sel_hi:[1,0]\n"
  "v_pk_mul_f32 v[28:29], v[28:29], s[60:61] op_sel_hi:[1,0]\n"
  "v_pk_mul_f32 v[30:31], v[30:31], s[60:61] op_sel_hi:[1,0]\n"
  "v_mfma_f32_16x16x32_bf16 v[4:7], a[44:47], v[52:55], v[92:95]\n"
  "v_exp_f32 v24, v24\n"
  "v_mfma_f32_16x16x32_bf16 v[4:7], a[40:43], v[48:51], v[4:7]\n"
  "s_add_u32 m0, s59, -256\n"
  "v_exp_f32 v25, v25\n"
  "v_mfma_f32_16x16x32_bf16 v[4:7], a[36:39], v[44:47], v[4:7]\n"
  "s_waitcnt lgkmcnt(3)\n"
  "buffer_load_dwordx4 v112, s[40:43], s57 offen offset:256 nt lds\n"
  "buffer_load_dwordx4 v113, s[40:43], s57 offen offset:1280 nt lds\n"
  "v_exp_f32 v26, v26\n"
  "v_mfma_f32_16x16x32_bf16 v[4:7], a[32:35], v[40:43], v[4:7]\n"
  "ds_read_b128 a[16:19], v87 offset:4096\n"
  "ds_read_b128 a[20:23], v88 offset:4096\n"
  "v_exp_f32 v27, v27\n"
  "v_mfma_f32_16x16x32_bf16 v[4:7], a[28:31], v[36:39], v[4:7]\n"
  "s_add_u32 m0, s59, 1920\n"
  "v_cvt_pk_bf16_f32 v24, v24, v25\n"
  "v_cvt_pk_bf16_f32 v25, v26, v27\n"
  "v_mfma_f32_16x16x32_bf16 v[4:7], a[24:27], v[32:35], v[4:7]\n"
  "s_waitcnt lgkmcnt(3)\n"
  "buffer_load_dwordx4 v112, s[40:43], s57 offen offset:128 nt lds\n"
  "buffer_load_dwordx4 v113, s[40:43], s57 offen offset:1152 nt lds\n"
  "v_add_u32 v81, s66, v83\n"
  "v_add_u32 v82, s66, v84\n"
  "v_mfma_f32_16x16x32_bf16 v[8:11], a[48:51], v[32:35], v[92:95]\n"
  "ds_read_b128 a[40:43], v87 offset:6144\n"
  "ds_read_b128 a[44:47], v88 offset:6144\n"
  "v_exp_f32 v28, v28\n"
  "v_mfma_f32_16x16x32_bf16 v[8:11], a[52:55], v[36:39], v[8:11]\n"
  "v_exp_f32 v29, v29\n"
  "v_mfma_f32_16x16x32_bf16 v[8:11], a[56:59], v[40:43], v[8:11]\n"
  "s_add_u32 m0, s59, 4096\n"
  "v_exp_f32 v30, v30\n"
  "v_mfma_f32_16x16x32_bf16 v[8:11], a[60:63], v[44:47], v[8:11]\n"
  "s_waitcnt lgkmcnt(2)\n"
  "buffer_load_dwordx4 v112, s[40:43], s57 offen offset:0 nt lds\n"
  "buffer_load_dwordx4 v113, s[40:43], s57 offen offset:1024 nt lds\n"
  "s_add_u32 s57, s57, 6144\n"
  "v_exp_f32 v31, v31\n"
  "v_mfma_f32_16x16x32_bf16 v[8:11], a[64:67], v[48:51], v[8:11]\n"
  "ds_read_b128 a[32:35], v87 offset:8192\n"
  "ds_read_b128 a[36:39], v88 offset:8192\n"
  "v_cvt_pk_bf16_f32 v26, v28, v29\n"
  "v_cvt_pk_bf16_f32 v27, v30, v31\n"
  "v_mfma_f32_16x16x32_bf16 v[8:11], a[68:71], v[52:55], v[8:11]\n"
  "s_add_u32 m0, s59, 6144\n"
  "ds_write_b128 v80, v[24:27] offset:7168\n"
  "v_pk_mul_f32 v[0:1], v[0:1], s[60:61] op_sel_hi:[1,0]\n"
  "v_pk_mul_f32 v[2:3], v[2:3], s[60:61] op_sel_hi:[1,0]\n"
  "v_pk_mul_f32 v[4:5], v[4:5], s[60:61] op_sel_hi:[1,0]\n"
  "v_pk_mul_f32 v[6:7], v[6:7], s[60:61] op_sel_hi:[1,0]\n"
  "v_mfma_f32_16x16x32_bf16 v[12:15], a[92:95], v[52:55], v[92:95]\n"
  "s_waitcnt lgkmcnt(3)\n"
  "buffer_load_dwordx4 v112, s[40:43], s57 offen offset:0 nt lds\n"
  "buffer_load_dwordx4 v113, s[40:43], s57 offen offset:1024 nt lds\n"
  "v_exp_f32 v0, v0\n"
  "v_mfma_f32_16x16x32_bf16 v[12:15], a[88:91], v[48:51], v[12:15]\n"
  "ds_read_b128 a[24:27], v87 offset:10240\n"
  "ds_read_b128 a[28:31], v88 offset:10240\n"
  "v_exp_f32 v1, v1\n"
  "v_mfma_f32_16x16x32_bf16 v[12:15], a[84:87], v[44:47], v[12:15]\n"
  "v_exp_f32 v2, v2\n"
  "v_mfma_f32_16x16x32_bf16 v[12:15], a[80:83], v[40:43], v[12:15]\n"
  "s_add_u32 m0, s59, 8064\n"
  "v_exp_f32 v3, v3\n"
  "v_mfma_f32_16x16x32_bf16 v[12:15], a[76:79], v[36:39], v[12:15]\n"
  "s_waitcnt lgkmcnt(3)\n"
  "buffer_load_dwordx4 v112, s[40:43], s57 offen offset:128 nt lds\n"
  "buffer_load_dwordx4 v113, s[40:43], s57 offen offset:1152 nt lds\n"
  "v_cvt_pk_bf16_f32 v0, v0, v1\n"
  "v_cvt_pk_bf16_f32 v1, v2, v3\n"
  "v_mfma_f32_16x16x32_bf16 v[12:15], a[72:75], v[32:35], v[12:15]\n"
  "ds_read_b128 a[48:51], v87 offset:12288\n"
  "ds_read_b128 a[52:55], v88 offset:12288\n"
  "s_waitcnt lgkmcnt(4)\n"
  "s_barrier\n"
  "v_xor_b32 v80, 0x4000, v80\n"
  "v_mfma_f32_16x16x32_bf16 v[16:19], a[96:99], v[32:35], v[92:95]\n"
  "v_exp_f32 v4, v4\n"
  "v_mfma_f32_16x16x32_bf16 v[16:19], a[100:103], v[36:39], v[16:19]\n"
  "s_add_u32 m0, s59, 9984\n"
  "v_exp_f32 v5, v5\n"
  "v_mfma_f32_16x16x32_bf16 v[16:19], a[104:107], v[40:43], v[16:19]\n"
  "s_waitcnt lgkmcnt(2)\n"
  "buffer_load_dwordx4 v112, s[40:43], s57 offen offset:256 nt lds\n"
  "buffer_load_dwordx4 v113, s[40:43], s57 offen offset:1280 nt lds\n"
  "s_add_u32 s57, s57, 6144\n"
  "v_exp_f32 v6, v6\n"
  "v_mfma_f32_16x16x32_bf16 v[16:19], a[108:111], v[44:47], v[16:19]\n"
  "ds_read_b128 a[56:59], v87 offset:14336\n"
  "ds_read_b128 a[60:63], v88 offset:14336\n"
  "v_exp_f32 v7, v7\n"
  "v_mfma_f32_16x16x32_bf16 v[16:19], a[112:115], v[48:51], v[16:19]\n"
  "s_add_u32 m0, s59, 12032\n"
  "v_cvt_pk_bf16_f32 v2, v4, v5\n"
  "v_cvt_pk_bf16_f32 v3, v6, v7\n"
  "v_mfma_f32_16x16x32_bf16 v[16:19], a[116:119], v[52:55], v[16:19]\n"
  "s_waitcnt lgkmcnt(2)\n"
  "buffer_load_dwordx4 v112, s[40:43], s57 offen offset:256 nt lds\n"
  "buffer_load_dwordx4 v113, s[40:43], s57 offen offset:1280 nt lds\n"
  "ds_write_b128 v80, v[0:3] offset:0\n"
  "v_pk_mul_f32 v[8:9], v[8:9], s[60:61] op_sel_hi:[1,0]\n"
  "v_pk_mul_f32 v[10:11], v[10:11], s[60:61] op_sel_hi:[1,0]\n"
  "v_pk_mul_f32 v[12:13], v[12:13], s[60:61] op_sel_hi:[1,0]\n"
  "v_pk_mul_f32 v[14:15], v[14:15], s[60:61] op_sel_hi:[1,0]\n"
  "v_mfma_f32_16x16x32_bf16 v[20:23], a[140:143], v[52:55], v[92:95]\n"
  "ds_read_b128 a[64:67], v87 offset:16384\n"
  "ds_read_b128 a[68:71], v88 offset:16384\n"
  "v_exp_f32 v8, v8\n"
  "v_mfma_f32_16x16x32_bf16 v[20:23], a[136:139], v[48:51], v[20:23]\n"
  "v_exp_f32 v9, v9\n"
  "v_mfma_f32_16x16x32_bf16 v[20:23], a[132:135], v[44:47], v[20:23]\n"
  "s_add_u32 m0, s59, 14208\n"
  "v_exp_f32 v10, v10\n"
  "v_mfma_f32_16x16x32_bf16 v[20:23], a[128:131], v[40:43], v[20:23]\n"
  "s_waitcnt lgkmcnt(3)\n"
  "buffer_load_dwordx4 v112, s[40:43], s57 offen offset:128 nt lds\n"
  "buffer_load_dwordx4 v113, s[40:43], s57 offen offset:1152 nt lds\n"
  "v_exp_f32 v11, v11\n"
  "v_mfma_f32_16x16x32_bf16 v[20:23], a[124:127], v[36:39], v[20:23]\n"
  "s_waitcnt vmcnt(14)\n"
  "ds_read_b128 a[88:91], v87 offset:0\n"
  "ds_read_b128 a[92:95], v88 offset:0\n"
  "v_cvt_pk_bf16_f32 v8, v8, v9\n"
  "v_cvt_pk_bf16_f32 v9, v10, v11\n"
  "v_mfma_f32_16x16x32_bf16 v[20:23], a[120:123], v[32:35], v[20:23]\n"
  "s_add_u32 m0, s59, 16384\n"
  "ds_read_b128 v[56:59], v81 offset:0\n"
  "ds_read_b128 v[60:63], v82 offset:0\n"
  "v_mfma_f32_16x16x32_bf16 v[24:27], a[144:147], v[32:35], v[92:95]\n"
  "s_waitcnt lgkmcnt(4)\n"
  "buffer_load_dwordx4 v112, s[40:43], s57 offen offset:0 nt lds\n"
  "buffer_load_dwordx4 v113, s[40:43], s57 offen offset:1024 nt lds\n"
  "s_add_u32 s57, s57, 6144\n"
  "v_exp_f32 v12, v12\n"
  "v_mfma_f32_16x16x32_bf16 v[24:27], a[148:151], v[36:39], v[24:27]\n"
  "s_waitcnt vmcnt(14)\n"
  "ds_read_b128 a[80:83], v87 offset:2048\n"
  "ds_read_b128 a[84:87], v88 offset:2048\n"
  "v_exp_f32 v13, v13\n"
  "v_mfma_f32_16x16x32_bf16 v[24:27], a[152:155], v[40:43], v[24:27]\n"
  "v_exp_f32 v14, v14\n"
  "v_mfma_f32_16x16x32_bf16 v[24:27], a[156:159], v[44:47], v[24:27]\n"
  "s_add_u32 m0, s59, 0\n"
  "v_exp_f32 v15, v15\n"
  "v_mfma_f32_16x16x32_bf16 v[24:27], a[160:163], v[48:51], v[24:27]\n"
  "s_waitcnt lgkmcnt(4)\n"
  "buffer_load_dwordx4 v112, s[40:43], s57 offen offset:0 nt lds\n"
  "buffer_load_dwordx4 v113, s[40:43], s57 offen offset:1024 nt lds\n"
  "v_cvt_pk_bf16_f32 v10, v12, v13\n"
  "v_cvt_pk_bf16_f32 v11, v14, v15\n"
  "v_mfma_f32_16x16x32_bf16 v[24:27], a[164:167], v[52:55], v[24:27]\n"
  "s_waitcnt vmcnt(14)\n"
  "ds_read_b128 a[72:75], v87 offset:4096\n"
  "ds_read_b128 a[76:79], v88 offset:4096\n"
  "ds_write_b128 v80, v[8:11] offset:1024\n"
  "v_pk_mul_f32 v[16:17], v[16:17], s[60:61] op_sel_hi:[1,0]\n"
  "v_pk_mul_f32 v[18:19], v[18:19], s[60:61] op_sel_hi:[1,0]\n"
  "v_pk_mul_f32 v[20:21], v[20:21], s[60:61] op_sel_hi:[1,0]\n"
  "v_pk_mul_f32 v[22:23], v[22:23], s[60:61] op_sel_hi:[1,0]\n"
  "v_mfma_f32_16x16x32_bf16 v[28:31], a[188:191], v[52:55], v[92:95]\n"
  "v_exp_f32 v16, v16\n"
  "v_mfma_f32_16x16x32_bf16 v[28:31], a[184:187], v[48:51], v[28:31]\n"
  "s_add_u32 m0, s59, 1920\n"
  "v_exp_f32 v17, v17\n"
  "v_mfma_f32_16x16x32_bf16 v[28:31], a[180:183], v[44:47], v[28:31]\n"
  "s_waitcnt lgkmcnt(3)\n"
  "buffer_load_dwordx4 v112, s[40:43], s57 offen offset:128 nt lds\n"
  "buffer_load_dwordx4 v113, s[40:43], s57 offen offset:1152 nt lds\n"
  "v_exp_f32 v18, v18\n"
  "v_mfma_f32_16x16x32_bf16 v[28:31], a[176:179], v[40:43], v[28:31]\n"
  "s_waitcnt vmcnt(14)\n"
  "ds_read_b128 a[96:99], v87 offset:6144\n"
  "ds_read_b128 a[100:103], v88 offset:6144\n"
  "v_exp_f32 v19, v19\n"
  "v_mfma_f32_16x16x32_bf16 v[28:31], a[172:175], v[36:39], v[28:31]\n"
  "s_add_u32 m0, s59, 3840\n"
  "v_cvt_pk_bf16_f32 v16, v16, v17\n"
  "v_cvt_pk_bf16_f32 v17, v18, v19\n"
  "v_mfma_f32_16x16x32_bf16 v[28:31], a[168:171], v[32:35], v[28:31]\n"
  "s_waitcnt lgkmcnt(3)\n"
  "buffer_load_dwordx4 v112, s[40:43], s57 offen offset:256 nt lds\n"
  "buffer_load_dwordx4 v113, s[40:43], s57 offen offset:1280 nt lds\n"
  "s_add_u32 s57, s57, 6144\n"
  "ds_read_b128 v[64:67], v81 offset:2048\n"
  "ds_read_b128 v[68:71], v82 offset:2048\n"
  "v_mfma_f32_16x16x32_bf16 v[0:3], a[192:195], v[32:35], v[92:95]\n"
  "s_waitcnt vmcnt(14)\n"
  "ds_read_b128 a[104:107], v87 offset:8192\n"
  "ds_read_b128 a[108:111], v88 offset:8192\n"
  "v_exp_f32 v20, v20\n"
  "v_mfma_f32_16x16x32_bf16 v[0:3], a[196:199], v[36:39], v[0:3]\n"
  "v_exp_f32 v21, v21\n"
  "v_mfma_f32_16x16x32_bf16 v[0:3], a[200:203], v[40:43], v[0:3]\n"
  "s_add_u32 m0, s59, 5888\n"
  "v_exp_f32 v22, v22\n"
  "v_mfma_f32_16x16x32_bf16 v[0:3], a[204:207], v[44:47], v[0:3]\n"
  "s_waitcnt lgkmcnt(4)\n"
  "buffer_load_dwordx4 v112, s[40:43], s57 offen offset:256 nt lds\n"
  "buffer_load_dwordx4 v113, s[40:43], s57 offen offset:1280 nt lds\n"
  "v_exp_f32 v23, v23\n"
  "v_mfma_f32_16x16x32_bf16 v[0:3], a[208:211], v[48:51], v[0:3]\n"
  "s_waitcnt vmcnt(14)\n"
  "ds_read_b128 a[112:115], v87 offset:10240\n"
  "ds_read_b128 a[116:119], v88 offset:10240\n"
  "v_cvt_pk_bf16_f32 v18, v20, v21\n"
  "v_cvt_pk_bf16_f32 v19, v22, v23\n"
  "v_mfma_f32_16x16x32_bf16 v[0:3], a[212:215], v[52:55], v[0:3]\n"
  "s_add_u32 m0, s59, 8064\n"
  "ds_write_b128 v80, v[16:19] offset:2048\n"
  "v_pk_mul_f32 v[24:25], v[24:25], s[60:61] op_sel_hi:[1,0]\n"
  "v_pk_mul_f32 v[26:27], v[26:27], s[60:61] op_sel_hi:[1,0]\n"
  "v_pk_mul_f32 v[28:29], v[28:29], s[60:61] op_sel_hi:[1,0]\n"
  "v_pk_mul_f32 v[30:31], v[30:31], s[60:61] op_sel_hi:[1,0]\n"
  "v_mfma_f32_16x16x32_bf16 v[4:7], a[236:239], v[52:55], v[92:95]\n"
  "s_waitcnt lgkmcnt(3)\n"
  "buffer_load_dwordx4 v112, s[40:43], s57 offen offset:128 nt lds\n"
  "buffer_load_dwordx4 v113, s[40:43], s57 offen offset:1152 nt lds\n"
  "v_exp_f32 v24, v24\n"
  "v_mfma_f32_16x16x32_bf16 v[4:7], a[232:235], v[48:51], v[4:7]\n"
  "s_waitcnt vmcnt(14)\n"
  "ds_read_b128 a[136:139], v87 offset:12288\n"
  "ds_read_b128 a[140:143], v88 offset:12288\n"
  "v_exp_f32 v25, v25\n"
  "v_mfma_f32_16x16x32_bf16 v[4:7], a[228:231], v[44:47], v[4:7]\n"
  "v_exp_f32 v26, v26\n"
  "v_mfma_f32_16x16x32_bf16 v[4:7], a[224:227], v[40:43], v[4:7]\n"
  "s_add_u32 m0, s59, 10240\n"
  "v_exp_f32 v27, v27\n"
  "v_mfma_f32_16x16x32_bf16 v[4:7], a[220:223], v[36:39], v[4:7]\n"
  "s_waitcnt lgkmcnt(3)\n"
  "buffer_load_dwordx4 v112, s[40:43], s57 offen offset:0 nt lds\n"
  "buffer_load_dwordx4 v113, s[40:43], s57 offen offset:1024 nt lds\n"
  "s_add_u32 s57, s57, 6144\n"
  "v_cvt_pk_bf16_f32 v24, v24, v25\n"
  "v_cvt_pk_bf16_f32 v25, v26, v27\n"
  "v_mfma_f32_16x16x32_bf16 v[4:7], a[216:219], v[32:35], v[4:7]\n"
  "s_waitcnt vmcnt(14)\n"
  "ds_read_b128 a[128:131], v87 offset:14336\n"
  "ds_read_b128 a[132:135], v88 offset:14336\n"
  "ds_read_b128 v[72:75], v81 offset:4096\n"
  "ds_read_b128 v[76:79], v82 offset:4096\n"
  "v_mfma_f32_16x16x32_bf16 v[8:11], a[240:243], v[32:35], v[92:95]\n"
  "v_exp_f32 v28, v28\n"
  "v_mfma_f32_16x16x32_bf16 v[8:11], a[244:247], v[36:39], v[8:11]\n"
  "s_add_u32 m0, s59, 12288\n"
  "v_exp_f32 v29, v29\n"
  "v_mfma_f32_16x16x32_bf16 v[8:11], a[248:251], v[40:43], v[8:11]\n"
  "s_waitcnt lgkmcnt(4)\n"
  "buffer_load_dwordx4 v112, s[40:43], s57 offen offset:0 nt lds\n"
  "buffer_load_dwordx4 v113, s[40:43], s57 offen offset:1024 nt lds\n"
  "v_exp_f32 v30, v30\n"
  "v_mfma_f32_16x16x32_bf16 v[8:11], a[252:255], v[44:47], v[8:11]\n"
  "s_waitcnt vmcnt(14)\n"
  "ds_read_b128 a[120:123], v87 offset:16384\n"
  "ds_read_b128 a[124:127], v88 offset:16384\n"
  "v_exp_f32 v31, v31\n"
  "v_mfma_f32_16x16x32_bf16 v[8:11], v[128:131], v[48:51], v[8:11]\n"
  "s_add_u32 m0, s59, 14208\n"
  "v_cvt_pk_bf16_f32 v26, v28, v29\n"
  "v_cvt_pk_bf16_f32 v27, v30, v31\n"
  "v_mfma_f32_16x16x32_bf16 v[8:11], v[132:135], v[52:55], v[8:11]\n"
  "s_waitcnt lgkmcnt(4)\n"
  "buffer_load_dwordx4 v112, s[40:43], s57 offen offset:128 nt lds\n"
  "buffer_load_dwordx4 v113, s[40:43], s57 offen offset:1152 nt lds\n"
  "ds_write_b128 v80, v[24:27] offset:3072\n"
  "v_pk_mul_f32 v[0:1], v[0:1], s[60:61] op_sel_hi:[1,0]\n"
  "v_pk_mul_f32 v[2:3], v[2:3], s[60:61] op_sel_hi:[1,0]\n"
  "v_pk_mul_f32 v[4:5], v[4:5], s[60:61] op_sel_hi:[1,0]\n"
  "v_pk_mul_f32 v[6:7], v[6:7], s[60:61] op_sel_hi:[1,0]\n"
  "v_mfma_f32_16x16x32_bf16 v[12:15], v[156:159], v[52:55], v[92:95]\n"
  "s_waitcnt vmcnt(14)\n"
  "ds_read_b128 a[144:147], v87 offset:0\n"
  "ds_read_b128 a[148:151], v88 offset:0\n"
  "v_exp_f32 v0, v0\n"
  "v_mfma_f32_16x16x32_bf16 v[12:15], v[152:155], v[48:51], v[12:15]\n"
  "v_exp_f32 v1, v1\n"
  "v_mfma_f32_16x16x32_bf16 v[12:15], v[148:151], v[44:47], v[12:15]\n"
  "s_add_u32 m0, s59, 16128\n"
  "v_exp_f32 v2, v2\n"
  "v_mfma_f32_16x16x32_bf16 v[12:15], v[144:147], v[40:43], v[12:15]\n"
  "s_waitcnt lgkmcnt(3)\n"
  "buffer_load_dwordx4 v112, s[40:43], s57 offen offset:256 nt lds\n"
  "buffer_load_dwordx4 v113, s[40:43], s57 offen offset:1280 nt lds\n"
  "s_add_u32 s57, s57, 6144\n"
  "v_exp_f32 v3, v3\n"
  "v_mfma_f32_16x16x32_bf16 v[12:15], v[140:143], v[36:39], v[12:15]\n"
  "s_waitcnt vmcnt(14)\n"
  "ds_read_b128 a[152:155], v87 offset:2048\n"
  "ds_read_b128 a[156:159], v88 offset:2048\n"
  "v_cvt_pk_bf16_f32 v0, v0, v1\n"
  "v_cvt_pk_bf16_f32 v1, v2, v3\n"
  "v_mfma_f32_16x16x32_bf16 v[12:15], v[136:139], v[32:35], v[12:15]\n"
  "s_add_u32 m0, s59, -256\n"
  "v_mul_f32 v8, s60, v8\n"
  "v_mul_f32 v9, s60, v9\n"
  "v_mfma_f32_16x16x32_bf16 v[16:19], v[160:163], v[32:35], v[92:95]\n"
  "s_waitcnt lgkmcnt(2)\n"
  "buffer_load_dwordx4 v112, s[40:43], s57 offen offset:256 nt lds\n"
  "buffer_load_dwordx4 v113, s[40:43], s57 offen offset:1280 nt lds\n"
  "v_exp_f32 v4, v4\n"
  "v_mfma_f32_16x16x32_bf16 v[16:19], v[164:167], v[36:39], v[16:19]\n"
  "s_waitcnt vmcnt(14)\n"
  "ds_read_b128 a[160:163], v87 offset:4096\n"
  "ds_read_b128 a[164:167], v88 offset:4096\n"
  "v_exp_f32 v5, v5\n"
  "v_mfma_f32_16x16x32_bf16 v[16:19], v[168:171], v[40:43], v[16:19]\n"
  "v_exp_f32 v6, v6\n"
  "v_mfma_f32_16x16x32_bf16 v[16:19], v[172:175], v[44:47], v[16:19]\n"
  "s_add_u32 m0, s59, 1920\n"
  "v_exp_f32 v7, v7\n"
  "v_mfma_f32_16x16x32_bf16 v[16:19], v[176:179], v[48:51], v[16:19]\n"
  "s_waitcnt lgkmcnt(2)\n"
  "buffer_load_dwordx4 v112, s[40:43], s57 offen offset:128 nt lds\n"
  "buffer_load_dwordx4 v113, s[40:43], s57 offen offset:1152 nt lds\n"
  "v_cvt_pk_bf16_f32 v2, v4, v5\n"
  "v_cvt_pk_bf16_f32 v3, v6, v7\n"
  "v_mfma_f32_16x16x32_bf16 v[16:19], v[180:183], v[52:55], v[16:19]\n"
  "s_waitcnt vmcnt(14)\n"
  "ds_read_b128 a[184:187], v87 offset:6144\n"
  "ds_read_b128 a[188:191], v88 offset:6144\n"
  "ds_write_b128 v80, v[0:3] offset:4096\n"
  "v_pk_mul_f32 v[10:11], v[10:11], s[60:61] op_sel_hi:[1,0]\n"
  "v_pk_mul_f32 v[12:13], v[12:13], s[60:61] op_sel_hi:[1,0]\n"
  "v_pk_mul_f32 v[14:15], v[14:15], s[60:61] op_sel_hi:[1,0]\n"
  "v_mfma_f32_16x16x32_bf16 v[20:23], v[204:207], v[52:55], v[92:95]\n"
  "v_exp_f32 v8, v8\n"
  "v_mfma_f32_16x16x32_bf16 v[20:23], v[200:203], v[48:51], v[20:23]\n"
  "s_add_u32 m0, s59, 4096\n"
  "v_exp_f32 v9, v9\n"
  "v_mfma_f32_16x16x32_bf16 v[20:23], v[196:199], v[44:47], v[20:23]\n"
  "s_waitcnt lgkmcnt(3)\n"
  "buffer_load_dwordx4 v112, s[40:43], s57 offen offset:0 nt lds\n"
  "buffer_load_dwordx4 v113, s[40:43], s57 offen offset:1024 nt lds\n"
  "s_add_u32 s57, s57, 6144\n"
  "v_exp_f32 v10, v10\n"
  "v_mfma_f32_16x16x32_bf16 v[20:23], v[192:195], v[40:43], v[20:23]\n"
  "s_waitcnt vmcnt(14)\n"
  "ds_read_b128 a[176:179], v87 offset:8192\n"
  "ds_read_b128 a[180:183], v88 offset:8192\n"
  "v_exp_f32 v11, v11\n"
  "v_mfma_f32_16x16x32_bf16 v[20:23], v[188:191], v[36:39], v[20:23]\n"
  "s_add_u32 m0, s59, 6144\n"
  "v_cvt_pk_bf16_f32 v8, v8, v9\n"
  "v_cvt_pk_bf16_f32 v9, v10, v11\n"
  "v_mfma_f32_16x16x32_bf16 v[20:23], v[184:187], v[32:35], v[20:23]\n"
  "s_waitcnt lgkmcnt(3)\n"
  "buffer_load_dwordx4 v112, s[40:43], s57 offen offset:0 nt lds\n"
  "buffer_load_dwordx4 v113, s[40:43], s57 offen offset:1024 nt lds\n"
  "v_mul_f32 v16, s60, v16\n"
  "v_mul_f32 v17, s60, v17\n"
  "v_mfma_f32_16x16x32_bf16 v[24:27], v[208:211], v[32:35], v[92:95]\n"
  "s_waitcnt vmcnt(14)\n"
  "ds_read_b128 a[168:171], v87 offset:10240\n"
  "ds_read_b128 a[172:175], v88 offset:10240\n"
  "v_exp_f32 v12, v12\n"
  "v_mfma_f32_16x16x32_bf16 v[24:27], v[212:215], v[36:39], v[24:27]\n"
  "v_exp_f32 v13, v13\n"
  "v_mfma_f32_16x16x32_bf16 v[24:27], v[216:219], v[40:43], v[24:27]\n"
  "s_add_u32 m0, s59, 8064\n"
  "v_exp_f32 v14, v14\n"
  "v_mfma_f32_16x16x32_bf16 v[24:27], v[220:223], v[44:47], v[24:27]\n"
  "s_waitcnt lgkmcnt(2)\n"
  "buffer_load_dwordx4 v112, s[40:43], s57 offen offset:128 nt lds\n"
  "buffer_load_dwordx4 v113, s[40:43], s57 offen offset:1152 nt lds\n"
  "v_exp_f32 v15, v15\n"
  "v_mfma_f32_16x16x32_bf16 v[24:27], v[224:227], v[48:51], v[24:27]\n"
  "s_waitcnt vmcnt(14)\n"
  "ds_read_b128 a[192:195], v87 offset:12288\n"
  "ds_read_b128 a[196:199], v88 offset:12288\n"
  "v_cvt_pk_bf16_f32 v10, v12, v13\n"
  "v_cvt_pk_bf16_f32 v11, v14, v15\n"
  "v_mfma_f32_16x16x32_bf16 v[24:27], v[228:231], v[52:55], v[24:27]\n"
  "s_add_u32 m0, s59, 9984\n"
  "ds_write_b128 v80, v[8:11] offset:5120\n"
  "v_pk_mul_f32 v[18:19], v[18:19], s[60:61] op_sel_hi:[1,0]\n"
  "v_pk_mul_f32 v[20:21], v[20:21], s[60:61] op_sel_hi:[1,0]\n"
  "v_pk_mul_f32 v[22:23], v[22:23], s[60:61] op_sel_hi:[1,0]\n"
  "v_mfma_f32_16x16x32_bf16 v[28:31], v[252:255], v[52:55], v[92:95]\n"
  "s_waitcnt lgkmcnt(3)\n"
  "buffer_load_dwordx4 v112, s[40:43], s57 offen offset:256 nt lds\n"
  "buffer_load_dwordx4 v113, s[40:43], s57 offen offset:1280 nt lds\n"
  "s_add_u32 s57, s57, 6144\n"
  "v_exp_f32 v16, v16\n"
  "v_mfma_f32_16x16x32_bf16 v[28:31], v[248:251], v[48:51], v[28:31]\n"
  "s_waitcnt vmcnt(14)\n"
  "ds_read_b128 a[200:203], v87 offset:14336\n"
  "ds_read_b128 a[204:207], v88 offset:14336\n"
  "v_exp_f32 v17, v17\n"
  "v_mfma_f32_16x16x32_bf16 v[28:31], v[244:247], v[44:47], v[28:31]\n"
  "v_exp_f32 v18, v18\n"
  "v_mfma_f32_16x16x32_bf16 v[28:31], v[240:243], v[40:43], v[28:31]\n"
  "s_add_u32 m0, s59, 12032\n"
  "v_exp_f32 v19, v19\n"
  "v_mfma_f32_16x16x32_bf16 v[28:31], v[236:239], v[36:39], v[28:31]\n"
  "s_waitcnt lgkmcnt(3)\n"
  "buffer_load_dwordx4 v112, s[40:43], s57 offen offset:256 nt lds\n"
  "buffer_load_dwordx4 v113, s[40:43], s57 offen offset:1280 nt lds\n"
  "v_cvt_pk_bf16_f32 v16, v16, v17\n"
  "v_cvt_pk_bf16_f32 v17, v18, v19\n"
  "v_mfma_f32_16x16x32_bf16 v[28:31], v[232:235], v[32:35], v[28:31]\n"
  "s_waitcnt vmcnt(14)\n"
  "ds_read_b128 a[208:211], v87 offset:16384\n"
  "ds_read_b128 a[212:215], v88 offset:16384\n"
  "v_mfma_f32_16x16x32_bf16 v[0:3], a[0:3], v[56:59], v[92:95]\n"
  "s_add_u32 s66, s66, 6144\n"
  "s_cmp_eq_u32 s66, 86016\n"
  "s_cselect_b32 s66, 49152, s66\n"
  "v_exp_f32 v20, v20\n"
  "v_mfma_f32_16x16x32_bf16 v[0:3], a[4:7], v[60:63], v[0:3]\n"
  "s_add_u32 m0, s59, 14208\n"
  "v_exp_f32 v21, v21\n"
  "v_mfma_f32_16x16x32_bf16 v[0:3], a[8:11], v[64:67], v[0:3]\n"
  "s_waitcnt lgkmcnt(2)\n"
  "buffer_load_dwordx4 v112, s[40:43], s57 offen offset:128 nt lds\n"
  "buffer_load_dwordx4 v113, s[40:43], s57 offen offset:1152 nt lds\n"
  "v_exp_f32 v22, v22\n"
  "v_mfma_f32_16x16x32_bf16 v[0:3], a[12:15], v[68:71], v[0:3]\n"
  "s_waitcnt vmcnt(14)\n"
  "ds_read_b128 a[232:235], v87 offset:0\n"
  "ds_read_b128 a[236:239], v88 offset:0\n"
  "v_exp_f32 v23, v23\n"
  "v_mfma_f32_16x16x32_bf16 v[0:3], a[16:19], v[72:75], v[0:3]\n"
  "s_add_u32 m0, s59, 16384\n"
  "v_cvt_pk_bf16_f32 v18, v20, v21\n"
  "v_cvt_pk_bf16_f32 v19, v22, v23\n"
  "v_mfma_f32_16x16x32_bf16 v[0:3], a[20:23], v[76:79], v[0:3]\n"
  "s_waitcnt lgkmcnt(2)\n"
  "buffer_load_dwordx4 v112, s[40:43], s57 offen offset:0 nt lds\n"
  "buffer_load_dwordx4 v113, s[40:43], s57 offen offset:1024 nt lds\n"
  "s_add_u32 s57, s57, 6144\n"
  "ds_write_b128 v80, v[16:19] offset:6144\n"
  "v_pk_mul_f32 v[24:25], v[24:25], s[60:61] op_sel_hi:[1,0]\n"
  "v_pk_mul_f32 v[26:27], v[26:27], s[60:61] op_sel_hi:[1,0]\n"
  "v_pk_mul_f32 v[28:29], v[28:29], s[60:61] op_sel_hi:[1,0]\n"
  "v_pk_mul_f32 v[30:31], v[30:31], s[60:61] op_sel_hi:[1,0]\n"
  "v_mfma_f32_16x16x32_bf16 v[4:7], a[44:47], v[76:79], v[92:95]\n"
  "s_waitcnt vmcnt(14)\n"
  "ds_read_b128 a[224:227], v87 offset:2048\n"
  "ds_read_b128 a[228:231], v88 offset:2048\n"
  "v_exp_f32 v24, v24\n"
  "v_mfma_f32_16x16x32_bf16 v[4:7], a[40:43], v[72:75], v[4:7]\n"
  "v_exp_f32 v25, v25\n"
  "v_mfma_f32_16x16x32_bf16 v[4:7], a[36:39], v[68:71], v[4:7]\n"
  "s_add_u32 m0, s59, 0\n"
  "v_exp_f32 v26, v26\n"
  "v_mfma_f32_16x16x32_bf16 v[4:7], a[32:35], v[64:67], v[4:7]\n"
  "s_waitcnt lgkmcnt(3)\n"
  "buffer_load_dwordx4 v112, s[40:43], s57 offen offset:0 nt lds\n"
  "buffer_load_dwordx4 v113, s[40:43], s57 offen offset:1024 nt lds\n"
  "v_exp_f32 v27, v27\n"
  "v_mfma_f32_16x16x32_bf16 v[4:7], a[28:31], v[60:63], v[4:7]\n"
  "s_waitcnt vmcnt(14)\n"
  "ds_read_b128 a[216:219], v87 offset:4096\n"
  "ds_read_b128 a[220:223], v88 offset:4096\n"
  "v_cvt_pk_bf16_f32 v24, v24, v25\n"
  "v_cvt_pk_bf16_f32 v25, v26, v27\n"
  "v_mfma_f32_16x16x32_bf16 v[4:7], a[24:27], v[56:59], v[4:7]\n"
  "s_add_u32 m0, s59, 1920\n"
  "v_add_u32 v81, s66, v83\n"
  "v_add_u32 v82, s66, v84\n"
  "v_mfma_f32_16x16x32_bf16 v[8:11], a[48:51], v[56:59], v[92:95]\n"
  "s_waitcnt lgkmcnt(2)\n"
  "buffer_load_dwordx4 v112, s[40:43], s57 offen offset:128 nt lds\n"
  "buffer_load_dwordx4 v113, s[40:43], s57 offen offset:1152 nt lds\n"
  "v_exp_f32 v28, v28\n"
  "v_mfma_f32_16x16x32_bf16 v[8:11], a[52:55], v[60:63], v[8:11]\n"
  "s_waitcnt vmcnt(14)\n"
  "ds_read_b128 a[240:243], v87 offset:6144\n"
  "ds_read_b128 a[244:247], v88 offset:6144\n"
  "v_exp_f32 v29, v29\n"
  "v_mfma_f32_16x16x32_bf16 v[8:11], a[56:59], v[64:67], v[8:11]\n"
  "v_exp_f32 v30, v30\n"
  "v_mfma_f32_16x16x32_bf16 v[8:11], a[60:63], v[68:71], v[8:11]\n"
  "s_add_u32 m0, s59, 3840\n"
  "v_exp_f32 v31, v31\n"
  "v_mfma_f32_16x16x32_bf16 v[8:11], a[64:67], v[72:75], v[8:11]\n"
  "s_waitcnt lgkmcnt(2)\n"
  "buffer_load_dwordx4 v112, s[40:43], s57 offen offset:256 nt lds\n"
  "buffer_load_dwordx4 v113, s[40:43], s57 offen offset:1280 nt lds\n"
  "s_add_u32 s57, s57, 6144\n"
  "v_cvt_pk_bf16_f32 v26, v28, v29\n"
  "v_cvt_pk_bf16_f32 v27, v30, v31\n"
  "v_mfma_f32_16x16x32_bf16 v[8:11], a[68:71], v[76:79], v[8:11]\n"
  "s_waitcnt vmcnt(14)\n"
  "ds_read_b128 a[248:251], v87 offset:8192\n"
  "ds_read_b128 a[252:255], v88 offset:8192\n"
  "ds_write_b128 v80, v[24:27] offset:7168\n"
  "v_pk_mul_f32 v[0:1], v[0:1], s[60:61] op_sel_hi:[1,0]\n"
  "v_pk_mul_f32 v[2:3], v[2:3], s[60:61] op_sel_hi:[1,0]\n"
  "v_pk_mul_f32 v[4:5], v[4:5], s[60:61] op_sel_hi:[1,0]\n"
  "v_pk_mul_f32 v[6:7], v[6:7], s[60:61] op_sel_hi:[1,0]\n"
  "v_mfma_f32_16x16x32_bf16 v[12:15], a[92:95], v[76:79], v[92:95]\n"
  "v_exp_f32 v0, v0\n"
  "v_mfma_f32_16x16x32_bf16 v[12:15], a[88:91], v[72:75], v[12:15]\n"
  "s_add_u32 m0, s59, 5888\n"
  "v_exp_f32 v1, v1\n"
  "v_mfma_f32_16x16x32_bf16 v[12:15], a[84:87], v[68:71], v[12:15]\n"
  "s_waitcnt lgkmcnt(3)\n"
  "buffer_load_dwordx4 v112, s[40:43], s57 offen offset:256 nt lds\n"
  "buffer_load_dwordx4 v113, s[40:43], s57 offen offset:1280 nt lds\n"
  "v_exp_f32 v2, v2\n"
  "v_mfma_f32_16x16x32_bf16 v[12:15], a[80:83], v[64:67], v[12:15]\n"
  "s_waitcnt vmcnt(14)\n"
  "ds_read_b128 v[128:131], v87 offset:10240\n"
  "ds_read_b128 v[132:135], v88 offset:10240\n"
  "v_exp_f32 v3, v3\n"
  "v_mfma_f32_16x16x32_bf16 v[12:15], a[76:79], v[60:63], v[12:15]\n"
  "s_add_u32 m0, s59, 8064\n"
  "v_cvt_pk_bf16_f32 v0, v0, v1\n"
  "v_cvt_pk_bf16_f32 v1, v2, v3\n"
  "v_mfma_f32_16x16x32_bf16 v[12:15], a[72:75], v[56:59], v[12:15]\n"
  "s_waitcnt lgkmcnt(3)\n"
  "buffer_load_dwordx4 v112, s[40:43], s57 offen offset:128 nt lds\n"
  "buffer_load_dwordx4 v113, s[40:43], s57 offen offset:1152 nt lds\n"
  "s_waitcnt lgkmcnt(2)\n"
  "s_barrier\n"
  "v_xor_b32 v80, 0x4000, v80\n"
  "v_mfma_f32_16x16x32_bf16 v[16:19], a[96:99], v[56:59], v[92:95]\n"
  "s_waitcnt vmcnt(14)\n"
  "ds_read_b128 v[152:155], v87 offset:12288\n"
  "ds_read_b128 v[156:159], v88 offset:12288\n"
  "v_exp_f32 v4, v4\n"
  "v_mfma_f32_16x16x32_bf16 v[16:19], a[100:103], v[60:63], v[16:19]\n"
  "v_exp_f32 v5, v5\n"
  "v_mfma_f32_16x16x32_bf16 v[16:19], a[104:107], v[64:67], v[16:19]\n"
  "s_add_u32 m0, s59, 10240\n"
  "v_exp_f32 v6, v6\n"
  "v_mfma_f32_16x16x32_bf16 v[16:19], a[108:111], v[68:71], v[16:19]\n"
  "s_waitcnt lgkmcnt(2)\n"
  "buffer_load_dwordx4 v112, s[40:43], s57 offen offset:0 nt lds\n"
  "buffer_load_dwordx4 v113, s[40:43], s57 offen offset:1024 nt lds\n"
  "s_add_u32 s57, s57, 6144\n"
  "v_exp_f32 v7, v7\n"
  "v_mfma_f32_16x16x32_bf16 v[16:19], a[112:115], v[72:75], v[16:19]\n"
  "s_waitcnt vmcnt(14)\n"
  "ds_read_b128 v[144:147], v87 offset:14336\n"
  "ds_read_b128 v[148:151], v88 offset:14336\n"
  "v_cvt_pk_bf16_f32 v2, v4, v5\n"
  "v_cvt_pk_bf16_f32 v3, v6, v7\n"
  "v_mfma_f32_16x16x32_bf16 v[16:19], a[116:119], v[76:79], v[16:19]\n"
  "s_add_u32 m0, s59, 12288\n"
  "ds_write_b128 v80, v[0:3] offset:0\n"
  "v_pk_mul_f32 v[8:9], v[8:9], s[60:61] op_sel_hi:[1,0]\n"
  "v_pk_mul_f32 v[10:11], v[10:11], s[60:61] op_sel_hi:[1,0]\n"
  "v_pk_mul_f32 v[12:13], v[12:13], s[60:61] op_sel_hi:[1,0]\n"
  "v_pk_mul_f32 v[14:15], v[14:15], s[60:61] op_sel_hi:[1,0]\n"
  "v_mfma_f32_16x16x32_bf16 v[20:23], a[140:143], v[76:79], v[92:95]\n"
  "s_waitcnt lgkmcnt(3)\n"
  "buffer_load_dwordx4 v112, s[40:43], s57 offen offset:0 nt lds\n"
  "buffer_load_dwordx4 v113, s[40:43], s57 offen offset:1024 nt lds\n"
  "v_exp_f32 v8, v8\n"
  "v_mfma_f32_16x16x32_bf16 v[20:23], a[136:139], v[72:75], v[20:23]\n"
  "s_waitcnt vmcnt(14)\n"
  "ds_read_b128 v[136:139], v87 offset:16384\n"
  "ds_read_b128 v[140:143], v88 offset:16384\n"
  "v_exp_f32 v9, v9\n"
  "v_mfma_f32_16x16x32_bf16 v[20:23], a[132:135], v[68:71], v[20:23]\n"
  "v_exp_f32 v10, v10\n"
  "v_mfma_f32_16x16x32_bf16 v[20:23], a[128:131], v[64:67], v[20:23]\n"
  "s_add_u32 m0, s59, 14208\n"
  "v_exp_f32 v11, v11\n"
  "v_mfma_f32_16x16x32_bf16 v[20:23], a[124:127], v[60:63], v[20:23]\n"
  "s_waitcnt lgkmcnt(3)\n"
  "buffer_load_dwordx4 v112, s[40:43], s57 offen offset:128 nt lds\n"
  "buffer_load_dwordx4 v113, s[40:43], s57 offen offset:1152 nt lds\n"
  "v_cvt_pk_bf16_f32 v8, v8, v9\n"
  "v_cvt_pk_bf16_f32 v9, v10, v11\n"
  "v_mfma_f32_16x16x32_bf16 v[20:23], a[120:123], v[56:59], v[20:23]\n"
  "s_waitcnt vmcnt(14)\n"
  "ds_read_b128 v[160:163], v87 offset:0\n"
  "ds_read_b128 v[164:167], v88 offset:0\n"
  "ds_read_b128 v[32:35], v81 offset:0\n"
  "ds_read_b128 v[36:39], v82 offset:0\n"
  "v_mfma_f32_16x16x32_bf16 v[24:27], a[144:147], v[56:59], v[92:95]\n"
  "v_exp_f32 v12, v12\n"
  "v_mfma_f32_16x16x32_bf16 v[24:27], a[148:151], v[60:63], v[24:27]\n"
  "s_add_u32 m0, s59, 16128\n"
  "v_exp_f32 v13, v13\n"
  "v_mfma_f32_16x16x32_bf16 v[24:27], a[152:155], v[64:67], v[24:27]\n"
  "s_waitcnt lgkmcnt(4)\n"
  "buffer_load_dwordx4 v112, s[40:43], s57 offen offset:256 nt lds\n"
  "buffer_load_dwordx4 v113, s[40:43], s57 offen offset:1280 nt lds\n"
  "s_add_u32 s57, s57, 6144\n"
  "v_exp_f32 v14, v14\n"
  "v_mfma_f32_16x16x32_bf16 v[24:27], a[156:159], v[68:71], v[24:27]\n"
  "s_waitcnt vmcnt(14)\n"
  "ds_read_b128 v[168:171], v87 offset:2048\n"
  "ds_read_b128 v[172:175], v88 offset:2048\n"
  "v_exp_f32 v15, v15\n"
  "v_mfma_f32_16x16x32_bf16 v[24:27], a[160:163], v[72:75], v[24:27]\n"
  "s_add_u32 m0, s59, -256\n"
  "v_cvt_pk_bf16_f32 v10, v12, v13\n"
  "v_cvt_pk_bf16_f32 v11, v14, v15\n"
  "v_mfma_f32_16x16x32_bf16 v[24:27], a[164:167], v[76:79], v[24:27]\n"
  "s_waitcnt lgkmcnt(4)\n"
  "buffer_load_dwordx4 v112, s[40:43], s57 offen offset:256 nt lds\n"
  "buffer_load_dwordx4 v113, s[40:43], s57 offen offset:1280 nt lds\n"
  "ds_write_b128 v80, v[8:11] offset:1024\n"
  "v_pk_mul_f32 v[16:17], v[16:17], s[60:61] op_sel_hi:[1,0]\n"
  "v_pk_mul_f32 v[18:19], v[18:19], s[60:61] op_sel_hi:[1,0]\n"
  "v_pk_mul_f32 v[20:21], v[20:21], s[60:61] op_sel_hi:[1,0]\n"
  "v_pk_mul_f32 v[22:23], v[22:23], s[60:61] op_sel_hi:[1,0]\n"
  "v_mfma_f32_16x16x32_bf16 v[28:31], a[188:191], v[76:79], v[92:95]\n"
  "s_waitcnt vmcnt(14)\n"
  "ds_read_b128 v[176:179], v87 offset:4096\n"
  "ds_read_b128 v[180:183], v88 offset:4096\n"
  "v_exp_f32 v16, v16\n"
  "v_mfma_f32_16x16x32_bf16 v[28:31], a[184:187], v[72:75], v[28:31]\n"
  "v_exp_f32 v17, v17\n"
  "v_mfma_f32_16x16x32_bf16 v[28:31], a[180:183], v[68:71], v[28:31]\n"
  "s_add_u32 m0, s59, 1920\n"
  "v_exp_f32 v18, v18\n"
  "v_mfma_f32_16x16x32_bf16 v[28:31], a[176:179], v[64:67], v[28:31]\n"
  "s_waitcnt lgkmcnt(3)\n"
  "buffer_load_dwordx4 v112, s[40:43], s57 offen offset:128 nt lds\n"
  "buffer_load_dwordx4 v113, s[40:43], s57 offen offset:1152 nt lds\n"
  "v_exp_f32 v19, v19\n"
  "v_mfma_f32_16x16x32_bf16 v[28:31], a[172:175], v[60:63], v[28:31]\n"
  "s_waitcnt vmcnt(14)\n"
  "ds_read_b128 v[200:203], v87 offset:6144\n"
  "ds_read_b128 v[204:207], v88 offset:6144\n"
  "v_cvt_pk_bf16_f32 v16, v16, v17\n"
  "v_cvt_pk_bf16_f32 v17, v18, v19\n"
  "v_mfma_f32_16x16x32_bf16 v[28:31], a[168:171], v[56:59], v[28:31]\n"
  "s_add_u32 m0, s59, 4096\n"
  "ds_read_b128 v[40:43], v81 offset:2048\n"
  "ds_read_b128 v[44:47], v82 offset:2048\n"
  "v_mfma_f32_16x16x32_bf16 v[0:3], a[192:195], v[56:59], v[92:95]\n"
  "s_waitcnt lgkmcnt(4)\n"
  "buffer_load_dwordx4 v112, s[40:43], s57 offen offset:0 nt lds\n"
  "buffer_load_dwordx4 v113, s[40:43], s57 offen offset:1024 nt lds\n"
  "s_add_u32 s57, s57, 6144\n"
  "v_exp_f32 v20, v20\n"
  "v_mfma_f32_16x16x32_bf16 v[0:3], a[196:199], v[60:63], v[0:3]\n"
  "s_waitcnt vmcnt(14)\n"
  "ds_read_b128 v[192:195], v87 offset:8192\n"
  "ds_read_b128 v[196:199], v88 offset:8192\n"
  "v_exp_f32 v21, v21\n"
  "v_mfma_f32_16x16x32_bf16 v[0:3], a[200:203], v[64:67], v[0:3]\n"
  "v_exp_f32 v22, v22\n"
  "v_mfma_f32_16x16x32_bf16 v[0:3], a[204:207], v[68:71], v[0:3]\n"
  "v_exp_f32 v23, v23\n"
  "v_mfma_f32_16x16x32_bf16 v[0:3], a[208:211], v[72:75], v[0:3]\n"
  "v_cvt_pk_bf16_f32 v18, v20, v21\n"
  "v_cvt_pk_bf16_f32 v19, v22, v23\n"
  "v_mfma_f32_16x16x32_bf16 v[0:3], a[212:215], v[76:79], v[0:3]\n"
  "s_waitcnt vmcnt(12)\n"
  "ds_read_b128 v[184:187], v87 offset:10240\n"
  "ds_read_b128 v[188:191], v88 offset:10240\n"
  "ds_write_b128 v80, v[16:19] offset:2048\n"
  "v_pk_mul_f32 v[24:25], v[24:25], s[60:61] op_sel_hi:[1,0]\n"
  "v_pk_mul_f32 v[26:27], v[26:27], s[60:61] op_sel_hi:[1,0]\n"
  "v_pk_mul_f32 v[28:29], v[28:29], s[60:61] op_sel_hi:[1,0]\n"
  "v_pk_mul_f32 v[30:31], v[30:31], s[60:61] op_sel_hi:[1,0]\n"
  "v_mfma_f32_16x16x32_bf16 v[4:7], a[236:239], v[76:79], v[92:95]\n"
  "v_exp_f32 v24, v24\n"
  "v_mfma_f32_16x16x32_bf16 v[4:7], a[232:235], v[72:75], v[4:7]\n"
  "v_exp_f32 v25, v25\n"
  "v_mfma_f32_16x16x32_bf16 v[4:7], a[228:231], v[68:71], v[4:7]\n"
  "v_exp_f32 v26, v26\n"
  "v_mfma_f32_16x16x32_bf16 v[4:7], a[224:227], v[64:67], v[4:7]\n"
  "s_waitcnt vmcnt(10)\n"
  "ds_read_b128 v[208:211], v87 offset:12288\n"
  "ds_read_b128 v[212:215], v88 offset:12288\n"
  "v_exp_f32 v27, v27\n"
  "v_mfma_f32_16x16x32_bf16 v[4:7], a[220:223], v[60:63], v[4:7]\n"
  "v_cvt_pk_bf16_f32 v24, v24, v25\n"
  "v_cvt_pk_bf16_f32 v25, v26, v27\n"
  "v_mfma_f32_16x16x32_bf16 v[4:7], a[216:219], v[56:59], v[4:7]\n"
  "ds_read_b128 v[48:51], v81 offset:4096\n"
  "ds_read_b128 v[52:55], v82 offset:4096\n"
  "v_mfma_f32_16x16x32_bf16 v[8:11], a[240:243], v[56:59], v[92:95]\n"
  "s_waitcnt vmcnt(8)\n"
  "ds_read_b128 v[216:219], v87 offset:14336\n"
  "ds_read_b128 v[220:223], v88 offset:14336\n"
  "v_exp_f32 v28, v28\n"
  "v_mfma_f32_16x16x32_bf16 v[8:11], a[244:247], v[60:63], v[8:11]\n"
  "v_exp_f32 v29, v29\n"
  "v_mfma_f32_16x16x32_bf16 v[8:11], a[248:251], v[64:67], v[8:11]\n"
  "v_exp_f32 v30, v30\n"
  "v_mfma_f32_16x16x32_bf16 v[8:11], a[252:255], v[68:71], v[8:11]\n"
  "v_exp_f32 v31, v31\n"
  "v_mfma_f32_16x16x32_bf16 v[8:11], v[128:131], v[72:75], v[8:11]\n"
  "s_waitcnt vmcnt(6)\n"
  "ds_read_b128 v[224:227], v87 offset:16384\n"
  "ds_read_b128 v[228:231], v88 offset:16384\n"
  "v_cvt_pk_bf16_f32 v26, v28, v29\n"
  "v_cvt_pk_bf16_f32 v27, v30, v31\n"
  "v_mfma_f32_16x16x32_bf16 v[8:11], v[132:135], v[76:79], v[8:11]\n"
  "ds_write_b128 v80, v[24:27] offset:3072\n"
  "v_pk_mul_f32 v[0:1], v[0:1], s[60:61] op_sel_hi:[1,0]\n"
  "v_pk_mul_f32 v[2:3], v[2:3], s[60:61] op_sel_hi:[1,0]\n"
  "v_pk_mul_f32 v[4:5], v[4:5], s[60:61] op_sel_hi:[1,0]\n"
  "v_pk_mul_f32 v[6:7], v[6:7], s[60:61] op_sel_hi:[1,0]\n"
  "v_mfma_f32_16x16x32_bf16 v[12:15], v[156:159], v[76:79], v[92:95]\n"
  "v_exp_f32 v0, v0\n"
  "v_mfma_f32_16x16x32_bf16 v[12:15], v[152:155], v[72:75], v[12:15]\n"
  "s_waitcnt vmcnt(4)\n"
  "ds_read_b128 v[248:251], v87 offset:0\n"
  "ds_read_b128 v[252:255], v88 offset:0\n"
  "v_exp_f32 v1, v1\n"
  "v_mfma_f32_16x16x32_bf16 v[12:15], v[148:151], v[68:71], v[12:15]\n"
  "v_exp_f32 v2, v2\n"
  "v_mfma_f32_16x16x32_bf16 v[12:15], v[144:147], v[64:67], v[12:15]\n"
  "v_exp_f32 v3, v3\n"
  "v_mfma_f32_16x16x32_bf16 v[12:15], v[140:143], v[60:63], v[12:15]\n"
  "v_cvt_pk_bf16_f32 v0, v0, v1\n"
  "v_cvt_pk_bf16_f32 v1, v2, v3\n"
  "v_mfma_f32_16x16x32_bf16 v[12:15], v[136:139], v[56:59], v[12:15]\n"
  "s_waitcnt vmcnt(2)\n"
  "ds_read_b128 v[240:243], v87 offset:2048\n"
  "ds_read_b128 v[244:247], v88 offset:2048\n"
  "v_mul_f32 v8, s60, v8\n"
  "v_mul_f32 v9, s60, v9\n"
  "v_mfma_f32_16x16x32_bf16 v[16:19], v[160:163], v[56:59], v[92:95]\n"
  "v_exp_f32 v4, v4\n"
  "v_mfma_f32_16x16x32_bf16 v[16:19], v[164:167], v[60:63], v[16:19]\n"
  "v_exp_f32 v5, v5\n"
  "v_mfma_f32_16x16x32_bf16 v[16:19], v[168:171], v[64:67], v[16:19]\n"
  "v_exp_f32 v6, v6\n"
  "v_mfma_f32_16x16x32_bf16 v[16:19], v[172:175], v[68:71], v[16:19]\n"
  "s_waitcnt vmcnt(0)\n"
  "ds_read_b128 v[232:235], v87 offset:4096\n"
  "ds_read_b128 v[236:239], v88 offset:4096\n"
  "v_exp_f32 v7, v7\n"
  "v_mfma_f32_16x16x32_bf16 v[16:19], v[176:179], v[72:75], v[16:19]\n"
  "v_cvt_pk_bf16_f32 v2, v4, v5\n"
  "v_cvt_pk_bf16_f32 v3, v6, v7\n"
  "v_mfma_f32_16x16x32_bf16 v[16:19], v[180:183], v[76:79], v[16:19]\n"
  "s_waitcnt lgkmcnt(15)\n"
  "ds_write_b128 v80, v[0:3] offset:4096\n"
  "v_pk_mul_f32 v[10:11], v[10:11], s[60:61] op_sel_hi:[1,0]\n"
  "v_pk_mul_f32 v[12:13], v[12:13], s[60:61] op_sel_hi:[1,0]\n"
  "v_pk_mul_f32 v[14:15], v[14:15], s[60:61] op_sel_hi:[1,0]\n"
  "v_mfma_f32_16x16x32_bf16 v[20:23], v[204:207], v[76:79], v[92:95]\n"
  "v_exp_f32 v8, v8\n"
  "v_mfma_f32_16x16x32_bf16 v[20:23], v[200:203], v[72:75], v[20:23]\n"
  "v_exp_f32 v9, v9\n"
  "v_mfma_f32_16x16x32_bf16 v[20:23], v[196:199], v[68:71], v[20:23]\n"
  "v_exp_f32 v10, v10\n"
  "v_mfma_f32_16x16x32_bf16 v[20:23], v[192:195], v[64:67], v[20:23]\n"
  "v_exp_f32 v11, v11\n"
  "v_mfma_f32_16x16x32_bf16 v[20:23], v[188:191], v[60:63], v[20:23]\n"
  "v_cvt_pk_bf16_f32 v8, v8, v9\n"
  "v_cvt_pk_bf16_f32 v9, v10, v11\n"
  "v_mfma_f32_16x16x32_bf16 v[20:23], v[184:187], v[56:59], v[20:23]\n"
  "s_waitcnt lgkmcnt(14)\n"
  "v_mul_f32 v16, s60, v16\n"
  "v_mul_f32 v17, s60, v17\n"
  "v_mfma_f32_16x16x32_bf16 v[24:27], v[208:211], v[56:59], v[92:95]\n"
  "v_exp_f32 v12, v12\n"
  "v_mfma_f32_16x16x32_bf16 v[24:27], v[212:215], v[60:63], v[24:27]\n"
  "s_waitcnt lgkmcnt(10)\n"
  "v_exp_f32 v13, v13\n"
  "v_mfma_f32_16x16x32_bf16 v[24:27], v[216:219], v[64:67], v[24:27]\n"
  "v_exp_f32 v14, v14\n"
  "v_mfma_f32_16x16x32_bf16 v[24:27], v[220:223], v[68:71], v[24:27]\n"
  "s_waitcnt lgkmcnt(8)\n"
  "v_exp_f32 v15, v15\n"
  "v_mfma_f32_16x16x32_bf16 v[24:27], v[224:227], v[72:75], v[24:27]\n"
  "v_cvt_pk_bf16_f32 v10, v12, v13\n"
  "v_cvt_pk_bf16_f32 v11, v14, v15\n"
  "v_mfma_f32_16x16x32_bf16 v[24:27], v[228:231], v[76:79], v[24:27]\n"
  "s_waitcnt lgkmcnt(5)\n"
  "ds_write_b128 v80, v[8:11] offset:5120\n"
  "v_pk_mul_f32 v[18:19], v[18:19], s[60:61] op_sel_hi:[1,0]\n"
  "v_pk_mul_f32 v[20:21], v[20:21], s[60:61] op_sel_hi:[1,0]\n"
  "v_pk_mul_f32 v[22:23], v[22:23], s[60:61] op_sel_hi:[1,0]\n"
  "v_mfma_f32_16x16x32_bf16 v[28:31], v[252:255], v[76:79], v[92:95]\n"
  "v_exp_f32 v16, v16\n"
  "v_mfma_f32_16x16x32_bf16 v[28:31], v[248:251], v[72:75], v[28:31]\n"
  "s_waitcnt lgkmcnt(4)\n"
  "v_exp_f32 v17, v17\n"
  "v_mfma_f32_16x16x32_bf16 v[28:31], v[244:247], v[68:71], v[28:31]\n"
  "v_exp_f32 v18, v18\n"
  "v_mfma_f32_16x16x32_bf16 v[28:31], v[240:243], v[64:67], v[28:31]\n"
  "s_waitcnt lgkmcnt(2)\n"
  "s_sub_u32 s56, s56, 1\n"
  "v_exp_f32 v19, v19\n"
  "v_mfma_f32_16x16x32_bf16 v[28:31], v[236:239], v[60:63], v[28:31]\n"
  "v_cvt_pk_bf16_f32 v16, v16, v17\n"
  "v_cvt_pk_bf16_f32 v17, v18, v19\n"
  "v_mfma_f32_16x16x32_bf16 v[28:31], v[232:235], v[56:59], v[28:31]\n"
  "s_branch A_H0_%=\n"
  "A_LAST_%=:\n"
  "v_exp_f32 v20, v20\n"
  "v_exp_f32 v21, v21\n"
  "v_exp_f32 v22, v22\n"
  "v_exp_f32 v23, v23\n"
  "v_pk_mul_f32 v[24:25], v[24:25], s[60:61] op_sel_hi:[1,0]\n"
  "v_pk_mul_f32 v[26:27], v[26:27], s[60:61] op_sel_hi:[1,0]\n"
  "v_cvt_pk_bf16_f32 v18, v20, v21\n"
  "v_cvt_pk_bf16_f32 v19, v22, v23\n"
  "ds_write_b128 v80, v[16:19] offset:6144\n"
  "v_pk_mul_f32 v[28:29], v[28:29], s[60:61] op_sel_hi:[1,0]\n"
  "v_pk_mul_f32 v[30:31], v[30:31], s[60:61] op_sel_hi:[1,0]\n"
  "v_exp_f32 v24, v24\n"
  "v_exp_f32 v25, v25\n"
  "v_exp_f32 v26, v26\n"
  "v_exp_f32 v27, v27\n"
  "v_exp_f32 v28, v28\n"
  "v_exp_f32 v29, v29\n"
  "v_cvt_pk_bf16_f32 v24, v24, v25\n"
  "v_cvt_pk_bf16_f32 v25, v26, v27\n"
  "v_exp_f32 v30, v30\n"
  "v_exp_f32 v31, v31\n"
  "v_cvt_pk_bf16_f32 v26, v28, v29\n"
  "v_cvt_pk_bf16_f32 v27, v30, v31\n"
  "ds_write_b128 v80, v[24:27] offset:7168\n"
  "s_waitcnt lgkmcnt(0)\n"
  "s_barrier\n"
  "s_mov_b32 %0, 0\n"
  "s_branch L_END_%=\n"
  "B_START_%=:\n"
  "v_add_u32 v113, 0x800, v112\n"
  "s_mov_b32 s43, 0x20000\n"
  "s_sub_u32 s62, 53, s75\n"
  "s_bitcmp1_b32 %3, 3\n"
  "s_cselect_b32 s52, s62, s75\n"
  "s_cselect_b32 s54, s75, s62\n"
  "s_cselect_b32 s62, s75, 0\n"
  "s_lshl_b32 s62, s62, 8\n"
  "s_or_b32 s52, s52, s62\n"
  "s_pack_ll_b32_b16 s52, s52, s86\n"
  "s_add_u32 s62, %3, 128\n"
  "s_pack_ll_b32_b16 s53, 53, s62\n"
  "s_bitcmp1_b32 %3, 0\n"
  "s_cselect_b32 s62, -2, 2\n"
  "s_add_u32 s54, s54, s62\n"
  "s_sub_u32 s62, 53, s54\n"
  "s_lshl_b32 s62, s62, 8\n"
  "s_bfe_u32 s63, %3, 0x10003\n"
  "s_cselect_b32 s62, s62, 0\n"
  "s_or_b32 s54, s54, s62\n"
  "s_xor_b32 s63, s63, s86\n"
  "s_add_u32 s63, s63, 384\n"
  "s_pack_ll_b32_b16 s54, s54, s63\n"
  "s_mov_b32 s55, 0\n"
  "s_sub_u32 s40, s80, 2048\n"
  "s_subb_u32 s41, s81, 0\n"
  "s_mov_b32 s42, 100665344\n"
  "s_mov_b64 s[48:49], s[82:83]\n"
  "s_mov_b32 s50, 166723584\n"
  "s_mov_b32 s51, 0x20000\n"
  "s_lshr_b32 s62, s52, 16\n"
  "s_lshl1_add_u32 s62, s62, s65\n"
  "s_mul_i32 s57, s62, 98304\n"
  "s_and_b32 s56, s52, 0xff\n"
  "s_cmp_lg_u32 s53, 0\n"
  "s_cselect_b32 s62, 6, 0\n"
  "s_sub_u32 s56, s56, s62\n"
  "s_mov_b32 s68, 0xf0000000\n"
  "s_mov_b32 s69, 0\n"
  "s_sub_u32 s70, s67, s68\n"
  "s_mov_b32 s64, s67\n"
  "s_and_b32 s90, s52, 0xff\n"
  "s_sub_u32 s90, s90, 1\n"
  "s_mul_i32 s91, s90, 6144\n"
  "s_add_u32 s91, s91, s64\n"
  "s_lshr_b32 s62, s53, 16\n"
  "s_mul_i32 s62, s62, 53\n"
  "s_bfe_u32 s63, s53, 0x80008\n"
  "s_add_u32 s62, s62, s63\n"
  "s_mul_i32 s64, s62, 6144\n"
  "s_cmp_eq_u32 s53, 0\n"
  "s_cmov_b32 s64, 0xf0000000\n"
  "s_sub_u32 s91, s64, s91\n"
  "s_mov_b32 s96, s91\n"
  "s_and_b32 s92, s53, 0xff\n"
  "s_sub_u32 s92, s92, 1\n"
  "s_mul_i32 s93, s92, 6144\n"
  "s_add_u32 s93, s93, s64\n"
  "s_lshr_b32 s62, s54, 16\n"
  "s_mul_i32 s62, s62, 53\n"
  "s_bfe_u32 s63, s54, 0x80008\n"
  "s_add_u32 s62, s62, s63\n"
  "s_mul_i32 s64, s62, 6144\n"
  "s_cmp_eq_u32 s54, 0\n"
  "s_cmov_b32 s64, 0xf0000000\n"
  "s_sub_u32 s93, s64, s93\n"
  "s_mov_b32 s97, s93\n"
  "s_and_b32 s94, s54, 0xff\n"
  "s_sub_u32 s94, s94, 1\n"
  "s_mul_i32 s95, s94, 6144\n"
  "s_add_u32 s95, s95, s64\n"
  "s_lshr_b32 s62, s55, 16\n"
  "s_mul_i32 s62, s62, 53\n"
  "s_bfe_u32 s63, s55, 0x80008\n"
  "s_add_u32 s62, s62, s63\n"
  "s_mul_i32 s64, s62, 6144\n"
  "s_cmp_eq_u32 s55, 0\n"
  "s_cmov_b32 s64, 0xf0000000\n"
  "s_sub_u32 s95, s64, s95\n"
  "s_add_u32 s66, s66, 6144\n"
  "s_cmp_ge_u32 s66, 86016\n"
  "s_cselect_b32 s64, 36864, 0\n"
  "s_sub_u32 s66, s66, s64\n"
  "s_sub_u32 s90, s90, 1\n"
  "s_cselect_b32 s63, s91, 6144\n"
  "s_cmov_b64 s[90:91], s[92:93]\n"
  "s_cmov_b64 s[92:93], s[94:95]\n"
  "s_add_u32 s67, s67, s63\n"
  "s_add_u32 s66, s66, 6144\n"
  "s_cmp_ge_u32 s66, 86016\n"
  "s_cselect_b32 s64, 36864, 0\n"
  "s_sub_u32 s66, s66, s64\n"
  "s_sub_u32 s90, s90, 1\n"
  "s_cselect_b32 s63, s91, 6144\n"
  "s_cmov_b64 s[90:91], s[92:93]\n"
  "s_cmov_b64 s[92:93], s[94:95]\n"
  "s_add_u32 s67, s67, s63\n"
  "v_lshrrev_b32 v1, 3, v0\n"
  "v_mul_u32_u24 v114, 0x180, v1\n"
  "v_lshrrev_b32 v1, 4, v0\n"
  "v_and_b32 v2, 7, v0\n"
  "v_xor_b32 v2, v2, v1\n"
  "v_lshl_add_u32 v114, v2, 4, v114\n"
  "s_lshl_b32 s62, s65, 8\n"
  "s_add_u32 s62, s62, 2048\n"
  "v_add_u32 v114, s62, v114\n"
  "v_add_u32 v115, 0x800, v114\n"
  "v_lshrrev_b32 v2, 2, v0\n"
  "v_mul_u32_u24 v116, 0x180, v2\n"
  "v_and_b32 v1, 1, v1\n"
  "v_and_b32 v2, 3, v0\n"
  "v_xor_b32 v2, v2, v1\n"
  "v_lshl_add_u32 v116, v2, 4, v116\n"
  "s_lshl_b32 s62, s65, 6\n"
  "s_add_u32 s62, s62, 128\n"
  "v_add_u32 v116, s62, v116\n"
  "s_mul_i32 s85, s65, 86016\n"
  "s_sub_u32 s57, s57, s85\n"
  "s_add_u32 s85, s85, 12288\n"
  "s_mul_i32 s84, s65, 24576\n"
  "s_sub_u32 s58, 6144, s84\n"
  "s_add_u32 s84, s84, 6144\n"
  "s_cmp_eq_u32 s65, 0\n"
  "s_cselect_b64 s[80:81], s[78:79], s[80:81]\n"
  "s_mov_b32 s82, 100663296\n"
  "s_mov_b32 s83, 0x20000\n"
  "s_mov_b64 s[76:77], s[78:79]\n"
  "s_mov_b32 s78, 100663296\n"
  "s_mov_b32 s79, 0x20000\n"
  "v_lshrrev_b32 v1, 2, v0\n"
  "v_lshlrev_b32 v117, 7, v1\n"
  "v_lshlrev_b32 v121, 6, v1\n"
  "v_and_b32 v2, 1, v0\n"
  "v_lshl_add_u32 v117, v2, 3, v117\n"
  "v_lshl_add_u32 v121, v2, 3, v121\n"
  "v_bfe_u32 v2, v0, 1, 1\n"
  "v_lshl_add_u32 v117, v2, 6, v117\n"
  "v_lshl_add_u32 v121, v2, 5, v121\n"
  "v_bfe_u32 v2, v0, 3, 2\n"
  "v_lshl_add_u32 v117, v2, 4, v117\n"
  "v_bfe_u32 v2, v0, 4, 1\n"
  "v_lshl_add_u32 v121, v2, 4, v121\n"
  "v_add_u32 v117, s59, v117\n"
  "v_add_u32 v121, s59, v121\n"
  "v_xor_b32 v118, 16, v117\n"
  "v_xor_b32 v119, 32, v117\n"
  "v_xor_b32 v120, 48, v117\n"
  "v_xor_b32 v122, 16, v121\n"
  "s_lshl_b32 s62, s65, 10\n"
  "v_lshlrev_b32 v104, 4, v0\n"
  "v_add_u32 v104, s62, v104\n"
  "v_xor_b32 v105, 0x400, v104\n"
  "v_lshrrev_b32 v1, 5, v0\n"
  "v_mul_u32_u24 v1, 0x480, v1\n"
  "v_and_b32 v2, 31, v0\n"
  "v_lshl_add_u32 v106, v2, 3, v1\n"
  "s_mul_i32 s62, s65, 4608\n"
  "s_add_u32 s62, s62, 32768\n"
  "v_add_u32 v106, s62, v106\n"
  "v_and_b32 v2, 7, v0\n"
  "v_lshl_add_u32 v107, v2, 3, v1\n"
  "v_bfe_u32 v2, v0, 3, 1\n"
  "v_lshl_add_u32 v107, v2, 2, v107\n"
  "v_bfe_u32 v2, v0, 4, 1\n"
  "v_lshl_add_u32 v107, v2, 7, v107\n"
  "s_lshl_b32 s62, s65, 6\n"
  "s_add_u32 s62, s62, 35072\n"
  "v_add_u32 v107, s62, v107\n"
  "v_lshrrev_b32 v1, 4, v0\n"
  "v_mul_u32_u24 v108, 0x120, v1\n"
  "v_and_b32 v2, 3, v0\n"
  "v_lshl_add_u32 v108, v2, 6, v108\n"
  "v_bfe_u32 v2, v0, 2, 2\n"
  "v_lshl_add_u32 v108, v2, 3, v108\n"
  "s_mul_i32 s62, s65, 1152\n"
  "s_add_u32 s62, s62, 40960\n"
  "v_add_u32 v108, s62, v108\n"
  "v_bfe_u32 v2, v0, 3, 1\n"
  "v_lshl_add_u32 v2, v2, 2, v1\n"
  "v_mul_u32_u24 v109, 0x180, v2\n"
  "v_and_b32 v2, 7, v0\n"
  "v_lshl_add_u32 v109, v2, 4, v109\n"
  "s_mul_i32 s62, s65, 3072\n"
  "v_add_u32 v109, s62, v109\n"
  "s_mov_b32 s62, 47872\n"
  "v_lshl_add_u32 v111, v1, 4, s62\n"
  "s_lshl_b32 s62, s65, 6\n"
  "s_add_u32 s62, s62, 39680\n"
  "v_lshl_add_u32 v110, v0, 2, s62\n"
  "v_mov_b32 v3, 0x40000\n"
  "v_cmp_gt_u32 vcc, 16, v0\n"
  "s_nop 1\n"
  "v_cndmask_b32 v110, v3, v110, vcc\n"
  "v_mov_b32 v44, 1.0\n"
  "ds_write_b32 v110, v44 offset:8192\n"
  "v_and_b32 v2, 15, v0\n"
  "v_mov_b32 v124, 0x44444444\n"
  "v_mov_b32 v3, 0xeeeeeeee\n"
  "v_cmp_eq_u32 vcc, 1, v2\n"
  "s_nop 1\n"
  "v_cndmask_b32 v124, v124, v3, vcc\n"
  "v_mov_b32 v3, 0x3f803f80\n"
  "v_cmp_gt_u32 vcc, 2, v2\n"
  "v_mov_b32 v80, 0\n"
  "v_mov_b32 v81, 0\n"
  "v_mov_b32 v82, 0\n"
  "v_mov_b32 v83, 0\n"
  "v_cndmask_b32 v88, 0, v3, vcc\n"
  "v_cndmask_b32 v89, 0, v3, vcc\n"
  "v_cndmask_b32 v90, 0, v3, vcc\n"
  "v_cndmask_b32 v91, 0, v3, vcc\n"
  "s_mov_b32 s99, 0\n"
  "s_waitcnt vmcnt(0)\n"
  "s_barrier\n"
  "v_lshlrev_b32 v125, 7, v0\n"
  "v_mov_b32 v126, 0\n"
  "s_mov_b32 s62, 0x20d00000\n"
  "v_writelane_b32 v126, s62, 2\n"
  "s_mov_b32 s62, 0x31142912\n"
  "v_writelane_b32 v126, s62, 3\n"
  "s_mov_b32 s62, 0x3956\n"
  "v_writelane_b32 v126, s62, 4\n"
  "s_mov_b32 s62, 0x4a1a41d8\n"
  "v_writelane_b32 v126, s62, 5\n"
  "s_mov_b32 s62, 0x5a5e521c\n"
  "v_writelane_b32 v126, s62, 6\n"
  "s_mov_b32 s62, 0x62e00000\n"
  "v_writelane_b32 v126, s62, 7\n"
  "s_mov_b32 s62, 0x73246b22\n"
  "v_writelane_b32 v126, s62, 8\n"
  "s_mov_b32 s62, 0x7b66\n"
  "v_writelane_b32 v126, s62, 9\n"
  "s_mov_b32 s62, 0x942a8be8\n"
  "v_writelane_b32 v126, s62, 10\n"
  "s_mov_b32 s62, 0xa4ae9c6c\n"
  "v_writelane_b32 v126, s62, 11\n"
  "s_mov_b32 s62, 0xad2da4ef\n"
  "v_writelane_b32 v126, s62, 12\n"
  "s_mov_b32 s62, 0xbd69b52b\n"
  "v_writelane_b32 v126, s62, 13\n"
  "s_mov_b32 s62, 0xc5e70000\n"
  "v_writelane_b32 v126, s62, 14\n"
  "s_mov_b32 s62, 0xd623ce25\n"
  "v_writelane_b32 v126, s62, 15\n"
  "s_mov_b32 s62, 0xde61\n"
  "v_writelane_b32 v126, s62, 16\n"
  "s_mov_b32 s62, 0xef1de6df\n"
  "v_writelane_b32 v126, s62, 17\n"
  "s_mov_b32 s62, 0xff59f71b\n"
  "v_writelane_b32 v126, s62, 18\n"
  "s_mov_b32 s62, 0x7d70000\n"
  "v_writelane_b32 v126, s62, 19\n"
  "s_mov_b32 s62, 0x130015\n"
  "v_writelane_b32 v126, s62, 20\n"
  "s_mov_b32 s62, 0x11\n"
  "v_writelane_b32 v126, s62, 21\n"
  "s_mov_b32 s62, 0x20f00000\n"
  "v_writelane_b32 v126, s62, 29\n"
  "s_mov_b32 s62, 0x31322931\n"
  "v_writelane_b32 v126, s62, 30\n"
  "s_mov_b32 s62, 0x3973\n"
  "v_writelane_b32 v126, s62, 31\n"
  "s_mov_b32 s62, 0x4a3541f4\n"
  "v_writelane_b32 v126, s62, 32\n"
  "s_mov_b32 s62, 0x5a775236\n"
  "v_writelane_b32 v126, s62, 33\n"
  "s_mov_b32 s62, 0x62f80000\n"
  "v_writelane_b32 v126, s62, 34\n"
  "s_mov_b32 s62, 0x733a6b39\n"
  "v_writelane_b32 v126, s62, 35\n"
  "s_mov_b32 s62, 0x7b7b\n"
  "v_writelane_b32 v126, s62, 36\n"
  "s_mov_b32 s62, 0x943d8bfc\n"
  "v_writelane_b32 v126, s62, 37\n"
  "s_mov_b32 s62, 0xa4bf9c7e\n"
  "v_writelane_b32 v126, s62, 38\n"
  "s_mov_b32 s62, 0xad0ea4cf\n"
  "v_writelane_b32 v126, s62, 39\n"
  "s_mov_b32 s62, 0xbd4cb50d\n"
  "v_writelane_b32 v126, s62, 40\n"
  "s_mov_b32 s62, 0xc5cb0000\n"
  "v_writelane_b32 v126, s62, 41\n"
  "s_mov_b32 s62, 0xd609ce0a\n"
  "v_writelane_b32 v126, s62, 42\n"
  "s_mov_b32 s62, 0xde48\n"
  "v_writelane_b32 v126, s62, 43\n"
  "s_mov_b32 s62, 0xef06e6c7\n"
  "v_writelane_b32 v126, s62, 44\n"
  "s_mov_b32 s62, 0xff44f705\n"
  "v_writelane_b32 v126, s62, 45\n"
  "s_mov_b32 s62, 0x7c30000\n"
  "v_writelane_b32 v126, s62, 46\n"
  "s_mov_b32 s62, 0x10002\n"
  "v_writelane_b32 v126, s62, 47\n"
  "s_mov_b32 s62, 0x800\n"
  "v_writelane_b32 v126, s62, 48\n"
  "s_sub_u32 s62, 31, s87\n"
  "s_bitcmp1_b32 %3, 3\n"
  "s_cselect_b32 s62, s62, s87\n"
  "s_and_b32 s63, %3, 7\n"
  "s_mul_i32 s63, s63, 24\n"
  "v_mov_b32 v3, 0xffff\n"
  "s_mul_i32 s64, s65, 5\n"
  "v_mov_b32 v6, s63\n"
  "s_and_b32 s63, %3, 7\n"
  "s_xor_b32 s63, s63, 1\n"
  "s_mul_i32 s63, s63, 24\n"
  "s_add_u32 s63, s63, 9216\n"
  "v_mov_b32 v7, s63\n"
  "s_add_u32 s64, s64, 6\n"
  "v_bfe_u32 v2, v126, 0, 6\n"
  "v_mul_u32_u24 v4, 192, v2\n"
  "v_lshrrev_b32 v1, s64, v126\n"
  "v_cmp_gt_u32 vcc, 16, v2\n"
  "s_nop 1\n"
  "v_cndmask_b32 v8, v6, v7, vcc\n"
  "v_bfe_u32 v2, v126, 0, 16\n"
  "v_sub_u32 v1, s62, v1\n"
  "v_and_b32 v1, 31, v1\n"
  "v_cmp_gt_u32 vcc, 24, v1\n"
  "v_add3_u32 v4, v4, v1, v8\n"
  "s_nop 0\n"
  "v_cndmask_b32 v4, v3, v4, vcc\n"
  "v_cmp_ne_u32 vcc, 0, v2\n"
  "s_nop 1\n"
  "v_cndmask_b32 v4, v3, v4, vcc\n"
  "s_add_u32 s64, s64, 16\n"
  "v_bfe_u32 v2, v126, 16, 6\n"
  "v_mul_u32_u24 v5, 192, v2\n"
  "v_lshrrev_b32 v1, s64, v126\n"
  "v_cmp_gt_u32 vcc, 16, v2\n"
  "s_nop 1\n"
  "v_cndmask_b32 v8, v6, v7, vcc\n"
  "v_bfe_u32 v2, v126, 16, 16\n"
  "v_sub_u32 v1, s62, v1\n"
  "v_and_b32 v1, 31, v1\n"
  "v_cmp_gt_u32 vcc, 24, v1\n"
  "v_add3_u32 v5, v5, v1, v8\n"
  "s_nop 0\n"
  "v_cndmask_b32 v5, v3, v5, vcc\n"
  "v_cmp_ne_u32 vcc, 0, v2\n"
  "s_nop 1\n"
  "v_cndmask_b32 v5, v3, v5, vcc\n"
  "v_lshl_or_b32 v126, v5, 16, v4\n"
  "s_mul_i32 s72, s65, -2\n"
  "s_add_u32 s72, s72, 1\n"
  "s_mov_b32 s71, 0x53800000\n"
  "s_sub_u32 s60, s59, 36864\n"
  "s_barrier\n"
  "s_add_u32 m0, s66, 0\n"
  "s_nop 0\n"
  "buffer_load_dwordx4 v112, s[44:47], s67 offen offset:0 nt lds\n"
  "s_add_u32 m0, s66, 1920\n"
  "s_nop 0\n"
  "buffer_load_dwordx4 v112, s[44:47], s67 offen offset:128 nt lds\n"
  "s_add_u32 m0, s66, 3840\n"
  "s_nop 0\n"
  "buffer_load_dwordx4 v112, s[44:47], s67 offen offset:256 nt lds\n"
  "s_add_u32 s66, s66, 6144\n"
  "s_cmp_ge_u32 s66, 86016\n"
  "s_cselect_b32 s64, 36864, 0\n"
  "s_sub_u32 s66, s66, s64\n"
  "s_sub_u32 s90, s90, 1\n"
  "s_cselect_b32 s63, s91, 6144\n"
  "s_cmov_b64 s[90:91], s[92:93]\n"
  "s_cmov_b64 s[92:93], s[94:95]\n"
  "s_add_u32 s67, s67, s63\n"
  "s_add_u32 m0, s66, 0\n"
  "s_nop 0\n"
  "buffer_load_dwordx4 v112, s[44:47], s67 offen offset:0 nt lds\n"
  "s_add_u32 m0, s66, 1920\n"
  "s_nop 0\n"
  "buffer_load_dwordx4 v112, s[44:47], s67 offen offset:128 nt lds\n"
  "s_add_u32 m0, s66, 3840\n"
  "s_nop 0\n"
  "buffer_load_dwordx4 v112, s[44:47], s67 offen offset:256 nt lds\n"
  "s_add_u32 s66, s66, 6144\n"
  "s_cmp_ge_u32 s66, 86016\n"
  "s_cselect_b32 s64, 36864, 0\n"
  "s_sub_u32 s66, s66, s64\n"
  "s_sub_u32 s90, s90, 1\n"
  "s_cselect_b32 s63, s91, 6144\n"
  "s_cmov_b64 s[90:91], s[92:93]\n"
  "s_cmov_b64 s[92:93], s[94:95]\n"
  "s_add_u32 s67, s67, s63\n"
  "s_add_u32 m0, s59, 0\n"
  "s_nop 0\n"
  "buffer_load_dwordx4 v114, s[40:43], s57 offen offset:0  lds\n"
  "buffer_load_dwordx4 v115, s[40:43], s57 offen offset:1024  lds\n"
  "buffer_load_dwordx4 v116, s[40:43], s57 offen offset:2048  lds\n"
  "s_add_u32 s57, s57, 6144\n"
  "s_add_u32 m0, s59, 3072\n"
  "s_nop 0\n"
  "buffer_load_dwordx4 v114, s[40:43], s57 offen offset:0  lds\n"
  "buffer_load_dwordx4 v115, s[40:43], s57 offen offset:1024  lds\n"
  "buffer_load_dwordx4 v116, s[40:43], s57 offen offset:2048  lds\n"
  "s_add_u32 s57, s57, s58\n"
  "s_add_u32 m0, s59, 6144\n"
  "s_nop 0\n"
  "buffer_load_dwordx4 v114, s[40:43], s57 offen offset:0  lds\n"
  "buffer_load_dwordx4 v115, s[40:43], s57 offen offset:1024  lds\n"
  "buffer_load_dwordx4 v116, s[40:43], s57 offen offset:2048  lds\n"
  "s_add_u32 s57, s57, 6144\n"
  "s_add_u32 m0, s59, 9216\n"
  "s_nop 0\n"
  "buffer_load_dwordx4 v114, s[40:43], s57 offen offset:0  lds\n"
  "buffer_load_dwordx4 v115, s[40:43], s57 offen offset:1024  lds\n"
  "buffer_load_dwordx4 v116, s[40:43], s57 offen offset:2048  lds\n"
  "s_add_u32 s57, s57, s84\n"
  "s_add_u32 m0, s59, 12288\n"
  "s_nop 0\n"
  "buffer_load_dwordx4 v114, s[40:43], s57 offen offset:0  lds\n"
  "buffer_load_dwordx4 v115, s[40:43], s57 offen offset:1024  lds\n"
  "buffer_load_dwordx4 v116, s[40:43], s57 offen offset:2048  lds\n"
  "s_add_u32 s57, s57, 6144\n"
  "s_add_u32 m0, s59, 15360\n"
  "s_nop 0\n"
  "buffer_load_dwordx4 v114, s[40:43], s57 offen offset:0  lds\n"
  "buffer_load_dwordx4 v115, s[40:43], s57 offen offset:1024  lds\n"
  "buffer_load_dwordx4 v116, s[40:43], s57 offen offset:2048  lds\n"
  "s_add_u32 s57, s57, s58\n"
  "s_waitcnt vmcnt(15)\n"
  "ds_read_b64 v[32:33], v117 offset:0\n"
  "ds_read_b64 v[34:35], v118 offset:0\n"
  "s_waitcnt lgkmcnt(0)\n"
  "v_max3_f32 v32, |v32|, |v33|, |v34|\n"
  "v_max3_f32 v32, v32, |v35|, 0\n"
  "v_max_f32 v32, 0, v32\n"
  "v_mul_f32 v33, 0x40000000, v32\n"
  "v_mul_f32 v32, 0x2b800000, v32\n"
  "v_max_f32 v82, v82, v32\n"
  "s_nop 0\n"
  "v_readlane_b32 s98, v33, 0\n"
  "v_readlane_b32 s62, v33, 21\n"
  "v_readlane_b32 s63, v33, 42\n"
  "v_readlane_b32 s64, v33, 63\n"
  "s_nop 1\n"
  "s_max_u32 s98, s98, s62\n"
  "s_max_u32 s63, s63, s64\n"
  "s_max_u32 s98, s98, s63\n"
  "s_and_b32 s98, s98, 0x7f800000\n"
  "s_cselect_b32 s62, 0x400000, 0\n"
  "s_or_b32 s98, s98, s62\n"
  "s_nop 0\n"
  "v_mov_b32 v100, s98\n"
  "v_mov_b32 v101, s98\n"
  "v_mov_b32 v102, s98\n"
  "v_mov_b32 v103, s98\n"
  "ds_read_b64_tr_b16 a[0:1], v117 offset:0\n"
  "ds_read_b64_tr_b16 a[4:5], v118 offset:0\n"
  "ds_read_b64_tr_b16 a[8:9], v119 offset:0\n"
  "ds_read_b64_tr_b16 a[12:13], v120 offset:0\n"
  "ds_read_b64_tr_b16 a[16:17], v121 offset:2048\n"
  "ds_read_b64_tr_b16 a[20:21], v122 offset:2048\n"
  "s_waitcnt vmcnt(12)\n"
  "ds_read_b64_tr_b16 a[2:3], v117 offset:3072\n"
  "ds_read_b64_tr_b16 a[6:7], v118 offset:3072\n"
  "s_add_u32 m0, s59, 0\n"
  "ds_read_b64_tr_b16 a[10:11], v119 offset:3072\n"
  "ds_read_b64_tr_b16 a[14:15], v120 offset:3072\n"
  "s_waitcnt lgkmcnt(4)\n"
  "buffer_load_dwordx4 v114, s[40:43], s57 offen offset:0  lds\n"
  "buffer_load_dwordx4 v115, s[40:43], s57 offen offset:1024  lds\n"
  "ds_read_b64_tr_b16 a[18:19], v121 offset:5120\n"
  "ds_read_b64_tr_b16 a[22:23], v122 offset:5120\n"
  "s_waitcnt vmcnt(11)\n"
  "ds_read_b64_tr_b16 a[24:25], v117 offset:6144\n"
  "ds_read_b64_tr_b16 a[28:29], v118 offset:6144\n"
  "buffer_load_dwordx4 v116, s[40:43], s57 offen offset:2048  lds\n"
  "s_add_u32 s57, s57, 6144\n"
  "s_add_u32 m0, s59, 3072\n"
  "ds_read_b64_tr_b16 a[32:33], v119 offset:6144\n"
  "ds_read_b64_tr_b16 a[36:37], v120 offset:6144\n"
  "s_waitcnt lgkmcnt(4)\n"
  "buffer_load_dwordx4 v114, s[40:43], s57 offen offset:0  lds\n"
  "buffer_load_dwordx4 v115, s[40:43], s57 offen offset:1024  lds\n"
  "ds_read_b64_tr_b16 a[40:41], v121 offset:8192\n"
  "ds_read_b64_tr_b16 a[44:45], v122 offset:8192\n"
  "s_waitcnt vmcnt(11)\n"
  "ds_read_b64_tr_b16 a[26:27], v117 offset:9216\n"
  "ds_read_b64_tr_b16 a[30:31], v118 offset:9216\n"
  "buffer_load_dwordx4 v116, s[40:43], s57 offen offset:2048  lds\n"
  "s_add_u32 s57, s57, s84\n"
  "s_add_u32 m0, s59, 6144\n"
  "ds_read_b64_tr_b16 a[34:35], v119 offset:9216\n"
  "ds_read_b64_tr_b16 a[38:39], v120 offset:9216\n"
  "s_waitcnt lgkmcnt(4)\n"
  "buffer_load_dwordx4 v114, s[40:43], s57 offen offset:0  lds\n"
  "buffer_load_dwordx4 v115, s[40:43], s57 offen offset:1024  lds\n"
  "ds_read_b64_tr_b16 a[42:43], v121 offset:11264\n"
  "ds_read_b64_tr_b16 a[46:47], v122 offset:11264\n"
  "s_waitcnt vmcnt(11)\n"
  "ds_read_b64_tr_b16 a[48:49], v117 offset:12288\n"
  "ds_read_b64_tr_b16 a[52:53], v118 offset:12288\n"
  "buffer_load_dwordx4 v116, s[40:43], s57 offen offset:2048  lds\n"
  "s_add_u32 s57, s57, 6144\n"
  "s_add_u32 m0, s59, 9216\n"
  "ds_read_b64_tr_b16 a[56:57], v119 offset:12288\n"
  "ds_read_b64_tr_b16 a[60:61], v120 offset:12288\n"
  "s_waitcnt lgkmcnt(4)\n"
  "buffer_load_dwordx4 v114, s[40:43], s57 offen offset:0  lds\n"
  "buffer_load_dwordx4 v115, s[40:43], s57 offen offset:1024  lds\n"
  "ds_read_b64_tr_b16 a[64:65], v121 offset:14336\n"
  "ds_read_b64_tr_b16 a[68:69], v122 offset:14336\n"
  "s_waitcnt vmcnt(11)\n"
  "ds_read_b64_tr_b16 a[50:51], v117 offset:15360\n"
  "ds_read_b64_tr_b16 a[54:55], v118 offset:15360\n"
  "buffer_load_dwordx4 v116, s[40:43], s57 offen offset:2048  lds\n"
  "s_add_u32 s57, s57, s58\n"
  "s_add_u32 m0, s59, 12288\n"
  "ds_read_b64_tr_b16 a[58:59], v119 offset:15360\n"
  "ds_read_b64_tr_b16 a[62:63], v120 offset:15360\n"
  "s_waitcnt lgkmcnt(4)\n"
  "buffer_load_dwordx4 v114, s[40:43], s57 offen offset:0  lds\n"
  "buffer_load_dwordx4 v115, s[40:43], s57 offen offset:1024  lds\n"
  "ds_read_b64_tr_b16 a[66:67], v121 offset:17408\n"
  "ds_read_b64_tr_b16 a[70:71], v122 offset:17408\n"
  "s_waitcnt vmcnt(11)\n"
  "ds_read_b64_tr_b16 a[72:73], v117 offset:0\n"
  "ds_read_b64_tr_b16 a[76:77], v118 offset:0\n"
  "buffer_load_dwordx4 v116, s[40:43], s57 offen offset:2048  lds\n"
  "s_add_u32 s57, s57, 6144\n"
  "s_add_u32 m0, s59, 15360\n"
  "ds_read_b64_tr_b16 a[80:81], v119 offset:0\n"
  "ds_read_b64_tr_b16 a[84:85], v120 offset:0\n"
  "s_waitcnt lgkmcnt(4)\n"
  "buffer_load_dwordx4 v114, s[40:43], s57 offen offset:0  lds\n"
  "buffer_load_dwordx4 v115, s[40:43], s57 offen offset:1024  lds\n"
  "ds_read_b64_tr_b16 a[88:89], v121 offset:2048\n"
  "ds_read_b64_tr_b16 a[92:93], v122 offset:2048\n"
  "s_waitcnt vmcnt(11)\n"
  "ds_read_b64_tr_b16 a[74:75], v117 offset:3072\n"
  "ds_read_b64_tr_b16 a[78:79], v118 offset:3072\n"
  "buffer_load_dwordx4 v116, s[40:43], s57 offen offset:2048  lds\n"
  "s_add_u32 s57, s57, s84\n"
  "s_add_u32 m0, s59, 0\n"
  "ds_read_b64_tr_b16 a[82:83], v119 offset:3072\n"
  "ds_read_b64_tr_b16 a[86:87], v120 offset:3072\n"
  "s_waitcnt lgkmcnt(0)\n"
  "s_barrier\n"
  "buffer_load_dwordx4 v114, s[40:43], s57 offen offset:0  lds\n"
  "buffer_load_dwordx4 v115, s[40:43], s57 offen offset:1024  lds\n"
  "ds_read_b128 v[48:51], v104 offset:0\n"
  "ds_read_b64_tr_b16 a[90:91], v121 offset:5120\n"
  "ds_read_b64_tr_b16 a[94:95], v122 offset:5120\n"
  "s_waitcnt vmcnt(11)\n"
  "ds_read_b64_tr_b16 a[96:97], v117 offset:6144\n"
  "ds_read_b64_tr_b16 a[100:101], v118 offset:6144\n"
  "ds_read_b128 v[84:87], v111\n"
  "ds_read_b128 v[96:99], v111 offset:64\n"
  "s_sub_u32 s56, s56, 1\n"
  "buffer_load_dwordx4 v116, s[40:43], s57 offen offset:2048  lds\n"
  "ds_read_b128 v[52:55], v104 offset:2048\n"
  "v_xor_b32 v111, 0x2000, v111\n"
  "s_add_u32 s57, s57, 6144\n"
  "s_add_u32 m0, s59, 3072\n"
  "ds_read_b64_tr_b16 a[104:105], v119 offset:6144\n"
  "ds_read_b64_tr_b16 a[108:109], v120 offset:6144\n"
  "s_waitcnt lgkmcnt(7)\n"
  "buffer_load_dwordx4 v114, s[40:43], s57 offen offset:0  lds\n"
  "buffer_load_dwordx4 v115, s[40:43], s57 offen offset:1024  lds\n"
  "ds_read_b64_tr_b16 a[112:113], v121 offset:8192\n"
  "ds_read_b64_tr_b16 a[116:117], v122 offset:8192\n"
  "s_waitcnt vmcnt(11)\n"
  "ds_read_b64_tr_b16 a[98:99], v117 offset:9216\n"
  "ds_read_b64_tr_b16 a[102:103], v118 offset:9216\n"
  "s_sub_i32 s61, s56, 5\n"
  "s_max_i32 s61, s61, 0\n"
  "buffer_load_dwordx4 v116, s[40:43], s57 offen offset:2048  lds\n"
  "ds_read_b128 v[56:59], v105 offset:0\n"
  "s_add_u32 s57, s57, s58\n"
  "s_add_u32 m0, s59, 6144\n"
  "ds_read_b64_tr_b16 a[106:107], v119 offset:9216\n"
  "ds_read_b64_tr_b16 a[110:111], v120 offset:9216\n"
  "s_waitcnt lgkmcnt(5)\n"
  "buffer_load_dwordx4 v114, s[40:43], s57 offen offset:0  lds\n"
  "buffer_load_dwordx4 v115, s[40:43], s57 offen offset:1024  lds\n"
  "ds_read_b64_tr_b16 a[114:115], v121 offset:11264\n"
  "ds_read_b64_tr_b16 a[118:119], v122 offset:11264\n"
  "s_waitcnt vmcnt(11)\n"
  "ds_read_b64_tr_b16 a[120:121], v117 offset:12288\n"
  "ds_read_b64_tr_b16 a[124:125], v118 offset:12288\n"
  "buffer_load_dwordx4 v116, s[40:43], s57 offen offset:2048  lds\n"
  "ds_read_b128 v[60:63], v105 offset:2048\n"
  "v_add_f32 v84, v84, v96\n"
  "s_add_u32 s57, s57, 6144\n"
  "s_add_u32 m0, s59, 9216\n"
  "ds_read_b64_tr_b16 a[128:129], v119 offset:12288\n"
  "ds_read_b64_tr_b16 a[132:133], v120 offset:12288\n"
  "v_add_f32 v85, v85, v97\n"
  "v_add_f32 v86, v86, v98\n"
  "s_waitcnt lgkmcnt(5)\n"
  "buffer_load_dwordx4 v114, s[40:43], s57 offen offset:0  lds\n"
  "buffer_load_dwordx4 v115, s[40:43], s57 offen offset:1024  lds\n"
  "ds_read_b64_tr_b16 a[136:137], v121 offset:14336\n"
  "ds_read_b64_tr_b16 a[140:141], v122 offset:14336\n"
  "s_waitcnt vmcnt(11)\n"
  "ds_read_b64_tr_b16 a[122:123], v117 offset:15360\n"
  "ds_read_b64_tr_b16 a[126:127], v118 offset:15360\n"
  "v_add_f32 v87, v87, v99\n"
  "v_max3_f32 v82, v82, v84, v85\n"
  "buffer_load_dwordx4 v116, s[40:43], s57 offen offset:2048  lds\n"
  "v_rcp_f32 v84, v84\n"
  "s_add_u32 s57, s57, s84\n"
  "s_add_u32 m0, s59, 12288\n"
  "ds_read_b64_tr_b16 a[130:131], v119 offset:15360\n"
  "ds_read_b64_tr_b16 a[134:135], v120 offset:15360\n"
  "v_rcp_f32 v85, v85\n"
  "s_waitcnt lgkmcnt(4)\n"
  "buffer_load_dwordx4 v114, s[40:43], s57 offen offset:0  lds\n"
  "buffer_load_dwordx4 v115, s[40:43], s57 offen offset:1024  lds\n"
  "ds_read_b64_tr_b16 a[138:139], v121 offset:17408\n"
  "ds_read_b64_tr_b16 a[142:143], v122 offset:17408\n"
  "s_waitcnt vmcnt(11)\n"
  "ds_read_b64_tr_b16 a[144:145], v117 offset:0\n"
  "ds_read_b64_tr_b16 a[148:149], v118 offset:0\n"
  "v_max3_f32 v82, v82, v86, v87\n"
  "v_max3_f32 v83, v83, v84, v85\n"
  "buffer_load_dwordx4 v116, s[40:43], s57 offen offset:2048  lds\n"
  "v_rcp_f32 v86, v86\n"
  "s_add_u32 s57, s57, 6144\n"
  "s_add_u32 m0, s59, 15360\n"
  "ds_read_b64_tr_b16 a[152:153], v119 offset:0\n"
  "ds_read_b64_tr_b16 a[156:157], v120 offset:0\n"
  "v_rcp_f32 v87, v87\n"
  "s_waitcnt lgkmcnt(4)\n"
  "buffer_load_dwordx4 v114, s[40:43], s57 offen offset:0  lds\n"
  "buffer_load_dwordx4 v115, s[40:43], s57 offen offset:1024  lds\n"
  "ds_read_b64_tr_b16 a[160:161], v121 offset:2048\n"
  "ds_read_b64_tr_b16 a[164:165], v122 offset:2048\n"
  "s_waitcnt vmcnt(11)\n"
  "ds_read_b64_tr_b16 a[146:147], v117 offset:3072\n"
  "ds_read_b64_tr_b16 a[150:151], v118 offset:3072\n"
  "v_max3_f32 v83, v83, v86, v87\n"
  "v_mul_f32 v96, s99, v84\n"
  "buffer_load_dwordx4 v116, s[40:43], s57 offen offset:2048  lds\n"
  "v_mul_f32 v97, s99, v85\n"
  "v_mul_f32 v98, s99, v86\n"
  "s_add_u32 s57, s57, s58\n"
  "s_add_u32 m0, s59, 0\n"
  "ds_read_b64_tr_b16 a[154:155], v119 offset:3072\n"
  "ds_read_b64_tr_b16 a[158:159], v120 offset:3072\n"
  "v_mul_f32 v99, s99, v87\n"
  "v_fma_f32 v40, v40, v84, v96\n"
  "v_mfma_f32_16x16x32_bf16 v[0:3], v[48:51], a[0:3], v[100:103]\n"
  "s_waitcnt lgkmcnt(4)\n"
  "buffer_load_dwordx4 v114, s[40:43], s57 offen offset:0  lds\n"
  "buffer_load_dwordx4 v115, s[40:43], s57 offen offset:1024  lds\n"
  "ds_read_b128 v[64:67], v104 offset:4096\n"
  "ds_read_b64_tr_b16 a[162:163], v121 offset:5120\n"
  "ds_read_b64_tr_b16 a[166:167], v122 offset:5120\n"
  "v_mfma_f32_16x16x32_bf16 v[0:3], v[56:59], a[24:27], v[0:3]\n"
  "s_waitcnt vmcnt(11)\n"
  "ds_read_b64_tr_b16 a[168:169], v117 offset:6144\n"
  "ds_read_b64_tr_b16 a[172:173], v118 offset:6144\n"
  "v_fma_f32 v44, v44, v84, v96\n"
  "v_fma_f32 v41, v41, v85, v97\n"
  "v_mfma_f32_16x16x32_bf16 v[4:7], v[56:59], a[28:31], v[100:103]\n"
  "s_xor_b32 s99, 0x80000000, s98\n"
  "buffer_load_dwordx4 v116, s[40:43], s57 offen offset:2048  lds\n"
  "ds_read_b128 v[68:71], v104 offset:6144\n"
  "v_fma_f32 v45, v45, v85, v97\n"
  "v_mfma_f32_16x16x32_bf16 v[4:7], v[48:51], a[4:7], v[4:7]\n"
  "s_add_u32 s57, s57, 6144\n"
  "s_add_u32 m0, s59, 3072\n"
  "ds_read_b64_tr_b16 a[176:177], v119 offset:6144\n"
  "ds_read_b64_tr_b16 a[180:181], v120 offset:6144\n"
  "ds_read_b64_tr_b16 v[92:93], v108 offset:0\n"
  "ds_read_b64_tr_b16 v[94:95], v108 offset:32\n"
  "v_mfma_f32_16x16x32_bf16 v[8:11], v[48:51], a[8:11], v[100:103]\n"
  "s_waitcnt lgkmcnt(7)\n"
  "buffer_load_dwordx4 v114, s[40:43], s57 offen offset:0  lds\n"
  "buffer_load_dwordx4 v115, s[40:43], s57 offen offset:1024  lds\n"
  "ds_read_b64_tr_b16 a[184:185], v121 offset:8192\n"
  "ds_read_b64_tr_b16 a[188:189], v122 offset:8192\n"
  "v_mfma_f32_16x16x32_bf16 v[8:11], v[56:59], a[32:35], v[8:11]\n"
  "s_waitcnt vmcnt(11)\n"
  "ds_read_b64_tr_b16 a[170:171], v117 offset:9216\n"
  "ds_read_b64_tr_b16 a[174:175], v118 offset:9216\n"
  "v_fma_f32 v42, v42, v86, v98\n"
  "v_fma_f32 v46, v46, v86, v98\n"
  "v_mfma_f32_16x16x32_bf16 v[12:15], v[56:59], a[36:39], v[100:103]\n"
  "buffer_load_dwordx4 v116, s[40:43], s57 offen offset:2048  lds\n"
  "ds_read_b128 v[72:75], v105 offset:4096\n"
  "v_fma_f32 v43, v43, v87, v99\n"
  "v_mfma_f32_16x16x32_bf16 v[12:15], v[48:51], a[12:15], v[12:15]\n"
  "s_add_u32 s57, s57, s84\n"
  "s_add_u32 m0, s59, 6144\n"
  "ds_read_b64_tr_b16 a[178:179], v119 offset:9216\n"
  "ds_read_b64_tr_b16 a[182:183], v120 offset:9216\n"
  "v_fma_f32 v47, v47, v87, v99\n"
  "v_fma_f32 v24, v24, v84, v96\n"
  "v_mfma_f32_16x16x32_bf16 v[16:19], v[48:51], a[16:19], v[100:103]\n"
  "s_waitcnt lgkmcnt(5)\n"
  "buffer_load_dwordx4 v114, s[40:43], s57 offen offset:0  lds\n"
  "buffer_load_dwordx4 v115, s[40:43], s57 offen offset:1024  lds\n"
  "ds_read_b64_tr_b16 a[186:187], v121 offset:11264\n"
  "ds_read_b64_tr_b16 a[190:191], v122 offset:11264\n"
  "v_mfma_f32_16x16x32_bf16 v[16:19], v[56:59], a[40:43], v[16:19]\n"
  "s_waitcnt vmcnt(11)\n"
  "ds_read_b64_tr_b16 a[192:193], v117 offset:12288\n"
  "ds_read_b64_tr_b16 a[196:197], v118 offset:12288\n"
  "v_fma_f32 v25, v25, v85, v97\n"
  "v_fma_f32 v28, v28, v84, v96\n"
  "v_mfma_f32_16x16x32_bf16 v[20:23], v[56:59], a[44:47], v[100:103]\n"
  "buffer_load_dwordx4 v116, s[40:43], s57 offen offset:2048  lds\n"
  "ds_read_b128 v[76:79], v105 offset:6144\n"
  "v_fma_f32 v29, v29, v85, v97\n"
  "v_mfma_f32_16x16x32_bf16 v[20:23], v[48:51], a[20:23], v[20:23]\n"
  "s_add_u32 s57, s57, 6144\n"
  "s_add_u32 m0, s59, 9216\n"
  "ds_read_b64_tr_b16 a[200:201], v119 offset:12288\n"
  "ds_read_b64_tr_b16 a[204:205], v120 offset:12288\n"
  "v_fma_f32 v32, v32, v84, v96\n"
  "v_fma_f32 v33, v33, v85, v97\n"
  "v_smfmac_f32_16x16x64_bf16 v[80:83], v[88:91], v[48:55], v124\n"
  "s_waitcnt lgkmcnt(5)\n"
  "buffer_load_dwordx4 v114, s[40:43], s57 offen offset:0  lds\n"
  "buffer_load_dwordx4 v115, s[40:43], s57 offen offset:1024  lds\n"
  "ds_read_b64_tr_b16 a[208:209], v121 offset:14336\n"
  "ds_read_b64_tr_b16 a[212:213], v122 offset:14336\n"
  "v_smfmac_f32_16x16x64_bf16 v[80:83], v[88:91], v[64:71], v124\n"
  "buffer_store_dwordx4 v[92:95], v109, s[48:51], s68 offen offset:0 nt\n"
  "s_waitcnt vmcnt(12)\n"
  "ds_read_b64_tr_b16 a[194:195], v117 offset:15360\n"
  "ds_read_b64_tr_b16 a[198:199], v118 offset:15360\n"
  "v_mfma_f32_16x16x32_bf16 v[20:23], v[52:55], a[68:71], v[20:23]\n"
  "buffer_load_dwordx4 v116, s[40:43], s57 offen offset:2048  lds\n"
  "v_fma_f32 v36, v36, v84, v96\n"
  "v_fma_f32 v37, v37, v85, v97\n"
  "v_mfma_f32_16x16x32_bf16 v[20:23], v[60:63], a[92:95], v[20:23]\n"
  "s_add_u32 s57, s57, s58\n"
  "s_add_u32 m0, s59, 12288\n"
  "ds_read_b64_tr_b16 a[202:203], v119 offset:15360\n"
  "ds_read_b64_tr_b16 a[206:207], v120 offset:15360\n"
  "v_fma_f32 v26, v26, v86, v98\n"
  "v_fma_f32 v27, v27, v87, v99\n"
  "v_mfma_f32_16x16x32_bf16 v[16:19], v[60:63], a[88:91], v[16:19]\n"
  "s_waitcnt lgkmcnt(4)\n"
  "buffer_load_dwordx4 v114, s[40:43], s57 offen offset:0  lds\n"
  "buffer_load_dwordx4 v115, s[40:43], s57 offen offset:1024  lds\n"
  "ds_read_b64_tr_b16 a[210:211], v121 offset:17408\n"
  "ds_read_b64_tr_b16 a[214:215], v122 offset:17408\n"
  "v_mfma_f32_16x16x32_bf16 v[16:19], v[52:55], a[64:67], v[16:19]\n"
  "s_waitcnt vmcnt(12)\n"
  "ds_read_b64_tr_b16 a[216:217], v117 offset:0\n"
  "ds_read_b64_tr_b16 a[220:221], v118 offset:0\n"
  "v_fma_f32 v30, v30, v86, v98\n"
  "v_fma_f32 v31, v31, v87, v99\n"
  "v_mfma_f32_16x16x32_bf16 v[12:15], v[52:55], a[60:63], v[12:15]\n"
  "buffer_load_dwordx4 v116, s[40:43], s57 offen offset:2048  lds\n"
  "v_fma_f32 v34, v34, v86, v98\n"
  "v_fma_f32 v35, v35, v87, v99\n"
  "v_mfma_f32_16x16x32_bf16 v[12:15], v[60:63], a[84:87], v[12:15]\n"
  "s_add_u32 s57, s57, 6144\n"
  "s_add_u32 m0, s59, 15360\n"
  "ds_read_b64_tr_b16 a[224:225], v119 offset:0\n"
  "ds_read_b64_tr_b16 a[228:229], v120 offset:0\n"
  "v_fma_f32 v38, v38, v86, v98\n"
  "v_fma_f32 v39, v39, v87, v99\n"
  "v_mfma_f32_16x16x32_bf16 v[8:11], v[60:63], a[80:83], v[8:11]\n"
  "s_waitcnt lgkmcnt(4)\n"
  "buffer_load_dwordx4 v114, s[40:43], s57 offen offset:0  lds\n"
  "buffer_load_dwordx4 v115, s[40:43], s57 offen offset:1024  lds\n"
  "ds_read_b64_tr_b16 a[232:233], v121 offset:2048\n"
  "ds_read_b64_tr_b16 a[236:237], v122 offset:2048\n"
  "v_mfma_f32_16x16x32_bf16 v[8:11], v[52:55], a[56:59], v[8:11]\n"
  "s_waitcnt vmcnt(12)\n"
  "ds_read_b64_tr_b16 a[218:219], v117 offset:3072\n"
  "ds_read_b64_tr_b16 a[222:223], v118 offset:3072\n"
  "ds_read_b64_tr_b16 v[96:97], v108 offset:2304\n"
  "ds_read_b64_tr_b16 v[98:99], v108 offset:2336\n"
  "v_mfma_f32_16x16x32_bf16 v[4:7], v[52:55], a[52:55], v[4:7]\n"
  "buffer_load_dwordx4 v116, s[40:43], s57 offen offset:2048  lds\n"
  "v_cvt_pk_bf16_f32 v40, v40, v44\n"
  "v_cvt_pk_bf16_f32 v41, v41, v45\n"
  "v_mfma_f32_16x16x32_bf16 v[4:7], v[60:63], a[76:79], v[4:7]\n"
  "s_add_u32 s57, s57, s84\n"
  "s_add_u32 m0, s59, 0\n"
  "ds_read_b64_tr_b16 a[226:227], v119 offset:3072\n"
  "ds_read_b64_tr_b16 a[230:231], v120 offset:3072\n"
  "v_cvt_pk_bf16_f32 v42, v42, v46\n"
  "v_cvt_pk_bf16_f32 v43, v43, v47\n"
  "v_mfma_f32_16x16x32_bf16 v[0:3], v[60:63], a[72:75], v[0:3]\n"
  "s_waitcnt lgkmcnt(6)\n"
  "buffer_load_dwordx4 v114, s[40:43], s57 offen offset:0  lds\n"
  "buffer_load_dwordx4 v115, s[40:43], s57 offen offset:1024  lds\n"
  "ds_read_b64_tr_b16 a[234:235], v121 offset:5120\n"
  "ds_read_b64_tr_b16 a[238:239], v122 offset:5120\n"
  "v_mfma_f32_16x16x32_bf16 v[0:3], v[52:55], a[48:51], v[0:3]\n"
  "s_waitcnt vmcnt(12)\n"
  "ds_read_b64_tr_b16 a[240:241], v117 offset:6144\n"
  "ds_read_b64_tr_b16 a[244:245], v118 offset:6144\n"
  "v_cvt_pk_bf16_f32 v24, v24, v28\n"
  "v_cvt_pk_bf16_f32 v28, v25, v29\n"
  "v_mfma_f32_16x16x32_bf16 v[0:3], v[64:67], a[96:99], v[0:3]\n"
  "buffer_load_dwordx4 v116, s[40:43], s57 offen offset:2048  lds\n"
  "ds_read_b128 v[48:51], v104 offset:8192\n"
  "v_cvt_pk_bf16_f32 v25, v32, v36\n"
  "v_mfma_f32_16x16x32_bf16 v[0:3], v[72:75], a[120:123], v[0:3]\n"
  "s_add_u32 s57, s57, 6144\n"
  "s_add_u32 m0, s59, 3072\n"
  "ds_read_b64_tr_b16 a[248:249], v119 offset:6144\n"
  "ds_read_b64_tr_b16 a[252:253], v120 offset:6144\n"
  "v_cvt_pk_bf16_f32 v29, v33, v37\n"
  "v_cvt_pk_bf16_f32 v26, v26, v30\n"
  "v_mfma_f32_16x16x32_bf16 v[4:7], v[72:75], a[124:127], v[4:7]\n"
  "s_waitcnt lgkmcnt(5)\n"
  "buffer_load_dwordx4 v114, s[40:43], s57 offen offset:0  lds\n"
  "buffer_load_dwordx4 v115, s[40:43], s57 offen offset:1024  lds\n"
  "ds_read_b128 v[52:55], v104 offset:10240\n"
  "ds_read_b64_tr_b16 v[128:129], v121 offset:8192\n"
  "ds_read_b64_tr_b16 v[132:133], v122 offset:8192\n"
  "v_mfma_f32_16x16x32_bf16 v[4:7], v[64:67], a[100:103], v[4:7]\n"
  "buffer_store_dwordx4 v[96:99], v109, s[48:51], s68 offen offset:128 nt\n"
  "s_waitcnt vmcnt(12)\n"
  "ds_read_b64_tr_b16 a[242:243], v117 offset:9216\n"
  "ds_read_b64_tr_b16 a[246:247], v118 offset:9216\n"
  "v_cvt_pk_bf16_f32 v30, v27, v31\n"
  "v_mfma_f32_16x16x32_bf16 v[8:11], v[64:67], a[104:107], v[8:11]\n"
  "buffer_load_dwordx4 v116, s[40:43], s57 offen offset:2048  lds\n"
  "v_cvt_pk_bf16_f32 v27, v34, v38\n"
  "v_cvt_pk_bf16_f32 v31, v35, v39\n"
  "v_mfma_f32_16x16x32_bf16 v[8:11], v[72:75], a[128:131], v[8:11]\n"
  "s_add_u32 s57, s57, s58\n"
  "s_add_u32 m0, s59, 6144\n"
  "ds_read_b64_tr_b16 a[250:251], v119 offset:9216\n"
  "ds_read_b64_tr_b16 a[254:255], v120 offset:9216\n"
  "ds_read_b64_tr_b16 v[92:93], v108 offset:4608\n"
  "ds_read_b64_tr_b16 v[94:95], v108 offset:4640\n"
  "v_mfma_f32_16x16x32_bf16 v[12:15], v[72:75], a[132:135], v[12:15]\n"
  "s_waitcnt lgkmcnt(6)\n"
  "buffer_load_dwordx4 v114, s[40:43], s57 offen offset:0  lds\n"
  "buffer_load_dwordx4 v115, s[40:43], s57 offen offset:1024  lds\n"
  "ds_read_b128 v[56:59], v105 offset:8192\n"
  "ds_read_b64_tr_b16 v[130:131], v121 offset:11264\n"
  "ds_read_b64_tr_b16 v[134:135], v122 offset:11264\n"
  "v_mfma_f32_16x16x32_bf16 v[12:15], v[64:67], a[108:111], v[12:15]\n"
  "s_waitcnt vmcnt(12)\n"
  "ds_read_b64_tr_b16 v[136:137], v117 offset:12288\n"
  "ds_read_b64_tr_b16 v[140:141], v118 offset:12288\n"
  "ds_write2_b32 v107, v40, v41 offset0:0 offset1:72\n"
  "v_mfma_f32_16x16x32_bf16 v[16:19], v[64:67], a[112:115], v[16:19]\n"
  "buffer_load_dwordx4 v116, s[40:43], s57 offen offset:2048  lds\n"
  "ds_write2_b32 v107, v42, v43 offset0:144 offset1:216\n"
  "v_mfma_f32_16x16x32_bf16 v[16:19], v[72:75], a[136:139], v[16:19]\n"
  "s_add_u32 s57, s57, 6144\n"
  "s_add_u32 m0, s59, 9216\n"
  "ds_read_b64_tr_b16 v[144:145], v119 offset:12288\n"
  "ds_read_b64_tr_b16 v[148:149], v120 offset:12288\n"
  "ds_write_b64 v106, v[24:25] offset:0\n"
  "v_mfma_f32_16x16x32_bf16 v[20:23], v[72:75], a[140:143], v[20:23]\n"
  "s_waitcnt lgkmcnt(7)\n"
  "buffer_load_dwordx4 v114, s[40:43], s57 offen offset:0  lds\n"
  "buffer_load_dwordx4 v115, s[40:43], s57 offen offset:1024  lds\n"
  "ds_read_b128 v[60:63], v105 offset:10240\n"
  "ds_read_b64_tr_b16 v[152:153], v121 offset:14336\n"
  "ds_read_b64_tr_b16 v[156:157], v122 offset:14336\n"
  "v_mfma_f32_16x16x32_bf16 v[20:23], v[64:67], a[116:119], v[20:23]\n"
  "s_waitcnt vmcnt(12)\n"
  "ds_read_b64_tr_b16 v[138:139], v117 offset:15360\n"
  "ds_read_b64_tr_b16 v[142:143], v118 offset:15360\n"
  "ds_write_b64 v106, v[28:29] offset:288\n"
  "v_mfma_f32_16x16x32_bf16 v[20:23], v[68:71], a[164:167], v[20:23]\n"
  "buffer_load_dwordx4 v116, s[40:43], s57 offen offset:2048  lds\n"
  "ds_write_b64 v106, v[26:27] offset:576\n"
  "v_mfma_f32_16x16x32_bf16 v[20:23], v[76:79], a[188:191], v[20:23]\n"
  "s_add_u32 s57, s57, s84\n"
  "s_add_u32 m0, s59, 12288\n"
  "ds_read_b64_tr_b16 v[146:147], v119 offset:15360\n"
  "ds_read_b64_tr_b16 v[150:151], v120 offset:15360\n"
  "ds_write_b64 v106, v[30:31] offset:864\n"
  "v_mfma_f32_16x16x32_bf16 v[16:19], v[76:79], a[184:187], v[16:19]\n"
  "s_waitcnt lgkmcnt(7)\n"
  "buffer_load_dwordx4 v114, s[40:43], s57 offen offset:0  lds\n"
  "buffer_load_dwordx4 v115, s[40:43], s57 offen offset:1024  lds\n"
  "ds_read_b64_tr_b16 v[154:155], v121 offset:17408\n"
  "ds_read_b64_tr_b16 v[158:159], v122 offset:17408\n"
  "v_mfma_f32_16x16x32_bf16 v[16:19], v[68:71], a[160:163], v[16:19]\n"
  "buffer_store_dwordx4 v[92:95], v109, s[48:51], s68 offen offset:256 nt\n"
  "s_waitcnt vmcnt(13)\n"
  "ds_read_b64_tr_b16 v[160:161], v117 offset:0\n"
  "ds_read_b64_tr_b16 v[164:165], v118 offset:0\n"
  "v_xor_b32 v108, 0x2000, v108\n"
  "v_mfma_f32_16x16x32_bf16 v[12:15], v[68:71], a[156:159], v[12:15]\n"
  "s_add_u32 s68, s68, s69\n"
  "buffer_load_dwordx4 v116, s[40:43], s57 offen offset:2048  lds\n"
  "v_mfma_f32_16x16x32_bf16 v[12:15], v[76:79], a[180:183], v[12:15]\n"
  "s_add_u32 s57, s57, 6144\n"
  "s_add_u32 m0, s59, 15360\n"
  "ds_read_b64_tr_b16 v[168:169], v119 offset:0\n"
  "ds_read_b64_tr_b16 v[172:173], v120 offset:0\n"
  "v_mfma_f32_16x16x32_bf16 v[8:11], v[76:79], a[176:179], v[8:11]\n"
  "s_waitcnt lgkmcnt(4)\n"
  "buffer_load_dwordx4 v114, s[40:43], s57 offen offset:0  lds\n"
  "buffer_load_dwordx4 v115, s[40:43], s57 offen offset:1024  lds\n"
  "ds_read_b64_tr_b16 v[176:177], v121 offset:2048\n"
  "ds_read_b64_tr_b16 v[180:181], v122 offset:2048\n"
  "v_mfma_f32_16x16x32_bf16 v[8:11], v[68:71], a[152:155], v[8:11]\n"
  "s_waitcnt vmcnt(12)\n"
  "ds_read_b64_tr_b16 v[162:163], v117 offset:3072\n"
  "ds_read_b64_tr_b16 v[166:167], v118 offset:3072\n"
  "v_mfma_f32_16x16x32_bf16 v[4:7], v[68:71], a[148:151], v[4:7]\n"
  "s_mov_b32 s69, s70\n"
  "buffer_load_dwordx4 v116, s[40:43], s57 offen offset:2048  lds\n"
  "v_mfma_f32_16x16x32_bf16 v[4:7], v[76:79], a[172:175], v[4:7]\n"
  "s_add_u32 s57, s57, s58\n"
  "s_add_u32 m0, s59, 0\n"
  "ds_read_b64_tr_b16 v[170:171], v119 offset:3072\n"
  "ds_read_b64_tr_b16 v[174:175], v120 offset:3072\n"
  "v_mfma_f32_16x16x32_bf16 v[0:3], v[76:79], a[168:171], v[0:3]\n"
  "s_waitcnt lgkmcnt(4)\n"
  "buffer_load_dwordx4 v114, s[40:43], s57 offen offset:0  lds\n"
  "buffer_load_dwordx4 v115, s[40:43], s57 offen offset:1024  lds\n"
  "ds_read_b64_tr_b16 v[178:179], v121 offset:5120\n"
  "ds_read_b64_tr_b16 v[182:183], v122 offset:5120\n"
  "v_mfma_f32_16x16x32_bf16 v[0:3], v[68:71], a[144:147], v[0:3]\n"
  "s_waitcnt vmcnt(12)\n"
  "ds_read_b64_tr_b16 v[184:185], v117 offset:6144\n"
  "ds_read_b64_tr_b16 v[188:189], v118 offset:6144\n"
  "v_mfma_f32_16x16x32_bf16 v[0:3], v[48:51], a[192:195], v[0:3]\n"
  "s_add_u32 s72, s72, 1\n"
  "buffer_load_dwordx4 v116, s[40:43], s57 offen offset:2048  lds\n"
  "ds_read_b128 v[64:67], v104 offset:12288\n"
  "v_mfma_f32_16x16x32_bf16 v[0:3], v[56:59], a[216:219], v[0:3]\n"
  "s_add_u32 s57, s57, 6144\n"
  "s_add_u32 m0, s59, 3072\n"
  "ds_read_b64_tr_b16 v[192:193], v119 offset:6144\n"
  "ds_read_b64_tr_b16 v[196:197], v120 offset:6144\n"
  "v_mfma_f32_16x16x32_bf16 v[4:7], v[56:59], a[220:223], v[4:7]\n"
  "s_waitcnt lgkmcnt(5)\n"
  "buffer_load_dwordx4 v114, s[40:43], s57 offen offset:0  lds\n"
  "buffer_load_dwordx4 v115, s[40:43], s57 offen offset:1024  lds\n"
  "ds_read_b128 v[68:71], v104 offset:14336\n"
  "ds_read_b64_tr_b16 v[200:201], v121 offset:8192\n"
  "ds_read_b64_tr_b16 v[204:205], v122 offset:8192\n"
  "v_mfma_f32_16x16x32_bf16 v[4:7], v[48:51], a[196:199], v[4:7]\n"
  "s_waitcnt vmcnt(12)\n"
  "ds_read_b64_tr_b16 v[186:187], v117 offset:9216\n"
  "ds_read_b64_tr_b16 v[190:191], v118 offset:9216\n"
  "v_xor_b32 v104, 0x4000, v104\n"
  "v_mfma_f32_16x16x32_bf16 v[8:11], v[48:51], a[200:203], v[8:11]\n"
  "s_lshr_b32 s74, s72, 1\n"
  "buffer_load_dwordx4 v116, s[40:43], s57 offen offset:2048  lds\n"
  "v_readlane_b32 s73, v126, s74\n"
  "v_mfma_f32_16x16x32_bf16 v[8:11], v[56:59], a[224:227], v[8:11]\n"
  "s_add_u32 s57, s57, s84\n"
  "ds_read_b64_tr_b16 v[194:195], v119 offset:9216\n"
  "ds_read_b64_tr_b16 v[198:199], v120 offset:9216\n"
  "v_mfma_f32_16x16x32_bf16 v[12:15], v[56:59], a[228:231], v[12:15]\n"
  "s_lshl_b32 s74, s72, 4\n"
  "ds_read_b128 v[72:75], v105 offset:12288\n"
  "ds_read_b64_tr_b16 v[202:203], v121 offset:11264\n"
  "ds_read_b64_tr_b16 v[206:207], v122 offset:11264\n"
  "v_mfma_f32_16x16x32_bf16 v[12:15], v[48:51], a[204:207], v[12:15]\n"
  "s_waitcnt vmcnt(9)\n"
  "ds_read_b64_tr_b16 v[208:209], v117 offset:12288\n"
  "ds_read_b64_tr_b16 v[212:213], v118 offset:12288\n"
  "v_mfma_f32_16x16x32_bf16 v[16:19], v[48:51], a[208:211], v[16:19]\n"
  "s_lshr_b32 s73, s73, s74\n"
  "v_mfma_f32_16x16x32_bf16 v[16:19], v[56:59], a[232:235], v[16:19]\n"
  "s_and_b32 s73, s73, 0xffff\n"
  "ds_read_b64_tr_b16 v[216:217], v119 offset:12288\n"
  "ds_read_b64_tr_b16 v[220:221], v120 offset:12288\n"
  "v_mfma_f32_16x16x32_bf16 v[20:23], v[56:59], a[236:239], v[20:23]\n"
  "s_lshl_b32 s73, s73, 13\n"
  "ds_read_b128 v[76:79], v105 offset:14336\n"
  "ds_read_b64_tr_b16 v[224:225], v121 offset:14336\n"
  "ds_read_b64_tr_b16 v[228:229], v122 offset:14336\n"
  "v_xor_b32 v105, 0x4000, v105\n"
  "v_mfma_f32_16x16x32_bf16 v[20:23], v[48:51], a[212:215], v[20:23]\n"
  "s_waitcnt vmcnt(6)\n"
  "ds_read_b64_tr_b16 v[210:211], v117 offset:15360\n"
  "ds_read_b64_tr_b16 v[214:215], v118 offset:15360\n"
  "v_smfmac_f32_16x16x64_bf16 v[80:83], v[88:91], v[48:55], v124\n"
  "s_waitcnt lgkmcnt(15)\n"
  "v_smfmac_f32_16x16x64_bf16 v[80:83], v[88:91], v[64:71], v124\n"
  "ds_read_b64_tr_b16 v[218:219], v119 offset:15360\n"
  "ds_read_b64_tr_b16 v[222:223], v120 offset:15360\n"
  "v_mfma_f32_16x16x32_bf16 v[20:23], v[52:55], v[132:135], v[20:23]\n"
  "s_movk_i32 s70, 6144\n"
  "ds_read_b64_tr_b16 v[226:227], v121 offset:17408\n"
  "ds_read_b64_tr_b16 v[230:231], v122 offset:17408\n"
  "v_mfma_f32_16x16x32_bf16 v[20:23], v[60:63], v[156:159], v[20:23]\n"
  "s_waitcnt vmcnt(3)\n"
  "ds_read_b64_tr_b16 v[232:233], v117 offset:0\n"
  "ds_read_b64_tr_b16 v[236:237], v118 offset:0\n"
  "v_mfma_f32_16x16x32_bf16 v[16:19], v[60:63], v[152:155], v[16:19]\n"
  "v_add_f32 v44, v80, v81\n"
  "v_mov_b32 v80, 0\n"
  "v_mov_b32 v81, 0\n"
  "v_mfma_f32_16x16x32_bf16 v[16:19], v[52:55], v[128:131], v[16:19]\n"
  "ds_read_b64_tr_b16 v[240:241], v119 offset:0\n"
  "ds_read_b64_tr_b16 v[244:245], v120 offset:0\n"
  "ds_write_b32 v110, v44\n"
  "v_mfma_f32_16x16x32_bf16 v[12:15], v[52:55], a[252:255], v[12:15]\n"
  "ds_read_b64_tr_b16 v[248:249], v121 offset:2048\n"
  "ds_read_b64_tr_b16 v[252:253], v122 offset:2048\n"
  "v_mfma_f32_16x16x32_bf16 v[12:15], v[60:63], v[148:151], v[12:15]\n"
  "s_waitcnt vmcnt(0)\n"
  "ds_read_b64_tr_b16 v[234:235], v117 offset:3072\n"
  "ds_read_b64_tr_b16 v[238:239], v118 offset:3072\n"
  "v_mfma_f32_16x16x32_bf16 v[8:11], v[60:63], v[144:147], v[8:11]\n"
  "v_mfma_f32_16x16x32_bf16 v[8:11], v[52:55], a[248:251], v[8:11]\n"
  "ds_read_b64_tr_b16 v[242:243], v119 offset:3072\n"
  "ds_read_b64_tr_b16 v[246:247], v120 offset:3072\n"
  "v_xor_b32 v106, 0x2000, v106\n"
  "v_xor_b32 v107, 0x2000, v107\n"
  "v_mfma_f32_16x16x32_bf16 v[4:7], v[52:55], a[244:247], v[4:7]\n"
  "ds_read_b64_tr_b16 v[250:251], v121 offset:5120\n"
  "ds_read_b64_tr_b16 v[254:255], v122 offset:5120\n"
  "v_xor_b32 v110, 0x2000, v110\n"
  "v_mfma_f32_16x16x32_bf16 v[4:7], v[60:63], v[140:143], v[4:7]\n"
  "v_mfma_f32_16x16x32_bf16 v[0:3], v[60:63], v[136:139], v[0:3]\n"
  "v_mfma_f32_16x16x32_bf16 v[0:3], v[52:55], a[240:243], v[0:3]\n"
  "s_add_u32 m0, s66, 0\n"
  "s_nop 0\n"
  "buffer_load_dwordx4 v112, s[44:47], s67 offen offset:0 nt lds\n"
  "s_add_u32 m0, s66, 1920\n"
  "s_nop 0\n"
  "buffer_load_dwordx4 v112, s[44:47], s67 offen offset:128 nt lds\n"
  "s_add_u32 m0, s66, 3840\n"
  "s_nop 0\n"
  "buffer_load_dwordx4 v112, s[44:47], s67 offen offset:256 nt lds\n"
  "s_add_u32 s66, s66, 6144\n"
  "s_cmp_ge_u32 s66, 86016\n"
  "s_cselect_b32 s64, 36864, 0\n"
  "s_sub_u32 s66, s66, s64\n"
  "s_sub_u32 s90, s90, 1\n"
  "s_cselect_b32 s63, s91, 6144\n"
  "s_cmov_b64 s[90:91], s[92:93]\n"
  "s_cmov_b64 s[92:93], s[94:95]\n"
  "s_add_u32 s67, s67, s63\n"
  "s_add_u32 m0, s66, 0\n"
  "s_nop 0\n"
  "buffer_load_dwordx4 v112, s[44:47], s67 offen offset:0 nt lds\n"
  "s_add_u32 m0, s66, 1920\n"
  "s_nop 0\n"
  "buffer_load_dwordx4 v112, s[44:47], s67 offen offset:128 nt lds\n"
  "s_add_u32 m0, s66, 3840\n"
  "s_nop 0\n"
  "buffer_load_dwordx4 v112, s[44:47], s67 offen offset:256 nt lds\n"
  "s_add_u32 s66, s66, 6144\n"
  "s_cmp_ge_u32 s66, 86016\n"
  "s_cselect_b32 s64, 36864, 0\n"
  "s_sub_u32 s66, s66, s64\n"
  "s_sub_u32 s90, s90, 1\n"
  "s_cselect_b32 s63, s91, 6144\n"
  "s_cmov_b64 s[90:91], s[92:93]\n"
  "s_cmov_b64 s[92:93], s[94:95]\n"
  "s_add_u32 s67, s67, s63\n"
  "s_add_u32 m0, s66, 0\n"
  "s_nop 0\n"
  "buffer_load_dwordx4 v112, s[44:47], s67 offen offset:0 nt lds\n"
  "s_add_u32 m0, s66, 1920\n"
  "s_nop 0\n"
  "buffer_load_dwordx4 v112, s[44:47], s67 offen offset:128 nt lds\n"
  "s_add_u32 m0, s66, 3840\n"
  "s_nop 0\n"
  "buffer_load_dwordx4 v112, s[44:47], s67 offen offset:256 nt lds\n"
  "s_add_u32 s66, s66, 6144\n"
  "s_cmp_ge_u32 s66, 86016\n"
  "s_cselect_b32 s64, 36864, 0\n"
  "s_sub_u32 s66, s66, s64\n"
  "s_sub_u32 s90, s90, 1\n"
  "s_cselect_b32 s63, s91, 6144\n"
  "s_cmov_b64 s[90:91], s[92:93]\n"
  "s_cmov_b64 s[92:93], s[94:95]\n"
  "s_add_u32 s67, s67, s63\n"
  "s_add_u32 m0, s66, 0\n"
  "s_nop 0\n"
  "buffer_load_dwordx4 v112, s[44:47], s67 offen offset:0 nt lds\n"
  "s_add_u32 m0, s66, 1920\n"
  "s_nop 0\n"
  "buffer_load_dwordx4 v112, s[44:47], s67 offen offset:128 nt lds\n"
  "s_add_u32 m0, s66, 3840\n"
  "s_nop 0\n"
  "buffer_load_dwordx4 v112, s[44:47], s67 offen offset:256 nt lds\n"
  "s_add_u32 s66, s66, 6144\n"
  "s_cmp_ge_u32 s66, 86016\n"
  "s_cselect_b32 s64, 36864, 0\n"
  "s_sub_u32 s66, s66, s64\n"
  "s_sub_u32 s90, s90, 1\n"
  "s_cselect_b32 s63, s91, 6144\n"
  "s_cmov_b64 s[90:91], s[92:93]\n"
  "s_cmov_b64 s[92:93], s[94:95]\n"
  "s_add_u32 s67, s67, s63\n"
  ".p2align 6\n"
  "s_nop 0\n"
  "B_H1_%=:\n"
  "s_waitcnt vmcnt(16)\n"
  "B_H_%=:\n"
  "s_waitcnt vmcnt(32) lgkmcnt(0)\n"
  "s_barrier\n"
  "v_mfma_f32_16x16x32_bf16 v[20:23], v[64:67], v[180:183], v[20:23]\n"
  "s_add_u32 m0, s66, 0\n"
  "ds_read_b128 v[48:51], v104 offset:0\n"
  "ds_read_b128 v[84:87], v111\n"
  "v_mfma_f32_16x16x32_bf16 v[20:23], v[72:75], v[204:207], v[20:23]\n"
  "s_sub_u32 s56, s56, 1\n"
  "ds_read_b128 v[96:99], v111 offset:64\n"
  "v_xor_b32 v111, 0x2000, v111\n"
  "v_mfma_f32_16x16x32_bf16 v[20:23], v[68:71], v[228:231], v[20:23]\n"
  "ds_read_b128 v[52:55], v104 offset:2048\n"
  "v_mfma_f32_16x16x32_bf16 v[44:47], v[76:79], v[252:255], v[20:23]\n"
  "buffer_load_dwordx4 v112, s[44:47], s67 offen offset:0 nt lds\n"
  "v_mfma_f32_16x16x32_bf16 v[16:19], v[76:79], v[248:251], v[16:19]\n"
  "s_add_u32 m0, s66, 1920\n"
  "v_mfma_f32_16x16x32_bf16 v[16:19], v[68:71], v[224:227], v[16:19]\n"
  "v_mfma_f32_16x16x32_bf16 v[16:19], v[72:75], v[200:203], v[16:19]\n"
  "ds_read_b128 v[56:59], v105 offset:0\n"
  "v_mfma_f32_16x16x32_bf16 v[40:43], v[64:67], v[176:179], v[16:19]\n"
  "buffer_load_dwordx4 v112, s[44:47], s67 offen offset:128 nt lds\n"
  "v_mfma_f32_16x16x32_bf16 v[12:15], v[64:67], v[172:175], v[12:15]\n"
  "s_add_u32 m0, s66, 3840\n"
  "v_mfma_f32_16x16x32_bf16 v[12:15], v[72:75], v[196:199], v[12:15]\n"
  "v_mfma_f32_16x16x32_bf16 v[12:15], v[68:71], v[220:223], v[12:15]\n"
  "s_waitcnt lgkmcnt(2)\n"
  "ds_read_b128 v[60:63], v105 offset:2048\n"
  "v_add_f32 v84, v84, v96\n"
  "v_mfma_f32_16x16x32_bf16 v[36:39], v[76:79], v[244:247], v[12:15]\n"
  "buffer_load_dwordx4 v112, s[44:47], s67 offen offset:256 nt lds\n"
  "v_mfma_f32_16x16x32_bf16 v[8:11], v[76:79], v[240:243], v[8:11]\n"
  "s_mov_b32 m0, 159744\n"
  "v_add_f32 v85, v85, v97\n"
  "v_add_f32 v86, v86, v98\n"
  "v_mfma_f32_16x16x32_bf16 v[8:11], v[68:71], v[216:219], v[8:11]\n"
  "v_add_f32 v87, v87, v99\n"
  "v_max3_f32 v82, v82, v84, v85\n"
  "v_mfma_f32_16x16x32_bf16 v[8:11], v[72:75], v[192:195], v[8:11]\n"
  "v_rcp_f32 v84, v84\n"
  "v_mfma_f32_16x16x32_bf16 v[32:35], v[64:67], v[168:171], v[8:11]\n"
  "buffer_load_dword v125, s[80:83], s73 offen  lds\n"
  "v_mfma_f32_16x16x32_bf16 v[4:7], v[64:67], v[164:167], v[4:7]\n"
  "v_rcp_f32 v85, v85\n"
  "v_mfma_f32_16x16x32_bf16 v[4:7], v[72:75], v[188:191], v[4:7]\n"
  "v_max3_f32 v82, v82, v86, v87\n"
  "v_max3_f32 v83, v83, v84, v85\n"
  "v_mfma_f32_16x16x32_bf16 v[4:7], v[68:71], v[212:215], v[4:7]\n"
  "v_rcp_f32 v86, v86\n"
  "v_mfma_f32_16x16x32_bf16 v[28:31], v[76:79], v[236:239], v[4:7]\n"
  "v_rcp_f32 v87, v87\n"
  "v_mfma_f32_16x16x32_bf16 v[0:3], v[76:79], v[232:235], v[0:3]\n"
  "v_max3_f32 v83, v83, v86, v87\n"
  "v_mul_f32 v96, s99, v84\n"
  "v_mfma_f32_16x16x32_bf16 v[0:3], v[68:71], v[208:211], v[0:3]\n"
  "v_mul_f32 v97, s99, v85\n"
  "v_fma_f32 v40, v40, v84, v96\n"
  "v_mfma_f32_16x16x32_bf16 v[0:3], v[72:75], v[184:187], v[0:3]\n"
  "v_fma_f32 v44, v44, v84, v96\n"
  "v_cvt_pk_bf16_f32 v40, v40, v44\n"
  "v_mfma_f32_16x16x32_bf16 v[24:27], v[64:67], v[160:163], v[0:3]\n"
  "s_waitcnt lgkmcnt(0)\n"
  "v_fma_f32 v41, v41, v85, v97\n"
  "v_fma_f32 v45, v45, v85, v97\n"
  "v_mfma_f32_16x16x32_bf16 v[0:3], v[48:51], a[0:3], v[100:103]\n"
  "ds_read_b128 v[64:67], v104 offset:4096\n"
  "v_cvt_pk_bf16_f32 v41, v41, v45\n"
  "v_mfma_f32_16x16x32_bf16 v[0:3], v[56:59], a[24:27], v[0:3]\n"
  "v_mul_f32 v98, s99, v86\n"
  "v_mul_f32 v99, s99, v87\n"
  "v_mfma_f32_16x16x32_bf16 v[0:3], v[52:55], a[48:51], v[0:3]\n"
  "ds_read_b128 v[68:71], v104 offset:6144\n"
  "v_fma_f32 v42, v42, v86, v98\n"
  "v_mfma_f32_16x16x32_bf16 v[0:3], v[60:63], a[72:75], v[0:3]\n"
  "ds_read_b64_tr_b16 v[92:93], v108 offset:0\n"
  "ds_read_b64_tr_b16 v[94:95], v108 offset:32\n"
  "v_mfma_f32_16x16x32_bf16 v[4:7], v[60:63], a[76:79], v[100:103]\n"
  "v_fma_f32 v46, v46, v86, v98\n"
  "v_cvt_pk_bf16_f32 v42, v42, v46\n"
  "v_mfma_f32_16x16x32_bf16 v[4:7], v[52:55], a[52:55], v[4:7]\n"
  "v_fma_f32 v43, v43, v87, v99\n"
  "v_fma_f32 v47, v47, v87, v99\n"
  "v_mfma_f32_16x16x32_bf16 v[4:7], v[56:59], a[28:31], v[4:7]\n"
  "ds_read_b128 v[72:75], v105 offset:4096\n"
  "v_cvt_pk_bf16_f32 v43, v43, v47\n"
  "v_mfma_f32_16x16x32_bf16 v[4:7], v[48:51], a[4:7], v[4:7]\n"
  "v_fma_f32 v24, v24, v84, v96\n"
  "v_fma_f32 v25, v25, v85, v97\n"
  "v_mfma_f32_16x16x32_bf16 v[8:11], v[48:51], a[8:11], v[100:103]\n"
  "v_fma_f32 v28, v28, v84, v96\n"
  "v_fma_f32 v29, v29, v85, v97\n"
  "v_mfma_f32_16x16x32_bf16 v[8:11], v[56:59], a[32:35], v[8:11]\n"
  "v_fma_f32 v32, v32, v84, v96\n"
  "v_fma_f32 v33, v33, v85, v97\n"
  "v_mfma_f32_16x16x32_bf16 v[8:11], v[52:55], a[56:59], v[8:11]\n"
  "ds_read_b128 v[76:79], v105 offset:6144\n"
  "v_fma_f32 v36, v36, v84, v96\n"
  "v_mfma_f32_16x16x32_bf16 v[8:11], v[60:63], a[80:83], v[8:11]\n"
  "v_fma_f32 v37, v37, v85, v97\n"
  "v_fma_f32 v26, v26, v86, v98\n"
  "v_smfmac_f32_16x16x64_bf16 v[80:83], v[88:91], v[48:55], v124\n"
  "s_waitcnt lgkmcnt(4)\n"
  "v_fma_f32 v27, v27, v87, v99\n"
  "v_smfmac_f32_16x16x64_bf16 v[80:83], v[88:91], v[64:71], v124\n"
  "s_waitcnt lgkmcnt(2)\n"
  "buffer_store_dwordx4 v[92:95], v109, s[48:51], s68 offen offset:0 nt\n"
  "ds_write2_b32 v107, v40, v41 offset0:0 offset1:72\n"
  "v_mfma_f32_16x16x32_bf16 v[12:15], v[60:63], a[84:87], v[100:103]\n"
  "v_fma_f32 v30, v30, v86, v98\n"
  "v_fma_f32 v31, v31, v87, v99\n"
  "v_mfma_f32_16x16x32_bf16 v[12:15], v[52:55], a[60:63], v[12:15]\n"
  "v_fma_f32 v34, v34, v86, v98\n"
  "v_fma_f32 v35, v35, v87, v99\n"
  "v_mfma_f32_16x16x32_bf16 v[12:15], v[56:59], a[36:39], v[12:15]\n"
  "v_fma_f32 v38, v38, v86, v98\n"
  "v_fma_f32 v39, v39, v87, v99\n"
  "v_mfma_f32_16x16x32_bf16 v[12:15], v[48:51], a[12:15], v[12:15]\n"
  "ds_write2_b32 v107, v42, v43 offset0:144 offset1:216\n"
  "v_mfma_f32_16x16x32_bf16 v[16:19], v[48:51], a[16:19], v[100:103]\n"
  "v_cvt_pk_bf16_f32 v24, v24, v28\n"
  "v_cvt_pk_bf16_f32 v28, v25, v29\n"
  "v_mfma_f32_16x16x32_bf16 v[16:19], v[56:59], a[40:43], v[16:19]\n"
  "ds_read_b64_tr_b16 v[96:97], v108 offset:2304\n"
  "ds_read_b64_tr_b16 v[98:99], v108 offset:2336\n"
  "v_mfma_f32_16x16x32_bf16 v[16:19], v[52:55], a[64:67], v[16:19]\n"
  "v_cvt_pk_bf16_f32 v25, v32, v36\n"
  "v_cvt_pk_bf16_f32 v29, v33, v37\n"
  "v_mfma_f32_16x16x32_bf16 v[16:19], v[60:63], a[88:91], v[16:19]\n"
  "v_cvt_pk_bf16_f32 v26, v26, v30\n"
  "v_cvt_pk_bf16_f32 v30, v27, v31\n"
  "v_mfma_f32_16x16x32_bf16 v[20:23], v[60:63], a[92:95], v[100:103]\n"
  "v_cvt_pk_bf16_f32 v27, v34, v38\n"
  "v_cvt_pk_bf16_f32 v31, v35, v39\n"
  "v_mfma_f32_16x16x32_bf16 v[20:23], v[52:55], a[68:71], v[20:23]\n"
  "v_mfma_f32_16x16x32_bf16 v[20:23], v[56:59], a[44:47], v[20:23]\n"
  "v_mfma_f32_16x16x32_bf16 v[20:23], v[48:51], a[20:23], v[20:23]\n"
  "s_waitcnt lgkmcnt(4)\n"
  "v_mfma_f32_16x16x32_bf16 v[20:23], v[64:67], a[116:119], v[20:23]\n"
  "s_add_u32 s66, s66, 6144\n"
  "ds_read_b128 v[48:51], v104 offset:8192\n"
  "v_mfma_f32_16x16x32_bf16 v[20:23], v[72:75], a[140:143], v[20:23]\n"
  "s_cmp_ge_u32 s66, 86016\n"
  "s_cselect_b32 s64, 36864, 0\n"
  "v_mfma_f32_16x16x32_bf16 v[20:23], v[68:71], a[164:167], v[20:23]\n"
  "s_sub_u32 s66, s66, s64\n"
  "ds_read_b128 v[52:55], v104 offset:10240\n"
  "v_mfma_f32_16x16x32_bf16 v[20:23], v[76:79], a[188:191], v[20:23]\n"
  "s_sub_u32 s90, s90, 1\n"
  "s_cselect_b32 s63, s91, 6144\n"
  "s_cmov_b64 s[90:91], s[92:93]\n"
  "s_cmov_b64 s[92:93], s[94:95]\n"
  "s_waitcnt lgkmcnt(2)\n"
  "buffer_store_dwordx4 v[96:99], v109, s[48:51], s68 offen offset:128 nt\n"
  "ds_write_b64 v106, v[24:25] offset:0\n"
  "v_mfma_f32_16x16x32_bf16 v[16:19], v[76:79], a[184:187], v[16:19]\n"
  "s_add_u32 s67, s67, s63\n"
  "v_mfma_f32_16x16x32_bf16 v[16:19], v[68:71], a[160:163], v[16:19]\n"
  "ds_read_b64_tr_b16 v[92:93], v108 offset:4608\n"
  "ds_read_b64_tr_b16 v[94:95], v108 offset:4640\n"
  "v_mfma_f32_16x16x32_bf16 v[16:19], v[72:75], a[136:139], v[16:19]\n"
  "ds_read_b128 v[56:59], v105 offset:8192\n"
  "v_xor_b32 v108, 0x2000, v108\n"
  "v_mfma_f32_16x16x32_bf16 v[16:19], v[64:67], a[112:115], v[16:19]\n"
  "v_mfma_f32_16x16x32_bf16 v[12:15], v[64:67], a[108:111], v[12:15]\n"
  "v_mfma_f32_16x16x32_bf16 v[12:15], v[72:75], a[132:135], v[12:15]\n"
  "v_mfma_f32_16x16x32_bf16 v[12:15], v[68:71], a[156:159], v[12:15]\n"
  "ds_read_b128 v[60:63], v105 offset:10240\n"
  "v_mfma_f32_16x16x32_bf16 v[12:15], v[76:79], a[180:183], v[12:15]\n"
  "v_mfma_f32_16x16x32_bf16 v[8:11], v[76:79], a[176:179], v[8:11]\n"
  "v_mfma_f32_16x16x32_bf16 v[8:11], v[68:71], a[152:155], v[8:11]\n"
  "v_mfma_f32_16x16x32_bf16 v[8:11], v[72:75], a[128:131], v[8:11]\n"
  "v_mfma_f32_16x16x32_bf16 v[8:11], v[64:67], a[104:107], v[8:11]\n"
  "s_waitcnt lgkmcnt(2)\n"
  "buffer_store_dwordx4 v[92:95], v109, s[48:51], s68 offen offset:256 nt\n"
  "ds_write_b64 v106, v[28:29] offset:288\n"
  "v_mfma_f32_16x16x32_bf16 v[4:7], v[64:67], a[100:103], v[4:7]\n"
  "s_add_u32 s68, s68, s69\n"
  "v_mfma_f32_16x16x32_bf16 v[4:7], v[72:75], a[124:127], v[4:7]\n"
  "s_mov_b32 s69, s70\n"
  "v_mfma_f32_16x16x32_bf16 v[4:7], v[68:71], a[148:151], v[4:7]\n"
  "s_movk_i32 s70, 6144\n"
  "v_mfma_f32_16x16x32_bf16 v[4:7], v[76:79], a[172:175], v[4:7]\n"
  "v_mfma_f32_16x16x32_bf16 v[0:3], v[76:79], a[168:171], v[0:3]\n"
  "v_mfma_f32_16x16x32_bf16 v[0:3], v[68:71], a[144:147], v[0:3]\n"
  "v_mfma_f32_16x16x32_bf16 v[0:3], v[72:75], a[120:123], v[0:3]\n"
  "v_mfma_f32_16x16x32_bf16 v[0:3], v[64:67], a[96:99], v[0:3]\n"
  "s_waitcnt lgkmcnt(1)\n"
  "v_mfma_f32_16x16x32_bf16 v[0:3], v[48:51], a[192:195], v[0:3]\n"
  "s_add_u32 s72, s72, 1\n"
  "ds_read_b128 v[64:67], v104 offset:12288\n"
  "v_mfma_f32_16x16x32_bf16 v[0:3], v[56:59], a[216:219], v[0:3]\n"
  "s_lshr_b32 s74, s72, 1\n"
  "v_readlane_b32 s73, v126, s74\n"
  "v_mfma_f32_16x16x32_bf16 v[0:3], v[52:55], a[240:243], v[0:3]\n"
  "s_lshl_b32 s74, s72, 4\n"
  "ds_read_b128 v[68:71], v104 offset:14336\n"
  "v_xor_b32 v104, 0x4000, v104\n"
  "v_mfma_f32_16x16x32_bf16 v[0:3], v[60:63], v[136:139], v[0:3]\n"
  "ds_write_b64 v106, v[26:27] offset:576\n"
  "v_mfma_f32_16x16x32_bf16 v[4:7], v[60:63], v[140:143], v[4:7]\n"
  "s_lshr_b32 s73, s73, s74\n"
  "v_mfma_f32_16x16x32_bf16 v[4:7], v[52:55], a[244:247], v[4:7]\n"
  "s_and_b32 s73, s73, 0xffff\n"
  "v_mfma_f32_16x16x32_bf16 v[4:7], v[56:59], a[220:223], v[4:7]\n"
  "s_lshl_b32 s73, s73, 13\n"
  "ds_read_b128 v[72:75], v105 offset:12288\n"
  "v_mfma_f32_16x16x32_bf16 v[4:7], v[48:51], a[196:199], v[4:7]\n"
  "ds_write_b64 v106, v[30:31] offset:864\n"
  "v_mfma_f32_16x16x32_bf16 v[8:11], v[48:51], a[200:203], v[8:11]\n"
  "v_mfma_f32_16x16x32_bf16 v[8:11], v[56:59], a[224:227], v[8:11]\n"
  "v_mfma_f32_16x16x32_bf16 v[8:11], v[52:55], a[248:251], v[8:11]\n"
  "ds_read_b128 v[76:79], v105 offset:14336\n"
  "v_xor_b32 v105, 0x4000, v105\n"
  "v_mfma_f32_16x16x32_bf16 v[8:11], v[60:63], v[144:147], v[8:11]\n"
  "v_smfmac_f32_16x16x64_bf16 v[80:83], v[88:91], v[48:55], v124\n"
  "s_waitcnt lgkmcnt(4)\n"
  "v_smfmac_f32_16x16x64_bf16 v[80:83], v[88:91], v[64:71], v124\n"
  "v_mfma_f32_16x16x32_bf16 v[12:15], v[60:63], v[148:151], v[12:15]\n"
  "v_mfma_f32_16x16x32_bf16 v[12:15], v[52:55], a[252:255], v[12:15]\n"
  "v_mfma_f32_16x16x32_bf16 v[12:15], v[56:59], a[228:231], v[12:15]\n"
  "v_add_f32 v44, v80, v81\n"
  "v_mov_b32 v80, 0\n"
  "v_mfma_f32_16x16x32_bf16 v[12:15], v[48:51], a[204:207], v[12:15]\n"
  "ds_write_b32 v110, v44\n"
  "v_mfma_f32_16x16x32_bf16 v[16:19], v[48:51], a[208:211], v[16:19]\n"
  "v_mov_b32 v81, 0\n"
  "v_mfma_f32_16x16x32_bf16 v[16:19], v[56:59], a[232:235], v[16:19]\n"
  "v_mfma_f32_16x16x32_bf16 v[16:19], v[52:55], v[128:131], v[16:19]\n"
  "v_mfma_f32_16x16x32_bf16 v[16:19], v[60:63], v[152:155], v[16:19]\n"
  "v_xor_b32 v106, 0x2000, v106\n"
  "v_xor_b32 v107, 0x2000, v107\n"
  "v_mfma_f32_16x16x32_bf16 v[20:23], v[60:63], v[156:159], v[20:23]\n"
  "v_xor_b32 v110, 0x2000, v110\n"
  "v_mfma_f32_16x16x32_bf16 v[20:23], v[52:55], v[132:135], v[20:23]\n"
  "v_mfma_f32_16x16x32_bf16 v[20:23], v[56:59], a[236:239], v[20:23]\n"
  "s_sub_u32 s62, s56, 1\n"
  "s_cmp_lt_u32 s62, s61\n"
  "v_mfma_f32_16x16x32_bf16 v[20:23], v[48:51], a[212:215], v[20:23]\n"
  "s_cbranch_scc1 B_H_%=\n"
  "s_cmp_lg_u32 s56, 0\n"
  "s_cbranch_scc1 B_H1_%=\n"
  "s_mov_b32 s52, s53\n"
  "s_mov_b32 s53, s54\n"
  "s_mov_b32 s54, s55\n"
  "s_cmp_eq_u32 s52, 0\n"
  "s_cbranch_scc1 B_LAST_%=\n"
  "s_lshr_b32 s62, s52, 16\n"
  "s_lshl1_add_u32 s62, s62, s65\n"
  "s_mul_i32 s57, s62, 98304\n"
  "s_and_b32 s56, s52, 0xff\n"
  "s_cmp_lg_u32 s53, 0\n"
  "s_cselect_b32 s62, 6, 0\n"
  "s_sub_u32 s56, s56, s62\n"
  "s_waitcnt vmcnt(16) lgkmcnt(0)\n"
  "s_barrier\n"
  "v_mfma_f32_16x16x32_bf16 v[20:23], v[64:67], v[180:183], v[20:23]\n"
  "s_add_u32 m0, s66, 0\n"
  "ds_read_b128 v[48:51], v104 offset:0\n"
  "ds_read_b128 v[84:87], v111\n"
  "v_mfma_f32_16x16x32_bf16 v[20:23], v[72:75], v[204:207], v[20:23]\n"
  "ds_read_b128 v[96:99], v111 offset:64\n"
  "v_xor_b32 v111, 0x2000, v111\n"
  "v_mfma_f32_16x16x32_bf16 v[20:23], v[68:71], v[228:231], v[20:23]\n"
  "ds_read_b128 v[52:55], v104 offset:2048\n"
  "v_mfma_f32_16x16x32_bf16 v[44:47], v[76:79], v[252:255], v[20:23]\n"
  "buffer_load_dwordx4 v112, s[44:47], s67 offen offset:0  lds\n"
  "v_mfma_f32_16x16x32_bf16 v[16:19], v[76:79], v[248:251], v[16:19]\n"
  "s_add_u32 m0, s66, 1920\n"
  "v_mfma_f32_16x16x32_bf16 v[16:19], v[68:71], v[224:227], v[16:19]\n"
  "v_mfma_f32_16x16x32_bf16 v[16:19], v[72:75], v[200:203], v[16:19]\n"
  "ds_read_b128 v[56:59], v105 offset:0\n"
  "v_mfma_f32_16x16x32_bf16 v[40:43], v[64:67], v[176:179], v[16:19]\n"
  "buffer_load_dwordx4 v112, s[44:47], s67 offen offset:128  lds\n"
  "v_mfma_f32_16x16x32_bf16 v[12:15], v[64:67], v[172:175], v[12:15]\n"
  "s_add_u32 m0, s66, 3840\n"
  "v_mfma_f32_16x16x32_bf16 v[12:15], v[72:75], v[196:199], v[12:15]\n"
  "v_mfma_f32_16x16x32_bf16 v[12:15], v[68:71], v[220:223], v[12:15]\n"
  "s_waitcnt lgkmcnt(2)\n"
  "ds_read_b128 v[60:63], v105 offset:2048\n"
  "v_add_f32 v84, v84, v96\n"
  "v_mfma_f32_16x16x32_bf16 v[36:39], v[76:79], v[244:247], v[12:15]\n"
  "buffer_load_dwordx4 v112, s[44:47], s67 offen offset:256  lds\n"
  "v_mfma_f32_16x16x32_bf16 v[8:11], v[76:79], v[240:243], v[8:11]\n"
  "v_add_f32 v85, v85, v97\n"
  "v_add_f32 v86, v86, v98\n"
  "v_mfma_f32_16x16x32_bf16 v[8:11], v[68:71], v[216:219], v[8:11]\n"
  "v_add_f32 v87, v87, v99\n"
  "v_max3_f32 v82, v82, v84, v85\n"
  "v_mfma_f32_16x16x32_bf16 v[8:11], v[72:75], v[192:195], v[8:11]\n"
  "v_rcp_f32 v84, v84\n"
  "v_mfma_f32_16x16x32_bf16 v[32:35], v[64:67], v[168:171], v[8:11]\n"
  "v_rcp_f32 v85, v85\n"
  "v_mfma_f32_16x16x32_bf16 v[4:7], v[64:67], v[164:167], v[4:7]\n"
  "v_max3_f32 v82, v82, v86, v87\n"
  "v_max3_f32 v83, v83, v84, v85\n"
  "v_mfma_f32_16x16x32_bf16 v[4:7], v[72:75], v[188:191], v[4:7]\n"
  "v_rcp_f32 v86, v86\n"
  "v_mfma_f32_16x16x32_bf16 v[4:7], v[68:71], v[212:215], v[4:7]\n"
  "v_rcp_f32 v87, v87\n"
  "v_mfma_f32_16x16x32_bf16 v[28:31], v[76:79], v[236:239], v[4:7]\n"
  "v_max3_f32 v83, v83, v86, v87\n"
  "v_mul_f32 v96, s99, v84\n"
  "v_mfma_f32_16x16x32_bf16 v[0:3], v[76:79], v[232:235], v[0:3]\n"
  "s_add_u32 m0, s60, 0\n"
  "v_mul_f32 v97, s99, v85\n"
  "v_mul_f32 v98, s99, v86\n"
  "v_mfma_f32_16x16x32_bf16 v[0:3], v[68:71], v[208:211], v[0:3]\n"
  "buffer_load_dwordx4 v112, s[76:79], s57 offen offset:0  lds\n"
  "v_mfma_f32_16x16x32_bf16 v[0:3], v[72:75], v[184:187], v[0:3]\n"
  "v_mul_f32 v99, s99, v87\n"
  "v_fma_f32 v40, v40, v84, v96\n"
  "v_mfma_f32_16x16x32_bf16 v[24:27], v[64:67], v[160:163], v[0:3]\n"
  "s_waitcnt lgkmcnt(0)\n"
  "v_fma_f32 v44, v44, v84, v96\n"
  "v_fma_f32 v41, v41, v85, v97\n"
  "v_mfma_f32_16x16x32_bf16 v[0:3], v[48:51], a[0:3], v[100:103]\n"
  "ds_read_b128 v[64:67], v104 offset:4096\n"
  "v_fma_f32 v45, v45, v85, v97\n"
  "v_mfma_f32_16x16x32_bf16 v[0:3], v[56:59], a[24:27], v[0:3]\n"
  "v_fma_f32 v42, v42, v86, v98\n"
  "v_fma_f32 v46, v46, v86, v98\n"
  "v_mfma_f32_16x16x32_bf16 v[0:3], v[52:55], a[48:51], v[0:3]\n"
  "ds_read_b128 v[68:71], v104 offset:6144\n"
  "v_fma_f32 v43, v43, v87, v99\n"
  "v_mfma_f32_16x16x32_bf16 v[0:3], v[60:63], a[72:75], v[0:3]\n"
  "ds_read_b64_tr_b16 v[92:93], v108 offset:0\n"
  "ds_read_b64_tr_b16 v[94:95], v108 offset:32\n"
  "v_mfma_f32_16x16x32_bf16 v[4:7], v[60:63], a[76:79], v[100:103]\n"
  "buffer_load_dwordx4 v113, s[76:79], s57 offen offset:1024  lds\n"
  "v_mfma_f32_16x16x32_bf16 v[4:7], v[52:55], a[52:55], v[4:7]\n"
  "v_fma_f32 v47, v47, v87, v99\n"
  "v_fma_f32 v24, v24, v84, v96\n"
  "v_mfma_f32_16x16x32_bf16 v[4:7], v[56:59], a[28:31], v[4:7]\n"
  "ds_read_b128 v[72:75], v105 offset:4096\n"
  "v_fma_f32 v25, v25, v85, v97\n"
  "v_mfma_f32_16x16x32_bf16 v[4:7], v[48:51], a[4:7], v[4:7]\n"
  "v_fma_f32 v28, v28, v84, v96\n"
  "v_fma_f32 v29, v29, v85, v97\n"
  "v_mfma_f32_16x16x32_bf16 v[8:11], v[48:51], a[8:11], v[100:103]\n"
  "v_fma_f32 v32, v32, v84, v96\n"
  "v_fma_f32 v33, v33, v85, v97\n"
  "v_mfma_f32_16x16x32_bf16 v[8:11], v[56:59], a[32:35], v[8:11]\n"
  "v_fma_f32 v36, v36, v84, v96\n"
  "v_fma_f32 v37, v37, v85, v97\n"
  "v_mfma_f32_16x16x32_bf16 v[8:11], v[52:55], a[56:59], v[8:11]\n"
  "s_add_u32 m0, s60, 1920\n"
  "ds_read_b128 v[76:79], v105 offset:6144\n"
  "v_fma_f32 v26, v26, v86, v98\n"
  "v_mfma_f32_16x16x32_bf16 v[8:11], v[60:63], a[80:83], v[8:11]\n"
  "buffer_load_dwordx4 v112, s[76:79], s57 offen offset:128  lds\n"
  "v_smfmac_f32_16x16x64_bf16 v[80:83], v[88:91], v[48:55], v124\n"
  "s_waitcnt lgkmcnt(4)\n"
  "v_fma_f32 v27, v27, v87, v99\n"
  "v_smfmac_f32_16x16x64_bf16 v[80:83], v[88:91], v[64:71], v124\n"
  "s_waitcnt lgkmcnt(2)\n"
  "buffer_store_dwordx4 v[92:95], v109, s[48:51], s68 offen offset:0 nt\n"
  "v_fma_f32 v30, v30, v86, v98\n"
  "v_fma_f32 v31, v31, v87, v99\n"
  "v_mfma_f32_16x16x32_bf16 v[12:15], v[60:63], a[84:87], v[100:103]\n"
  "v_fma_f32 v34, v34, v86, v98\n"
  "v_fma_f32 v35, v35, v87, v99\n"
  "v_mfma_f32_16x16x32_bf16 v[12:15], v[52:55], a[60:63], v[12:15]\n"
  "v_fma_f32 v38, v38, v86, v98\n"
  "v_fma_f32 v39, v39, v87, v99\n"
  "v_mfma_f32_16x16x32_bf16 v[12:15], v[56:59], a[36:39], v[12:15]\n"
  "v_cvt_pk_bf16_f32 v40, v40, v44\n"
  "v_cvt_pk_bf16_f32 v41, v41, v45\n"
  "v_mfma_f32_16x16x32_bf16 v[12:15], v[48:51], a[12:15], v[12:15]\n"
  "v_cvt_pk_bf16_f32 v42, v42, v46\n"
  "v_cvt_pk_bf16_f32 v43, v43, v47\n"
  "v_mfma_f32_16x16x32_bf16 v[16:19], v[48:51], a[16:19], v[100:103]\n"
  "buffer_load_dwordx4 v113, s[76:79], s57 offen offset:1152  lds\n"
  "v_mfma_f32_16x16x32_bf16 v[16:19], v[56:59], a[40:43], v[16:19]\n"
  "ds_read_b64_tr_b16 v[96:97], v108 offset:2304\n"
  "ds_read_b64_tr_b16 v[98:99], v108 offset:2336\n"
  "v_mfma_f32_16x16x32_bf16 v[16:19], v[52:55], a[64:67], v[16:19]\n"
  "v_cvt_pk_bf16_f32 v24, v24, v28\n"
  "v_cvt_pk_bf16_f32 v28, v25, v29\n"
  "v_mfma_f32_16x16x32_bf16 v[16:19], v[60:63], a[88:91], v[16:19]\n"
  "v_cvt_pk_bf16_f32 v25, v32, v36\n"
  "v_cvt_pk_bf16_f32 v29, v33, v37\n"
  "v_mfma_f32_16x16x32_bf16 v[20:23], v[60:63], a[92:95], v[100:103]\n"
  "v_cvt_pk_bf16_f32 v26, v26, v30\n"
  "v_cvt_pk_bf16_f32 v30, v27, v31\n"
  "v_mfma_f32_16x16x32_bf16 v[20:23], v[52:55], a[68:71], v[20:23]\n"
  "v_cvt_pk_bf16_f32 v27, v34, v38\n"
  "v_cvt_pk_bf16_f32 v31, v35, v39\n"
  "v_mfma_f32_16x16x32_bf16 v[20:23], v[56:59], a[44:47], v[20:23]\n"
  "s_add_u32 m0, s60, 3840\n"
  "v_mfma_f32_16x16x32_bf16 v[20:23], v[48:51], a[20:23], v[20:23]\n"
  "s_waitcnt lgkmcnt(2)\n"
  "buffer_load_dwordx4 v112, s[76:79], s57 offen offset:256  lds\n"
  "v_mfma_f32_16x16x32_bf16 v[20:23], v[64:67], a[116:119], v[20:23]\n"
  "s_add_u32 s66, s66, 6144\n"
  "ds_read_b128 v[48:51], v104 offset:8192\n"
  "v_mfma_f32_16x16x32_bf16 v[20:23], v[72:75], a[140:143], v[20:23]\n"
  "s_cmp_ge_u32 s66, 86016\n"
  "s_cselect_b32 s64, 36864, 0\n"
  "v_mfma_f32_16x16x32_bf16 v[20:23], v[68:71], a[164:167], v[20:23]\n"
  "s_sub_u32 s66, s66, s64\n"
  "ds_read_b128 v[52:55], v104 offset:10240\n"
  "v_mfma_f32_16x16x32_bf16 v[20:23], v[76:79], a[188:191], v[20:23]\n"
  "s_sub_u32 s90, s90, 1\n"
  "s_cselect_b32 s63, s91, 6144\n"
  "s_cmov_b64 s[90:91], s[92:93]\n"
  "s_cmov_b64 s[92:93], s[94:95]\n"
  "s_waitcnt lgkmcnt(2)\n"
  "buffer_store_dwordx4 v[96:99], v109, s[48:51], s68 offen offset:128 nt\n"
  "ds_write2_b32 v107, v40, v41 offset0:0 offset1:72\n"
  "v_mfma_f32_16x16x32_bf16 v[16:19], v[76:79], a[184:187], v[16:19]\n"
  "s_add_u32 s67, s67, s63\n"
  "v_mfma_f32_16x16x32_bf16 v[16:19], v[68:71], a[160:163], v[16:19]\n"
  "ds_read_b64_tr_b16 v[92:93], v108 offset:4608\n"
  "ds_read_b64_tr_b16 v[94:95], v108 offset:4640\n"
  "v_mfma_f32_16x16x32_bf16 v[16:19], v[72:75], a[136:139], v[16:19]\n"
  "buffer_load_dwordx4 v113, s[76:79], s57 offen offset:1280  lds\n"
  "ds_read_b128 v[56:59], v105 offset:8192\n"
  "v_mfma_f32_16x16x32_bf16 v[16:19], v[64:67], a[112:115], v[16:19]\n"
  "s_add_u32 s57, s57, 6144\n"
  "ds_write2_b32 v107, v42, v43 offset0:144 offset1:216\n"
  "v_mfma_f32_16x16x32_bf16 v[12:15], v[64:67], a[108:111], v[12:15]\n"
  "v_xor_b32 v108, 0x2000, v108\n"
  "v_mfma_f32_16x16x32_bf16 v[12:15], v[72:75], a[132:135], v[12:15]\n"
  "v_mfma_f32_16x16x32_bf16 v[12:15], v[68:71], a[156:159], v[12:15]\n"
  "ds_read_b128 v[60:63], v105 offset:10240\n"
  "v_mfma_f32_16x16x32_bf16 v[12:15], v[76:79], a[180:183], v[12:15]\n"
  "ds_write_b64 v106, v[24:25] offset:0\n"
  "v_mfma_f32_16x16x32_bf16 v[8:11], v[76:79], a[176:179], v[8:11]\n"
  "s_add_u32 m0, s60, 5888\n"
  "v_mfma_f32_16x16x32_bf16 v[8:11], v[68:71], a[152:155], v[8:11]\n"
  "buffer_load_dwordx4 v112, s[76:79], s57 offen offset:256  lds\n"
  "v_mfma_f32_16x16x32_bf16 v[8:11], v[72:75], a[128:131], v[8:11]\n"
  "v_mfma_f32_16x16x32_bf16 v[8:11], v[64:67], a[104:107], v[8:11]\n"
  "s_waitcnt lgkmcnt(4)\n"
  "buffer_store_dwordx4 v[92:95], v109, s[48:51], s68 offen offset:256 nt\n"
  "ds_write_b64 v106, v[28:29] offset:288\n"
  "v_mfma_f32_16x16x32_bf16 v[4:7], v[64:67], a[100:103], v[4:7]\n"
  "s_add_u32 s68, s68, s69\n"
  "v_mfma_f32_16x16x32_bf16 v[4:7], v[72:75], a[124:127], v[4:7]\n"
  "s_mov_b32 s69, s70\n"
  "v_mfma_f32_16x16x32_bf16 v[4:7], v[68:71], a[148:151], v[4:7]\n"
  "s_movk_i32 s70, 6144\n"
  "v_mfma_f32_16x16x32_bf16 v[4:7], v[76:79], a[172:175], v[4:7]\n"
  "ds_write_b64 v106, v[26:27] offset:576\n"
  "v_mfma_f32_16x16x32_bf16 v[0:3], v[76:79], a[168:171], v[0:3]\n"
  "buffer_load_dwordx4 v113, s[76:79], s57 offen offset:1280  lds\n"
  "v_mfma_f32_16x16x32_bf16 v[0:3], v[68:71], a[144:147], v[0:3]\n"
  "v_mfma_f32_16x16x32_bf16 v[0:3], v[72:75], a[120:123], v[0:3]\n"
  "v_mfma_f32_16x16x32_bf16 v[0:3], v[64:67], a[96:99], v[0:3]\n"
  "s_waitcnt lgkmcnt(3)\n"
  "v_mfma_f32_16x16x32_bf16 v[0:3], v[48:51], a[192:195], v[0:3]\n"
  "ds_read_b128 v[64:67], v104 offset:12288\n"
  "v_mfma_f32_16x16x32_bf16 v[0:3], v[56:59], a[216:219], v[0:3]\n"
  "v_mfma_f32_16x16x32_bf16 v[0:3], v[52:55], a[240:243], v[0:3]\n"
  "s_add_u32 m0, s60, 8064\n"
  "ds_read_b128 v[68:71], v104 offset:14336\n"
  "v_xor_b32 v104, 0x4000, v104\n"
  "v_mfma_f32_16x16x32_bf16 v[0:3], v[60:63], v[136:139], v[0:3]\n"
  "buffer_load_dwordx4 v112, s[76:79], s57 offen offset:128  lds\n"
  "v_mfma_f32_16x16x32_bf16 v[4:7], v[60:63], v[140:143], v[4:7]\n"
  "v_mfma_f32_16x16x32_bf16 v[4:7], v[52:55], a[244:247], v[4:7]\n"
  "v_mfma_f32_16x16x32_bf16 v[4:7], v[56:59], a[220:223], v[4:7]\n"
  "ds_read_b128 v[72:75], v105 offset:12288\n"
  "v_mfma_f32_16x16x32_bf16 v[4:7], v[48:51], a[196:199], v[4:7]\n"
  "ds_write_b64 v106, v[30:31] offset:864\n"
  "v_mfma_f32_16x16x32_bf16 v[8:11], v[48:51], a[200:203], v[8:11]\n"
  "v_mfma_f32_16x16x32_bf16 v[8:11], v[56:59], a[224:227], v[8:11]\n"
  "v_mfma_f32_16x16x32_bf16 v[8:11], v[52:55], a[248:251], v[8:11]\n"
  "buffer_load_dwordx4 v113, s[76:79], s57 offen offset:1152  lds\n"
  "ds_read_b128 v[76:79], v105 offset:14336\n"
  "v_mfma_f32_16x16x32_bf16 v[8:11], v[60:63], v[144:147], v[8:11]\n"
  "v_xor_b32 v105, 0x4000, v105\n"
  "v_smfmac_f32_16x16x64_bf16 v[80:83], v[88:91], v[48:55], v124\n"
  "s_waitcnt lgkmcnt(3)\n"
  "v_smfmac_f32_16x16x64_bf16 v[80:83], v[88:91], v[64:71], v124\n"
  "v_mfma_f32_16x16x32_bf16 v[12:15], v[60:63], v[148:151], v[12:15]\n"
  "v_mfma_f32_16x16x32_bf16 v[12:15], v[52:55], a[252:255], v[12:15]\n"
  "v_mfma_f32_16x16x32_bf16 v[12:15], v[56:59], a[228:231], v[12:15]\n"
  "v_add_f32 v44, v80, v81\n"
  "v_mov_b32 v80, 0\n"
  "v_mfma_f32_16x16x32_bf16 v[12:15], v[48:51], a[204:207], v[12:15]\n"
  "ds_write_b32 v110, v44\n"
  "v_mfma_f32_16x16x32_bf16 v[16:19], v[48:51], a[208:211], v[16:19]\n"
  "v_mov_b32 v81, 0\n"
  "v_mfma_f32_16x16x32_bf16 v[16:19], v[56:59], a[232:235], v[16:19]\n"
  "v_mfma_f32_16x16x32_bf16 v[16:19], v[52:55], v[128:131], v[16:19]\n"
  "v_mfma_f32_16x16x32_bf16 v[16:19], v[60:63], v[152:155], v[16:19]\n"
  "v_xor_b32 v106, 0x2000, v106\n"
  "v_xor_b32 v107, 0x2000, v107\n"
  "v_mfma_f32_16x16x32_bf16 v[20:23], v[60:63], v[156:159], v[20:23]\n"
  "v_xor_b32 v110, 0x2000, v110\n"
  "v_mfma_f32_16x16x32_bf16 v[20:23], v[52:55], v[132:135], v[20:23]\n"
  "v_mfma_f32_16x16x32_bf16 v[20:23], v[56:59], a[236:239], v[20:23]\n"
  "v_mfma_f32_16x16x32_bf16 v[20:23], v[48:51], a[212:215], v[20:23]\n"
  "s_waitcnt vmcnt(29) lgkmcnt(0)\n"
  "s_barrier\n"
  "v_mfma_f32_16x16x32_bf16 v[20:23], v[64:67], v[180:183], v[20:23]\n"
  "s_add_u32 m0, s66, 0\n"
  "ds_read_b128 v[48:51], v104 offset:0\n"
  "ds_read_b128 v[84:87], v111\n"
  "v_mfma_f32_16x16x32_bf16 v[20:23], v[72:75], v[204:207], v[20:23]\n"
  "ds_read_b128 v[96:99], v111 offset:64\n"
  "v_xor_b32 v111, 0x2000, v111\n"
  "v_mfma_f32_16x16x32_bf16 v[20:23], v[68:71], v[228:231], v[20:23]\n"
  "ds_read_b128 v[52:55], v104 offset:2048\n"
  "v_mfma_f32_16x16x32_bf16 v[44:47], v[76:79], v[252:255], v[20:23]\n"
  "buffer_load_dwordx4 v112, s[44:47], s67 offen offset:0  lds\n"
  "v_mfma_f32_16x16x32_bf16 v[16:19], v[76:79], v[248:251], v[16:19]\n"
  "s_add_u32 m0, s66, 1920\n"
  "v_mfma_f32_16x16x32_bf16 v[16:19], v[68:71], v[224:227], v[16:19]\n"
  "v_mfma_f32_16x16x32_bf16 v[16:19], v[72:75], v[200:203], v[16:19]\n"
  "ds_read_b128 v[56:59], v105 offset:0\n"
  "v_mfma_f32_16x16x32_bf16 v[40:43], v[64:67], v[176:179], v[16:19]\n"
  "buffer_load_dwordx4 v112, s[44:47], s67 offen offset:128  lds\n"
  "v_mfma_f32_16x16x32_bf16 v[12:15], v[64:67], v[172:175], v[12:15]\n"
  "s_add_u32 m0, s66, 3840\n"
  "v_mfma_f32_16x16x32_bf16 v[12:15], v[72:75], v[196:199], v[12:15]\n"
  "v_mfma_f32_16x16x32_bf16 v[12:15], v[68:71], v[220:223], v[12:15]\n"
  "s_waitcnt lgkmcnt(2)\n"
  "ds_read_b128 v[60:63], v105 offset:2048\n"
  "v_add_f32 v84, v84, v96\n"
  "v_mfma_f32_16x16x32_bf16 v[36:39], v[76:79], v[244:247], v[12:15]\n"
  "buffer_load_dwordx4 v112, s[44:47], s67 offen offset:256  lds\n"
  "v_mfma_f32_16x16x32_bf16 v[8:11], v[76:79], v[240:243], v[8:11]\n"
  "v_add_f32 v85, v85, v97\n"
  "v_add_f32 v86, v86, v98\n"
  "v_mfma_f32_16x16x32_bf16 v[8:11], v[68:71], v[216:219], v[8:11]\n"
  "v_add_f32 v87, v87, v99\n"
  "v_max3_f32 v82, v82, v84, v85\n"
  "v_mfma_f32_16x16x32_bf16 v[8:11], v[72:75], v[192:195], v[8:11]\n"
  "v_rcp_f32 v84, v84\n"
  "v_mfma_f32_16x16x32_bf16 v[32:35], v[64:67], v[168:171], v[8:11]\n"
  "v_rcp_f32 v85, v85\n"
  "v_mfma_f32_16x16x32_bf16 v[4:7], v[64:67], v[164:167], v[4:7]\n"
  "v_max3_f32 v82, v82, v86, v87\n"
  "v_max3_f32 v83, v83, v84, v85\n"
  "v_mfma_f32_16x16x32_bf16 v[4:7], v[72:75], v[188:191], v[4:7]\n"
  "v_rcp_f32 v86, v86\n"
  "v_mfma_f32_16x16x32_bf16 v[4:7], v[68:71], v[212:215], v[4:7]\n"
  "v_rcp_f32 v87, v87\n"
  "v_mfma_f32_16x16x32_bf16 v[28:31], v[76:79], v[236:239], v[4:7]\n"
  "v_max3_f32 v83, v83, v86, v87\n"
  "v_mul_f32 v96, s99, v84\n"
  "v_mfma_f32_16x16x32_bf16 v[0:3], v[76:79], v[232:235], v[0:3]\n"
  "s_add_u32 m0, s60, 10240\n"
  "v_mul_f32 v97, s99, v85\n"
  "v_mul_f32 v98, s99, v86\n"
  "v_mfma_f32_16x16x32_bf16 v[0:3], v[68:71], v[208:211], v[0:3]\n"
  "buffer_load_dwordx4 v112, s[76:79], s57 offen offset:0  lds\n"
  "v_mfma_f32_16x16x32_bf16 v[0:3], v[72:75], v[184:187], v[0:3]\n"
  "v_mul_f32 v99, s99, v87\n"
  "v_fma_f32 v40, v40, v84, v96\n"
  "v_mfma_f32_16x16x32_bf16 v[24:27], v[64:67], v[160:163], v[0:3]\n"
  "s_waitcnt lgkmcnt(0)\n"
  "v_fma_f32 v44, v44, v84, v96\n"
  "v_fma_f32 v41, v41, v85, v97\n"
  "v_mfma_f32_16x16x32_bf16 v[0:3], v[48:51], a[0:3], v[100:103]\n"
  "ds_read_b128 v[64:67], v104 offset:4096\n"
  "v_fma_f32 v45, v45, v85, v97\n"
  "v_mfma_f32_16x16x32_bf16 v[0:3], v[56:59], a[24:27], v[0:3]\n"
  "v_fma_f32 v42, v42, v86, v98\n"
  "v_fma_f32 v46, v46, v86, v98\n"
  "v_mfma_f32_16x16x32_bf16 v[0:3], v[52:55], a[48:51], v[0:3]\n"
  "ds_read_b128 v[68:71], v104 offset:6144\n"
  "v_fma_f32 v43, v43, v87, v99\n"
  "v_mfma_f32_16x16x32_bf16 v[0:3], v[60:63], a[72:75], v[0:3]\n"
  "buffer_load_dwordx4 v113, s[76:79], s57 offen offset:1024  lds\n"
  "v_mfma_f32_16x16x32_bf16 v[4:7], v[60:63], a[76:79], v[100:103]\n"
  "s_add_u32 s57, s57, 6144\n"
  "ds_read_b64_tr_b16 v[92:93], v108 offset:0\n"
  "ds_read_b64_tr_b16 v[94:95], v108 offset:32\n"
  "v_mfma_f32_16x16x32_bf16 v[4:7], v[52:55], a[52:55], v[4:7]\n"
  "v_fma_f32 v47, v47, v87, v99\n"
  "v_fma_f32 v24, v24, v84, v96\n"
  "v_mfma_f32_16x16x32_bf16 v[4:7], v[56:59], a[28:31], v[4:7]\n"
  "ds_read_b128 v[72:75], v105 offset:4096\n"
  "v_fma_f32 v25, v25, v85, v97\n"
  "v_mfma_f32_16x16x32_bf16 v[4:7], v[48:51], a[4:7], v[4:7]\n"
  "v_fma_f32 v28, v28, v84, v96\n"
  "v_fma_f32 v29, v29, v85, v97\n"
  "v_mfma_f32_16x16x32_bf16 v[8:11], v[48:51], a[8:11], v[100:103]\n"
  "s_add_u32 m0, s60, 12288\n"
  "v_fma_f32 v32, v32, v84, v96\n"
  "v_fma_f32 v33, v33, v85, v97\n"
  "v_mfma_f32_16x16x32_bf16 v[8:11], v[56:59], a[32:35], v[8:11]\n"
  "buffer_load_dwordx4 v112, s[76:79], s57 offen offset:0  lds\n"
  "v_mfma_f32_16x16x32_bf16 v[8:11], v[52:55], a[56:59], v[8:11]\n"
  "ds_read_b128 v[76:79], v105 offset:6144\n"
  "v_fma_f32 v36, v36, v84, v96\n"
  "v_mfma_f32_16x16x32_bf16 v[8:11], v[60:63], a[80:83], v[8:11]\n"
  "v_fma_f32 v37, v37, v85, v97\n"
  "v_fma_f32 v26, v26, v86, v98\n"
  "v_smfmac_f32_16x16x64_bf16 v[80:83], v[88:91], v[48:55], v124\n"
  "s_waitcnt lgkmcnt(4)\n"
  "v_fma_f32 v27, v27, v87, v99\n"
  "v_smfmac_f32_16x16x64_bf16 v[80:83], v[88:91], v[64:71], v124\n"
  "s_waitcnt lgkmcnt(2)\n"
  "buffer_store_dwordx4 v[92:95], v109, s[48:51], s68 offen offset:0 nt\n"
  "v_fma_f32 v30, v30, v86, v98\n"
  "v_fma_f32 v31, v31, v87, v99\n"
  "v_mfma_f32_16x16x32_bf16 v[12:15], v[60:63], a[84:87], v[100:103]\n"
  "v_fma_f32 v34, v34, v86, v98\n"
  "v_fma_f32 v35, v35, v87, v99\n"
  "v_mfma_f32_16x16x32_bf16 v[12:15], v[52:55], a[60:63], v[12:15]\n"
  "buffer_load_dwordx4 v113, s[76:79], s57 offen offset:1024  lds\n"
  "v_mfma_f32_16x16x32_bf16 v[12:15], v[56:59], a[36:39], v[12:15]\n"
  "v_fma_f32 v38, v38, v86, v98\n"
  "v_fma_f32 v39, v39, v87, v99\n"
  "v_mfma_f32_16x16x32_bf16 v[12:15], v[48:51], a[12:15], v[12:15]\n"
  "v_cvt_pk_bf16_f32 v40, v40, v44\n"
  "v_cvt_pk_bf16_f32 v41, v41, v45\n"
  "v_mfma_f32_16x16x32_bf16 v[16:19], v[48:51], a[16:19], v[100:103]\n"
  "v_cvt_pk_bf16_f32 v42, v42, v46\n"
  "v_cvt_pk_bf16_f32 v43, v43, v47\n"
  "v_mfma_f32_16x16x32_bf16 v[16:19], v[56:59], a[40:43], v[16:19]\n"
  "ds_read_b64_tr_b16 v[96:97], v108 offset:2304\n"
  "ds_read_b64_tr_b16 v[98:99], v108 offset:2336\n"
  "v_mfma_f32_16x16x32_bf16 v[16:19], v[52:55], a[64:67], v[16:19]\n"
  "s_add_u32 m0, s60, 14208\n"
  "v_cvt_pk_bf16_f32 v24, v24, v28\n"
  "v_cvt_pk_bf16_f32 v28, v25, v29\n"
  "v_mfma_f32_16x16x32_bf16 v[16:19], v[60:63], a[88:91], v[16:19]\n"
  "buffer_load_dwordx4 v112, s[76:79], s57 offen offset:128  lds\n"
  "v_mfma_f32_16x16x32_bf16 v[20:23], v[60:63], a[92:95], v[100:103]\n"
  "v_cvt_pk_bf16_f32 v25, v32, v36\n"
  "v_cvt_pk_bf16_f32 v29, v33, v37\n"
  "v_mfma_f32_16x16x32_bf16 v[20:23], v[52:55], a[68:71], v[20:23]\n"
  "v_cvt_pk_bf16_f32 v26, v26, v30\n"
  "v_cvt_pk_bf16_f32 v30, v27, v31\n"
  "v_mfma_f32_16x16x32_bf16 v[20:23], v[56:59], a[44:47], v[20:23]\n"
  "v_cvt_pk_bf16_f32 v27, v34, v38\n"
  "v_cvt_pk_bf16_f32 v31, v35, v39\n"
  "v_mfma_f32_16x16x32_bf16 v[20:23], v[48:51], a[20:23], v[20:23]\n"
  "s_waitcnt lgkmcnt(2)\n"
  "v_mfma_f32_16x16x32_bf16 v[20:23], v[64:67], a[116:119], v[20:23]\n"
  "s_add_u32 s66, s66, 6144\n"
  "ds_read_b128 v[48:51], v104 offset:8192\n"
  "v_mfma_f32_16x16x32_bf16 v[20:23], v[72:75], a[140:143], v[20:23]\n"
  "s_cmp_ge_u32 s66, 86016\n"
  "s_cselect_b32 s64, 36864, 0\n"
  "buffer_load_dwordx4 v113, s[76:79], s57 offen offset:1152  lds\n"
  "v_mfma_f32_16x16x32_bf16 v[20:23], v[68:71], a[164:167], v[20:23]\n"
  "s_sub_u32 s66, s66, s64\n"
  "ds_read_b128 v[52:55], v104 offset:10240\n"
  "v_mfma_f32_16x16x32_bf16 v[20:23], v[76:79], a[188:191], v[20:23]\n"
  "s_sub_u32 s90, s90, 1\n"
  "s_cselect_b32 s63, s91, 6144\n"
  "s_cmov_b64 s[90:91], s[92:93]\n"
  "s_cmov_b64 s[92:93], s[94:95]\n"
  "s_waitcnt lgkmcnt(2)\n"
  "buffer_store_dwordx4 v[96:99], v109, s[48:51], s68 offen offset:128 nt\n"
  "ds_write2_b32 v107, v40, v41 offset0:0 offset1:72\n"
  "v_mfma_f32_16x16x32_bf16 v[16:19], v[76:79], a[184:187], v[16:19]\n"
  "s_add_u32 s67, s67, s63\n"
  "v_mfma_f32_16x16x32_bf16 v[16:19], v[68:71], a[160:163], v[16:19]\n"
  "ds_read_b64_tr_b16 v[92:93], v108 offset:4608\n"
  "ds_read_b64_tr_b16 v[94:95], v108 offset:4640\n"
  "v_mfma_f32_16x16x32_bf16 v[16:19], v[72:75], a[136:139], v[16:19]\n"
  "s_add_u32 m0, s60, 16128\n"
  "ds_read_b128 v[56:59], v105 offset:8192\n"
  "v_xor_b32 v108, 0x2000, v108\n"
  "v_mfma_f32_16x16x32_bf16 v[16:19], v[64:67], a[112:115], v[16:19]\n"
  "buffer_load_dwordx4 v112, s[76:79], s57 offen offset:256  lds\n"
  "v_mfma_f32_16x16x32_bf16 v[12:15], v[64:67], a[108:111], v[12:15]\n"
  "v_mfma_f32_16x16x32_bf16 v[12:15], v[72:75], a[132:135], v[12:15]\n"
  "v_mfma_f32_16x16x32_bf16 v[12:15], v[68:71], a[156:159], v[12:15]\n"
  "ds_read_b128 v[60:63], v105 offset:10240\n"
  "v_mfma_f32_16x16x32_bf16 v[12:15], v[76:79], a[180:183], v[12:15]\n"
  "ds_write2_b32 v107, v42, v43 offset0:144 offset1:216\n"
  "v_mfma_f32_16x16x32_bf16 v[8:11], v[76:79], a[176:179], v[8:11]\n"
  "v_mfma_f32_16x16x32_bf16 v[8:11], v[68:71], a[152:155], v[8:11]\n"
  "buffer_load_dwordx4 v113, s[76:79], s57 offen offset:1280  lds\n"
  "v_mfma_f32_16x16x32_bf16 v[8:11], v[72:75], a[128:131], v[8:11]\n"
  "s_sub_u32 s57, s57, s85\n"
  "v_mfma_f32_16x16x32_bf16 v[8:11], v[64:67], a[104:107], v[8:11]\n"
  "s_waitcnt lgkmcnt(3)\n"
  "buffer_store_dwordx4 v[92:95], v109, s[48:51], s68 offen offset:256 nt\n"
  "ds_write_b64 v106, v[24:25] offset:0\n"
  "v_mfma_f32_16x16x32_bf16 v[4:7], v[64:67], a[100:103], v[4:7]\n"
  "s_add_u32 s68, s68, s69\n"
  "v_mfma_f32_16x16x32_bf16 v[4:7], v[72:75], a[124:127], v[4:7]\n"
  "s_mov_b32 s69, s70\n"
  "v_mfma_f32_16x16x32_bf16 v[4:7], v[68:71], a[148:151], v[4:7]\n"
  "s_add_u32 m0, s59, 0\n"
  "v_mfma_f32_16x16x32_bf16 v[4:7], v[76:79], a[172:175], v[4:7]\n"
  "s_movk_i32 s70, 6144\n"
  "buffer_load_dwordx4 v114, s[40:43], s57 offen offset:0  lds\n"
  "v_mfma_f32_16x16x32_bf16 v[0:3], v[76:79], a[168:171], v[0:3]\n"
  "v_mfma_f32_16x16x32_bf16 v[0:3], v[68:71], a[144:147], v[0:3]\n"
  "v_mfma_f32_16x16x32_bf16 v[0:3], v[72:75], a[120:123], v[0:3]\n"
  "v_mfma_f32_16x16x32_bf16 v[0:3], v[64:67], a[96:99], v[0:3]\n"
  "s_waitcnt lgkmcnt(2)\n"
  "v_mfma_f32_16x16x32_bf16 v[0:3], v[48:51], a[192:195], v[0:3]\n"
  "ds_read_b128 v[64:67], v104 offset:12288\n"
  "v_mfma_f32_16x16x32_bf16 v[0:3], v[56:59], a[216:219], v[0:3]\n"
  "buffer_load_dwordx4 v115, s[40:43], s57 offen offset:1024  lds\n"
  "v_mfma_f32_16x16x32_bf16 v[0:3], v[52:55], a[240:243], v[0:3]\n"
  "ds_read_b128 v[68:71], v104 offset:14336\n"
  "v_xor_b32 v104, 0x4000, v104\n"
  "v_mfma_f32_16x16x32_bf16 v[0:3], v[60:63], v[136:139], v[0:3]\n"
  "ds_write_b64 v106, v[28:29] offset:288\n"
  "v_mfma_f32_16x16x32_bf16 v[4:7], v[60:63], v[140:143], v[4:7]\n"
  "v_mfma_f32_16x16x32_bf16 v[4:7], v[52:55], a[244:247], v[4:7]\n"
  "v_mfma_f32_16x16x32_bf16 v[4:7], v[56:59], a[220:223], v[4:7]\n"
  "ds_read_b128 v[72:75], v105 offset:12288\n"
  "v_mfma_f32_16x16x32_bf16 v[4:7], v[48:51], a[196:199], v[4:7]\n"
  "buffer_load_dwordx4 v116, s[40:43], s57 offen offset:2048  lds\n"
  "v_mfma_f32_16x16x32_bf16 v[8:11], v[48:51], a[200:203], v[8:11]\n"
  "s_add_u32 s57, s57, 6144\n"
  "v_mfma_f32_16x16x32_bf16 v[8:11], v[56:59], a[224:227], v[8:11]\n"
  "v_mfma_f32_16x16x32_bf16 v[8:11], v[52:55], a[248:251], v[8:11]\n"
  "ds_read_b128 v[76:79], v105 offset:14336\n"
  "v_xor_b32 v105, 0x4000, v105\n"
  "v_mfma_f32_16x16x32_bf16 v[8:11], v[60:63], v[144:147], v[8:11]\n"
  "ds_write_b64 v106, v[26:27] offset:576\n"
  "v_smfmac_f32_16x16x64_bf16 v[80:83], v[88:91], v[48:55], v124\n"
  "s_waitcnt lgkmcnt(4)\n"
  "v_smfmac_f32_16x16x64_bf16 v[80:83], v[88:91], v[64:71], v124\n"
  "v_mfma_f32_16x16x32_bf16 v[12:15], v[60:63], v[148:151], v[12:15]\n"
  "v_mfma_f32_16x16x32_bf16 v[12:15], v[52:55], a[252:255], v[12:15]\n"
  "v_mfma_f32_16x16x32_bf16 v[12:15], v[56:59], a[228:231], v[12:15]\n"
  "v_add_f32 v44, v80, v81\n"
  "v_mov_b32 v80, 0\n"
  "v_mfma_f32_16x16x32_bf16 v[12:15], v[48:51], a[204:207], v[12:15]\n"
  "ds_write_b64 v106, v[30:31] offset:864\n"
  "ds_write_b32 v110, v44\n"
  "v_mfma_f32_16x16x32_bf16 v[16:19], v[48:51], a[208:211], v[16:19]\n"
  "v_mov_b32 v81, 0\n"
  "v_mfma_f32_16x16x32_bf16 v[16:19], v[56:59], a[232:235], v[16:19]\n"
  "v_mfma_f32_16x16x32_bf16 v[16:19], v[52:55], v[128:131], v[16:19]\n"
  "v_mfma_f32_16x16x32_bf16 v[16:19], v[60:63], v[152:155], v[16:19]\n"
  "v_xor_b32 v106, 0x2000, v106\n"
  "v_xor_b32 v107, 0x2000, v107\n"
  "v_mfma_f32_16x16x32_bf16 v[20:23], v[60:63], v[156:159], v[20:23]\n"
  "v_xor_b32 v110, 0x2000, v110\n"
  "v_mfma_f32_16x16x32_bf16 v[20:23], v[52:55], v[132:135], v[20:23]\n"
  "v_mfma_f32_16x16x32_bf16 v[20:23], v[56:59], a[236:239], v[20:23]\n"
  "v_mfma_f32_16x16x32_bf16 v[20:23], v[48:51], a[212:215], v[20:23]\n"
  "s_waitcnt vmcnt(43) lgkmcnt(0)\n"
  "s_barrier\n"
  "v_mfma_f32_16x16x32_bf16 v[20:23], v[64:67], v[180:183], v[20:23]\n"
  "s_add_u32 m0, s66, 0\n"
  "ds_read_b128 v[48:51], v104 offset:0\n"
  "ds_read_b128 v[84:87], v111\n"
  "v_mfma_f32_16x16x32_bf16 v[20:23], v[72:75], v[204:207], v[20:23]\n"
  "ds_read_b128 v[96:99], v111 offset:64\n"
  "v_xor_b32 v111, 0x2000, v111\n"
  "v_mfma_f32_16x16x32_bf16 v[20:23], v[68:71], v[228:231], v[20:23]\n"
  "ds_read_b128 v[52:55], v104 offset:2048\n"
  "v_mfma_f32_16x16x32_bf16 v[44:47], v[76:79], v[252:255], v[20:23]\n"
  "buffer_load_dwordx4 v112, s[44:47], s67 offen offset:0  lds\n"
  "v_mfma_f32_16x16x32_bf16 v[16:19], v[76:79], v[248:251], v[16:19]\n"
  "s_add_u32 m0, s66, 1920\n"
  "v_mfma_f32_16x16x32_bf16 v[16:19], v[68:71], v[224:227], v[16:19]\n"
  "v_mfma_f32_16x16x32_bf16 v[16:19], v[72:75], v[200:203], v[16:19]\n"
  "ds_read_b128 v[56:59], v105 offset:0\n"
  "v_mfma_f32_16x16x32_bf16 v[40:43], v[64:67], v[176:179], v[16:19]\n"
  "buffer_load_dwordx4 v112, s[44:47], s67 offen offset:128  lds\n"
  "v_mfma_f32_16x16x32_bf16 v[12:15], v[64:67], v[172:175], v[12:15]\n"
  "s_add_u32 m0, s66, 3840\n"
  "v_mfma_f32_16x16x32_bf16 v[12:15], v[72:75], v[196:199], v[12:15]\n"
  "v_mfma_f32_16x16x32_bf16 v[12:15], v[68:71], v[220:223], v[12:15]\n"
  "s_waitcnt lgkmcnt(2)\n"
  "ds_read_b128 v[60:63], v105 offset:2048\n"
  "v_add_f32 v84, v84, v96\n"
  "v_mfma_f32_16x16x32_bf16 v[36:39], v[76:79], v[244:247], v[12:15]\n"
  "buffer_load_dwordx4 v112, s[44:47], s67 offen offset:256  lds\n"
  "v_mfma_f32_16x16x32_bf16 v[8:11], v[76:79], v[240:243], v[8:11]\n"
  "v_add_f32 v85, v85, v97\n"
  "v_add_f32 v86, v86, v98\n"
  "v_mfma_f32_16x16x32_bf16 v[8:11], v[68:71], v[216:219], v[8:11]\n"
  "v_add_f32 v87, v87, v99\n"
  "v_max3_f32 v82, v82, v84, v85\n"
  "v_mfma_f32_16x16x32_bf16 v[8:11], v[72:75], v[192:195], v[8:11]\n"
  "v_rcp_f32 v84, v84\n"
  "v_mfma_f32_16x16x32_bf16 v[32:35], v[64:67], v[168:171], v[8:11]\n"
  "v_rcp_f32 v85, v85\n"
  "v_mfma_f32_16x16x32_bf16 v[4:7], v[64:67], v[164:167], v[4:7]\n"
  "v_max3_f32 v82, v82, v86, v87\n"
  "v_max3_f32 v83, v83, v84, v85\n"
  "v_mfma_f32_16x16x32_bf16 v[4:7], v[72:75], v[188:191], v[4:7]\n"
  "v_rcp_f32 v86, v86\n"
  "v_mfma_f32_16x16x32_bf16 v[4:7], v[68:71], v[212:215], v[4:7]\n"
  "v_rcp_f32 v87, v87\n"
  "v_mfma_f32_16x16x32_bf16 v[28:31], v[76:79], v[236:239], v[4:7]\n"
  "v_max3_f32 v83, v83, v86, v87\n"
  "v_mul_f32 v96, s99, v84\n"
  "v_mfma_f32_16x16x32_bf16 v[0:3], v[76:79], v[232:235], v[0:3]\n"
  "s_add_u32 m0, s59, 3072\n"
  "v_mul_f32 v97, s99, v85\n"
  "v_mul_f32 v98, s99, v86\n"
  "v_mfma_f32_16x16x32_bf16 v[0:3], v[68:71], v[208:211], v[0:3]\n"
  "buffer_load_dwordx4 v114, s[40:43], s57 offen offset:0  lds\n"
  "v_mfma_f32_16x16x32_bf16 v[0:3], v[72:75], v[184:187], v[0:3]\n"
  "v_mul_f32 v99, s99, v87\n"
  "v_fma_f32 v40, v40, v84, v96\n"
  "v_mfma_f32_16x16x32_bf16 v[24:27], v[64:67], v[160:163], v[0:3]\n"
  "s_waitcnt lgkmcnt(0)\n"
  "v_fma_f32 v44, v44, v84, v96\n"
  "v_fma_f32 v41, v41, v85, v97\n"
  "v_mfma_f32_16x16x32_bf16 v[0:3], v[48:51], a[0:3], v[100:103]\n"
  "ds_read_b128 v[64:67], v104 offset:4096\n"
  "v_fma_f32 v45, v45, v85, v97\n"
  "v_mfma_f32_16x16x32_bf16 v[0:3], v[56:59], a[24:27], v[0:3]\n"
  "v_fma_f32 v42, v42, v86, v98\n"
  "v_fma_f32 v46, v46, v86, v98\n"
  "v_mfma_f32_16x16x32_bf16 v[0:3], v[52:55], a[48:51], v[0:3]\n"
  "ds_read_b128 v[68:71], v104 offset:6144\n"
  "v_fma_f32 v43, v43, v87, v99\n"
  "v_mfma_f32_16x16x32_bf16 v[0:3], v[60:63], a[72:75], v[0:3]\n"
  "ds_read_b64_tr_b16 v[92:93], v108 offset:0\n"
  "ds_read_b64_tr_b16 v[94:95], v108 offset:32\n"
  "v_mfma_f32_16x16x32_bf16 v[4:7], v[60:63], a[76:79], v[100:103]\n"
  "v_fma_f32 v47, v47, v87, v99\n"
  "v_fma_f32 v24, v24, v84, v96\n"
  "v_mfma_f32_16x16x32_bf16 v[4:7], v[52:55], a[52:55], v[4:7]\n"
  "buffer_load_dwordx4 v115, s[40:43], s57 offen offset:1024  lds\n"
  "v_mfma_f32_16x16x32_bf16 v[4:7], v[56:59], a[28:31], v[4:7]\n"
  "ds_read_b128 v[72:75], v105 offset:4096\n"
  "v_fma_f32 v25, v25, v85, v97\n"
  "v_mfma_f32_16x16x32_bf16 v[4:7], v[48:51], a[4:7], v[4:7]\n"
  "v_fma_f32 v28, v28, v84, v96\n"
  "v_fma_f32 v29, v29, v85, v97\n"
  "v_mfma_f32_16x16x32_bf16 v[8:11], v[48:51], a[8:11], v[100:103]\n"
  "v_fma_f32 v32, v32, v84, v96\n"
  "v_fma_f32 v33, v33, v85, v97\n"
  "v_mfma_f32_16x16x32_bf16 v[8:11], v[56:59], a[32:35], v[8:11]\n"
  "v_fma_f32 v36, v36, v84, v96\n"
  "v_fma_f32 v37, v37, v85, v97\n"
  "v_mfma_f32_16x16x32_bf16 v[8:11], v[52:55], a[56:59], v[8:11]\n"
  "ds_read_b128 v[76:79], v105 offset:6144\n"
  "v_fma_f32 v26, v26, v86, v98\n"
  "v_mfma_f32_16x16x32_bf16 v[8:11], v[60:63], a[80:83], v[8:11]\n"
  "v_fma_f32 v27, v27, v87, v99\n"
  "v_fma_f32 v30, v30, v86, v98\n"
  "v_smfmac_f32_16x16x64_bf16 v[80:83], v[88:91], v[48:55], v124\n"
  "s_waitcnt lgkmcnt(4)\n"
  "v_fma_f32 v31, v31, v87, v99\n"
  "v_smfmac_f32_16x16x64_bf16 v[80:83], v[88:91], v[64:71], v124\n"
  "buffer_load_dwordx4 v116, s[40:43], s57 offen offset:2048  lds\n"
  "v_mfma_f32_16x16x32_bf16 v[12:15], v[60:63], a[84:87], v[100:103]\n"
  "s_add_u32 s57, s57, s58\n"
  "v_fma_f32 v34, v34, v86, v98\n"
  "v_fma_f32 v35, v35, v87, v99\n"
  "v_mfma_f32_16x16x32_bf16 v[12:15], v[52:55], a[60:63], v[12:15]\n"
  "v_fma_f32 v38, v38, v86, v98\n"
  "v_fma_f32 v39, v39, v87, v99\n"
  "v_mfma_f32_16x16x32_bf16 v[12:15], v[56:59], a[36:39], v[12:15]\n"
  "v_cvt_pk_bf16_f32 v40, v40, v44\n"
  "v_cvt_pk_bf16_f32 v41, v41, v45\n"
  "v_mfma_f32_16x16x32_bf16 v[12:15], v[48:51], a[12:15], v[12:15]\n"
  "s_waitcnt lgkmcnt(2)\n"
  "buffer_store_dwordx4 v[92:95], v109, s[48:51], s68 offen offset:0 nt\n"
  "ds_write2_b32 v107, v40, v41 offset0:0 offset1:72\n"
  "v_mfma_f32_16x16x32_bf16 v[16:19], v[48:51], a[16:19], v[100:103]\n"
  "v_cvt_pk_bf16_f32 v42, v42, v46\n"
  "v_cvt_pk_bf16_f32 v43, v43, v47\n"
  "v_mfma_f32_16x16x32_bf16 v[16:19], v[56:59], a[40:43], v[16:19]\n"
  "ds_read_b64_tr_b16 v[96:97], v108 offset:2304\n"
  "ds_read_b64_tr_b16 v[98:99], v108 offset:2336\n"
  "v_mfma_f32_16x16x32_bf16 v[16:19], v[52:55], a[64:67], v[16:19]\n"
  "s_add_u32 m0, s59, 6144\n"
  "v_cvt_pk_bf16_f32 v24, v24, v28\n"
  "v_cvt_pk_bf16_f32 v28, v25, v29\n"
  "v_mfma_f32_16x16x32_bf16 v[16:19], v[60:63], a[88:91], v[16:19]\n"
  "buffer_load_dwordx4 v114, s[40:43], s57 offen offset:0  lds\n"
  "v_mfma_f32_16x16x32_bf16 v[20:23], v[60:63], a[92:95], v[100:103]\n"
  "v_cvt_pk_bf16_f32 v25, v32, v36\n"
  "v_cvt_pk_bf16_f32 v29, v33, v37\n"
  "v_mfma_f32_16x16x32_bf16 v[20:23], v[52:55], a[68:71], v[20:23]\n"
  "v_cvt_pk_bf16_f32 v26, v26, v30\n"
  "v_cvt_pk_bf16_f32 v30, v27, v31\n"
  "v_mfma_f32_16x16x32_bf16 v[20:23], v[56:59], a[44:47], v[20:23]\n"
  "v_cvt_pk_bf16_f32 v27, v34, v38\n"
  "v_cvt_pk_bf16_f32 v31, v35, v39\n"
  "v_mfma_f32_16x16x32_bf16 v[20:23], v[48:51], a[20:23], v[20:23]\n"
  "s_waitcnt lgkmcnt(3)\n"
  "v_mfma_f32_16x16x32_bf16 v[20:23], v[64:67], a[116:119], v[20:23]\n"
  "s_add_u32 s66, s66, 6144\n"
  "ds_read_b128 v[48:51], v104 offset:8192\n"
  "v_mfma_f32_16x16x32_bf16 v[20:23], v[72:75], a[140:143], v[20:23]\n"
  "s_cmp_ge_u32 s66, 86016\n"
  "s_cselect_b32 s64, 36864, 0\n"
  "v_mfma_f32_16x16x32_bf16 v[20:23], v[68:71], a[164:167], v[20:23]\n"
  "s_sub_u32 s66, s66, s64\n"
  "ds_read_b128 v[52:55], v104 offset:10240\n"
  "v_mfma_f32_16x16x32_bf16 v[20:23], v[76:79], a[188:191], v[20:23]\n"
  "s_sub_u32 s90, s90, 1\n"
  "s_cselect_b32 s63, s91, 6144\n"
  "s_cmov_b64 s[90:91], s[92:93]\n"
  "s_cmov_b64 s[92:93], s[94:95]\n"
  "buffer_load_dwordx4 v115, s[40:43], s57 offen offset:1024  lds\n"
  "v_mfma_f32_16x16x32_bf16 v[16:19], v[76:79], a[184:187], v[16:19]\n"
  "s_add_u32 s67, s67, s63\n"
  "v_mfma_f32_16x16x32_bf16 v[16:19], v[68:71], a[160:163], v[16:19]\n"
  "ds_read_b64_tr_b16 v[92:93], v108 offset:4608\n"
  "ds_read_b64_tr_b16 v[94:95], v108 offset:4640\n"
  "v_mfma_f32_16x16x32_bf16 v[16:19], v[72:75], a[136:139], v[16:19]\n"
  "ds_read_b128 v[56:59], v105 offset:8192\n"
  "v_xor_b32 v108, 0x2000, v108\n"
  "v_mfma_f32_16x16x32_bf16 v[16:19], v[64:67], a[112:115], v[16:19]\n"
  "s_waitcnt lgkmcnt(5)\n"
  "buffer_store_dwordx4 v[96:99], v109, s[48:51], s68 offen offset:128 nt\n"
  "ds_write2_b32 v107, v42, v43 offset0:144 offset1:216\n"
  "v_mfma_f32_16x16x32_bf16 v[12:15], v[64:67], a[108:111], v[12:15]\n"
  "v_mfma_f32_16x16x32_bf16 v[12:15], v[72:75], a[132:135], v[12:15]\n"
  "v_mfma_f32_16x16x32_bf16 v[12:15], v[68:71], a[156:159], v[12:15]\n"
  "ds_read_b128 v[60:63], v105 offset:10240\n"
  "v_mfma_f32_16x16x32_bf16 v[12:15], v[76:79], a[180:183], v[12:15]\n"
  "buffer_load_dwordx4 v116, s[40:43], s57 offen offset:2048  lds\n"
  "v_mfma_f32_16x16x32_bf16 v[8:11], v[76:79], a[176:179], v[8:11]\n"
  "s_add_u32 s57, s57, 6144\n"
  "v_mfma_f32_16x16x32_bf16 v[8:11], v[68:71], a[152:155], v[8:11]\n"
  "v_mfma_f32_16x16x32_bf16 v[8:11], v[72:75], a[128:131], v[8:11]\n"
  "v_mfma_f32_16x16x32_bf16 v[8:11], v[64:67], a[104:107], v[8:11]\n"
  "s_waitcnt lgkmcnt(3)\n"
  "buffer_store_dwordx4 v[92:95], v109, s[48:51], s68 offen offset:256 nt\n"
  "ds_write_b64 v106, v[24:25] offset:0\n"
  "v_mfma_f32_16x16x32_bf16 v[4:7], v[64:67], a[100:103], v[4:7]\n"
  "s_add_u32 s68, s68, s69\n"
  "v_mfma_f32_16x16x32_bf16 v[4:7], v[72:75], a[124:127], v[4:7]\n"
  "s_mov_b32 s69, s70\n"
  "v_mfma_f32_16x16x32_bf16 v[4:7], v[68:71], a[148:151], v[4:7]\n"
  "s_add_u32 m0, s59, 9216\n"
  "v_mfma_f32_16x16x32_bf16 v[4:7], v[76:79], a[172:175], v[4:7]\n"
  "s_movk_i32 s70, 6144\n"
  "buffer_load_dwordx4 v114, s[40:43], s57 offen offset:0  lds\n"
  "v_mfma_f32_16x16x32_bf16 v[0:3], v[76:79], a[168:171], v[0:3]\n"
  "v_mfma_f32_16x16x32_bf16 v[0:3], v[68:71], a[144:147], v[0:3]\n"
  "v_mfma_f32_16x16x32_bf16 v[0:3], v[72:75], a[120:123], v[0:3]\n"
  "v_mfma_f32_16x16x32_bf16 v[0:3], v[64:67], a[96:99], v[0:3]\n"
  "s_waitcnt lgkmcnt(1)\n"
  "v_mfma_f32_16x16x32_bf16 v[0:3], v[48:51], a[192:195], v[0:3]\n"
  "ds_read_b128 v[64:67], v104 offset:12288\n"
  "v_mfma_f32_16x16x32_bf16 v[0:3], v[56:59], a[216:219], v[0:3]\n"
  "v_mfma_f32_16x16x32_bf16 v[0:3], v[52:55], a[240:243], v[0:3]\n"
  "ds_read_b128 v[68:71], v104 offset:14336\n"
  "v_xor_b32 v104, 0x4000, v104\n"
  "v_mfma_f32_16x16x32_bf16 v[0:3], v[60:63], v[136:139], v[0:3]\n"
  "buffer_load_dwordx4 v115, s[40:43], s57 offen offset:1024  lds\n"
  "v_mfma_f32_16x16x32_bf16 v[4:7], v[60:63], v[140:143], v[4:7]\n"
  "v_mfma_f32_16x16x32_bf16 v[4:7], v[52:55], a[244:247], v[4:7]\n"
  "v_mfma_f32_16x16x32_bf16 v[4:7], v[56:59], a[220:223], v[4:7]\n"
  "ds_read_b128 v[72:75], v105 offset:12288\n"
  "v_mfma_f32_16x16x32_bf16 v[4:7], v[48:51], a[196:199], v[4:7]\n"
  "ds_write_b64 v106, v[28:29] offset:288\n"
  "v_mfma_f32_16x16x32_bf16 v[8:11], v[48:51], a[200:203], v[8:11]\n"
  "v_mfma_f32_16x16x32_bf16 v[8:11], v[56:59], a[224:227], v[8:11]\n"
  "v_mfma_f32_16x16x32_bf16 v[8:11], v[52:55], a[248:251], v[8:11]\n"
  "ds_read_b128 v[76:79], v105 offset:14336\n"
  "v_xor_b32 v105, 0x4000, v105\n"
  "v_mfma_f32_16x16x32_bf16 v[8:11], v[60:63], v[144:147], v[8:11]\n"
  "buffer_load_dwordx4 v116, s[40:43], s57 offen offset:2048  lds\n"
  "v_smfmac_f32_16x16x64_bf16 v[80:83], v[88:91], v[48:55], v124\n"
  "s_waitcnt lgkmcnt(3)\n"
  "s_add_u32 s57, s57, s84\n"
  "v_smfmac_f32_16x16x64_bf16 v[80:83], v[88:91], v[64:71], v124\n"
  "v_mfma_f32_16x16x32_bf16 v[12:15], v[60:63], v[148:151], v[12:15]\n"
  "v_mfma_f32_16x16x32_bf16 v[12:15], v[52:55], a[252:255], v[12:15]\n"
  "v_mfma_f32_16x16x32_bf16 v[12:15], v[56:59], a[228:231], v[12:15]\n"
  "v_add_f32 v44, v80, v81\n"
  "v_mov_b32 v80, 0\n"
  "v_mfma_f32_16x16x32_bf16 v[12:15], v[48:51], a[204:207], v[12:15]\n"
  "ds_write_b64 v106, v[26:27] offset:576\n"
  "ds_write_b64 v106, v[30:31] offset:864\n"
  "ds_write_b32 v110, v44\n"
  "v_mfma_f32_16x16x32_bf16 v[16:19], v[48:51], a[208:211], v[16:19]\n"
  "v_mov_b32 v81, 0\n"
  "v_mfma_f32_16x16x32_bf16 v[16:19], v[56:59], a[232:235], v[16:19]\n"
  "v_mfma_f32_16x16x32_bf16 v[16:19], v[52:55], v[128:131], v[16:19]\n"
  "v_mfma_f32_16x16x32_bf16 v[16:19], v[60:63], v[152:155], v[16:19]\n"
  "v_xor_b32 v106, 0x2000, v106\n"
  "v_xor_b32 v107, 0x2000, v107\n"
  "v_mfma_f32_16x16x32_bf16 v[20:23], v[60:63], v[156:159], v[20:23]\n"
  "v_xor_b32 v110, 0x2000, v110\n"
  "v_mfma_f32_16x16x32_bf16 v[20:23], v[52:55], v[132:135], v[20:23]\n"
  "v_mfma_f32_16x16x32_bf16 v[20:23], v[56:59], a[236:239], v[20:23]\n"
  "v_mfma_f32_16x16x32_bf16 v[20:23], v[48:51], a[212:215], v[20:23]\n"
  "s_waitcnt vmcnt(19) lgkmcnt(0)\n"
  "s_barrier\n"
  "v_mfma_f32_16x16x32_bf16 v[20:23], v[64:67], v[180:183], v[20:23]\n"
  "s_add_u32 m0, s66, 0\n"
  "ds_read_b128 v[48:51], v104 offset:0\n"
  "ds_read_b128 v[84:87], v111\n"
  "v_mfma_f32_16x16x32_bf16 v[20:23], v[72:75], v[204:207], v[20:23]\n"
  "ds_read_b128 v[96:99], v111 offset:64\n"
  "v_xor_b32 v111, 0x2000, v111\n"
  "v_mfma_f32_16x16x32_bf16 v[20:23], v[68:71], v[228:231], v[20:23]\n"
  "ds_read_b128 v[52:55], v104 offset:2048\n"
  "v_mfma_f32_16x16x32_bf16 v[44:47], v[76:79], v[252:255], v[20:23]\n"
  "buffer_load_dwordx4 v112, s[44:47], s67 offen offset:0  lds\n"
  "v_mfma_f32_16x16x32_bf16 v[16:19], v[76:79], v[248:251], v[16:19]\n"
  "s_add_u32 m0, s66, 1920\n"
  "v_mfma_f32_16x16x32_bf16 v[16:19], v[68:71], v[224:227], v[16:19]\n"
  "v_mfma_f32_16x16x32_bf16 v[16:19], v[72:75], v[200:203], v[16:19]\n"
  "ds_read_b128 v[56:59], v105 offset:0\n"
  "v_mfma_f32_16x16x32_bf16 v[40:43], v[64:67], v[176:179], v[16:19]\n"
  "buffer_load_dwordx4 v112, s[44:47], s67 offen offset:128  lds\n"
  "v_mfma_f32_16x16x32_bf16 v[12:15], v[64:67], v[172:175], v[12:15]\n"
  "s_add_u32 m0, s66, 3840\n"
  "v_mfma_f32_16x16x32_bf16 v[12:15], v[72:75], v[196:199], v[12:15]\n"
  "v_mfma_f32_16x16x32_bf16 v[12:15], v[68:71], v[220:223], v[12:15]\n"
  "s_waitcnt lgkmcnt(2)\n"
  "ds_read_b128 v[60:63], v105 offset:2048\n"
  "v_add_f32 v84, v84, v96\n"
  "v_mfma_f32_16x16x32_bf16 v[36:39], v[76:79], v[244:247], v[12:15]\n"
  "buffer_load_dwordx4 v112, s[44:47], s67 offen offset:256  lds\n"
  "v_mfma_f32_16x16x32_bf16 v[8:11], v[76:79], v[240:243], v[8:11]\n"
  "v_add_f32 v85, v85, v97\n"
  "v_add_f32 v86, v86, v98\n"
  "v_mfma_f32_16x16x32_bf16 v[8:11], v[68:71], v[216:219], v[8:11]\n"
  "v_add_f32 v87, v87, v99\n"
  "v_max3_f32 v82, v82, v84, v85\n"
  "v_mfma_f32_16x16x32_bf16 v[8:11], v[72:75], v[192:195], v[8:11]\n"
  "s_add_u32 m0, s59, 12288\n"
  "v_rcp_f32 v84, v84\n"
  "v_mfma_f32_16x16x32_bf16 v[32:35], v[64:67], v[168:171], v[8:11]\n"
  "buffer_load_dwordx4 v114, s[40:43], s57 offen offset:0  lds\n"
  "v_mfma_f32_16x16x32_bf16 v[4:7], v[64:67], v[164:167], v[4:7]\n"
  "v_rcp_f32 v85, v85\n"
  "v_mfma_f32_16x16x32_bf16 v[4:7], v[72:75], v[188:191], v[4:7]\n"
  "v_max3_f32 v82, v82, v86, v87\n"
  "v_max3_f32 v83, v83, v84, v85\n"
  "v_mfma_f32_16x16x32_bf16 v[4:7], v[68:71], v[212:215], v[4:7]\n"
  "v_rcp_f32 v86, v86\n"
  "v_mfma_f32_16x16x32_bf16 v[28:31], v[76:79], v[236:239], v[4:7]\n"
  "buffer_load_dwordx4 v115, s[40:43], s57 offen offset:1024  lds\n"
  "v_mfma_f32_16x16x32_bf16 v[0:3], v[76:79], v[232:235], v[0:3]\n"
  "v_rcp_f32 v87, v87\n"
  "v_mfma_f32_16x16x32_bf16 v[0:3], v[68:71], v[208:211], v[0:3]\n"
  "v_max3_f32 v83, v83, v86, v87\n"
  "v_mul_f32 v96, s99, v84\n"
  "v_mfma_f32_16x16x32_bf16 v[0:3], v[72:75], v[184:187], v[0:3]\n"
  "v_mul_f32 v97, s99, v85\n"
  "v_mul_f32 v98, s99, v86\n"
  "v_mfma_f32_16x16x32_bf16 v[24:27], v[64:67], v[160:163], v[0:3]\n"
  "s_waitcnt lgkmcnt(0)\n"
  "buffer_load_dwordx4 v116, s[40:43], s57 offen offset:2048  lds\n"
  "v_mfma_f32_16x16x32_bf16 v[0:3], v[48:51], a[0:3], v[100:103]\n"
  "s_add_u32 s57, s57, 6144\n"
  "ds_read_b128 v[64:67], v104 offset:4096\n"
  "v_mul_f32 v99, s99, v87\n"
  "v_mfma_f32_16x16x32_bf16 v[0:3], v[56:59], a[24:27], v[0:3]\n"
  "v_fma_f32 v40, v40, v84, v96\n"
  "v_fma_f32 v44, v44, v84, v96\n"
  "v_mfma_f32_16x16x32_bf16 v[0:3], v[52:55], a[48:51], v[0:3]\n"
  "s_add_u32 m0, s59, 15360\n"
  "ds_read_b128 v[68:71], v104 offset:6144\n"
  "v_fma_f32 v41, v41, v85, v97\n"
  "v_mfma_f32_16x16x32_bf16 v[0:3], v[60:63], a[72:75], v[0:3]\n"
  "buffer_load_dwordx4 v114, s[40:43], s57 offen offset:0  lds\n"
  "v_mfma_f32_16x16x32_bf16 v[4:7], v[60:63], a[76:79], v[100:103]\n"
  "ds_read_b64_tr_b16 v[92:93], v108 offset:0\n"
  "ds_read_b64_tr_b16 v[94:95], v108 offset:32\n"
  "v_mfma_f32_16x16x32_bf16 v[4:7], v[52:55], a[52:55], v[4:7]\n"
  "v_fma_f32 v45, v45, v85, v97\n"
  "v_fma_f32 v42, v42, v86, v98\n"
  "v_mfma_f32_16x16x32_bf16 v[4:7], v[56:59], a[28:31], v[4:7]\n"
  "ds_read_b128 v[72:75], v105 offset:4096\n"
  "v_fma_f32 v46, v46, v86, v98\n"
  "v_mfma_f32_16x16x32_bf16 v[4:7], v[48:51], a[4:7], v[4:7]\n"
  "buffer_load_dwordx4 v115, s[40:43], s57 offen offset:1024  lds\n"
  "v_mfma_f32_16x16x32_bf16 v[8:11], v[48:51], a[8:11], v[100:103]\n"
  "v_fma_f32 v43, v43, v87, v99\n"
  "v_fma_f32 v47, v47, v87, v99\n"
  "v_mfma_f32_16x16x32_bf16 v[8:11], v[56:59], a[32:35], v[8:11]\n"
  "v_fma_f32 v24, v24, v84, v96\n"
  "v_fma_f32 v25, v25, v85, v97\n"
  "v_mfma_f32_16x16x32_bf16 v[8:11], v[52:55], a[56:59], v[8:11]\n"
  "ds_read_b128 v[76:79], v105 offset:6144\n"
  "v_fma_f32 v28, v28, v84, v96\n"
  "v_mfma_f32_16x16x32_bf16 v[8:11], v[60:63], a[80:83], v[8:11]\n"
  "buffer_load_dwordx4 v116, s[40:43], s57 offen offset:2048  lds\n"
  "v_smfmac_f32_16x16x64_bf16 v[80:83], v[88:91], v[48:55], v124\n"
  "s_waitcnt lgkmcnt(4)\n"
  "s_add_u32 s57, s57, s58\n"
  "v_fma_f32 v29, v29, v85, v97\n"
  "v_smfmac_f32_16x16x64_bf16 v[80:83], v[88:91], v[64:71], v124\n"
  "s_waitcnt lgkmcnt(2)\n"
  "buffer_store_dwordx4 v[92:95], v109, s[48:51], s68 offen offset:0 nt\n"
  "v_fma_f32 v32, v32, v84, v96\n"
  "v_fma_f32 v33, v33, v85, v97\n"
  "v_mfma_f32_16x16x32_bf16 v[12:15], v[60:63], a[84:87], v[100:103]\n"
  "v_fma_f32 v36, v36, v84, v96\n"
  "v_fma_f32 v37, v37, v85, v97\n"
  "v_mfma_f32_16x16x32_bf16 v[12:15], v[52:55], a[60:63], v[12:15]\n"
  "v_fma_f32 v26, v26, v86, v98\n"
  "v_fma_f32 v27, v27, v87, v99\n"
  "v_mfma_f32_16x16x32_bf16 v[12:15], v[56:59], a[36:39], v[12:15]\n"
  "v_fma_f32 v30, v30, v86, v98\n"
  "v_fma_f32 v31, v31, v87, v99\n"
  "v_mfma_f32_16x16x32_bf16 v[12:15], v[48:51], a[12:15], v[12:15]\n"
  "v_fma_f32 v34, v34, v86, v98\n"
  "v_fma_f32 v35, v35, v87, v99\n"
  "v_mfma_f32_16x16x32_bf16 v[16:19], v[48:51], a[16:19], v[100:103]\n"
  "v_fma_f32 v38, v38, v86, v98\n"
  "v_fma_f32 v39, v39, v87, v99\n"
  "v_mfma_f32_16x16x32_bf16 v[16:19], v[56:59], a[40:43], v[16:19]\n"
  "ds_read_b64_tr_b16 v[96:97], v108 offset:2304\n"
  "ds_read_b64_tr_b16 v[98:99], v108 offset:2336\n"
  "v_mfma_f32_16x16x32_bf16 v[16:19], v[52:55], a[64:67], v[16:19]\n"
  "v_cvt_pk_bf16_f32 v40, v40, v44\n"
  "v_cvt_pk_bf16_f32 v41, v41, v45\n"
  "v_mfma_f32_16x16x32_bf16 v[16:19], v[60:63], a[88:91], v[16:19]\n"
  "v_cvt_pk_bf16_f32 v42, v42, v46\n"
  "v_cvt_pk_bf16_f32 v43, v43, v47\n"
  "v_mfma_f32_16x16x32_bf16 v[20:23], v[60:63], a[92:95], v[100:103]\n"
  "v_cvt_pk_bf16_f32 v24, v24, v28\n"
  "v_cvt_pk_bf16_f32 v28, v25, v29\n"
  "v_mfma_f32_16x16x32_bf16 v[20:23], v[52:55], a[68:71], v[20:23]\n"
  "v_cvt_pk_bf16_f32 v25, v32, v36\n"
  "v_cvt_pk_bf16_f32 v29, v33, v37\n"
  "v_mfma_f32_16x16x32_bf16 v[20:23], v[56:59], a[44:47], v[20:23]\n"
  "v_cvt_pk_bf16_f32 v26, v26, v30\n"
  "v_cvt_pk_bf16_f32 v30, v27, v31\n"
  "v_mfma_f32_16x16x32_bf16 v[20:23], v[48:51], a[20:23], v[20:23]\n"
  "s_waitcnt lgkmcnt(2)\n"
  "v_cvt_pk_bf16_f32 v27, v34, v38\n"
  "v_cvt_pk_bf16_f32 v31, v35, v39\n"
  "v_mfma_f32_16x16x32_bf16 v[20:23], v[64:67], a[116:119], v[20:23]\n"
  "s_add_u32 s66, s66, 6144\n"
  "ds_read_b128 v[48:51], v104 offset:8192\n"
  "v_mfma_f32_16x16x32_bf16 v[20:23], v[72:75], a[140:143], v[20:23]\n"
  "s_cmp_ge_u32 s66, 86016\n"
  "s_cselect_b32 s64, 36864, 0\n"
  "v_mfma_f32_16x16x32_bf16 v[20:23], v[68:71], a[164:167], v[20:23]\n"
  "s_sub_u32 s66, s66, s64\n"
  "ds_read_b128 v[52:55], v104 offset:10240\n"
  "v_mfma_f32_16x16x32_bf16 v[20:23], v[76:79], a[188:191], v[20:23]\n"
  "s_sub_u32 s90, s90, 1\n"
  "s_cselect_b32 s63, s91, 6144\n"
  "s_cmov_b64 s[90:91], s[92:93]\n"
  "s_cmov_b64 s[92:93], s[94:95]\n"
  "s_waitcnt lgkmcnt(2)\n"
  "buffer_store_dwordx4 v[96:99], v109, s[48:51], s68 offen offset:128 nt\n"
  "ds_write2_b32 v107, v40, v41 offset0:0 offset1:72\n"
  "v_mfma_f32_16x16x32_bf16 v[16:19], v[76:79], a[184:187], v[16:19]\n"
  "s_add_u32 s67, s67, s63\n"
  "v_mfma_f32_16x16x32_bf16 v[16:19], v[68:71], a[160:163], v[16:19]\n"
  "ds_read_b64_tr_b16 v[92:93], v108 offset:4608\n"
  "ds_read_b64_tr_b16 v[94:95], v108 offset:4640\n"
  "v_mfma_f32_16x16x32_bf16 v[16:19], v[72:75], a[136:139], v[16:19]\n"
  "ds_read_b128 v[56:59], v105 offset:8192\n"
  "v_xor_b32 v108, 0x2000, v108\n"
  "v_mfma_f32_16x16x32_bf16 v[16:19], v[64:67], a[112:115], v[16:19]\n"
  "ds_write2_b32 v107, v42, v43 offset0:144 offset1:216\n"
  "v_mfma_f32_16x16x32_bf16 v[12:15], v[64:67], a[108:111], v[12:15]\n"
  "v_mfma_f32_16x16x32_bf16 v[12:15], v[72:75], a[132:135], v[12:15]\n"
  "v_mfma_f32_16x16x32_bf16 v[12:15], v[68:71], a[156:159], v[12:15]\n"
  "ds_read_b128 v[60:63], v105 offset:10240\n"
  "v_mfma_f32_16x16x32_bf16 v[12:15], v[76:79], a[180:183], v[12:15]\n"
  "ds_write_b64 v106, v[24:25] offset:0\n"
  "v_mfma_f32_16x16x32_bf16 v[8:11], v[76:79], a[176:179], v[8:11]\n"
  "v_mfma_f32_16x16x32_bf16 v[8:11], v[68:71], a[152:155], v[8:11]\n"
  "v_mfma_f32_16x16x32_bf16 v[8:11], v[72:75], a[128:131], v[8:11]\n"
  "v_mfma_f32_16x16x32_bf16 v[8:11], v[64:67], a[104:107], v[8:11]\n"
  "s_waitcnt lgkmcnt(4)\n"
  "buffer_store_dwordx4 v[92:95], v109, s[48:51], s68 offen offset:256 nt\n"
  "ds_write_b64 v106, v[28:29] offset:288\n"
  "v_mfma_f32_16x16x32_bf16 v[4:7], v[64:67], a[100:103], v[4:7]\n"
  "s_add_u32 s68, s68, s69\n"
  "v_mfma_f32_16x16x32_bf16 v[4:7], v[72:75], a[124:127], v[4:7]\n"
  "s_mov_b32 s69, s70\n"
  "v_mfma_f32_16x16x32_bf16 v[4:7], v[68:71], a[148:151], v[4:7]\n"
  "s_movk_i32 s70, 6144\n"
  "v_mfma_f32_16x16x32_bf16 v[4:7], v[76:79], a[172:175], v[4:7]\n"
  "ds_write_b64 v106, v[26:27] offset:576\n"
  "v_mfma_f32_16x16x32_bf16 v[0:3], v[76:79], a[168:171], v[0:3]\n"
  "v_mfma_f32_16x16x32_bf16 v[0:3], v[68:71], a[144:147], v[0:3]\n"
  "v_mfma_f32_16x16x32_bf16 v[0:3], v[72:75], a[120:123], v[0:3]\n"
  "v_mfma_f32_16x16x32_bf16 v[0:3], v[64:67], a[96:99], v[0:3]\n"
  "s_waitcnt lgkmcnt(3)\n"
  "v_mfma_f32_16x16x32_bf16 v[0:3], v[48:51], a[192:195], v[0:3]\n"
  "ds_read_b128 v[64:67], v104 offset:12288\n"
  "v_mfma_f32_16x16x32_bf16 v[0:3], v[56:59], a[216:219], v[0:3]\n"
  "v_mfma_f32_16x16x32_bf16 v[0:3], v[52:55], a[240:243], v[0:3]\n"
  "ds_read_b128 v[68:71], v104 offset:14336\n"
  "v_xor_b32 v104, 0x4000, v104\n"
  "v_mfma_f32_16x16x32_bf16 v[0:3], v[60:63], v[136:139], v[0:3]\n"
  "ds_write_b64 v106, v[30:31] offset:864\n"
  "v_mfma_f32_16x16x32_bf16 v[4:7], v[60:63], v[140:143], v[4:7]\n"
  "v_mfma_f32_16x16x32_bf16 v[4:7], v[52:55], a[244:247], v[4:7]\n"
  "v_mfma_f32_16x16x32_bf16 v[4:7], v[56:59], a[220:223], v[4:7]\n"
  "ds_read_b128 v[72:75], v105 offset:12288\n"
  "v_mfma_f32_16x16x32_bf16 v[4:7], v[48:51], a[196:199], v[4:7]\n"
  "v_mfma_f32_16x16x32_bf16 v[8:11], v[48:51], a[200:203], v[8:11]\n"
  "v_mfma_f32_16x16x32_bf16 v[8:11], v[56:59], a[224:227], v[8:11]\n"
  "v_mfma_f32_16x16x32_bf16 v[8:11], v[52:55], a[248:251], v[8:11]\n"
  "ds_read_b128 v[76:79], v105 offset:14336\n"
  "v_xor_b32 v105, 0x4000, v105\n"
  "v_mfma_f32_16x16x32_bf16 v[8:11], v[60:63], v[144:147], v[8:11]\n"
  "v_smfmac_f32_16x16x64_bf16 v[80:83], v[88:91], v[48:55], v124\n"
  "s_waitcnt lgkmcnt(3)\n"
  "v_smfmac_f32_16x16x64_bf16 v[80:83], v[88:91], v[64:71], v124\n"
  "v_mfma_f32_16x16x32_bf16 v[12:15], v[60:63], v[148:151], v[12:15]\n"
  "v_mfma_f32_16x16x32_bf16 v[12:15], v[52:55], a[252:255], v[12:15]\n"
  "v_mfma_f32_16x16x32_bf16 v[12:15], v[56:59], a[228:231], v[12:15]\n"
  "v_add_f32 v44, v80, v81\n"
  "v_mov_b32 v80, 0\n"
  "v_mfma_f32_16x16x32_bf16 v[12:15], v[48:51], a[204:207], v[12:15]\n"
  "ds_write_b32 v110, v44\n"
  "v_mfma_f32_16x16x32_bf16 v[16:19], v[48:51], a[208:211], v[16:19]\n"
  "v_mov_b32 v81, 0\n"
  "v_mfma_f32_16x16x32_bf16 v[16:19], v[56:59], a[232:235], v[16:19]\n"
  "v_mfma_f32_16x16x32_bf16 v[16:19], v[52:55], v[128:131], v[16:19]\n"
  "v_mfma_f32_16x16x32_bf16 v[16:19], v[60:63], v[152:155], v[16:19]\n"
  "v_xor_b32 v106, 0x2000, v106\n"
  "v_xor_b32 v107, 0x2000, v107\n"
  "v_mfma_f32_16x16x32_bf16 v[20:23], v[60:63], v[156:159], v[20:23]\n"
  "v_xor_b32 v110, 0x2000, v110\n"
  "v_mfma_f32_16x16x32_bf16 v[20:23], v[52:55], v[132:135], v[20:23]\n"
  "v_mfma_f32_16x16x32_bf16 v[20:23], v[56:59], a[236:239], v[20:23]\n"
  "v_mfma_f32_16x16x32_bf16 v[20:23], v[48:51], a[212:215], v[20:23]\n"
  "s_waitcnt lgkmcnt(0)\n"
  "s_barrier\n"
  "v_mfma_f32_16x16x32_bf16 v[20:23], v[64:67], v[180:183], v[20:23]\n"
  "ds_read_b128 v[48:51], v104 offset:0\n"
  "ds_read_b128 v[84:87], v111\n"
  "v_mfma_f32_16x16x32_bf16 v[20:23], v[72:75], v[204:207], v[20:23]\n"
  "ds_read_b128 v[96:99], v111 offset:64\n"
  "v_xor_b32 v111, 0x2000, v111\n"
  "v_mfma_f32_16x16x32_bf16 v[20:23], v[68:71], v[228:231], v[20:23]\n"
  "ds_read_b128 v[52:55], v104 offset:2048\n"
  "v_mfma_f32_16x16x32_bf16 v[44:47], v[76:79], v[252:255], v[20:23]\n"
  "v_mfma_f32_16x16x32_bf16 v[16:19], v[76:79], v[248:251], v[16:19]\n"
  "v_mfma_f32_16x16x32_bf16 v[16:19], v[68:71], v[224:227], v[16:19]\n"
  "v_mfma_f32_16x16x32_bf16 v[16:19], v[72:75], v[200:203], v[16:19]\n"
  "ds_read_b128 v[56:59], v105 offset:0\n"
  "v_mfma_f32_16x16x32_bf16 v[40:43], v[64:67], v[176:179], v[16:19]\n"
  "v_mfma_f32_16x16x32_bf16 v[12:15], v[64:67], v[172:175], v[12:15]\n"
  "v_mfma_f32_16x16x32_bf16 v[12:15], v[72:75], v[196:199], v[12:15]\n"
  "v_mfma_f32_16x16x32_bf16 v[12:15], v[68:71], v[220:223], v[12:15]\n"
  "s_waitcnt lgkmcnt(2)\n"
  "ds_read_b128 v[60:63], v105 offset:2048\n"
  "v_add_f32 v84, v84, v96\n"
  "v_mfma_f32_16x16x32_bf16 v[36:39], v[76:79], v[244:247], v[12:15]\n"
  "v_add_f32 v85, v85, v97\n"
  "v_add_f32 v86, v86, v98\n"
  "v_mfma_f32_16x16x32_bf16 v[8:11], v[76:79], v[240:243], v[8:11]\n"
  "v_add_f32 v87, v87, v99\n"
  "v_max3_f32 v82, v82, v84, v85\n"
  "v_mfma_f32_16x16x32_bf16 v[8:11], v[68:71], v[216:219], v[8:11]\n"
  "v_rcp_f32 v84, v84\n"
  "v_mfma_f32_16x16x32_bf16 v[8:11], v[72:75], v[192:195], v[8:11]\n"
  "v_rcp_f32 v85, v85\n"
  "v_mfma_f32_16x16x32_bf16 v[32:35], v[64:67], v[168:171], v[8:11]\n"
  "v_max3_f32 v82, v82, v86, v87\n"
  "v_max3_f32 v83, v83, v84, v85\n"
  "v_mfma_f32_16x16x32_bf16 v[4:7], v[64:67], v[164:167], v[4:7]\n"
  "v_rcp_f32 v86, v86\n"
  "v_mfma_f32_16x16x32_bf16 v[4:7], v[72:75], v[188:191], v[4:7]\n"
  "v_rcp_f32 v87, v87\n"
  "v_mfma_f32_16x16x32_bf16 v[4:7], v[68:71], v[212:215], v[4:7]\n"
  "v_max3_f32 v83, v83, v86, v87\n"
  "v_mul_f32 v96, s99, v84\n"
  "v_mfma_f32_16x16x32_bf16 v[28:31], v[76:79], v[236:239], v[4:7]\n"
  "v_mul_f32 v97, s99, v85\n"
  "v_fma_f32 v40, v40, v84, v96\n"
  "v_mfma_f32_16x16x32_bf16 v[0:3], v[76:79], v[232:235], v[0:3]\n"
  "v_fma_f32 v44, v44, v84, v96\n"
  "v_cvt_pk_bf16_f32 v40, v40, v44\n"
  "v_mfma_f32_16x16x32_bf16 v[0:3], v[68:71], v[208:211], v[0:3]\n"
  "v_fma_f32 v41, v41, v85, v97\n"
  "v_fma_f32 v45, v45, v85, v97\n"
  "v_mfma_f32_16x16x32_bf16 v[0:3], v[72:75], v[184:187], v[0:3]\n"
  "v_cvt_pk_bf16_f32 v41, v41, v45\n"
  "v_mul_f32 v98, s99, v86\n"
  "v_mfma_f32_16x16x32_bf16 v[24:27], v[64:67], v[160:163], v[0:3]\n"
  "s_waitcnt lgkmcnt(0)\n"
  "v_mul_f32 v99, s99, v87\n"
  "v_fma_f32 v42, v42, v86, v98\n"
  "v_mfma_f32_16x16x32_bf16 v[0:3], v[48:51], a[0:3], v[100:103]\n"
  "ds_read_b128 v[64:67], v104 offset:4096\n"
  "v_fma_f32 v46, v46, v86, v98\n"
  "v_mfma_f32_16x16x32_bf16 v[0:3], v[56:59], a[24:27], v[0:3]\n"
  "v_cvt_pk_bf16_f32 v42, v42, v46\n"
  "v_fma_f32 v43, v43, v87, v99\n"
  "v_mfma_f32_16x16x32_bf16 v[0:3], v[52:55], a[48:51], v[0:3]\n"
  "ds_read_b128 v[68:71], v104 offset:6144\n"
  "v_fma_f32 v47, v47, v87, v99\n"
  "v_mfma_f32_16x16x32_bf16 v[0:3], v[60:63], a[72:75], v[0:3]\n"
  "ds_read_b64_tr_b16 v[92:93], v108 offset:0\n"
  "ds_read_b64_tr_b16 v[94:95], v108 offset:32\n"
  "v_mfma_f32_16x16x32_bf16 v[4:7], v[60:63], a[76:79], v[100:103]\n"
  "v_cvt_pk_bf16_f32 v43, v43, v47\n"
  "v_fma_f32 v24, v24, v84, v96\n"
  "v_mfma_f32_16x16x32_bf16 v[4:7], v[52:55], a[52:55], v[4:7]\n"
  "v_fma_f32 v25, v25, v85, v97\n"
  "v_fma_f32 v28, v28, v84, v96\n"
  "v_mfma_f32_16x16x32_bf16 v[4:7], v[56:59], a[28:31], v[4:7]\n"
  "ds_read_b128 v[72:75], v105 offset:4096\n"
  "v_fma_f32 v29, v29, v85, v97\n"
  "v_mfma_f32_16x16x32_bf16 v[4:7], v[48:51], a[4:7], v[4:7]\n"
  "v_fma_f32 v32, v32, v84, v96\n"
  "v_fma_f32 v33, v33, v85, v97\n"
  "v_mfma_f32_16x16x32_bf16 v[8:11], v[48:51], a[8:11], v[100:103]\n"
  "v_fma_f32 v36, v36, v84, v96\n"
  "v_fma_f32 v37, v37, v85, v97\n"
  "v_mfma_f32_16x16x32_bf16 v[8:11], v[56:59], a[32:35], v[8:11]\n"
  "v_fma_f32 v26, v26, v86, v98\n"
  "v_fma_f32 v27, v27, v87, v99\n"
  "v_mfma_f32_16x16x32_bf16 v[8:11], v[52:55], a[56:59], v[8:11]\n"
  "ds_read_b128 v[76:79], v105 offset:6144\n"
  "v_fma_f32 v30, v30, v86, v98\n"
  "v_mfma_f32_16x16x32_bf16 v[8:11], v[60:63], a[80:83], v[8:11]\n"
  "v_fma_f32 v31, v31, v87, v99\n"
  "v_fma_f32 v34, v34, v86, v98\n"
  "v_smfmac_f32_16x16x64_bf16 v[80:83], v[88:91], v[48:55], v124\n"
  "s_waitcnt lgkmcnt(4)\n"
  "v_fma_f32 v35, v35, v87, v99\n"
  "v_smfmac_f32_16x16x64_bf16 v[80:83], v[88:91], v[64:71], v124\n"
  "s_waitcnt lgkmcnt(2)\n"
  "buffer_store_dwordx4 v[92:95], v109, s[48:51], s68 offen offset:0 nt\n"
  "ds_write2_b32 v107, v40, v41 offset0:0 offset1:72\n"
  "v_mfma_f32_16x16x32_bf16 v[12:15], v[60:63], a[84:87], v[100:103]\n"
  "v_fma_f32 v38, v38, v86, v98\n"
  "v_fma_f32 v39, v39, v87, v99\n"
  "v_mfma_f32_16x16x32_bf16 v[12:15], v[52:55], a[60:63], v[12:15]\n"
  "v_cvt_pk_bf16_f32 v24, v24, v28\n"
  "v_cvt_pk_bf16_f32 v28, v25, v29\n"
  "v_mfma_f32_16x16x32_bf16 v[12:15], v[56:59], a[36:39], v[12:15]\n"
  "v_cvt_pk_bf16_f32 v25, v32, v36\n"
  "v_cvt_pk_bf16_f32 v29, v33, v37\n"
  "v_mfma_f32_16x16x32_bf16 v[12:15], v[48:51], a[12:15], v[12:15]\n"
  "ds_write2_b32 v107, v42, v43 offset0:144 offset1:216\n"
  "v_mfma_f32_16x16x32_bf16 v[16:19], v[48:51], a[16:19], v[100:103]\n"
  "v_cvt_pk_bf16_f32 v26, v26, v30\n"
  "v_cvt_pk_bf16_f32 v30, v27, v31\n"
  "v_mfma_f32_16x16x32_bf16 v[16:19], v[56:59], a[40:43], v[16:19]\n"
  "ds_read_b64_tr_b16 v[96:97], v108 offset:2304\n"
  "ds_read_b64_tr_b16 v[98:99], v108 offset:2336\n"
  "v_mfma_f32_16x16x32_bf16 v[16:19], v[52:55], a[64:67], v[16:19]\n"
  "v_cvt_pk_bf16_f32 v27, v34, v38\n"
  "v_cvt_pk_bf16_f32 v31, v35, v39\n"
  "v_mfma_f32_16x16x32_bf16 v[16:19], v[60:63], a[88:91], v[16:19]\n"
  "v_mfma_f32_16x16x32_bf16 v[20:23], v[60:63], a[92:95], v[100:103]\n"
  "v_mfma_f32_16x16x32_bf16 v[20:23], v[52:55], a[68:71], v[20:23]\n"
  "v_mfma_f32_16x16x32_bf16 v[20:23], v[56:59], a[44:47], v[20:23]\n"
  "v_mfma_f32_16x16x32_bf16 v[20:23], v[48:51], a[20:23], v[20:23]\n"
  "s_waitcnt lgkmcnt(4)\n"
  "v_mfma_f32_16x16x32_bf16 v[20:23], v[64:67], a[116:119], v[20:23]\n"
  "ds_read_b128 v[48:51], v104 offset:8192\n"
  "v_mfma_f32_16x16x32_bf16 v[20:23], v[72:75], a[140:143], v[20:23]\n"
  "v_mfma_f32_16x16x32_bf16 v[20:23], v[68:71], a[164:167], v[20:23]\n"
  "ds_read_b128 v[52:55], v104 offset:10240\n"
  "v_mfma_f32_16x16x32_bf16 v[20:23], v[76:79], a[188:191], v[20:23]\n"
  "s_waitcnt lgkmcnt(2)\n"
  "buffer_store_dwordx4 v[96:99], v109, s[48:51], s68 offen offset:128 nt\n"
  "ds_write_b64 v106, v[24:25] offset:0\n"
  "v_mfma_f32_16x16x32_bf16 v[16:19], v[76:79], a[184:187], v[16:19]\n"
  "v_mfma_f32_16x16x32_bf16 v[16:19], v[68:71], a[160:163], v[16:19]\n"
  "ds_read_b64_tr_b16 v[92:93], v108 offset:4608\n"
  "ds_read_b64_tr_b16 v[94:95], v108 offset:4640\n"
  "v_mfma_f32_16x16x32_bf16 v[16:19], v[72:75], a[136:139], v[16:19]\n"
  "ds_read_b128 v[56:59], v105 offset:8192\n"
  "v_xor_b32 v108, 0x2000, v108\n"
  "v_mfma_f32_16x16x32_bf16 v[16:19], v[64:67], a[112:115], v[16:19]\n"
  "v_mfma_f32_16x16x32_bf16 v[12:15], v[64:67], a[108:111], v[12:15]\n"
  "v_mfma_f32_16x16x32_bf16 v[12:15], v[72:75], a[132:135], v[12:15]\n"
  "v_mfma_f32_16x16x32_bf16 v[12:15], v[68:71], a[156:159], v[12:15]\n"
  "ds_read_b128 v[60:63], v105 offset:10240\n"
  "v_mfma_f32_16x16x32_bf16 v[12:15], v[76:79], a[180:183], v[12:15]\n"
  "v_mfma_f32_16x16x32_bf16 v[8:11], v[76:79], a[176:179], v[8:11]\n"
  "v_mfma_f32_16x16x32_bf16 v[8:11], v[68:71], a[152:155], v[8:11]\n"
  "v_mfma_f32_16x16x32_bf16 v[8:11], v[72:75], a[128:131], v[8:11]\n"
  "v_mfma_f32_16x16x32_bf16 v[8:11], v[64:67], a[104:107], v[8:11]\n"
  "s_waitcnt lgkmcnt(2)\n"
  "buffer_store_dwordx4 v[92:95], v109, s[48:51], s68 offen offset:256 nt\n"
  "ds_write_b64 v106, v[28:29] offset:288\n"
  "v_mfma_f32_16x16x32_bf16 v[4:7], v[64:67], a[100:103], v[4:7]\n"
  "s_add_u32 s68, s68, s69\n"
  "v_mfma_f32_16x16x32_bf16 v[4:7], v[72:75], a[124:127], v[4:7]\n"
  "s_mov_b32 s69, s70\n"
  "v_mfma_f32_16x16x32_bf16 v[4:7], v[68:71], a[148:151], v[4:7]\n"
  "s_movk_i32 s70, 6144\n"
  "v_mfma_f32_16x16x32_bf16 v[4:7], v[76:79], a[172:175], v[4:7]\n"
  "v_mfma_f32_16x16x32_bf16 v[0:3], v[76:79], a[168:171], v[0:3]\n"
  "v_mfma_f32_16x16x32_bf16 v[0:3], v[68:71], a[144:147], v[0:3]\n"
  "v_mfma_f32_16x16x32_bf16 v[0:3], v[72:75], a[120:123], v[0:3]\n"
  "v_mfma_f32_16x16x32_bf16 v[0:3], v[64:67], a[96:99], v[0:3]\n"
  "s_waitcnt lgkmcnt(1)\n"
  "v_mfma_f32_16x16x32_bf16 v[0:3], v[48:51], a[192:195], v[0:3]\n"
  "ds_read_b128 v[64:67], v104 offset:12288\n"
  "v_mfma_f32_16x16x32_bf16 v[0:3], v[56:59], a[216:219], v[0:3]\n"
  "v_mfma_f32_16x16x32_bf16 v[0:3], v[52:55], a[240:243], v[0:3]\n"
  "ds_read_b128 v[68:71], v104 offset:14336\n"
  "v_xor_b32 v104, 0x4000, v104\n"
  "v_mfma_f32_16x16x32_bf16 v[0:3], v[60:63], v[136:139], v[0:3]\n"
  "ds_write_b64 v106, v[26:27] offset:576\n"
  "v_mfma_f32_16x16x32_bf16 v[4:7], v[60:63], v[140:143], v[4:7]\n"
  "v_mfma_f32_16x16x32_bf16 v[4:7], v[52:55], a[244:247], v[4:7]\n"
  "v_mfma_f32_16x16x32_bf16 v[4:7], v[56:59], a[220:223], v[4:7]\n"
  "ds_read_b128 v[72:75], v105 offset:12288\n"
  "v_mfma_f32_16x16x32_bf16 v[4:7], v[48:51], a[196:199], v[4:7]\n"
  "ds_write_b64 v106, v[30:31] offset:864\n"
  "v_mfma_f32_16x16x32_bf16 v[8:11], v[48:51], a[200:203], v[8:11]\n"
  "v_mfma_f32_16x16x32_bf16 v[8:11], v[56:59], a[224:227], v[8:11]\n"
  "v_mfma_f32_16x16x32_bf16 v[8:11], v[52:55], a[248:251], v[8:11]\n"
  "ds_read_b128 v[76:79], v105 offset:14336\n"
  "v_xor_b32 v105, 0x4000, v105\n"
  "v_mfma_f32_16x16x32_bf16 v[8:11], v[60:63], v[144:147], v[8:11]\n"
  "v_smfmac_f32_16x16x64_bf16 v[80:83], v[88:91], v[48:55], v124\n"
  "s_waitcnt lgkmcnt(4)\n"
  "v_smfmac_f32_16x16x64_bf16 v[80:83], v[88:91], v[64:71], v124\n"
  "v_mfma_f32_16x16x32_bf16 v[12:15], v[60:63], v[148:151], v[12:15]\n"
  "v_mfma_f32_16x16x32_bf16 v[12:15], v[52:55], a[252:255], v[12:15]\n"
  "v_mfma_f32_16x16x32_bf16 v[12:15], v[56:59], a[228:231], v[12:15]\n"
  "v_add_f32 v44, v80, v81\n"
  "v_mov_b32 v80, 0\n"
  "v_mfma_f32_16x16x32_bf16 v[12:15], v[48:51], a[204:207], v[12:15]\n"
  "ds_write_b32 v110, v44\n"
  "v_mfma_f32_16x16x32_bf16 v[16:19], v[48:51], a[208:211], v[16:19]\n"
  "v_mov_b32 v81, 0\n"
  "v_mfma_f32_16x16x32_bf16 v[16:19], v[56:59], a[232:235], v[16:19]\n"
  "v_mfma_f32_16x16x32_bf16 v[16:19], v[52:55], v[128:131], v[16:19]\n"
  "v_mfma_f32_16x16x32_bf16 v[16:19], v[60:63], v[152:155], v[16:19]\n"
  "v_xor_b32 v106, 0x2000, v106\n"
  "v_xor_b32 v107, 0x2000, v107\n"
  "v_mfma_f32_16x16x32_bf16 v[20:23], v[60:63], v[156:159], v[20:23]\n"
  "v_xor_b32 v110, 0x2000, v110\n"
  "v_mfma_f32_16x16x32_bf16 v[20:23], v[52:55], v[132:135], v[20:23]\n"
  "v_mfma_f32_16x16x32_bf16 v[20:23], v[56:59], a[236:239], v[20:23]\n"
  "v_mfma_f32_16x16x32_bf16 v[20:23], v[48:51], a[212:215], v[20:23]\n"
  "s_waitcnt lgkmcnt(0)\n"
  "s_barrier\n"
  "v_mfma_f32_16x16x32_bf16 v[20:23], v[64:67], v[180:183], v[20:23]\n"
  "ds_read_b128 v[48:51], v104 offset:0\n"
  "ds_read_b128 v[84:87], v111\n"
  "ds_read_b128 v[96:99], v111 offset:64\n"
  "v_xor_b32 v111, 0x2000, v111\n"
  "v_mfma_f32_16x16x32_bf16 v[20:23], v[72:75], v[204:207], v[20:23]\n"
  "v_mfma_f32_16x16x32_bf16 v[20:23], v[68:71], v[228:231], v[20:23]\n"
  "ds_read_b128 v[52:55], v104 offset:2048\n"
  "v_mfma_f32_16x16x32_bf16 v[44:47], v[76:79], v[252:255], v[20:23]\n"
  "v_mfma_f32_16x16x32_bf16 v[16:19], v[76:79], v[248:251], v[16:19]\n"
  "v_mfma_f32_16x16x32_bf16 v[16:19], v[68:71], v[224:227], v[16:19]\n"
  "v_mfma_f32_16x16x32_bf16 v[16:19], v[72:75], v[200:203], v[16:19]\n"
  "ds_read_b128 v[56:59], v105 offset:0\n"
  "v_mfma_f32_16x16x32_bf16 v[40:43], v[64:67], v[176:179], v[16:19]\n"
  "v_mfma_f32_16x16x32_bf16 v[12:15], v[64:67], v[172:175], v[12:15]\n"
  "v_mfma_f32_16x16x32_bf16 v[12:15], v[72:75], v[196:199], v[12:15]\n"
  "v_mfma_f32_16x16x32_bf16 v[12:15], v[68:71], v[220:223], v[12:15]\n"
  "s_waitcnt lgkmcnt(2)\n"
  "ds_read_b128 v[60:63], v105 offset:2048\n"
  "v_add_f32 v84, v84, v96\n"
  "v_add_f32 v85, v85, v97\n"
  "v_add_f32 v86, v86, v98\n"
  "v_mfma_f32_16x16x32_bf16 v[36:39], v[76:79], v[244:247], v[12:15]\n"
  "v_add_f32 v87, v87, v99\n"
  "v_max3_f32 v82, v82, v84, v85\n"
  "v_rcp_f32 v84, v84\n"
  "v_mfma_f32_16x16x32_bf16 v[8:11], v[76:79], v[240:243], v[8:11]\n"
  "v_rcp_f32 v85, v85\n"
  "v_max3_f32 v82, v82, v86, v87\n"
  "v_mul_f32 v96, s99, v84\n"
  "v_mfma_f32_16x16x32_bf16 v[8:11], v[68:71], v[216:219], v[8:11]\n"
  "v_rcp_f32 v86, v86\n"
  "v_rcp_f32 v87, v87\n"
  "v_mfma_f32_16x16x32_bf16 v[8:11], v[72:75], v[192:195], v[8:11]\n"
  "v_max3_f32 v83, v83, v84, v85\n"
  "v_max3_f32 v83, v83, v86, v87\n"
  "v_mul_f32 v97, s99, v85\n"
  "v_mul_f32 v98, s99, v86\n"
  "v_mfma_f32_16x16x32_bf16 v[32:35], v[64:67], v[168:171], v[8:11]\n"
  "v_mul_f32 v99, s99, v87\n"
  "v_fma_f32 v36, v36, v84, v96\n"
  "v_fma_f32 v37, v37, v85, v97\n"
  "v_fma_f32 v38, v38, v86, v98\n"
  "v_mfma_f32_16x16x32_bf16 v[4:7], v[64:67], v[164:167], v[4:7]\n"
  "v_fma_f32 v39, v39, v87, v99\n"
  "v_fma_f32 v40, v40, v84, v96\n"
  "v_fma_f32 v44, v44, v84, v96\n"
  "v_fma_f32 v41, v41, v85, v97\n"
  "v_mfma_f32_16x16x32_bf16 v[4:7], v[72:75], v[188:191], v[4:7]\n"
  "v_fma_f32 v45, v45, v85, v97\n"
  "v_fma_f32 v42, v42, v86, v98\n"
  "v_fma_f32 v46, v46, v86, v98\n"
  "v_fma_f32 v43, v43, v87, v99\n"
  "v_mfma_f32_16x16x32_bf16 v[4:7], v[68:71], v[212:215], v[4:7]\n"
  "v_fma_f32 v32, v32, v84, v96\n"
  "v_fma_f32 v33, v33, v85, v97\n"
  "v_fma_f32 v34, v34, v86, v98\n"
  "v_fma_f32 v35, v35, v87, v99\n"
  "v_mfma_f32_16x16x32_bf16 v[28:31], v[76:79], v[236:239], v[4:7]\n"
  "v_fma_f32 v47, v47, v87, v99\n"
  "v_cvt_pk_bf16_f32 v40, v40, v44\n"
  "v_cvt_pk_bf16_f32 v41, v41, v45\n"
  "v_cvt_pk_bf16_f32 v42, v42, v46\n"
  "v_mfma_f32_16x16x32_bf16 v[0:3], v[76:79], v[232:235], v[0:3]\n"
  "ds_write2_b32 v107, v40, v41 offset0:0 offset1:72\n"
  "v_cvt_pk_bf16_f32 v43, v43, v47\n"
  "v_mfma_f32_16x16x32_bf16 v[0:3], v[68:71], v[208:211], v[0:3]\n"
  "ds_write2_b32 v107, v42, v43 offset0:144 offset1:216\n"
  "v_mfma_f32_16x16x32_bf16 v[0:3], v[72:75], v[184:187], v[0:3]\n"
  "v_fma_f32 v28, v28, v84, v96\n"
  "v_fma_f32 v29, v29, v85, v97\n"
  "v_fma_f32 v30, v30, v86, v98\n"
  "v_fma_f32 v31, v31, v87, v99\n"
  "v_mfma_f32_16x16x32_bf16 v[24:27], v[64:67], v[160:163], v[0:3]\n"
  "s_waitcnt lgkmcnt(2)\n"
  "v_mfma_f32_16x16x32_bf16 v[0:3], v[48:51], a[0:3], v[100:103]\n"
  "ds_read_b128 v[64:67], v104 offset:4096\n"
  "v_mfma_f32_16x16x32_bf16 v[0:3], v[56:59], a[24:27], v[0:3]\n"
  "v_mfma_f32_16x16x32_bf16 v[4:7], v[56:59], a[28:31], v[100:103]\n"
  "ds_read_b128 v[68:71], v104 offset:6144\n"
  "v_fma_f32 v24, v24, v84, v96\n"
  "v_fma_f32 v25, v25, v85, v97\n"
  "v_cvt_pk_bf16_f32 v24, v24, v28\n"
  "v_mfma_f32_16x16x32_bf16 v[4:7], v[48:51], a[4:7], v[4:7]\n"
  "ds_read_b64_tr_b16 v[92:93], v108 offset:0\n"
  "ds_read_b64_tr_b16 v[94:95], v108 offset:32\n"
  "v_cvt_pk_bf16_f32 v28, v25, v29\n"
  "v_cvt_pk_bf16_f32 v25, v32, v36\n"
  "v_mfma_f32_16x16x32_bf16 v[8:11], v[48:51], a[8:11], v[100:103]\n"
  "ds_write_b64 v106, v[24:25] offset:0\n"
  "v_cvt_pk_bf16_f32 v29, v33, v37\n"
  "v_mfma_f32_16x16x32_bf16 v[8:11], v[56:59], a[32:35], v[8:11]\n"
  "ds_write_b64 v106, v[28:29] offset:288\n"
  "v_fma_f32 v26, v26, v86, v98\n"
  "v_mfma_f32_16x16x32_bf16 v[12:15], v[56:59], a[36:39], v[100:103]\n"
  "ds_read_b128 v[72:75], v105 offset:4096\n"
  "v_fma_f32 v27, v27, v87, v99\n"
  "v_cvt_pk_bf16_f32 v26, v26, v30\n"
  "v_cvt_pk_bf16_f32 v30, v27, v31\n"
  "v_mfma_f32_16x16x32_bf16 v[12:15], v[48:51], a[12:15], v[12:15]\n"
  "v_cvt_pk_bf16_f32 v27, v34, v38\n"
  "v_cvt_pk_bf16_f32 v31, v35, v39\n"
  "v_mfma_f32_16x16x32_bf16 v[16:19], v[48:51], a[16:19], v[100:103]\n"
  "ds_write_b64 v106, v[26:27] offset:576\n"
  "v_mfma_f32_16x16x32_bf16 v[16:19], v[56:59], a[40:43], v[16:19]\n"
  "ds_write_b64 v106, v[30:31] offset:864\n"
  "v_mfma_f32_16x16x32_bf16 v[20:23], v[56:59], a[44:47], v[100:103]\n"
  "ds_read_b128 v[76:79], v105 offset:6144\n"
  "v_mfma_f32_16x16x32_bf16 v[20:23], v[48:51], a[20:23], v[20:23]\n"
  "v_smfmac_f32_16x16x64_bf16 v[80:83], v[88:91], v[48:55], v124\n"
  "s_waitcnt lgkmcnt(8)\n"
  "s_waitcnt vmcnt(30)\n"
  "ds_read_b64_tr_b16 a[0:1], v117 offset:0\n"
  "ds_read_b64_tr_b16 a[4:5], v118 offset:0\n"
  "v_smfmac_f32_16x16x64_bf16 v[80:83], v[88:91], v[64:71], v124\n"
  "s_waitcnt lgkmcnt(8)\n"
  "buffer_store_dwordx4 v[92:95], v109, s[48:51], s68 offen offset:0 nt\n"
  "v_mfma_f32_16x16x32_bf16 v[20:23], v[52:55], a[68:71], v[20:23]\n"
  "ds_read_b64_tr_b16 a[8:9], v119 offset:0\n"
  "ds_read_b64_tr_b16 a[12:13], v120 offset:0\n"
  "v_mfma_f32_16x16x32_bf16 v[20:23], v[60:63], a[92:95], v[20:23]\n"
  "ds_read_b64_tr_b16 a[16:17], v121 offset:2048\n"
  "ds_read_b64_tr_b16 a[20:21], v122 offset:2048\n"
  "v_mfma_f32_16x16x32_bf16 v[16:19], v[60:63], a[88:91], v[16:19]\n"
  "v_mfma_f32_16x16x32_bf16 v[16:19], v[52:55], a[64:67], v[16:19]\n"
  "s_waitcnt vmcnt(25)\n"
  "ds_read_b64_tr_b16 a[2:3], v117 offset:3072\n"
  "ds_read_b64_tr_b16 a[6:7], v118 offset:3072\n"
  "v_mfma_f32_16x16x32_bf16 v[12:15], v[52:55], a[60:63], v[12:15]\n"
  "v_mfma_f32_16x16x32_bf16 v[12:15], v[60:63], a[84:87], v[12:15]\n"
  "ds_read_b64_tr_b16 a[10:11], v119 offset:3072\n"
  "ds_read_b64_tr_b16 a[14:15], v120 offset:3072\n"
  "ds_read_b64_tr_b16 v[96:97], v108 offset:2304\n"
  "ds_read_b64_tr_b16 v[98:99], v108 offset:2336\n"
  "v_mfma_f32_16x16x32_bf16 v[8:11], v[60:63], a[80:83], v[8:11]\n"
  "s_add_u32 m0, s59, 0\n"
  "ds_read_b64_tr_b16 a[18:19], v121 offset:5120\n"
  "ds_read_b64_tr_b16 a[22:23], v122 offset:5120\n"
  "v_mfma_f32_16x16x32_bf16 v[8:11], v[52:55], a[56:59], v[8:11]\n"
  "s_waitcnt lgkmcnt(8)\n"
  "buffer_load_dwordx4 v114, s[40:43], s57 offen offset:0 nt lds\n"
  "buffer_load_dwordx4 v115, s[40:43], s57 offen offset:1024 nt lds\n"
  "v_mfma_f32_16x16x32_bf16 v[4:7], v[52:55], a[52:55], v[4:7]\n"
  "s_waitcnt vmcnt(22)\n"
  "ds_read_b64_tr_b16 a[24:25], v117 offset:6144\n"
  "ds_read_b64_tr_b16 a[28:29], v118 offset:6144\n"
  "v_mfma_f32_16x16x32_bf16 v[4:7], v[60:63], a[76:79], v[4:7]\n"
  "buffer_load_dwordx4 v116, s[40:43], s57 offen offset:2048  lds\n"
  "v_mfma_f32_16x16x32_bf16 v[0:3], v[60:63], a[72:75], v[0:3]\n"
  "s_add_u32 s57, s57, 6144\n"
  "ds_read_b64_tr_b16 a[32:33], v119 offset:6144\n"
  "ds_read_b64_tr_b16 a[36:37], v120 offset:6144\n"
  "v_mfma_f32_16x16x32_bf16 v[0:3], v[52:55], a[48:51], v[0:3]\n"
  "s_add_u32 m0, s59, 3072\n"
  "ds_read_b64_tr_b16 a[40:41], v121 offset:8192\n"
  "ds_read_b64_tr_b16 a[44:45], v122 offset:8192\n"
  "v_mfma_f32_16x16x32_bf16 v[0:3], v[64:67], a[96:99], v[0:3]\n"
  "s_waitcnt lgkmcnt(6)\n"
  "buffer_load_dwordx4 v114, s[40:43], s57 offen offset:0 nt lds\n"
  "buffer_load_dwordx4 v115, s[40:43], s57 offen offset:1024 nt lds\n"
  "ds_read_b128 v[48:51], v104 offset:8192\n"
  "v_mfma_f32_16x16x32_bf16 v[0:3], v[72:75], a[120:123], v[0:3]\n"
  "s_waitcnt vmcnt(21)\n"
  "ds_read_b64_tr_b16 a[26:27], v117 offset:9216\n"
  "ds_read_b64_tr_b16 a[30:31], v118 offset:9216\n"
  "s_waitcnt vmcnt(12)\n"
  "ds_read_b64 v[32:33], v117 offset:15360\n"
  "ds_read_b64 v[34:35], v118 offset:15360\n"
  "v_mfma_f32_16x16x32_bf16 v[4:7], v[72:75], a[124:127], v[4:7]\n"
  "buffer_load_dwordx4 v116, s[40:43], s57 offen offset:2048  lds\n"
  "ds_read_b128 v[52:55], v104 offset:10240\n"
  "v_mfma_f32_16x16x32_bf16 v[4:7], v[64:67], a[100:103], v[4:7]\n"
  "s_add_u32 s57, s57, s84\n"
  "buffer_store_dwordx4 v[96:99], v109, s[48:51], s68 offen offset:128 nt\n"
  "ds_read_b64_tr_b16 a[34:35], v119 offset:9216\n"
  "ds_read_b64_tr_b16 a[38:39], v120 offset:9216\n"
  "v_mfma_f32_16x16x32_bf16 v[8:11], v[64:67], a[104:107], v[8:11]\n"
  "s_add_u32 m0, s59, 6144\n"
  "ds_read_b64_tr_b16 a[42:43], v121 offset:11264\n"
  "ds_read_b64_tr_b16 a[46:47], v122 offset:11264\n"
  "v_mfma_f32_16x16x32_bf16 v[8:11], v[72:75], a[128:131], v[8:11]\n"
  "s_waitcnt lgkmcnt(10)\n"
  "buffer_load_dwordx4 v114, s[40:43], s57 offen offset:0 nt lds\n"
  "buffer_load_dwordx4 v115, s[40:43], s57 offen offset:1024 nt lds\n"
  "v_mfma_f32_16x16x32_bf16 v[12:15], v[72:75], a[132:135], v[12:15]\n"
  "ds_read_b128 v[56:59], v105 offset:8192\n"
  "ds_read_b64_tr_b16 a[48:49], v117 offset:12288\n"
  "ds_read_b64_tr_b16 a[52:53], v118 offset:12288\n"
  "ds_read_b64_tr_b16 v[92:93], v108 offset:4608\n"
  "v_mfma_f32_16x16x32_bf16 v[12:15], v[64:67], a[108:111], v[12:15]\n"
  "buffer_load_dwordx4 v116, s[40:43], s57 offen offset:2048  lds\n"
  "ds_read_b64_tr_b16 v[94:95], v108 offset:4640\n"
  "s_waitcnt lgkmcnt(10)\n"
  "v_max3_f32 v32, |v32|, |v33|, |v34|\n"
  "v_mfma_f32_16x16x32_bf16 v[16:19], v[64:67], a[112:115], v[16:19]\n"
  "s_add_u32 s57, s57, 6144\n"
  "ds_read_b64_tr_b16 a[56:57], v119 offset:12288\n"
  "ds_read_b64_tr_b16 a[60:61], v120 offset:12288\n"
  "v_max3_f32 v32, v32, |v35|, 0\n"
  "v_max_f32 v32, 0, v32\n"
  "v_mfma_f32_16x16x32_bf16 v[16:19], v[72:75], a[136:139], v[16:19]\n"
  "s_add_u32 m0, s59, 9216\n"
  "ds_read_b64_tr_b16 a[64:65], v121 offset:14336\n"
  "ds_read_b64_tr_b16 a[68:69], v122 offset:14336\n"
  "v_mul_f32 v33, 0x40000000, v32\n"
  "v_mul_f32 v32, 0x2b800000, v32\n"
  "v_mfma_f32_16x16x32_bf16 v[20:23], v[72:75], a[140:143], v[20:23]\n"
  "s_waitcnt lgkmcnt(9)\n"
  "buffer_load_dwordx4 v114, s[40:43], s57 offen offset:0 nt lds\n"
  "buffer_load_dwordx4 v115, s[40:43], s57 offen offset:1024 nt lds\n"
  "ds_read_b128 v[60:63], v105 offset:10240\n"
  "v_mfma_f32_16x16x32_bf16 v[20:23], v[64:67], a[116:119], v[20:23]\n"
  "ds_read_b64_tr_b16 a[50:51], v117 offset:15360\n"
  "ds_read_b64_tr_b16 a[54:55], v118 offset:15360\n"
  "v_max_f32 v82, v82, v32\n"
  "v_xor_b32 v108, 0x2000, v108\n"
  "v_mfma_f32_16x16x32_bf16 v[20:23], v[68:71], a[164:167], v[20:23]\n"
  "buffer_load_dwordx4 v116, s[40:43], s57 offen offset:2048  lds\n"
  "v_readlane_b32 s98, v33, 0\n"
  "v_mfma_f32_16x16x32_bf16 v[20:23], v[76:79], a[188:191], v[20:23]\n"
  "s_add_u32 s57, s57, s58\n"
  "ds_read_b64_tr_b16 a[58:59], v119 offset:15360\n"
  "ds_read_b64_tr_b16 a[62:63], v120 offset:15360\n"
  "v_readlane_b32 s62, v33, 21\n"
  "v_mfma_f32_16x16x32_bf16 v[16:19], v[76:79], a[184:187], v[16:19]\n"
  "s_add_u32 m0, s59, 12288\n"
  "ds_read_b64_tr_b16 a[66:67], v121 offset:17408\n"
  "ds_read_b64_tr_b16 a[70:71], v122 offset:17408\n"
  "v_readlane_b32 s63, v33, 42\n"
  "v_mfma_f32_16x16x32_bf16 v[16:19], v[68:71], a[160:163], v[16:19]\n"
  "s_waitcnt lgkmcnt(7)\n"
  "buffer_load_dwordx4 v114, s[40:43], s57 offen offset:0 nt lds\n"
  "buffer_load_dwordx4 v115, s[40:43], s57 offen offset:1024 nt lds\n"
  "v_mfma_f32_16x16x32_bf16 v[12:15], v[68:71], a[156:159], v[12:15]\n"
  "buffer_store_dwordx4 v[92:95], v109, s[48:51], s68 offen offset:256 nt\n"
  "s_waitcnt vmcnt(13)\n"
  "ds_read_b64_tr_b16 a[72:73], v117 offset:0\n"
  "ds_read_b64_tr_b16 a[76:77], v118 offset:0\n"
  "v_mfma_f32_16x16x32_bf16 v[12:15], v[76:79], a[180:183], v[12:15]\n"
  "s_add_u32 s68, s68, s69\n"
  "buffer_load_dwordx4 v116, s[40:43], s57 offen offset:2048  lds\n"
  "v_readlane_b32 s64, v33, 63\n"
  "v_mfma_f32_16x16x32_bf16 v[8:11], v[76:79], a[176:179], v[8:11]\n"
  "s_add_u32 s57, s57, 6144\n"
  "ds_read_b64_tr_b16 a[80:81], v119 offset:0\n"
  "ds_read_b64_tr_b16 a[84:85], v120 offset:0\n"
  "v_mfma_f32_16x16x32_bf16 v[8:11], v[68:71], a[152:155], v[8:11]\n"
  "s_add_u32 m0, s59, 15360\n"
  "ds_read_b64_tr_b16 a[88:89], v121 offset:2048\n"
  "ds_read_b64_tr_b16 a[92:93], v122 offset:2048\n"
  "v_mfma_f32_16x16x32_bf16 v[4:7], v[68:71], a[148:151], v[4:7]\n"
  "s_waitcnt lgkmcnt(6)\n"
  "buffer_load_dwordx4 v114, s[40:43], s57 offen offset:0 nt lds\n"
  "buffer_load_dwordx4 v115, s[40:43], s57 offen offset:1024 nt lds\n"
  "v_mfma_f32_16x16x32_bf16 v[4:7], v[76:79], a[172:175], v[4:7]\n"
  "s_waitcnt vmcnt(13)\n"
  "ds_read_b64_tr_b16 a[74:75], v117 offset:3072\n"
  "ds_read_b64_tr_b16 a[78:79], v118 offset:3072\n"
  "v_mfma_f32_16x16x32_bf16 v[0:3], v[76:79], a[168:171], v[0:3]\n"
  "s_mov_b32 s69, s70\n"
  "buffer_load_dwordx4 v116, s[40:43], s57 offen offset:2048  lds\n"
  "v_mfma_f32_16x16x32_bf16 v[0:3], v[68:71], a[144:147], v[0:3]\n"
  "s_add_u32 s57, s57, s84\n"
  "ds_read_b64_tr_b16 a[82:83], v119 offset:3072\n"
  "ds_read_b64_tr_b16 a[86:87], v120 offset:3072\n"
  "v_mfma_f32_16x16x32_bf16 v[0:3], v[48:51], a[192:195], v[0:3]\n"
  "s_add_u32 m0, s59, 0\n"
  "ds_read_b128 v[64:67], v104 offset:12288\n"
  "ds_read_b64_tr_b16 a[90:91], v121 offset:5120\n"
  "ds_read_b64_tr_b16 a[94:95], v122 offset:5120\n"
  "v_mfma_f32_16x16x32_bf16 v[0:3], v[56:59], a[216:219], v[0:3]\n"
  "s_waitcnt lgkmcnt(7)\n"
  "buffer_load_dwordx4 v114, s[40:43], s57 offen offset:0 nt lds\n"
  "buffer_load_dwordx4 v115, s[40:43], s57 offen offset:1024 nt lds\n"
  "v_mfma_f32_16x16x32_bf16 v[4:7], v[56:59], a[220:223], v[4:7]\n"
  "ds_read_b128 v[68:71], v104 offset:14336\n"
  "s_waitcnt vmcnt(12)\n"
  "ds_read_b64_tr_b16 a[96:97], v117 offset:6144\n"
  "ds_read_b64_tr_b16 a[100:101], v118 offset:6144\n"
  "v_xor_b32 v104, 0x4000, v104\n"
  "v_mfma_f32_16x16x32_bf16 v[4:7], v[48:51], a[196:199], v[4:7]\n"
  "s_mov_b32 s70, s96\n"
  "buffer_load_dwordx4 v116, s[40:43], s57 offen offset:2048  lds\n"
  "v_mfma_f32_16x16x32_bf16 v[8:11], v[48:51], a[200:203], v[8:11]\n"
  "s_add_u32 s57, s57, 6144\n"
  "ds_read_b64_tr_b16 a[104:105], v119 offset:6144\n"
  "ds_read_b64_tr_b16 a[108:109], v120 offset:6144\n"
  "v_mfma_f32_16x16x32_bf16 v[8:11], v[56:59], a[224:227], v[8:11]\n"
  "s_add_u32 m0, s59, 3072\n"
  "ds_read_b64_tr_b16 a[112:113], v121 offset:8192\n"
  "ds_read_b64_tr_b16 a[116:117], v122 offset:8192\n"
  "v_mfma_f32_16x16x32_bf16 v[12:15], v[56:59], a[228:231], v[12:15]\n"
  "s_waitcnt lgkmcnt(7)\n"
  "buffer_load_dwordx4 v114, s[40:43], s57 offen offset:0 nt lds\n"
  "buffer_load_dwordx4 v115, s[40:43], s57 offen offset:1024 nt lds\n"
  "ds_read_b128 v[72:75], v105 offset:12288\n"
  "v_mfma_f32_16x16x32_bf16 v[12:15], v[48:51], a[204:207], v[12:15]\n"
  "s_waitcnt vmcnt(12)\n"
  "ds_read_b64_tr_b16 a[98:99], v117 offset:9216\n"
  "ds_read_b64_tr_b16 a[102:103], v118 offset:9216\n"
  "v_mfma_f32_16x16x32_bf16 v[16:19], v[48:51], a[208:211], v[16:19]\n"
  "s_mov_b32 s96, s97\n"
  "buffer_load_dwordx4 v116, s[40:43], s57 offen offset:2048  lds\n"
  "v_mfma_f32_16x16x32_bf16 v[16:19], v[56:59], a[232:235], v[16:19]\n"
  "s_add_u32 s57, s57, s58\n"
  "ds_read_b64_tr_b16 a[106:107], v119 offset:9216\n"
  "ds_read_b64_tr_b16 a[110:111], v120 offset:9216\n"
  "v_mfma_f32_16x16x32_bf16 v[20:23], v[56:59], a[236:239], v[20:23]\n"
  "s_add_u32 m0, s59, 6144\n"
  "ds_read_b128 v[76:79], v105 offset:14336\n"
  "ds_read_b64_tr_b16 a[114:115], v121 offset:11264\n"
  "ds_read_b64_tr_b16 a[118:119], v122 offset:11264\n"
  "v_xor_b32 v105, 0x4000, v105\n"
  "v_mfma_f32_16x16x32_bf16 v[20:23], v[48:51], a[212:215], v[20:23]\n"
  "s_waitcnt lgkmcnt(8)\n"
  "buffer_load_dwordx4 v114, s[40:43], s57 offen offset:0 nt lds\n"
  "buffer_load_dwordx4 v115, s[40:43], s57 offen offset:1024 nt lds\n"
  "v_smfmac_f32_16x16x64_bf16 v[80:83], v[88:91], v[48:55], v124\n"
  "s_waitcnt vmcnt(11)\n"
  "ds_read_b64_tr_b16 a[120:121], v117 offset:12288\n"
  "ds_read_b64_tr_b16 a[124:125], v118 offset:12288\n"
  "v_smfmac_f32_16x16x64_bf16 v[80:83], v[88:91], v[64:71], v124\n"
  "s_nop 1\n"
  "s_max_u32 s98, s98, s62\n"
  "s_max_u32 s63, s63, s64\n"
  "s_max_u32 s98, s98, s63\n"
  "s_and_b32 s98, s98, 0x7f800000\n"
  "s_cselect_b32 s62, 0x400000, 0\n"
  "s_or_b32 s98, s98, s62\n"
  "s_nop 0\n"
  "buffer_load_dwordx4 v116, s[40:43], s57 offen offset:2048  lds\n"
  "v_mov_b32 v100, s98\n"
  "v_mfma_f32_16x16x32_bf16 v[20:23], v[52:55], v[132:135], v[20:23]\n"
  "s_add_u32 s57, s57, 6144\n"
  "ds_read_b64_tr_b16 a[128:129], v119 offset:12288\n"
  "ds_read_b64_tr_b16 a[132:133], v120 offset:12288\n"
  "v_mov_b32 v101, s98\n"
  "v_mov_b32 v102, s98\n"
  "v_mfma_f32_16x16x32_bf16 v[20:23], v[60:63], v[156:159], v[20:23]\n"
  "s_add_u32 m0, s59, 9216\n"
  "ds_read_b64_tr_b16 a[136:137], v121 offset:14336\n"
  "ds_read_b64_tr_b16 a[140:141], v122 offset:14336\n"
  "v_mov_b32 v103, s98\n"
  "v_mfma_f32_16x16x32_bf16 v[16:19], v[60:63], v[152:155], v[16:19]\n"
  "s_waitcnt lgkmcnt(6)\n"
  "buffer_load_dwordx4 v114, s[40:43], s57 offen offset:0 nt lds\n"
  "buffer_load_dwordx4 v115, s[40:43], s57 offen offset:1024 nt lds\n"
  "v_mfma_f32_16x16x32_bf16 v[16:19], v[52:55], v[128:131], v[16:19]\n"
  "s_waitcnt vmcnt(11)\n"
  "ds_read_b64_tr_b16 a[122:123], v117 offset:15360\n"
  "ds_read_b64_tr_b16 a[126:127], v118 offset:15360\n"
  "v_add_f32 v44, v80, v81\n"
  "v_mov_b32 v80, 0\n"
  "v_mfma_f32_16x16x32_bf16 v[12:15], v[52:55], a[252:255], v[12:15]\n"
  "buffer_load_dwordx4 v116, s[40:43], s57 offen offset:2048  lds\n"
  "ds_write_b32 v110, v44\n"
  "v_mfma_f32_16x16x32_bf16 v[12:15], v[60:63], v[148:151], v[12:15]\n"
  "s_add_u32 s57, s57, s84\n"
  "ds_read_b64_tr_b16 a[130:131], v119 offset:15360\n"
  "ds_read_b64_tr_b16 a[134:135], v120 offset:15360\n"
  "v_mov_b32 v81, 0\n"
  "v_mfma_f32_16x16x32_bf16 v[8:11], v[60:63], v[144:147], v[8:11]\n"
  "s_add_u32 m0, s59, 12288\n"
  "ds_read_b64_tr_b16 a[138:139], v121 offset:17408\n"
  "ds_read_b64_tr_b16 a[142:143], v122 offset:17408\n"
  "v_mfma_f32_16x16x32_bf16 v[8:11], v[52:55], a[248:251], v[8:11]\n"
  "s_waitcnt lgkmcnt(7)\n"
  "buffer_load_dwordx4 v114, s[40:43], s57 offen offset:0 nt lds\n"
  "buffer_load_dwordx4 v115, s[40:43], s57 offen offset:1024 nt lds\n"
  "v_mfma_f32_16x16x32_bf16 v[4:7], v[52:55], a[244:247], v[4:7]\n"
  "s_waitcnt vmcnt(11)\n"
  "ds_read_b64_tr_b16 a[144:145], v117 offset:0\n"
  "ds_read_b64_tr_b16 a[148:149], v118 offset:0\n"
  "v_xor_b32 v106, 0x2000, v106\n"
  "v_xor_b32 v107, 0x2000, v107\n"
  "v_mfma_f32_16x16x32_bf16 v[4:7], v[60:63], v[140:143], v[4:7]\n"
  "buffer_load_dwordx4 v116, s[40:43], s57 offen offset:2048  lds\n"
  "v_xor_b32 v110, 0x2000, v110\n"
  "v_mfma_f32_16x16x32_bf16 v[0:3], v[60:63], v[136:139], v[0:3]\n"
  "s_add_u32 s57, s57, 6144\n"
  "ds_read_b64_tr_b16 a[152:153], v119 offset:0\n"
  "ds_read_b64_tr_b16 a[156:157], v120 offset:0\n"
  "v_mfma_f32_16x16x32_bf16 v[0:3], v[52:55], a[240:243], v[0:3]\n"
  "s_add_u32 m0, s59, 15360\n"
  "ds_read_b64_tr_b16 a[160:161], v121 offset:2048\n"
  "ds_read_b64_tr_b16 a[164:165], v122 offset:2048\n"
  "s_waitcnt lgkmcnt(0)\n"
  "s_barrier\n"
  "v_mfma_f32_16x16x32_bf16 v[0:3], v[64:67], v[160:163], v[0:3]\n"
  "buffer_load_dwordx4 v114, s[40:43], s57 offen offset:0 nt lds\n"
  "buffer_load_dwordx4 v115, s[40:43], s57 offen offset:1024 nt lds\n"
  "ds_read_b128 v[48:51], v104 offset:0\n"
  "v_mfma_f32_16x16x32_bf16 v[0:3], v[72:75], v[184:187], v[0:3]\n"
  "s_waitcnt vmcnt(11)\n"
  "ds_read_b64_tr_b16 a[146:147], v117 offset:3072\n"
  "ds_read_b64_tr_b16 a[150:151], v118 offset:3072\n"
  "ds_read_b128 v[84:87], v111\n"
  "ds_read_b128 v[96:99], v111 offset:64\n"
  "v_mfma_f32_16x16x32_bf16 v[4:7], v[72:75], v[188:191], v[4:7]\n"
  "s_sub_u32 s90, s90, 1\n"
  "s_cselect_b32 s63, s91, 6144\n"
  "s_cmov_b64 s[90:91], s[92:93]\n"
  "s_cmov_b64 s[92:93], s[94:95]\n"
  "buffer_load_dwordx4 v116, s[40:43], s57 offen offset:2048  lds\n"
  "ds_read_b128 v[52:55], v104 offset:2048\n"
  "v_xor_b32 v111, 0x2000, v111\n"
  "v_mfma_f32_16x16x32_bf16 v[4:7], v[64:67], v[164:167], v[4:7]\n"
  "s_add_u32 s57, s57, s58\n"
  "ds_read_b64_tr_b16 a[154:155], v119 offset:3072\n"
  "ds_read_b64_tr_b16 a[158:159], v120 offset:3072\n"
  "v_mfma_f32_16x16x32_bf16 v[8:11], v[64:67], v[168:171], v[8:11]\n"
  "s_add_u32 m0, s59, 0\n"
  "ds_read_b64_tr_b16 a[162:163], v121 offset:5120\n"
  "ds_read_b64_tr_b16 a[166:167], v122 offset:5120\n"
  "v_mfma_f32_16x16x32_bf16 v[8:11], v[72:75], v[192:195], v[8:11]\n"
  "buffer_load_dwordx4 v114, s[40:43], s57 offen offset:0 nt lds\n"
  "buffer_load_dwordx4 v115, s[40:43], s57 offen offset:1024 nt lds\n"
  "v_mfma_f32_16x16x32_bf16 v[12:15], v[72:75], v[196:199], v[12:15]\n"
  "ds_read_b128 v[56:59], v105 offset:0\n"
  "s_waitcnt vmcnt(11)\n"
  "ds_read_b64_tr_b16 a[168:169], v117 offset:6144\n"
  "ds_read_b64_tr_b16 a[172:173], v118 offset:6144\n"
  "v_mfma_f32_16x16x32_bf16 v[12:15], v[64:67], v[172:175], v[12:15]\n"
  "s_add_u32 s72, s72, 7\n"
  "buffer_load_dwordx4 v116, s[40:43], s57 offen offset:2048  lds\n"
  "v_mfma_f32_16x16x32_bf16 v[16:19], v[64:67], v[176:179], v[16:19]\n"
  "s_add_u32 s57, s57, 6144\n"
  "ds_read_b64_tr_b16 a[176:177], v119 offset:6144\n"
  "ds_read_b64_tr_b16 a[180:181], v120 offset:6144\n"
  "v_mfma_f32_16x16x32_bf16 v[16:19], v[72:75], v[200:203], v[16:19]\n"
  "s_add_u32 m0, s59, 3072\n"
  "ds_read_b64_tr_b16 a[184:185], v121 offset:8192\n"
  "ds_read_b64_tr_b16 a[188:189], v122 offset:8192\n"
  "v_mfma_f32_16x16x32_bf16 v[20:23], v[72:75], v[204:207], v[20:23]\n"
  "s_waitcnt lgkmcnt(7)\n"
  "buffer_load_dwordx4 v114, s[40:43], s57 offen offset:0 nt lds\n"
  "buffer_load_dwordx4 v115, s[40:43], s57 offen offset:1024 nt lds\n"
  "ds_read_b128 v[60:63], v105 offset:2048\n"
  "v_mfma_f32_16x16x32_bf16 v[20:23], v[64:67], v[180:183], v[20:23]\n"
  "s_waitcnt vmcnt(11)\n"
  "ds_read_b64_tr_b16 a[170:171], v117 offset:9216\n"
  "ds_read_b64_tr_b16 a[174:175], v118 offset:9216\n"
  "v_mfma_f32_16x16x32_bf16 v[20:23], v[68:71], v[228:231], v[20:23]\n"
  "buffer_load_dwordx4 v116, s[40:43], s57 offen offset:2048  lds\n"
  "v_add_f32 v84, v84, v96\n"
  "v_add_f32 v85, v85, v97\n"
  "v_mfma_f32_16x16x32_bf16 v[44:47], v[76:79], v[252:255], v[20:23]\n"
  "s_add_u32 s57, s57, s84\n"
  "ds_read_b64_tr_b16 a[178:179], v119 offset:9216\n"
  "ds_read_b64_tr_b16 a[182:183], v120 offset:9216\n"
  "v_add_f32 v86, v86, v98\n"
  "v_add_f32 v87, v87, v99\n"
  "v_mfma_f32_16x16x32_bf16 v[16:19], v[76:79], v[248:251], v[16:19]\n"
  "s_add_u32 m0, s59, 6144\n"
  "ds_read_b64_tr_b16 a[186:187], v121 offset:11264\n"
  "ds_read_b64_tr_b16 a[190:191], v122 offset:11264\n"
  "v_max3_f32 v82, v82, v84, v85\n"
  "v_max3_f32 v82, v82, v86, v87\n"
  "v_mfma_f32_16x16x32_bf16 v[40:43], v[68:71], v[224:227], v[16:19]\n"
  "s_waitcnt lgkmcnt(7)\n"
  "buffer_load_dwordx4 v114, s[40:43], s57 offen offset:0 nt lds\n"
  "buffer_load_dwordx4 v115, s[40:43], s57 offen offset:1024 nt lds\n"
  "v_mfma_f32_16x16x32_bf16 v[12:15], v[68:71], v[220:223], v[12:15]\n"
  "s_waitcnt vmcnt(11)\n"
  "ds_read_b64_tr_b16 a[192:193], v117 offset:12288\n"
  "ds_read_b64_tr_b16 a[196:197], v118 offset:12288\n"
  "v_rcp_f32 v84, v84\n"
  "v_mfma_f32_16x16x32_bf16 v[36:39], v[76:79], v[244:247], v[12:15]\n"
  "s_sub_u32 s56, s56, 1\n"
  "buffer_load_dwordx4 v116, s[40:43], s57 offen offset:2048  lds\n"
  "v_rcp_f32 v85, v85\n"
  "v_mfma_f32_16x16x32_bf16 v[8:11], v[76:79], v[240:243], v[8:11]\n"
  "s_add_u32 s57, s57, 6144\n"
  "ds_read_b64_tr_b16 a[200:201], v119 offset:12288\n"
  "ds_read_b64_tr_b16 a[204:205], v120 offset:12288\n"
  "v_rcp_f32 v86, v86\n"
  "v_mfma_f32_16x16x32_bf16 v[32:35], v[68:71], v[216:219], v[8:11]\n"
  "s_add_u32 m0, s59, 9216\n"
  "ds_read_b64_tr_b16 a[208:209], v121 offset:14336\n"
  "ds_read_b64_tr_b16 a[212:213], v122 offset:14336\n"
  "v_rcp_f32 v87, v87\n"
  "v_mfma_f32_16x16x32_bf16 v[4:7], v[68:71], v[212:215], v[4:7]\n"
  "s_waitcnt lgkmcnt(6)\n"
  "buffer_load_dwordx4 v114, s[40:43], s57 offen offset:0 nt lds\n"
  "buffer_load_dwordx4 v115, s[40:43], s57 offen offset:1024 nt lds\n"
  "v_mfma_f32_16x16x32_bf16 v[28:31], v[76:79], v[236:239], v[4:7]\n"
  "s_waitcnt vmcnt(11)\n"
  "ds_read_b64_tr_b16 a[194:195], v117 offset:15360\n"
  "ds_read_b64_tr_b16 a[198:199], v118 offset:15360\n"
  "v_max3_f32 v83, v83, v84, v85\n"
  "v_max3_f32 v83, v83, v86, v87\n"
  "v_mfma_f32_16x16x32_bf16 v[0:3], v[76:79], v[232:235], v[0:3]\n"
  "s_sub_i32 s61, s56, 5\n"
  "s_max_i32 s61, s61, 0\n"
  "buffer_load_dwordx4 v116, s[40:43], s57 offen offset:2048  lds\n"
  "v_mul_f32 v96, s99, v84\n"
  "v_mul_f32 v97, s99, v85\n"
  "v_mfma_f32_16x16x32_bf16 v[24:27], v[68:71], v[208:211], v[0:3]\n"
  "s_add_u32 s57, s57, s58\n"
  "ds_read_b64_tr_b16 a[202:203], v119 offset:15360\n"
  "ds_read_b64_tr_b16 a[206:207], v120 offset:15360\n"
  "v_mul_f32 v98, s99, v86\n"
  "v_mul_f32 v99, s99, v87\n"
  "v_mfma_f32_16x16x32_bf16 v[0:3], v[48:51], a[0:3], v[100:103]\n"
  "s_add_u32 m0, s59, 12288\n"
  "ds_read_b128 v[64:67], v104 offset:4096\n"
  "ds_read_b64_tr_b16 a[210:211], v121 offset:17408\n"
  "ds_read_b64_tr_b16 a[214:215], v122 offset:17408\n"
  "v_fma_f32 v40, v40, v84, v96\n"
  "v_mfma_f32_16x16x32_bf16 v[0:3], v[56:59], a[24:27], v[0:3]\n"
  "s_waitcnt lgkmcnt(7)\n"
  "buffer_load_dwordx4 v114, s[40:43], s57 offen offset:0 nt lds\n"
  "buffer_load_dwordx4 v115, s[40:43], s57 offen offset:1024 nt lds\n"
  "v_mfma_f32_16x16x32_bf16 v[4:7], v[56:59], a[28:31], v[100:103]\n"
  "ds_read_b128 v[68:71], v104 offset:6144\n"
  "s_waitcnt vmcnt(11)\n"
  "ds_read_b64_tr_b16 a[216:217], v117 offset:0\n"
  "ds_read_b64_tr_b16 a[220:221], v118 offset:0\n"
  "v_fma_f32 v44, v44, v84, v96\n"
  "v_mfma_f32_16x16x32_bf16 v[4:7], v[48:51], a[4:7], v[4:7]\n"
  "s_xor_b32 s99, 0x80000000, s98\n"
  "buffer_load_dwordx4 v116, s[40:43], s57 offen offset:2048  lds\n"
  "ds_read_b64_tr_b16 v[92:93], v108 offset:0\n"
  "ds_read_b64_tr_b16 v[94:95], v108 offset:32\n"
  "v_mfma_f32_16x16x32_bf16 v[8:11], v[48:51], a[8:11], v[100:103]\n"
  "s_add_u32 s57, s57, 6144\n"
  "ds_read_b64_tr_b16 a[224:225], v119 offset:0\n"
  "ds_read_b64_tr_b16 a[228:229], v120 offset:0\n"
  "v_fma_f32 v41, v41, v85, v97\n"
  "v_fma_f32 v45, v45, v85, v97\n"
  "v_mfma_f32_16x16x32_bf16 v[8:11], v[56:59], a[32:35], v[8:11]\n"
  "s_add_u32 m0, s59, 15360\n"
  "ds_read_b64_tr_b16 a[232:233], v121 offset:2048\n"
  "ds_read_b64_tr_b16 a[236:237], v122 offset:2048\n"
  "v_fma_f32 v42, v42, v86, v98\n"
  "v_fma_f32 v46, v46, v86, v98\n"
  "v_mfma_f32_16x16x32_bf16 v[12:15], v[56:59], a[36:39], v[100:103]\n"
  "s_waitcnt lgkmcnt(9)\n"
  "buffer_load_dwordx4 v114, s[40:43], s57 offen offset:0 nt lds\n"
  "buffer_load_dwordx4 v115, s[40:43], s57 offen offset:1024 nt lds\n"
  "ds_read_b128 v[72:75], v105 offset:4096\n"
  "v_mfma_f32_16x16x32_bf16 v[12:15], v[48:51], a[12:15], v[12:15]\n"
  "s_waitcnt vmcnt(11)\n"
  "ds_read_b64_tr_b16 a[218:219], v117 offset:3072\n"
  "ds_read_b64_tr_b16 a[222:223], v118 offset:3072\n"
  "v_fma_f32 v43, v43, v87, v99\n"
  "v_fma_f32 v47, v47, v87, v99\n"
  "v_mfma_f32_16x16x32_bf16 v[16:19], v[48:51], a[16:19], v[100:103]\n"
  "buffer_load_dwordx4 v116, s[40:43], s57 offen offset:2048  lds\n"
  "v_fma_f32 v24, v24, v84, v96\n"
  "v_fma_f32 v25, v25, v85, v97\n"
  "v_mfma_f32_16x16x32_bf16 v[16:19], v[56:59], a[40:43], v[16:19]\n"
  "s_add_u32 s57, s57, s84\n"
  "ds_read_b64_tr_b16 a[226:227], v119 offset:3072\n"
  "ds_read_b64_tr_b16 a[230:231], v120 offset:3072\n"
  "v_fma_f32 v28, v28, v84, v96\n"
  "v_fma_f32 v29, v29, v85, v97\n"
  "v_mfma_f32_16x16x32_bf16 v[20:23], v[56:59], a[44:47], v[100:103]\n"
  "s_add_u32 m0, s59, 0\n"
  "ds_read_b128 v[76:79], v105 offset:6144\n"
  "ds_read_b64_tr_b16 a[234:235], v121 offset:5120\n"
  "ds_read_b64_tr_b16 a[238:239], v122 offset:5120\n"
  "v_fma_f32 v32, v32, v84, v96\n"
  "v_mfma_f32_16x16x32_bf16 v[20:23], v[48:51], a[20:23], v[20:23]\n"
  "s_waitcnt lgkmcnt(8)\n"
  "buffer_load_dwordx4 v114, s[40:43], s57 offen offset:0 nt lds\n"
  "buffer_load_dwordx4 v115, s[40:43], s57 offen offset:1024 nt lds\n"
  "v_smfmac_f32_16x16x64_bf16 v[80:83], v[88:91], v[48:55], v124\n"
  "s_waitcnt vmcnt(11)\n"
  "ds_read_b64_tr_b16 a[240:241], v117 offset:6144\n"
  "ds_read_b64_tr_b16 a[244:245], v118 offset:6144\n"
  "v_fma_f32 v33, v33, v85, v97\n"
  "v_smfmac_f32_16x16x64_bf16 v[80:83], v[88:91], v[64:71], v124\n"
  "buffer_load_dwordx4 v116, s[40:43], s57 offen offset:2048  lds\n"
  "v_fma_f32 v36, v36, v84, v96\n"
  "v_mfma_f32_16x16x32_bf16 v[20:23], v[52:55], a[68:71], v[20:23]\n"
  "s_add_u32 s57, s57, 6144\n"
  "buffer_store_dwordx4 v[92:95], v109, s[48:51], s68 offen offset:0 nt\n"
  "ds_read_b64_tr_b16 a[248:249], v119 offset:6144\n"
  "ds_read_b64_tr_b16 a[252:253], v120 offset:6144\n"
  "v_fma_f32 v37, v37, v85, v97\n"
  "v_mfma_f32_16x16x32_bf16 v[20:23], v[60:63], a[92:95], v[20:23]\n"
  "s_add_u32 m0, s59, 3072\n"
  "ds_read_b64_tr_b16 v[128:129], v121 offset:8192\n"
  "ds_read_b64_tr_b16 v[132:133], v122 offset:8192\n"
  "v_fma_f32 v26, v26, v86, v98\n"
  "v_fma_f32 v27, v27, v87, v99\n"
  "v_mfma_f32_16x16x32_bf16 v[16:19], v[60:63], a[88:91], v[16:19]\n"
  "s_waitcnt lgkmcnt(6)\n"
  "buffer_load_dwordx4 v114, s[40:43], s57 offen offset:0 nt lds\n"
  "buffer_load_dwordx4 v115, s[40:43], s57 offen offset:1024 nt lds\n"
  "v_mfma_f32_16x16x32_bf16 v[16:19], v[52:55], a[64:67], v[16:19]\n"
  "s_waitcnt vmcnt(12)\n"
  "ds_read_b64_tr_b16 a[242:243], v117 offset:9216\n"
  "ds_read_b64_tr_b16 a[246:247], v118 offset:9216\n"
  "v_fma_f32 v30, v30, v86, v98\n"
  "v_fma_f32 v31, v31, v87, v99\n"
  "v_mfma_f32_16x16x32_bf16 v[12:15], v[52:55], a[60:63], v[12:15]\n"
  "buffer_load_dwordx4 v116, s[40:43], s57 offen offset:2048  lds\n"
  "v_fma_f32 v34, v34, v86, v98\n"
  "v_fma_f32 v35, v35, v87, v99\n"
  "v_mfma_f32_16x16x32_bf16 v[12:15], v[60:63], a[84:87], v[12:15]\n"
  "s_add_u32 s57, s57, s58\n"
  "ds_read_b64_tr_b16 a[250:251], v119 offset:9216\n"
  "ds_read_b64_tr_b16 a[254:255], v120 offset:9216\n"
  "v_fma_f32 v38, v38, v86, v98\n"
  "v_fma_f32 v39, v39, v87, v99\n"
  "v_mfma_f32_16x16x32_bf16 v[8:11], v[60:63], a[80:83], v[8:11]\n"
  "s_add_u32 m0, s59, 6144\n"
  "ds_read_b64_tr_b16 v[130:131], v121 offset:11264\n"
  "ds_read_b64_tr_b16 v[134:135], v122 offset:11264\n"
  "ds_read_b64_tr_b16 v[96:97], v108 offset:2304\n"
  "ds_read_b64_tr_b16 v[98:99], v108 offset:2336\n"
  "v_mfma_f32_16x16x32_bf16 v[8:11], v[52:55], a[56:59], v[8:11]\n"
  "s_waitcnt lgkmcnt(8)\n"
  "buffer_load_dwordx4 v114, s[40:43], s57 offen offset:0 nt lds\n"
  "buffer_load_dwordx4 v115, s[40:43], s57 offen offset:1024 nt lds\n"
  "v_mfma_f32_16x16x32_bf16 v[4:7], v[52:55], a[52:55], v[4:7]\n"
  "s_waitcnt vmcnt(12)\n"
  "ds_read_b64_tr_b16 v[136:137], v117 offset:12288\n"
  "ds_read_b64_tr_b16 v[140:141], v118 offset:12288\n"
  "v_cvt_pk_bf16_f32 v40, v40, v44\n"
  "v_cvt_pk_bf16_f32 v41, v41, v45\n"
  "v_mfma_f32_16x16x32_bf16 v[4:7], v[60:63], a[76:79], v[4:7]\n"
  "buffer_load_dwordx4 v116, s[40:43], s57 offen offset:2048  lds\n"
  "v_cvt_pk_bf16_f32 v42, v42, v46\n"
  "v_cvt_pk_bf16_f32 v43, v43, v47\n"
  "v_mfma_f32_16x16x32_bf16 v[0:3], v[60:63], a[72:75], v[0:3]\n"
  "s_add_u32 s57, s57, 6144\n"
  "ds_read_b64_tr_b16 v[144:145], v119 offset:12288\n"
  "ds_read_b64_tr_b16 v[148:149], v120 offset:12288\n"
  "v_cvt_pk_bf16_f32 v24, v24, v28\n"
  "v_cvt_pk_bf16_f32 v28, v25, v29\n"
  "v_mfma_f32_16x16x32_bf16 v[0:3], v[52:55], a[48:51], v[0:3]\n"
  "s_add_u32 m0, s59, 9216\n"
  "ds_read_b64_tr_b16 v[152:153], v121 offset:14336\n"
  "ds_read_b64_tr_b16 v[156:157], v122 offset:14336\n"
  "v_cvt_pk_bf16_f32 v25, v32, v36\n"
  "v_cvt_pk_bf16_f32 v29, v33, v37\n"
  "v_mfma_f32_16x16x32_bf16 v[0:3], v[64:67], a[96:99], v[0:3]\n"
  "s_waitcnt lgkmcnt(8)\n"
  "buffer_load_dwordx4 v114, s[40:43], s57 offen offset:0 nt lds\n"
  "buffer_load_dwordx4 v115, s[40:43], s57 offen offset:1024 nt lds\n"
  "ds_read_b128 v[48:51], v104 offset:8192\n"
  "v_mfma_f32_16x16x32_bf16 v[0:3], v[72:75], a[120:123], v[0:3]\n"
  "s_waitcnt vmcnt(12)\n"
  "ds_read_b64_tr_b16 v[138:139], v117 offset:15360\n"
  "ds_read_b64_tr_b16 v[142:143], v118 offset:15360\n"
  "v_cvt_pk_bf16_f32 v26, v26, v30\n"
  "v_cvt_pk_bf16_f32 v30, v27, v31\n"
  "v_mfma_f32_16x16x32_bf16 v[4:7], v[72:75], a[124:127], v[4:7]\n"
  "buffer_load_dwordx4 v116, s[40:43], s57 offen offset:2048  lds\n"
  "ds_read_b128 v[52:55], v104 offset:10240\n"
  "v_cvt_pk_bf16_f32 v27, v34, v38\n"
  "v_mfma_f32_16x16x32_bf16 v[4:7], v[64:67], a[100:103], v[4:7]\n"
  "s_add_u32 s57, s57, s84\n"
  "s_waitcnt lgkmcnt(10)\n"
  "buffer_store_dwordx4 v[96:99], v109, s[48:51], s68 offen offset:128 nt\n"
  "ds_read_b64_tr_b16 v[146:147], v119 offset:15360\n"
  "ds_read_b64_tr_b16 v[150:151], v120 offset:15360\n"
  "v_cvt_pk_bf16_f32 v31, v35, v39\n"
  "v_mfma_f32_16x16x32_bf16 v[8:11], v[64:67], a[104:107], v[8:11]\n"
  "s_add_u32 m0, s59, 12288\n"
  "ds_read_b64_tr_b16 v[154:155], v121 offset:17408\n"
  "ds_read_b64_tr_b16 v[158:159], v122 offset:17408\n"
  "ds_write2_b32 v107, v40, v41 offset0:0 offset1:72\n"
  "v_mfma_f32_16x16x32_bf16 v[8:11], v[72:75], a[128:131], v[8:11]\n"
  "s_waitcnt lgkmcnt(9)\n"
  "buffer_load_dwordx4 v114, s[40:43], s57 offen offset:0 nt lds\n"
  "buffer_load_dwordx4 v115, s[40:43], s57 offen offset:1024 nt lds\n"
  "v_mfma_f32_16x16x32_bf16 v[12:15], v[72:75], a[132:135], v[12:15]\n"
  "ds_read_b128 v[56:59], v105 offset:8192\n"
  "s_waitcnt vmcnt(13)\n"
  "ds_read_b64_tr_b16 v[160:161], v117 offset:0\n"
  "ds_read_b64_tr_b16 v[164:165], v118 offset:0\n"
  "ds_read_b64_tr_b16 v[92:93], v108 offset:4608\n"
  "v_mfma_f32_16x16x32_bf16 v[12:15], v[64:67], a[108:111], v[12:15]\n"
  "buffer_load_dwordx4 v116, s[40:43], s57 offen offset:2048  lds\n"
  "ds_read_b64_tr_b16 v[94:95], v108 offset:4640\n"
  "v_xor_b32 v108, 0x2000, v108\n"
  "v_mfma_f32_16x16x32_bf16 v[16:19], v[64:67], a[112:115], v[16:19]\n"
  "s_add_u32 s57, s57, 6144\n"
  "ds_read_b64_tr_b16 v[168:169], v119 offset:0\n"
  "ds_read_b64_tr_b16 v[172:173], v120 offset:0\n"
  "ds_write2_b32 v107, v42, v43 offset0:144 offset1:216\n"
  "v_mfma_f32_16x16x32_bf16 v[16:19], v[72:75], a[136:139], v[16:19]\n"
  "s_add_u32 m0, s59, 15360\n"
  "ds_read_b64_tr_b16 v[176:177], v121 offset:2048\n"
  "ds_read_b64_tr_b16 v[180:181], v122 offset:2048\n"
  "ds_write_b64 v106, v[24:25] offset:0\n"
  "v_mfma_f32_16x16x32_bf16 v[20:23], v[72:75], a[140:143], v[20:23]\n"
  "s_waitcnt lgkmcnt(12)\n"
  "buffer_load_dwordx4 v114, s[40:43], s57 offen offset:0 nt lds\n"
  "buffer_load_dwordx4 v115, s[40:43], s57 offen offset:1024 nt lds\n"
  "ds_read_b128 v[60:63], v105 offset:10240\n"
  "v_mfma_f32_16x16x32_bf16 v[20:23], v[64:67], a[116:119], v[20:23]\n"
  "s_waitcnt vmcnt(12)\n"
  "ds_read_b64_tr_b16 v[162:163], v117 offset:3072\n"
  "ds_read_b64_tr_b16 v[166:167], v118 offset:3072\n"
  "ds_write_b64 v106, v[28:29] offset:288\n"
  "v_mfma_f32_16x16x32_bf16 v[20:23], v[68:71], a[164:167], v[20:23]\n"
  "buffer_load_dwordx4 v116, s[40:43], s57 offen offset:2048  lds\n"
  "ds_write_b64 v106, v[26:27] offset:576\n"
  "v_mfma_f32_16x16x32_bf16 v[20:23], v[76:79], a[188:191], v[20:23]\n"
  "s_add_u32 s57, s57, s58\n"
  "ds_read_b64_tr_b16 v[170:171], v119 offset:3072\n"
  "ds_read_b64_tr_b16 v[174:175], v120 offset:3072\n"
  "ds_write_b64 v106, v[30:31] offset:864\n"
  "v_mfma_f32_16x16x32_bf16 v[16:19], v[76:79], a[184:187], v[16:19]\n"
  "s_add_u32 m0, s59, 0\n"
  "ds_read_b64_tr_b16 v[178:179], v121 offset:5120\n"
  "ds_read_b64_tr_b16 v[182:183], v122 offset:5120\n"
  "v_mfma_f32_16x16x32_bf16 v[16:19], v[68:71], a[160:163], v[16:19]\n"
  "s_waitcnt lgkmcnt(11)\n"
  "buffer_load_dwordx4 v114, s[40:43], s57 offen offset:0 nt lds\n"
  "buffer_load_dwordx4 v115, s[40:43], s57 offen offset:1024 nt lds\n"
  "v_mfma_f32_16x16x32_bf16 v[12:15], v[68:71], a[156:159], v[12:15]\n"
  "buffer_store_dwordx4 v[92:95], v109, s[48:51], s68 offen offset:256 nt\n"
  "s_waitcnt vmcnt(13)\n"
  "ds_read_b64_tr_b16 v[184:185], v117 offset:6144\n"
  "ds_read_b64_tr_b16 v[188:189], v118 offset:6144\n"
  "v_mfma_f32_16x16x32_bf16 v[12:15], v[76:79], a[180:183], v[12:15]\n"
  "s_add_u32 s68, s68, s69\n"
  "buffer_load_dwordx4 v116, s[40:43], s57 offen offset:2048  lds\n"
  "v_mfma_f32_16x16x32_bf16 v[8:11], v[76:79], a[176:179], v[8:11]\n"
  "s_add_u32 s57, s57, 6144\n"
  "ds_read_b64_tr_b16 v[192:193], v119 offset:6144\n"
  "ds_read_b64_tr_b16 v[196:197], v120 offset:6144\n"
  "v_mfma_f32_16x16x32_bf16 v[8:11], v[68:71], a[152:155], v[8:11]\n"
  "s_add_u32 m0, s59, 3072\n"
  "ds_read_b64_tr_b16 v[200:201], v121 offset:8192\n"
  "ds_read_b64_tr_b16 v[204:205], v122 offset:8192\n"
  "v_mfma_f32_16x16x32_bf16 v[4:7], v[68:71], a[148:151], v[4:7]\n"
  "s_waitcnt lgkmcnt(6)\n"
  "buffer_load_dwordx4 v114, s[40:43], s57 offen offset:0 nt lds\n"
  "buffer_load_dwordx4 v115, s[40:43], s57 offen offset:1024 nt lds\n"
  "v_mfma_f32_16x16x32_bf16 v[4:7], v[76:79], a[172:175], v[4:7]\n"
  "s_waitcnt vmcnt(13)\n"
  "ds_read_b64_tr_b16 v[186:187], v117 offset:9216\n"
  "ds_read_b64_tr_b16 v[190:191], v118 offset:9216\n"
  "v_mfma_f32_16x16x32_bf16 v[0:3], v[76:79], a[168:171], v[0:3]\n"
  "s_mov_b32 s69, s70\n"
  "buffer_load_dwordx4 v116, s[40:43], s57 offen offset:2048  lds\n"
  "v_mfma_f32_16x16x32_bf16 v[0:3], v[68:71], a[144:147], v[0:3]\n"
  "s_add_u32 s57, s57, s84\n"
  "s_add_u32 m0, s66, 0\n"
  "ds_read_b64_tr_b16 v[194:195], v119 offset:9216\n"
  "ds_read_b64_tr_b16 v[198:199], v120 offset:9216\n"
  "v_mfma_f32_16x16x32_bf16 v[0:3], v[48:51], a[192:195], v[0:3]\n"
  "buffer_load_dwordx4 v112, s[44:47], s67 offen offset:0  lds\n"
  "ds_read_b128 v[64:67], v104 offset:12288\n"
  "ds_read_b64_tr_b16 v[202:203], v121 offset:11264\n"
  "ds_read_b64_tr_b16 v[206:207], v122 offset:11264\n"
  "v_mfma_f32_16x16x32_bf16 v[0:3], v[56:59], a[216:219], v[0:3]\n"
  "s_add_u32 m0, s66, 1920\n"
  "v_mfma_f32_16x16x32_bf16 v[4:7], v[56:59], a[220:223], v[4:7]\n"
  "buffer_load_dwordx4 v112, s[44:47], s67 offen offset:128  lds\n"
  "ds_read_b128 v[68:71], v104 offset:14336\n"
  "s_waitcnt vmcnt(12)\n"
  "ds_read_b64_tr_b16 v[208:209], v117 offset:12288\n"
  "ds_read_b64_tr_b16 v[212:213], v118 offset:12288\n"
  "v_mfma_f32_16x16x32_bf16 v[4:7], v[48:51], a[196:199], v[4:7]\n"
  "s_add_u32 m0, s66, 3840\n"
  "v_xor_b32 v104, 0x4000, v104\n"
  "v_mfma_f32_16x16x32_bf16 v[8:11], v[48:51], a[200:203], v[8:11]\n"
  "buffer_load_dwordx4 v112, s[44:47], s67 offen offset:256  lds\n"
  "ds_read_b64_tr_b16 v[216:217], v119 offset:12288\n"
  "ds_read_b64_tr_b16 v[220:221], v120 offset:12288\n"
  "v_mfma_f32_16x16x32_bf16 v[8:11], v[56:59], a[224:227], v[8:11]\n"
  "s_add_u32 s66, s66, 6144\n"
  "ds_read_b64_tr_b16 v[224:225], v121 offset:14336\n"
  "ds_read_b64_tr_b16 v[228:229], v122 offset:14336\n"
  "v_mfma_f32_16x16x32_bf16 v[12:15], v[56:59], a[228:231], v[12:15]\n"
  "s_cmp_ge_u32 s66, 86016\n"
  "s_cselect_b32 s64, 36864, 0\n"
  "s_sub_u32 s66, s66, s64\n"
  "s_add_u32 m0, s66, 0\n"
  "ds_read_b128 v[72:75], v105 offset:12288\n"
  "v_mfma_f32_16x16x32_bf16 v[12:15], v[48:51], a[204:207], v[12:15]\n"
  "s_add_u32 s67, s67, s63\n"
  "buffer_load_dwordx4 v112, s[44:47], s67 offen offset:0  lds\n"
  "s_waitcnt vmcnt(11)\n"
  "ds_read_b64_tr_b16 v[210:211], v117 offset:15360\n"
  "ds_read_b64_tr_b16 v[214:215], v118 offset:15360\n"
  "v_mfma_f32_16x16x32_bf16 v[16:19], v[48:51], a[208:211], v[16:19]\n"
  "s_add_u32 m0, s66, 1920\n"
  "v_mfma_f32_16x16x32_bf16 v[16:19], v[56:59], a[232:235], v[16:19]\n"
  "buffer_load_dwordx4 v112, s[44:47], s67 offen offset:128  lds\n"
  "ds_read_b64_tr_b16 v[218:219], v119 offset:15360\n"
  "ds_read_b64_tr_b16 v[222:223], v120 offset:15360\n"
  "v_mfma_f32_16x16x32_bf16 v[20:23], v[56:59], a[236:239], v[20:23]\n"
  "s_add_u32 m0, s66, 3840\n"
  "ds_read_b128 v[76:79], v105 offset:14336\n"
  "ds_read_b64_tr_b16 v[226:227], v121 offset:17408\n"
  "ds_read_b64_tr_b16 v[230:231], v122 offset:17408\n"
  "v_xor_b32 v105, 0x4000, v105\n"
  "v_mfma_f32_16x16x32_bf16 v[20:23], v[48:51], a[212:215], v[20:23]\n"
  "buffer_load_dwordx4 v112, s[44:47], s67 offen offset:256  lds\n"
  "v_smfmac_f32_16x16x64_bf16 v[80:83], v[88:91], v[48:55], v124\n"
  "s_waitcnt lgkmcnt(14)\n"
  "s_waitcnt vmcnt(9)\n"
  "ds_read_b64_tr_b16 v[232:233], v117 offset:0\n"
  "ds_read_b64_tr_b16 v[236:237], v118 offset:0\n"
  "v_smfmac_f32_16x16x64_bf16 v[80:83], v[88:91], v[64:71], v124\n"
  "s_add_u32 s66, s66, 6144\n"
  "s_cmp_ge_u32 s66, 86016\n"
  "s_cselect_b32 s64, 36864, 0\n"
  "s_sub_u32 s66, s66, s64\n"
  "s_add_u32 m0, s66, 0\n"
  "v_mfma_f32_16x16x32_bf16 v[20:23], v[52:55], v[132:135], v[20:23]\n"
  "s_sub_u32 s90, s90, 1\n"
  "s_cselect_b32 s63, s91, 6144\n"
  "s_cmov_b64 s[90:91], s[92:93]\n"
  "s_cmov_b64 s[92:93], s[94:95]\n"
  "s_add_u32 s67, s67, s63\n"
  "buffer_load_dwordx4 v112, s[44:47], s67 offen offset:0  lds\n"
  "ds_read_b64_tr_b16 v[240:241], v119 offset:0\n"
  "ds_read_b64_tr_b16 v[244:245], v120 offset:0\n"
  "v_mfma_f32_16x16x32_bf16 v[20:23], v[60:63], v[156:159], v[20:23]\n"
  "s_add_u32 m0, s66, 1920\n"
  "ds_read_b64_tr_b16 v[248:249], v121 offset:2048\n"
  "ds_read_b64_tr_b16 v[252:253], v122 offset:2048\n"
  "v_mfma_f32_16x16x32_bf16 v[16:19], v[60:63], v[152:155], v[16:19]\n"
  "buffer_load_dwordx4 v112, s[44:47], s67 offen offset:128  lds\n"
  "v_mfma_f32_16x16x32_bf16 v[16:19], v[52:55], v[128:131], v[16:19]\n"
  "s_add_u32 m0, s66, 3840\n"
  "s_waitcnt vmcnt(8)\n"
  "ds_read_b64_tr_b16 v[234:235], v117 offset:3072\n"
  "ds_read_b64_tr_b16 v[238:239], v118 offset:3072\n"
  "v_add_f32 v44, v80, v81\n"
  "v_mov_b32 v80, 0\n"
  "v_mfma_f32_16x16x32_bf16 v[12:15], v[52:55], a[252:255], v[12:15]\n"
  "buffer_load_dwordx4 v112, s[44:47], s67 offen offset:256  lds\n"
  "v_mfma_f32_16x16x32_bf16 v[12:15], v[60:63], v[148:151], v[12:15]\n"
  "s_add_u32 s72, s72, 1\n"
  "ds_read_b64_tr_b16 v[242:243], v119 offset:3072\n"
  "ds_read_b64_tr_b16 v[246:247], v120 offset:3072\n"
  "ds_write_b32 v110, v44\n"
  "v_mfma_f32_16x16x32_bf16 v[8:11], v[60:63], v[144:147], v[8:11]\n"
  "s_lshr_b32 s74, s72, 1\n"
  "ds_read_b64_tr_b16 v[250:251], v121 offset:5120\n"
  "ds_read_b64_tr_b16 v[254:255], v122 offset:5120\n"
  "v_readlane_b32 s73, v126, s74\n"
  "v_mfma_f32_16x16x32_bf16 v[8:11], v[52:55], a[248:251], v[8:11]\n"
  "s_add_u32 s66, s66, 6144\n"
  "v_xor_b32 v106, 0x2000, v106\n"
  "v_xor_b32 v107, 0x2000, v107\n"
  "v_mov_b32 v81, 0\n"
  "v_mfma_f32_16x16x32_bf16 v[4:7], v[52:55], a[244:247], v[4:7]\n"
  "s_cmp_ge_u32 s66, 86016\n"
  "s_cselect_b32 s64, 36864, 0\n"
  "v_xor_b32 v110, 0x2000, v110\n"
  "v_mfma_f32_16x16x32_bf16 v[4:7], v[60:63], v[140:143], v[4:7]\n"
  "s_sub_u32 s66, s66, s64\n"
  "v_mfma_f32_16x16x32_bf16 v[0:3], v[60:63], v[136:139], v[0:3]\n"
  "s_sub_u32 s90, s90, 1\n"
  "s_cselect_b32 s63, s91, 6144\n"
  "s_cmov_b64 s[90:91], s[92:93]\n"
  "s_cmov_b64 s[92:93], s[94:95]\n"
  "v_mfma_f32_16x16x32_bf16 v[0:3], v[52:55], a[240:243], v[0:3]\n"
  "s_add_u32 s67, s67, s63\n"
  "s_lshl_b32 s74, s72, 4\n"
  "s_lshr_b32 s73, s73, s74\n"
  "s_and_b32 s73, s73, 0xffff\n"
  "s_lshl_b32 s73, s73, 13\n"
  "s_movk_i32 s70, 6144\n"
  "s_branch B_H1_%=\n"
  "B_LAST_%=:\n"
  "s_waitcnt lgkmcnt(0)\n"
  "s_barrier\n"
  "v_mfma_f32_16x16x32_bf16 v[20:23], v[64:67], v[180:183], v[20:23]\n"
  "ds_read_b64_tr_b16 v[48:49], v108 offset:0\n"
  "ds_read_b64_tr_b16 v[50:51], v108 offset:32\n"
  "ds_read_b128 v[84:87], v111\n"
  "v_mfma_f32_16x16x32_bf16 v[20:23], v[72:75], v[204:207], v[20:23]\n"
  "ds_read_b64_tr_b16 v[52:53], v108 offset:2304\n"
  "ds_read_b64_tr_b16 v[54:55], v108 offset:2336\n"
  "ds_read_b128 v[96:99], v111 offset:64\n"
  "v_mfma_f32_16x16x32_bf16 v[20:23], v[68:71], v[228:231], v[20:23]\n"
  "v_xor_b32 v111, 0x2000, v111\n"
  "v_mfma_f32_16x16x32_bf16 v[44:47], v[76:79], v[252:255], v[20:23]\n"
  "v_mfma_f32_16x16x32_bf16 v[16:19], v[76:79], v[248:251], v[16:19]\n"
  "v_mfma_f32_16x16x32_bf16 v[16:19], v[68:71], v[224:227], v[16:19]\n"
  "ds_read_b64_tr_b16 v[56:57], v108 offset:4608\n"
  "ds_read_b64_tr_b16 v[58:59], v108 offset:4640\n"
  "v_xor_b32 v108, 0x2000, v108\n"
  "v_mfma_f32_16x16x32_bf16 v[16:19], v[72:75], v[200:203], v[16:19]\n"
  "v_mfma_f32_16x16x32_bf16 v[40:43], v[64:67], v[176:179], v[16:19]\n"
  "s_waitcnt lgkmcnt(2)\n"
  "buffer_store_dwordx4 v[48:51], v109, s[48:51], s68 offen offset:0 nt\n"
  "v_add_f32 v84, v84, v96\n"
  "v_add_f32 v85, v85, v97\n"
  "v_add_f32 v86, v86, v98\n"
  "v_mfma_f32_16x16x32_bf16 v[12:15], v[64:67], v[172:175], v[12:15]\n"
  "v_add_f32 v87, v87, v99\n"
  "v_max3_f32 v82, v82, v84, v85\n"
  "v_max3_f32 v82, v82, v86, v87\n"
  "v_mfma_f32_16x16x32_bf16 v[12:15], v[72:75], v[196:199], v[12:15]\n"
  "v_rcp_f32 v84, v84\n"
  "v_mfma_f32_16x16x32_bf16 v[12:15], v[68:71], v[220:223], v[12:15]\n"
  "v_rcp_f32 v85, v85\n"
  "v_mul_f32 v96, s99, v84\n"
  "v_mfma_f32_16x16x32_bf16 v[36:39], v[76:79], v[244:247], v[12:15]\n"
  "buffer_store_dwordx4 v[52:55], v109, s[48:51], s68 offen offset:128 nt\n"
  "v_rcp_f32 v86, v86\n"
  "v_mul_f32 v97, s99, v85\n"
  "v_mfma_f32_16x16x32_bf16 v[8:11], v[76:79], v[240:243], v[8:11]\n"
  "v_rcp_f32 v87, v87\n"
  "v_mul_f32 v98, s99, v86\n"
  "v_mfma_f32_16x16x32_bf16 v[8:11], v[68:71], v[216:219], v[8:11]\n"
  "v_mul_f32 v99, s99, v87\n"
  "v_fma_f32 v40, v40, v84, v96\n"
  "v_fma_f32 v44, v44, v84, v96\n"
  "v_mfma_f32_16x16x32_bf16 v[8:11], v[72:75], v[192:195], v[8:11]\n"
  "v_cvt_pk_bf16_f32 v40, v40, v44\n"
  "v_fma_f32 v41, v41, v85, v97\n"
  "v_fma_f32 v45, v45, v85, v97\n"
  "v_mfma_f32_16x16x32_bf16 v[32:35], v[64:67], v[168:171], v[8:11]\n"
  "s_waitcnt lgkmcnt(0)\n"
  "buffer_store_dwordx4 v[56:59], v109, s[48:51], s68 offen offset:256 nt\n"
  "v_cvt_pk_bf16_f32 v41, v41, v45\n"
  "v_fma_f32 v42, v42, v86, v98\n"
  "v_fma_f32 v46, v46, v86, v98\n"
  "v_mfma_f32_16x16x32_bf16 v[4:7], v[64:67], v[164:167], v[4:7]\n"
  "s_add_u32 s68, s68, s69\n"
  "ds_write2_b32 v107, v40, v41 offset0:0 offset1:72\n"
  "v_mfma_f32_16x16x32_bf16 v[4:7], v[72:75], v[188:191], v[4:7]\n"
  "s_mov_b32 s69, s70\n"
  "v_cvt_pk_bf16_f32 v42, v42, v46\n"
  "v_fma_f32 v43, v43, v87, v99\n"
  "v_fma_f32 v47, v47, v87, v99\n"
  "v_mfma_f32_16x16x32_bf16 v[4:7], v[68:71], v[212:215], v[4:7]\n"
  "s_movk_i32 s70, 6144\n"
  "v_cvt_pk_bf16_f32 v43, v43, v47\n"
  "v_fma_f32 v32, v32, v84, v96\n"
  "v_fma_f32 v33, v33, v85, v97\n"
  "v_mfma_f32_16x16x32_bf16 v[28:31], v[76:79], v[236:239], v[4:7]\n"
  "ds_write2_b32 v107, v42, v43 offset0:144 offset1:216\n"
  "v_mfma_f32_16x16x32_bf16 v[0:3], v[76:79], v[232:235], v[0:3]\n"
  "v_fma_f32 v36, v36, v84, v96\n"
  "v_fma_f32 v37, v37, v85, v97\n"
  "v_fma_f32 v34, v34, v86, v98\n"
  "v_mfma_f32_16x16x32_bf16 v[0:3], v[68:71], v[208:211], v[0:3]\n"
  "v_fma_f32 v35, v35, v87, v99\n"
  "v_fma_f32 v38, v38, v86, v98\n"
  "v_fma_f32 v39, v39, v87, v99\n"
  "v_mfma_f32_16x16x32_bf16 v[0:3], v[72:75], v[184:187], v[0:3]\n"
  "v_fma_f32 v28, v28, v84, v96\n"
  "v_fma_f32 v29, v29, v85, v97\n"
  "v_fma_f32 v30, v30, v86, v98\n"
  "v_mfma_f32_16x16x32_bf16 v[24:27], v[64:67], v[160:163], v[0:3]\n"
  "v_fma_f32 v31, v31, v87, v99\n"
  "v_max3_f32 v83, v83, v84, v85\n"
  "v_max3_f32 v83, v83, v86, v87\n"
  "s_nop 4\n"
  "v_fma_f32 v24, v24, v84, v96\n"
  "v_fma_f32 v25, v25, v85, v97\n"
  "v_cvt_pk_bf16_f32 v24, v24, v28\n"
  "v_cvt_pk_bf16_f32 v28, v25, v29\n"
  "v_cvt_pk_bf16_f32 v25, v32, v36\n"
  "v_cvt_pk_bf16_f32 v29, v33, v37\n"
  "ds_write_b64 v106, v[24:25] offset:0\n"
  "ds_write_b64 v106, v[28:29] offset:288\n"
  "v_fma_f32 v26, v26, v86, v98\n"
  "v_fma_f32 v27, v27, v87, v99\n"
  "v_cvt_pk_bf16_f32 v26, v26, v30\n"
  "v_cvt_pk_bf16_f32 v30, v27, v31\n"
  "v_cvt_pk_bf16_f32 v27, v34, v38\n"
  "v_cvt_pk_bf16_f32 v31, v35, v39\n"
  "ds_write_b64 v106, v[26:27] offset:576\n"
  "ds_write_b64 v106, v[30:31] offset:864\n"
  "v_mul_f32 v83, 0x51800000, v83\n"
  "v_add_f32 v82, v82, v83\n"
  "ds_write_b32 v104, v82\n"
  "s_waitcnt lgkmcnt(0)\n"
  "s_barrier\n"
  "ds_read_b32 v83, v105\n"
  "ds_read_b64_tr_b16 v[48:49], v108 offset:0\n"
  "ds_read_b64_tr_b16 v[50:51], v108 offset:32\n"
  "ds_read_b64_tr_b16 v[52:53], v108 offset:2304\n"
  "ds_read_b64_tr_b16 v[54:55], v108 offset:2336\n"
  "ds_read_b64_tr_b16 v[56:57], v108 offset:4608\n"
  "ds_read_b64_tr_b16 v[58:59], v108 offset:4640\n"
  "s_waitcnt lgkmcnt(4)\n"
  "buffer_store_dwordx4 v[48:51], v109, s[48:51], s68 offen offset:0 nt\n"
  "s_waitcnt lgkmcnt(2)\n"
  "buffer_store_dwordx4 v[52:55], v109, s[48:51], s68 offen offset:128 nt\n"
  "s_waitcnt lgkmcnt(0)\n"
  "buffer_store_dwordx4 v[56:59], v109, s[48:51], s68 offen offset:256 nt\n"
  "v_add_f32 v82, v82, v83\n"
  "v_cmp_ngt_f32 vcc, s71, v82\n"
  "s_nop 4\n"
  "s_cmp_lg_u64 vcc, 0\n"
  "s_cselect_b32 %0, 1, 0\n"
  "L_END_%=:\n"
  : "=&s"(bad)
  : "s"((unsigned long long)__builtin_amdgcn_kernarg_segment_ptr()), "s"(wid), "s"(wg)
  : "v0", "v1", "v2", "v3", "v4", "v5", "v6", "v7", "v8", "v9", "v10", "v11", "v12", "v13", "v14", "v15", "v16", "v17", "v18", "v19", "v20", "v21", "v22", "v23", "v24", "v25", "v26", "v27", "v28", "v29", "v30", "v31", "v32", "v33", "v34", "v35", "v36", "v37", "v38", "v39", "v40", "v41", "v42", "v43", "v44", "v45", "v46", "v47", "v48", "v49", "v50", "v51", "v52", "v53", "v54", "v55", "v56", "v57", "v58", "v59", "v60", "v61", "v62", "v63", "v64", "v65", "v66", "v67", "v68", "v69", "v70", "v71", "v72", "v73", "v74", "v75", "v76", "v77", "v78", "v79", "v80", "v81", "v82", "v83", "v84", "v85", "v86", "v87", "v88", "v89", "v90", "v91", "v92", "v93", "v94", "v95", "v96", "v97", "v98", "v99", "v100", "v101", "v102", "v103", "v104", "v105", "v106", "v107", "v108", "v109", "v110", "v111", "v112", "v113", "v114", "v115", "v116", "v117", "v118", "v119", "v120", "v121", "v122", "v123", "v124", "v125", "v126", "v127", "v128", "v129", "v130", "v131", "v132", "v133", "v134", "v135", "v136", "v137", "v138", "v139", "v140", "v141", "v142", "v143", "v144", "v145", "v146", "v147", "v148", "v149", "v150", "v151", "v152", "v153", "v154", "v155", "v156", "v157", "v158", "v159", "v160", "v161", "v162", "v163", "v164", "v165", "v166", "v167", "v168", "v169", "v170", "v171", "v172", "v173", "v174", "v175", "v176", "v177", "v178", "v179", "v180", "v181", "v182", "v183", "v184", "v185", "v186", "v187", "v188", "v189", "v190", "v191", "v192", "v193", "v194", "v195", "v196", "v197", "v198", "v199", "v200", "v201", "v202", "v203", "v204", "v205", "v206", "v207", "v208", "v209", "v210", "v211", "v212", "v213", "v214", "v215", "v216", "v217", "v218", "v219", "v220", "v221", "v222", "v223", "v224", "v225", "v226", "v227", "v228", "v229", "v230", "v231", "v232", "v233", "v234", "v235", "v236", "v237", "v238", "v239", "v240", "v241", "v242", "v243", "v244", "v245", "v246", "v247", "v248", "v249", "v250", "v251", "v252", "v253", "v254", "v255", "a0", "a1", "a2", "a3", "a4", "a5", "a6", "a7", "a8", "a9", "a10", "a11", "a12", "a13", "a14", "a15", "a16", "a17", "a18", "a19", "a20", "a21", "a22", "a23", "a24", "a25", "a26", "a27", "a28", "a29", "a30", "a31", "a32", "a33", "a34", "a35", "a36", "a37", "a38", "a39", "a40", "a41", "a42", "a43", "a44", "a45", "a46", "a47", "a48", "a49", "a50", "a51", "a52", "a53", "a54", "a55", "a56", "a57", "a58", "a59", "a60", "a61", "a62", "a63", "a64", "a65", "a66", "a67", "a68", "a69", "a70", "a71", "a72", "a73", "a74", "a75", "a76", "a77", "a78", "a79", "a80", "a81", "a82", "a83", "a84", "a85", "a86", "a87", "a88", "a89", "a90", "a91", "a92", "a93", "a94", "a95", "a96", "a97", "a98", "a99", "a100", "a101", "a102", "a103", "a104", "a105", "a106", "a107", "a108", "a109", "a110", "a111", "a112", "a113", "a114", "a115", "a116", "a117", "a118", "a119", "a120", "a121", "a122", "a123", "a124", "a125", "a126", "a127", "a128", "a129", "a130", "a131", "a132", "a133", "a134", "a135", "a136", "a137", "a138", "a139", "a140", "a141", "a142", "a143", "a144", "a145", "a146", "a147", "a148", "a149", "a150", "a151", "a152", "a153", "a154", "a155", "a156", "a157", "a158", "a159", "a160", "a161", "a162", "a163", "a164", "a165", "a166", "a167", "a168", "a169", "a170", "a171", "a172", "a173", "a174", "a175", "a176", "a177", "a178", "a179", "a180", "a181", "a182", "a183", "a184", "a185", "a186", "a187", "a188", "a189", "a190", "a191", "a192", "a193", "a194", "a195", "a196", "a197", "a198", "a199", "a200", "a201", "a202", "a203", "a204", "a205", "a206", "a207", "a208", "a209", "a210", "a211", "a212", "a213", "a214", "a215", "a216", "a217", "a218", "a219", "a220", "a221", "a222", "a223", "a224", "a225", "a226", "a227", "a228", "a229", "a230", "a231", "a232", "a233", "a234", "a235", "a236", "a237", "a238", "a239", "a240", "a241", "a242", "a243", "a244", "a245", "a246", "a247", "a248", "a249", "a250", "a251", "a252", "a253", "a254", "a255", "s40", "s41", "s42", "s43", "s44", "s45", "s46", "s47", "s48", "s49", "s50", "s51", "s52", "s53", "s54", "s55", "s56", "s57", "s58", "s59", "s60", "s61", "s62", "s63", "s64", "s65", "s66", "s67", "s68", "s69", "s70", "s71", "s72", "s73", "s74", "s75", "s76", "s77", "s78", "s79", "s80", "s81", "s82", "s83", "s84", "s85", "s86", "s87", "s88", "s89", "s90", "s91", "s92", "s93", "s94", "s95", "s96", "s97", "s98", "s99", "vcc", "scc", "memory");
  if (bad) {
    unsigned long long ka = (unsigned long long)__builtin_amdgcn_kernarg_segment_ptr();
    asm volatile("s_waitcnt vmcnt(0)\n\ts_barrier" : "+s"(ka) :: "memory");
    const unsigned short* const* arg = (const unsigned short* const*)ka;
    const int p = wg >> 4, side = (wg >> 3) & 1, x = 8 + (p * 20 >> 4), h0 = 8 * p + (wg & 7), n0 = side ? 53 - x : x;
    const int y = 53 - n0 + (wg & 1 ? -(2) : 2);
#pragma unroll 1
    for (int i = 0; i < 3; ++i) {
      const int head = i == 0 ? h0 : i == 1 ? 128 + wg : 384 + (h0 ^ side), n = i == 0 ? n0 : i == 1 ? 53 : y, t0 = side ? 53 - n : 0;
#pragma unroll 1
      for (int t = wid & 1; t < n; t += 2) exact_tile(arg[0], arg[1], arg[2], (unsigned short*)arg[3], head, 16 * (t0 + t));
    }
  }
}

extern "C" void kernel_launch(void* const* d_in, const int* in_sizes, int n_in,
                              void* d_out, int out_size, void* d_ws, size_t ws_size,
                              hipStream_t stream) {
  (void)hipFuncSetAttribute((const void*)attn_fwd_g, hipFuncAttributeMaxDynamicSharedMemorySize, LDS_BYTES);
  hipLaunchKernelGGL(attn_fwd_g, dim3(256), dim3(256), LDS_BYTES, stream, (const unsigned short*)d_in[0], (const unsigned short*)d_in[1],
                     (const unsigned short*)d_in[2], (unsigned short*)d_out);
}
```

```cpp
#include <hip/hip_runtime.h>
#include <hip/hip_bf16.h>

typedef __bf16   bf16x8 __attribute__((ext_vector_type(8)));
typedef __bf16   bf16x2 __attribute__((ext_vector_type(2)));
typedef float    f32x4  __attribute__((ext_vector_type(4)));
typedef float    f32x2  __attribute__((ext_vector_type(2)));
typedef unsigned u32x4  __attribute__((ext_vector_type(4)));
typedef unsigned u32x2  __attribute__((ext_vector_type(2)));
typedef unsigned short u16x8 __attribute__((ext_vector_type(8)));

constexpr int SQ = 848, SK = 512, DH = 192, ROWB = 384;
constexpr int LDS_BYTES = 163840;
constexpr float CSC = 0.07216878364870322f * 1.4426950408889634f;

#define DI __device__ __forceinline__
DI unsigned cvtpk(float lo, float hi) { f32x2 v = {lo, hi}; bf16x2 b = __builtin_convertvector(v, bf16x2); return __builtin_bit_cast(unsigned, b); }
DI float xmax(float x) {
  auto r = __builtin_amdgcn_permlane32_swap(__float_as_uint(x), __float_as_uint(x), false, false);
  x = fmaxf(__uint_as_float(r[0]), __uint_as_float(r[1]));
  auto q = __builtin_amdgcn_permlane16_swap(__float_as_uint(x), __float_as_uint(x), false, false);
  return fmaxf(__uint_as_float(q[0]), __uint_as_float(q[1]));
}
DI float xsum(float x) {
  auto r = __builtin_amdgcn_permlane32_swap(__float_as_uint(x), __float_as_uint(x), false, false);
  x = __uint_as_float(r[0]) + __uint_as_float(r[1]);
  auto q = __builtin_amdgcn_permlane16_swap(__float_as_uint(x), __float_as_uint(x), false, false);
  return __uint_as_float(q[0]) + __uint_as_float(q[1]);
}
DI f32x4 score_tile(const unsigned short* krow, const bf16x8 (&qf)[6]) {
  f32x4 acc = {0.f, 0.f, 0.f, 0.f};
#pragma unroll
  for (int s = 0; s < 6; ++s) acc = __builtin_amdgcn_mfma_f32_16x16x32_bf16(*(const bf16x8*)(krow + 32 * s), qf[s], acc, 0, 0, 0);
  return acc;
}
DI void exact_tile(const unsigned short* Q, const unsigned short* K, const unsigned short* V, unsigned short* O, int head, int row0) {
  const unsigned lane = __builtin_amdgcn_mbcnt_hi(~0u, __builtin_amdgcn_mbcnt_lo(~0u, 0u));
  const int c = lane & 15, g4 = lane >> 4;
  const int qrow = min(row0 + c, SQ - 1);
  bf16x8 qf[6];
#pragma unroll
  for (int s = 0; s < 6; ++s) qf[s] = *(const bf16x8*)(Q + ((size_t)head * SQ + qrow) * DH + 32 * s + 8 * g4);
  const unsigned short* Kh = K + ((size_t)head * SK + c) * DH + 8 * g4;
  float m = -__builtin_huge_valf();
#pragma unroll 1
  for (int kb = 0; kb < SK / 16; ++kb) {
    const f32x4 a = score_tile(Kh + (size_t)kb * 16 * DH, qf);
    m = fmaxf(fmaxf(m, fmaxf(a[0], a[1])), fmaxf(a[2], a[3]));
  }
  const float mc = -xmax(m) * CSC;
  float l = 0.f;
  f32x4 o[12];
#pragma unroll
  for (int dt = 0; dt < 12; ++dt) o[dt] = f32x4{0.f, 0.f, 0.f, 0.f};
#pragma unroll 1
  for (int j = 0; j < SK / 32; ++j) {
    u32x4 p;
#pragma unroll
    for (int kt = 0; kt < 2; ++kt) {
      f32x4 a = score_tile(Kh + (size_t)(32 * j + 16 * kt) * DH, qf);
#pragma unroll
      for (int r = 0; r < 4; ++r) { a[r] = __builtin_amdgcn_exp2f(fmaf(a[r], CSC, mc)); l += a[r]; }
      p[2 * kt] = cvtpk(a[0], a[1]); p[2 * kt + 1] = cvtpk(a[2], a[3]);
    }
    const unsigned short* Vj = V + ((size_t)head * SK + 32 * j + 4 * g4) * DH + c;
#pragma unroll
    for (int dt = 0; dt < 12; ++dt) {
      u16x8 vf;
#pragma unroll
      for (int i = 0; i < 8; ++i) vf[i] = Vj[(16 * (i >> 2) + (i & 3)) * DH + 16 * dt];
      o[dt] = __builtin_amdgcn_mfma_f32_16x16x32_bf16(__builtin_bit_cast(bf16x8, vf), __builtin_bit_cast(bf16x8, p), o[dt], 0, 0, 0);
    }
  }
  const float inv = 1.0f / xsum(l);
  if (row0 + c < SQ) {
#pragma unroll
    for (int dt = 0; dt < 12; ++dt)
      *(u32x2*)(O + ((size_t)head * SQ + qrow) * DH + 16 * dt + 4 * g4) = u32x2{cvtpk(o[dt][0] * inv, o[dt][1] * inv), cvtpk(o[dt][2] * inv, o[dt][3] * inv)};
  }
}

__global__ __launch_bounds__(256, 1)
void attn_fwd_g(const unsigned short* __restrict__ Qg, const unsigned short* __restrict__ Kg,
                const unsigned short* __restrict__ Vg, unsigned short* __restrict__ Og) {
  const int wid = __builtin_amdgcn_readfirstlane(threadIdx.x >> 6);
  const int wg = blockIdx.x;
  unsigned bad;
  asm volatile(
  "s_load_dwordx8 s[76:83], %1, 0\n"
  "L_PC_%=:\n"
  "s_getpc_b64 s[84:85]\n"
  "v_mbcnt_lo_u32_b32 v0, -1, 0\n"
  "v_mbcnt_hi_u32_b32 v0, -1, v0\n"
  "v_lshrrev_b32 v2, 3, v0\n"
  "v_and_b32 v1, 7, v0\n"
  "v_xor_b32 v1, v1, v2\n"
  "v_mul_u32_u24 v112, 0x180, v2\n"
  "v_lshl_add_u32 v112, v1, 4, v112\n"
  "s_lshr_b32 s87, %3, 4\n"
  "s_and_b32 s63, %3, 7\n"
  "s_lshl3_add_u32 s86, s87, s63\n"
  "s_and_b32 s65, %2, 1\n"
  "s_mul_i32 s59, s65, 30720\n"
  "s_add_u32 s59, s59, 102400\n"
  "s_cmp_gt_u32 %2, 1\n"
  "s_cbranch_scc0 A_START_%=\n"
  "s_lshr_b32 s62, %3, 3\n"
  "s_lshl1_add_u32 s62, s62, s65\n"
  "s_lshl_b32 s62, s62, 13\n"
  "s_add_u32 s48, s84, s62\n"
  "s_addc_u32 s49, s85, 0\n"
  "s_sub_i32 s50, L_END_%=-L_PC_%=, s62\n"
  "s_max_i32 s50, s50, 0\n"
  "s_mov_b32 s51, 0x20000\n"
  "s_mov_b32 m0, 56320\n"
  "v_lshlrev_b32 v125, 7, v0\n"
  "buffer_load_dword v125, s[48:51], 0 offen lds\n"
  "s_mul_i32 s75, s87, 20\n"
  "s_lshr_b32 s75, s75, 4\n"
  "s_add_u32 s75, s75, 8\n"
  "s_bitcmp1_b32 %3, 3\n"
  "s_cselect_b32 s62, s75, 0\n"
  "s_mul_i32 s63, s86, 53\n"
  "s_add_u32 s62, s62, s63\n"
  "s_mul_i32 s67, s62, 6144\n"
  "s_add_u32 s64, s67, 6144\n"
  "s_lshl_b32 s66, s65, 10\n"
  "s_add_u32 s66, s66, 65536\n"
  "s_mul_i32 s62, s65, 3072\n"
  "s_sub_u32 s46, 166723584, s62\n"
  "s_mov_b32 s47, 0x20000\n"
  "s_waitcnt lgkmcnt(0)\n"
  "s_add_u32 s44, s76, s62\n"
  "s_addc_u32 s45, s77, 0\n"
  "s_add_u32 m0, s66, 0\n"
  "s_nop 0\n"
  "buffer_load_dwordx4 v112, s[44:47], s67 offen offset:0 nt lds\n"
  "s_add_u32 m0, s66, 1920\n"
  "s_nop 0\n"
  "buffer_load_dwordx4 v112, s[44:47], s67 offen offset:128 nt lds\n"
  "s_add_u32 m0, s66, 3840\n"
  "s_nop 0\n"
  "buffer_load_dwordx4 v112, s[44:47], s67 offen offset:256 nt lds\n"
  "s_add_u32 m0, s66, 6144\n"
  "s_nop 0\n"
  "buffer_load_dwordx4 v112, s[44:47], s64 offen offset:0 nt lds\n"
  "s_add_u32 m0, s66, 8064\n"
  "s_nop 0\n"
  "buffer_load_dwordx4 v112, s[44:47], s64 offen offset:128 nt lds\n"
  "s_add_u32 m0, s66, 9984\n"
  "s_nop 0\n"
  "buffer_load_dwordx4 v112, s[44:47], s64 offen offset:256 nt lds\n"
  "s_branch B_START_%=\n"
  "A_START_%=:\n"
  "s_lshl1_add_u32 s62, s86, s65\n"
  "s_mul_i32 s57, s62, 98304\n"
  "v_add_u32 v113, 0x800, v112\n"
  "s_mov_b32 s42, 100663296\n"
  "s_mov_b32 s43, 0x20000\n"
  "s_waitcnt lgkmcnt(0)\n"
  "s_mov_b64 s[40:41], s[78:79]\n"
  "s_add_u32 m0, s59, 0\n"
  "s_nop 0\n"
  "buffer_load_dwordx4 v112, s[40:43], s57 offen offset:0  lds\n"
  "buffer_load_dwordx4 v113, s[40:43], s57 offen offset:1024  lds\n"
  "s_add_u32 m0, s59, 1920\n"
  "s_nop 0\n"
  "buffer_load_dwordx4 v112, s[40:43], s57 offen offset:128  lds\n"
  "buffer_load_dwordx4 v113, s[40:43], s57 offen offset:1152  lds\n"
  "s_add_u32 m0, s59, 3840\n"
  "s_nop 0\n"
  "buffer_load_dwordx4 v112, s[40:43], s57 offen offset:256  lds\n"
  "buffer_load_dwordx4 v113, s[40:43], s57 offen offset:1280  lds\n"
  "s_add_u32 s57, s57, 6144\n"
  "s_add_u32 m0, s59, 5888\n"
  "s_nop 0\n"
  "buffer_load_dwordx4 v112, s[40:43], s57 offen offset:256  lds\n"
  "buffer_load_dwordx4 v113, s[40:43], s57 offen offset:1280  lds\n"
  "s_add_u32 m0, s59, 8064\n"
  "s_nop 0\n"
  "buffer_load_dwordx4 v112, s[40:43], s57 offen offset:128  lds\n"
  "buffer_load_dwordx4 v113, s[40:43], s57 offen offset:1152  lds\n"
  "s_add_u32 m0, s59, 10240\n"
  "s_nop 0\n"
  "buffer_load_dwordx4 v112, s[40:43], s57 offen offset:0  lds\n"
  "buffer_load_dwordx4 v113, s[40:43], s57 offen offset:1024  lds\n"
  "s_add_u32 s57, s57, 6144\n"
  "s_add_u32 m0, s59, 12288\n"
  "s_nop 0\n"
  "buffer_load_dwordx4 v112, s[40:43], s57 offen offset:0  lds\n"
  "buffer_load_dwordx4 v113, s[40:43], s57 offen offset:1024  lds\n"
  "s_add_u32 m0, s59, 14208\n"
  "s_nop 0\n"
  "buffer_load_dwordx4 v112, s[40:43], s57 offen offset:128  lds\n"
  "buffer_load_dwordx4 v113, s[40:43], s57 offen offset:1152  lds\n"
  "s_add_u32 m0, s59, 16128\n"
  "s_nop 0\n"
  "buffer_load_dwordx4 v112, s[40:43], s57 offen offset:256  lds\n"
  "buffer_load_dwordx4 v113, s[40:43], s57 offen offset:1280  lds\n"
  "s_add_u32 s57, s57, 6144\n"
  "s_add_u32 m0, s59, 18176\n"
  "s_nop 0\n"
  "buffer_load_dwordx4 v112, s[40:43], s57 offen offset:256  lds\n"
  "buffer_load_dwordx4 v113, s[40:43], s57 offen offset:1280  lds\n"
  "s_mul_i32 s75, s87, 20\n"
  "s_lshr_b32 s75, s75, 4\n"
  "s_add_u32 s75, s75, 8\n"
  "s_sub_u32 s62, 53, s75\n"
  "s_bitcmp1_b32 %3, 3\n"
  "s_cselect_b32 s52, s62, s75\n"
  "s_cselect_b32 s54, s75, s62\n"
  "s_cselect_b32 s62, s75, 0\n"
  "s_lshl_b32 s62, s62, 8\n"
  "s_or_b32 s52, s52, s62\n"
  "s_pack_ll_b32_b16 s52, s52, s86\n"
  "s_add_u32 s62, %3, 128\n"
  "s_pack_ll_b32_b16 s53, 53, s62\n"
  "s_bitcmp1_b32 %3, 0\n"
  "s_cselect_b32 s62, -2, 2\n"
  "s_add_u32 s54, s54, s62\n"
  "s_sub_u32 s62, 53, s54\n"
  "s_lshl_b32 s62, s62, 8\n"
  "s_bfe_u32 s63, %3, 0x10003\n"
  "s_cselect_b32 s62, s62, 0\n"
  "s_or_b32 s54, s54, s62\n"
  "s_xor_b32 s63, s63, s86\n"
  "s_add_u32 s63, s63, 384\n"
  "s_pack_ll_b32_b16 s54, s54, s63\n"
  "s_mov_b32 s55, 0\n"
  "s_and_b32 s56, s52, 0xff\n"
  "s_cmp_lg_u32 s53, 0\n"
  "s_cselect_b32 s62, 1, 0\n"
  "s_sub_u32 s56, s56, s62\n"
  "s_mov_b32 s88, 0x4000\n"
  "s_mov_b32 s89, 0xc000\n"
  "v_and_b32 v2, 7, v0\n"
  "v_lshrrev_b32 v83, 4, v0\n"
  "v_xor_b32 v83, v83, v2\n"
  "v_lshlrev_b32 v83, 4, v83\n"
  "v_lshl_add_u32 v83, v2, 7, v83\n"
  "v_bfe_u32 v1, v0, 3, 1\n"
  "v_lshl_add_u32 v83, v1, 10, v83\n"
  "v_xor_b32 v84, 64, v83\n"
  "v_add_u32 v87, s59, v83\n"
  "v_add_u32 v88, s59, v84\n"
  "s_mov_b32 s66, 65536\n"
  "v_add_u32 v81, s66, v83\n"
  "v_add_u32 v82, s66, v84\n"
  "s_lshl_b32 s62, s65, 13\n"
  "v_lshlrev_b32 v80, 4, v0\n"
  "v_add_u32 v80, s62, v80\n"
  "s_mov_b32 s60, 0x3dd53b94\n"
  "s_mov_b32 s61, 0x409fecaf\n"
  "v_mov_b32 v92, 0xc2400000\n"
  "v_mov_b32 v93, 0xc2400000\n"
  "v_mov_b32 v94, 0xc2400000\n"
  "v_mov_b32 v95, 0xc2400000\n"
  "s_waitcnt vmcnt(18)\n"
  "ds_read_b128 a[0:3], v87 offset:0\n"
  "ds_read_b128 a[4:7], v88 offset:0\n"
  "s_waitcnt vmcnt(16)\n"
  "ds_read_b128 a[8:11], v87 offset:2048\n"
  "ds_read_b128 a[12:15], v88 offset:2048\n"
  "s_add_u32 m0, s59, -128\n"
  "s_waitcnt lgkmcnt(2)\n"
  "buffer_load_dwordx4 v112, s[40:43], s57 offen offset:128  lds\n"
  "buffer_load_dwordx4 v113, s[40:43], s57 offen offset:1152  lds\n"
  "s_waitcnt vmcnt(16)\n"
  "ds_read_b128 a[16:19], v87 offset:4096\n"
  "ds_read_b128 a[20:23], v88 offset:4096\n"
  "s_add_u32 m0, s59, 2048\n"
  "s_waitcnt lgkmcnt(2)\n"
  "buffer_load_dwordx4 v112, s[40:43], s57 offen offset:0  lds\n"
  "buffer_load_dwordx4 v113, s[40:43], s57 offen offset:1024  lds\n"
  "s_add_u32 s57, s57, 6144\n"
  "s_waitcnt vmcnt(16)\n"
  "ds_read_b128 a[40:43], v87 offset:6144\n"
  "ds_read_b128 a[44:47], v88 offset:6144\n"
  "s_add_u32 m0, s59, 4096\n"
  "s_waitcnt lgkmcnt(2)\n"
  "buffer_load_dwordx4 v112, s[40:43], s57 offen offset:0  lds\n"
  "buffer_load_dwordx4 v113, s[40:43], s57 offen offset:1024  lds\n"
  "s_waitcnt vmcnt(16)\n"
  "ds_read_b128 a[32:35], v87 offset:8192\n"
  "ds_read_b128 a[36:39], v88 offset:8192\n"
  "s_add_u32 m0, s59, 6016\n"
  "s_waitcnt lgkmcnt(2)\n"
  "buffer_load_dwordx4 v112, s[40:43], s57 offen offset:128  lds\n"
  "buffer_load_dwordx4 v113, s[40:43], s57 offen offset:1152  lds\n"
  "s_waitcnt vmcnt(16)\n"
  "ds_read_b128 a[24:27], v87 offset:10240\n"
  "ds_read_b128 a[28:31], v88 offset:10240\n"
  "s_add_u32 m0, s59, 7936\n"
  "s_waitcnt lgkmcnt(2)\n"
  "buffer_load_dwordx4 v112, s[40:43], s57 offen offset:256  lds\n"
  "buffer_load_dwordx4 v113, s[40:43], s57 offen offset:1280  lds\n"
  "s_add_u32 s57, s57, 6144\n"
  "s_waitcnt vmcnt(16)\n"
  "ds_read_b128 a[48:51], v87 offset:12288\n"
  "ds_read_b128 a[52:55], v88 offset:12288\n"
  "s_add_u32 m0, s59, 9984\n"
  "s_waitcnt lgkmcnt(2)\n"
  "buffer_load_dwordx4 v112, s[40:43], s57 offen offset:256  lds\n"
  "buffer_load_dwordx4 v113, s[40:43], s57 offen offset:1280  lds\n"
  "s_waitcnt vmcnt(16)\n"
  "ds_read_b128 a[56:59], v87 offset:14336\n"
  "ds_read_b128 a[60:63], v88 offset:14336\n"
  "s_add_u32 m0, s59, 12160\n"
  "s_waitcnt lgkmcnt(2)\n"
  "buffer_load_dwordx4 v112, s[40:43], s57 offen offset:128  lds\n"
  "buffer_load_dwordx4 v113, s[40:43], s57 offen offset:1152  lds\n"
  "s_waitcnt vmcnt(16)\n"
  "ds_read_b128 a[64:67], v87 offset:16384\n"
  "ds_read_b128 a[68:71], v88 offset:16384\n"
  "s_add_u32 m0, s59, 14336\n"
  "s_waitcnt lgkmcnt(2)\n"
  "buffer_load_dwordx4 v112, s[40:43], s57 offen offset:0  lds\n"
  "buffer_load_dwordx4 v113, s[40:43], s57 offen offset:1024  lds\n"
  "s_add_u32 s57, s57, 6144\n"
  "s_waitcnt vmcnt(16)\n"
  "ds_read_b128 a[88:91], v87 offset:18432\n"
  "ds_read_b128 a[92:95], v88 offset:18432\n"
  "s_add_u32 m0, s59, 16384\n"
  "s_waitcnt lgkmcnt(2)\n"
  "buffer_load_dwordx4 v112, s[40:43], s57 offen offset:0  lds\n"
  "buffer_load_dwordx4 v113, s[40:43], s57 offen offset:1024  lds\n"
  "s_waitcnt vmcnt(16)\n"
  "ds_read_b128 a[80:83], v87 offset:0\n"
  "ds_read_b128 a[84:87], v88 offset:0\n"
  "s_add_u32 m0, s59, 18304\n"
  "s_barrier\n"
  "ds_read_b128 v[32:35], v81 offset:0\n"
  "ds_read_b128 v[36:39], v82 offset:0\n"
  "ds_read_b128 v[40:43], v81 offset:2048\n"
  "ds_read_b128 v[44:47], v82 offset:2048\n"
  "ds_read_b128 v[48:51], v81 offset:4096\n"
  "ds_read_b128 v[52:55], v82 offset:4096\n"
  "s_waitcnt lgkmcnt(0)\n"
  ".p2align 3\n"
  "v_mfma_f32_16x16x32_bf16 v[0:3], a[0:3], v[32:35], v[92:95]\n"
  "s_add_u32 s66, s66, 6144\n"
  "s_cmp_eq_u32 s66, 102400\n"
  "s_cselect_b32 s66, 65536, s66\n"
  "buffer_load_dwordx4 v112, s[40:43], s57 offen offset:128  lds\n"
  "buffer_load_dwordx4 v113, s[40:43], s57 offen offset:1152  lds\n"
  "v_mfma_f32_16x16x32_bf16 v[0:3], a[4:7], v[36:39], v[0:3]\n"
  "s_waitcnt vmcnt(16)\n"
  "ds_read_b128 a[72:75], v87 offset:2048\n"
  "ds_read_b128 a[76:79], v88 offset:2048\n"
  "s_add_u32 m0, s59, -256\n"
  "v_mfma_f32_16x16x32_bf16 v[0:3], a[8:11], v[40:43], v[0:3]\n"
  "buffer_load_dwordx4 v112, s[40:43], s57 offen offset:256  lds\n"
  "buffer_load_dwordx4 v113, s[40:43], s57 offen offset:1280  lds\n"
  "s_add_u32 s57, s57, 6144\n"
  "v_mfma_f32_16x16x32_bf16 v[0:3], a[12:15], v[44:47], v[0:3]\n"
  "s_waitcnt vmcnt(16)\n"
  "ds_read_b128 a[96:99], v87 offset:4096\n"
  "ds_read_b128 a[100:103], v88 offset:4096\n"
  "s_add_u32 m0, s59, 1792\n"
  "v_mfma_f32_16x16x32_bf16 v[0:3], a[16:19], v[48:51], v[0:3]\n"
  "s_waitcnt lgkmcnt(2)\n"
  "buffer_load_dwordx4 v112, s[40:43], s57 offen offset:256  lds\n"
  "buffer_load_dwordx4 v113, s[40:43], s57 offen offset:1280  lds\n"
  "v_mfma_f32_16x16x32_bf16 v[0:3], a[20:23], v[52:55], v[0:3]\n"
  "s_waitcnt vmcnt(16)\n"
  "ds_read_b128 a[104:107], v87 offset:6144\n"
  "ds_read_b128 a[108:111], v88 offset:6144\n"
  "s_add_u32 m0, s59, 3968\n"
  "v_mfma_f32_16x16x32_bf16 v[4:7], a[44:47], v[52:55], v[92:95]\n"
  "s_waitcnt lgkmcnt(2)\n"
  "buffer_load_dwordx4 v112, s[40:43], s57 offen offset:128  lds\n"
  "buffer_load_dwordx4 v113, s[40:43], s57 offen offset:1152  lds\n"
  "v_mfma_f32_16x16x32_bf16 v[4:7], a[40:43], v[48:51], v[4:7]\n"
  "s_waitcnt vmcnt(16)\n"
  "ds_read_b128 a[112:115], v87 offset:8192\n"
  "ds_read_b128 a[116:119], v88 offset:8192\n"
  "s_add_u32 m0, s59, 6144\n"
  "v_mfma_f32_16x16x32_bf16 v[4:7], a[36:39], v[44:47], v[4:7]\n"
  "s_waitcnt lgkmcnt(2)\n"
  "buffer_load_dwordx4 v112, s[40:43], s57 offen offset:0  lds\n"
  "buffer_load_dwordx4 v113, s[40:43], s57 offen offset:1024  lds\n"
  "s_add_u32 s57, s57, 6144\n"
  "v_mfma_f32_16x16x32_bf16 v[4:7], a[32:35], v[40:43], v[4:7]\n"
  "s_waitcnt vmcnt(16)\n"
  "ds_read_b128 a[136:139], v87 offset:10240\n"
  "ds_read_b128 a[140:143], v88 offset:10240\n"
  "s_add_u32 m0, s59, 8192\n"
  "v_mfma_f32_16x16x32_bf16 v[4:7], a[28:31], v[36:39], v[4:7]\n"
  "s_waitcnt lgkmcnt(2)\n"
  "buffer_load_dwordx4 v112, s[40:43], s57 offen offset:0  lds\n"
  "buffer_load_dwordx4 v113, s[40:43], s57 offen offset:1024  lds\n"
  "v_mfma_f32_16x16x32_bf16 v[4:7], a[24:27], v[32:35], v[4:7]\n"
  "s_waitcnt vmcnt(16)\n"
  "ds_read_b128 a[128:131], v87 offset:12288\n"
  "ds_read_b128 a[132:135], v88 offset:12288\n"
  "s_add_u32 m0, s59, 10112\n"
  "v_add_u32 v81, s66, v83\n"
  "v_add_u32 v82, s66, v84\n"
  "v_mfma_f32_16x16x32_bf16 v[8:11], a[48:51], v[32:35], v[92:95]\n"
  "s_waitcnt lgkmcnt(2)\n"
  "buffer_load_dwordx4 v112, s[40:43], s57 offen offset:128  lds\n"
  "buffer_load_dwordx4 v113, s[40:43], s57 offen offset:1152  lds\n"
  "v_mfma_f32_16x16x32_bf16 v[8:11], a[52:55], v[36:39], v[8:11]\n"
  "s_waitcnt vmcnt(16)\n"
  "ds_read_b128 a[120:123], v87 offset:14336\n"
  "ds_read_b128 a[124:127], v88 offset:14336\n"
  "s_add_u32 m0, s59, 12032\n"
  "v_mfma_f32_16x16x32_bf16 v[8:11], a[56:59], v[40:43], v[8:11]\n"
  "s_waitcnt lgkmcnt(2)\n"
  "buffer_load_dwordx4 v112, s[40:43], s57 offen offset:256  lds\n"
  "buffer_load_dwordx4 v113, s[40:43], s57 offen offset:1280  lds\n"
  "s_add_u32 s57, s57, 6144\n"
  "v_mfma_f32_16x16x32_bf16 v[8:11], a[60:63], v[44:47], v[8:11]\n"
  "s_waitcnt vmcnt(16)\n"
  "ds_read_b128 a[144:147], v87 offset:16384\n"
  "ds_read_b128 a[148:151], v88 offset:16384\n"
  "s_add_u32 m0, s59, 14080\n"
  "v_mfma_f32_16x16x32_bf16 v[8:11], a[64:67], v[48:51], v[8:11]\n"
  "s_waitcnt lgkmcnt(2)\n"
  "buffer_load_dwordx4 v112, s[40:43], s57 offen offset:256  lds\n"
  "buffer_load_dwordx4 v113, s[40:43], s57 offen offset:1280  lds\n"
  "v_mfma_f32_16x16x32_bf16 v[8:11], a[68:71], v[52:55], v[8:11]\n"
  "s_waitcnt vmcnt(16)\n"
  "ds_read_b128 a[152:155], v87 offset:18432\n"
  "ds_read_b128 a[156:159], v88 offset:18432\n"
  "s_add_u32 m0, s59, 16256\n"
  "v_pk_mul_f32 v[0:1], v[0:1], s[60:61] op_sel_hi:[1,0]\n"
  "v_pk_mul_f32 v[2:3], v[2:3], s[60:61] op_sel_hi:[1,0]\n"
  "v_pk_mul_f32 v[4:5], v[4:5], s[60:61] op_sel_hi:[1,0]\n"
  "v_pk_mul_f32 v[6:7], v[6:7], s[60:61] op_sel_hi:[1,0]\n"
  "v_mfma_f32_16x16x32_bf16 v[12:15], a[92:95], v[52:55], v[92:95]\n"
  "s_waitcnt lgkmcnt(2)\n"
  "buffer_load_dwordx4 v112, s[40:43], s57 offen offset:128  lds\n"
  "buffer_load_dwordx4 v113, s[40:43], s57 offen offset:1152  lds\n"
  "v_exp_f32 v0, v0\n"
  "v_mfma_f32_16x16x32_bf16 v[12:15], a[88:91], v[48:51], v[12:15]\n"
  "s_waitcnt vmcnt(16)\n"
  "ds_read_b128 a[160:163], v87 offset:0\n"
  "ds_read_b128 a[164:167], v88 offset:0\n"
  "s_add_u32 m0, s59, 18432\n"
  "v_exp_f32 v1, v1\n"
  "v_mfma_f32_16x16x32_bf16 v[12:15], a[84:87], v[44:47], v[12:15]\n"
  "s_waitcnt lgkmcnt(2)\n"
  "buffer_load_dwordx4 v112, s[40:43], s57 offen offset:0  lds\n"
  "buffer_load_dwordx4 v113, s[40:43], s57 offen offset:1024  lds\n"
  "s_add_u32 s57, s57, 6144\n"
  "v_exp_f32 v2, v2\n"
  "v_mfma_f32_16x16x32_bf16 v[12:15], a[80:83], v[40:43], v[12:15]\n"
  "s_waitcnt vmcnt(16)\n"
  "ds_read_b128 a[184:187], v87 offset:2048\n"
  "ds_read_b128 a[188:191], v88 offset:2048\n"
  "s_add_u32 m0, s59, 0\n"
  "v_exp_f32 v3, v3\n"
  "v_mfma_f32_16x16x32_bf16 v[12:15], a[76:79], v[36:39], v[12:15]\n"
  "s_waitcnt lgkmcnt(2)\n"
  "buffer_load_dwordx4 v112, s[40:43], s57 offen offset:0  lds\n"
  "buffer_load_dwordx4 v113, s[40:43], s57 offen offset:1024  lds\n"
  "v_cvt_pk_bf16_f32 v0, v0, v1\n"
  "v_cvt_pk_bf16_f32 v1, v2, v3\n"
  "v_mfma_f32_16x16x32_bf16 v[12:15], a[72:75], v[32:35], v[12:15]\n"
  "s_waitcnt vmcnt(16)\n"
  "ds_read_b128 a[176:179], v87 offset:4096\n"
  "ds_read_b128 a[180:183], v88 offset:4096\n"
  "s_add_u32 m0, s59, 1920\n"
  "v_mfma_f32_16x16x32_bf16 v[16:19], a[96:99], v[32:35], v[92:95]\n"
  "s_waitcnt lgkmcnt(2)\n"
  "buffer_load_dwordx4 v112, s[40:43], s57 offen offset:128  lds\n"
  "buffer_load_dwordx4 v113, s[40:43], s57 offen offset:1152  lds\n"
  "v_exp_f32 v4, v4\n"
  "v_mfma_f32_16x16x32_bf16 v[16:19], a[100:103], v[36:39], v[16:19]\n"
  "s_waitcnt vmcnt(16)\n"
  "ds_read_b128 a[168:171], v87 offset:6144\n"
  "ds_read_b128 a[172:175], v88 offset:6144\n"
  "s_add_u32 m0, s59, 3840\n"
  "v_exp_f32 v5, v5\n"
  "v_mfma_f32_16x16x32_bf16 v[16:19], a[104:107], v[40:43], v[16:19]\n"
  "s_waitcnt lgkmcnt(2)\n"
  "buffer_load_dwordx4 v112, s[40:43], s57 offen offset:256  lds\n"
  "buffer_load_dwordx4 v113, s[40:43], s57 offen offset:1280  lds\n"
  "s_add_u32 s57, s57, 6144\n"
  "v_exp_f32 v6, v6\n"
  "v_mfma_f32_16x16x32_bf16 v[16:19], a[108:111], v[44:47], v[16:19]\n"
  "s_waitcnt vmcnt(16)\n"
  "ds_read_b128 a[192:195], v87 offset:8192\n"
  "ds_read_b128 a[196:199], v88 offset:8192\n"
  "s_add_u32 m0, s59, 5888\n"
  "v_exp_f32 v7, v7\n"
  "v_mfma_f32_16x16x32_bf16 v[16:19], a[112:115], v[48:51], v[16:19]\n"
  "s_waitcnt lgkmcnt(2)\n"
  "buffer_load_dwordx4 v112, s[40:43], s57 offen offset:256  lds\n"
  "buffer_load_dwordx4 v113, s[40:43], s57 offen offset:1280  lds\n"
  "v_cvt_pk_bf16_f32 v2, v4, v5\n"
  "v_cvt_pk_bf16_f32 v3, v6, v7\n"
  "v_mfma_f32_16x16x32_bf16 v[16:19], a[116:119], v[52:55], v[16:19]\n"
  "s_waitcnt vmcnt(16)\n"
  "ds_read_b128 a[200:203], v87 offset:10240\n"
  "ds_read_b128 a[204:207], v88 offset:10240\n"
  "s_add_u32 m0, s59, 8064\n"
  "ds_write_b128 v80, v[0:3] offset:0\n"
  "v_pk_mul_f32 v[8:9], v[8:9], s[60:61] op_sel_hi:[1,0]\n"
  "v_pk_mul_f32 v[10:11], v[10:11], s[60:61] op_sel_hi:[1,0]\n"
  "v_pk_mul_f32 v[12:13], v[12:13], s[60:61] op_sel_hi:[1,0]\n"
  "v_pk_mul_f32 v[14:15], v[14:15], s[60:61] op_sel_hi:[1,0]\n"
  "v_mfma_f32_16x16x32_bf16 v[20:23], a[140:143], v[52:55], v[92:95]\n"
  "s_waitcnt lgkmcnt(3)\n"
  "buffer_load_dwordx4 v112, s[40:43], s57 offen offset:128  lds\n"
  "buffer_load_dwordx4 v113, s[40:43], s57 offen offset:1152  lds\n"
  "v_exp_f32 v8, v8\n"
  "v_mfma_f32_16x16x32_bf16 v[20:23], a[136:139], v[48:51], v[20:23]\n"
  "s_waitcnt vmcnt(16)\n"
  "ds_read_b128 a[208:211], v87 offset:12288\n"
  "ds_read_b128 a[212:215], v88 offset:12288\n"
  "s_add_u32 m0, s59, 10240\n"
  "v_exp_f32 v9, v9\n"
  "v_mfma_f32_16x16x32_bf16 v[20:23], a[132:135], v[44:47], v[20:23]\n"
  "s_waitcnt lgkmcnt(3)\n"
  "buffer_load_dwordx4 v112, s[40:43], s57 offen offset:0  lds\n"
  "buffer_load_dwordx4 v113, s[40:43], s57 offen offset:1024  lds\n"
  "s_add_u32 s57, s57, 6144\n"
  "v_exp_f32 v10, v10\n"
  "v_mfma_f32_16x16x32_bf16 v[20:23], a[128:131], v[40:43], v[20:23]\n"
  "s_waitcnt vmcnt(16)\n"
  "ds_read_b128 a[232:235], v87 offset:14336\n"
  "ds_read_b128 a[236:239], v88 offset:14336\n"
  "s_add_u32 m0, s59, 12288\n"
  "v_exp_f32 v11, v11\n"
  "v_mfma_f32_16x16x32_bf16 v[20:23], a[124:127], v[36:39], v[20:23]\n"
  "s_waitcnt lgkmcnt(2)\n"
  "buffer_load_dwordx4 v112, s[40:43], s57 offen offset:0  lds\n"
  "buffer_load_dwordx4 v113, s[40:43], s57 offen offset:1024  lds\n"
  "v_cvt_pk_bf16_f32 v8, v8, v9\n"
  "v_cvt_pk_bf16_f32 v9, v10, v11\n"
  "v_mfma_f32_16x16x32_bf16 v[20:23], a[120:123], v[32:35], v[20:23]\n"
  "s_waitcnt vmcnt(16)\n"
  "ds_read_b128 a[224:227], v87 offset:16384\n"
  "ds_read_b128 a[228:231], v88 offset:16384\n"
  "s_add_u32 m0, s59, 14208\n"
  "ds_read_b128 v[56:59], v81 offset:0\n"
  "ds_read_b128 v[60:63], v82 offset:0\n"
  "v_mfma_f32_16x16x32_bf16 v[24:27], a[144:147], v[32:35], v[92:95]\n"
  "s_waitcnt lgkmcnt(4)\n"
  "buffer_load_dwordx4 v112, s[40:43], s57 offen offset:128  lds\n"
  "buffer_load_dwordx4 v113, s[40:43], s57 offen offset:1152  lds\n"
  "v_exp_f32 v12, v12\n"
  "v_mfma_f32_16x16x32_bf16 v[24:27], a[148:151], v[36:39], v[24:27]\n"
  "s_waitcnt vmcnt(16)\n"
  "ds_read_b128 a[216:219], v87 offset:18432\n"
  "ds_read_b128 a[220:223], v88 offset:18432\n"
  "s_add_u32 m0, s59, 16128\n"
  "v_exp_f32 v13, v13\n"
  "v_mfma_f32_16x16x32_bf16 v[24:27], a[152:155], v[40:43], v[24:27]\n"
  "s_waitcnt lgkmcnt(4)\n"
  "buffer_load_dwordx4 v112, s[40:43], s57 offen offset:256  lds\n"
  "buffer_load_dwordx4 v113, s[40:43], s57 offen offset:1280  lds\n"
  "s_add_u32 s57, s57, 6144\n"
  "v_exp_f32 v14, v14\n"
  "v_mfma_f32_16x16x32_bf16 v[24:27], a[156:159], v[44:47], v[24:27]\n"
  "s_waitcnt vmcnt(16)\n"
  "ds_read_b128 a[240:243], v87 offset:0\n"
  "ds_read_b128 a[244:247], v88 offset:0\n"
  "s_add_u32 m0, s59, 18176\n"
  "v_exp_f32 v15, v15\n"
  "v_mfma_f32_16x16x32_bf16 v[24:27], a[160:163], v[48:51], v[24:27]\n"
  "s_waitcnt lgkmcnt(2)\n"
  "buffer_load_dwordx4 v112, s[40:43], s57 offen offset:256  lds\n"
  "buffer_load_dwordx4 v113, s[40:43], s57 offen offset:1280  lds\n"
  "v_cvt_pk_bf16_f32 v10, v12, v13\n"
  "v_cvt_pk_bf16_f32 v11, v14, v15\n"
  "v_mfma_f32_16x16x32_bf16 v[24:27], a[164:167], v[52:55], v[24:27]\n"
  "s_waitcnt vmcnt(16)\n"
  "ds_read_b128 a[248:251], v87 offset:2048\n"
  "ds_read_b128 a[252:255], v88 offset:2048\n"
  "s_add_u32 m0, s59, -128\n"
  "ds_write_b128 v80, v[8:11] offset:1024\n"
  "v_pk_mul_f32 v[16:17], v[16:17], s[60:61] op_sel_hi:[1,0]\n"
  "v_pk_mul_f32 v[18:19], v[18:19], s[60:61] op_sel_hi:[1,0]\n"
  "v_pk_mul_f32 v[20:21], v[20:21], s[60:61] op_sel_hi:[1,0]\n"
  "v_pk_mul_f32 v[22:23], v[22:23], s[60:61] op_sel_hi:[1,0]\n"
  "v_mfma_f32_16x16x32_bf16 v[28:31], a[188:191], v[52:55], v[92:95]\n"
  "s_waitcnt lgkmcnt(3)\n"
  "buffer_load_dwordx4 v112, s[40:43], s57 offen offset:128  lds\n"
  "buffer_load_dwordx4 v113, s[40:43], s57 offen offset:1152  lds\n"
  "v_exp_f32 v16, v16\n"
  "v_mfma_f32_16x16x32_bf16 v[28:31], a[184:187], v[48:51], v[28:31]\n"
  "s_waitcnt vmcnt(16)\n"
  "ds_read_b128 v[128:131], v87 offset:4096\n"
  "ds_read_b128 v[132:135], v88 offset:4096\n"
  "s_add_u32 m0, s59, 2048\n"
  "v_exp_f32 v17, v17\n"
  "v_mfma_f32_16x16x32_bf16 v[28:31], a[180:183], v[44:47], v[28:31]\n"
  "s_waitcnt lgkmcnt(3)\n"
  "buffer_load_dwordx4 v112, s[40:43], s57 offen offset:0  lds\n"
  "buffer_load_dwordx4 v113, s[40:43], s57 offen offset:1024  lds\n"
  "s_add_u32 s57, s57, 6144\n"
  "v_exp_f32 v18, v18\n"
  "v_mfma_f32_16x16x32_bf16 v[28:31], a[176:179], v[40:43], v[28:31]\n"
  "s_waitcnt vmcnt(16)\n"
  "ds_read_b128 v[152:155], v87 offset:6144\n"
  "ds_read_b128 v[156:159], v88 offset:6144\n"
  "s_add_u32 m0, s59, 4096\n"
  "v_exp_f32 v19, v19\n"
  "v_mfma_f32_16x16x32_bf16 v[28:31], a[172:175], v[36:39], v[28:31]\n"
  "s_waitcnt lgkmcnt(2)\n"
  "buffer_load_dwordx4 v112, s[40:43], s57 offen offset:0  lds\n"
  "buffer_load_dwordx4 v113, s[40:43], s57 offen offset:1024  lds\n"
  "v_cvt_pk_bf16_f32 v16, v16, v17\n"
  "v_cvt_pk_bf16_f32 v17, v18, v19\n"
  "v_mfma_f32_16x16x32_bf16 v[28:31], a[168:171], v[32:35], v[28:31]\n"
  "s_waitcnt vmcnt(16)\n"
  "ds_read_b128 v[144:147], v87 offset:8192\n"
  "ds_read_b128 v[148:151], v88 offset:8192\n"
  "s_add_u32 m0, s59, 6016\n"
  "ds_read_b128 v[64:67], v81 offset:2048\n"
  "ds_read_b128 v[68:71], v82 offset:2048\n"
  "v_mfma_f32_16x16x32_bf16 v[0:3], a[192:195], v[32:35], v[92:95]\n"
  "s_waitcnt lgkmcnt(4)\n"
  "buffer_load_dwordx4 v112, s[40:43], s57 offen offset:128  lds\n"
  "buffer_load_dwordx4 v113, s[40:43], s57 offen offset:1152  lds\n"
  "v_exp_f32 v20, v20\n"
  "v_mfma_f32_16x16x32_bf16 v[0:3], a[196:199], v[36:39], v[0:3]\n"
  "s_waitcnt vmcnt(16)\n"
  "ds_read_b128 v[136:139], v87 offset:10240\n"
  "ds_read_b128 v[140:143], v88 offset:10240\n"
  "s_add_u32 m0, s59, 7936\n"
  "v_exp_f32 v21, v21\n"
  "v_mfma_f32_16x16x32_bf16 v[0:3], a[200:203], v[40:43], v[0:3]\n"
  "s_waitcnt lgkmcnt(4)\n"
  "buffer_load_dwordx4 v112, s[40:43], s57 offen offset:256  lds\n"
  "buffer_load_dwordx4 v113, s[40:43], s57 offen offset:1280  lds\n"
  "s_add_u32 s57, s57, 6144\n"
  "v_exp_f32 v22, v22\n"
  "v_mfma_f32_16x16x32_bf16 v[0:3], a[204:207], v[44:47], v[0:3]\n"
  "s_waitcnt vmcnt(16)\n"
  "ds_read_b128 v[160:163], v87 offset:12288\n"
  "ds_read_b128 v[164:167], v88 offset:12288\n"
  "s_add_u32 m0, s59, 9984\n"
  "v_exp_f32 v23, v23\n"
  "v_mfma_f32_16x16x32_bf16 v[0:3], a[208:211], v[48:51], v[0:3]\n"
  "s_waitcnt lgkmcnt(2)\n"
  "buffer_load_dwordx4 v112, s[40:43], s57 offen offset:256  lds\n"
  "buffer_load_dwordx4 v113, s[40:43], s57 offen offset:1280  lds\n"
  "v_cvt_pk_bf16_f32 v18, v20, v21\n"
  "v_cvt_pk_bf16_f32 v19, v22, v23\n"
  "v_mfma_f32_16x16x32_bf16 v[0:3], a[212:215], v[52:55], v[0:3]\n"
  "s_waitcnt vmcnt(16)\n"
  "ds_read_b128 v[168:171], v87 offset:14336\n"
  "ds_read_b128 v[172:175], v88 offset:14336\n"
  "s_add_u32 m0, s59, 12160\n"
  "ds_write_b128 v80, v[16:19] offset:2048\n"
  "v_pk_mul_f32 v[24:25], v[24:25], s[60:61] op_sel_hi:[1,0]\n"
  "v_pk_mul_f32 v[26:27], v[26:27], s[60:61] op_sel_hi:[1,0]\n"
  "v_pk_mul_f32 v[28:29], v[28:29], s[60:61] op_sel_hi:[1,0]\n"
  "v_pk_mul_f32 v[30:31], v[30:31], s[60:61] op_sel_hi:[1,0]\n"
  "v_mfma_f32_16x16x32_bf16 v[4:7], a[236:239], v[52:55], v[92:95]\n"
  "s_waitcnt lgkmcnt(3)\n"
  "buffer_load_dwordx4 v112, s[40:43], s57 offen offset:128  lds\n"
  "buffer_load_dwordx4 v113, s[40:43], s57 offen offset:1152  lds\n"
  "v_exp_f32 v24, v24\n"
  "v_mfma_f32_16x16x32_bf16 v[4:7], a[232:235], v[48:51], v[4:7]\n"
  "s_waitcnt vmcnt(16)\n"
  "ds_read_b128 v[176:179], v87 offset:16384\n"
  "ds_read_b128 v[180:183], v88 offset:16384\n"
  "s_add_u32 m0, s59, 14336\n"
  "v_exp_f32 v25, v25\n"
  "v_mfma_f32_16x16x32_bf16 v[4:7], a[228:231], v[44:47], v[4:7]\n"
  "s_waitcnt lgkmcnt(3)\n"
  "buffer_load_dwordx4 v112, s[40:43], s57 offen offset:0  lds\n"
  "buffer_load_dwordx4 v113, s[40:43], s57 offen offset:1024  lds\n"
  "s_add_u32 s57, s57, 6144\n"
  "v_exp_f32 v26, v26\n"
  "v_mfma_f32_16x16x32_bf16 v[4:7], a[224:227], v[40:43], v[4:7]\n"
  "s_waitcnt vmcnt(16)\n"
  "ds_read_b128 v[200:203], v87 offset:18432\n"
  "ds_read_b128 v[204:207], v88 offset:18432\n"
  "v_exp_f32_e64 v27, v27\n"
  "v_mfma_f32_16x16x32_bf16 v[4:7], a[220:223], v[36:39], v[4:7]\n"
  "v_cvt_pk_bf16_f32 v24, v24, v25\n"
  "v_cvt_pk_bf16_f32 v25, v26, v27\n"
  "v_mfma_f32_16x16x32_bf16 v[4:7], a[216:219], v[32:35], v[4:7]\n"
  "s_waitcnt vmcnt(14)\n"
  "ds_read_b128 v[192:195], v87 offset:0\n"
  "ds_read_b128 v[196:199], v88 offset:0\n"
  "ds_read_b128 v[72:75], v81 offset:4096\n"
  "ds_read_b128 v[76:79], v82 offset:4096\n"
  "v_mfma_f32_16x16x32_bf16 v[8:11], a[240:243], v[32:35], v[92:95]\n"
  "s_barrier\n"
  "v_exp_f32 v28, v28\n"
  "v_mfma_f32_16x16x32_bf16 v[8:11], a[244:247], v[36:39], v[8:11]\n"
  "s_waitcnt vmcnt(12)\n"
  "ds_read_b128 v[184:187], v87 offset:2048\n"
  "ds_read_b128 v[188:191], v88 offset:2048\n"
  "v_exp_f32 v29, v29\n"
  "v_mfma_f32_16x16x32_bf16 v[8:11], a[248:251], v[40:43], v[8:11]\n"
  "v_exp_f32 v30, v30\n"
  "v_mfma_f32_16x16x32_bf16 v[8:11], a[252:255], v[44:47], v[8:11]\n"
  "s_waitcnt vmcnt(10)\n"
  "ds_read_b128 v[208:211], v87 offset:4096\n"
  "ds_read_b128 v[212:215], v88 offset:4096\n"
  "v_exp_f32 v31, v31\n"
  "v_mfma_f32_16x16x32_bf16 v[8:11], v[128:131], v[48:51], v[8:11]\n"
  "v_cvt_pk_bf16_f32 v26, v28, v29\n"
  "v_cvt_pk_bf16_f32 v27, v30, v31\n"
  "v_mfma_f32_16x16x32_bf16 v[8:11], v[132:135], v[52:55], v[8:11]\n"
  "s_waitcnt vmcnt(8)\n"
  "ds_read_b128 v[216:219], v87 offset:6144\n"
  "ds_read_b128 v[220:223], v88 offset:6144\n"
  "ds_write_b128 v80, v[24:27] offset:3072\n"
  "v_pk_mul_f32 v[0:1], v[0:1], s[60:61] op_sel_hi:[1,0]\n"
  "v_pk_mul_f32 v[2:3], v[2:3], s[60:61] op_sel_hi:[1,0]\n"
  "v_pk_mul_f32 v[4:5], v[4:5], s[60:61] op_sel_hi:[1,0]\n"
  "v_pk_mul_f32 v[6:7], v[6:7], s[60:61] op_sel_hi:[1,0]\n"
  "v_mfma_f32_16x16x32_bf16 v[12:15], v[156:159], v[52:55], v[92:95]\n"
  "v_exp_f32 v0, v0\n"
  "v_mfma_f32_16x16x32_bf16 v[12:15], v[152:155], v[48:51], v[12:15]\n"
  "s_waitcnt vmcnt(6)\n"
  "ds_read_b128 v[224:227], v87 offset:8192\n"
  "ds_read_b128 v[228:231], v88 offset:8192\n"
  "v_exp_f32 v1, v1\n"
  "v_mfma_f32_16x16x32_bf16 v[12:15], v[148:151], v[44:47], v[12:15]\n"
  "v_exp_f32 v2, v2\n"
  "v_mfma_f32_16x16x32_bf16 v[12:15], v[144:147], v[40:43], v[12:15]\n"
  "s_waitcnt vmcnt(4)\n"
  "ds_read_b128 v[248:251], v87 offset:10240\n"
  "ds_read_b128 v[252:255], v88 offset:10240\n"
  "v_exp_f32_e64 v3, v3\n"
  "v_mfma_f32_16x16x32_bf16 v[12:15], v[140:143], v[36:39], v[12:15]\n"
  "v_cvt_pk_bf16_f32 v0, v0, v1\n"
  "v_cvt_pk_bf16_f32 v1, v2, v3\n"
  "v_mfma_f32_16x16x32_bf16 v[12:15], v[136:139], v[32:35], v[12:15]\n"
  "s_waitcnt vmcnt(2)\n"
  "ds_read_b128 v[240:243], v87 offset:12288\n"
  "ds_read_b128 v[244:247], v88 offset:12288\n"
  "v_mul_f32 v8, s60, v8\n"
  "v_mul_f32 v9, s60, v9\n"
  "v_mfma_f32_16x16x32_bf16 v[16:19], v[160:163], v[32:35], v[92:95]\n"
  "v_exp_f32 v4, v4\n"
  "v_mfma_f32_16x16x32_bf16 v[16:19], v[164:167], v[36:39], v[16:19]\n"
  "s_waitcnt vmcnt(0)\n"
  "ds_read_b128 v[232:235], v87 offset:14336\n"
  "ds_read_b128 v[236:239], v88 offset:14336\n"
  "v_exp_f32 v5, v5\n"
  "v_mfma_f32_16x16x32_bf16 v[16:19], v[168:171], v[40:43], v[16:19]\n"
  "v_exp_f32 v6, v6\n"
  "v_mfma_f32_16x16x32_bf16 v[16:19], v[172:175], v[44:47], v[16:19]\n"
  "s_waitcnt lgkmcnt(0)\n"
  "s_barrier\n"
  "v_exp_f32 v7, v7\n"
  "v_mfma_f32_16x16x32_bf16 v[16:19], v[176:179], v[48:51], v[16:19]\n"
  "v_cvt_pk_bf16_f32 v2, v4, v5\n"
  "v_cvt_pk_bf16_f32 v3, v6, v7\n"
  "v_mfma_f32_16x16x32_bf16 v[16:19], v[180:183], v[52:55], v[16:19]\n"
  "ds_write_b128 v80, v[0:3] offset:4096\n"
  "v_pk_mul_f32 v[10:11], v[10:11], s[60:61] op_sel_hi:[1,0]\n"
  "v_pk_mul_f32 v[12:13], v[12:13], s[60:61] op_sel_hi:[1,0]\n"
  "v_pk_mul_f32 v[14:15], v[14:15], s[60:61] op_sel_hi:[1,0]\n"
  "v_mfma_f32_16x16x32_bf16 v[20:23], v[204:207], v[52:55], v[92:95]\n"
  "v_exp_f32 v8, v8\n"
  "v_mfma_f32_16x16x32_bf16 v[20:23], v[200:203], v[48:51], v[20:23]\n"
  "v_exp_f32 v9, v9\n"
  "v_mfma_f32_16x16x32_bf16 v[20:23], v[196:199], v[44:47], v[20:23]\n"
  "v_exp_f32 v10, v10\n"
  "v_mfma_f32_16x16x32_bf16 v[20:23], v[192:195], v[40:43], v[20:23]\n"
  "v_exp_f32 v11, v11\n"
  "v_mfma_f32_16x16x32_bf16 v[20:23], v[188:191], v[36:39], v[20:23]\n"
  "v_cvt_pk_bf16_f32 v8, v8, v9\n"
  "v_cvt_pk_bf16_f32 v9, v10, v11\n"
  "v_mfma_f32_16x16x32_bf16 v[20:23], v[184:187], v[32:35], v[20:23]\n"
  "v_mul_f32 v16, s60, v16\n"
  "v_mul_f32 v17, s60, v17\n"
  "v_mfma_f32_16x16x32_bf16 v[24:27], v[208:211], v[32:35], v[92:95]\n"
  "v_exp_f32 v12, v12\n"
  "v_mfma_f32_16x16x32_bf16 v[24:27], v[212:215], v[36:39], v[24:27]\n"
  "v_exp_f32 v13, v13\n"
  "v_mfma_f32_16x16x32_bf16 v[24:27], v[216:219], v[40:43], v[24:27]\n"
  "v_exp_f32 v14, v14\n"
  "v_mfma_f32_16x16x32_bf16 v[24:27], v[220:223], v[44:47], v[24:27]\n"
  "v_exp_f32 v15, v15\n"
  "v_mfma_f32_16x16x32_bf16 v[24:27], v[224:227], v[48:51], v[24:27]\n"
  "v_cvt_pk_bf16_f32 v10, v12, v13\n"
  "v_cvt_pk_bf16_f32 v11, v14, v15\n"
  "v_mfma_f32_16x16x32_bf16 v[24:27], v[228:231], v[52:55], v[24:27]\n"
  "ds_write_b128 v80, v[8:11] offset:5120\n"
  "v_pk_mul_f32 v[18:19], v[18:19], s[60:61] op_sel_hi:[1,0]\n"
  "v_pk_mul_f32 v[20:21], v[20:21], s[60:61] op_sel_hi:[1,0]\n"
  "v_pk_mul_f32 v[22:23], v[22:23], s[60:61] op_sel_hi:[1,0]\n"
  "v_mfma_f32_16x16x32_bf16 v[28:31], v[252:255], v[52:55], v[92:95]\n"
  "v_exp_f32 v16, v16\n"
  "v_mfma_f32_16x16x32_bf16 v[28:31], v[248:251], v[48:51], v[28:31]\n"
  "v_exp_f32 v17, v17\n"
  "v_mfma_f32_16x16x32_bf16 v[28:31], v[244:247], v[44:47], v[28:31]\n"
  "v_exp_f32 v18, v18\n"
  "v_mfma_f32_16x16x32_bf16 v[28:31], v[240:243], v[40:43], v[28:31]\n"
  "s_sub_u32 s56, s56, 1\n"
  "v_exp_f32_e64 v19, v19\n"
  "v_mfma_f32_16x16x32_bf16 v[28:31], v[236:239], v[36:39], v[28:31]\n"
  "v_cvt_pk_bf16_f32 v16, v16, v17\n"
  "v_cvt_pk_bf16_f32 v17, v18, v19\n"
  "v_mfma_f32_16x16x32_bf16 v[28:31], v[232:235], v[32:35], v[28:31]\n"
  ".p2align 3\n"
  "v_mfma_f32_16x16x32_bf16 v[0:3], a[0:3], v[56:59], v[92:95]\n"
  "s_add_u32 s66, s66, 6144\n"
  "v_exp_f32 v20, v20\n"
  "v_mfma_f32_16x16x32_bf16 v[0:3], a[4:7], v[60:63], v[0:3]\n"
  "s_cmp_eq_u32 s66, 102400\n"
  "v_exp_f32 v21, v21\n"
  "v_mfma_f32_16x16x32_bf16 v[0:3], a[8:11], v[64:67], v[0:3]\n"
  "s_cselect_b32 s66, 65536, s66\n"
  "v_exp_f32 v22, v22\n"
  "v_mfma_f32_16x16x32_bf16 v[0:3], a[12:15], v[68:71], v[0:3]\n"
  "v_exp_f32 v23, v23\n"
  "v_mfma_f32_16x16x32_bf16 v[0:3], a[16:19], v[72:75], v[0:3]\n"
  "v_cvt_pk_bf16_f32 v18, v20, v21\n"
  "v_cvt_pk_bf16_f32 v19, v22, v23\n"
  "v_mfma_f32_16x16x32_bf16 v[0:3], a[20:23], v[76:79], v[0:3]\n"
  "ds_write_b128 v80, v[16:19] offset:6144\n"
  "v_pk_mul_f32 v[24:25], v[24:25], s[60:61] op_sel_hi:[1,0]\n"
  "v_pk_mul_f32 v[26:27], v[26:27], s[60:61] op_sel_hi:[1,0]\n"
  "v_pk_mul_f32 v[28:29], v[28:29], s[60:61] op_sel_hi:[1,0]\n"
  "v_pk_mul_f32 v[30:31], v[30:31], s[60:61] op_sel_hi:[1,0]\n"
  "v_mfma_f32_16x16x32_bf16 v[4:7], a[44:47], v[76:79], v[92:95]\n"
  "v_exp_f32 v24, v24\n"
  "v_mfma_f32_16x16x32_bf16 v[4:7], a[40:43], v[72:75], v[4:7]\n"
  "v_exp_f32 v25, v25\n"
  "v_mfma_f32_16x16x32_bf16 v[4:7], a[36:39], v[68:71], v[4:7]\n"
  "v_exp_f32 v26, v26\n"
  "v_mfma_f32_16x16x32_bf16 v[4:7], a[32:35], v[64:67], v[4:7]\n"
  "v_exp_f32 v27, v27\n"
  "v_mfma_f32_16x16x32_bf16 v[4:7], a[28:31], v[60:63], v[4:7]\n"
  "v_cvt_pk_bf16_f32 v24, v24, v25\n"
  "v_cvt_pk_bf16_f32 v25, v26, v27\n"
  "v_mfma_f32_16x16x32_bf16 v[4:7], a[24:27], v[56:59], v[4:7]\n"
  "v_add_u32 v81, s66, v83\n"
  "v_add_u32 v82, s66, v84\n"
  "v_mfma_f32_16x16x32_bf16 v[8:11], a[48:51], v[56:59], v[92:95]\n"
  "v_exp_f32 v28, v28\n"
  "v_mfma_f32_16x16x32_bf16 v[8:11], a[52:55], v[60:63], v[8:11]\n"
  "v_exp_f32 v29, v29\n"
  "v_mfma_f32_16x16x32_bf16 v[8:11], a[56:59], v[64:67], v[8:11]\n"
  "v_exp_f32 v30, v30\n"
  "v_mfma_f32_16x16x32_bf16 v[8:11], a[60:63], v[68:71], v[8:11]\n"
  "v_exp_f32 v31, v31\n"
  "v_mfma_f32_16x16x32_bf16 v[8:11], a[64:67], v[72:75], v[8:11]\n"
  "v_cvt_pk_bf16_f32 v26, v28, v29\n"
  "v_cvt_pk_bf16_f32 v27, v30, v31\n"
  "v_mfma_f32_16x16x32_bf16 v[8:11], a[68:71], v[76:79], v[8:11]\n"
  "ds_write_b128 v80, v[24:27] offset:7168\n"
  "v_pk_mul_f32 v[0:1], v[0:1], s[60:61] op_sel_hi:[1,0]\n"
  "v_pk_mul_f32 v[2:3], v[2:3], s[60:61] op_sel_hi:[1,0]\n"
  "v_pk_mul_f32 v[4:5], v[4:5], s[60:61] op_sel_hi:[1,0]\n"
  "v_pk_mul_f32 v[6:7], v[6:7], s[60:61] op_sel_hi:[1,0]\n"
  "v_mfma_f32_16x16x32_bf16 v[12:15], a[92:95], v[76:79], v[92:95]\n"
  "v_exp_f32 v0, v0\n"
  "v_mfma_f32_16x16x32_bf16 v[12:15], a[88:91], v[72:75], v[12:15]\n"
  "v_exp_f32 v1, v1\n"
  "v_mfma_f32_16x16x32_bf16 v[12:15], a[84:87], v[68:71], v[12:15]\n"
  "v_exp_f32 v2, v2\n"
  "v_mfma_f32_16x16x32_bf16 v[12:15], a[80:83], v[64:67], v[12:15]\n"
  "v_exp_f32 v3, v3\n"
  "v_mfma_f32_16x16x32_bf16 v[12:15], a[76:79], v[60:63], v[12:15]\n"
  "v_cvt_pk_bf16_f32 v0, v0, v1\n"
  "v_cvt_pk_bf16_f32 v1, v2, v3\n"
  "v_mfma_f32_16x16x32_bf16 v[12:15], a[72:75], v[56:59], v[12:15]\n"
  "s_waitcnt lgkmcnt(0)\n"
  "s_barrier\n"
  "v_xor_b32 v80, s88, v80\n"
  "v_mfma_f32_16x16x32_bf16 v[16:19], a[96:99], v[56:59], v[92:95]\n"
  "v_exp_f32 v4, v4\n"
  "v_mfma_f32_16x16x32_bf16 v[16:19], a[100:103], v[60:63], v[16:19]\n"
  "s_xor_b32 s89, s88, s89\n"
  "v_exp_f32 v5, v5\n"
  "v_mfma_f32_16x16x32_bf16 v[16:19], a[104:107], v[64:67], v[16:19]\n"
  "s_xor_b32 s88, s88, s89\n"
  "v_exp_f32 v6, v6\n"
  "v_mfma_f32_16x16x32_bf16 v[16:19], a[108:111], v[68:71], v[16:19]\n"
  "v_exp_f32_e64 v7, v7\n"
  "v_mfma_f32_16x16x32_bf16 v[16:19], a[112:115], v[72:75], v[16:19]\n"
  "v_cvt_pk_bf16_f32 v2, v4, v5\n"
  "v_cvt_pk_bf16_f32 v3, v6, v7\n"
  "v_mfma_f32_16x16x32_bf16 v[16:19], a[116:119], v[76:79], v[16:19]\n"
  "ds_write_b128 v80, v[0:3] offset:0\n"
  "v_pk_mul_f32 v[8:9], v[8:9], s[60:61] op_sel_hi:[1,0]\n"
  "v_pk_mul_f32 v[10:11], v[10:11], s[60:61] op_sel_hi:[1,0]\n"
  "v_pk_mul_f32 v[12:13], v[12:13], s[60:61] op_sel_hi:[1,0]\n"
  "v_pk_mul_f32 v[14:15], v[14:15], s[60:61] op_sel_hi:[1,0]\n"
  "v_mfma_f32_16x16x32_bf16 v[20:23], a[140:143], v[76:79], v[92:95]\n"
  "v_exp_f32 v8, v8\n"
  "v_mfma_f32_16x16x32_bf16 v[20:23], a[136:139], v[72:75], v[20:23]\n"
  "v_exp_f32 v9, v9\n"
  "v_mfma_f32_16x16x32_bf16 v[20:23], a[132:135], v[68:71], v[20:23]\n"
  "v_exp_f32 v10, v10\n"
  "v_mfma_f32_16x16x32_bf16 v[20:23], a[128:131], v[64:67], v[20:23]\n"
  "v_exp_f32 v11, v11\n"
  "v_mfma_f32_16x16x32_bf16 v[20:23], a[124:127], v[60:63], v[20:23]\n"
  "v_cvt_pk_bf16_f32 v8, v8, v9\n"
  "v_cvt_pk_bf16_f32 v9, v10, v11\n"
  "v_mfma_f32_16x16x32_bf16 v[20:23], a[120:123], v[56:59], v[20:23]\n"
  "ds_read_b128 v[32:35], v81 offset:0\n"
  "ds_read_b128 v[36:39], v82 offset:0\n"
  "v_mfma_f32_16x16x32_bf16 v[24:27], a[144:147], v[56:59], v[92:95]\n"
  "v_exp_f32 v12, v12\n"
  "v_mfma_f32_16x16x32_bf16 v[24:27], a[148:151], v[60:63], v[24:27]\n"
  "v_exp_f32 v13, v13\n"
  "v_mfma_f32_16x16x32_bf16 v[24:27], a[152:155], v[64:67], v[24:27]\n"
  "v_exp_f32 v14, v14\n"
  "v_mfma_f32_16x16x32_bf16 v[24:27], a[156:159], v[68:71], v[24:27]\n"
  "v_exp_f32 v15, v15\n"
  "v_mfma_f32_16x16x32_bf16 v[24:27], a[160:163], v[72:75], v[24:27]\n"
  "v_cvt_pk_bf16_f32 v10, v12, v13\n"
  "v_cvt_pk_bf16_f32 v11, v14, v15\n"
  "v_mfma_f32_16x16x32_bf16 v[24:27], a[164:167], v[76:79], v[24:27]\n"
  "ds_write_b128 v80, v[8:11] offset:1024\n"
  "v_pk_mul_f32 v[16:17], v[16:17], s[60:61] op_sel_hi:[1,0]\n"
  "v_pk_mul_f32 v[18:19], v[18:19], s[60:61] op_sel_hi:[1,0]\n"
  "v_pk_mul_f32 v[20:21], v[20:21], s[60:61] op_sel_hi:[1,0]\n"
  "v_pk_mul_f32 v[22:23], v[22:23], s[60:61] op_sel_hi:[1,0]\n"
  "v_mfma_f32_16x16x32_bf16 v[28:31], a[188:191], v[76:79], v[92:95]\n"
  "v_exp_f32 v16, v16\n"
  "v_mfma_f32_16x16x32_bf16 v[28:31], a[184:187], v[72:75], v[28:31]\n"
  "v_exp_f32 v17, v17\n"
  "v_mfma_f32_16x16x32_bf16 v[28:31], a[180:183], v[68:71], v[28:31]\n"
  "v_exp_f32 v18, v18\n"
  "v_mfma_f32_16x16x32_bf16 v[28:31], a[176:179], v[64:67], v[28:31]\n"
  "v_exp_f32 v19, v19\n"
  "v_mfma_f32_16x16x32_bf16 v[28:31], a[172:175], v[60:63], v[28:31]\n"
  "v_cvt_pk_bf16_f32 v16, v16, v17\n"
  "v_cvt_pk_bf16_f32 v17, v18, v19\n"
  "v_mfma_f32_16x16x32_bf16 v[28:31], a[168:171], v[56:59], v[28:31]\n"
  "ds_read_b128 v[40:43], v81 offset:2048\n"
  "ds_read_b128 v[44:47], v82 offset:2048\n"
  "v_mfma_f32_16x16x32_bf16 v[0:3], a[192:195], v[56:59], v[92:95]\n"
  "v_exp_f32 v20, v20\n"
  "v_mfma_f32_16x16x32_bf16 v[0:3], a[196:199], v[60:63], v[0:3]\n"
  "v_exp_f32 v21, v21\n"
  "v_mfma_f32_16x16x32_bf16 v[0:3], a[200:203], v[64:67], v[0:3]\n"
  "v_exp_f32 v22, v22\n"
  "v_mfma_f32_16x16x32_bf16 v[0:3], a[204:207], v[68:71], v[0:3]\n"
  "v_exp_f32 v23, v23\n"
  "v_mfma_f32_16x16x32_bf16 v[0:3], a[208:211], v[72:75], v[0:3]\n"
  "v_cvt_pk_bf16_f32 v18, v20, v21\n"
  "v_cvt_pk_bf16_f32 v19, v22, v23\n"
  "v_mfma_f32_16x16x32_bf16 v[0:3], a[212:215], v[76:79], v[0:3]\n"
  "ds_write_b128 v80, v[16:19] offset:2048\n"
  "v_pk_mul_f32 v[24:25], v[24:25], s[60:61] op_sel_hi:[1,0]\n"
  "v_pk_mul_f32 v[26:27], v[26:27], s[60:61] op_sel_hi:[1,0]\n"
  "v_pk_mul_f32 v[28:29], v[28:29], s[60:61] op_sel_hi:[1,0]\n"
  "v_pk_mul_f32 v[30:31], v[30:31], s[60:61] op_sel_hi:[1,0]\n"
  "v_mfma_f32_16x16x32_bf16 v[4:7], a[236:239], v[76:79], v[92:95]\n"
  "v_exp_f32 v24, v24\n"
  "v_mfma_f32_16x16x32_bf16 v[4:7], a[232:235], v[72:75], v[4:7]\n"
  "v_exp_f32 v25, v25\n"
  "v_mfma_f32_16x16x32_bf16 v[4:7], a[228:231], v[68:71], v[4:7]\n"
  "v_exp_f32 v26, v26\n"
  "v_mfma_f32_16x16x32_bf16 v[4:7], a[224:227], v[64:67], v[4:7]\n"
  "v_exp_f32 v27, v27\n"
  "v_mfma_f32_16x16x32_bf16 v[4:7], a[220:223], v[60:63], v[4:7]\n"
  "v_cvt_pk_bf16_f32 v24, v24, v25\n"
  "v_cvt_pk_bf16_f32 v25, v26, v27\n"
  "v_mfma_f32_16x16x32_bf16 v[4:7], a[216:219], v[56:59], v[4:7]\n"
  "ds_read_b128 v[48:51], v81 offset:4096\n"
  "ds_read_b128 v[52:55], v82 offset:4096\n"
  "v_mfma_f32_16x16x32_bf16 v[8:11], a[240:243], v[56:59], v[92:95]\n"
  "v_exp_f32 v28, v28\n"
  "v_mfma_f32_16x16x32_bf16 v[8:11], a[244:247], v[60:63], v[8:11]\n"
  "v_exp_f32 v29, v29\n"
  "v_mfma_f32_16x16x32_bf16 v[8:11], a[248:251], v[64:67], v[8:11]\n"
  "v_exp_f32 v30, v30\n"
  "v_mfma_f32_16x16x32_bf16 v[8:11], a[252:255], v[68:71], v[8:11]\n"
  "v_exp_f32 v31, v31\n"
  "v_mfma_f32_16x16x32_bf16 v[8:11], v[128:131], v[72:75], v[8:11]\n"
  "v_cvt_pk_bf16_f32 v26, v28, v29\n"
  "v_cvt_pk_bf16_f32 v27, v30, v31\n"
  "v_mfma_f32_16x16x32_bf16 v[8:11], v[132:135], v[76:79], v[8:11]\n"
  "ds_write_b128 v80, v[24:27] offset:3072\n"
  "v_pk_mul_f32 v[0:1], v[0:1], s[60:61] op_sel_hi:[1,0]\n"
  "v_pk_mul_f32 v[2:3], v[2:3], s[60:61] op_sel_hi:[1,0]\n"
  "v_pk_mul_f32 v[4:5], v[4:5], s[60:61] op_sel_hi:[1,0]\n"
  "v_pk_mul_f32 v[6:7], v[6:7], s[60:61] op_sel_hi:[1,0]\n"
  "v_mfma_f32_16x16x32_bf16 v[12:15], v[156:159], v[76:79], v[92:95]\n"
  "v_exp_f32 v0, v0\n"
  "v_mfma_f32_16x16x32_bf16 v[12:15], v[152:155], v[72:75], v[12:15]\n"
  "v_exp_f32 v1, v1\n"
  "v_mfma_f32_16x16x32_bf16 v[12:15], v[148:151], v[68:71], v[12:15]\n"
  "v_exp_f32 v2, v2\n"
  "v_mfma_f32_16x16x32_bf16 v[12:15], v[144:147], v[64:67], v[12:15]\n"
  "v_exp_f32 v3, v3\n"
  "v_mfma_f32_16x16x32_bf16 v[12:15], v[140:143], v[60:63], v[12:15]\n"
  "v_cvt_pk_bf16_f32 v0, v0, v1\n"
  "v_cvt_pk_bf16_f32 v1, v2, v3\n"
  "v_mfma_f32_16x16x32_bf16 v[12:15], v[136:139], v[56:59], v[12:15]\n"
  "v_mul_f32 v8, s60, v8\n"
  "v_mul_f32 v9, s60, v9\n"
  "v_mfma_f32_16x16x32_bf16 v[16:19], v[160:163], v[56:59], v[92:95]\n"
  "v_exp_f32 v4, v4\n"
  "v_mfma_f32_16x16x32_bf16 v[16:19], v[164:167], v[60:63], v[16:19]\n"
  "v_exp_f32 v5, v5\n"
  "v_mfma_f32_16x16x32_bf16 v[16:19], v[168:171], v[64:67], v[16:19]\n"
  "v_exp_f32 v6, v6\n"
  "v_mfma_f32_16x16x32_bf16 v[16:19], v[172:175], v[68:71], v[16:19]\n"
  "v_exp_f32 v7, v7\n"
  "v_mfma_f32_16x16x32_bf16 v[16:19], v[176:179], v[72:75], v[16:19]\n"
  "v_cvt_pk_bf16_f32 v2, v4, v5\n"
  "v_cvt_pk_bf16_f32 v3, v6, v7\n"
  "v_mfma_f32_16x16x32_bf16 v[16:19], v[180:183], v[76:79], v[16:19]\n"
  "ds_write_b128 v80, v[0:3] offset:4096\n"
  "v_pk_mul_f32 v[10:11], v[10:11], s[60:61] op_sel_hi:[1,0]\n"
  "v_pk_mul_f32 v[12:13], v[12:13], s[60:61] op_sel_hi:[1,0]\n"
  "v_pk_mul_f32 v[14:15], v[14:15], s[60:61] op_sel_hi:[1,0]\n"
  "v_mfma_f32_16x16x32_bf16 v[20:23], v[204:207], v[76:79], v[92:95]\n"
  "v_exp_f32 v8, v8\n"
  "v_mfma_f32_16x16x32_bf16 v[20:23], v[200:203], v[72:75], v[20:23]\n"
  "v_exp_f32 v9, v9\n"
  "v_mfma_f32_16x16x32_bf16 v[20:23], v[196:199], v[68:71], v[20:23]\n"
  "v_exp_f32 v10, v10\n"
  "v_mfma_f32_16x16x32_bf16 v[20:23], v[192:195], v[64:67], v[20:23]\n"
  "v_exp_f32 v11, v11\n"
  "v_mfma_f32_16x16x32_bf16 v[20:23], v[188:191], v[60:63], v[20:23]\n"
  "v_cvt_pk_bf16_f32 v8, v8, v9\n"
  "v_cvt_pk_bf16_f32 v9, v10, v11\n"
  "v_mfma_f32_16x16x32_bf16 v[20:23], v[184:187], v[56:59], v[20:23]\n"
  "v_mul_f32 v16, s60, v16\n"
  "v_mul_f32 v17, s60, v17\n"
  "v_mfma_f32_16x16x32_bf16 v[24:27], v[208:211], v[56:59], v[92:95]\n"
  "v_exp_f32 v12, v12\n"
  "v_mfma_f32_16x16x32_bf16 v[24:27], v[212:215], v[60:63], v[24:27]\n"
  "v_exp_f32 v13, v13\n"
  "v_mfma_f32_16x16x32_bf16 v[24:27], v[216:219], v[64:67], v[24:27]\n"
  "v_exp_f32 v14, v14\n"
  "v_mfma_f32_16x16x32_bf16 v[24:27], v[220:223], v[68:71], v[24:27]\n"
  "v_exp_f32 v15, v15\n"
  "v_mfma_f32_16x16x32_bf16 v[24:27], v[224:227], v[72:75], v[24:27]\n"
  "v_cvt_pk_bf16_f32 v10, v12, v13\n"
  "v_cvt_pk_bf16_f32 v11, v14, v15\n"
  "v_mfma_f32_16x16x32_bf16 v[24:27], v[228:231], v[76:79], v[24:27]\n"
  "ds_write_b128 v80, v[8:11] offset:5120\n"
  "v_pk_mul_f32 v[18:19], v[18:19], s[60:61] op_sel_hi:[1,0]\n"
  "v_pk_mul_f32 v[20:21], v[20:21], s[60:61] op_sel_hi:[1,0]\n"
  "v_pk_mul_f32 v[22:23], v[22:23], s[60:61] op_sel_hi:[1,0]\n"
  "v_mfma_f32_16x16x32_bf16 v[28:31], v[252:255], v[76:79], v[92:95]\n"
  "v_exp_f32 v16, v16\n"
  "v_mfma_f32_16x16x32_bf16 v[28:31], v[248:251], v[72:75], v[28:31]\n"
  "v_exp_f32 v17, v17\n"
  "v_mfma_f32_16x16x32_bf16 v[28:31], v[244:247], v[68:71], v[28:31]\n"
  "v_exp_f32 v18, v18\n"
  "v_mfma_f32_16x16x32_bf16 v[28:31], v[240:243], v[64:67], v[28:31]\n"
  "s_sub_u32 s56, s56, 1\n"
  "v_exp_f32_e64 v19, v19\n"
  "v_mfma_f32_16x16x32_bf16 v[28:31], v[236:239], v[60:63], v[28:31]\n"
  "v_cvt_pk_bf16_f32 v16, v16, v17\n"
  "v_cvt_pk_bf16_f32 v17, v18, v19\n"
  "v_mfma_f32_16x16x32_bf16 v[28:31], v[232:235], v[56:59], v[28:31]\n"
  "s_waitcnt lgkmcnt(3)\n"
  "v_mfma_f32_16x16x32_bf16 v[0:3], a[0:3], v[32:35], v[92:95]\n"
  "s_add_u32 s66, s66, 6144\n"
  "v_exp_f32 v20, v20\n"
  "v_mfma_f32_16x16x32_bf16 v[0:3], a[4:7], v[36:39], v[0:3]\n"
  "s_cmp_eq_u32 s66, 102400\n"
  "v_exp_f32 v21, v21\n"
  "v_mfma_f32_16x16x32_bf16 v[0:3], a[8:11], v[40:43], v[0:3]\n"
  "s_cselect_b32 s66, 65536, s66\n"
  "v_exp_f32 v22, v22\n"
  "v_mfma_f32_16x16x32_bf16 v[0:3], a[12:15], v[44:47], v[0:3]\n"
  "v_exp_f32_e64 v23, v23\n"
  "v_mfma_f32_16x16x32_bf16 v[0:3], a[16:19], v[48:51], v[0:3]\n"
  "v_cvt_pk_bf16_f32 v18, v20, v21\n"
  "v_cvt_pk_bf16_f32 v19, v22, v23\n"
  "v_mfma_f32_16x16x32_bf16 v[0:3], a[20:23], v[52:55], v[0:3]\n"
  "ds_write_b128 v80, v[16:19] offset:6144\n"
  "v_pk_mul_f32 v[24:25], v[24:25], s[60:61] op_sel_hi:[1,0]\n"
  "v_pk_mul_f32 v[26:27], v[26:27], s[60:61] op_sel_hi:[1,0]\n"
  "v_pk_mul_f32 v[28:29], v[28:29], s[60:61] op_sel_hi:[1,0]\n"
  "v_pk_mul_f32 v[30:31], v[30:31], s[60:61] op_sel_hi:[1,0]\n"
  "v_mfma_f32_16x16x32_bf16 v[4:7], a[44:47], v[52:55], v[92:95]\n"
  "v_exp_f32 v24, v24\n"
  "v_mfma_f32_16x16x32_bf16 v[4:7], a[40:43], v[48:51], v[4:7]\n"
  "v_exp_f32 v25, v25\n"
  "v_mfma_f32_16x16x32_bf16 v[4:7], a[36:39], v[44:47], v[4:7]\n"
  "v_exp_f32 v26, v26\n"
  "v_mfma_f32_16x16x32_bf16 v[4:7], a[32:35], v[40:43], v[4:7]\n"
  "v_exp_f32 v27, v27\n"
  "v_mfma_f32_16x16x32_bf16 v[4:7], a[28:31], v[36:39], v[4:7]\n"
  "v_cvt_pk_bf16_f32 v24, v24, v25\n"
  "v_cvt_pk_bf16_f32 v25, v26, v27\n"
  "v_mfma_f32_16x16x32_bf16 v[4:7], a[24:27], v[32:35], v[4:7]\n"
  "v_add_u32 v81, s66, v83\n"
  "v_add_u32 v82, s66, v84\n"
  "v_mfma_f32_16x16x32_bf16 v[8:11], a[48:51], v[32:35], v[92:95]\n"
  "v_exp_f32 v28, v28\n"
  "v_mfma_f32_16x16x32_bf16 v[8:11], a[52:55], v[36:39], v[8:11]\n"
  "v_exp_f32 v29, v29\n"
  "v_mfma_f32_16x16x32_bf16 v[8:11], a[56:59], v[40:43], v[8:11]\n"
  "v_exp_f32 v30, v30\n"
  "v_mfma_f32_16x16x32_bf16 v[8:11], a[60:63], v[44:47], v[8:11]\n"
  "v_exp_f32 v31, v31\n"
  "v_mfma_f32_16x16x32_bf16 v[8:11], a[64:67], v[48:51], v[8:11]\n"
  "v_cvt_pk_bf16_f32 v26, v28, v29\n"
  "v_cvt_pk_bf16_f32 v27, v30, v31\n"
  "v_mfma_f32_16x16x32_bf16 v[8:11], a[68:71], v[52:55], v[8:11]\n"
  "ds_write_b128 v80, v[24:27] offset:7168\n"
  "v_pk_mul_f32 v[0:1], v[0:1], s[60:61] op_sel_hi:[1,0]\n"
  "v_pk_mul_f32 v[2:3], v[2:3], s[60:61] op_sel_hi:[1,0]\n"
  "v_pk_mul_f32 v[4:5], v[4:5], s[60:61] op_sel_hi:[1,0]\n"
  "v_pk_mul_f32 v[6:7], v[6:7], s[60:61] op_sel_hi:[1,0]\n"
  "v_mfma_f32_16x16x32_bf16 v[12:15], a[92:95], v[52:55], v[92:95]\n"
  "v_exp_f32 v0, v0\n"
  "v_mfma_f32_16x16x32_bf16 v[12:15], a[88:91], v[48:51], v[12:15]\n"
  "v_exp_f32 v1, v1\n"
  "v_mfma_f32_16x16x32_bf16 v[12:15], a[84:87], v[44:47], v[12:15]\n"
  "v_exp_f32 v2, v2\n"
  "v_mfma_f32_16x16x32_bf16 v[12:15], a[80:83], v[40:43], v[12:15]\n"
  "v_exp_f32 v3, v3\n"
  "v_mfma_f32_16x16x32_bf16 v[12:15], a[76:79], v[36:39], v[12:15]\n"
  "v_cvt_pk_bf16_f32 v0, v0, v1\n"
  "v_cvt_pk_bf16_f32 v1, v2, v3\n"
  "v_mfma_f32_16x16x32_bf16 v[12:15], a[72:75], v[32:35], v[12:15]\n"
  "v_xor_b32 v80, s88, v80\n"
  "v_mfma_f32_16x16x32_bf16 v[16:19], a[96:99], v[32:35], v[92:95]\n"
  "v_exp_f32 v4, v4\n"
  "v_mfma_f32_16x16x32_bf16 v[16:19], a[100:103], v[36:39], v[16:19]\n"
  "s_xor_b32 s89, s88, s89\n"
  "v_exp_f32 v5, v5\n"
  "v_mfma_f32_16x16x32_bf16 v[16:19], a[104:107], v[40:43], v[16:19]\n"
  "s_xor_b32 s88, s88, s89\n"
  "v_exp_f32 v6, v6\n"
  "v_mfma_f32_16x16x32_bf16 v[16:19], a[108:111], v[44:47], v[16:19]\n"
  "v_exp_f32_e64 v7, v7\n"
  "v_mfma_f32_16x16x32_bf16 v[16:19], a[112:115], v[48:51], v[16:19]\n"
  "v_cvt_pk_bf16_f32 v2, v4, v5\n"
  "v_cvt_pk_bf16_f32 v3, v6, v7\n"
  "v_mfma_f32_16x16x32_bf16 v[16:19], a[116:119], v[52:55], v[16:19]\n"
  "ds_write_b128 v80, v[0:3] offset:0\n"
  "v_pk_mul_f32 v[8:9], v[8:9], s[60:61] op_sel_hi:[1,0]\n"
  "v_pk_mul_f32 v[10:11], v[10:11], s[60:61] op_sel_hi:[1,0]\n"
  "v_pk_mul_f32 v[12:13], v[12:13], s[60:61] op_sel_hi:[1,0]\n"
  "v_pk_mul_f32 v[14:15], v[14:15], s[60:61] op_sel_hi:[1,0]\n"
  "v_mfma_f32_16x16x32_bf16 v[20:23], a[140:143], v[52:55], v[92:95]\n"
  "v_exp_f32 v8, v8\n"
  "v_mfma_f32_16x16x32_bf16 v[20:23], a[136:139], v[48:51], v[20:23]\n"
  "v_exp_f32 v9, v9\n"
  "v_mfma_f32_16x16x32_bf16 v[20:23], a[132:135], v[44:47], v[20:23]\n"
  "v_exp_f32 v10, v10\n"
  "v_mfma_f32_16x16x32_bf16 v[20:23], a[128:131], v[40:43], v[20:23]\n"
  "v_exp_f32 v11, v11\n"
  "v_mfma_f32_16x16x32_bf16 v[20:23], a[124:127], v[36:39], v[20:23]\n"
  "v_cvt_pk_bf16_f32 v8, v8, v9\n"
  "v_cvt_pk_bf16_f32 v9, v10, v11\n"
  "v_mfma_f32_16x16x32_bf16 v[20:23], a[120:123], v[32:35], v[20:23]\n"
  "ds_read_b128 v[56:59], v81 offset:0\n"
  "ds_read_b128 v[60:63], v82 offset:0\n"
  "v_mfma_f32_16x16x32_bf16 v[24:27], a[144:147], v[32:35], v[92:95]\n"
  "v_exp_f32 v12, v12\n"
  "v_mfma_f32_16x16x32_bf16 v[24:27], a[148:151], v[36:39], v[24:27]\n"
  "v_exp_f32 v13, v13\n"
  "v_mfma_f32_16x16x32_bf16 v[24:27], a[152:155], v[40:43], v[24:27]\n"
  "v_exp_f32 v14, v14\n"
  "v_mfma_f32_16x16x32_bf16 v[24:27], a[156:159], v[44:47], v[24:27]\n"
  "v_exp_f32 v15, v15\n"
  "v_mfma_f32_16x16x32_bf16 v[24:27], a[160:163], v[48:51], v[24:27]\n"
  "v_cvt_pk_bf16_f32 v10, v12, v13\n"
  "v_cvt_pk_bf16_f32 v11, v14, v15\n"
  "v_mfma_f32_16x16x32_bf16 v[24:27], a[164:167], v[52:55], v[24:27]\n"
  "ds_write_b128 v80, v[8:11] offset:1024\n"
  "v_pk_mul_f32 v[16:17], v[16:17], s[60:61] op_sel_hi:[1,0]\n"
  "v_pk_mul_f32 v[18:19], v[18:19], s[60:61] op_sel_hi:[1,0]\n"
  "v_pk_mul_f32 v[20:21], v[20:21], s[60:61] op_sel_hi:[1,0]\n"
  "v_pk_mul_f32 v[22:23], v[22:23], s[60:61] op_sel_hi:[1,0]\n"
  "v_mfma_f32_16x16x32_bf16 v[28:31], a[188:191], v[52:55], v[92:95]\n"
  "v_exp_f32 v16, v16\n"
  "v_mfma_f32_16x16x32_bf16 v[28:31], a[184:187], v[48:51], v[28:31]\n"
  "v_exp_f32 v17, v17\n"
  "v_mfma_f32_16x16x32_bf16 v[28:31], a[180:183], v[44:47], v[28:31]\n"
  "v_exp_f32 v18, v18\n"
  "v_mfma_f32_16x16x32_bf16 v[28:31], a[176:179], v[40:43], v[28:31]\n"
  "v_exp_f32 v19, v19\n"
  "v_mfma_f32_16x16x32_bf16 v[28:31], a[172:175], v[36:39], v[28:31]\n"
  "v_cvt_pk_bf16_f32 v16, v16, v17\n"
  "v_cvt_pk_bf16_f32 v17, v18, v19\n"
  "v_mfma_f32_16x16x32_bf16 v[28:31], a[168:171], v[32:35], v[28:31]\n"
  "ds_read_b128 v[64:67], v81 offset:2048\n"
  "ds_read_b128 v[68:71], v82 offset:2048\n"
  "v_mfma_f32_16x16x32_bf16 v[0:3], a[192:195], v[32:35], v[92:95]\n"
  "v_exp_f32 v20, v20\n"
  "v_mfma_f32_16x16x32_bf16 v[0:3], a[196:199], v[36:39], v[0:3]\n"
  "v_exp_f32 v21, v21\n"
  "v_mfma_f32_16x16x32_bf16 v[0:3], a[200:203], v[40:43], v[0:3]\n"
  "v_exp_f32 v22, v22\n"
  "v_mfma_f32_16x16x32_bf16 v[0:3], a[204:207], v[44:47], v[0:3]\n"
  "v_exp_f32 v23, v23\n"
  "v_mfma_f32_16x16x32_bf16 v[0:3], a[208:211], v[48:51], v[0:3]\n"
  "v_cvt_pk_bf16_f32 v18, v20, v21\n"
  "v_cvt_pk_bf16_f32 v19, v22, v23\n"
  "v_mfma_f32_16x16x32_bf16 v[0:3], a[212:215], v[52:55], v[0:3]\n"
  "ds_write_b128 v80, v[16:19] offset:2048\n"
  "v_pk_mul_f32 v[24:25], v[24:25], s[60:61] op_sel_hi:[1,0]\n"
  "v_pk_mul_f32 v[26:27], v[26:27], s[60:61] op_sel_hi:[1,0]\n"
  "v_pk_mul_f32 v[28:29], v[28:29], s[60:61] op_sel_hi:[1,0]\n"
  "v_pk_mul_f32 v[30:31], v[30:31], s[60:61] op_sel_hi:[1,0]\n"
  "v_mfma_f32_16x16x32_bf16 v[4:7], a[236:239], v[52:55], v[92:95]\n"
  "v_exp_f32 v24, v24\n"
  "v_mfma_f32_16x16x32_bf16 v[4:7], a[232:235], v[48:51], v[4:7]\n"
  "v_exp_f32 v25, v25\n"
  "v_mfma_f32_16x16x32_bf16 v[4:7], a[228:231], v[44:47], v[4:7]\n"
  "v_exp_f32 v26, v26\n"
  "v_mfma_f32_16x16x32_bf16 v[4:7], a[224:227], v[40:43], v[4:7]\n"
  "v_exp_f32 v27, v27\n"
  "v_mfma_f32_16x16x32_bf16 v[4:7], a[220:223], v[36:39], v[4:7]\n"
  "v_cvt_pk_bf16_f32 v24, v24, v25\n"
  "v_cvt_pk_bf16_f32 v25, v26, v27\n"
  "v_mfma_f32_16x16x32_bf16 v[4:7], a[216:219], v[32:35], v[4:7]\n"
  "ds_read_b128 v[72:75], v81 offset:4096\n"
  "ds_read_b128 v[76:79], v82 offset:4096\n"
  "v_mfma_f32_16x16x32_bf16 v[8:11], a[240:243], v[32:35], v[92:95]\n"
  "v_exp_f32 v28, v28\n"
  "v_mfma_f32_16x16x32_bf16 v[8:11], a[244:247], v[36:39], v[8:11]\n"
  "v_exp_f32 v29, v29\n"
  "v_mfma_f32_16x16x32_bf16 v[8:11], a[248:251], v[40:43], v[8:11]\n"
  "v_exp_f32 v30, v30\n"
  "v_mfma_f32_16x16x32_bf16 v[8:11], a[252:255], v[44:47], v[8:11]\n"
  "v_exp_f32 v31, v31\n"
  "v_mfma_f32_16x16x32_bf16 v[8:11], v[128:131], v[48:51], v[8:11]\n"
  "v_cvt_pk_bf16_f32 v26, v28, v29\n"
  "v_cvt_pk_bf16_f32 v27, v30, v31\n"
  "v_mfma_f32_16x16x32_bf16 v[8:11], v[132:135], v[52:55], v[8:11]\n"
  "ds_write_b128 v80, v[24:27] offset:3072\n"
  "v_pk_mul_f32 v[0:1], v[0:1], s[60:61] op_sel_hi:[1,0]\n"
  "v_pk_mul_f32 v[2:3], v[2:3], s[60:61] op_sel_hi:[1,0]\n"
  "v_pk_mul_f32 v[4:5], v[4:5], s[60:61] op_sel_hi:[1,0]\n"
  "v_pk_mul_f32 v[6:7], v[6:7], s[60:61] op_sel_hi:[1,0]\n"
  "v_mfma_f32_16x16x32_bf16 v[12:15], v[156:159], v[52:55], v[92:95]\n"
  "v_exp_f32 v0, v0\n"
  "v_mfma_f32_16x16x32_bf16 v[12:15], v[152:155], v[48:51], v[12:15]\n"
  "v_exp_f32 v1, v1\n"
  "v_mfma_f32_16x16x32_bf16 v[12:15], v[148:151], v[44:47], v[12:15]\n"
  "v_exp_f32 v2, v2\n"
  "v_mfma_f32_16x16x32_bf16 v[12:15], v[144:147], v[40:43], v[12:15]\n"
  "v_exp_f32 v3, v3\n"
  "v_mfma_f32_16x16x32_bf16 v[12:15], v[140:143], v[36:39], v[12:15]\n"
  "v_cvt_pk_bf16_f32 v0, v0, v1\n"
  "v_cvt_pk_bf16_f32 v1, v2, v3\n"
  "v_mfma_f32_16x16x32_bf16 v[12:15], v[136:139], v[32:35], v[12:15]\n"
  "v_mul_f32 v8, s60, v8\n"
  "v_mul_f32 v9, s60, v9\n"
  "v_mfma_f32_16x16x32_bf16 v[16:19], v[160:163], v[32:35], v[92:95]\n"
  "v_exp_f32 v4, v4\n"
  "v_mfma_f32_16x16x32_bf16 v[16:19], v[164:167], v[36:39], v[16:19]\n"
  "v_exp_f32 v5, v5\n"
  "v_mfma_f32_16x16x32_bf16 v[16:19], v[168:171], v[40:43], v[16:19]\n"
  "v_exp_f32 v6, v6\n"
  "v_mfma_f32_16x16x32_bf16 v[16:19], v[172:175], v[44:47], v[16:19]\n"
  "v_exp_f32 v7, v7\n"
  "v_mfma_f32_16x16x32_bf16 v[16:19], v[176:179], v[48:51], v[16:19]\n"
  "v_cvt_pk_bf16_f32 v2, v4, v5\n"
  "v_cvt_pk_bf16_f32 v3, v6, v7\n"
  "v_mfma_f32_16x16x32_bf16 v[16:19], v[180:183], v[52:55], v[16:19]\n"
  "ds_write_b128 v80, v[0:3] offset:4096\n"
  "v_pk_mul_f32 v[10:11], v[10:11], s[60:61] op_sel_hi:[1,0]\n"
  "v_pk_mul_f32 v[12:13], v[12:13], s[60:61] op_sel_hi:[1,0]\n"
  "v_pk_mul_f32 v[14:15], v[14:15], s[60:61] op_sel_hi:[1,0]\n"
  "v_mfma_f32_16x16x32_bf16 v[20:23], v[204:207], v[52:55], v[92:95]\n"
  "v_exp_f32 v8, v8\n"
  "v_mfma_f32_16x16x32_bf16 v[20:23], v[200:203], v[48:51], v[20:23]\n"
  "v_exp_f32 v9, v9\n"
  "v_mfma_f32_16x16x32_bf16 v[20:23], v[196:199], v[44:47], v[20:23]\n"
  "v_exp_f32 v10, v10\n"
  "v_mfma_f32_16x16x32_bf16 v[20:23], v[192:195], v[40:43], v[20:23]\n"
  "v_exp_f32 v11, v11\n"
  "v_mfma_f32_16x16x32_bf16 v[20:23], v[188:191], v[36:39], v[20:23]\n"
  "v_cvt_pk_bf16_f32 v8, v8, v9\n"
  "v_cvt_pk_bf16_f32 v9, v10, v11\n"
  "v_mfma_f32_16x16x32_bf16 v[20:23], v[184:187], v[32:35], v[20:23]\n"
  "v_mul_f32 v16, s60, v16\n"
  "v_mul_f32 v17, s60, v17\n"
  "v_mfma_f32_16x16x32_bf16 v[24:27], v[208:211], v[32:35], v[92:95]\n"
  "v_exp_f32 v12, v12\n"
  "v_mfma_f32_16x16x32_bf16 v[24:27], v[212:215], v[36:39], v[24:27]\n"
  "v_exp_f32 v13, v13\n"
  "v_mfma_f32_16x16x32_bf16 v[24:27], v[216:219], v[40:43], v[24:27]\n"
  "v_exp_f32 v14, v14\n"
  "v_mfma_f32_16x16x32_bf16 v[24:27], v[220:223], v[44:47], v[24:27]\n"
  "v_exp_f32 v15, v15\n"
  "v_mfma_f32_16x16x32_bf16 v[24:27], v[224:227], v[48:51], v[24:27]\n"
  "v_cvt_pk_bf16_f32 v10, v12, v13\n"
  "v_cvt_pk_bf16_f32 v11, v14, v15\n"
  "v_mfma_f32_16x16x32_bf16 v[24:27], v[228:231], v[52:55], v[24:27]\n"
  "ds_write_b128 v80, v[8:11] offset:5120\n"
  "v_pk_mul_f32 v[18:19], v[18:19], s[60:61] op_sel_hi:[1,0]\n"
  "v_pk_mul_f32 v[20:21], v[20:21], s[60:61] op_sel_hi:[1,0]\n"
  "v_pk_mul_f32 v[22:23], v[22:23], s[60:61] op_sel_hi:[1,0]\n"
  "v_mfma_f32_16x16x32_bf16 v[28:31], v[252:255], v[52:55], v[92:95]\n"
  "v_exp_f32 v16, v16\n"
  "v_mfma_f32_16x16x32_bf16 v[28:31], v[248:251], v[48:51], v[28:31]\n"
  "v_exp_f32 v17, v17\n"
  "v_mfma_f32_16x16x32_bf16 v[28:31], v[244:247], v[44:47], v[28:31]\n"
  "v_exp_f32 v18, v18\n"
  "v_mfma_f32_16x16x32_bf16 v[28:31], v[240:243], v[40:43], v[28:31]\n"
  "s_sub_u32 s56, s56, 1\n"
  "v_exp_f32_e64 v19, v19\n"
  "v_mfma_f32_16x16x32_bf16 v[28:31], v[236:239], v[36:39], v[28:31]\n"
  "v_cvt_pk_bf16_f32 v16, v16, v17\n"
  "v_cvt_pk_bf16_f32 v17, v18, v19\n"
  "v_mfma_f32_16x16x32_bf16 v[28:31], v[232:235], v[32:35], v[28:31]\n"
  "s_waitcnt lgkmcnt(3)\n"
  ".p2align 3\n"
  ".p2align 6\n"
  "A_H1_%=:\n"
  "v_mfma_f32_16x16x32_bf16 v[0:3], a[0:3], v[56:59], v[92:95]\n"
  "s_add_u32 s66, s66, 6144\n"
  "v_exp_f32 v20, v20\n"
  "v_mfma_f32_16x16x32_bf16 v[0:3], a[4:7], v[60:63], v[0:3]\n"
  "s_cmp_eq_u32 s66, 102400\n"
  "v_exp_f32 v21, v21\n"
  "v_mfma_f32_16x16x32_bf16 v[0:3], a[8:11], v[64:67], v[0:3]\n"
  "s_cselect_b32 s66, 65536, s66\n"
  "v_exp_f32 v22, v22\n"
  "v_mfma_f32_16x16x32_bf16 v[0:3], a[12:15], v[68:71], v[0:3]\n"
  "v_exp_f32 v23, v23\n"
  "v_mfma_f32_16x16x32_bf16 v[0:3], a[16:19], v[72:75], v[0:3]\n"
  "v_cvt_pk_bf16_f32 v18, v20, v21\n"
  "v_cvt_pk_bf16_f32 v19, v22, v23\n"
  "v_mfma_f32_16x16x32_bf16 v[0:3], a[20:23], v[76:79], v[0:3]\n"
  "ds_write_b128 v80, v[16:19] offset:6144\n"
  "v_pk_mul_f32 v[24:25], v[24:25], s[60:61] op_sel_hi:[1,0]\n"
  "v_pk_mul_f32 v[26:27], v[26:27], s[60:61] op_sel_hi:[1,0]\n"
  "v_pk_mul_f32 v[28:29], v[28:29], s[60:61] op_sel_hi:[1,0]\n"
  "v_pk_mul_f32 v[30:31], v[30:31], s[60:61] op_sel_hi:[1,0]\n"
  "v_mfma_f32_16x16x32_bf16 v[4:7], a[44:47], v[76:79], v[92:95]\n"
  "v_exp_f32 v24, v24\n"
  "v_mfma_f32_16x16x32_bf16 v[4:7], a[40:43], v[72:75], v[4:7]\n"
  "v_exp_f32 v25, v25\n"
  "v_mfma_f32_16x16x32_bf16 v[4:7], a[36:39], v[68:71], v[4:7]\n"
  "v_exp_f32 v26, v26\n"
  "v_mfma_f32_16x16x32_bf16 v[4:7], a[32:35], v[64:67], v[4:7]\n"
  "v_exp_f32 v27, v27\n"
  "v_mfma_f32_16x16x32_bf16 v[4:7], a[28:31], v[60:63], v[4:7]\n"
  "v_cvt_pk_bf16_f32 v24, v24, v25\n"
  "v_cvt_pk_bf16_f32 v25, v26, v27\n"
  "v_mfma_f32_16x16x32_bf16 v[4:7], a[24:27], v[56:59], v[4:7]\n"
  "v_add_u32 v81, s66, v83\n"
  "v_add_u32 v82, s66, v84\n"
  "v_mfma_f32_16x16x32_bf16 v[8:11], a[48:51], v[56:59], v[92:95]\n"
  "v_exp_f32 v28, v28\n"
  "v_mfma_f32_16x16x32_bf16 v[8:11], a[52:55], v[60:63], v[8:11]\n"
  "v_exp_f32 v29, v29\n"
  "v_mfma_f32_16x16x32_bf16 v[8:11], a[56:59], v[64:67], v[8:11]\n"
  "v_exp_f32 v30, v30\n"
  "v_mfma_f32_16x16x32_bf16 v[8:11], a[60:63], v[68:71], v[8:11]\n"
  "v_exp_f32 v31, v31\n"
  "v_mfma_f32_16x16x32_bf16 v[8:11], a[64:67], v[72:75], v[8:11]\n"
  "v_cvt_pk_bf16_f32 v26, v28, v29\n"
  "v_cvt_pk_bf16_f32 v27, v30, v31\n"
  "v_mfma_f32_16x16x32_bf16 v[8:11], a[68:71], v[76:79], v[8:11]\n"
  "ds_write_b128 v80, v[24:27] offset:7168\n"
  "v_pk_mul_f32 v[0:1], v[0:1], s[60:61] op_sel_hi:[1,0]\n"
  "v_pk_mul_f32 v[2:3], v[2:3], s[60:61] op_sel_hi:[1,0]\n"
  "v_pk_mul_f32 v[4:5], v[4:5], s[60:61] op_sel_hi:[1,0]\n"
  "v_pk_mul_f32 v[6:7], v[6:7], s[60:61] op_sel_hi:[1,0]\n"
  "v_mfma_f32_16x16x32_bf16 v[12:15], a[92:95], v[76:79], v[92:95]\n"
  "v_exp_f32 v0, v0\n"
  "v_mfma_f32_16x16x32_bf16 v[12:15], a[88:91], v[72:75], v[12:15]\n"
  "v_exp_f32 v1, v1\n"
  "v_mfma_f32_16x16x32_bf16 v[12:15], a[84:87], v[68:71], v[12:15]\n"
  "v_exp_f32 v2, v2\n"
  "v_mfma_f32_16x16x32_bf16 v[12:15], a[80:83], v[64:67], v[12:15]\n"
  "v_exp_f32 v3, v3\n"
  "v_mfma_f32_16x16x32_bf16 v[12:15], a[76:79], v[60:63], v[12:15]\n"
  "v_cvt_pk_bf16_f32 v0, v0, v1\n"
  "v_cvt_pk_bf16_f32 v1, v2, v3\n"
  "v_mfma_f32_16x16x32_bf16 v[12:15], a[72:75], v[56:59], v[12:15]\n"
  "s_barrier\n"
  "v_xor_b32 v80, s88, v80\n"
  "v_mfma_f32_16x16x32_bf16 v[16:19], a[96:99], v[56:59], v[92:95]\n"
  "v_exp_f32 v4, v4\n"
  "v_mfma_f32_16x16x32_bf16 v[16:19], a[100:103], v[60:63], v[16:19]\n"
  "s_xor_b32 s89, s88, s89\n"
  "v_exp_f32 v5, v5\n"
  "v_mfma_f32_16x16x32_bf16 v[16:19], a[104:107], v[64:67], v[16:19]\n"
  "s_xor_b32 s88, s88, s89\n"
  "v_exp_f32 v6, v6\n"
  "v_mfma_f32_16x16x32_bf16 v[16:19], a[108:111], v[68:71], v[16:19]\n"
  "v_exp_f32 v7, v7\n"
  "v_mfma_f32_16x16x32_bf16 v[16:19], a[112:115], v[72:75], v[16:19]\n"
  "v_cvt_pk_bf16_f32 v2, v4, v5\n"
  "v_cvt_pk_bf16_f32 v3, v6, v7\n"
  "v_mfma_f32_16x16x32_bf16 v[16:19], a[116:119], v[76:79], v[16:19]\n"
  "ds_write_b128 v80, v[0:3] offset:0\n"
  "v_pk_mul_f32 v[8:9], v[8:9], s[60:61] op_sel_hi:[1,0]\n"
  "v_pk_mul_f32 v[10:11], v[10:11], s[60:61] op_sel_hi:[1,0]\n"
  "v_pk_mul_f32 v[12:13], v[12:13], s[60:61] op_sel_hi:[1,0]\n"
  "v_pk_mul_f32 v[14:15], v[14:15], s[60:61] op_sel_hi:[1,0]\n"
  "v_mfma_f32_16x16x32_bf16 v[20:23], a[140:143], v[76:79], v[92:95]\n"
  "v_exp_f32 v8, v8\n"
  "v_mfma_f32_16x16x32_bf16 v[20:23], a[136:139], v[72:75], v[20:23]\n"
  "v_exp_f32 v9, v9\n"
  "v_mfma_f32_16x16x32_bf16 v[20:23], a[132:135], v[68:71], v[20:23]\n"
  "v_exp_f32 v10, v10\n"
  "v_mfma_f32_16x16x32_bf16 v[20:23], a[128:131], v[64:67], v[20:23]\n"
  "v_exp_f32 v11, v11\n"
  "v_mfma_f32_16x16x32_bf16 v[20:23], a[124:127], v[60:63], v[20:23]\n"
  "v_cvt_pk_bf16_f32 v8, v8, v9\n"
  "v_cvt_pk_bf16_f32 v9, v10, v11\n"
  "v_mfma_f32_16x16x32_bf16 v[20:23], a[120:123], v[56:59], v[20:23]\n"
  "ds_read_b128 v[32:35], v81 offset:0\n"
  "ds_read_b128 v[36:39], v82 offset:0\n"
  "v_mfma_f32_16x16x32_bf16 v[24:27], a[144:147], v[56:59], v[92:95]\n"
  "v_exp_f32 v12, v12\n"
  "v_mfma_f32_16x16x32_bf16 v[24:27], a[148:151], v[60:63], v[24:27]\n"
  "v_exp_f32 v13, v13\n"
  "v_mfma_f32_16x16x32_bf16 v[24:27], a[152:155], v[64:67], v[24:27]\n"
  "v_exp_f32 v14, v14\n"
  "v_mfma_f32_16x16x32_bf16 v[24:27], a[156:159], v[68:71], v[24:27]\n"
  "v_exp_f32 v15, v15\n"
  "v_mfma_f32_16x16x32_bf16 v[24:27], a[160:163], v[72:75], v[24:27]\n"
  "v_cvt_pk_bf16_f32 v10, v12, v13\n"
  "v_cvt_pk_bf16_f32 v11, v14, v15\n"
  "v_mfma_f32_16x16x32_bf16 v[24:27], a[164:167], v[76:79], v[24:27]\n"
  "ds_write_b128 v80, v[8:11] offset:1024\n"
  "v_pk_mul_f32 v[16:17], v[16:17], s[60:61] op_sel_hi:[1,0]\n"
  "v_pk_mul_f32 v[18:19], v[18:19], s[60:61] op_sel_hi:[1,0]\n"
  "v_pk_mul_f32 v[20:21], v[20:21], s[60:61] op_sel_hi:[1,0]\n"
  "v_pk_mul_f32 v[22:23], v[22:23], s[60:61] op_sel_hi:[1,0]\n"
  "v_mfma_f32_16x16x32_bf16 v[28:31], a[188:191], v[76:79], v[92:95]\n"
  "v_exp_f32 v16, v16\n"
  "v_mfma_f32_16x16x32_bf16 v[28:31], a[184:187], v[72:75], v[28:31]\n"
  "v_exp_f32 v17, v17\n"
  "v_mfma_f32_16x16x32_bf16 v[28:31], a[180:183], v[68:71], v[28:31]\n"
  "v_exp_f32 v18, v18\n"
  "v_mfma_f32_16x16x32_bf16 v[28:31], a[176:179], v[64:67], v[28:31]\n"
  "v_exp_f32 v19, v19\n"
  "v_mfma_f32_16x16x32_bf16 v[28:31], a[172:175], v[60:63], v[28:31]\n"
  "v_cvt_pk_bf16_f32 v16, v16, v17\n"
  "v_cvt_pk_bf16_f32 v17, v18, v19\n"
  "v_mfma_f32_16x16x32_bf16 v[28:31], a[168:171], v[56:59], v[28:31]\n"
  "ds_read_b128 v[40:43], v81 offset:2048\n"
  "ds_read_b128 v[44:47], v82 offset:2048\n"
  "v_mfma_f32_16x16x32_bf16 v[0:3], a[192:195], v[56:59], v[92:95]\n"
  "v_exp_f32 v20, v20\n"
  "v_mfma_f32_16x16x32_bf16 v[0:3], a[196:199], v[60:63], v[0:3]\n"
  "v_exp_f32 v21, v21\n"
  "v_mfma_f32_16x16x32_bf16 v[0:3], a[200:203], v[64:67], v[0:3]\n"
  "v_exp_f32 v22, v22\n"
  "v_mfma_f32_16x16x32_bf16 v[0:3], a[204:207], v[68:71], v[0:3]\n"
  "v_exp_f32 v23, v23\n"
  "v_mfma_f32_16x16x32_bf16 v[0:3], a[208:211], v[72:75], v[0:3]\n"
  "v_cvt_pk_bf16_f32 v18, v20, v21\n"
  "v_cvt_pk_bf16_f32 v19, v22, v23\n"
  "v_mfma_f32_16x16x32_bf16 v[0:3], a[212:215], v[76:79], v[0:3]\n"
  "ds_write_b128 v80, v[16:19] offset:2048\n"
  "v_pk_mul_f32 v[24:25], v[24:25], s[60:61] op_sel_hi:[1,0]\n"
  "v_pk_mul_f32 v[26:27], v[26:27], s[60:61] op_sel_hi:[1,0]\n"
  "v_pk_mul_f32 v[28:29], v[28:29], s[60:61] op_sel_hi:[1,0]\n"
  "v_pk_mul_f32 v[30:31], v[30:31], s[60:61] op_sel_hi:[1,0]\n"
  "v_mfma_f32_16x16x32_bf16 v[4:7], a[236:239], v[76:79], v[92:95]\n"
  "v_exp_f32 v24, v24\n"
  "v_mfma_f32_16x16x32_bf16 v[4:7], a[232:235], v[72:75], v[4:7]\n"
  "v_exp_f32 v25, v25\n"
  "v_mfma_f32_16x16x32_bf16 v[4:7], a[228:231], v[68:71], v[4:7]\n"
  "v_exp_f32 v26, v26\n"
  "v_mfma_f32_16x16x32_bf16 v[4:7], a[224:227], v[64:67], v[4:7]\n"
  "v_exp_f32 v27, v27\n"
  "v_mfma_f32_16x16x32_bf16 v[4:7], a[220:223], v[60:63], v[4:7]\n"
  "v_cvt_pk_bf16_f32 v24, v24, v25\n"
  "v_cvt_pk_bf16_f32 v25, v26, v27\n"
  "v_mfma_f32_16x16x32_bf16 v[4:7], a[216:219], v[56:59], v[4:7]\n"
  "ds_read_b128 v[48:51], v81 offset:4096\n"
  "ds_read_b128 v[52:55], v82 offset:4096\n"
  "v_mfma_f32_16x16x32_bf16 v[8:11], a[240:243], v[56:59], v[92:95]\n"
  "v_exp_f32 v28, v28\n"
  "v_mfma_f32_16x16x32_bf16 v[8:11], a[244:247], v[60:63], v[8:11]\n"
  "v_exp_f32 v29, v29\n"
  "v_mfma_f32_16x16x32_bf16 v[8:11], a[248:251], v[64:67], v[8:11]\n"
  "v_exp_f32 v30, v30\n"
  "v_mfma_f32_16x16x32_bf16 v[8:11], a[252:255], v[68:71], v[8:11]\n"
  "v_exp_f32 v31, v31\n"
  "v_mfma_f32_16x16x32_bf16 v[8:11], v[128:131], v[72:75], v[8:11]\n"
  "v_cvt_pk_bf16_f32 v26, v28, v29\n"
  "v_cvt_pk_bf16_f32 v27, v30, v31\n"
  "v_mfma_f32_16x16x32_bf16 v[8:11], v[132:135], v[76:79], v[8:11]\n"
  "ds_write_b128 v80, v[24:27] offset:3072\n"
  "v_pk_mul_f32 v[0:1], v[0:1], s[60:61] op_sel_hi:[1,0]\n"
  "v_pk_mul_f32 v[2:3], v[2:3], s[60:61] op_sel_hi:[1,0]\n"
  "v_pk_mul_f32 v[4:5], v[4:5], s[60:61] op_sel_hi:[1,0]\n"
  "v_pk_mul_f32 v[6:7], v[6:7], s[60:61] op_sel_hi:[1,0]\n"
  "v_mfma_f32_16x16x32_bf16 v[12:15], v[156:159], v[76:79], v[92:95]\n"
  "v_exp_f32 v0, v0\n"
  "v_mfma_f32_16x16x32_bf16 v[12:15], v[152:155], v[72:75], v[12:15]\n"
  "v_exp_f32 v1, v1\n"
  "v_mfma_f32_16x16x32_bf16 v[12:15], v[148:151], v[68:71], v[12:15]\n"
  "v_exp_f32 v2, v2\n"
  "v_mfma_f32_16x16x32_bf16 v[12:15], v[144:147], v[64:67], v[12:15]\n"
  "v_exp_f32 v3, v3\n"
  "v_mfma_f32_16x16x32_bf16 v[12:15], v[140:143], v[60:63], v[12:15]\n"
  "v_cvt_pk_bf16_f32 v0, v0, v1\n"
  "v_cvt_pk_bf16_f32 v1, v2, v3\n"
  "v_mfma_f32_16x16x32_bf16 v[12:15], v[136:139], v[56:59], v[12:15]\n"
  "v_mul_f32 v8, s60, v8\n"
  "v_mul_f32 v9, s60, v9\n"
  "v_mfma_f32_16x16x32_bf16 v[16:19], v[160:163], v[56:59], v[92:95]\n"
  "v_exp_f32 v4, v4\n"
  "v_mfma_f32_16x16x32_bf16 v[16:19], v[164:167], v[60:63], v[16:19]\n"
  "v_exp_f32 v5, v5\n"
  "v_mfma_f32_16x16x32_bf16 v[16:19], v[168:171], v[64:67], v[16:19]\n"
  "v_exp_f32 v6, v6\n"
  "v_mfma_f32_16x16x32_bf16 v[16:19], v[172:175], v[68:71], v[16:19]\n"
  "v_exp_f32 v7, v7\n"
  "v_mfma_f32_16x16x32_bf16 v[16:19], v[176:179], v[72:75], v[16:19]\n"
  "v_cvt_pk_bf16_f32 v2, v4, v5\n"
  "v_cvt_pk_bf16_f32 v3, v6, v7\n"
  "v_mfma_f32_16x16x32_bf16 v[16:19], v[180:183], v[76:79], v[16:19]\n"
  "ds_write_b128 v80, v[0:3] offset:4096\n"
  "v_pk_mul_f32 v[10:11], v[10:11], s[60:61] op_sel_hi:[1,0]\n"
  "v_pk_mul_f32 v[12:13], v[12:13], s[60:61] op_sel_hi:[1,0]\n"
  "v_pk_mul_f32 v[14:15], v[14:15], s[60:61] op_sel_hi:[1,0]\n"
  "v_mfma_f32_16x16x32_bf16 v[20:23], v[204:207], v[76:79], v[92:95]\n"
  "v_exp_f32 v8, v8\n"
  "v_mfma_f32_16x16x32_bf16 v[20:23], v[200:203], v[72:75], v[20:23]\n"
  "v_exp_f32 v9, v9\n"
  "v_mfma_f32_16x16x32_bf16 v[20:23], v[196:199], v[68:71], v[20:23]\n"
  "v_exp_f32 v10, v10\n"
  "v_mfma_f32_16x16x32_bf16 v[20:23], v[192:195], v[64:67], v[20:23]\n"
  "v_exp_f32 v11, v11\n"
  "v_mfma_f32_16x16x32_bf16 v[20:23], v[188:191], v[60:63], v[20:23]\n"
  "v_cvt_pk_bf16_f32 v8, v8, v9\n"
  "v_cvt_pk_bf16_f32 v9, v10, v11\n"
  "v_mfma_f32_16x16x32_bf16 v[20:23], v[184:187], v[56:59], v[20:23]\n"
  "v_mul_f32 v16, s60, v16\n"
  "v_mul_f32 v17, s60, v17\n"
  "v_mfma_f32_16x16x32_bf16 v[24:27], v[208:211], v[56:59], v[92:95]\n"
  "v_exp_f32 v12, v12\n"
  "v_mfma_f32_16x16x32_bf16 v[24:27], v[212:215], v[60:63], v[24:27]\n"
  "v_exp_f32 v13, v13\n"
  "v_mfma_f32_16x16x32_bf16 v[24:27], v[216:219], v[64:67], v[24:27]\n"
  "v_exp_f32 v14, v14\n"
  "v_mfma_f32_16x16x32_bf16 v[24:27], v[220:223], v[68:71], v[24:27]\n"
  "v_exp_f32 v15, v15\n"
  "v_mfma_f32_16x16x32_bf16 v[24:27], v[224:227], v[72:75], v[24:27]\n"
  "v_cvt_pk_bf16_f32 v10, v12, v13\n"
  "v_cvt_pk_bf16_f32 v11, v14, v15\n"
  "v_mfma_f32_16x16x32_bf16 v[24:27], v[228:231], v[76:79], v[24:27]\n"
  "ds_write_b128 v80, v[8:11] offset:5120\n"
  "v_pk_mul_f32 v[18:19], v[18:19], s[60:61] op_sel_hi:[1,0]\n"
  "v_pk_mul_f32 v[20:21], v[20:21], s[60:61] op_sel_hi:[1,0]\n"
  "v_pk_mul_f32 v[22:23], v[22:23], s[60:61] op_sel_hi:[1,0]\n"
  "v_mfma_f32_16x16x32_bf16 v[28:31], v[252:255], v[76:79], v[92:95]\n"
  "v_exp_f32 v16, v16\n"
  "v_mfma_f32_16x16x32_bf16 v[28:31], v[248:251], v[72:75], v[28:31]\n"
  "v_exp_f32 v17, v17\n"
  "v_mfma_f32_16x16x32_bf16 v[28:31], v[244:247], v[68:71], v[28:31]\n"
  "v_exp_f32 v18, v18\n"
  "v_mfma_f32_16x16x32_bf16 v[28:31], v[240:243], v[64:67], v[28:31]\n"
  "s_sub_u32 s56, s56, 1\n"
  "v_exp_f32 v19, v19\n"
  "v_mfma_f32_16x16x32_bf16 v[28:31], v[236:239], v[60:63], v[28:31]\n"
  "s_cmp_eq_u32 s56, 0\n"
  "v_cvt_pk_bf16_f32 v16, v16, v17\n"
  "v_cvt_pk_bf16_f32 v17, v18, v19\n"
  "v_mfma_f32_16x16x32_bf16 v[28:31], v[232:235], v[56:59], v[28:31]\n"
  "s_waitcnt lgkmcnt(3)\n"
  "s_cbranch_scc1 A_X0_%=\n"
  ".p2align 3\n"
  "A_H0_%=:\n"
  "v_mfma_f32_16x16x32_bf16 v[0:3], a[0:3], v[32:35], v[92:95]\n"
  "s_add_u32 s66, s66, 6144\n"
  "v_exp_f32 v20, v20\n"
  "v_mfma_f32_16x16x32_bf16 v[0:3], a[4:7], v[36:39], v[0:3]\n"
  "s_cmp_eq_u32 s66, 102400\n"
  "v_exp_f32 v21, v21\n"
  "v_mfma_f32_16x16x32_bf16 v[0:3], a[8:11], v[40:43], v[0:3]\n"
  "s_cselect_b32 s66, 65536, s66\n"
  "v_exp_f32 v22, v22\n"
  "v_mfma_f32_16x16x32_bf16 v[0:3], a[12:15], v[44:47], v[0:3]\n"
  "v_exp_f32 v23, v23\n"
  "v_mfma_f32_16x16x32_bf16 v[0:3], a[16:19], v[48:51], v[0:3]\n"
  "v_cvt_pk_bf16_f32 v18, v20, v21\n"
  "v_cvt_pk_bf16_f32 v19, v22, v23\n"
  "v_mfma_f32_16x16x32_bf16 v[0:3], a[20:23], v[52:55], v[0:3]\n"
  "ds_write_b128 v80, v[16:19] offset:6144\n"
  "v_pk_mul_f32 v[24:25], v[24:25], s[60:61] op_sel_hi:[1,0]\n"
  "v_pk_mul_f32 v[26:27], v[26:27], s[60:61] op_sel_hi:[1,0]\n"
  "v_pk_mul_f32 v[28:29], v[28:29], s[60:61] op_sel_hi:[1,0]\n"
  "v_pk_mul_f32 v[30:31], v[30:31], s[60:61] op_sel_hi:[1,0]\n"
  "v_mfma_f32_16x16x32_bf16 v[4:7], a[44:47], v[52:55], v[92:95]\n"
  "v_exp_f32 v24, v24\n"
  "v_mfma_f32_16x16x32_bf16 v[4:7], a[40:43], v[48:51], v[4:7]\n"
  "v_exp_f32 v25, v25\n"
  "v_mfma_f32_16x16x32_bf16 v[4:7], a[36:39], v[44:47], v[4:7]\n"
  "v_exp_f32 v26, v26\n"
  "v_mfma_f32_16x16x32_bf16 v[4:7], a[32:35], v[40:43], v[4:7]\n"
  "v_exp_f32 v27, v27\n"
  "v_mfma_f32_16x16x32_bf16 v[4:7], a[28:31], v[36:39], v[4:7]\n"
  "v_cvt_pk_bf16_f32 v24, v24, v25\n"
  "v_cvt_pk_bf16_f32 v25, v26, v27\n"
  "v_mfma_f32_16x16x32_bf16 v[4:7], a[24:27], v[32:35], v[4:7]\n"
  "v_add_u32 v81, s66, v83\n"
  "v_add_u32 v82, s66, v84\n"
  "v_mfma_f32_16x16x32_bf16 v[8:11], a[48:51], v[32:35], v[92:95]\n"
  "v_exp_f32 v28, v28\n"
  "v_mfma_f32_16x16x32_bf16 v[8:11], a[52:55], v[36:39], v[8:11]\n"
  "v_exp_f32 v29, v29\n"
  "v_mfma_f32_16x16x32_bf16 v[8:11], a[56:59], v[40:43], v[8:11]\n"
  "v_exp_f32 v30, v30\n"
  "v_mfma_f32_16x16x32_bf16 v[8:11], a[60:63], v[44:47], v[8:11]\n"
  "v_exp_f32 v31, v31\n"
  "v_mfma_f32_16x16x32_bf16 v[8:11], a[64:67], v[48:51], v[8:11]\n"
  "v_cvt_pk_bf16_f32 v26, v28, v29\n"
  "v_cvt_pk_bf16_f32 v27, v30, v31\n"
  "v_mfma_f32_16x16x32_bf16 v[8:11], a[68:71], v[52:55], v[8:11]\n"
  "ds_write_b128 v80, v[24:27] offset:7168\n"
  "v_pk_mul_f32 v[0:1], v[0:1], s[60:61] op_sel_hi:[1,0]\n"
  "v_pk_mul_f32 v[2:3], v[2:3], s[60:61] op_sel_hi:[1,0]\n"
  "v_pk_mul_f32 v[4:5], v[4:5], s[60:61] op_sel_hi:[1,0]\n"
  "v_pk_mul_f32 v[6:7], v[6:7], s[60:61] op_sel_hi:[1,0]\n"
  "v_mfma_f32_16x16x32_bf16 v[12:15], a[92:95], v[52:55], v[92:95]\n"
  "v_exp_f32 v0, v0\n"
  "v_mfma_f32_16x16x32_bf16 v[12:15], a[88:91], v[48:51], v[12:15]\n"
  "v_exp_f32 v1, v1\n"
  "v_mfma_f32_16x16x32_bf16 v[12:15], a[84:87], v[44:47], v[12:15]\n"
  "v_exp_f32 v2, v2\n"
  "v_mfma_f32_16x16x32_bf16 v[12:15], a[80:83], v[40:43], v[12:15]\n"
  "v_exp_f32 v3, v3\n"
  "v_mfma_f32_16x16x32_bf16 v[12:15], a[76:79], v[36:39], v[12:15]\n"
  "v_cvt_pk_bf16_f32 v0, v0, v1\n"
  "v_cvt_pk_bf16_f32 v1, v2, v3\n"
  "v_mfma_f32_16x16x32_bf16 v[12:15], a[72:75], v[32:35], v[12:15]\n"
  "s_barrier\n"
  "v_xor_b32 v80, s88, v80\n"
  "v_mfma_f32_16x16x32_bf16 v[16:19], a[96:99], v[32:35], v[92:95]\n"
  "v_exp_f32 v4, v4\n"
  "v_mfma_f32_16x16x32_bf16 v[16:19], a[100:103], v[36:39], v[16:19]\n"
  "s_xor_b32 s89, s88, s89\n"
  "v_exp_f32 v5, v5\n"
  "v_mfma_f32_16x16x32_bf16 v[16:19], a[104:107], v[40:43], v[16:19]\n"
  "s_xor_b32 s88, s88, s89\n"
  "v_exp_f32 v6, v6\n"
  "v_mfma_f32_16x16x32_bf16 v[16:19], a[108:111], v[44:47], v[16:19]\n"
  "v_exp_f32 v7, v7\n"
  "v_mfma_f32_16x16x32_bf16 v[16:19], a[112:115], v[48:51], v[16:19]\n"
  "v_cvt_pk_bf16_f32 v2, v4, v5\n"
  "v_cvt_pk_bf16_f32 v3, v6, v7\n"
  "v_mfma_f32_16x16x32_bf16 v[16:19], a[116:119], v[52:55], v[16:19]\n"
  "ds_write_b128 v80, v[0:3] offset:0\n"
  "v_pk_mul_f32 v[8:9], v[8:9], s[60:61] op_sel_hi:[1,0]\n"
  "v_pk_mul_f32 v[10:11], v[10:11], s[60:61] op_sel_hi:[1,0]\n"
  "v_pk_mul_f32 v[12:13], v[12:13], s[60:61] op_sel_hi:[1,0]\n"
  "v_pk_mul_f32 v[14:15], v[14:15], s[60:61] op_sel_hi:[1,0]\n"
  "v_mfma_f32_16x16x32_bf16 v[20:23], a[140:143], v[52:55], v[92:95]\n"
  "v_exp_f32 v8, v8\n"
  "v_mfma_f32_16x16x32_bf16 v[20:23], a[136:139], v[48:51], v[20:23]\n"
  "v_exp_f32 v9, v9\n"
  "v_mfma_f32_16x16x32_bf16 v[20:23], a[132:135], v[44:47], v[20:23]\n"
  "v_exp_f32 v10, v10\n"
  "v_mfma_f32_16x16x32_bf16 v[20:23], a[128:131], v[40:43], v[20:23]\n"
  "v_exp_f32 v11, v11\n"
  "v_mfma_f32_16x16x32_bf16 v[20:23], a[124:127], v[36:39], v[20:23]\n"
  "v_cvt_pk_bf16_f32 v8, v8, v9\n"
  "v_cvt_pk_bf16_f32 v9, v10, v11\n"
  "v_mfma_f32_16x16x32_bf16 v[20:23], a[120:123], v[32:35], v[20:23]\n"
  "ds_read_b128 v[56:59], v81 offset:0\n"
  "ds_read_b128 v[60:63], v82 offset:0\n"
  "v_mfma_f32_16x16x32_bf16 v[24:27], a[144:147], v[32:35], v[92:95]\n"
  "v_exp_f32 v12, v12\n"
  "v_mfma_f32_16x16x32_bf16 v[24:27], a[148:151], v[36:39], v[24:27]\n"
  "v_exp_f32 v13, v13\n"
  "v_mfma_f32_16x16x32_bf16 v[24:27], a[152:155], v[40:43], v[24:27]\n"
  "v_exp_f32 v14, v14\n"
  "v_mfma_f32_16x16x32_bf16 v[24:27], a[156:159], v[44:47], v[24:27]\n"
  "v_exp_f32 v15, v15\n"
  "v_mfma_f32_16x16x32_bf16 v[24:27], a[160:163], v[48:51], v[24:27]\n"
  "v_cvt_pk_bf16_f32 v10, v12, v13\n"
  "v_cvt_pk_bf16_f32 v11, v14, v15\n"
  "v_mfma_f32_16x16x32_bf16 v[24:27], a[164:167], v[52:55], v[24:27]\n"
  "ds_write_b128 v80, v[8:11] offset:1024\n"
  "v_pk_mul_f32 v[16:17], v[16:17], s[60:61] op_sel_hi:[1,0]\n"
  "v_pk_mul_f32 v[18:19], v[18:19], s[60:61] op_sel_hi:[1,0]\n"
  "v_pk_mul_f32 v[20:21], v[20:21], s[60:61] op_sel_hi:[1,0]\n"
  "v_pk_mul_f32 v[22:23], v[22:23], s[60:61] op_sel_hi:[1,0]\n"
  "v_mfma_f32_16x16x32_bf16 v[28:31], a[188:191], v[52:55], v[92:95]\n"
  "v_exp_f32 v16, v16\n"
  "v_mfma_f32_16x16x32_bf16 v[28:31], a[184:187], v[48:51], v[28:31]\n"
  "v_exp_f32 v17, v17\n"
  "v_mfma_f32_16x16x32_bf16 v[28:31], a[180:183], v[44:47], v[28:31]\n"
  "v_exp_f32 v18, v18\n"
  "v_mfma_f32_16x16x32_bf16 v[28:31], a[176:179], v[40:43], v[28:31]\n"
  "v_exp_f32 v19, v19\n"
  "v_mfma_f32_16x16x32_bf16 v[28:31], a[172:175], v[36:39], v[28:31]\n"
  "v_cvt_pk_bf16_f32 v16, v16, v17\n"
  "v_cvt_pk_bf16_f32 v17, v18, v19\n"
  "v_mfma_f32_16x16x32_bf16 v[28:31], a[168:171], v[32:35], v[28:31]\n"
  "ds_read_b128 v[64:67], v81 offset:2048\n"
  "ds_read_b128 v[68:71], v82 offset:2048\n"
  "v_mfma_f32_16x16x32_bf16 v[0:3], a[192:195], v[32:35], v[92:95]\n"
  "v_exp_f32 v20, v20\n"
  "v_mfma_f32_16x16x32_bf16 v[0:3], a[196:199], v[36:39], v[0:3]\n"
  "v_exp_f32 v21, v21\n"
  "v_mfma_f32_16x16x32_bf16 v[0:3], a[200:203], v[40:43], v[0:3]\n"
  "v_exp_f32 v22, v22\n"
  "v_mfma_f32_16x16x32_bf16 v[0:3], a[204:207], v[44:47], v[0:3]\n"
  "v_exp_f32 v23, v23\n"
  "v_mfma_f32_16x16x32_bf16 v[0:3], a[208:211], v[48:51], v[0:3]\n"
  "v_cvt_pk_bf16_f32 v18, v20, v21\n"
  "v_cvt_pk_bf16_f32 v19, v22, v23\n"
  "v_mfma_f32_16x16x32_bf16 v[0:3], a[212:215], v[52:55], v[0:3]\n"
  "ds_write_b128 v80, v[16:19] offset:2048\n"
  "v_pk_mul_f32 v[24:25], v[24:25], s[60:61] op_sel_hi:[1,0]\n"
  "v_pk_mul_f32 v[26:27], v[26:27], s[60:61] op_sel_hi:[1,0]\n"
  "v_pk_mul_f32 v[28:29], v[28:29], s[60:61] op_sel_hi:[1,0]\n"
  "v_pk_mul_f32 v[30:31], v[30:31], s[60:61] op_sel_hi:[1,0]\n"
  "v_mfma_f32_16x16x32_bf16 v[4:7], a[236:239], v[52:55], v[92:95]\n"
  "v_exp_f32 v24, v24\n"
  "v_mfma_f32_16x16x32_bf16 v[4:7], a[232:235], v[48:51], v[4:7]\n"
  "v_exp_f32 v25, v25\n"
  "v_mfma_f32_16x16x32_bf16 v[4:7], a[228:231], v[44:47], v[4:7]\n"
  "v_exp_f32 v26, v26\n"
  "v_mfma_f32_16x16x32_bf16 v[4:7], a[224:227], v[40:43], v[4:7]\n"
  "v_exp_f32 v27, v27\n"
  "v_mfma_f32_16x16x32_bf16 v[4:7], a[220:223], v[36:39], v[4:7]\n"
  "v_cvt_pk_bf16_f32 v24, v24, v25\n"
  "v_cvt_pk_bf16_f32 v25, v26, v27\n"
  "v_mfma_f32_16x16x32_bf16 v[4:7], a[216:219], v[32:35], v[4:7]\n"
  "ds_read_b128 v[72:75], v81 offset:4096\n"
  "ds_read_b128 v[76:79], v82 offset:4096\n"
  "v_mfma_f32_16x16x32_bf16 v[8:11], a[240:243], v[32:35], v[92:95]\n"
  "v_exp_f32 v28, v28\n"
  "v_mfma_f32_16x16x32_bf16 v[8:11], a[244:247], v[36:39], v[8:11]\n"
  "v_exp_f32 v29, v29\n"
  "v_mfma_f32_16x16x32_bf16 v[8:11], a[248:251], v[40:43], v[8:11]\n"
  "v_exp_f32 v30, v30\n"
  "v_mfma_f32_16x16x32_bf16 v[8:11], a[252:255], v[44:47], v[8:11]\n"
  "v_exp_f32 v31, v31\n"
  "v_mfma_f32_16x16x32_bf16 v[8:11], v[128:131], v[48:51], v[8:11]\n"
  "v_cvt_pk_bf16_f32 v26, v28, v29\n"
  "v_cvt_pk_bf16_f32 v27, v30, v31\n"
  "v_mfma_f32_16x16x32_bf16 v[8:11], v[132:135], v[52:55], v[8:11]\n"
  "ds_write_b128 v80, v[24:27] offset:3072\n"
  "v_pk_mul_f32 v[0:1], v[0:1], s[60:61] op_sel_hi:[1,0]\n"
  "v_pk_mul_f32 v[2:3], v[2:3], s[60:61] op_sel_hi:[1,0]\n"
  "v_pk_mul_f32 v[4:5], v[4:5], s[60:61] op_sel_hi:[1,0]\n"
  "v_pk_mul_f32 v[6:7], v[6:7], s[60:61] op_sel_hi:[1,0]\n"
  "v_mfma_f32_16x16x32_bf16 v[12:15], v[156:159], v[52:55], v[92:95]\n"
  "v_exp_f32 v0, v0\n"
  "v_mfma_f32_16x16x32_bf16 v[12:15], v[152:155], v[48:51], v[12:15]\n"
  "v_exp_f32 v1, v1\n"
  "v_mfma_f32_16x16x32_bf16 v[12:15], v[148:151], v[44:47], v[12:15]\n"
  "v_exp_f32 v2, v2\n"
  "v_mfma_f32_16x16x32_bf16 v[12:15], v[144:147], v[40:43], v[12:15]\n"
  "v_exp_f32 v3, v3\n"
  "v_mfma_f32_16x16x32_bf16 v[12:15], v[140:143], v[36:39], v[12:15]\n"
  "v_cvt_pk_bf16_f32 v0, v0, v1\n"
  "v_cvt_pk_bf16_f32 v1, v2, v3\n"
  "v_mfma_f32_16x16x32_bf16 v[12:15], v[136:139], v[32:35], v[12:15]\n"
  "v_mul_f32 v8, s60, v8\n"
  "v_mul_f32 v9, s60, v9\n"
  "v_mfma_f32_16x16x32_bf16 v[16:19], v[160:163], v[32:35], v[92:95]\n"
  "v_exp_f32 v4, v4\n"
  "v_mfma_f32_16x16x32_bf16 v[16:19], v[164:167], v[36:39], v[16:19]\n"
  "v_exp_f32 v5, v5\n"
  "v_mfma_f32_16x16x32_bf16 v[16:19], v[168:171], v[40:43], v[16:19]\n"
  "v_exp_f32 v6, v6\n"
  "v_mfma_f32_16x16x32_bf16 v[16:19], v[172:175], v[44:47], v[16:19]\n"
  "v_exp_f32 v7, v7\n"
  "v_mfma_f32_16x16x32_bf16 v[16:19], v[176:179], v[48:51], v[16:19]\n"
  "v_cvt_pk_bf16_f32 v2, v4, v5\n"
  "v_cvt_pk_bf16_f32 v3, v6, v7\n"
  "v_mfma_f32_16x16x32_bf16 v[16:19], v[180:183], v[52:55], v[16:19]\n"
  "ds_write_b128 v80, v[0:3] offset:4096\n"
  "v_pk_mul_f32 v[10:11], v[10:11], s[60:61] op_sel_hi:[1,0]\n"
  "v_pk_mul_f32 v[12:13], v[12:13], s[60:61] op_sel_hi:[1,0]\n"
  "v_pk_mul_f32 v[14:15], v[14:15], s[60:61] op_sel_hi:[1,0]\n"
  "v_mfma_f32_16x16x32_bf16 v[20:23], v[204:207], v[52:55], v[92:95]\n"
  "v_exp_f32 v8, v8\n"
  "v_mfma_f32_16x16x32_bf16 v[20:23], v[200:203], v[48:51], v[20:23]\n"
  "v_exp_f32 v9, v9\n"
  "v_mfma_f32_16x16x32_bf16 v[20:23], v[196:199], v[44:47], v[20:23]\n"
  "v_exp_f32 v10, v10\n"
  "v_mfma_f32_16x16x32_bf16 v[20:23], v[192:195], v[40:43], v[20:23]\n"
  "v_exp_f32 v11, v11\n"
  "v_mfma_f32_16x16x32_bf16 v[20:23], v[188:191], v[36:39], v[20:23]\n"
  "v_cvt_pk_bf16_f32 v8, v8, v9\n"
  "v_cvt_pk_bf16_f32 v9, v10, v11\n"
  "v_mfma_f32_16x16x32_bf16 v[20:23], v[184:187], v[32:35], v[20:23]\n"
  "v_mul_f32 v16, s60, v16\n"
  "v_mul_f32 v17, s60, v17\n"
  "v_mfma_f32_16x16x32_bf16 v[24:27], v[208:211], v[32:35], v[92:95]\n"
  "v_exp_f32 v12, v12\n"
  "v_mfma_f32_16x16x32_bf16 v[24:27], v[212:215], v[36:39], v[24:27]\n"
  "v_exp_f32 v13, v13\n"
  "v_mfma_f32_16x16x32_bf16 v[24:27], v[216:219], v[40:43], v[24:27]\n"
  "v_exp_f32 v14, v14\n"
  "v_mfma_f32_16x16x32_bf16 v[24:27], v[220:223], v[44:47], v[24:27]\n"
  "v_exp_f32 v15, v15\n"
  "v_mfma_f32_16x16x32_bf16 v[24:27], v[224:227], v[48:51], v[24:27]\n"
  "v_cvt_pk_bf16_f32 v10, v12, v13\n"
  "v_cvt_pk_bf16_f32 v11, v14, v15\n"
  "v_mfma_f32_16x16x32_bf16 v[24:27], v[228:231], v[52:55], v[24:27]\n"
  "ds_write_b128 v80, v[8:11] offset:5120\n"
  "v_pk_mul_f32 v[18:19], v[18:19], s[60:61] op_sel_hi:[1,0]\n"
  "v_pk_mul_f32 v[20:21], v[20:21], s[60:61] op_sel_hi:[1,0]\n"
  "v_pk_mul_f32 v[22:23], v[22:23], s[60:61] op_sel_hi:[1,0]\n"
  "v_mfma_f32_16x16x32_bf16 v[28:31], v[252:255], v[52:55], v[92:95]\n"
  "v_exp_f32 v16, v16\n"
  "v_mfma_f32_16x16x32_bf16 v[28:31], v[248:251], v[48:51], v[28:31]\n"
  "v_exp_f32 v17, v17\n"
  "v_mfma_f32_16x16x32_bf16 v[28:31], v[244:247], v[44:47], v[28:31]\n"
  "v_exp_f32 v18, v18\n"
  "v_mfma_f32_16x16x32_bf16 v[28:31], v[240:243], v[40:43], v[28:31]\n"
  "s_sub_u32 s56, s56, 1\n"
  "v_exp_f32 v19, v19\n"
  "v_mfma_f32_16x16x32_bf16 v[28:31], v[236:239], v[36:39], v[28:31]\n"
  "s_cmp_eq_u32 s56, 0\n"
  "v_cvt_pk_bf16_f32 v16, v16, v17\n"
  "v_cvt_pk_bf16_f32 v17, v18, v19\n"
  "v_mfma_f32_16x16x32_bf16 v[28:31], v[232:235], v[32:35], v[28:31]\n"
  "s_waitcnt lgkmcnt(3)\n"
  "s_cbranch_scc0 A_H1_%=\n"
  ".p2align 3\n"
  "A_X1_%=:\n"
  "s_mov_b32 s52, s53\n"
  "s_mov_b32 s53, s54\n"
  "s_mov_b32 s54, s55\n"
  "s_cmp_eq_u32 s52, 0\n"
  "s_cbranch_scc1 A_LAST_%=\n"
  "s_lshr_b32 s62, s52, 16\n"
  "s_lshl1_add_u32 s62, s62, s65\n"
  "s_mul_i32 s57, s62, 98304\n"
  "s_and_b32 s56, s52, 0xff\n"
  "s_cmp_lg_u32 s53, 0\n"
  "s_cselect_b32 s62, 1, 0\n"
  "s_sub_u32 s56, s56, s62\n"
  "s_add_u32 s57, s57, 18432\n"
  "v_mfma_f32_16x16x32_bf16 v[0:3], a[0:3], v[56:59], v[92:95]\n"
  "s_add_u32 s66, s66, 6144\n"
  "s_cmp_eq_u32 s66, 102400\n"
  "s_cselect_b32 s66, 65536, s66\n"
  "v_exp_f32 v20, v20\n"
  "v_mfma_f32_16x16x32_bf16 v[0:3], a[4:7], v[60:63], v[0:3]\n"
  "v_exp_f32 v21, v21\n"
  "v_mfma_f32_16x16x32_bf16 v[0:3], a[8:11], v[64:67], v[0:3]\n"
  "ds_read_b128 a[0:3], v87 offset:0\n"
  "ds_read_b128 a[4:7], v88 offset:0\n"
  "v_exp_f32 v22, v22\n"
  "v_mfma_f32_16x16x32_bf16 v[0:3], a[12:15], v[68:71], v[0:3]\n"
  "v_exp_f32 v23, v23\n"
  "v_mfma_f32_16x16x32_bf16 v[0:3], a[16:19], v[72:75], v[0:3]\n"
  "v_cvt_pk_bf16_f32 v18, v20, v21\n"
  "v_cvt_pk_bf16_f32 v19, v22, v23\n"
  "v_mfma_f32_16x16x32_bf16 v[0:3], a[20:23], v[76:79], v[0:3]\n"
  "ds_read_b128 a[8:11], v87 offset:2048\n"
  "ds_read_b128 a[12:15], v88 offset:2048\n"
  "ds_write_b128 v80, v[16:19] offset:6144\n"
  "v_pk_mul_f32 v[24:25], v[24:25], s[60:61] op_sel_hi:[1,0]\n"
  "v_pk_mul_f32 v[26:27], v[26:27], s[60:61] op_sel_hi:[1,0]\n"
  "v_pk_mul_f32 v[28:29], v[28:29], s[60:61] op_sel_hi:[1,0]\n"
  "v_pk_mul_f32 v[30:31], v[30:31], s[60:61] op_sel_hi:[1,0]\n"
  "v_mfma_f32_16x16x32_bf16 v[4:7], a[44:47], v[76:79], v[92:95]\n"
  "v_exp_f32 v24, v24\n"
  "v_mfma_f32_16x16x32_bf16 v[4:7], a[40:43], v[72:75], v[4:7]\n"
  "s_add_u32 m0, s59, -128\n"
  "v_exp_f32 v25, v25\n"
  "v_mfma_f32_16x16x32_bf16 v[4:7], a[36:39], v[68:71], v[4:7]\n"
  "s_waitcnt lgkmcnt(3)\n"
  "buffer_load_dwordx4 v112, s[40:43], s57 offen offset:128 nt lds\n"
  "buffer_load_dwordx4 v113, s[40:43], s57 offen offset:1152 nt lds\n"
  "v_exp_f32 v26, v26\n"
  "v_mfma_f32_16x16x32_bf16 v[4:7], a[32:35], v[64:67], v[4:7]\n"
  "ds_read_b128 a[16:19], v87 offset:4096\n"
  "ds_read_b128 a[20:23], v88 offset:4096\n"
  "v_exp_f32_e64 v27, v27\n"
  "v_mfma_f32_16x16x32_bf16 v[4:7], a[28:31], v[60:63], v[4:7]\n"
  "s_add_u32 m0, s59, 2048\n"
  "v_cvt_pk_bf16_f32 v24, v24, v25\n"
  "v_cvt_pk_bf16_f32 v25, v26, v27\n"
  "v_mfma_f32_16x16x32_bf16 v[4:7], a[24:27], v[56:59], v[4:7]\n"
  "s_waitcnt lgkmcnt(3)\n"
  "buffer_load_dwordx4 v112, s[40:43], s57 offen offset:0 nt lds\n"
  "buffer_load_dwordx4 v113, s[40:43], s57 offen offset:1024 nt lds\n"
  "s_add_u32 s57, s57, 6144\n"
  "v_add_u32 v81, s66, v83\n"
  "v_add_u32 v82, s66, v84\n"
  "v_mfma_f32_16x16x32_bf16 v[8:11], a[48:51], v[56:59], v[92:95]\n"
  "ds_read_b128 a[40:43], v87 offset:6144\n"
  "ds_read_b128 a[44:47], v88 offset:6144\n"
  "v_exp_f32 v28, v28\n"
  "v_mfma_f32_16x16x32_bf16 v[8:11], a[52:55], v[60:63], v[8:11]\n"
  "s_add_u32 m0, s59, 4096\n"
  "v_exp_f32 v29, v29\n"
  "v_mfma_f32_16x16x32_bf16 v[8:11], a[56:59], v[64:67], v[8:11]\n"
  "s_waitcnt lgkmcnt(2)\n"
  "buffer_load_dwordx4 v112, s[40:43], s57 offen offset:0 nt lds\n"
  "buffer_load_dwordx4 v113, s[40:43], s57 offen offset:1024 nt lds\n"
  "v_exp_f32 v30, v30\n"
  "v_mfma_f32_16x16x32_bf16 v[8:11], a[60:63], v[68:71], v[8:11]\n"
  "ds_read_b128 a[32:35], v87 offset:8192\n"
  "ds_read_b128 a[36:39], v88 offset:8192\n"
  "v_exp_f32 v31, v31\n"
  "v_mfma_f32_16x16x32_bf16 v[8:11], a[64:67], v[72:75], v[8:11]\n"
  "v_cvt_pk_bf16_f32 v26, v28, v29\n"
  "v_cvt_pk_bf16_f32 v27, v30, v31\n"
  "v_mfma_f32_16x16x32_bf16 v[8:11], a[68:71], v[76:79], v[8:11]\n"
  "s_barrier\n"
  "s_add_u32 m0, s59, 6016\n"
  "ds_write_b128 v80, v[24:27] offset:7168\n"
  "v_pk_mul_f32 v[0:1], v[0:1], s[60:61] op_sel_hi:[1,0]\n"
  "v_pk_mul_f32 v[2:3], v[2:3], s[60:61] op_sel_hi:[1,0]\n"
  "v_pk_mul_f32 v[4:5], v[4:5], s[60:61] op_sel_hi:[1,0]\n"
  "v_pk_mul_f32 v[6:7], v[6:7], s[60:61] op_sel_hi:[1,0]\n"
  "v_mfma_f32_16x16x32_bf16 v[12:15], a[92:95], v[76:79], v[92:95]\n"
  "s_waitcnt lgkmcnt(3)\n"
  "buffer_load_dwordx4 v112, s[40:43], s57 offen offset:128 nt lds\n"
  "buffer_load_dwordx4 v113, s[40:43], s57 offen offset:1152 nt lds\n"
  "v_exp_f32 v0, v0\n"
  "v_mfma_f32_16x16x32_bf16 v[12:15], a[88:91], v[72:75], v[12:15]\n"
  "ds_read_b128 a[24:27], v87 offset:10240\n"
  "ds_read_b128 a[28:31], v88 offset:10240\n"
  "v_exp_f32 v1, v1\n"
  "v_mfma_f32_16x16x32_bf16 v[12:15], a[84:87], v[68:71], v[12:15]\n"
  "s_add_u32 m0, s59, 7936\n"
  "v_exp_f32 v2, v2\n"
  "v_mfma_f32_16x16x32_bf16 v[12:15], a[80:83], v[64:67], v[12:15]\n"
  "s_waitcnt lgkmcnt(3)\n"
  "buffer_load_dwordx4 v112, s[40:43], s57 offen offset:256 nt lds\n"
  "buffer_load_dwordx4 v113, s[40:43], s57 offen offset:1280 nt lds\n"
  "s_add_u32 s57, s57, 6144\n"
  "v_exp_f32_e64 v3, v3\n"
  "v_mfma_f32_16x16x32_bf16 v[12:15], a[76:79], v[60:63], v[12:15]\n"
  "ds_read_b128 a[48:51], v87 offset:12288\n"
  "ds_read_b128 a[52:55], v88 offset:12288\n"
  "v_cvt_pk_bf16_f32 v0, v0, v1\n"
  "v_cvt_pk_bf16_f32 v1, v2, v3\n"
  "v_mfma_f32_16x16x32_bf16 v[12:15], a[72:75], v[56:59], v[12:15]\n"
  "v_xor_b32 v80, s88, v80\n"
  "v_mfma_f32_16x16x32_bf16 v[16:19], a[96:99], v[56:59], v[92:95]\n"
  "s_add_u32 m0, s59, 9984\n"
  "v_exp_f32 v4, v4\n"
  "v_mfma_f32_16x16x32_bf16 v[16:19], a[100:103], v[60:63], v[16:19]\n"
  "s_xor_b32 s89, s88, s89\n"
  "s_waitcnt lgkmcnt(2)\n"
  "buffer_load_dwordx4 v112, s[40:43], s57 offen offset:256 nt lds\n"
  "buffer_load_dwordx4 v113, s[40:43], s57 offen offset:1280 nt lds\n"
  "v_exp_f32 v5, v5\n"
  "v_mfma_f32_16x16x32_bf16 v[16:19], a[104:107], v[64:67], v[16:19]\n"
  "s_xor_b32 s88, s88, s89\n"
  "ds_read_b128 a[56:59], v87 offset:14336\n"
  "ds_read_b128 a[60:63], v88 offset:14336\n"
  "s_waitcnt vmcnt(10)\n"
  "ds_read_b128 a[80:83], v87 offset:0\n"
  "ds_read_b128 a[84:87], v88 offset:0\n"
  "v_exp_f32 v6, v6\n"
  "v_mfma_f32_16x16x32_bf16 v[16:19], a[108:111], v[68:71], v[16:19]\n"
  "s_add_u32 m0, s59, 12160\n"
  "v_exp_f32 v7, v7\n"
  "v_mfma_f32_16x16x32_bf16 v[16:19], a[112:115], v[72:75], v[16:19]\n"
  "s_waitcnt lgkmcnt(4)\n"
  "buffer_load_dwordx4 v112, s[40:43], s57 offen offset:128 nt lds\n"
  "buffer_load_dwordx4 v113, s[40:43], s57 offen offset:1152 nt lds\n"
  "v_cvt_pk_bf16_f32 v2, v4, v5\n"
  "v_cvt_pk_bf16_f32 v3, v6, v7\n"
  "v_mfma_f32_16x16x32_bf16 v[16:19], a[116:119], v[76:79], v[16:19]\n"
  "ds_read_b128 a[64:67], v87 offset:16384\n"
  "ds_read_b128 a[68:71], v88 offset:16384\n"
  "s_waitcnt vmcnt(10)\n"
  "ds_read_b128 a[72:75], v87 offset:2048\n"
  "ds_read_b128 a[76:79], v88 offset:2048\n"
  "ds_write_b128 v80, v[0:3] offset:0\n"
  "v_pk_mul_f32 v[8:9], v[8:9], s[60:61] op_sel_hi:[1,0]\n"
  "v_pk_mul_f32 v[10:11], v[10:11], s[60:61] op_sel_hi:[1,0]\n"
  "v_pk_mul_f32 v[12:13], v[12:13], s[60:61] op_sel_hi:[1,0]\n"
  "v_pk_mul_f32 v[14:15], v[14:15], s[60:61] op_sel_hi:[1,0]\n"
  "v_mfma_f32_16x16x32_bf16 v[20:23], a[140:143], v[76:79], v[92:95]\n"
  "s_add_u32 m0, s59, 14336\n"
  "v_exp_f32 v8, v8\n"
  "v_mfma_f32_16x16x32_bf16 v[20:23], a[136:139], v[72:75], v[20:23]\n"
  "s_waitcnt lgkmcnt(7)\n"
  "buffer_load_dwordx4 v112, s[40:43], s57 offen offset:0 nt lds\n"
  "buffer_load_dwordx4 v113, s[40:43], s57 offen offset:1024 nt lds\n"
  "s_add_u32 s57, s57, 6144\n"
  "v_exp_f32 v9, v9\n"
  "v_mfma_f32_16x16x32_bf16 v[20:23], a[132:135], v[68:71], v[20:23]\n"
  "ds_read_b128 a[88:91], v87 offset:18432\n"
  "ds_read_b128 a[92:95], v88 offset:18432\n"
  "s_waitcnt vmcnt(10)\n"
  "ds_read_b128 a[96:99], v87 offset:4096\n"
  "ds_read_b128 a[100:103], v88 offset:4096\n"
  "v_exp_f32 v10, v10\n"
  "v_mfma_f32_16x16x32_bf16 v[20:23], a[128:131], v[64:67], v[20:23]\n"
  "v_exp_f32_e64 v11, v11\n"
  "v_mfma_f32_16x16x32_bf16 v[20:23], a[124:127], v[60:63], v[20:23]\n"
  "s_add_u32 m0, s59, 16384\n"
  "v_cvt_pk_bf16_f32 v8, v8, v9\n"
  "v_cvt_pk_bf16_f32 v9, v10, v11\n"
  "v_mfma_f32_16x16x32_bf16 v[20:23], a[120:123], v[56:59], v[20:23]\n"
  "s_waitcnt lgkmcnt(7)\n"
  "buffer_load_dwordx4 v112, s[40:43], s57 offen offset:0 nt lds\n"
  "buffer_load_dwordx4 v113, s[40:43], s57 offen offset:1024 nt lds\n"
  "ds_read_b128 v[32:35], v81 offset:0\n"
  "ds_read_b128 v[36:39], v82 offset:0\n"
  "v_mfma_f32_16x16x32_bf16 v[24:27], a[144:147], v[56:59], v[92:95]\n"
  "s_waitcnt vmcnt(10)\n"
  "ds_read_b128 a[104:107], v87 offset:6144\n"
  "ds_read_b128 a[108:111], v88 offset:6144\n"
  "v_exp_f32 v12, v12\n"
  "v_mfma_f32_16x16x32_bf16 v[24:27], a[148:151], v[60:63], v[24:27]\n"
  "s_add_u32 m0, s59, 18304\n"
  "v_exp_f32 v13, v13\n"
  "v_mfma_f32_16x16x32_bf16 v[24:27], a[152:155], v[64:67], v[24:27]\n"
  "s_waitcnt lgkmcnt(6)\n"
  "buffer_load_dwordx4 v112, s[40:43], s57 offen offset:128 nt lds\n"
  "buffer_load_dwordx4 v113, s[40:43], s57 offen offset:1152 nt lds\n"
  "v_exp_f32 v14, v14\n"
  "v_mfma_f32_16x16x32_bf16 v[24:27], a[156:159], v[68:71], v[24:27]\n"
  "s_waitcnt vmcnt(10)\n"
  "ds_read_b128 a[112:115], v87 offset:8192\n"
  "ds_read_b128 a[116:119], v88 offset:8192\n"
  "v_exp_f32 v15, v15\n"
  "v_mfma_f32_16x16x32_bf16 v[24:27], a[160:163], v[72:75], v[24:27]\n"
  "v_cvt_pk_bf16_f32 v10, v12, v13\n"
  "v_cvt_pk_bf16_f32 v11, v14, v15\n"
  "v_mfma_f32_16x16x32_bf16 v[24:27], a[164:167], v[76:79], v[24:27]\n"
  "s_add_u32 m0, s59, -256\n"
  "ds_write_b128 v80, v[8:11] offset:1024\n"
  "v_pk_mul_f32 v[16:17], v[16:17], s[60:61] op_sel_hi:[1,0]\n"
  "v_pk_mul_f32 v[18:19], v[18:19], s[60:61] op_sel_hi:[1,0]\n"
  "v_pk_mul_f32 v[20:21], v[20:21], s[60:61] op_sel_hi:[1,0]\n"
  "v_pk_mul_f32 v[22:23], v[22:23], s[60:61] op_sel_hi:[1,0]\n"
  "v_mfma_f32_16x16x32_bf16 v[28:31], a[188:191], v[76:79], v[92:95]\n"
  "buffer_load_dwordx4 v112, s[40:43], s57 offen offset:256 nt lds\n"
  "buffer_load_dwordx4 v113, s[40:43], s57 offen offset:1280 nt lds\n"
  "s_add_u32 s57, s57, 6144\n"
  "v_exp_f32 v16, v16\n"
  "v_mfma_f32_16x16x32_bf16 v[28:31], a[184:187], v[72:75], v[28:31]\n"
  "s_waitcnt vmcnt(10)\n"
  "ds_read_b128 a[136:139], v87 offset:10240\n"
  "ds_read_b128 a[140:143], v88 offset:10240\n"
  "v_exp_f32 v17, v17\n"
  "v_mfma_f32_16x16x32_bf16 v[28:31], a[180:183], v[68:71], v[28:31]\n"
  "s_add_u32 m0, s59, 1792\n"
  "v_exp_f32 v18, v18\n"
  "v_mfma_f32_16x16x32_bf16 v[28:31], a[176:179], v[64:67], v[28:31]\n"
  "buffer_load_dwordx4 v112, s[40:43], s57 offen offset:256 nt lds\n"
  "buffer_load_dwordx4 v113, s[40:43], s57 offen offset:1280 nt lds\n"
  "v_exp_f32 v19, v19\n"
  "v_mfma_f32_16x16x32_bf16 v[28:31], a[172:175], v[60:63], v[28:31]\n"
  "s_waitcnt vmcnt(10)\n"
  "ds_read_b128 a[128:131], v87 offset:12288\n"
  "ds_read_b128 a[132:135], v88 offset:12288\n"
  "v_cvt_pk_bf16_f32 v16, v16, v17\n"
  "v_cvt_pk_bf16_f32 v17, v18, v19\n"
  "v_mfma_f32_16x16x32_bf16 v[28:31], a[168:171], v[56:59], v[28:31]\n"
  "s_add_u32 m0, s59, 3968\n"
  "ds_read_b128 v[40:43], v81 offset:2048\n"
  "ds_read_b128 v[44:47], v82 offset:2048\n"
  "v_mfma_f32_16x16x32_bf16 v[0:3], a[192:195], v[56:59], v[92:95]\n"
  "s_waitcnt lgkmcnt(13)\n"
  "buffer_load_dwordx4 v112, s[40:43], s57 offen offset:128 nt lds\n"
  "buffer_load_dwordx4 v113, s[40:43], s57 offen offset:1152 nt lds\n"
  "v_exp_f32 v20, v20\n"
  "v_mfma_f32_16x16x32_bf16 v[0:3], a[196:199], v[60:63], v[0:3]\n"
  "s_waitcnt vmcnt(10)\n"
  "ds_read_b128 a[120:123], v87 offset:14336\n"
  "ds_read_b128 a[124:127], v88 offset:14336\n"
  "v_exp_f32 v21, v21\n"
  "v_mfma_f32_16x16x32_bf16 v[0:3], a[200:203], v[64:67], v[0:3]\n"
  "v_exp_f32 v22, v22\n"
  "v_mfma_f32_16x16x32_bf16 v[0:3], a[204:207], v[68:71], v[0:3]\n"
  "s_add_u32 m0, s59, 6144\n"
  "v_exp_f32_e64 v23, v23\n"
  "v_mfma_f32_16x16x32_bf16 v[0:3], a[208:211], v[72:75], v[0:3]\n"
  "s_waitcnt lgkmcnt(11)\n"
  "buffer_load_dwordx4 v112, s[40:43], s57 offen offset:0 nt lds\n"
  "buffer_load_dwordx4 v113, s[40:43], s57 offen offset:1024 nt lds\n"
  "s_add_u32 s57, s57, 6144\n"
  "v_cvt_pk_bf16_f32 v18, v20, v21\n"
  "v_cvt_pk_bf16_f32 v19, v22, v23\n"
  "v_mfma_f32_16x16x32_bf16 v[0:3], a[212:215], v[76:79], v[0:3]\n"
  "s_waitcnt vmcnt(10)\n"
  "ds_read_b128 a[144:147], v87 offset:16384\n"
  "ds_read_b128 a[148:151], v88 offset:16384\n"
  "ds_write_b128 v80, v[16:19] offset:2048\n"
  "v_pk_mul_f32 v[24:25], v[24:25], s[60:61] op_sel_hi:[1,0]\n"
  "v_pk_mul_f32 v[26:27], v[26:27], s[60:61] op_sel_hi:[1,0]\n"
  "v_pk_mul_f32 v[28:29], v[28:29], s[60:61] op_sel_hi:[1,0]\n"
  "v_pk_mul_f32 v[30:31], v[30:31], s[60:61] op_sel_hi:[1,0]\n"
  "v_mfma_f32_16x16x32_bf16 v[4:7], a[236:239], v[76:79], v[92:95]\n"
  "s_add_u32 m0, s59, 8192\n"
  "v_exp_f32 v24, v24\n"
  "v_mfma_f32_16x16x32_bf16 v[4:7], a[232:235], v[72:75], v[4:7]\n"
  "s_waitcnt lgkmcnt(12)\n"
  "buffer_load_dwordx4 v112, s[40:43], s57 offen offset:0 nt lds\n"
  "buffer_load_dwordx4 v113, s[40:43], s57 offen offset:1024 nt lds\n"
  "v_exp_f32 v25, v25\n"
  "v_mfma_f32_16x16x32_bf16 v[4:7], a[228:231], v[68:71], v[4:7]\n"
  "s_waitcnt vmcnt(10)\n"
  "ds_read_b128 a[152:155], v87 offset:18432\n"
  "ds_read_b128 a[156:159], v88 offset:18432\n"
  "v_exp_f32 v26, v26\n"
  "v_mfma_f32_16x16x32_bf16 v[4:7], a[224:227], v[64:67], v[4:7]\n"
  "v_exp_f32_e64 v27, v27\n"
  "v_mfma_f32_16x16x32_bf16 v[4:7], a[220:223], v[60:63], v[4:7]\n"
  "s_add_u32 m0, s59, 10112\n"
  "v_cvt_pk_bf16_f32 v24, v24, v25\n"
  "v_cvt_pk_bf16_f32 v25, v26, v27\n"
  "v_mfma_f32_16x16x32_bf16 v[4:7], a[216:219], v[56:59], v[4:7]\n"
  "s_waitcnt lgkmcnt(11)\n"
  "buffer_load_dwordx4 v112, s[40:43], s57 offen offset:128 nt lds\n"
  "buffer_load_dwordx4 v113, s[40:43], s57 offen offset:1152 nt lds\n"
  "ds_read_b128 v[48:51], v81 offset:4096\n"
  "ds_read_b128 v[52:55], v82 offset:4096\n"
  "v_mfma_f32_16x16x32_bf16 v[8:11], a[240:243], v[56:59], v[92:95]\n"
  "s_waitcnt vmcnt(10)\n"
  "ds_read_b128 a[160:163], v87 offset:0\n"
  "ds_read_b128 a[164:167], v88 offset:0\n"
  "v_exp_f32 v28, v28\n"
  "v_mfma_f32_16x16x32_bf16 v[8:11], a[244:247], v[60:63], v[8:11]\n"
  "s_add_u32 m0, s59, 12032\n"
  "v_exp_f32 v29, v29\n"
  "v_mfma_f32_16x16x32_bf16 v[8:11], a[248:251], v[64:67], v[8:11]\n"
  "s_waitcnt lgkmcnt(13)\n"
  "buffer_load_dwordx4 v112, s[40:43], s57 offen offset:256 nt lds\n"
  "buffer_load_dwordx4 v113, s[40:43], s57 offen offset:1280 nt lds\n"
  "s_add_u32 s57, s57, 6144\n"
  "v_exp_f32 v30, v30\n"
  "v_mfma_f32_16x16x32_bf16 v[8:11], a[252:255], v[68:71], v[8:11]\n"
  "s_waitcnt vmcnt(10)\n"
  "ds_read_b128 a[184:187], v87 offset:2048\n"
  "ds_read_b128 a[188:191], v88 offset:2048\n"
  "v_exp_f32 v31, v31\n"
  "v_mfma_f32_16x16x32_bf16 v[8:11], v[128:131], v[72:75], v[8:11]\n"
  "s_add_u32 m0, s59, 14080\n"
  "v_cvt_pk_bf16_f32 v26, v28, v29\n"
  "v_cvt_pk_bf16_f32 v27, v30, v31\n"
  "v_mfma_f32_16x16x32_bf16 v[8:11], v[132:135], v[76:79], v[8:11]\n"
  "s_waitcnt lgkmcnt(11)\n"
  "buffer_load_dwordx4 v112, s[40:43], s57 offen offset:256 nt lds\n"
  "buffer_load_dwordx4 v113, s[40:43], s57 offen offset:1280 nt lds\n"
  "ds_write_b128 v80, v[24:27] offset:3072\n"
  "v_pk_mul_f32 v[0:1], v[0:1], s[60:61] op_sel_hi:[1,0]\n"
  "v_pk_mul_f32 v[2:3], v[2:3], s[60:61] op_sel_hi:[1,0]\n"
  "v_pk_mul_f32 v[4:5], v[4:5], s[60:61] op_sel_hi:[1,0]\n"
  "v_pk_mul_f32 v[6:7], v[6:7], s[60:61] op_sel_hi:[1,0]\n"
  "v_mfma_f32_16x16x32_bf16 v[12:15], v[156:159], v[76:79], v[92:95]\n"
  "s_waitcnt vmcnt(10)\n"
  "ds_read_b128 a[176:179], v87 offset:4096\n"
  "ds_read_b128 a[180:183], v88 offset:4096\n"
  "v_exp_f32 v0, v0\n"
  "v_mfma_f32_16x16x32_bf16 v[12:15], v[152:155], v[72:75], v[12:15]\n"
  "v_exp_f32 v1, v1\n"
  "v_mfma_f32_16x16x32_bf16 v[12:15], v[148:151], v[68:71], v[12:15]\n"
  "s_add_u32 m0, s59, 16256\n"
  "v_exp_f32 v2, v2\n"
  "v_mfma_f32_16x16x32_bf16 v[12:15], v[144:147], v[64:67], v[12:15]\n"
  "s_waitcnt lgkmcnt(12)\n"
  "buffer_load_dwordx4 v112, s[40:43], s57 offen offset:128 nt lds\n"
  "buffer_load_dwordx4 v113, s[40:43], s57 offen offset:1152 nt lds\n"
  "v_exp_f32 v3, v3\n"
  "v_mfma_f32_16x16x32_bf16 v[12:15], v[140:143], v[60:63], v[12:15]\n"
  "s_waitcnt vmcnt(10)\n"
  "ds_read_b128 a[168:171], v87 offset:6144\n"
  "ds_read_b128 a[172:175], v88 offset:6144\n"
  "v_cvt_pk_bf16_f32 v0, v0, v1\n"
  "v_cvt_pk_bf16_f32 v1, v2, v3\n"
  "v_mfma_f32_16x16x32_bf16 v[12:15], v[136:139], v[56:59], v[12:15]\n"
  "s_add_u32 m0, s59, 18432\n"
  "v_mul_f32 v8, s60, v8\n"
  "v_mul_f32 v9, s60, v9\n"
  "v_mfma_f32_16x16x32_bf16 v[16:19], v[160:163], v[56:59], v[92:95]\n"
  "s_waitcnt lgkmcnt(11)\n"
  "buffer_load_dwordx4 v112, s[40:43], s57 offen offset:0 nt lds\n"
  "buffer_load_dwordx4 v113, s[40:43], s57 offen offset:1024 nt lds\n"
  "s_add_u32 s57, s57, 6144\n"
  "v_exp_f32 v4, v4\n"
  "v_mfma_f32_16x16x32_bf16 v[16:19], v[164:167], v[60:63], v[16:19]\n"
  "s_waitcnt vmcnt(10)\n"
  "ds_read_b128 a[192:195], v87 offset:8192\n"
  "ds_read_b128 a[196:199], v88 offset:8192\n"
  "v_exp_f32 v5, v5\n"
  "v_mfma_f32_16x16x32_bf16 v[16:19], v[168:171], v[64:67], v[16:19]\n"
  "v_exp_f32 v6, v6\n"
  "v_mfma_f32_16x16x32_bf16 v[16:19], v[172:175], v[68:71], v[16:19]\n"
  "s_add_u32 m0, s59, 0\n"
  "v_exp_f32 v7, v7\n"
  "v_mfma_f32_16x16x32_bf16 v[16:19], v[176:179], v[72:75], v[16:19]\n"
  "s_waitcnt lgkmcnt(9)\n"
  "buffer_load_dwordx4 v112, s[40:43], s57 offen offset:0 nt lds\n"
  "buffer_load_dwordx4 v113, s[40:43], s57 offen offset:1024 nt lds\n"
  "v_cvt_pk_bf16_f32 v2, v4, v5\n"
  "v_cvt_pk_bf16_f32 v3, v6, v7\n"
  "v_mfma_f32_16x16x32_bf16 v[16:19], v[180:183], v[76:79], v[16:19]\n"
  "s_waitcnt vmcnt(10)\n"
  "ds_read_b128 a[200:203], v87 offset:10240\n"
  "ds_read_b128 a[204:207], v88 offset:10240\n"
  "ds_write_b128 v80, v[0:3] offset:4096\n"
  "v_pk_mul_f32 v[10:11], v[10:11], s[60:61] op_sel_hi:[1,0]\n"
  "v_pk_mul_f32 v[12:13], v[12:13], s[60:61] op_sel_hi:[1,0]\n"
  "v_pk_mul_f32 v[14:15], v[14:15], s[60:61] op_sel_hi:[1,0]\n"
  "v_mfma_f32_16x16x32_bf16 v[20:23], v[204:207], v[76:79], v[92:95]\n"
  "s_add_u32 m0, s59, 1920\n"
  "v_exp_f32 v8, v8\n"
  "v_mfma_f32_16x16x32_bf16 v[20:23], v[200:203], v[72:75], v[20:23]\n"
  "s_waitcnt lgkmcnt(10)\n"
  "buffer_load_dwordx4 v112, s[40:43], s57 offen offset:128 nt lds\n"
  "buffer_load_dwordx4 v113, s[40:43], s57 offen offset:1152 nt lds\n"
  "v_exp_f32 v9, v9\n"
  "v_mfma_f32_16x16x32_bf16 v[20:23], v[196:199], v[68:71], v[20:23]\n"
  "s_waitcnt vmcnt(10)\n"
  "ds_read_b128 a[208:211], v87 offset:12288\n"
  "ds_read_b128 a[212:215], v88 offset:12288\n"
  "v_exp_f32 v10, v10\n"
  "v_mfma_f32_16x16x32_bf16 v[20:23], v[192:195], v[64:67], v[20:23]\n"
  "s_add_u32 m0, s59, 3840\n"
  "v_exp_f32 v11, v11\n"
  "v_mfma_f32_16x16x32_bf16 v[20:23], v[188:191], v[60:63], v[20:23]\n"
  "s_waitcnt lgkmcnt(9)\n"
  "buffer_load_dwordx4 v112, s[40:43], s57 offen offset:256 nt lds\n"
  "buffer_load_dwordx4 v113, s[40:43], s57 offen offset:1280 nt lds\n"
  "s_add_u32 s57, s57, 6144\n"
  "v_cvt_pk_bf16_f32 v8, v8, v9\n"
  "v_cvt_pk_bf16_f32 v9, v10, v11\n"
  "v_mfma_f32_16x16x32_bf16 v[20:23], v[184:187], v[56:59], v[20:23]\n"
  "s_waitcnt vmcnt(10)\n"
  "ds_read_b128 a[232:235], v87 offset:14336\n"
  "ds_read_b128 a[236:239], v88 offset:14336\n"
  "v_mul_f32 v16, s60, v16\n"
  "v_mul_f32 v17, s60, v17\n"
  "v_mfma_f32_16x16x32_bf16 v[24:27], v[208:211], v[56:59], v[92:95]\n"
  "v_exp_f32 v12, v12\n"
  "v_mfma_f32_16x16x32_bf16 v[24:27], v[212:215], v[60:63], v[24:27]\n"
  "s_add_u32 m0, s59, 5888\n"
  "v_exp_f32 v13, v13\n"
  "v_mfma_f32_16x16x32_bf16 v[24:27], v[216:219], v[64:67], v[24:27]\n"
  "s_waitcnt lgkmcnt(9)\n"
  "buffer_load_dwordx4 v112, s[40:43], s57 offen offset:256 nt lds\n"
  "buffer_load_dwordx4 v113, s[40:43], s57 offen offset:1280 nt lds\n"
  "v_exp_f32 v14, v14\n"
  "v_mfma_f32_16x16x32_bf16 v[24:27], v[220:223], v[68:71], v[24:27]\n"
  "s_waitcnt vmcnt(10)\n"
  "ds_read_b128 a[224:227], v87 offset:16384\n"
  "ds_read_b128 a[228:231], v88 offset:16384\n"
  "v_exp_f32_e64 v15, v15\n"
  "v_mfma_f32_16x16x32_bf16 v[24:27], v[224:227], v[72:75], v[24:27]\n"
  "s_add_u32 m0, s59, 8064\n"
  "v_cvt_pk_bf16_f32 v10, v12, v13\n"
  "v_cvt_pk_bf16_f32 v11, v14, v15\n"
  "v_mfma_f32_16x16x32_bf16 v[24:27], v[228:231], v[76:79], v[24:27]\n"
  "s_barrier\n"
  "s_waitcnt lgkmcnt(9)\n"
  "buffer_load_dwordx4 v112, s[40:43], s57 offen offset:128 nt lds\n"
  "buffer_load_dwordx4 v113, s[40:43], s57 offen offset:1152 nt lds\n"
  "ds_write_b128 v80, v[8:11] offset:5120\n"
  "v_pk_mul_f32 v[18:19], v[18:19], s[60:61] op_sel_hi:[1,0]\n"
  "v_pk_mul_f32 v[20:21], v[20:21], s[60:61] op_sel_hi:[1,0]\n"
  "v_pk_mul_f32 v[22:23], v[22:23], s[60:61] op_sel_hi:[1,0]\n"
  "v_mfma_f32_16x16x32_bf16 v[28:31], v[252:255], v[76:79], v[92:95]\n"
  "s_waitcnt vmcnt(10)\n"
  "ds_read_b128 a[216:219], v87 offset:18432\n"
  "ds_read_b128 a[220:223], v88 offset:18432\n"
  "v_exp_f32 v16, v16\n"
  "v_mfma_f32_16x16x32_bf16 v[28:31], v[248:251], v[72:75], v[28:31]\n"
  "v_exp_f32 v17, v17\n"
  "v_mfma_f32_16x16x32_bf16 v[28:31], v[244:247], v[68:71], v[28:31]\n"
  "s_add_u32 m0, s59, 10240\n"
  "v_exp_f32 v18, v18\n"
  "v_mfma_f32_16x16x32_bf16 v[28:31], v[240:243], v[64:67], v[28:31]\n"
  "s_waitcnt lgkmcnt(10)\n"
  "buffer_load_dwordx4 v112, s[40:43], s57 offen offset:0 nt lds\n"
  "buffer_load_dwordx4 v113, s[40:43], s57 offen offset:1024 nt lds\n"
  "s_add_u32 s57, s57, 6144\n"
  "v_exp_f32_e64 v19, v19\n"
  "v_mfma_f32_16x16x32_bf16 v[28:31], v[236:239], v[60:63], v[28:31]\n"
  "s_waitcnt vmcnt(10)\n"
  "ds_read_b128 a[240:243], v87 offset:0\n"
  "ds_read_b128 a[244:247], v88 offset:0\n"
  "v_cvt_pk_bf16_f32 v16, v16, v17\n"
  "v_cvt_pk_bf16_f32 v17, v18, v19\n"
  "v_mfma_f32_16x16x32_bf16 v[28:31], v[232:235], v[56:59], v[28:31]\n"
  "s_add_u32 m0, s59, 12288\n"
  ".p2align 3\n"
  "v_mfma_f32_16x16x32_bf16 v[0:3], a[0:3], v[32:35], v[92:95]\n"
  "s_add_u32 s66, s66, 6144\n"
  "s_cmp_eq_u32 s66, 102400\n"
  "s_cselect_b32 s66, 65536, s66\n"
  "s_waitcnt lgkmcnt(9)\n"
  "buffer_load_dwordx4 v112, s[40:43], s57 offen offset:0 nt lds\n"
  "buffer_load_dwordx4 v113, s[40:43], s57 offen offset:1024 nt lds\n"
  "v_exp_f32 v20, v20\n"
  "v_mfma_f32_16x16x32_bf16 v[0:3], a[4:7], v[36:39], v[0:3]\n"
  "s_waitcnt vmcnt(10)\n"
  "ds_read_b128 a[248:251], v87 offset:2048\n"
  "ds_read_b128 a[252:255], v88 offset:2048\n"
  "v_exp_f32 v21, v21\n"
  "v_mfma_f32_16x16x32_bf16 v[0:3], a[8:11], v[40:43], v[0:3]\n"
  "s_add_u32 m0, s59, 14208\n"
  "v_exp_f32 v22, v22\n"
  "v_mfma_f32_16x16x32_bf16 v[0:3], a[12:15], v[44:47], v[0:3]\n"
  "s_waitcnt lgkmcnt(9)\n"
  "buffer_load_dwordx4 v112, s[40:43], s57 offen offset:128 nt lds\n"
  "buffer_load_dwordx4 v113, s[40:43], s57 offen offset:1152 nt lds\n"
  "v_exp_f32 v23, v23\n"
  "v_mfma_f32_16x16x32_bf16 v[0:3], a[16:19], v[48:51], v[0:3]\n"
  "s_waitcnt vmcnt(10)\n"
  "ds_read_b128 v[128:131], v87 offset:4096\n"
  "ds_read_b128 v[132:135], v88 offset:4096\n"
  "v_cvt_pk_bf16_f32 v18, v20, v21\n"
  "v_cvt_pk_bf16_f32 v19, v22, v23\n"
  "v_mfma_f32_16x16x32_bf16 v[0:3], a[20:23], v[52:55], v[0:3]\n"
  "ds_write_b128 v80, v[16:19] offset:6144\n"
  "v_pk_mul_f32 v[24:25], v[24:25], s[60:61] op_sel_hi:[1,0]\n"
  "v_pk_mul_f32 v[26:27], v[26:27], s[60:61] op_sel_hi:[1,0]\n"
  "v_pk_mul_f32 v[28:29], v[28:29], s[60:61] op_sel_hi:[1,0]\n"
  "v_pk_mul_f32 v[30:31], v[30:31], s[60:61] op_sel_hi:[1,0]\n"
  "v_mfma_f32_16x16x32_bf16 v[4:7], a[44:47], v[52:55], v[92:95]\n"
  "s_add_u32 m0, s59, 16128\n"
  "v_exp_f32 v24, v24\n"
  "v_mfma_f32_16x16x32_bf16 v[4:7], a[40:43], v[48:51], v[4:7]\n"
  "s_waitcnt lgkmcnt(10)\n"
  "buffer_load_dwordx4 v112, s[40:43], s57 offen offset:256 nt lds\n"
  "buffer_load_dwordx4 v113, s[40:43], s57 offen offset:1280 nt lds\n"
  "s_add_u32 s57, s57, 6144\n"
  "v_exp_f32 v25, v25\n"
  "v_mfma_f32_16x16x32_bf16 v[4:7], a[36:39], v[44:47], v[4:7]\n"
  "s_waitcnt vmcnt(10)\n"
  "ds_read_b128 v[152:155], v87 offset:6144\n"
  "ds_read_b128 v[156:159], v88 offset:6144\n"
  "v_exp_f32 v26, v26\n"
  "v_mfma_f32_16x16x32_bf16 v[4:7], a[32:35], v[40:43], v[4:7]\n"
  "s_add_u32 m0, s59, 18176\n"
  "v_exp_f32_e64 v27, v27\n"
  "v_mfma_f32_16x16x32_bf16 v[4:7], a[28:31], v[36:39], v[4:7]\n"
  "s_waitcnt lgkmcnt(9)\n"
  "buffer_load_dwordx4 v112, s[40:43], s57 offen offset:256 nt lds\n"
  "buffer_load_dwordx4 v113, s[40:43], s57 offen offset:1280 nt lds\n"
  "v_cvt_pk_bf16_f32 v24, v24, v25\n"
  "v_cvt_pk_bf16_f32 v25, v26, v27\n"
  "v_mfma_f32_16x16x32_bf16 v[4:7], a[24:27], v[32:35], v[4:7]\n"
  "s_waitcnt vmcnt(10)\n"
  "ds_read_b128 v[144:147], v87 offset:8192\n"
  "ds_read_b128 v[148:151], v88 offset:8192\n"
  "v_add_u32 v81, s66, v83\n"
  "v_add_u32 v82, s66, v84\n"
  "v_mfma_f32_16x16x32_bf16 v[8:11], a[48:51], v[32:35], v[92:95]\n"
  "v_exp_f32 v28, v28\n"
  "v_mfma_f32_16x16x32_bf16 v[8:11], a[52:55], v[36:39], v[8:11]\n"
  "s_add_u32 m0, s59, -128\n"
  "v_exp_f32 v29, v29\n"
  "v_mfma_f32_16x16x32_bf16 v[8:11], a[56:59], v[40:43], v[8:11]\n"
  "s_waitcnt lgkmcnt(9)\n"
  "buffer_load_dwordx4 v112, s[40:43], s57 offen offset:128 nt lds\n"
  "buffer_load_dwordx4 v113, s[40:43], s57 offen offset:1152 nt lds\n"
  "v_exp_f32 v30, v30\n"
  "v_mfma_f32_16x16x32_bf16 v[8:11], a[60:63], v[44:47], v[8:11]\n"
  "s_waitcnt vmcnt(10)\n"
  "ds_read_b128 v[136:139], v87 offset:10240\n"
  "ds_read_b128 v[140:143], v88 offset:10240\n"
  "v_exp_f32_e64 v31, v31\n"
  "v_mfma_f32_16x16x32_bf16 v[8:11], a[64:67], v[48:51], v[8:11]\n"
  "s_add_u32 m0, s59, 2048\n"
  "v_cvt_pk_bf16_f32 v26, v28, v29\n"
  "v_cvt_pk_bf16_f32 v27, v30, v31\n"
  "v_mfma_f32_16x16x32_bf16 v[8:11], a[68:71], v[52:55], v[8:11]\n"
  "s_waitcnt lgkmcnt(9)\n"
  "buffer_load_dwordx4 v112, s[40:43], s57 offen offset:0 nt lds\n"
  "buffer_load_dwordx4 v113, s[40:43], s57 offen offset:1024 nt lds\n"
  "s_add_u32 s57, s57, 6144\n"
  "ds_write_b128 v80, v[24:27] offset:7168\n"
  "v_pk_mul_f32 v[0:1], v[0:1], s[60:61] op_sel_hi:[1,0]\n"
  "v_pk_mul_f32 v[2:3], v[2:3], s[60:61] op_sel_hi:[1,0]\n"
  "v_pk_mul_f32 v[4:5], v[4:5], s[60:61] op_sel_hi:[1,0]\n"
  "v_pk_mul_f32 v[6:7], v[6:7], s[60:61] op_sel_hi:[1,0]\n"
  "v_mfma_f32_16x16x32_bf16 v[12:15], a[92:95], v[52:55], v[92:95]\n"
  "s_waitcnt vmcnt(10)\n"
  "ds_read_b128 v[160:163], v87 offset:12288\n"
  "ds_read_b128 v[164:167], v88 offset:12288\n"
  "v_exp_f32 v0, v0\n"
  "v_mfma_f32_16x16x32_bf16 v[12:15], a[88:91], v[48:51], v[12:15]\n"
  "s_add_u32 m0, s59, 4096\n"
  "v_exp_f32 v1, v1\n"
  "v_mfma_f32_16x16x32_bf16 v[12:15], a[84:87], v[44:47], v[12:15]\n"
  "s_waitcnt lgkmcnt(10)\n"
  "buffer_load_dwordx4 v112, s[40:43], s57 offen offset:0 nt lds\n"
  "buffer_load_dwordx4 v113, s[40:43], s57 offen offset:1024 nt lds\n"
  "v_exp_f32 v2, v2\n"
  "v_mfma_f32_16x16x32_bf16 v[12:15], a[80:83], v[40:43], v[12:15]\n"
  "s_waitcnt vmcnt(10)\n"
  "ds_read_b128 v[168:171], v87 offset:14336\n"
  "ds_read_b128 v[172:175], v88 offset:14336\n"
  "v_exp_f32 v3, v3\n"
  "v_mfma_f32_16x16x32_bf16 v[12:15], a[76:79], v[36:39], v[12:15]\n"
  "v_cvt_pk_bf16_f32 v0, v0, v1\n"
  "v_cvt_pk_bf16_f32 v1, v2, v3\n"
  "v_mfma_f32_16x16x32_bf16 v[12:15], a[72:75], v[32:35], v[12:15]\n"
  "s_add_u32 m0, s59, 6016\n"
  "v_xor_b32 v80, s88, v80\n"
  "v_mfma_f32_16x16x32_bf16 v[16:19], a[96:99], v[32:35], v[92:95]\n"
  "s_waitcnt lgkmcnt(9)\n"
  "buffer_load_dwordx4 v112, s[40:43], s57 offen offset:128 nt lds\n"
  "buffer_load_dwordx4 v113, s[40:43], s57 offen offset:1152 nt lds\n"
  "v_exp_f32 v4, v4\n"
  "v_mfma_f32_16x16x32_bf16 v[16:19], a[100:103], v[36:39], v[16:19]\n"
  "s_xor_b32 s89, s88, s89\n"
  "s_waitcnt vmcnt(10)\n"
  "ds_read_b128 v[176:179], v87 offset:16384\n"
  "ds_read_b128 v[180:183], v88 offset:16384\n"
  "v_exp_f32 v5, v5\n"
  "v_mfma_f32_16x16x32_bf16 v[16:19], a[104:107], v[40:43], v[16:19]\n"
  "s_xor_b32 s88, s88, s89\n"
  "s_add_u32 m0, s59, 7936\n"
  "v_exp_f32 v6, v6\n"
  "v_mfma_f32_16x16x32_bf16 v[16:19], a[108:111], v[44:47], v[16:19]\n"
  "s_waitcnt lgkmcnt(9)\n"
  "buffer_load_dwordx4 v112, s[40:43], s57 offen offset:256 nt lds\n"
  "buffer_load_dwordx4 v113, s[40:43], s57 offen offset:1280 nt lds\n"
  "s_add_u32 s57, s57, 6144\n"
  "v_exp_f32_e64 v7, v7\n"
  "v_mfma_f32_16x16x32_bf16 v[16:19], a[112:115], v[48:51], v[16:19]\n"
  "s_waitcnt vmcnt(10)\n"
  "ds_read_b128 v[200:203], v87 offset:18432\n"
  "ds_read_b128 v[204:207], v88 offset:18432\n"
  "v_cvt_pk_bf16_f32 v2, v4, v5\n"
  "v_cvt_pk_bf16_f32 v3, v6, v7\n"
  "v_mfma_f32_16x16x32_bf16 v[16:19], a[116:119], v[52:55], v[16:19]\n"
  "ds_write_b128 v80, v[0:3] offset:0\n"
  "v_pk_mul_f32 v[8:9], v[8:9], s[60:61] op_sel_hi:[1,0]\n"
  "v_pk_mul_f32 v[10:11], v[10:11], s[60:61] op_sel_hi:[1,0]\n"
  "v_pk_mul_f32 v[12:13], v[12:13], s[60:61] op_sel_hi:[1,0]\n"
  "v_pk_mul_f32 v[14:15], v[14:15], s[60:61] op_sel_hi:[1,0]\n"
  "v_mfma_f32_16x16x32_bf16 v[20:23], a[140:143], v[52:55], v[92:95]\n"
  "s_add_u32 m0, s59, 9984\n"
  "v_exp_f32 v8, v8\n"
  "v_mfma_f32_16x16x32_bf16 v[20:23], a[136:139], v[48:51], v[20:23]\n"
  "s_waitcnt lgkmcnt(10)\n"
  "buffer_load_dwordx4 v112, s[40:43], s57 offen offset:256 nt lds\n"
  "buffer_load_dwordx4 v113, s[40:43], s57 offen offset:1280 nt lds\n"
  "v_exp_f32 v9, v9\n"
  "v_mfma_f32_16x16x32_bf16 v[20:23], a[132:135], v[44:47], v[20:23]\n"
  "s_waitcnt vmcnt(10)\n"
  "ds_read_b128 v[192:195], v87 offset:0\n"
  "ds_read_b128 v[196:199], v88 offset:0\n"
  "v_exp_f32 v10, v10\n"
  "v_mfma_f32_16x16x32_bf16 v[20:23], a[128:131], v[40:43], v[20:23]\n"
  "s_add_u32 m0, s59, 12160\n"
  "v_exp_f32_e64 v11, v11\n"
  "v_mfma_f32_16x16x32_bf16 v[20:23], a[124:127], v[36:39], v[20:23]\n"
  "s_waitcnt lgkmcnt(9)\n"
  "buffer_load_dwordx4 v112, s[40:43], s57 offen offset:128 nt lds\n"
  "buffer_load_dwordx4 v113, s[40:43], s57 offen offset:1152 nt lds\n"
  "v_cvt_pk_bf16_f32 v8, v8, v9\n"
  "v_cvt_pk_bf16_f32 v9, v10, v11\n"
  "v_mfma_f32_16x16x32_bf16 v[20:23], a[120:123], v[32:35], v[20:23]\n"
  "s_waitcnt vmcnt(10)\n"
  "ds_read_b128 v[184:187], v87 offset:2048\n"
  "ds_read_b128 v[188:191], v88 offset:2048\n"
  "ds_read_b128 v[56:59], v81 offset:0\n"
  "ds_read_b128 v[60:63], v82 offset:0\n"
  "v_mfma_f32_16x16x32_bf16 v[24:27], a[144:147], v[32:35], v[92:95]\n"
  "s_add_u32 m0, s59, 14336\n"
  "v_exp_f32 v12, v12\n"
  "v_mfma_f32_16x16x32_bf16 v[24:27], a[148:151], v[36:39], v[24:27]\n"
  "s_waitcnt lgkmcnt(11)\n"
  "buffer_load_dwordx4 v112, s[40:43], s57 offen offset:0 nt lds\n"
  "buffer_load_dwordx4 v113, s[40:43], s57 offen offset:1024 nt lds\n"
  "s_add_u32 s57, s57, 6144\n"
  "v_exp_f32 v13, v13\n"
  "v_mfma_f32_16x16x32_bf16 v[24:27], a[152:155], v[40:43], v[24:27]\n"
  "s_waitcnt vmcnt(10)\n"
  "ds_read_b128 v[208:211], v87 offset:4096\n"
  "ds_read_b128 v[212:215], v88 offset:4096\n"
  "v_exp_f32 v14, v14\n"
  "v_mfma_f32_16x16x32_bf16 v[24:27], a[156:159], v[44:47], v[24:27]\n"
  "v_exp_f32_e64 v15, v15\n"
  "v_mfma_f32_16x16x32_bf16 v[24:27], a[160:163], v[48:51], v[24:27]\n"
  "v_cvt_pk_bf16_f32 v10, v12, v13\n"
  "v_cvt_pk_bf16_f32 v11, v14, v15\n"
  "v_mfma_f32_16x16x32_bf16 v[24:27], a[164:167], v[52:55], v[24:27]\n"
  "ds_write_b128 v80, v[8:11] offset:1024\n"
  "v_pk_mul_f32 v[16:17], v[16:17], s[60:61] op_sel_hi:[1,0]\n"
  "v_pk_mul_f32 v[18:19], v[18:19], s[60:61] op_sel_hi:[1,0]\n"
  "v_pk_mul_f32 v[20:21], v[20:21], s[60:61] op_sel_hi:[1,0]\n"
  "v_pk_mul_f32 v[22:23], v[22:23], s[60:61] op_sel_hi:[1,0]\n"
  "v_mfma_f32_16x16x32_bf16 v[28:31], a[188:191], v[52:55], v[92:95]\n"
  "s_waitcnt vmcnt(8)\n"
  "ds_read_b128 v[216:219], v87 offset:6144\n"
  "ds_read_b128 v[220:223], v88 offset:6144\n"
  "v_exp_f32 v16, v16\n"
  "v_mfma_f32_16x16x32_bf16 v[28:31], a[184:187], v[48:51], v[28:31]\n"
  "v_exp_f32 v17, v17\n"
  "v_mfma_f32_16x16x32_bf16 v[28:31], a[180:183], v[44:47], v[28:31]\n"
  "v_exp_f32 v18, v18\n"
  "v_mfma_f32_16x16x32_bf16 v[28:31], a[176:179], v[40:43], v[28:31]\n"
  "s_waitcnt vmcnt(6)\n"
  "ds_read_b128 v[224:227], v87 offset:8192\n"
  "ds_read_b128 v[228:231], v88 offset:8192\n"
  "v_exp_f32 v19, v19\n"
  "v_mfma_f32_16x16x32_bf16 v[28:31], a[172:175], v[36:39], v[28:31]\n"
  "v_cvt_pk_bf16_f32 v16, v16, v17\n"
  "v_cvt_pk_bf16_f32 v17, v18, v19\n"
  "v_mfma_f32_16x16x32_bf16 v[28:31], a[168:171], v[32:35], v[28:31]\n"
  "ds_read_b128 v[64:67], v81 offset:2048\n"
  "ds_read_b128 v[68:71], v82 offset:2048\n"
  "v_mfma_f32_16x16x32_bf16 v[0:3], a[192:195], v[32:35], v[92:95]\n"
  "v_exp_f32 v20, v20\n"
  "v_mfma_f32_16x16x32_bf16 v[0:3], a[196:199], v[36:39], v[0:3]\n"
  "s_waitcnt vmcnt(4)\n"
  "ds_read_b128 v[248:251], v87 offset:10240\n"
  "ds_read_b128 v[252:255], v88 offset:10240\n"
  "v_exp_f32 v21, v21\n"
  "v_mfma_f32_16x16x32_bf16 v[0:3], a[200:203], v[40:43], v[0:3]\n"
  "v_exp_f32 v22, v22\n"
  "v_mfma_f32_16x16x32_bf16 v[0:3], a[204:207], v[44:47], v[0:3]\n"
  "v_exp_f32 v23, v23\n"
  "v_mfma_f32_16x16x32_bf16 v[0:3], a[208:211], v[48:51], v[0:3]\n"
  "s_waitcnt vmcnt(2)\n"
  "ds_read_b128 v[240:243], v87 offset:12288\n"
  "ds_read_b128 v[244:247], v88 offset:12288\n"
  "v_cvt_pk_bf16_f32 v18, v20, v21\n"
  "v_cvt_pk_bf16_f32 v19, v22, v23\n"
  "v_mfma_f32_16x16x32_bf16 v[0:3], a[212:215], v[52:55], v[0:3]\n"
  "ds_write_b128 v80, v[16:19] offset:2048\n"
  "v_pk_mul_f32 v[24:25], v[24:25], s[60:61] op_sel_hi:[1,0]\n"
  "v_pk_mul_f32 v[26:27], v[26:27], s[60:61] op_sel_hi:[1,0]\n"
  "v_pk_mul_f32 v[28:29], v[28:29], s[60:61] op_sel_hi:[1,0]\n"
  "v_pk_mul_f32 v[30:31], v[30:31], s[60:61] op_sel_hi:[1,0]\n"
  "v_mfma_f32_16x16x32_bf16 v[4:7], a[236:239], v[52:55], v[92:95]\n"
  "v_exp_f32 v24, v24\n"
  "v_mfma_f32_16x16x32_bf16 v[4:7], a[232:235], v[48:51], v[4:7]\n"
  "s_waitcnt vmcnt(0)\n"
  "ds_read_b128 v[232:235], v87 offset:14336\n"
  "ds_read_b128 v[236:239], v88 offset:14336\n"
  "v_exp_f32 v25, v25\n"
  "v_mfma_f32_16x16x32_bf16 v[4:7], a[228:231], v[44:47], v[4:7]\n"
  "v_exp_f32 v26, v26\n"
  "v_mfma_f32_16x16x32_bf16 v[4:7], a[224:227], v[40:43], v[4:7]\n"
  "v_exp_f32_e64 v27, v27\n"
  "v_mfma_f32_16x16x32_bf16 v[4:7], a[220:223], v[36:39], v[4:7]\n"
  "v_cvt_pk_bf16_f32 v24, v24, v25\n"
  "v_cvt_pk_bf16_f32 v25, v26, v27\n"
  "v_mfma_f32_16x16x32_bf16 v[4:7], a[216:219], v[32:35], v[4:7]\n"
  "s_waitcnt lgkmcnt(0)\n"
  "s_barrier\n"
  "ds_read_b128 v[72:75], v81 offset:4096\n"
  "ds_read_b128 v[76:79], v82 offset:4096\n"
  "v_mfma_f32_16x16x32_bf16 v[8:11], a[240:243], v[32:35], v[92:95]\n"
  "v_exp_f32 v28, v28\n"
  "v_mfma_f32_16x16x32_bf16 v[8:11], a[244:247], v[36:39], v[8:11]\n"
  "v_exp_f32 v29, v29\n"
  "v_mfma_f32_16x16x32_bf16 v[8:11], a[248:251], v[40:43], v[8:11]\n"
  "v_exp_f32 v30, v30\n"
  "v_mfma_f32_16x16x32_bf16 v[8:11], a[252:255], v[44:47], v[8:11]\n"
  "v_exp_f32 v31, v31\n"
  "v_mfma_f32_16x16x32_bf16 v[8:11], v[128:131], v[48:51], v[8:11]\n"
  "v_cvt_pk_bf16_f32 v26, v28, v29\n"
  "v_cvt_pk_bf16_f32 v27, v30, v31\n"
  "v_mfma_f32_16x16x32_bf16 v[8:11], v[132:135], v[52:55], v[8:11]\n"
  "ds_write_b128 v80, v[24:27] offset:3072\n"
  "v_pk_mul_f32 v[0:1], v[0:1], s[60:61] op_sel_hi:[1,0]\n"
  "v_pk_mul_f32 v[2:3], v[2:3], s[60:61] op_sel_hi:[1,0]\n"
  "v_pk_mul_f32 v[4:5], v[4:5], s[60:61] op_sel_hi:[1,0]\n"
  "v_pk_mul_f32 v[6:7], v[6:7], s[60:61] op_sel_hi:[1,0]\n"
  "v_mfma_f32_16x16x32_bf16 v[12:15], v[156:159], v[52:55], v[92:95]\n"
  "v_exp_f32 v0, v0\n"
  "v_mfma_f32_16x16x32_bf16 v[12:15], v[152:155], v[48:51], v[12:15]\n"
  "v_exp_f32 v1, v1\n"
  "v_mfma_f32_16x16x32_bf16 v[12:15], v[148:151], v[44:47], v[12:15]\n"
  "v_exp_f32 v2, v2\n"
  "v_mfma_f32_16x16x32_bf16 v[12:15], v[144:147], v[40:43], v[12:15]\n"
  "v_exp_f32 v3, v3\n"
  "v_mfma_f32_16x16x32_bf16 v[12:15], v[140:143], v[36:39], v[12:15]\n"
  "v_cvt_pk_bf16_f32 v0, v0, v1\n"
  "v_cvt_pk_bf16_f32 v1, v2, v3\n"
  "v_mfma_f32_16x16x32_bf16 v[12:15], v[136:139], v[32:35], v[12:15]\n"
  "v_mul_f32 v8, s60, v8\n"
  "v_mul_f32 v9, s60, v9\n"
  "v_mfma_f32_16x16x32_bf16 v[16:19], v[160:163], v[32:35], v[92:95]\n"
  "v_exp_f32 v4, v4\n"
  "v_mfma_f32_16x16x32_bf16 v[16:19], v[164:167], v[36:39], v[16:19]\n"
  "v_exp_f32 v5, v5\n"
  "v_mfma_f32_16x16x32_bf16 v[16:19], v[168:171], v[40:43], v[16:19]\n"
  "v_exp_f32 v6, v6\n"
  "v_mfma_f32_16x16x32_bf16 v[16:19], v[172:175], v[44:47], v[16:19]\n"
  "v_exp_f32 v7, v7\n"
  "v_mfma_f32_16x16x32_bf16 v[16:19], v[176:179], v[48:51], v[16:19]\n"
  "v_cvt_pk_bf16_f32 v2, v4, v5\n"
  "v_cvt_pk_bf16_f32 v3, v6, v7\n"
  "v_mfma_f32_16x16x32_bf16 v[16:19], v[180:183], v[52:55], v[16:19]\n"
  "ds_write_b128 v80, v[0:3] offset:4096\n"
  "v_pk_mul_f32 v[10:11], v[10:11], s[60:61] op_sel_hi:[1,0]\n"
  "v_pk_mul_f32 v[12:13], v[12:13], s[60:61] op_sel_hi:[1,0]\n"
  "v_pk_mul_f32 v[14:15], v[14:15], s[60:61] op_sel_hi:[1,0]\n"
  "v_mfma_f32_16x16x32_bf16 v[20:23], v[204:207], v[52:55], v[92:95]\n"
  "v_exp_f32 v8, v8\n"
  "v_mfma_f32_16x16x32_bf16 v[20:23], v[200:203], v[48:51], v[20:23]\n"
  "v_exp_f32 v9, v9\n"
  "v_mfma_f32_16x16x32_bf16 v[20:23], v[196:199], v[44:47], v[20:23]\n"
  "v_exp_f32 v10, v10\n"
  "v_mfma_f32_16x16x32_bf16 v[20:23], v[192:195], v[40:43], v[20:23]\n"
  "v_exp_f32 v11, v11\n"
  "v_mfma_f32_16x16x32_bf16 v[20:23], v[188:191], v[36:39], v[20:23]\n"
  "v_cvt_pk_bf16_f32 v8, v8, v9\n"
  "v_cvt_pk_bf16_f32 v9, v10, v11\n"
  "v_mfma_f32_16x16x32_bf16 v[20:23], v[184:187], v[32:35], v[20:23]\n"
  "v_mul_f32 v16, s60, v16\n"
  "v_mul_f32 v17, s60, v17\n"
  "v_mfma_f32_16x16x32_bf16 v[24:27], v[208:211], v[32:35], v[92:95]\n"
  "v_exp_f32 v12, v12\n"
  "v_mfma_f32_16x16x32_bf16 v[24:27], v[212:215], v[36:39], v[24:27]\n"
  "v_exp_f32 v13, v13\n"
  "v_mfma_f32_16x16x32_bf16 v[24:27], v[216:219], v[40:43], v[24:27]\n"
  "v_exp_f32 v14, v14\n"
  "v_mfma_f32_16x16x32_bf16 v[24:27], v[220:223], v[44:47], v[24:27]\n"
  "v_exp_f32 v15, v15\n"
  "v_mfma_f32_16x16x32_bf16 v[24:27], v[224:227], v[48:51], v[24:27]\n"
  "v_cvt_pk_bf16_f32 v10, v12, v13\n"
  "v_cvt_pk_bf16_f32 v11, v14, v15\n"
  "v_mfma_f32_16x16x32_bf16 v[24:27], v[228:231], v[52:55], v[24:27]\n"
  "ds_write_b128 v80, v[8:11] offset:5120\n"
  "v_pk_mul_f32 v[18:19], v[18:19], s[60:61] op_sel_hi:[1,0]\n"
  "v_pk_mul_f32 v[20:21], v[20:21], s[60:61] op_sel_hi:[1,0]\n"
  "v_pk_mul_f32 v[22:23], v[22:23], s[60:61] op_sel_hi:[1,0]\n"
  "v_mfma_f32_16x16x32_bf16 v[28:31], v[252:255], v[52:55], v[92:95]\n"
  "v_exp_f32 v16, v16\n"
  "v_mfma_f32_16x16x32_bf16 v[28:31], v[248:251], v[48:51], v[28:31]\n"
  "v_exp_f32 v17, v17\n"
  "v_mfma_f32_16x16x32_bf16 v[28:31], v[244:247], v[44:47], v[28:31]\n"
  "v_exp_f32 v18, v18\n"
  "v_mfma_f32_16x16x32_bf16 v[28:31], v[240:243], v[40:43], v[28:31]\n"
  "s_sub_u32 s56, s56, 1\n"
  "v_exp_f32_e64 v19, v19\n"
  "v_mfma_f32_16x16x32_bf16 v[28:31], v[236:239], v[36:39], v[28:31]\n"
  "v_cvt_pk_bf16_f32 v16, v16, v17\n"
  "v_cvt_pk_bf16_f32 v17, v18, v19\n"
  "v_mfma_f32_16x16x32_bf16 v[28:31], v[232:235], v[32:35], v[28:31]\n"
  "s_waitcnt lgkmcnt(3)\n"
  ".p2align 3\n"
  "v_mfma_f32_16x16x32_bf16 v[0:3], a[0:3], v[56:59], v[92:95]\n"
  "s_add_u32 s66, s66, 6144\n"
  "v_exp_f32 v20, v20\n"
  "v_mfma_f32_16x16x32_bf16 v[0:3], a[4:7], v[60:63], v[0:3]\n"
  "s_cmp_eq_u32 s66, 102400\n"
  "v_exp_f32 v21, v21\n"
  "v_mfma_f32_16x16x32_bf16 v[0:3], a[8:11], v[64:67], v[0:3]\n"
  "s_cselect_b32 s66, 65536, s66\n"
  "v_exp_f32 v22, v22\n"
  "v_mfma_f32_16x16x32_bf16 v[0:3], a[12:15], v[68:71], v[0:3]\n"
  "v_exp_f32 v23, v23\n"
  "v_mfma_f32_16x16x32_bf16 v[0:3], a[16:19], v[72:75], v[0:3]\n"
  "v_cvt_pk_bf16_f32 v18, v20, v21\n"
  "v_cvt_pk_bf16_f32 v19, v22, v23\n"
  "v_mfma_f32_16x16x32_bf16 v[0:3], a[20:23], v[76:79], v[0:3]\n"
  "ds_write_b128 v80, v[16:19] offset:6144\n"
  "v_pk_mul_f32 v[24:25], v[24:25], s[60:61] op_sel_hi:[1,0]\n"
  "v_pk_mul_f32 v[26:27], v[26:27], s[60:61] op_sel_hi:[1,0]\n"
  "v_pk_mul_f32 v[28:29], v[28:29], s[60:61] op_sel_hi:[1,0]\n"
  "v_pk_mul_f32 v[30:31], v[30:31], s[60:61] op_sel_hi:[1,0]\n"
  "v_mfma_f32_16x16x32_bf16 v[4:7], a[44:47], v[76:79], v[92:95]\n"
  "v_exp_f32 v24, v24\n"
  "v_mfma_f32_16x16x32_bf16 v[4:7], a[40:43], v[72:75], v[4:7]\n"
  "v_exp_f32 v25, v25\n"
  "v_mfma_f32_16x16x32_bf16 v[4:7], a[36:39], v[68:71], v[4:7]\n"
  "v_exp_f32 v26, v26\n"
  "v_mfma_f32_16x16x32_bf16 v[4:7], a[32:35], v[64:67], v[4:7]\n"
  "v_exp_f32 v27, v27\n"
  "v_mfma_f32_16x16x32_bf16 v[4:7], a[28:31], v[60:63], v[4:7]\n"
  "v_cvt_pk_bf16_f32 v24, v24, v25\n"
  "v_cvt_pk_bf16_f32 v25, v26, v27\n"
  "v_mfma_f32_16x16x32_bf16 v[4:7], a[24:27], v[56:59], v[4:7]\n"
  "v_add_u32 v81, s66, v83\n"
  "v_add_u32 v82, s66, v84\n"
  "v_mfma_f32_16x16x32_bf16 v[8:11], a[48:51], v[56:59], v[92:95]\n"
  "v_exp_f32 v28, v28\n"
  "v_mfma_f32_16x16x32_bf16 v[8:11], a[52:55], v[60:63], v[8:11]\n"
  "v_exp_f32 v29, v29\n"
  "v_mfma_f32_16x16x32_bf16 v[8:11], a[56:59], v[64:67], v[8:11]\n"
  "v_exp_f32 v30, v30\n"
  "v_mfma_f32_16x16x32_bf16 v[8:11], a[60:63], v[68:71], v[8:11]\n"
  "v_exp_f32 v31, v31\n"
  "v_mfma_f32_16x16x32_bf16 v[8:11], a[64:67], v[72:75], v[8:11]\n"
  "v_cvt_pk_bf16_f32 v26, v28, v29\n"
  "v_cvt_pk_bf16_f32 v27, v30, v31\n"
  "v_mfma_f32_16x16x32_bf16 v[8:11], a[68:71], v[76:79], v[8:11]\n"
  "ds_write_b128 v80, v[24:27] offset:7168\n"
  "v_pk_mul_f32 v[0:1], v[0:1], s[60:61] op_sel_hi:[1,0]\n"
  "v_pk_mul_f32 v[2:3], v[2:3], s[60:61] op_sel_hi:[1,0]\n"
  "v_pk_mul_f32 v[4:5], v[4:5], s[60:61] op_sel_hi:[1,0]\n"
  "v_pk_mul_f32 v[6:7], v[6:7], s[60:61] op_sel_hi:[1,0]\n"
  "v_mfma_f32_16x16x32_bf16 v[12:15], a[92:95], v[76:79], v[92:95]\n"
  "v_exp_f32 v0, v0\n"
  "v_mfma_f32_16x16x32_bf16 v[12:15], a[88:91], v[72:75], v[12:15]\n"
  "v_exp_f32 v1, v1\n"
  "v_mfma_f32_16x16x32_bf16 v[12:15], a[84:87], v[68:71], v[12:15]\n"
  "v_exp_f32 v2, v2\n"
  "v_mfma_f32_16x16x32_bf16 v[12:15], a[80:83], v[64:67], v[12:15]\n"
  "v_exp_f32 v3, v3\n"
  "v_mfma_f32_16x16x32_bf16 v[12:15], a[76:79], v[60:63], v[12:15]\n"
  "v_cvt_pk_bf16_f32 v0, v0, v1\n"
  "v_cvt_pk_bf16_f32 v1, v2, v3\n"
  "v_mfma_f32_16x16x32_bf16 v[12:15], a[72:75], v[56:59], v[12:15]\n"
  "v_xor_b32 v80, s88, v80\n"
  "v_mfma_f32_16x16x32_bf16 v[16:19], a[96:99], v[56:59], v[92:95]\n"
  "v_exp_f32 v4, v4\n"
  "v_mfma_f32_16x16x32_bf16 v[16:19], a[100:103], v[60:63], v[16:19]\n"
  "s_xor_b32 s89, s88, s89\n"
  "v_exp_f32 v5, v5\n"
  "v_mfma_f32_16x16x32_bf16 v[16:19], a[104:107], v[64:67], v[16:19]\n"
  "s_xor_b32 s88, s88, s89\n"
  "v_exp_f32 v6, v6\n"
  "v_mfma_f32_16x16x32_bf16 v[16:19], a[108:111], v[68:71], v[16:19]\n"
  "v_exp_f32_e64 v7, v7\n"
  "v_mfma_f32_16x16x32_bf16 v[16:19], a[112:115], v[72:75], v[16:19]\n"
  "v_cvt_pk_bf16_f32 v2, v4, v5\n"
  "v_cvt_pk_bf16_f32 v3, v6, v7\n"
  "v_mfma_f32_16x16x32_bf16 v[16:19], a[116:119], v[76:79], v[16:19]\n"
  "ds_write_b128 v80, v[0:3] offset:0\n"
  "v_pk_mul_f32 v[8:9], v[8:9], s[60:61] op_sel_hi:[1,0]\n"
  "v_pk_mul_f32 v[10:11], v[10:11], s[60:61] op_sel_hi:[1,0]\n"
  "v_pk_mul_f32 v[12:13], v[12:13], s[60:61] op_sel_hi:[1,0]\n"
  "v_pk_mul_f32 v[14:15], v[14:15], s[60:61] op_sel_hi:[1,0]\n"
  "v_mfma_f32_16x16x32_bf16 v[20:23], a[140:143], v[76:79], v[92:95]\n"
  "v_exp_f32 v8, v8\n"
  "v_mfma_f32_16x16x32_bf16 v[20:23], a[136:139], v[72:75], v[20:23]\n"
  "v_exp_f32 v9, v9\n"
  "v_mfma_f32_16x16x32_bf16 v[20:23], a[132:135], v[68:71], v[20:23]\n"
  "v_exp_f32 v10, v10\n"
  "v_mfma_f32_16x16x32_bf16 v[20:23], a[128:131], v[64:67], v[20:23]\n"
  "v_exp_f32 v11, v11\n"
  "v_mfma_f32_16x16x32_bf16 v[20:23], a[124:127], v[60:63], v[20:23]\n"
  "v_cvt_pk_bf16_f32 v8, v8, v9\n"
  "v_cvt_pk_bf16_f32 v9, v10, v11\n"
  "v_mfma_f32_16x16x32_bf16 v[20:23], a[120:123], v[56:59], v[20:23]\n"
  "ds_read_b128 v[32:35], v81 offset:0\n"
  "ds_read_b128 v[36:39], v82 offset:0\n"
  "v_mfma_f32_16x16x32_bf16 v[24:27], a[144:147], v[56:59], v[92:95]\n"
  "v_exp_f32 v12, v12\n"
  "v_mfma_f32_16x16x32_bf16 v[24:27], a[148:151], v[60:63], v[24:27]\n"
  "v_exp_f32 v13, v13\n"
  "v_mfma_f32_16x16x32_bf16 v[24:27], a[152:155], v[64:67], v[24:27]\n"
  "v_exp_f32 v14, v14\n"
  "v_mfma_f32_16x16x32_bf16 v[24:27], a[156:159], v[68:71], v[24:27]\n"
  "v_exp_f32 v15, v15\n"
  "v_mfma_f32_16x16x32_bf16 v[24:27], a[160:163], v[72:75], v[24:27]\n"
  "v_cvt_pk_bf16_f32 v10, v12, v13\n"
  "v_cvt_pk_bf16_f32 v11, v14, v15\n"
  "v_mfma_f32_16x16x32_bf16 v[24:27], a[164:167], v[76:79], v[24:27]\n"
  "ds_write_b128 v80, v[8:11] offset:1024\n"
  "v_pk_mul_f32 v[16:17], v[16:17], s[60:61] op_sel_hi:[1,0]\n"
  "v_pk_mul_f32 v[18:19], v[18:19], s[60:61] op_sel_hi:[1,0]\n"
  "v_pk_mul_f32 v[20:21], v[20:21], s[60:61] op_sel_hi:[1,0]\n"
  "v_pk_mul_f32 v[22:23], v[22:23], s[60:61] op_sel_hi:[1,0]\n"
  "v_mfma_f32_16x16x32_bf16 v[28:31], a[188:191], v[76:79], v[92:95]\n"
  "v_exp_f32 v16, v16\n"
  "v_mfma_f32_16x16x32_bf16 v[28:31], a[184:187], v[72:75], v[28:31]\n"
  "v_exp_f32 v17, v17\n"
  "v_mfma_f32_16x16x32_bf16 v[28:31], a[180:183], v[68:71], v[28:31]\n"
  "v_exp_f32 v18, v18\n"
  "v_mfma_f32_16x16x32_bf16 v[28:31], a[176:179], v[64:67], v[28:31]\n"
  "v_exp_f32 v19, v19\n"
  "v_mfma_f32_16x16x32_bf16 v[28:31], a[172:175], v[60:63], v[28:31]\n"
  "v_cvt_pk_bf16_f32 v16, v16, v17\n"
  "v_cvt_pk_bf16_f32 v17, v18, v19\n"
  "v_mfma_f32_16x16x32_bf16 v[28:31], a[168:171], v[56:59], v[28:31]\n"
  "ds_read_b128 v[40:43], v81 offset:2048\n"
  "ds_read_b128 v[44:47], v82 offset:2048\n"
  "v_mfma_f32_16x16x32_bf16 v[0:3], a[192:195], v[56:59], v[92:95]\n"
  "v_exp_f32 v20, v20\n"
  "v_mfma_f32_16x16x32_bf16 v[0:3], a[196:199], v[60:63], v[0:3]\n"
  "v_exp_f32 v21, v21\n"
  "v_mfma_f32_16x16x32_bf16 v[0:3], a[200:203], v[64:67], v[0:3]\n"
  "v_exp_f32 v22, v22\n"
  "v_mfma_f32_16x16x32_bf16 v[0:3], a[204:207], v[68:71], v[0:3]\n"
  "v_exp_f32 v23, v23\n"
  "v_mfma_f32_16x16x32_bf16 v[0:3], a[208:211], v[72:75], v[0:3]\n"
  "v_cvt_pk_bf16_f32 v18, v20, v21\n"
  "v_cvt_pk_bf16_f32 v19, v22, v23\n"
  "v_mfma_f32_16x16x32_bf16 v[0:3], a[212:215], v[76:79], v[0:3]\n"
  "ds_write_b128 v80, v[16:19] offset:2048\n"
  "v_pk_mul_f32 v[24:25], v[24:25], s[60:61] op_sel_hi:[1,0]\n"
  "v_pk_mul_f32 v[26:27], v[26:27], s[60:61] op_sel_hi:[1,0]\n"
  "v_pk_mul_f32 v[28:29], v[28:29], s[60:61] op_sel_hi:[1,0]\n"
  "v_pk_mul_f32 v[30:31], v[30:31], s[60:61] op_sel_hi:[1,0]\n"
  "v_mfma_f32_16x16x32_bf16 v[4:7], a[236:239], v[76:79], v[92:95]\n"
  "v_exp_f32 v24, v24\n"
  "v_mfma_f32_16x16x32_bf16 v[4:7], a[232:235], v[72:75], v[4:7]\n"
  "v_exp_f32 v25, v25\n"
  "v_mfma_f32_16x16x32_bf16 v[4:7], a[228:231], v[68:71], v[4:7]\n"
  "v_exp_f32 v26, v26\n"
  "v_mfma_f32_16x16x32_bf16 v[4:7], a[224:227], v[64:67], v[4:7]\n"
  "v_exp_f32 v27, v27\n"
  "v_mfma_f32_16x16x32_bf16 v[4:7], a[220:223], v[60:63], v[4:7]\n"
  "v_cvt_pk_bf16_f32 v24, v24, v25\n"
  "v_cvt_pk_bf16_f32 v25, v26, v27\n"
  "v_mfma_f32_16x16x32_bf16 v[4:7], a[216:219], v[56:59], v[4:7]\n"
  "ds_read_b128 v[48:51], v81 offset:4096\n"
  "ds_read_b128 v[52:55], v82 offset:4096\n"
  "v_mfma_f32_16x16x32_bf16 v[8:11], a[240:243], v[56:59], v[92:95]\n"
  "v_exp_f32 v28, v28\n"
  "v_mfma_f32_16x16x32_bf16 v[8:11], a[244:247], v[60:63], v[8:11]\n"
  "v_exp_f32 v29, v29\n"
  "v_mfma_f32_16x16x32_bf16 v[8:11], a[248:251], v[64:67], v[8:11]\n"
  "v_exp_f32 v30, v30\n"
  "v_mfma_f32_16x16x32_bf16 v[8:11], a[252:255], v[68:71], v[8:11]\n"
  "v_exp_f32 v31, v31\n"
  "v_mfma_f32_16x16x32_bf16 v[8:11], v[128:131], v[72:75], v[8:11]\n"
  "v_cvt_pk_bf16_f32 v26, v28, v29\n"
  "v_cvt_pk_bf16_f32 v27, v30, v31\n"
  "v_mfma_f32_16x16x32_bf16 v[8:11], v[132:135], v[76:79], v[8:11]\n"
  "ds_write_b128 v80, v[24:27] offset:3072\n"
  "v_pk_mul_f32 v[0:1], v[0:1], s[60:61] op_sel_hi:[1,0]\n"
  "v_pk_mul_f32 v[2:3], v[2:3], s[60:61] op_sel_hi:[1,0]\n"
  "v_pk_mul_f32 v[4:5], v[4:5], s[60:61] op_sel_hi:[1,0]\n"
  "v_pk_mul_f32 v[6:7], v[6:7], s[60:61] op_sel_hi:[1,0]\n"
  "v_mfma_f32_16x16x32_bf16 v[12:15], v[156:159], v[76:79], v[92:95]\n"
  "v_exp_f32 v0, v0\n"
  "v_mfma_f32_16x16x32_bf16 v[12:15], v[152:155], v[72:75], v[12:15]\n"
  "v_exp_f32 v1, v1\n"
  "v_mfma_f32_16x16x32_bf16 v[12:15], v[148:151], v[68:71], v[12:15]\n"
  "v_exp_f32 v2, v2\n"
  "v_mfma_f32_16x16x32_bf16 v[12:15], v[144:147], v[64:67], v[12:15]\n"
  "v_exp_f32 v3, v3\n"
  "v_mfma_f32_16x16x32_bf16 v[12:15], v[140:143], v[60:63], v[12:15]\n"
  "v_cvt_pk_bf16_f32 v0, v0, v1\n"
  "v_cvt_pk_bf16_f32 v1, v2, v3\n"
  "v_mfma_f32_16x16x32_bf16 v[12:15], v[136:139], v[56:59], v[12:15]\n"
  "s_waitcnt lgkmcnt(10)\n"
  "s_barrier\n"
  "v_mul_f32 v8, s60, v8\n"
  "v_mul_f32 v9, s60, v9\n"
  "v_mfma_f32_16x16x32_bf16 v[16:19], v[160:163], v[56:59], v[92:95]\n"
  "v_exp_f32 v4, v4\n"
  "v_mfma_f32_16x16x32_bf16 v[16:19], v[164:167], v[60:63], v[16:19]\n"
  "v_exp_f32 v5, v5\n"
  "v_mfma_f32_16x16x32_bf16 v[16:19], v[168:171], v[64:67], v[16:19]\n"
  "v_exp_f32 v6, v6\n"
  "v_mfma_f32_16x16x32_bf16 v[16:19], v[172:175], v[68:71], v[16:19]\n"
  "v_exp_f32 v7, v7\n"
  "v_mfma_f32_16x16x32_bf16 v[16:19], v[176:179], v[72:75], v[16:19]\n"
  "v_cvt_pk_bf16_f32 v2, v4, v5\n"
  "v_cvt_pk_bf16_f32 v3, v6, v7\n"
  "v_mfma_f32_16x16x32_bf16 v[16:19], v[180:183], v[76:79], v[16:19]\n"
  "ds_write_b128 v80, v[0:3] offset:4096\n"
  "v_pk_mul_f32 v[10:11], v[10:11], s[60:61] op_sel_hi:[1,0]\n"
  "v_pk_mul_f32 v[12:13], v[12:13], s[60:61] op_sel_hi:[1,0]\n"
  "v_pk_mul_f32 v[14:15], v[14:15], s[60:61] op_sel_hi:[1,0]\n"
  "v_mfma_f32_16x16x32_bf16 v[20:23], v[204:207], v[76:79], v[92:95]\n"
  "v_exp_f32 v8, v8\n"
  "v_mfma_f32_16x16x32_bf16 v[20:23], v[200:203], v[72:75], v[20:23]\n"
  "v_exp_f32 v9, v9\n"
  "v_mfma_f32_16x16x32_bf16 v[20:23], v[196:199], v[68:71], v[20:23]\n"
  "v_exp_f32 v10, v10\n"
  "v_mfma_f32_16x16x32_bf16 v[20:23], v[192:195], v[64:67], v[20:23]\n"
  "v_exp_f32 v11, v11\n"
  "v_mfma_f32_16x16x32_bf16 v[20:23], v[188:191], v[60:63], v[20:23]\n"
  "v_cvt_pk_bf16_f32 v8, v8, v9\n"
  "v_cvt_pk_bf16_f32 v9, v10, v11\n"
  "v_mfma_f32_16x16x32_bf16 v[20:23], v[184:187], v[56:59], v[20:23]\n"
  "v_mul_f32 v16, s60, v16\n"
  "v_mul_f32 v17, s60, v17\n"
  "v_mfma_f32_16x16x32_bf16 v[24:27], v[208:211], v[56:59], v[92:95]\n"
  "v_exp_f32 v12, v12\n"
  "v_mfma_f32_16x16x32_bf16 v[24:27], v[212:215], v[60:63], v[24:27]\n"
  "v_exp_f32 v13, v13\n"
  "v_mfma_f32_16x16x32_bf16 v[24:27], v[216:219], v[64:67], v[24:27]\n"
  "v_exp_f32 v14, v14\n"
  "v_mfma_f32_16x16x32_bf16 v[24:27], v[220:223], v[68:71], v[24:27]\n"
  "v_exp_f32 v15, v15\n"
  "v_mfma_f32_16x16x32_bf16 v[24:27], v[224:227], v[72:75], v[24:27]\n"
  "v_cvt_pk_bf16_f32 v10, v12, v13\n"
  "v_cvt_pk_bf16_f32 v11, v14, v15\n"
  "v_mfma_f32_16x16x32_bf16 v[24:27], v[228:231], v[76:79], v[24:27]\n"
  "ds_write_b128 v80, v[8:11] offset:5120\n"
  "v_pk_mul_f32 v[18:19], v[18:19], s[60:61] op_sel_hi:[1,0]\n"
  "v_pk_mul_f32 v[20:21], v[20:21], s[60:61] op_sel_hi:[1,0]\n"
  "v_pk_mul_f32 v[22:23], v[22:23], s[60:61] op_sel_hi:[1,0]\n"
  "v_mfma_f32_16x16x32_bf16 v[28:31], v[252:255], v[76:79], v[92:95]\n"
  "v_exp_f32 v16, v16\n"
  "v_mfma_f32_16x16x32_bf16 v[28:31], v[248:251], v[72:75], v[28:31]\n"
  "v_exp_f32 v17, v17\n"
  "v_mfma_f32_16x16x32_bf16 v[28:31], v[244:247], v[68:71], v[28:31]\n"
  "v_exp_f32 v18, v18\n"
  "v_mfma_f32_16x16x32_bf16 v[28:31], v[240:243], v[64:67], v[28:31]\n"
  "s_sub_u32 s56, s56, 1\n"
  "v_exp_f32_e64 v19, v19\n"
  "v_mfma_f32_16x16x32_bf16 v[28:31], v[236:239], v[60:63], v[28:31]\n"
  "v_cvt_pk_bf16_f32 v16, v16, v17\n"
  "v_cvt_pk_bf16_f32 v17, v18, v19\n"
  "v_mfma_f32_16x16x32_bf16 v[28:31], v[232:235], v[56:59], v[28:31]\n"
  "s_waitcnt lgkmcnt(3)\n"
  ".p2align 3\n"
  "A_N0_%=:\n"
  "v_mfma_f32_16x16x32_bf16 v[0:3], a[0:3], v[32:35], v[92:95]\n"
  "s_add_u32 s66, s66, 6144\n"
  "v_exp_f32 v20, v20\n"
  "v_mfma_f32_16x16x32_bf16 v[0:3], a[4:7], v[36:39], v[0:3]\n"
  "s_cmp_eq_u32 s66, 102400\n"
  "v_exp_f32 v21, v21\n"
  "v_mfma_f32_16x16x32_bf16 v[0:3], a[8:11], v[40:43], v[0:3]\n"
  "s_cselect_b32 s66, 65536, s66\n"
  "v_exp_f32 v22, v22\n"
  "v_mfma_f32_16x16x32_bf16 v[0:3], a[12:15], v[44:47], v[0:3]\n"
  "v_exp_f32 v23, v23\n"
  "v_mfma_f32_16x16x32_bf16 v[0:3], a[16:19], v[48:51], v[0:3]\n"
  "v_cvt_pk_bf16_f32 v18, v20, v21\n"
  "v_cvt_pk_bf16_f32 v19, v22, v23\n"
  "v_mfma_f32_16x16x32_bf16 v[0:3], a[20:23], v[52:55], v[0:3]\n"
  "ds_write_b128 v80, v[16:19] offset:6144\n"
  "v_pk_mul_f32 v[24:25], v[24:25], s[60:61] op_sel_hi:[1,0]\n"
  "v_pk_mul_f32 v[26:27], v[26:27], s[60:61] op_sel_hi:[1,0]\n"
  "v_pk_mul_f32 v[28:29], v[28:29], s[60:61] op_sel_hi:[1,0]\n"
  "v_pk_mul_f32 v[30:31], v[30:31], s[60:61] op_sel_hi:[1,0]\n"
  "v_mfma_f32_16x16x32_bf16 v[4:7], a[44:47], v[52:55], v[92:95]\n"
  "v_exp_f32 v24, v24\n"
  "v_mfma_f32_16x16x32_bf16 v[4:7], a[40:43], v[48:51], v[4:7]\n"
  "v_exp_f32 v25, v25\n"
  "v_mfma_f32_16x16x32_bf16 v[4:7], a[36:39], v[44:47], v[4:7]\n"
  "v_exp_f32 v26, v26\n"
  "v_mfma_f32_16x16x32_bf16 v[4:7], a[32:35], v[40:43], v[4:7]\n"
  "v_exp_f32 v27, v27\n"
  "v_mfma_f32_16x16x32_bf16 v[4:7], a[28:31], v[36:39], v[4:7]\n"
  "v_cvt_pk_bf16_f32 v24, v24, v25\n"
  "v_cvt_pk_bf16_f32 v25, v26, v27\n"
  "v_mfma_f32_16x16x32_bf16 v[4:7], a[24:27], v[32:35], v[4:7]\n"
  "v_add_u32 v81, s66, v83\n"
  "v_add_u32 v82, s66, v84\n"
  "v_mfma_f32_16x16x32_bf16 v[8:11], a[48:51], v[32:35], v[92:95]\n"
  "v_exp_f32 v28, v28\n"
  "v_mfma_f32_16x16x32_bf16 v[8:11], a[52:55], v[36:39], v[8:11]\n"
  "v_exp_f32 v29, v29\n"
  "v_mfma_f32_16x16x32_bf16 v[8:11], a[56:59], v[40:43], v[8:11]\n"
  "v_exp_f32 v30, v30\n"
  "v_mfma_f32_16x16x32_bf16 v[8:11], a[60:63], v[44:47], v[8:11]\n"
  "v_exp_f32 v31, v31\n"
  "v_mfma_f32_16x16x32_bf16 v[8:11], a[64:67], v[48:51], v[8:11]\n"
  "v_cvt_pk_bf16_f32 v26, v28, v29\n"
  "v_cvt_pk_bf16_f32 v27, v30, v31\n"
  "v_mfma_f32_16x16x32_bf16 v[8:11], a[68:71], v[52:55], v[8:11]\n"
  "ds_write_b128 v80, v[24:27] offset:7168\n"
  "v_pk_mul_f32 v[0:1], v[0:1], s[60:61] op_sel_hi:[1,0]\n"
  "v_pk_mul_f32 v[2:3], v[2:3], s[60:61] op_sel_hi:[1,0]\n"
  "v_pk_mul_f32 v[4:5], v[4:5], s[60:61] op_sel_hi:[1,0]\n"
  "v_pk_mul_f32 v[6:7], v[6:7], s[60:61] op_sel_hi:[1,0]\n"
  "v_mfma_f32_16x16x32_bf16 v[12:15], a[92:95], v[52:55], v[92:95]\n"
  "v_exp_f32 v0, v0\n"
  "v_mfma_f32_16x16x32_bf16 v[12:15], a[88:91], v[48:51], v[12:15]\n"
  "v_exp_f32 v1, v1\n"
  "v_mfma_f32_16x16x32_bf16 v[12:15], a[84:87], v[44:47], v[12:15]\n"
  "v_exp_f32 v2, v2\n"
  "v_mfma_f32_16x16x32_bf16 v[12:15], a[80:83], v[40:43], v[12:15]\n"
  "v_exp_f32 v3, v3\n"
  "v_mfma_f32_16x16x32_bf16 v[12:15], a[76:79], v[36:39], v[12:15]\n"
  "v_cvt_pk_bf16_f32 v0, v0, v1\n"
  "v_cvt_pk_bf16_f32 v1, v2, v3\n"
  "v_mfma_f32_16x16x32_bf16 v[12:15], a[72:75], v[32:35], v[12:15]\n"
  "v_xor_b32 v80, s88, v80\n"
  "v_mfma_f32_16x16x32_bf16 v[16:19], a[96:99], v[32:35], v[92:95]\n"
  "v_exp_f32 v4, v4\n"
  "v_mfma_f32_16x16x32_bf16 v[16:19], a[100:103], v[36:39], v[16:19]\n"
  "s_xor_b32 s89, s88, s89\n"
  "v_exp_f32 v5, v5\n"
  "v_mfma_f32_16x16x32_bf16 v[16:19], a[104:107], v[40:43], v[16:19]\n"
  "s_xor_b32 s88, s88, s89\n"
  "v_exp_f32 v6, v6\n"
  "v_mfma_f32_16x16x32_bf16 v[16:19], a[108:111], v[44:47], v[16:19]\n"
  "v_exp_f32_e64 v7, v7\n"
  "v_mfma_f32_16x16x32_bf16 v[16:19], a[112:115], v[48:51], v[16:19]\n"
  "v_cvt_pk_bf16_f32 v2, v4, v5\n"
  "v_cvt_pk_bf16_f32 v3, v6, v7\n"
  "v_mfma_f32_16x16x32_bf16 v[16:19], a[116:119], v[52:55], v[16:19]\n"
  "ds_write_b128 v80, v[0:3] offset:0\n"
  "v_pk_mul_f32 v[8:9], v[8:9], s[60:61] op_sel_hi:[1,0]\n"
  "v_pk_mul_f32 v[10:11], v[10:11], s[60:61] op_sel_hi:[1,0]\n"
  "v_pk_mul_f32 v[12:13], v[12:13], s[60:61] op_sel_hi:[1,0]\n"
  "v_pk_mul_f32 v[14:15], v[14:15], s[60:61] op_sel_hi:[1,0]\n"
  "v_mfma_f32_16x16x32_bf16 v[20:23], a[140:143], v[52:55], v[92:95]\n"
  "v_exp_f32 v8, v8\n"
  "v_mfma_f32_16x16x32_bf16 v[20:23], a[136:139], v[48:51], v[20:23]\n"
  "v_exp_f32 v9, v9\n"
  "v_mfma_f32_16x16x32_bf16 v[20:23], a[132:135], v[44:47], v[20:23]\n"
  "v_exp_f32 v10, v10\n"
  "v_mfma_f32_16x16x32_bf16 v[20:23], a[128:131], v[40:43], v[20:23]\n"
  "v_exp_f32 v11, v11\n"
  "v_mfma_f32_16x16x32_bf16 v[20:23], a[124:127], v[36:39], v[20:23]\n"
  "v_cvt_pk_bf16_f32 v8, v8, v9\n"
  "v_cvt_pk_bf16_f32 v9, v10, v11\n"
  "v_mfma_f32_16x16x32_bf16 v[20:23], a[120:123], v[32:35], v[20:23]\n"
  "ds_read_b128 v[56:59], v81 offset:0\n"
  "ds_read_b128 v[60:63], v82 offset:0\n"
  "v_mfma_f32_16x16x32_bf16 v[24:27], a[144:147], v[32:35], v[92:95]\n"
  "v_exp_f32 v12, v12\n"
  "v_mfma_f32_16x16x32_bf16 v[24:27], a[148:151], v[36:39], v[24:27]\n"
  "v_exp_f32 v13, v13\n"
  "v_mfma_f32_16x16x32_bf16 v[24:27], a[152:155], v[40:43], v[24:27]\n"
  "v_exp_f32 v14, v14\n"
  "v_mfma_f32_16x16x32_bf16 v[24:27], a[156:159], v[44:47], v[24:27]\n"
  "v_exp_f32 v15, v15\n"
  "v_mfma_f32_16x16x32_bf16 v[24:27], a[160:163], v[48:51], v[24:27]\n"
  "v_cvt_pk_bf16_f32 v10, v12, v13\n"
  "v_cvt_pk_bf16_f32 v11, v14, v15\n"
  "v_mfma_f32_16x16x32_bf16 v[24:27], a[164:167], v[52:55], v[24:27]\n"
  "ds_write_b128 v80, v[8:11] offset:1024\n"
  "v_pk_mul_f32 v[16:17], v[16:17], s[60:61] op_sel_hi:[1,0]\n"
  "v_pk_mul_f32 v[18:19], v[18:19], s[60:61] op_sel_hi:[1,0]\n"
  "v_pk_mul_f32 v[20:21], v[20:21], s[60:61] op_sel_hi:[1,0]\n"
  "v_pk_mul_f32 v[22:23], v[22:23], s[60:61] op_sel_hi:[1,0]\n"
  "v_mfma_f32_16x16x32_bf16 v[28:31], a[188:191], v[52:55], v[92:95]\n"
  "v_exp_f32 v16, v16\n"
  "v_mfma_f32_16x16x32_bf16 v[28:31], a[184:187], v[48:51], v[28:31]\n"
  "v_exp_f32 v17, v17\n"
  "v_mfma_f32_16x16x32_bf16 v[28:31], a[180:183], v[44:47], v[28:31]\n"
  "v_exp_f32 v18, v18\n"
  "v_mfma_f32_16x16x32_bf16 v[28:31], a[176:179], v[40:43], v[28:31]\n"
  "v_exp_f32 v19, v19\n"
  "v_mfma_f32_16x16x32_bf16 v[28:31], a[172:175], v[36:39], v[28:31]\n"
  "v_cvt_pk_bf16_f32 v16, v16, v17\n"
  "v_cvt_pk_bf16_f32 v17, v18, v19\n"
  "v_mfma_f32_16x16x32_bf16 v[28:31], a[168:171], v[32:35], v[28:31]\n"
  "ds_read_b128 v[64:67], v81 offset:2048\n"
  "ds_read_b128 v[68:71], v82 offset:2048\n"
  "v_mfma_f32_16x16x32_bf16 v[0:3], a[192:195], v[32:35], v[92:95]\n"
  "v_exp_f32 v20, v20\n"
  "v_mfma_f32_16x16x32_bf16 v[0:3], a[196:199], v[36:39], v[0:3]\n"
  "v_exp_f32 v21, v21\n"
  "v_mfma_f32_16x16x32_bf16 v[0:3], a[200:203], v[40:43], v[0:3]\n"
  "v_exp_f32 v22, v22\n"
  "v_mfma_f32_16x16x32_bf16 v[0:3], a[204:207], v[44:47], v[0:3]\n"
  "v_exp_f32 v23, v23\n"
  "v_mfma_f32_16x16x32_bf16 v[0:3], a[208:211], v[48:51], v[0:3]\n"
  "v_cvt_pk_bf16_f32 v18, v20, v21\n"
  "v_cvt_pk_bf16_f32 v19, v22, v23\n"
  "v_mfma_f32_16x16x32_bf16 v[0:3], a[212:215], v[52:55], v[0:3]\n"
  "ds_write_b128 v80, v[16:19] offset:2048\n"
  "v_pk_mul_f32 v[24:25], v[24:25], s[60:61] op_sel_hi:[1,0]\n"
  "v_pk_mul_f32 v[26:27], v[26:27], s[60:61] op_sel_hi:[1,0]\n"
  "v_pk_mul_f32 v[28:29], v[28:29], s[60:61] op_sel_hi:[1,0]\n"
  "v_pk_mul_f32 v[30:31], v[30:31], s[60:61] op_sel_hi:[1,0]\n"
  "v_mfma_f32_16x16x32_bf16 v[4:7], a[236:239], v[52:55], v[92:95]\n"
  "v_exp_f32 v24, v24\n"
  "v_mfma_f32_16x16x32_bf16 v[4:7], a[232:235], v[48:51], v[4:7]\n"
  "v_exp_f32 v25, v25\n"
  "v_mfma_f32_16x16x32_bf16 v[4:7], a[228:231], v[44:47], v[4:7]\n"
  "v_exp_f32 v26, v26\n"
  "v_mfma_f32_16x16x32_bf16 v[4:7], a[224:227], v[40:43], v[4:7]\n"
  "v_exp_f32 v27, v27\n"
  "v_mfma_f32_16x16x32_bf16 v[4:7], a[220:223], v[36:39], v[4:7]\n"
  "v_cvt_pk_bf16_f32 v24, v24, v25\n"
  "v_cvt_pk_bf16_f32 v25, v26, v27\n"
  "v_mfma_f32_16x16x32_bf16 v[4:7], a[216:219], v[32:35], v[4:7]\n"
  "ds_read_b128 v[72:75], v81 offset:4096\n"
  "ds_read_b128 v[76:79], v82 offset:4096\n"
  "v_mfma_f32_16x16x32_bf16 v[8:11], a[240:243], v[32:35], v[92:95]\n"
  "v_exp_f32 v28, v28\n"
  "v_mfma_f32_16x16x32_bf16 v[8:11], a[244:247], v[36:39], v[8:11]\n"
  "v_exp_f32 v29, v29\n"
  "v_mfma_f32_16x16x32_bf16 v[8:11], a[248:251], v[40:43], v[8:11]\n"
  "v_exp_f32 v30, v30\n"
  "v_mfma_f32_16x16x32_bf16 v[8:11], a[252:255], v[44:47], v[8:11]\n"
  "v_exp_f32 v31, v31\n"
  "v_mfma_f32_16x16x32_bf16 v[8:11], v[128:131], v[48:51], v[8:11]\n"
  "v_cvt_pk_bf16_f32 v26, v28, v29\n"
  "v_cvt_pk_bf16_f32 v27, v30, v31\n"
  "v_mfma_f32_16x16x32_bf16 v[8:11], v[132:135], v[52:55], v[8:11]\n"
  "ds_write_b128 v80, v[24:27] offset:3072\n"
  "v_pk_mul_f32 v[0:1], v[0:1], s[60:61] op_sel_hi:[1,0]\n"
  "v_pk_mul_f32 v[2:3], v[2:3], s[60:61] op_sel_hi:[1,0]\n"
  "v_pk_mul_f32 v[4:5], v[4:5], s[60:61] op_sel_hi:[1,0]\n"
  "v_pk_mul_f32 v[6:7], v[6:7], s[60:61] op_sel_hi:[1,0]\n"
  "v_mfma_f32_16x16x32_bf16 v[12:15], v[156:159], v[52:55], v[92:95]\n"
  "v_exp_f32 v0, v0\n"
  "v_mfma_f32_16x16x32_bf16 v[12:15], v[152:155], v[48:51], v[12:15]\n"
  "v_exp_f32 v1, v1\n"
  "v_mfma_f32_16x16x32_bf16 v[12:15], v[148:151], v[44:47], v[12:15]\n"
  "v_exp_f32 v2, v2\n"
  "v_mfma_f32_16x16x32_bf16 v[12:15], v[144:147], v[40:43], v[12:15]\n"
  "v_exp_f32 v3, v3\n"
  "v_mfma_f32_16x16x32_bf16 v[12:15], v[140:143], v[36:39], v[12:15]\n"
  "v_cvt_pk_bf16_f32 v0, v0, v1\n"
  "v_cvt_pk_bf16_f32 v1, v2, v3\n"
  "v_mfma_f32_16x16x32_bf16 v[12:15], v[136:139], v[32:35], v[12:15]\n"
  "v_mul_f32 v8, s60, v8\n"
  "v_mul_f32 v9, s60, v9\n"
  "v_mfma_f32_16x16x32_bf16 v[16:19], v[160:163], v[32:35], v[92:95]\n"
  "v_exp_f32 v4, v4\n"
  "v_mfma_f32_16x16x32_bf16 v[16:19], v[164:167], v[36:39], v[16:19]\n"
  "v_exp_f32 v5, v5\n"
  "v_mfma_f32_16x16x32_bf16 v[16:19], v[168:171], v[40:43], v[16:19]\n"
  "v_exp_f32 v6, v6\n"
  "v_mfma_f32_16x16x32_bf16 v[16:19], v[172:175], v[44:47], v[16:19]\n"
  "v_exp_f32 v7, v7\n"
  "v_mfma_f32_16x16x32_bf16 v[16:19], v[176:179], v[48:51], v[16:19]\n"
  "v_cvt_pk_bf16_f32 v2, v4, v5\n"
  "v_cvt_pk_bf16_f32 v3, v6, v7\n"
  "v_mfma_f32_16x16x32_bf16 v[16:19], v[180:183], v[52:55], v[16:19]\n"
  "ds_write_b128 v80, v[0:3] offset:4096\n"
  "v_pk_mul_f32 v[10:11], v[10:11], s[60:61] op_sel_hi:[1,0]\n"
  "v_pk_mul_f32 v[12:13], v[12:13], s[60:61] op_sel_hi:[1,0]\n"
  "v_pk_mul_f32 v[14:15], v[14:15], s[60:61] op_sel_hi:[1,0]\n"
  "v_mfma_f32_16x16x32_bf16 v[20:23], v[204:207], v[52:55], v[92:95]\n"
  "v_exp_f32 v8, v8\n"
  "v_mfma_f32_16x16x32_bf16 v[20:23], v[200:203], v[48:51], v[20:23]\n"
  "v_exp_f32 v9, v9\n"
  "v_mfma_f32_16x16x32_bf16 v[20:23], v[196:199], v[44:47], v[20:23]\n"
  "v_exp_f32 v10, v10\n"
  "v_mfma_f32_16x16x32_bf16 v[20:23], v[192:195], v[40:43], v[20:23]\n"
  "v_exp_f32 v11, v11\n"
  "v_mfma_f32_16x16x32_bf16 v[20:23], v[188:191], v[36:39], v[20:23]\n"
  "v_cvt_pk_bf16_f32 v8, v8, v9\n"
  "v_cvt_pk_bf16_f32 v9, v10, v11\n"
  "v_mfma_f32_16x16x32_bf16 v[20:23], v[184:187], v[32:35], v[20:23]\n"
  "v_mul_f32 v16, s60, v16\n"
  "v_mul_f32 v17, s60, v17\n"
  "v_mfma_f32_16x16x32_bf16 v[24:27], v[208:211], v[32:35], v[92:95]\n"
  "v_exp_f32 v12, v12\n"
  "v_mfma_f32_16x16x32_bf16 v[24:27], v[212:215], v[36:39], v[24:27]\n"
  "v_exp_f32 v13, v13\n"
  "v_mfma_f32_16x16x32_bf16 v[24:27], v[216:219], v[40:43], v[24:27]\n"
  "v_exp_f32 v14, v14\n"
  "v_mfma_f32_16x16x32_bf16 v[24:27], v[220:223], v[44:47], v[24:27]\n"
  "v_exp_f32 v15, v15\n"
  "v_mfma_f32_16x16x32_bf16 v[24:27], v[224:227], v[48:51], v[24:27]\n"
  "v_cvt_pk_bf16_f32 v10, v12, v13\n"
  "v_cvt_pk_bf16_f32 v11, v14, v15\n"
  "v_mfma_f32_16x16x32_bf16 v[24:27], v[228:231], v[52:55], v[24:27]\n"
  "ds_write_b128 v80, v[8:11] offset:5120\n"
  "v_pk_mul_f32 v[18:19], v[18:19], s[60:61] op_sel_hi:[1,0]\n"
  "v_pk_mul_f32 v[20:21], v[20:21], s[60:61] op_sel_hi:[1,0]\n"
  "v_pk_mul_f32 v[22:23], v[22:23], s[60:61] op_sel_hi:[1,0]\n"
  "v_mfma_f32_16x16x32_bf16 v[28:31], v[252:255], v[52:55], v[92:95]\n"
  "v_exp_f32 v16, v16\n"
  "v_mfma_f32_16x16x32_bf16 v[28:31], v[248:251], v[48:51], v[28:31]\n"
  "v_exp_f32 v17, v17\n"
  "v_mfma_f32_16x16x32_bf16 v[28:31], v[244:247], v[44:47], v[28:31]\n"
  "v_exp_f32 v18, v18\n"
  "v_mfma_f32_16x16x32_bf16 v[28:31], v[240:243], v[40:43], v[28:31]\n"
  "s_sub_u32 s56, s56, 1\n"
  "v_exp_f32_e64 v19, v19\n"
  "v_mfma_f32_16x16x32_bf16 v[28:31], v[236:239], v[36:39], v[28:31]\n"
  "v_cvt_pk_bf16_f32 v16, v16, v17\n"
  "v_cvt_pk_bf16_f32 v17, v18, v19\n"
  "v_mfma_f32_16x16x32_bf16 v[28:31], v[232:235], v[32:35], v[28:31]\n"
  "s_waitcnt lgkmcnt(3)\n"
  "s_branch A_H1_%=\n"
  ".p2align 3\n"
  "A_X0_%=:\n"
  "s_mov_b32 s52, s53\n"
  "s_mov_b32 s53, s54\n"
  "s_mov_b32 s54, s55\n"
  "s_cmp_eq_u32 s52, 0\n"
  "s_cbranch_scc1 A_LAST_%=\n"
  "s_lshr_b32 s62, s52, 16\n"
  "s_lshl1_add_u32 s62, s62, s65\n"
  "s_mul_i32 s57, s62, 98304\n"
  "s_and_b32 s56, s52, 0xff\n"
  "s_cmp_lg_u32 s53, 0\n"
  "s_cselect_b32 s62, 1, 0\n"
  "s_sub_u32 s56, s56, s62\n"
  "s_add_u32 s57, s57, 18432\n"
  "v_mfma_f32_16x16x32_bf16 v[0:3], a[0:3], v[32:35], v[92:95]\n"
  "s_add_u32 s66, s66, 6144\n"
  "s_cmp_eq_u32 s66, 102400\n"
  "s_cselect_b32 s66, 65536, s66\n"
  "v_exp_f32 v20, v20\n"
  "v_mfma_f32_16x16x32_bf16 v[0:3], a[4:7], v[36:39], v[0:3]\n"
  "v_exp_f32 v21, v21\n"
  "v_mfma_f32_16x16x32_bf16 v[0:3], a[8:11], v[40:43], v[0:3]\n"
  "ds_read_b128 a[0:3], v87 offset:0\n"
  "ds_read_b128 a[4:7], v88 offset:0\n"
  "v_exp_f32 v22, v22\n"
  "v_mfma_f32_16x16x32_bf16 v[0:3], a[12:15], v[44:47], v[0:3]\n"
  "v_exp_f32 v23, v23\n"
  "v_mfma_f32_16x16x32_bf16 v[0:3], a[16:19], v[48:51], v[0:3]\n"
  "v_cvt_pk_bf16_f32 v18, v20, v21\n"
  "v_cvt_pk_bf16_f32 v19, v22, v23\n"
  "v_mfma_f32_16x16x32_bf16 v[0:3], a[20:23], v[52:55], v[0:3]\n"
  "ds_read_b128 a[8:11], v87 offset:2048\n"
  "ds_read_b128 a[12:15], v88 offset:2048\n"
  "ds_write_b128 v80, v[16:19] offset:6144\n"
  "v_pk_mul_f32 v[24:25], v[24:25], s[60:61] op_sel_hi:[1,0]\n"
  "v_pk_mul_f32 v[26:27], v[26:27], s[60:61] op_sel_hi:[1,0]\n"
  "v_pk_mul_f32 v[28:29], v[28:29], s[60:61] op_sel_hi:[1,0]\n"
  "v_pk_mul_f32 v[30:31], v[30:31], s[60:61] op_sel_hi:[1,0]\n"
  "v_mfma_f32_16x16x32_bf16 v[4:7], a[44:47], v[52:55], v[92:95]\n"
  "v_exp_f32 v24, v24\n"
  "v_mfma_f32_16x16x32_bf16 v[4:7], a[40:43], v[48:51], v[4:7]\n"
  "s_add_u32 m0, s59, -128\n"
  "v_exp_f32 v25, v25\n"
  "v_mfma_f32_16x16x32_bf16 v[4:7], a[36:39], v[44:47], v[4:7]\n"
  "s_waitcnt lgkmcnt(3)\n"
  "buffer_load_dwordx4 v112, s[40:43], s57 offen offset:128 nt lds\n"
  "buffer_load_dwordx4 v113, s[40:43], s57 offen offset:1152 nt lds\n"
  "v_exp_f32 v26, v26\n"
  "v_mfma_f32_16x16x32_bf16 v[4:7], a[32:35], v[40:43], v[4:7]\n"
  "ds_read_b128 a[16:19], v87 offset:4096\n"
  "ds_read_b128 a[20:23], v88 offset:4096\n"
  "v_exp_f32_e64 v27, v27\n"
  "v_mfma_f32_16x16x32_bf16 v[4:7], a[28:31], v[36:39], v[4:7]\n"
  "s_add_u32 m0, s59, 2048\n"
  "v_cvt_pk_bf16_f32 v24, v24, v25\n"
  "v_cvt_pk_bf16_f32 v25, v26, v27\n"
  "v_mfma_f32_16x16x32_bf16 v[4:7], a[24:27], v[32:35], v[4:7]\n"
  "s_waitcnt lgkmcnt(3)\n"
  "buffer_load_dwordx4 v112, s[40:43], s57 offen offset:0 nt lds\n"
  "buffer_load_dwordx4 v113, s[40:43], s57 offen offset:1024 nt lds\n"
  "s_add_u32 s57, s57, 6144\n"
  "v_add_u32 v81, s66, v83\n"
  "v_add_u32 v82, s66, v84\n"
  "v_mfma_f32_16x16x32_bf16 v[8:11], a[48:51], v[32:35], v[92:95]\n"
  "ds_read_b128 a[40:43], v87 offset:6144\n"
  "ds_read_b128 a[44:47], v88 offset:6144\n"
  "v_exp_f32 v28, v28\n"
  "v_mfma_f32_16x16x32_bf16 v[8:11], a[52:55], v[36:39], v[8:11]\n"
  "s_add_u32 m0, s59, 4096\n"
  "v_exp_f32 v29, v29\n"
  "v_mfma_f32_16x16x32_bf16 v[8:11], a[56:59], v[40:43], v[8:11]\n"
  "s_waitcnt lgkmcnt(2)\n"
  "buffer_load_dwordx4 v112, s[40:43], s57 offen offset:0 nt lds\n"
  "buffer_load_dwordx4 v113, s[40:43], s57 offen offset:1024 nt lds\n"
  "v_exp_f32 v30, v30\n"
  "v_mfma_f32_16x16x32_bf16 v[8:11], a[60:63], v[44:47], v[8:11]\n"
  "ds_read_b128 a[32:35], v87 offset:8192\n"
  "ds_read_b128 a[36:39], v88 offset:8192\n"
  "v_exp_f32 v31, v31\n"
  "v_mfma_f32_16x16x32_bf16 v[8:11], a[64:67], v[48:51], v[8:11]\n"
  "v_cvt_pk_bf16_f32 v26, v28, v29\n"
  "v_cvt_pk_bf16_f32 v27, v30, v31\n"
  "v_mfma_f32_16x16x32_bf16 v[8:11], a[68:71], v[52:55], v[8:11]\n"
  "s_barrier\n"
  "s_add_u32 m0, s59, 6016\n"
  "ds_write_b128 v80, v[24:27] offset:7168\n"
  "v_pk_mul_f32 v[0:1], v[0:1], s[60:61] op_sel_hi:[1,0]\n"
  "v_pk_mul_f32 v[2:3], v[2:3], s[60:61] op_sel_hi:[1,0]\n"
  "v_pk_mul_f32 v[4:5], v[4:5], s[60:61] op_sel_hi:[1,0]\n"
  "v_pk_mul_f32 v[6:7], v[6:7], s[60:61] op_sel_hi:[1,0]\n"
  "v_mfma_f32_16x16x32_bf16 v[12:15], a[92:95], v[52:55], v[92:95]\n"
  "s_waitcnt lgkmcnt(3)\n"
  "buffer_load_dwordx4 v112, s[40:43], s57 offen offset:128 nt lds\n"
  "buffer_load_dwordx4 v113, s[40:43], s57 offen offset:1152 nt lds\n"
  "v_exp_f32 v0, v0\n"
  "v_mfma_f32_16x16x32_bf16 v[12:15], a[88:91], v[48:51], v[12:15]\n"
  "ds_read_b128 a[24:27], v87 offset:10240\n"
  "ds_read_b128 a[28:31], v88 offset:10240\n"
  "v_exp_f32 v1, v1\n"
  "v_mfma_f32_16x16x32_bf16 v[12:15], a[84:87], v[44:47], v[12:15]\n"
  "s_add_u32 m0, s59, 7936\n"
  "v_exp_f32 v2, v2\n"
  "v_mfma_f32_16x16x32_bf16 v[12:15], a[80:83], v[40:43], v[12:15]\n"
  "s_waitcnt lgkmcnt(3)\n"
  "buffer_load_dwordx4 v112, s[40:43], s57 offen offset:256 nt lds\n"
  "buffer_load_dwordx4 v113, s[40:43], s57 offen offset:1280 nt lds\n"
  "s_add_u32 s57, s57, 6144\n"
  "v_exp_f32_e64 v3, v3\n"
  "v_mfma_f32_16x16x32_bf16 v[12:15], a[76:79], v[36:39], v[12:15]\n"
  "ds_read_b128 a[48:51], v87 offset:12288\n"
  "ds_read_b128 a[52:55], v88 offset:12288\n"
  "v_cvt_pk_bf16_f32 v0, v0, v1\n"
  "v_cvt_pk_bf16_f32 v1, v2, v3\n"
  "v_mfma_f32_16x16x32_bf16 v[12:15], a[72:75], v[32:35], v[12:15]\n"
  "v_xor_b32 v80, s88, v80\n"
  "v_mfma_f32_16x16x32_bf16 v[16:19], a[96:99], v[32:35], v[92:95]\n"
  "s_add_u32 m0, s59, 9984\n"
  "v_exp_f32 v4, v4\n"
  "v_mfma_f32_16x16x32_bf16 v[16:19], a[100:103], v[36:39], v[16:19]\n"
  "s_xor_b32 s89, s88, s89\n"
  "s_waitcnt lgkmcnt(2)\n"
  "buffer_load_dwordx4 v112, s[40:43], s57 offen offset:256 nt lds\n"
  "buffer_load_dwordx4 v113, s[40:43], s57 offen offset:1280 nt lds\n"
  "v_exp_f32 v5, v5\n"
  "v_mfma_f32_16x16x32_bf16 v[16:19], a[104:107], v[40:43], v[16:19]\n"
  "s_xor_b32 s88, s88, s89\n"
  "ds_read_b128 a[56:59], v87 offset:14336\n"
  "ds_read_b128 a[60:63], v88 offset:14336\n"
  "s_waitcnt vmcnt(10)\n"
  "ds_read_b128 a[80:83], v87 offset:0\n"
  "ds_read_b128 a[84:87], v88 offset:0\n"
  "v_exp_f32 v6, v6\n"
  "v_mfma_f32_16x16x32_bf16 v[16:19], a[108:111], v[44:47], v[16:19]\n"
  "s_add_u32 m0, s59, 12160\n"
  "v_exp_f32 v7, v7\n"
  "v_mfma_f32_16x16x32_bf16 v[16:19], a[112:115], v[48:51], v[16:19]\n"
  "s_waitcnt lgkmcnt(4)\n"
  "buffer_load_dwordx4 v112, s[40:43], s57 offen offset:128 nt lds\n"
  "buffer_load_dwordx4 v113, s[40:43], s57 offen offset:1152 nt lds\n"
  "v_cvt_pk_bf16_f32 v2, v4, v5\n"
  "v_cvt_pk_bf16_f32 v3, v6, v7\n"
  "v_mfma_f32_16x16x32_bf16 v[16:19], a[116:119], v[52:55], v[16:19]\n"
  "ds_read_b128 a[64:67], v87 offset:16384\n"
  "ds_read_b128 a[68:71], v88 offset:16384\n"
  "s_waitcnt vmcnt(10)\n"
  "ds_read_b128 a[72:75], v87 offset:2048\n"
  "ds_read_b128 a[76:79], v88 offset:2048\n"
  "ds_write_b128 v80, v[0:3] offset:0\n"
  "v_pk_mul_f32 v[8:9], v[8:9], s[60:61] op_sel_hi:[1,0]\n"
  "v_pk_mul_f32 v[10:11], v[10:11], s[60:61] op_sel_hi:[1,0]\n"
  "v_pk_mul_f32 v[12:13], v[12:13], s[60:61] op_sel_hi:[1,0]\n"
  "v_pk_mul_f32 v[14:15], v[14:15], s[60:61] op_sel_hi:[1,0]\n"
  "v_mfma_f32_16x16x32_bf16 v[20:23], a[140:143], v[52:55], v[92:95]\n"
  "s_add_u32 m0, s59, 14336\n"
  "v_exp_f32 v8, v8\n"
  "v_mfma_f32_16x16x32_bf16 v[20:23], a[136:139], v[48:51], v[20:23]\n"
  "s_waitcnt lgkmcnt(7)\n"
  "buffer_load_dwordx4 v112, s[40:43], s57 offen offset:0 nt lds\n"
  "buffer_load_dwordx4 v113, s[40:43], s57 offen offset:1024 nt lds\n"
  "s_add_u32 s57, s57, 6144\n"
  "v_exp_f32 v9, v9\n"
  "v_mfma_f32_16x16x32_bf16 v[20:23], a[132:135], v[44:47], v[20:23]\n"
  "ds_read_b128 a[88:91], v87 offset:18432\n"
  "ds_read_b128 a[92:95], v88 offset:18432\n"
  "s_waitcnt vmcnt(10)\n"
  "ds_read_b128 a[96:99], v87 offset:4096\n"
  "ds_read_b128 a[100:103], v88 offset:4096\n"
  "v_exp_f32 v10, v10\n"
  "v_mfma_f32_16x16x32_bf16 v[20:23], a[128:131], v[40:43], v[20:23]\n"
  "v_exp_f32_e64 v11, v11\n"
  "v_mfma_f32_16x16x32_bf16 v[20:23], a[124:127], v[36:39], v[20:23]\n"
  "s_add_u32 m0, s59, 16384\n"
  "v_cvt_pk_bf16_f32 v8, v8, v9\n"
  "v_cvt_pk_bf16_f32 v9, v10, v11\n"
  "v_mfma_f32_16x16x32_bf16 v[20:23], a[120:123], v[32:35], v[20:23]\n"
  "s_waitcnt lgkmcnt(7)\n"
  "buffer_load_dwordx4 v112, s[40:43], s57 offen offset:0 nt lds\n"
  "buffer_load_dwordx4 v113, s[40:43], s57 offen offset:1024 nt lds\n"
  "ds_read_b128 v[56:59], v81 offset:0\n"
  "ds_read_b128 v[60:63], v82 offset:0\n"
  "v_mfma_f32_16x16x32_bf16 v[24:27], a[144:147], v[32:35], v[92:95]\n"
  "s_waitcnt vmcnt(10)\n"
  "ds_read_b128 a[104:107], v87 offset:6144\n"
  "ds_read_b128 a[108:111], v88 offset:6144\n"
  "v_exp_f32 v12, v12\n"
  "v_mfma_f32_16x16x32_bf16 v[24:27], a[148:151], v[36:39], v[24:27]\n"
  "s_add_u32 m0, s59, 18304\n"
  "v_exp_f32 v13, v13\n"
  "v_mfma_f32_16x16x32_bf16 v[24:27], a[152:155], v[40:43], v[24:27]\n"
  "s_waitcnt lgkmcnt(6)\n"
  "buffer_load_dwordx4 v112, s[40:43], s57 offen offset:128 nt lds\n"
  "buffer_load_dwordx4 v113, s[40:43], s57 offen offset:1152 nt lds\n"
  "v_exp_f32 v14, v14\n"
  "v_mfma_f32_16x16x32_bf16 v[24:27], a[156:159], v[44:47], v[24:27]\n"
  "s_waitcnt vmcnt(10)\n"
  "ds_read_b128 a[112:115], v87 offset:8192\n"
  "ds_read_b128 a[116:119], v88 offset:8192\n"
  "v_exp_f32 v15, v15\n"
  "v_mfma_f32_16x16x32_bf16 v[24:27], a[160:163], v[48:51], v[24:27]\n"
  "v_cvt_pk_bf16_f32 v10, v12, v13\n"
  "v_cvt_pk_bf16_f32 v11, v14, v15\n"
  "v_mfma_f32_16x16x32_bf16 v[24:27], a[164:167], v[52:55], v[24:27]\n"
  "s_add_u32 m0, s59, -256\n"
  "ds_write_b128 v80, v[8:11] offset:1024\n"
  "v_pk_mul_f32 v[16:17], v[16:17], s[60:61] op_sel_hi:[1,0]\n"
  "v_pk_mul_f32 v[18:19], v[18:19], s[60:61] op_sel_hi:[1,0]\n"
  "v_pk_mul_f32 v[20:21], v[20:21], s[60:61] op_sel_hi:[1,0]\n"
  "v_pk_mul_f32 v[22:23], v[22:23], s[60:61] op_sel_hi:[1,0]\n"
  "v_mfma_f32_16x16x32_bf16 v[28:31], a[188:191], v[52:55], v[92:95]\n"
  "buffer_load_dwordx4 v112, s[40:43], s57 offen offset:256 nt lds\n"
  "buffer_load_dwordx4 v113, s[40:43], s57 offen offset:1280 nt lds\n"
  "s_add_u32 s57, s57, 6144\n"
  "v_exp_f32 v16, v16\n"
  "v_mfma_f32_16x16x32_bf16 v[28:31], a[184:187], v[48:51], v[28:31]\n"
  "s_waitcnt vmcnt(10)\n"
  "ds_read_b128 a[136:139], v87 offset:10240\n"
  "ds_read_b128 a[140:143], v88 offset:10240\n"
  "v_exp_f32 v17, v17\n"
  "v_mfma_f32_16x16x32_bf16 v[28:31], a[180:183], v[44:47], v[28:31]\n"
  "s_add_u32 m0, s59, 1792\n"
  "v_exp_f32 v18, v18\n"
  "v_mfma_f32_16x16x32_bf16 v[28:31], a[176:179], v[40:43], v[28:31]\n"
  "buffer_load_dwordx4 v112, s[40:43], s57 offen offset:256 nt lds\n"
  "buffer_load_dwordx4 v113, s[40:43], s57 offen offset:1280 nt lds\n"
  "v_exp_f32 v19, v19\n"
  "v_mfma_f32_16x16x32_bf16 v[28:31], a[172:175], v[36:39], v[28:31]\n"
  "s_waitcnt vmcnt(10)\n"
  "ds_read_b128 a[128:131], v87 offset:12288\n"
  "ds_read_b128 a[132:135], v88 offset:12288\n"
  "v_cvt_pk_bf16_f32 v16, v16, v17\n"
  "v_cvt_pk_bf16_f32 v17, v18, v19\n"
  "v_mfma_f32_16x16x32_bf16 v[28:31], a[168:171], v[32:35], v[28:31]\n"
  "s_add_u32 m0, s59, 3968\n"
  "ds_read_b128 v[64:67], v81 offset:2048\n"
  "ds_read_b128 v[68:71], v82 offset:2048\n"
  "v_mfma_f32_16x16x32_bf16 v[0:3], a[192:195], v[32:35], v[92:95]\n"
  "s_waitcnt lgkmcnt(13)\n"
  "buffer_load_dwordx4 v112, s[40:43], s57 offen offset:128 nt lds\n"
  "buffer_load_dwordx4 v113, s[40:43], s57 offen offset:1152 nt lds\n"
  "v_exp_f32 v20, v20\n"
  "v_mfma_f32_16x16x32_bf16 v[0:3], a[196:199], v[36:39], v[0:3]\n"
  "s_waitcnt vmcnt(10)\n"
  "ds_read_b128 a[120:123], v87 offset:14336\n"
  "ds_read_b128 a[124:127], v88 offset:14336\n"
  "v_exp_f32 v21, v21\n"
  "v_mfma_f32_16x16x32_bf16 v[0:3], a[200:203], v[40:43], v[0:3]\n"
  "v_exp_f32 v22, v22\n"
  "v_mfma_f32_16x16x32_bf16 v[0:3], a[204:207], v[44:47], v[0:3]\n"
  "s_add_u32 m0, s59, 6144\n"
  "v_exp_f32_e64 v23, v23\n"
  "v_mfma_f32_16x16x32_bf16 v[0:3], a[208:211], v[48:51], v[0:3]\n"
  "s_waitcnt lgkmcnt(11)\n"
  "buffer_load_dwordx4 v112, s[40:43], s57 offen offset:0 nt lds\n"
  "buffer_load_dwordx4 v113, s[40:43], s57 offen offset:1024 nt lds\n"
  "s_add_u32 s57, s57, 6144\n"
  "v_cvt_pk_bf16_f32 v18, v20, v21\n"
  "v_cvt_pk_bf16_f32 v19, v22, v23\n"
  "v_mfma_f32_16x16x32_bf16 v[0:3], a[212:215], v[52:55], v[0:3]\n"
  "s_waitcnt vmcnt(10)\n"
  "ds_read_b128 a[144:147], v87 offset:16384\n"
  "ds_read_b128 a[148:151], v88 offset:16384\n"
  "ds_write_b128 v80, v[16:19] offset:2048\n"
  "v_pk_mul_f32 v[24:25], v[24:25], s[60:61] op_sel_hi:[1,0]\n"
  "v_pk_mul_f32 v[26:27], v[26:27], s[60:61] op_sel_hi:[1,0]\n"
  "v_pk_mul_f32 v[28:29], v[28:29], s[60:61] op_sel_hi:[1,0]\n"
  "v_pk_mul_f32 v[30:31], v[30:31], s[60:61] op_sel_hi:[1,0]\n"
  "v_mfma_f32_16x16x32_bf16 v[4:7], a[236:239], v[52:55], v[92:95]\n"
  "s_add_u32 m0, s59, 8192\n"
  "v_exp_f32 v24, v24\n"
  "v_mfma_f32_16x16x32_bf16 v[4:7], a[232:235], v[48:51], v[4:7]\n"
  "s_waitcnt lgkmcnt(12)\n"
  "buffer_load_dwordx4 v112, s[40:43], s57 offen offset:0 nt lds\n"
  "buffer_load_dwordx4 v113, s[40:43], s57 offen offset:1024 nt lds\n"
  "v_exp_f32 v25, v25\n"
  "v_mfma_f32_16x16x32_bf16 v[4:7], a[228:231], v[44:47], v[4:7]\n"
  "s_waitcnt vmcnt(10)\n"
  "ds_read_b128 a[152:155], v87 offset:18432\n"
  "ds_read_b128 a[156:159], v88 offset:18432\n"
  "v_exp_f32 v26, v26\n"
  "v_mfma_f32_16x16x32_bf16 v[4:7], a[224:227], v[40:43], v[4:7]\n"
  "v_exp_f32_e64 v27, v27\n"
  "v_mfma_f32_16x16x32_bf16 v[4:7], a[220:223], v[36:39], v[4:7]\n"
  "s_add_u32 m0, s59, 10112\n"
  "v_cvt_pk_bf16_f32 v24, v24, v25\n"
  "v_cvt_pk_bf16_f32 v25, v26, v27\n"
  "v_mfma_f32_16x16x32_bf16 v[4:7], a[216:219], v[32:35], v[4:7]\n"
  "s_waitcnt lgkmcnt(11)\n"
  "buffer_load_dwordx4 v112, s[40:43], s57 offen offset:128 nt lds\n"
  "buffer_load_dwordx4 v113, s[40:43], s57 offen offset:1152 nt lds\n"
  "ds_read_b128 v[72:75], v81 offset:4096\n"
  "ds_read_b128 v[76:79], v82 offset:4096\n"
  "v_mfma_f32_16x16x32_bf16 v[8:11], a[240:243], v[32:35], v[92:95]\n"
  "s_waitcnt vmcnt(10)\n"
  "ds_read_b128 a[160:163], v87 offset:0\n"
  "ds_read_b128 a[164:167], v88 offset:0\n"
  "v_exp_f32 v28, v28\n"
  "v_mfma_f32_16x16x32_bf16 v[8:11], a[244:247], v[36:39], v[8:11]\n"
  "s_add_u32 m0, s59, 12032\n"
  "v_exp_f32 v29, v29\n"
  "v_mfma_f32_16x16x32_bf16 v[8:11], a[248:251], v[40:43], v[8:11]\n"
  "s_waitcnt lgkmcnt(13)\n"
  "buffer_load_dwordx4 v112, s[40:43], s57 offen offset:256 nt lds\n"
  "buffer_load_dwordx4 v113, s[40:43], s57 offen offset:1280 nt lds\n"
  "s_add_u32 s57, s57, 6144\n"
  "v_exp_f32 v30, v30\n"
  "v_mfma_f32_16x16x32_bf16 v[8:11], a[252:255], v[44:47], v[8:11]\n"
  "s_waitcnt vmcnt(10)\n"
  "ds_read_b128 a[184:187], v87 offset:2048\n"
  "ds_read_b128 a[188:191], v88 offset:2048\n"
  "v_exp_f32 v31, v31\n"
  "v_mfma_f32_16x16x32_bf16 v[8:11], v[128:131], v[48:51], v[8:11]\n"
  "s_add_u32 m0, s59, 14080\n"
  "v_cvt_pk_bf16_f32 v26, v28, v29\n"
  "v_cvt_pk_bf16_f32 v27, v30, v31\n"
  "v_mfma_f32_16x16x32_bf16 v[8:11], v[132:135], v[52:55], v[8:11]\n"
  "s_waitcnt lgkmcnt(11)\n"
  "buffer_load_dwordx4 v112, s[40:43], s57 offen offset:256 nt lds\n"
  "buffer_load_dwordx4 v113, s[40:43], s57 offen offset:1280 nt lds\n"
  "ds_write_b128 v80, v[24:27] offset:3072\n"
  "v_pk_mul_f32 v[0:1], v[0:1], s[60:61] op_sel_hi:[1,0]\n"
  "v_pk_mul_f32 v[2:3], v[2:3], s[60:61] op_sel_hi:[1,0]\n"
  "v_pk_mul_f32 v[4:5], v[4:5], s[60:61] op_sel_hi:[1,0]\n"
  "v_pk_mul_f32 v[6:7], v[6:7], s[60:61] op_sel_hi:[1,0]\n"
  "v_mfma_f32_16x16x32_bf16 v[12:15], v[156:159], v[52:55], v[92:95]\n"
  "s_waitcnt vmcnt(10)\n"
  "ds_read_b128 a[176:179], v87 offset:4096\n"
  "ds_read_b128 a[180:183], v88 offset:4096\n"
  "v_exp_f32 v0, v0\n"
  "v_mfma_f32_16x16x32_bf16 v[12:15], v[152:155], v[48:51], v[12:15]\n"
  "v_exp_f32 v1, v1\n"
  "v_mfma_f32_16x16x32_bf16 v[12:15], v[148:151], v[44:47], v[12:15]\n"
  "s_add_u32 m0, s59, 16256\n"
  "v_exp_f32 v2, v2\n"
  "v_mfma_f32_16x16x32_bf16 v[12:15], v[144:147], v[40:43], v[12:15]\n"
  "s_waitcnt lgkmcnt(12)\n"
  "buffer_load_dwordx4 v112, s[40:43], s57 offen offset:128 nt lds\n"
  "buffer_load_dwordx4 v113, s[40:43], s57 offen offset:1152 nt lds\n"
  "v_exp_f32 v3, v3\n"
  "v_mfma_f32_16x16x32_bf16 v[12:15], v[140:143], v[36:39], v[12:15]\n"
  "s_waitcnt vmcnt(10)\n"
  "ds_read_b128 a[168:171], v87 offset:6144\n"
  "ds_read_b128 a[172:175], v88 offset:6144\n"
  "v_cvt_pk_bf16_f32 v0, v0, v1\n"
  "v_cvt_pk_bf16_f32 v1, v2, v3\n"
  "v_mfma_f32_16x16x32_bf16 v[12:15], v[136:139], v[32:35], v[12:15]\n"
  "s_add_u32 m0, s59, 18432\n"
  "v_mul_f32 v8, s60, v8\n"
  "v_mul_f32 v9, s60, v9\n"
  "v_mfma_f32_16x16x32_bf16 v[16:19], v[160:163], v[32:35], v[92:95]\n"
  "s_waitcnt lgkmcnt(11)\n"
  "buffer_load_dwordx4 v112, s[40:43], s57 offen offset:0 nt lds\n"
  "buffer_load_dwordx4 v113, s[40:43], s57 offen offset:1024 nt lds\n"
  "s_add_u32 s57, s57, 6144\n"
  "v_exp_f32 v4, v4\n"
  "v_mfma_f32_16x16x32_bf16 v[16:19], v[164:167], v[36:39], v[16:19]\n"
  "s_waitcnt vmcnt(10)\n"
  "ds_read_b128 a[192:195], v87 offset:8192\n"
  "ds_read_b128 a[196:199], v88 offset:8192\n"
  "v_exp_f32 v5, v5\n"
  "v_mfma_f32_16x16x32_bf16 v[16:19], v[168:171], v[40:43], v[16:19]\n"
  "v_exp_f32 v6, v6\n"
  "v_mfma_f32_16x16x32_bf16 v[16:19], v[172:175], v[44:47], v[16:19]\n"
  "s_add_u32 m0, s59, 0\n"
  "v_exp_f32 v7, v7\n"
  "v_mfma_f32_16x16x32_bf16 v[16:19], v[176:179], v[48:51], v[16:19]\n"
  "s_waitcnt lgkmcnt(9)\n"
  "buffer_load_dwordx4 v112, s[40:43], s57 offen offset:0 nt lds\n"
  "buffer_load_dwordx4 v113, s[40:43], s57 offen offset:1024 nt lds\n"
  "v_cvt_pk_bf16_f32 v2, v4, v5\n"
  "v_cvt_pk_bf16_f32 v3, v6, v7\n"
  "v_mfma_f32_16x16x32_bf16 v[16:19], v[180:183], v[52:55], v[16:19]\n"
  "s_waitcnt vmcnt(10)\n"
  "ds_read_b128 a[200:203], v87 offset:10240\n"
  "ds_read_b128 a[204:207], v88 offset:10240\n"
  "ds_write_b128 v80, v[0:3] offset:4096\n"
  "v_pk_mul_f32 v[10:11], v[10:11], s[60:61] op_sel_hi:[1,0]\n"
  "v_pk_mul_f32 v[12:13], v[12:13], s[60:61] op_sel_hi:[1,0]\n"
  "v_pk_mul_f32 v[14:15], v[14:15], s[60:61] op_sel_hi:[1,0]\n"
  "v_mfma_f32_16x16x32_bf16 v[20:23], v[204:207], v[52:55], v[92:95]\n"
  "s_add_u32 m0, s59, 1920\n"
  "v_exp_f32 v8, v8\n"
  "v_mfma_f32_16x16x32_bf16 v[20:23], v[200:203], v[48:51], v[20:23]\n"
  "s_waitcnt lgkmcnt(10)\n"
  "buffer_load_dwordx4 v112, s[40:43], s57 offen offset:128 nt lds\n"
  "buffer_load_dwordx4 v113, s[40:43], s57 offen offset:1152 nt lds\n"
  "v_exp_f32 v9, v9\n"
  "v_mfma_f32_16x16x32_bf16 v[20:23], v[196:199], v[44:47], v[20:23]\n"
  "s_waitcnt vmcnt(10)\n"
  "ds_read_b128 a[208:211], v87 offset:12288\n"
  "ds_read_b128 a[212:215], v88 offset:12288\n"
  "v_exp_f32 v10, v10\n"
  "v_mfma_f32_16x16x32_bf16 v[20:23], v[192:195], v[40:43], v[20:23]\n"
  "s_add_u32 m0, s59, 3840\n"
  "v_exp_f32 v11, v11\n"
  "v_mfma_f32_16x16x32_bf16 v[20:23], v[188:191], v[36:39], v[20:23]\n"
  "s_waitcnt lgkmcnt(9)\n"
  "buffer_load_dwordx4 v112, s[40:43], s57 offen offset:256 nt lds\n"
  "buffer_load_dwordx4 v113, s[40:43], s57 offen offset:1280 nt lds\n"
  "s_add_u32 s57, s57, 6144\n"
  "v_cvt_pk_bf16_f32 v8, v8, v9\n"
  "v_cvt_pk_bf16_f32 v9, v10, v11\n"
  "v_mfma_f32_16x16x32_bf16 v[20:23], v[184:187], v[32:35], v[20:23]\n"
  "s_waitcnt vmcnt(10)\n"
  "ds_read_b128 a[232:235], v87 offset:14336\n"
  "ds_read_b128 a[236:239], v88 offset:14336\n"
  "v_mul_f32 v16, s60, v16\n"
  "v_mul_f32 v17, s60, v17\n"
  "v_mfma_f32_16x16x32_bf16 v[24:27], v[208:211], v[32:35], v[92:95]\n"
  "v_exp_f32 v12, v12\n"
  "v_mfma_f32_16x16x32_bf16 v[24:27], v[212:215], v[36:39], v[24:27]\n"
  "s_add_u32 m0, s59, 5888\n"
  "v_exp_f32 v13, v13\n"
  "v_mfma_f32_16x16x32_bf16 v[24:27], v[216:219], v[40:43], v[24:27]\n"
  "s_waitcnt lgkmcnt(9)\n"
  "buffer_load_dwordx4 v112, s[40:43], s57 offen offset:256 nt lds\n"
  "buffer_load_dwordx4 v113, s[40:43], s57 offen offset:1280 nt lds\n"
  "v_exp_f32 v14, v14\n"
  "v_mfma_f32_16x16x32_bf16 v[24:27], v[220:223], v[44:47], v[24:27]\n"
  "s_waitcnt vmcnt(10)\n"
  "ds_read_b128 a[224:227], v87 offset:16384\n"
  "ds_read_b128 a[228:231], v88 offset:16384\n"
  "v_exp_f32_e64 v15, v15\n"
  "v_mfma_f32_16x16x32_bf16 v[24:27], v[224:227], v[48:51], v[24:27]\n"
  "s_add_u32 m0, s59, 8064\n"
  "v_cvt_pk_bf16_f32 v10, v12, v13\n"
  "v_cvt_pk_bf16_f32 v11, v14, v15\n"
  "v_mfma_f32_16x16x32_bf16 v[24:27], v[228:231], v[52:55], v[24:27]\n"
  "s_barrier\n"
  "s_waitcnt lgkmcnt(9)\n"
  "buffer_load_dwordx4 v112, s[40:43], s57 offen offset:128 nt lds\n"
  "buffer_load_dwordx4 v113, s[40:43], s57 offen offset:1152 nt lds\n"
  "ds_write_b128 v80, v[8:11] offset:5120\n"
  "v_pk_mul_f32 v[18:19], v[18:19], s[60:61] op_sel_hi:[1,0]\n"
  "v_pk_mul_f32 v[20:21], v[20:21], s[60:61] op_sel_hi:[1,0]\n"
  "v_pk_mul_f32 v[22:23], v[22:23], s[60:61] op_sel_hi:[1,0]\n"
  "v_mfma_f32_16x16x32_bf16 v[28:31], v[252:255], v[52:55], v[92:95]\n"
  "s_waitcnt vmcnt(10)\n"
  "ds_read_b128 a[216:219], v87 offset:18432\n"
  "ds_read_b128 a[220:223], v88 offset:18432\n"
  "v_exp_f32 v16, v16\n"
  "v_mfma_f32_16x16x32_bf16 v[28:31], v[248:251], v[48:51], v[28:31]\n"
  "v_exp_f32 v17, v17\n"
  "v_mfma_f32_16x16x32_bf16 v[28:31], v[244:247], v[44:47], v[28:31]\n"
  "s_add_u32 m0, s59, 10240\n"
  "v_exp_f32 v18, v18\n"
  "v_mfma_f32_16x16x32_bf16 v[28:31], v[240:243], v[40:43], v[28:31]\n"
  "s_waitcnt lgkmcnt(10)\n"
  "buffer_load_dwordx4 v112, s[40:43], s57 offen offset:0 nt lds\n"
  "buffer_load_dwordx4 v113, s[40:43], s57 offen offset:1024 nt lds\n"
  "s_add_u32 s57, s57, 6144\n"
  "v_exp_f32_e64 v19, v19\n"
  "v_mfma_f32_16x16x32_bf16 v[28:31], v[236:239], v[36:39], v[28:31]\n"
  "s_waitcnt vmcnt(10)\n"
  "ds_read_b128 a[240:243], v87 offset:0\n"
  "ds_read_b128 a[244:247], v88 offset:0\n"
  "v_cvt_pk_bf16_f32 v16, v16, v17\n"
  "v_cvt_pk_bf16_f32 v17, v18, v19\n"
  "v_mfma_f32_16x16x32_bf16 v[28:31], v[232:235], v[32:35], v[28:31]\n"
  "s_add_u32 m0, s59, 12288\n"
  ".p2align 3\n"
  "v_mfma_f32_16x16x32_bf16 v[0:3], a[0:3], v[56:59], v[92:95]\n"
  "s_add_u32 s66, s66, 6144\n"
  "s_cmp_eq_u32 s66, 102400\n"
  "s_cselect_b32 s66, 65536, s66\n"
  "s_waitcnt lgkmcnt(9)\n"
  "buffer_load_dwordx4 v112, s[40:43], s57 offen offset:0 nt lds\n"
  "buffer_load_dwordx4 v113, s[40:43], s57 offen offset:1024 nt lds\n"
  "v_exp_f32 v20, v20\n"
  "v_mfma_f32_16x16x32_bf16 v[0:3], a[4:7], v[60:63], v[0:3]\n"
  "s_waitcnt vmcnt(10)\n"
  "ds_read_b128 a[248:251], v87 offset:2048\n"
  "ds_read_b128 a[252:255], v88 offset:2048\n"
  "v_exp_f32 v21, v21\n"
  "v_mfma_f32_16x16x32_bf16 v[0:3], a[8:11], v[64:67], v[0:3]\n"
  "s_add_u32 m0, s59, 14208\n"
  "v_exp_f32 v22, v22\n"
  "v_mfma_f32_16x16x32_bf16 v[0:3], a[12:15], v[68:71], v[0:3]\n"
  "s_waitcnt lgkmcnt(9)\n"
  "buffer_load_dwordx4 v112, s[40:43], s57 offen offset:128 nt lds\n"
  "buffer_load_dwordx4 v113, s[40:43], s57 offen offset:1152 nt lds\n"
  "v_exp_f32 v23, v23\n"
  "v_mfma_f32_16x16x32_bf16 v[0:3], a[16:19], v[72:75], v[0:3]\n"
  "s_waitcnt vmcnt(10)\n"
  "ds_read_b128 v[128:131], v87 offset:4096\n"
  "ds_read_b128 v[132:135], v88 offset:4096\n"
  "v_cvt_pk_bf16_f32 v18, v20, v21\n"
  "v_cvt_pk_bf16_f32 v19, v22, v23\n"
  "v_mfma_f32_16x16x32_bf16 v[0:3], a[20:23], v[76:79], v[0:3]\n"
  "ds_write_b128 v80, v[16:19] offset:6144\n"
  "v_pk_mul_f32 v[24:25], v[24:25], s[60:61] op_sel_hi:[1,0]\n"
  "v_pk_mul_f32 v[26:27], v[26:27], s[60:61] op_sel_hi:[1,0]\n"
  "v_pk_mul_f32 v[28:29], v[28:29], s[60:61] op_sel_hi:[1,0]\n"
  "v_pk_mul_f32 v[30:31], v[30:31], s[60:61] op_sel_hi:[1,0]\n"
  "v_mfma_f32_16x16x32_bf16 v[4:7], a[44:47], v[76:79], v[92:95]\n"
  "s_add_u32 m0, s59, 16128\n"
  "v_exp_f32 v24, v24\n"
  "v_mfma_f32_16x16x32_bf16 v[4:7], a[40:43], v[72:75], v[4:7]\n"
  "s_waitcnt lgkmcnt(10)\n"
  "buffer_load_dwordx4 v112, s[40:43], s57 offen offset:256 nt lds\n"
  "buffer_load_dwordx4 v113, s[40:43], s57 offen offset:1280 nt lds\n"
  "s_add_u32 s57, s57, 6144\n"
  "v_exp_f32 v25, v25\n"
  "v_mfma_f32_16x16x32_bf16 v[4:7], a[36:39], v[68:71], v[4:7]\n"
  "s_waitcnt vmcnt(10)\n"
  "ds_read_b128 v[152:155], v87 offset:6144\n"
  "ds_read_b128 v[156:159], v88 offset:6144\n"
  "v_exp_f32 v26, v26\n"
  "v_mfma_f32_16x16x32_bf16 v[4:7], a[32:35], v[64:67], v[4:7]\n"
  "s_add_u32 m0, s59, 18176\n"
  "v_exp_f32_e64 v27, v27\n"
  "v_mfma_f32_16x16x32_bf16 v[4:7], a[28:31], v[60:63], v[4:7]\n"
  "s_waitcnt lgkmcnt(9)\n"
  "buffer_load_dwordx4 v112, s[40:43], s57 offen offset:256 nt lds\n"
  "buffer_load_dwordx4 v113, s[40:43], s57 offen offset:1280 nt lds\n"
  "v_cvt_pk_bf16_f32 v24, v24, v25\n"
  "v_cvt_pk_bf16_f32 v25, v26, v27\n"
  "v_mfma_f32_16x16x32_bf16 v[4:7], a[24:27], v[56:59], v[4:7]\n"
  "s_waitcnt vmcnt(10)\n"
  "ds_read_b128 v[144:147], v87 offset:8192\n"
  "ds_read_b128 v[148:151], v88 offset:8192\n"
  "v_add_u32 v81, s66, v83\n"
  "v_add_u32 v82, s66, v84\n"
  "v_mfma_f32_16x16x32_bf16 v[8:11], a[48:51], v[56:59], v[92:95]\n"
  "v_exp_f32 v28, v28\n"
  "v_mfma_f32_16x16x32_bf16 v[8:11], a[52:55], v[60:63], v[8:11]\n"
  "s_add_u32 m0, s59, -128\n"
  "v_exp_f32 v29, v29\n"
  "v_mfma_f32_16x16x32_bf16 v[8:11], a[56:59], v[64:67], v[8:11]\n"
  "s_waitcnt lgkmcnt(9)\n"
  "buffer_load_dwordx4 v112, s[40:43], s57 offen offset:128 nt lds\n"
  "buffer_load_dwordx4 v113, s[40:43], s57 offen offset:1152 nt lds\n"
  "v_exp_f32 v30, v30\n"
  "v_mfma_f32_16x16x32_bf16 v[8:11], a[60:63], v[68:71], v[8:11]\n"
  "s_waitcnt vmcnt(10)\n"
  "ds_read_b128 v[136:139], v87 offset:10240\n"
  "ds_read_b128 v[140:143], v88 offset:10240\n"
  "v_exp_f32_e64 v31, v31\n"
  "v_mfma_f32_16x16x32_bf16 v[8:11], a[64:67], v[72:75], v[8:11]\n"
  "s_add_u32 m0, s59, 2048\n"
  "v_cvt_pk_bf16_f32 v26, v28, v29\n"
  "v_cvt_pk_bf16_f32 v27, v30, v31\n"
  "v_mfma_f32_16x16x32_bf16 v[8:11], a[68:71], v[76:79], v[8:11]\n"
  "s_waitcnt lgkmcnt(9)\n"
  "buffer_load_dwordx4 v112, s[40:43], s57 offen offset:0 nt lds\n"
  "buffer_load_dwordx4 v113, s[40:43], s57 offen offset:1024 nt lds\n"
  "s_add_u32 s57, s57, 6144\n"
  "ds_write_b128 v80, v[24:27] offset:7168\n"
  "v_pk_mul_f32 v[0:1], v[0:1], s[60:61] op_sel_hi:[1,0]\n"
  "v_pk_mul_f32 v[2:3], v[2:3], s[60:61] op_sel_hi:[1,0]\n"
  "v_pk_mul_f32 v[4:5], v[4:5], s[60:61] op_sel_hi:[1,0]\n"
  "v_pk_mul_f32 v[6:7], v[6:7], s[60:61] op_sel_hi:[1,0]\n"
  "v_mfma_f32_16x16x32_bf16 v[12:15], a[92:95], v[76:79], v[92:95]\n"
  "s_waitcnt vmcnt(10)\n"
  "ds_read_b128 v[160:163], v87 offset:12288\n"
  "ds_read_b128 v[164:167], v88 offset:12288\n"
  "v_exp_f32 v0, v0\n"
  "v_mfma_f32_16x16x32_bf16 v[12:15], a[88:91], v[72:75], v[12:15]\n"
  "s_add_u32 m0, s59, 4096\n"
  "v_exp_f32 v1, v1\n"
  "v_mfma_f32_16x16x32_bf16 v[12:15], a[84:87], v[68:71], v[12:15]\n"
  "s_waitcnt lgkmcnt(10)\n"
  "buffer_load_dwordx4 v112, s[40:43], s57 offen offset:0 nt lds\n"
  "buffer_load_dwordx4 v113, s[40:43], s57 offen offset:1024 nt lds\n"
  "v_exp_f32 v2, v2\n"
  "v_mfma_f32_16x16x32_bf16 v[12:15], a[80:83], v[64:67], v[12:15]\n"
  "s_waitcnt vmcnt(10)\n"
  "ds_read_b128 v[168:171], v87 offset:14336\n"
  "ds_read_b128 v[172:175], v88 offset:14336\n"
  "v_exp_f32 v3, v3\n"
  "v_mfma_f32_16x16x32_bf16 v[12:15], a[76:79], v[60:63], v[12:15]\n"
  "v_cvt_pk_bf16_f32 v0, v0, v1\n"
  "v_cvt_pk_bf16_f32 v1, v2, v3\n"
  "v_mfma_f32_16x16x32_bf16 v[12:15], a[72:75], v[56:59], v[12:15]\n"
  "s_add_u32 m0, s59, 6016\n"
  "v_xor_b32 v80, s88, v80\n"
  "v_mfma_f32_16x16x32_bf16 v[16:19], a[96:99], v[56:59], v[92:95]\n"
  "s_waitcnt lgkmcnt(9)\n"
  "buffer_load_dwordx4 v112, s[40:43], s57 offen offset:128 nt lds\n"
  "buffer_load_dwordx4 v113, s[40:43], s57 offen offset:1152 nt lds\n"
  "v_exp_f32 v4, v4\n"
  "v_mfma_f32_16x16x32_bf16 v[16:19], a[100:103], v[60:63], v[16:19]\n"
  "s_xor_b32 s89, s88, s89\n"
  "s_waitcnt vmcnt(10)\n"
  "ds_read_b128 v[176:179], v87 offset:16384\n"
  "ds_read_b128 v[180:183], v88 offset:16384\n"
  "v_exp_f32 v5, v5\n"
  "v_mfma_f32_16x16x32_bf16 v[16:19], a[104:107], v[64:67], v[16:19]\n"
  "s_xor_b32 s88, s88, s89\n"
  "s_add_u32 m0, s59, 7936\n"
  "v_exp_f32 v6, v6\n"
  "v_mfma_f32_16x16x32_bf16 v[16:19], a[108:111], v[68:71], v[16:19]\n"
  "s_waitcnt lgkmcnt(9)\n"
  "buffer_load_dwordx4 v112, s[40:43], s57 offen offset:256 nt lds\n"
  "buffer_load_dwordx4 v113, s[40:43], s57 offen offset:1280 nt lds\n"
  "s_add_u32 s57, s57, 6144\n"
  "v_exp_f32_e64 v7, v7\n"
  "v_mfma_f32_16x16x32_bf16 v[16:19], a[112:115], v[72:75], v[16:19]\n"
  "s_waitcnt vmcnt(10)\n"
  "ds_read_b128 v[200:203], v87 offset:18432\n"
  "ds_read_b128 v[204:207], v88 offset:18432\n"
  "v_cvt_pk_bf16_f32 v2, v4, v5\n"
  "v_cvt_pk_bf16_f32 v3, v6, v7\n"
  "v_mfma_f32_16x16x32_bf16 v[16:19], a[116:119], v[76:79], v[16:19]\n"
  "ds_write_b128 v80, v[0:3] offset:0\n"
  "v_pk_mul_f32 v[8:9], v[8:9], s[60:61] op_sel_hi:[1,0]\n"
  "v_pk_mul_f32 v[10:11], v[10:11], s[60:61] op_sel_hi:[1,0]\n"
  "v_pk_mul_f32 v[12:13], v[12:13], s[60:61] op_sel_hi:[1,0]\n"
  "v_pk_mul_f32 v[14:15], v[14:15], s[60:61] op_sel_hi:[1,0]\n"
  "v_mfma_f32_16x16x32_bf16 v[20:23], a[140:143], v[76:79], v[92:95]\n"
  "s_add_u32 m0, s59, 9984\n"
  "v_exp_f32 v8, v8\n"
  "v_mfma_f32_16x16x32_bf16 v[20:23], a[136:139], v[72:75], v[20:23]\n"
  "s_waitcnt lgkmcnt(10)\n"
  "buffer_load_dwordx4 v112, s[40:43], s57 offen offset:256 nt lds\n"
  "buffer_load_dwordx4 v113, s[40:43], s57 offen offset:1280 nt lds\n"
  "v_exp_f32 v9, v9\n"
  "v_mfma_f32_16x16x32_bf16 v[20:23], a[132:135], v[68:71], v[20:23]\n"
  "s_waitcnt vmcnt(10)\n"
  "ds_read_b128 v[192:195], v87 offset:0\n"
  "ds_read_b128 v[196:199], v88 offset:0\n"
  "v_exp_f32 v10, v10\n"
  "v_mfma_f32_16x16x32_bf16 v[20:23], a[128:131], v[64:67], v[20:23]\n"
  "s_add_u32 m0, s59, 12160\n"
  "v_exp_f32_e64 v11, v11\n"
  "v_mfma_f32_16x16x32_bf16 v[20:23], a[124:127], v[60:63], v[20:23]\n"
  "s_waitcnt lgkmcnt(9)\n"
  "buffer_load_dwordx4 v112, s[40:43], s57 offen offset:128 nt lds\n"
  "buffer_load_dwordx4 v113, s[40:43], s57 offen offset:1152 nt lds\n"
  "v_cvt_pk_bf16_f32 v8, v8, v9\n"
  "v_cvt_pk_bf16_f32 v9, v10, v11\n"
  "v_mfma_f32_16x16x32_bf16 v[20:23], a[120:123], v[56:59], v[20:23]\n"
  "s_waitcnt vmcnt(10)\n"
  "ds_read_b128 v[184:187], v87 offset:2048\n"
  "ds_read_b128 v[188:191], v88 offset:2048\n"
  "ds_read_b128 v[32:35], v81 offset:0\n"
  "ds_read_b128 v[36:39], v82 offset:0\n"
  "v_mfma_f32_16x16x32_bf16 v[24:27], a[144:147], v[56:59], v[92:95]\n"
  "s_add_u32 m0, s59, 14336\n"
  "v_exp_f32 v12, v12\n"
  "v_mfma_f32_16x16x32_bf16 v[24:27], a[148:151], v[60:63], v[24:27]\n"
  "s_waitcnt lgkmcnt(11)\n"
  "buffer_load_dwordx4 v112, s[40:43], s57 offen offset:0 nt lds\n"
  "buffer_load_dwordx4 v113, s[40:43], s57 offen offset:1024 nt lds\n"
  "s_add_u32 s57, s57, 6144\n"
  "v_exp_f32 v13, v13\n"
  "v_mfma_f32_16x16x32_bf16 v[24:27], a[152:155], v[64:67], v[24:27]\n"
  "s_waitcnt vmcnt(10)\n"
  "ds_read_b128 v[208:211], v87 offset:4096\n"
  "ds_read_b128 v[212:215], v88 offset:4096\n"
  "v_exp_f32 v14, v14\n"
  "v_mfma_f32_16x16x32_bf16 v[24:27], a[156:159], v[68:71], v[24:27]\n"
  "v_exp_f32_e64 v15, v15\n"
  "v_mfma_f32_16x16x32_bf16 v[24:27], a[160:163], v[72:75], v[24:27]\n"
  "v_cvt_pk_bf16_f32 v10, v12, v13\n"
  "v_cvt_pk_bf16_f32 v11, v14, v15\n"
  "v_mfma_f32_16x16x32_bf16 v[24:27], a[164:167], v[76:79], v[24:27]\n"
  "ds_write_b128 v80, v[8:11] offset:1024\n"
  "v_pk_mul_f32 v[16:17], v[16:17], s[60:61] op_sel_hi:[1,0]\n"
  "v_pk_mul_f32 v[18:19], v[18:19], s[60:61] op_sel_hi:[1,0]\n"
  "v_pk_mul_f32 v[20:21], v[20:21], s[60:61] op_sel_hi:[1,0]\n"
  "v_pk_mul_f32 v[22:23], v[22:23], s[60:61] op_sel_hi:[1,0]\n"
  "v_mfma_f32_16x16x32_bf16 v[28:31], a[188:191], v[76:79], v[92:95]\n"
  "s_waitcnt vmcnt(8)\n"
  "ds_read_b128 v[216:219], v87 offset:6144\n"
  "ds_read_b128 v[220:223], v88 offset:6144\n"
  "v_exp_f32 v16, v16\n"
  "v_mfma_f32_16x16x32_bf16 v[28:31], a[184:187], v[72:75], v[28:31]\n"
  "v_exp_f32 v17, v17\n"
  "v_mfma_f32_16x16x32_bf16 v[28:31], a[180:183], v[68:71], v[28:31]\n"
  "v_exp_f32 v18, v18\n"
  "v_mfma_f32_16x16x32_bf16 v[28:31], a[176:179], v[64:67], v[28:31]\n"
  "s_waitcnt vmcnt(6)\n"
  "ds_read_b128 v[224:227], v87 offset:8192\n"
  "ds_read_b128 v[228:231], v88 offset:8192\n"
  "v_exp_f32 v19, v19\n"
  "v_mfma_f32_16x16x32_bf16 v[28:31], a[172:175], v[60:63], v[28:31]\n"
  "v_cvt_pk_bf16_f32 v16, v16, v17\n"
  "v_cvt_pk_bf16_f32 v17, v18, v19\n"
  "v_mfma_f32_16x16x32_bf16 v[28:31], a[168:171], v[56:59], v[28:31]\n"
  "ds_read_b128 v[40:43], v81 offset:2048\n"
  "ds_read_b128 v[44:47], v82 offset:2048\n"
  "v_mfma_f32_16x16x32_bf16 v[0:3], a[192:195], v[56:59], v[92:95]\n"
  "v_exp_f32 v20, v20\n"
  "v_mfma_f32_16x16x32_bf16 v[0:3], a[196:199], v[60:63], v[0:3]\n"
  "s_waitcnt vmcnt(4)\n"
  "ds_read_b128 v[248:251], v87 offset:10240\n"
  "ds_read_b128 v[252:255], v88 offset:10240\n"
  "v_exp_f32 v21, v21\n"
  "v_mfma_f32_16x16x32_bf16 v[0:3], a[200:203], v[64:67], v[0:3]\n"
  "v_exp_f32 v22, v22\n"
  "v_mfma_f32_16x16x32_bf16 v[0:3], a[204:207], v[68:71], v[0:3]\n"
  "v_exp_f32 v23, v23\n"
  "v_mfma_f32_16x16x32_bf16 v[0:3], a[208:211], v[72:75], v[0:3]\n"
  "s_waitcnt vmcnt(2)\n"
  "ds_read_b128 v[240:243], v87 offset:12288\n"
  "ds_read_b128 v[244:247], v88 offset:12288\n"
  "v_cvt_pk_bf16_f32 v18, v20, v21\n"
  "v_cvt_pk_bf16_f32 v19, v22, v23\n"
  "v_mfma_f32_16x16x32_bf16 v[0:3], a[212:215], v[76:79], v[0:3]\n"
  "ds_write_b128 v80, v[16:19] offset:2048\n"
  "v_pk_mul_f32 v[24:25], v[24:25], s[60:61] op_sel_hi:[1,0]\n"
  "v_pk_mul_f32 v[26:27], v[26:27], s[60:61] op_sel_hi:[1,0]\n"
  "v_pk_mul_f32 v[28:29], v[28:29], s[60:61] op_sel_hi:[1,0]\n"
  "v_pk_mul_f32 v[30:31], v[30:31], s[60:61] op_sel_hi:[1,0]\n"
  "v_mfma_f32_16x16x32_bf16 v[4:7], a[236:239], v[76:79], v[92:95]\n"
  "v_exp_f32 v24, v24\n"
  "v_mfma_f32_16x16x32_bf16 v[4:7], a[232:235], v[72:75], v[4:7]\n"
  "s_waitcnt vmcnt(0)\n"
  "ds_read_b128 v[232:235], v87 offset:14336\n"
  "ds_read_b128 v[236:239], v88 offset:14336\n"
  "v_exp_f32 v25, v25\n"
  "v_mfma_f32_16x16x32_bf16 v[4:7], a[228:231], v[68:71], v[4:7]\n"
  "v_exp_f32 v26, v26\n"
  "v_mfma_f32_16x16x32_bf16 v[4:7], a[224:227], v[64:67], v[4:7]\n"
  "v_exp_f32_e64 v27, v27\n"
  "v_mfma_f32_16x16x32_bf16 v[4:7], a[220:223], v[60:63], v[4:7]\n"
  "v_cvt_pk_bf16_f32 v24, v24, v25\n"
  "v_cvt_pk_bf16_f32 v25, v26, v27\n"
  "v_mfma_f32_16x16x32_bf16 v[4:7], a[216:219], v[56:59], v[4:7]\n"
  "s_waitcnt lgkmcnt(0)\n"
  "s_barrier\n"
  "ds_read_b128 v[48:51], v81 offset:4096\n"
  "ds_read_b128 v[52:55], v82 offset:4096\n"
  "v_mfma_f32_16x16x32_bf16 v[8:11], a[240:243], v[56:59], v[92:95]\n"
  "v_exp_f32 v28, v28\n"
  "v_mfma_f32_16x16x32_bf16 v[8:11], a[244:247], v[60:63], v[8:11]\n"
  "v_exp_f32 v29, v29\n"
  "v_mfma_f32_16x16x32_bf16 v[8:11], a[248:251], v[64:67], v[8:11]\n"
  "v_exp_f32 v30, v30\n"
  "v_mfma_f32_16x16x32_bf16 v[8:11], a[252:255], v[68:71], v[8:11]\n"
  "v_exp_f32 v31, v31\n"
  "v_mfma_f32_16x16x32_bf16 v[8:11], v[128:131], v[72:75], v[8:11]\n"
  "v_cvt_pk_bf16_f32 v26, v28, v29\n"
  "v_cvt_pk_bf16_f32 v27, v30, v31\n"
  "v_mfma_f32_16x16x32_bf16 v[8:11], v[132:135], v[76:79], v[8:11]\n"
  "ds_write_b128 v80, v[24:27] offset:3072\n"
  "v_pk_mul_f32 v[0:1], v[0:1], s[60:61] op_sel_hi:[1,0]\n"
  "v_pk_mul_f32 v[2:3], v[2:3], s[60:61] op_sel_hi:[1,0]\n"
  "v_pk_mul_f32 v[4:5], v[4:5], s[60:61] op_sel_hi:[1,0]\n"
  "v_pk_mul_f32 v[6:7], v[6:7], s[60:61] op_sel_hi:[1,0]\n"
  "v_mfma_f32_16x16x32_bf16 v[12:15], v[156:159], v[76:79], v[92:95]\n"
  "v_exp_f32 v0, v0\n"
  "v_mfma_f32_16x16x32_bf16 v[12:15], v[152:155], v[72:75], v[12:15]\n"
  "v_exp_f32 v1, v1\n"
  "v_mfma_f32_16x16x32_bf16 v[12:15], v[148:151], v[68:71], v[12:15]\n"
  "v_exp_f32 v2, v2\n"
  "v_mfma_f32_16x16x32_bf16 v[12:15], v[144:147], v[64:67], v[12:15]\n"
  "v_exp_f32 v3, v3\n"
  "v_mfma_f32_16x16x32_bf16 v[12:15], v[140:143], v[60:63], v[12:15]\n"
  "v_cvt_pk_bf16_f32 v0, v0, v1\n"
  "v_cvt_pk_bf16_f32 v1, v2, v3\n"
  "v_mfma_f32_16x16x32_bf16 v[12:15], v[136:139], v[56:59], v[12:15]\n"
  "v_mul_f32 v8, s60, v8\n"
  "v_mul_f32 v9, s60, v9\n"
  "v_mfma_f32_16x16x32_bf16 v[16:19], v[160:163], v[56:59], v[92:95]\n"
  "v_exp_f32 v4, v4\n"
  "v_mfma_f32_16x16x32_bf16 v[16:19], v[164:167], v[60:63], v[16:19]\n"
  "v_exp_f32 v5, v5\n"
  "v_mfma_f32_16x16x32_bf16 v[16:19], v[168:171], v[64:67], v[16:19]\n"
  "v_exp_f32 v6, v6\n"
  "v_mfma_f32_16x16x32_bf16 v[16:19], v[172:175], v[68:71], v[16:19]\n"
  "v_exp_f32 v7, v7\n"
  "v_mfma_f32_16x16x32_bf16 v[16:19], v[176:179], v[72:75], v[16:19]\n"
  "v_cvt_pk_bf16_f32 v2, v4, v5\n"
  "v_cvt_pk_bf16_f32 v3, v6, v7\n"
  "v_mfma_f32_16x16x32_bf16 v[16:19], v[180:183], v[76:79], v[16:19]\n"
  "ds_write_b128 v80, v[0:3] offset:4096\n"
  "v_pk_mul_f32 v[10:11], v[10:11], s[60:61] op_sel_hi:[1,0]\n"
  "v_pk_mul_f32 v[12:13], v[12:13], s[60:61] op_sel_hi:[1,0]\n"
  "v_pk_mul_f32 v[14:15], v[14:15], s[60:61] op_sel_hi:[1,0]\n"
  "v_mfma_f32_16x16x32_bf16 v[20:23], v[204:207], v[76:79], v[92:95]\n"
  "v_exp_f32 v8, v8\n"
  "v_mfma_f32_16x16x32_bf16 v[20:23], v[200:203], v[72:75], v[20:23]\n"
  "v_exp_f32 v9, v9\n"
  "v_mfma_f32_16x16x32_bf16 v[20:23], v[196:199], v[68:71], v[20:23]\n"
  "v_exp_f32 v10, v10\n"
  "v_mfma_f32_16x16x32_bf16 v[20:23], v[192:195], v[64:67], v[20:23]\n"
  "v_exp_f32 v11, v11\n"
  "v_mfma_f32_16x16x32_bf16 v[20:23], v[188:191], v[60:63], v[20:23]\n"
  "v_cvt_pk_bf16_f32 v8, v8, v9\n"
  "v_cvt_pk_bf16_f32 v9, v10, v11\n"
  "v_mfma_f32_16x16x32_bf16 v[20:23], v[184:187], v[56:59], v[20:23]\n"
  "v_mul_f32 v16, s60, v16\n"
  "v_mul_f32 v17, s60, v17\n"
  "v_mfma_f32_16x16x32_bf16 v[24:27], v[208:211], v[56:59], v[92:95]\n"
  "v_exp_f32 v12, v12\n"
  "v_mfma_f32_16x16x32_bf16 v[24:27], v[212:215], v[60:63], v[24:27]\n"
  "v_exp_f32 v13, v13\n"
  "v_mfma_f32_16x16x32_bf16 v[24:27], v[216:219], v[64:67], v[24:27]\n"
  "v_exp_f32 v14, v14\n"
  "v_mfma_f32_16x16x32_bf16 v[24:27], v[220:223], v[68:71], v[24:27]\n"
  "v_exp_f32 v15, v15\n"
  "v_mfma_f32_16x16x32_bf16 v[24:27], v[224:227], v[72:75], v[24:27]\n"
  "v_cvt_pk_bf16_f32 v10, v12, v13\n"
  "v_cvt_pk_bf16_f32 v11, v14, v15\n"
  "v_mfma_f32_16x16x32_bf16 v[24:27], v[228:231], v[76:79], v[24:27]\n"
  "ds_write_b128 v80, v[8:11] offset:5120\n"
  "v_pk_mul_f32 v[18:19], v[18:19], s[60:61] op_sel_hi:[1,0]\n"
  "v_pk_mul_f32 v[20:21], v[20:21], s[60:61] op_sel_hi:[1,0]\n"
  "v_pk_mul_f32 v[22:23], v[22:23], s[60:61] op_sel_hi:[1,0]\n"
  "v_mfma_f32_16x16x32_bf16 v[28:31], v[252:255], v[76:79], v[92:95]\n"
  "v_exp_f32 v16, v16\n"
  "v_mfma_f32_16x16x32_bf16 v[28:31], v[248:251], v[72:75], v[28:31]\n"
  "v_exp_f32 v17, v17\n"
  "v_mfma_f32_16x16x32_bf16 v[28:31], v[244:247], v[68:71], v[28:31]\n"
  "v_exp_f32 v18, v18\n"
  "v_mfma_f32_16x16x32_bf16 v[28:31], v[240:243], v[64:67], v[28:31]\n"
  "s_sub_u32 s56, s56, 1\n"
  "v_exp_f32_e64 v19, v19\n"
  "v_mfma_f32_16x16x32_bf16 v[28:31], v[236:239], v[60:63], v[28:31]\n"
  "v_cvt_pk_bf16_f32 v16, v16, v17\n"
  "v_cvt_pk_bf16_f32 v17, v18, v19\n"
  "v_mfma_f32_16x16x32_bf16 v[28:31], v[232:235], v[56:59], v[28:31]\n"
  "s_waitcnt lgkmcnt(3)\n"
  ".p2align 3\n"
  "v_mfma_f32_16x16x32_bf16 v[0:3], a[0:3], v[32:35], v[92:95]\n"
  "s_add_u32 s66, s66, 6144\n"
  "v_exp_f32 v20, v20\n"
  "v_mfma_f32_16x16x32_bf16 v[0:3], a[4:7], v[36:39], v[0:3]\n"
  "s_cmp_eq_u32 s66, 102400\n"
  "v_exp_f32 v21, v21\n"
  "v_mfma_f32_16x16x32_bf16 v[0:3], a[8:11], v[40:43], v[0:3]\n"
  "s_cselect_b32 s66, 65536, s66\n"
  "v_exp_f32 v22, v22\n"
  "v_mfma_f32_16x16x32_bf16 v[0:3], a[12:15], v[44:47], v[0:3]\n"
  "v_exp_f32 v23, v23\n"
  "v_mfma_f32_16x16x32_bf16 v[0:3], a[16:19], v[48:51], v[0:3]\n"
  "v_cvt_pk_bf16_f32 v18, v20, v21\n"
  "v_cvt_pk_bf16_f32 v19, v22, v23\n"
  "v_mfma_f32_16x16x32_bf16 v[0:3], a[20:23], v[52:55], v[0:3]\n"
  "ds_write_b128 v80, v[16:19] offset:6144\n"
  "v_pk_mul_f32 v[24:25], v[24:25], s[60:61] op_sel_hi:[1,0]\n"
  "v_pk_mul_f32 v[26:27], v[26:27], s[60:61] op_sel_hi:[1,0]\n"
  "v_pk_mul_f32 v[28:29], v[28:29], s[60:61] op_sel_hi:[1,0]\n"
  "v_pk_mul_f32 v[30:31], v[30:31], s[60:61] op_sel_hi:[1,0]\n"
  "v_mfma_f32_16x16x32_bf16 v[4:7], a[44:47], v[52:55], v[92:95]\n"
  "v_exp_f32 v24, v24\n"
  "v_mfma_f32_16x16x32_bf16 v[4:7], a[40:43], v[48:51], v[4:7]\n"
  "v_exp_f32 v25, v25\n"
  "v_mfma_f32_16x16x32_bf16 v[4:7], a[36:39], v[44:47], v[4:7]\n"
  "v_exp_f32 v26, v26\n"
  "v_mfma_f32_16x16x32_bf16 v[4:7], a[32:35], v[40:43], v[4:7]\n"
  "v_exp_f32 v27, v27\n"
  "v_mfma_f32_16x16x32_bf16 v[4:7], a[28:31], v[36:39], v[4:7]\n"
  "v_cvt_pk_bf16_f32 v24, v24, v25\n"
  "v_cvt_pk_bf16_f32 v25, v26, v27\n"
  "v_mfma_f32_16x16x32_bf16 v[4:7], a[24:27], v[32:35], v[4:7]\n"
  "v_add_u32 v81, s66, v83\n"
  "v_add_u32 v82, s66, v84\n"
  "v_mfma_f32_16x16x32_bf16 v[8:11], a[48:51], v[32:35], v[92:95]\n"
  "v_exp_f32 v28, v28\n"
  "v_mfma_f32_16x16x32_bf16 v[8:11], a[52:55], v[36:39], v[8:11]\n"
  "v_exp_f32 v29, v29\n"
  "v_mfma_f32_16x16x32_bf16 v[8:11], a[56:59], v[40:43], v[8:11]\n"
  "v_exp_f32 v30, v30\n"
  "v_mfma_f32_16x16x32_bf16 v[8:11], a[60:63], v[44:47], v[8:11]\n"
  "v_exp_f32 v31, v31\n"
  "v_mfma_f32_16x16x32_bf16 v[8:11], a[64:67], v[48:51], v[8:11]\n"
  "v_cvt_pk_bf16_f32 v26, v28, v29\n"
  "v_cvt_pk_bf16_f32 v27, v30, v31\n"
  "v_mfma_f32_16x16x32_bf16 v[8:11], a[68:71], v[52:55], v[8:11]\n"
  "ds_write_b128 v80, v[24:27] offset:7168\n"
  "v_pk_mul_f32 v[0:1], v[0:1], s[60:61] op_sel_hi:[1,0]\n"
  "v_pk_mul_f32 v[2:3], v[2:3], s[60:61] op_sel_hi:[1,0]\n"
  "v_pk_mul_f32 v[4:5], v[4:5], s[60:61] op_sel_hi:[1,0]\n"
  "v_pk_mul_f32 v[6:7], v[6:7], s[60:61] op_sel_hi:[1,0]\n"
  "v_mfma_f32_16x16x32_bf16 v[12:15], a[92:95], v[52:55], v[92:95]\n"
  "v_exp_f32 v0, v0\n"
  "v_mfma_f32_16x16x32_bf16 v[12:15], a[88:91], v[48:51], v[12:15]\n"
  "v_exp_f32 v1, v1\n"
  "v_mfma_f32_16x16x32_bf16 v[12:15], a[84:87], v[44:47], v[12:15]\n"
  "v_exp_f32 v2, v2\n"
  "v_mfma_f32_16x16x32_bf16 v[12:15], a[80:83], v[40:43], v[12:15]\n"
  "v_exp_f32 v3, v3\n"
  "v_mfma_f32_16x16x32_bf16 v[12:15], a[76:79], v[36:39], v[12:15]\n"
  "v_cvt_pk_bf16_f32 v0, v0, v1\n"
  "v_cvt_pk_bf16_f32 v1, v2, v3\n"
  "v_mfma_f32_16x16x32_bf16 v[12:15], a[72:75], v[32:35], v[12:15]\n"
  "v_xor_b32 v80, s88, v80\n"
  "v_mfma_f32_16x16x32_bf16 v[16:19], a[96:99], v[32:35], v[92:95]\n"
  "v_exp_f32 v4, v4\n"
  "v_mfma_f32_16x16x32_bf16 v[16:19], a[100:103], v[36:39], v[16:19]\n"
  "s_xor_b32 s89, s88, s89\n"
  "v_exp_f32 v5, v5\n"
  "v_mfma_f32_16x16x32_bf16 v[16:19], a[104:107], v[40:43], v[16:19]\n"
  "s_xor_b32 s88, s88, s89\n"
  "v_exp_f32 v6, v6\n"
  "v_mfma_f32_16x16x32_bf16 v[16:19], a[108:111], v[44:47], v[16:19]\n"
  "v_exp_f32_e64 v7, v7\n"
  "v_mfma_f32_16x16x32_bf16 v[16:19], a[112:115], v[48:51], v[16:19]\n"
  "v_cvt_pk_bf16_f32 v2, v4, v5\n"
  "v_cvt_pk_bf16_f32 v3, v6, v7\n"
  "v_mfma_f32_16x16x32_bf16 v[16:19], a[116:119], v[52:55], v[16:19]\n"
  "ds_write_b128 v80, v[0:3] offset:0\n"
  "v_pk_mul_f32 v[8:9], v[8:9], s[60:61] op_sel_hi:[1,0]\n"
  "v_pk_mul_f32 v[10:11], v[10:11], s[60:61] op_sel_hi:[1,0]\n"
  "v_pk_mul_f32 v[12:13], v[12:13], s[60:61] op_sel_hi:[1,0]\n"
  "v_pk_mul_f32 v[14:15], v[14:15], s[60:61] op_sel_hi:[1,0]\n"
  "v_mfma_f32_16x16x32_bf16 v[20:23], a[140:143], v[52:55], v[92:95]\n"
  "v_exp_f32 v8, v8\n"
  "v_mfma_f32_16x16x32_bf16 v[20:23], a[136:139], v[48:51], v[20:23]\n"
  "v_exp_f32 v9, v9\n"
  "v_mfma_f32_16x16x32_bf16 v[20:23], a[132:135], v[44:47], v[20:23]\n"
  "v_exp_f32 v10, v10\n"
  "v_mfma_f32_16x16x32_bf16 v[20:23], a[128:131], v[40:43], v[20:23]\n"
  "v_exp_f32 v11, v11\n"
  "v_mfma_f32_16x16x32_bf16 v[20:23], a[124:127], v[36:39], v[20:23]\n"
  "v_cvt_pk_bf16_f32 v8, v8, v9\n"
  "v_cvt_pk_bf16_f32 v9, v10, v11\n"
  "v_mfma_f32_16x16x32_bf16 v[20:23], a[120:123], v[32:35], v[20:23]\n"
  "ds_read_b128 v[56:59], v81 offset:0\n"
  "ds_read_b128 v[60:63], v82 offset:0\n"
  "v_mfma_f32_16x16x32_bf16 v[24:27], a[144:147], v[32:35], v[92:95]\n"
  "v_exp_f32 v12, v12\n"
  "v_mfma_f32_16x16x32_bf16 v[24:27], a[148:151], v[36:39], v[24:27]\n"
  "v_exp_f32 v13, v13\n"
  "v_mfma_f32_16x16x32_bf16 v[24:27], a[152:155], v[40:43], v[24:27]\n"
  "v_exp_f32 v14, v14\n"
  "v_mfma_f32_16x16x32_bf16 v[24:27], a[156:159], v[44:47], v[24:27]\n"
  "v_exp_f32 v15, v15\n"
  "v_mfma_f32_16x16x32_bf16 v[24:27], a[160:163], v[48:51], v[24:27]\n"
  "v_cvt_pk_bf16_f32 v10, v12, v13\n"
  "v_cvt_pk_bf16_f32 v11, v14, v15\n"
  "v_mfma_f32_16x16x32_bf16 v[24:27], a[164:167], v[52:55], v[24:27]\n"
  "ds_write_b128 v80, v[8:11] offset:1024\n"
  "v_pk_mul_f32 v[16:17], v[16:17], s[60:61] op_sel_hi:[1,0]\n"
  "v_pk_mul_f32 v[18:19], v[18:19], s[60:61] op_sel_hi:[1,0]\n"
  "v_pk_mul_f32 v[20:21], v[20:21], s[60:61] op_sel_hi:[1,0]\n"
  "v_pk_mul_f32 v[22:23], v[22:23], s[60:61] op_sel_hi:[1,0]\n"
  "v_mfma_f32_16x16x32_bf16 v[28:31], a[188:191], v[52:55], v[92:95]\n"
  "v_exp_f32 v16, v16\n"
  "v_mfma_f32_16x16x32_bf16 v[28:31], a[184:187], v[48:51], v[28:31]\n"
  "v_exp_f32 v17, v17\n"
  "v_mfma_f32_16x16x32_bf16 v[28:31], a[180:183], v[44:47], v[28:31]\n"
  "v_exp_f32 v18, v18\n"
  "v_mfma_f32_16x16x32_bf16 v[28:31], a[176:179], v[40:43], v[28:31]\n"
  "v_exp_f32 v19, v19\n"
  "v_mfma_f32_16x16x32_bf16 v[28:31], a[172:175], v[36:39], v[28:31]\n"
  "v_cvt_pk_bf16_f32 v16, v16, v17\n"
  "v_cvt_pk_bf16_f32 v17, v18, v19\n"
  "v_mfma_f32_16x16x32_bf16 v[28:31], a[168:171], v[32:35], v[28:31]\n"
  "ds_read_b128 v[64:67], v81 offset:2048\n"
  "ds_read_b128 v[68:71], v82 offset:2048\n"
  "v_mfma_f32_16x16x32_bf16 v[0:3], a[192:195], v[32:35], v[92:95]\n"
  "v_exp_f32 v20, v20\n"
  "v_mfma_f32_16x16x32_bf16 v[0:3], a[196:199], v[36:39], v[0:3]\n"
  "v_exp_f32 v21, v21\n"
  "v_mfma_f32_16x16x32_bf16 v[0:3], a[200:203], v[40:43], v[0:3]\n"
  "v_exp_f32 v22, v22\n"
  "v_mfma_f32_16x16x32_bf16 v[0:3], a[204:207], v[44:47], v[0:3]\n"
  "v_exp_f32 v23, v23\n"
  "v_mfma_f32_16x16x32_bf16 v[0:3], a[208:211], v[48:51], v[0:3]\n"
  "v_cvt_pk_bf16_f32 v18, v20, v21\n"
  "v_cvt_pk_bf16_f32 v19, v22, v23\n"
  "v_mfma_f32_16x16x32_bf16 v[0:3], a[212:215], v[52:55], v[0:3]\n"
  "ds_write_b128 v80, v[16:19] offset:2048\n"
  "v_pk_mul_f32 v[24:25], v[24:25], s[60:61] op_sel_hi:[1,0]\n"
  "v_pk_mul_f32 v[26:27], v[26:27], s[60:61] op_sel_hi:[1,0]\n"
  "v_pk_mul_f32 v[28:29], v[28:29], s[60:61] op_sel_hi:[1,0]\n"
  "v_pk_mul_f32 v[30:31], v[30:31], s[60:61] op_sel_hi:[1,0]\n"
  "v_mfma_f32_16x16x32_bf16 v[4:7], a[236:239], v[52:55], v[92:95]\n"
  "v_exp_f32 v24, v24\n"
  "v_mfma_f32_16x16x32_bf16 v[4:7], a[232:235], v[48:51], v[4:7]\n"
  "v_exp_f32 v25, v25\n"
  "v_mfma_f32_16x16x32_bf16 v[4:7], a[228:231], v[44:47], v[4:7]\n"
  "v_exp_f32 v26, v26\n"
  "v_mfma_f32_16x16x32_bf16 v[4:7], a[224:227], v[40:43], v[4:7]\n"
  "v_exp_f32 v27, v27\n"
  "v_mfma_f32_16x16x32_bf16 v[4:7], a[220:223], v[36:39], v[4:7]\n"
  "v_cvt_pk_bf16_f32 v24, v24, v25\n"
  "v_cvt_pk_bf16_f32 v25, v26, v27\n"
  "v_mfma_f32_16x16x32_bf16 v[4:7], a[216:219], v[32:35], v[4:7]\n"
  "ds_read_b128 v[72:75], v81 offset:4096\n"
  "ds_read_b128 v[76:79], v82 offset:4096\n"
  "v_mfma_f32_16x16x32_bf16 v[8:11], a[240:243], v[32:35], v[92:95]\n"
  "v_exp_f32 v28, v28\n"
  "v_mfma_f32_16x16x32_bf16 v[8:11], a[244:247], v[36:39], v[8:11]\n"
  "v_exp_f32 v29, v29\n"
  "v_mfma_f32_16x16x32_bf16 v[8:11], a[248:251], v[40:43], v[8:11]\n"
  "v_exp_f32 v30, v30\n"
  "v_mfma_f32_16x16x32_bf16 v[8:11], a[252:255], v[44:47], v[8:11]\n"
  "v_exp_f32 v31, v31\n"
  "v_mfma_f32_16x16x32_bf16 v[8:11], v[128:131], v[48:51], v[8:11]\n"
  "v_cvt_pk_bf16_f32 v26, v28, v29\n"
  "v_cvt_pk_bf16_f32 v27, v30, v31\n"
  "v_mfma_f32_16x16x32_bf16 v[8:11], v[132:135], v[52:55], v[8:11]\n"
  "ds_write_b128 v80, v[24:27] offset:3072\n"
  "v_pk_mul_f32 v[0:1], v[0:1], s[60:61] op_sel_hi:[1,0]\n"
  "v_pk_mul_f32 v[2:3], v[2:3], s[60:61] op_sel_hi:[1,0]\n"
  "v_pk_mul_f32 v[4:5], v[4:5], s[60:61] op_sel_hi:[1,0]\n"
  "v_pk_mul_f32 v[6:7], v[6:7], s[60:61] op_sel_hi:[1,0]\n"
  "v_mfma_f32_16x16x32_bf16 v[12:15], v[156:159], v[52:55], v[92:95]\n"
  "v_exp_f32 v0, v0\n"
  "v_mfma_f32_16x16x32_bf16 v[12:15], v[152:155], v[48:51], v[12:15]\n"
  "v_exp_f32 v1, v1\n"
  "v_mfma_f32_16x16x32_bf16 v[12:15], v[148:151], v[44:47], v[12:15]\n"
  "v_exp_f32 v2, v2\n"
  "v_mfma_f32_16x16x32_bf16 v[12:15], v[144:147], v[40:43], v[12:15]\n"
  "v_exp_f32 v3, v3\n"
  "v_mfma_f32_16x16x32_bf16 v[12:15], v[140:143], v[36:39], v[12:15]\n"
  "v_cvt_pk_bf16_f32 v0, v0, v1\n"
  "v_cvt_pk_bf16_f32 v1, v2, v3\n"
  "v_mfma_f32_16x16x32_bf16 v[12:15], v[136:139], v[32:35], v[12:15]\n"
  "s_waitcnt lgkmcnt(10)\n"
  "s_barrier\n"
  "v_mul_f32 v8, s60, v8\n"
  "v_mul_f32 v9, s60, v9\n"
  "v_mfma_f32_16x16x32_bf16 v[16:19], v[160:163], v[32:35], v[92:95]\n"
  "v_exp_f32 v4, v4\n"
  "v_mfma_f32_16x16x32_bf16 v[16:19], v[164:167], v[36:39], v[16:19]\n"
  "v_exp_f32 v5, v5\n"
  "v_mfma_f32_16x16x32_bf16 v[16:19], v[168:171], v[40:43], v[16:19]\n"
  "v_exp_f32 v6, v6\n"
  "v_mfma_f32_16x16x32_bf16 v[16:19], v[172:175], v[44:47], v[16:19]\n"
  "v_exp_f32 v7, v7\n"
  "v_mfma_f32_16x16x32_bf16 v[16:19], v[176:179], v[48:51], v[16:19]\n"
  "v_cvt_pk_bf16_f32 v2, v4, v5\n"
  "v_cvt_pk_bf16_f32 v3, v6, v7\n"
  "v_mfma_f32_16x16x32_bf16 v[16:19], v[180:183], v[52:55], v[16:19]\n"
  "ds_write_b128 v80, v[0:3] offset:4096\n"
  "v_pk_mul_f32 v[10:11], v[10:11], s[60:61] op_sel_hi:[1,0]\n"
  "v_pk_mul_f32 v[12:13], v[12:13], s[60:61] op_sel_hi:[1,0]\n"
  "v_pk_mul_f32 v[14:15], v[14:15], s[60:61] op_sel_hi:[1,0]\n"
  "v_mfma_f32_16x16x32_bf16 v[20:23], v[204:207], v[52:55], v[92:95]\n"
  "v_exp_f32 v8, v8\n"
  "v_mfma_f32_16x16x32_bf16 v[20:23], v[200:203], v[48:51], v[20:23]\n"
  "v_exp_f32 v9, v9\n"
  "v_mfma_f32_16x16x32_bf16 v[20:23], v[196:199], v[44:47], v[20:23]\n"
  "v_exp_f32 v10, v10\n"
  "v_mfma_f32_16x16x32_bf16 v[20:23], v[192:195], v[40:43], v[20:23]\n"
  "v_exp_f32 v11, v11\n"
  "v_mfma_f32_16x16x32_bf16 v[20:23], v[188:191], v[36:39], v[20:23]\n"
  "v_cvt_pk_bf16_f32 v8, v8, v9\n"
  "v_cvt_pk_bf16_f32 v9, v10, v11\n"
  "v_mfma_f32_16x16x32_bf16 v[20:23], v[184:187], v[32:35], v[20:23]\n"
  "v_mul_f32 v16, s60, v16\n"
  "v_mul_f32 v17, s60, v17\n"
  "v_mfma_f32_16x16x32_bf16 v[24:27], v[208:211], v[32:35], v[92:95]\n"
  "v_exp_f32 v12, v12\n"
  "v_mfma_f32_16x16x32_bf16 v[24:27], v[212:215], v[36:39], v[24:27]\n"
  "v_exp_f32 v13, v13\n"
  "v_mfma_f32_16x16x32_bf16 v[24:27], v[216:219], v[40:43], v[24:27]\n"
  "v_exp_f32 v14, v14\n"
  "v_mfma_f32_16x16x32_bf16 v[24:27], v[220:223], v[44:47], v[24:27]\n"
  "v_exp_f32 v15, v15\n"
  "v_mfma_f32_16x16x32_bf16 v[24:27], v[224:227], v[48:51], v[24:27]\n"
  "v_cvt_pk_bf16_f32 v10, v12, v13\n"
  "v_cvt_pk_bf16_f32 v11, v14, v15\n"
  "v_mfma_f32_16x16x32_bf16 v[24:27], v[228:231], v[52:55], v[24:27]\n"
  "ds_write_b128 v80, v[8:11] offset:5120\n"
  "v_pk_mul_f32 v[18:19], v[18:19], s[60:61] op_sel_hi:[1,0]\n"
  "v_pk_mul_f32 v[20:21], v[20:21], s[60:61] op_sel_hi:[1,0]\n"
  "v_pk_mul_f32 v[22:23], v[22:23], s[60:61] op_sel_hi:[1,0]\n"
  "v_mfma_f32_16x16x32_bf16 v[28:31], v[252:255], v[52:55], v[92:95]\n"
  "v_exp_f32 v16, v16\n"
  "v_mfma_f32_16x16x32_bf16 v[28:31], v[248:251], v[48:51], v[28:31]\n"
  "v_exp_f32 v17, v17\n"
  "v_mfma_f32_16x16x32_bf16 v[28:31], v[244:247], v[44:47], v[28:31]\n"
  "v_exp_f32 v18, v18\n"
  "v_mfma_f32_16x16x32_bf16 v[28:31], v[240:243], v[40:43], v[28:31]\n"
  "s_sub_u32 s56, s56, 1\n"
  "v_exp_f32_e64 v19, v19\n"
  "v_mfma_f32_16x16x32_bf16 v[28:31], v[236:239], v[36:39], v[28:31]\n"
  "v_cvt_pk_bf16_f32 v16, v16, v17\n"
  "v_cvt_pk_bf16_f32 v17, v18, v19\n"
  "v_mfma_f32_16x16x32_bf16 v[28:31], v[232:235], v[32:35], v[28:31]\n"
  "s_waitcnt lgkmcnt(3)\n"
  ".p2align 3\n"
  "A_N1_%=:\n"
  "v_mfma_f32_16x16x32_bf16 v[0:3], a[0:3], v[56:59], v[92:95]\n"
  "s_add_u32 s66, s66, 6144\n"
  "v_exp_f32 v20, v20\n"
  "v_mfma_f32_16x16x32_bf16 v[0:3], a[4:7], v[60:63], v[0:3]\n"
  "s_cmp_eq_u32 s66, 102400\n"
  "v_exp_f32 v21, v21\n"
  "v_mfma_f32_16x16x32_bf16 v[0:3], a[8:11], v[64:67], v[0:3]\n"
  "s_cselect_b32 s66, 65536, s66\n"
  "v_exp_f32 v22, v22\n"
  "v_mfma_f32_16x16x32_bf16 v[0:3], a[12:15], v[68:71], v[0:3]\n"
  "v_exp_f32 v23, v23\n"
  "v_mfma_f32_16x16x32_bf16 v[0:3], a[16:19], v[72:75], v[0:3]\n"
  "v_cvt_pk_bf16_f32 v18, v20, v21\n"
  "v_cvt_pk_bf16_f32 v19, v22, v23\n"
  "v_mfma_f32_16x16x32_bf16 v[0:3], a[20:23], v[76:79], v[0:3]\n"
  "ds_write_b128 v80, v[16:19] offset:6144\n"
  "v_pk_mul_f32 v[24:25], v[24:25], s[60:61] op_sel_hi:[1,0]\n"
  "v_pk_mul_f32 v[26:27], v[26:27], s[60:61] op_sel_hi:[1,0]\n"
  "v_pk_mul_f32 v[28:29], v[28:29], s[60:61] op_sel_hi:[1,0]\n"
  "v_pk_mul_f32 v[30:31], v[30:31], s[60:61] op_sel_hi:[1,0]\n"
  "v_mfma_f32_16x16x32_bf16 v[4:7], a[44:47], v[76:79], v[92:95]\n"
  "v_exp_f32 v24, v24\n"
  "v_mfma_f32_16x16x32_bf16 v[4:7], a[40:43], v[72:75], v[4:7]\n"
  "v_exp_f32 v25, v25\n"
  "v_mfma_f32_16x16x32_bf16 v[4:7], a[36:39], v[68:71], v[4:7]\n"
  "v_exp_f32 v26, v26\n"
  "v_mfma_f32_16x16x32_bf16 v[4:7], a[32:35], v[64:67], v[4:7]\n"
  "v_exp_f32 v27, v27\n"
  "v_mfma_f32_16x16x32_bf16 v[4:7], a[28:31], v[60:63], v[4:7]\n"
  "v_cvt_pk_bf16_f32 v24, v24, v25\n"
  "v_cvt_pk_bf16_f32 v25, v26, v27\n"
  "v_mfma_f32_16x16x32_bf16 v[4:7], a[24:27], v[56:59], v[4:7]\n"
  "v_add_u32 v81, s66, v83\n"
  "v_add_u32 v82, s66, v84\n"
  "v_mfma_f32_16x16x32_bf16 v[8:11], a[48:51], v[56:59], v[92:95]\n"
  "v_exp_f32 v28, v28\n"
  "v_mfma_f32_16x16x32_bf16 v[8:11], a[52:55], v[60:63], v[8:11]\n"
  "v_exp_f32 v29, v29\n"
  "v_mfma_f32_16x16x32_bf16 v[8:11], a[56:59], v[64:67], v[8:11]\n"
  "v_exp_f32 v30, v30\n"
  "v_mfma_f32_16x16x32_bf16 v[8:11], a[60:63], v[68:71], v[8:11]\n"
  "v_exp_f32 v31, v31\n"
  "v_mfma_f32_16x16x32_bf16 v[8:11], a[64:67], v[72:75], v[8:11]\n"
  "v_cvt_pk_bf16_f32 v26, v28, v29\n"
  "v_cvt_pk_bf16_f32 v27, v30, v31\n"
  "v_mfma_f32_16x16x32_bf16 v[8:11], a[68:71], v[76:79], v[8:11]\n"
  "ds_write_b128 v80, v[24:27] offset:7168\n"
  "v_pk_mul_f32 v[0:1], v[0:1], s[60:61] op_sel_hi:[1,0]\n"
  "v_pk_mul_f32 v[2:3], v[2:3], s[60:61] op_sel_hi:[1,0]\n"
  "v_pk_mul_f32 v[4:5], v[4:5], s[60:61] op_sel_hi:[1,0]\n"
  "v_pk_mul_f32 v[6:7], v[6:7], s[60:61] op_sel_hi:[1,0]\n"
  "v_mfma_f32_16x16x32_bf16 v[12:15], a[92:95], v[76:79], v[92:95]\n"
  "v_exp_f32 v0, v0\n"
  "v_mfma_f32_16x16x32_bf16 v[12:15], a[88:91], v[72:75], v[12:15]\n"
  "v_exp_f32 v1, v1\n"
  "v_mfma_f32_16x16x32_bf16 v[12:15], a[84:87], v[68:71], v[12:15]\n"
  "v_exp_f32 v2, v2\n"
  "v_mfma_f32_16x16x32_bf16 v[12:15], a[80:83], v[64:67], v[12:15]\n"
  "v_exp_f32 v3, v3\n"
  "v_mfma_f32_16x16x32_bf16 v[12:15], a[76:79], v[60:63], v[12:15]\n"
  "v_cvt_pk_bf16_f32 v0, v0, v1\n"
  "v_cvt_pk_bf16_f32 v1, v2, v3\n"
  "v_mfma_f32_16x16x32_bf16 v[12:15], a[72:75], v[56:59], v[12:15]\n"
  "v_xor_b32 v80, s88, v80\n"
  "v_mfma_f32_16x16x32_bf16 v[16:19], a[96:99], v[56:59], v[92:95]\n"
  "v_exp_f32 v4, v4\n"
  "v_mfma_f32_16x16x32_bf16 v[16:19], a[100:103], v[60:63], v[16:19]\n"
  "s_xor_b32 s89, s88, s89\n"
  "v_exp_f32 v5, v5\n"
  "v_mfma_f32_16x16x32_bf16 v[16:19], a[104:107], v[64:67], v[16:19]\n"
  "s_xor_b32 s88, s88, s89\n"
  "v_exp_f32 v6, v6\n"
  "v_mfma_f32_16x16x32_bf16 v[16:19], a[108:111], v[68:71], v[16:19]\n"
  "v_exp_f32_e64 v7, v7\n"
  "v_mfma_f32_16x16x32_bf16 v[16:19], a[112:115], v[72:75], v[16:19]\n"
  "v_cvt_pk_bf16_f32 v2, v4, v5\n"
  "v_cvt_pk_bf16_f32 v3, v6, v7\n"
  "v_mfma_f32_16x16x32_bf16 v[16:19], a[116:119], v[76:79], v[16:19]\n"
  "ds_write_b128 v80, v[0:3] offset:0\n"
  "v_pk_mul_f32 v[8:9], v[8:9], s[60:61] op_sel_hi:[1,0]\n"
  "v_pk_mul_f32 v[10:11], v[10:11], s[60:61] op_sel_hi:[1,0]\n"
  "v_pk_mul_f32 v[12:13], v[12:13], s[60:61] op_sel_hi:[1,0]\n"
  "v_pk_mul_f32 v[14:15], v[14:15], s[60:61] op_sel_hi:[1,0]\n"
  "v_mfma_f32_16x16x32_bf16 v[20:23], a[140:143], v[76:79], v[92:95]\n"
  "v_exp_f32 v8, v8\n"
  "v_mfma_f32_16x16x32_bf16 v[20:23], a[136:139], v[72:75], v[20:23]\n"
  "v_exp_f32 v9, v9\n"
  "v_mfma_f32_16x16x32_bf16 v[20:23], a[132:135], v[68:71], v[20:23]\n"
  "v_exp_f32 v10, v10\n"
  "v_mfma_f32_16x16x32_bf16 v[20:23], a[128:131], v[64:67], v[20:23]\n"
  "v_exp_f32 v11, v11\n"
  "v_mfma_f32_16x16x32_bf16 v[20:23], a[124:127], v[60:63], v[20:23]\n"
  "v_cvt_pk_bf16_f32 v8, v8, v9\n"
  "v_cvt_pk_bf16_f32 v9, v10, v11\n"
  "v_mfma_f32_16x16x32_bf16 v[20:23], a[120:123], v[56:59], v[20:23]\n"
  "ds_read_b128 v[32:35], v81 offset:0\n"
  "ds_read_b128 v[36:39], v82 offset:0\n"
  "v_mfma_f32_16x16x32_bf16 v[24:27], a[144:147], v[56:59], v[92:95]\n"
  "v_exp_f32 v12, v12\n"
  "v_mfma_f32_16x16x32_bf16 v[24:27], a[148:151], v[60:63], v[24:27]\n"
  "v_exp_f32 v13, v13\n"
  "v_mfma_f32_16x16x32_bf16 v[24:27], a[152:155], v[64:67], v[24:27]\n"
  "v_exp_f32 v14, v14\n"
  "v_mfma_f32_16x16x32_bf16 v[24:27], a[156:159], v[68:71], v[24:27]\n"
  "v_exp_f32 v15, v15\n"
  "v_mfma_f32_16x16x32_bf16 v[24:27], a[160:163], v[72:75], v[24:27]\n"
  "v_cvt_pk_bf16_f32 v10, v12, v13\n"
  "v_cvt_pk_bf16_f32 v11, v14, v15\n"
  "v_mfma_f32_16x16x32_bf16 v[24:27], a[164:167], v[76:79], v[24:27]\n"
  "ds_write_b128 v80, v[8:11] offset:1024\n"
  "v_pk_mul_f32 v[16:17], v[16:17], s[60:61] op_sel_hi:[1,0]\n"
  "v_pk_mul_f32 v[18:19], v[18:19], s[60:61] op_sel_hi:[1,0]\n"
  "v_pk_mul_f32 v[20:21], v[20:21], s[60:61] op_sel_hi:[1,0]\n"
  "v_pk_mul_f32 v[22:23], v[22:23], s[60:61] op_sel_hi:[1,0]\n"
  "v_mfma_f32_16x16x32_bf16 v[28:31], a[188:191], v[76:79], v[92:95]\n"
  "v_exp_f32 v16, v16\n"
  "v_mfma_f32_16x16x32_bf16 v[28:31], a[184:187], v[72:75], v[28:31]\n"
  "v_exp_f32 v17, v17\n"
  "v_mfma_f32_16x16x32_bf16 v[28:31], a[180:183], v[68:71], v[28:31]\n"
  "v_exp_f32 v18, v18\n"
  "v_mfma_f32_16x16x32_bf16 v[28:31], a[176:179], v[64:67], v[28:31]\n"
  "v_exp_f32 v19, v19\n"
  "v_mfma_f32_16x16x32_bf16 v[28:31], a[172:175], v[60:63], v[28:31]\n"
  "v_cvt_pk_bf16_f32 v16, v16, v17\n"
  "v_cvt_pk_bf16_f32 v17, v18, v19\n"
  "v_mfma_f32_16x16x32_bf16 v[28:31], a[168:171], v[56:59], v[28:31]\n"
  "ds_read_b128 v[40:43], v81 offset:2048\n"
  "ds_read_b128 v[44:47], v82 offset:2048\n"
  "v_mfma_f32_16x16x32_bf16 v[0:3], a[192:195], v[56:59], v[92:95]\n"
  "v_exp_f32 v20, v20\n"
  "v_mfma_f32_16x16x32_bf16 v[0:3], a[196:199], v[60:63], v[0:3]\n"
  "v_exp_f32 v21, v21\n"
  "v_mfma_f32_16x16x32_bf16 v[0:3], a[200:203], v[64:67], v[0:3]\n"
  "v_exp_f32 v22, v22\n"
  "v_mfma_f32_16x16x32_bf16 v[0:3], a[204:207], v[68:71], v[0:3]\n"
  "v_exp_f32 v23, v23\n"
  "v_mfma_f32_16x16x32_bf16 v[0:3], a[208:211], v[72:75], v[0:3]\n"
  "v_cvt_pk_bf16_f32 v18, v20, v21\n"
  "v_cvt_pk_bf16_f32 v19, v22, v23\n"
  "v_mfma_f32_16x16x32_bf16 v[0:3], a[212:215], v[76:79], v[0:3]\n"
  "ds_write_b128 v80, v[16:19] offset:2048\n"
  "v_pk_mul_f32 v[24:25], v[24:25], s[60:61] op_sel_hi:[1,0]\n"
  "v_pk_mul_f32 v[26:27], v[26:27], s[60:61] op_sel_hi:[1,0]\n"
  "v_pk_mul_f32 v[28:29], v[28:29], s[60:61] op_sel_hi:[1,0]\n"
  "v_pk_mul_f32 v[30:31], v[30:31], s[60:61] op_sel_hi:[1,0]\n"
  "v_mfma_f32_16x16x32_bf16 v[4:7], a[236:239], v[76:79], v[92:95]\n"
  "v_exp_f32 v24, v24\n"
  "v_mfma_f32_16x16x32_bf16 v[4:7], a[232:235], v[72:75], v[4:7]\n"
  "v_exp_f32 v25, v25\n"
  "v_mfma_f32_16x16x32_bf16 v[4:7], a[228:231], v[68:71], v[4:7]\n"
  "v_exp_f32 v26, v26\n"
  "v_mfma_f32_16x16x32_bf16 v[4:7], a[224:227], v[64:67], v[4:7]\n"
  "v_exp_f32 v27, v27\n"
  "v_mfma_f32_16x16x32_bf16 v[4:7], a[220:223], v[60:63], v[4:7]\n"
  "v_cvt_pk_bf16_f32 v24, v24, v25\n"
  "v_cvt_pk_bf16_f32 v25, v26, v27\n"
  "v_mfma_f32_16x16x32_bf16 v[4:7], a[216:219], v[56:59], v[4:7]\n"
  "ds_read_b128 v[48:51], v81 offset:4096\n"
  "ds_read_b128 v[52:55], v82 offset:4096\n"
  "v_mfma_f32_16x16x32_bf16 v[8:11], a[240:243], v[56:59], v[92:95]\n"
  "v_exp_f32 v28, v28\n"
  "v_mfma_f32_16x16x32_bf16 v[8:11], a[244:247], v[60:63], v[8:11]\n"
  "v_exp_f32 v29, v29\n"
  "v_mfma_f32_16x16x32_bf16 v[8:11], a[248:251], v[64:67], v[8:11]\n"
  "v_exp_f32 v30, v30\n"
  "v_mfma_f32_16x16x32_bf16 v[8:11], a[252:255], v[68:71], v[8:11]\n"
  "v_exp_f32 v31, v31\n"
  "v_mfma_f32_16x16x32_bf16 v[8:11], v[128:131], v[72:75], v[8:11]\n"
  "v_cvt_pk_bf16_f32 v26, v28, v29\n"
  "v_cvt_pk_bf16_f32 v27, v30, v31\n"
  "v_mfma_f32_16x16x32_bf16 v[8:11], v[132:135], v[76:79], v[8:11]\n"
  "ds_write_b128 v80, v[24:27] offset:3072\n"
  "v_pk_mul_f32 v[0:1], v[0:1], s[60:61] op_sel_hi:[1,0]\n"
  "v_pk_mul_f32 v[2:3], v[2:3], s[60:61] op_sel_hi:[1,0]\n"
  "v_pk_mul_f32 v[4:5], v[4:5], s[60:61] op_sel_hi:[1,0]\n"
  "v_pk_mul_f32 v[6:7], v[6:7], s[60:61] op_sel_hi:[1,0]\n"
  "v_mfma_f32_16x16x32_bf16 v[12:15], v[156:159], v[76:79], v[92:95]\n"
  "v_exp_f32 v0, v0\n"
  "v_mfma_f32_16x16x32_bf16 v[12:15], v[152:155], v[72:75], v[12:15]\n"
  "v_exp_f32 v1, v1\n"
  "v_mfma_f32_16x16x32_bf16 v[12:15], v[148:151], v[68:71], v[12:15]\n"
  "v_exp_f32 v2, v2\n"
  "v_mfma_f32_16x16x32_bf16 v[12:15], v[144:147], v[64:67], v[12:15]\n"
  "v_exp_f32 v3, v3\n"
  "v_mfma_f32_16x16x32_bf16 v[12:15], v[140:143], v[60:63], v[12:15]\n"
  "v_cvt_pk_bf16_f32 v0, v0, v1\n"
  "v_cvt_pk_bf16_f32 v1, v2, v3\n"
  "v_mfma_f32_16x16x32_bf16 v[12:15], v[136:139], v[56:59], v[12:15]\n"
  "v_mul_f32 v8, s60, v8\n"
  "v_mul_f32 v9, s60, v9\n"
  "v_mfma_f32_16x16x32_bf16 v[16:19], v[160:163], v[56:59], v[92:95]\n"
  "v_exp_f32 v4, v4\n"
  "v_mfma_f32_16x16x32_bf16 v[16:19], v[164:167], v[60:63], v[16:19]\n"
  "v_exp_f32 v5, v5\n"
  "v_mfma_f32_16x16x32_bf16 v[16:19], v[168:171], v[64:67], v[16:19]\n"
  "v_exp_f32 v6, v6\n"
  "v_mfma_f32_16x16x32_bf16 v[16:19], v[172:175], v[68:71], v[16:19]\n"
  "v_exp_f32 v7, v7\n"
  "v_mfma_f32_16x16x32_bf16 v[16:19], v[176:179], v[72:75], v[16:19]\n"
  "v_cvt_pk_bf16_f32 v2, v4, v5\n"
  "v_cvt_pk_bf16_f32 v3, v6, v7\n"
  "v_mfma_f32_16x16x32_bf16 v[16:19], v[180:183], v[76:79], v[16:19]\n"
  "ds_write_b128 v80, v[0:3] offset:4096\n"
  "v_pk_mul_f32 v[10:11], v[10:11], s[60:61] op_sel_hi:[1,0]\n"
  "v_pk_mul_f32 v[12:13], v[12:13], s[60:61] op_sel_hi:[1,0]\n"
  "v_pk_mul_f32 v[14:15], v[14:15], s[60:61] op_sel_hi:[1,0]\n"
  "v_mfma_f32_16x16x32_bf16 v[20:23], v[204:207], v[76:79], v[92:95]\n"
  "v_exp_f32 v8, v8\n"
  "v_mfma_f32_16x16x32_bf16 v[20:23], v[200:203], v[72:75], v[20:23]\n"
  "v_exp_f32 v9, v9\n"
  "v_mfma_f32_16x16x32_bf16 v[20:23], v[196:199], v[68:71], v[20:23]\n"
  "v_exp_f32 v10, v10\n"
  "v_mfma_f32_16x16x32_bf16 v[20:23], v[192:195], v[64:67], v[20:23]\n"
  "v_exp_f32 v11, v11\n"
  "v_mfma_f32_16x16x32_bf16 v[20:23], v[188:191], v[60:63], v[20:23]\n"
  "v_cvt_pk_bf16_f32 v8, v8, v9\n"
  "v_cvt_pk_bf16_f32 v9, v10, v11\n"
  "v_mfma_f32_16x16x32_bf16 v[20:23], v[184:187], v[56:59], v[20:23]\n"
  "v_mul_f32 v16, s60, v16\n"
  "v_mul_f32 v17, s60, v17\n"
  "v_mfma_f32_16x16x32_bf16 v[24:27], v[208:211], v[56:59], v[92:95]\n"
  "v_exp_f32 v12, v12\n"
  "v_mfma_f32_16x16x32_bf16 v[24:27], v[212:215], v[60:63], v[24:27]\n"
  "v_exp_f32 v13, v13\n"
  "v_mfma_f32_16x16x32_bf16 v[24:27], v[216:219], v[64:67], v[24:27]\n"
  "v_exp_f32 v14, v14\n"
  "v_mfma_f32_16x16x32_bf16 v[24:27], v[220:223], v[68:71], v[24:27]\n"
  "v_exp_f32 v15, v15\n"
  "v_mfma_f32_16x16x32_bf16 v[24:27], v[224:227], v[72:75], v[24:27]\n"
  "v_cvt_pk_bf16_f32 v10, v12, v13\n"
  "v_cvt_pk_bf16_f32 v11, v14, v15\n"
  "v_mfma_f32_16x16x32_bf16 v[24:27], v[228:231], v[76:79], v[24:27]\n"
  "ds_write_b128 v80, v[8:11] offset:5120\n"
  "v_pk_mul_f32 v[18:19], v[18:19], s[60:61] op_sel_hi:[1,0]\n"
  "v_pk_mul_f32 v[20:21], v[20:21], s[60:61] op_sel_hi:[1,0]\n"
  "v_pk_mul_f32 v[22:23], v[22:23], s[60:61] op_sel_hi:[1,0]\n"
  "v_mfma_f32_16x16x32_bf16 v[28:31], v[252:255], v[76:79], v[92:95]\n"
  "v_exp_f32 v16, v16\n"
  "v_mfma_f32_16x16x32_bf16 v[28:31], v[248:251], v[72:75], v[28:31]\n"
  "v_exp_f32 v17, v17\n"
  "v_mfma_f32_16x16x32_bf16 v[28:31], v[244:247], v[68:71], v[28:31]\n"
  "v_exp_f32 v18, v18\n"
  "v_mfma_f32_16x16x32_bf16 v[28:31], v[240:243], v[64:67], v[28:31]\n"
  "s_sub_u32 s56, s56, 1\n"
  "v_exp_f32_e64 v19, v19\n"
  "v_mfma_f32_16x16x32_bf16 v[28:31], v[236:239], v[60:63], v[28:31]\n"
  "v_cvt_pk_bf16_f32 v16, v16, v17\n"
  "v_cvt_pk_bf16_f32 v17, v18, v19\n"
  "v_mfma_f32_16x16x32_bf16 v[28:31], v[232:235], v[56:59], v[28:31]\n"
  "s_waitcnt lgkmcnt(3)\n"
  "s_branch A_H0_%=\n"
  "A_LAST_%=:\n"
  "v_exp_f32 v20, v20\n"
  "v_exp_f32 v21, v21\n"
  "v_exp_f32 v22, v22\n"
  "v_exp_f32 v23, v23\n"
  "v_pk_mul_f32 v[24:25], v[24:25], s[60:61] op_sel_hi:[1,0]\n"
  "v_pk_mul_f32 v[26:27], v[26:27], s[60:61] op_sel_hi:[1,0]\n"
  "v_cvt_pk_bf16_f32 v18, v20, v21\n"
  "v_cvt_pk_bf16_f32 v19, v22, v23\n"
  "ds_write_b128 v80, v[16:19] offset:6144\n"
  "v_pk_mul_f32 v[28:29], v[28:29], s[60:61] op_sel_hi:[1,0]\n"
  "v_pk_mul_f32 v[30:31], v[30:31], s[60:61] op_sel_hi:[1,0]\n"
  "v_exp_f32 v24, v24\n"
  "v_exp_f32 v25, v25\n"
  "v_exp_f32 v26, v26\n"
  "v_exp_f32 v27, v27\n"
  "v_exp_f32 v28, v28\n"
  "v_exp_f32 v29, v29\n"
  "v_cvt_pk_bf16_f32 v24, v24, v25\n"
  "v_cvt_pk_bf16_f32 v25, v26, v27\n"
  "v_exp_f32 v30, v30\n"
  "v_exp_f32 v31, v31\n"
  "v_cvt_pk_bf16_f32 v26, v28, v29\n"
  "v_cvt_pk_bf16_f32 v27, v30, v31\n"
  "ds_write_b128 v80, v[24:27] offset:7168\n"
  "s_waitcnt lgkmcnt(0)\n"
  "s_barrier\n"
  "s_mov_b32 %0, 0\n"
  "s_branch L_END_%=\n"
  "B_START_%=:\n"
  "v_add_u32 v113, 0x800, v112\n"
  "s_mov_b32 s43, 0x20000\n"
  "s_sub_u32 s62, 53, s75\n"
  "s_bitcmp1_b32 %3, 3\n"
  "s_cselect_b32 s52, s62, s75\n"
  "s_cselect_b32 s54, s75, s62\n"
  "s_cselect_b32 s62, s75, 0\n"
  "s_lshl_b32 s62, s62, 8\n"
  "s_or_b32 s52, s52, s62\n"
  "s_pack_ll_b32_b16 s52, s52, s86\n"
  "s_add_u32 s62, %3, 128\n"
  "s_pack_ll_b32_b16 s53, 53, s62\n"
  "s_bitcmp1_b32 %3, 0\n"
  "s_cselect_b32 s62, -2, 2\n"
  "s_add_u32 s54, s54, s62\n"
  "s_sub_u32 s62, 53, s54\n"
  "s_lshl_b32 s62, s62, 8\n"
  "s_bfe_u32 s63, %3, 0x10003\n"
  "s_cselect_b32 s62, s62, 0\n"
  "s_or_b32 s54, s54, s62\n"
  "s_xor_b32 s63, s63, s86\n"
  "s_add_u32 s63, s63, 384\n"
  "s_pack_ll_b32_b16 s54, s54, s63\n"
  "s_mov_b32 s55, 0\n"
  "s_sub_u32 s40, s80, 2048\n"
  "s_subb_u32 s41, s81, 0\n"
  "s_mov_b32 s42, 100665344\n"
  "s_mov_b64 s[48:49], s[82:83]\n"
  "s_mov_b32 s50, 166723584\n"
  "s_mov_b32 s51, 0x20000\n"
  "s_lshr_b32 s62, s52, 16\n"
  "s_lshl1_add_u32 s62, s62, s65\n"
  "s_mul_i32 s57, s62, 98304\n"
  "s_and_b32 s56, s52, 0xff\n"
  "s_cmp_lg_u32 s53, 0\n"
  "s_cselect_b32 s62, 6, 0\n"
  "s_sub_u32 s56, s56, s62\n"
  "s_mov_b32 s88, 0x4000\n"
  "s_mov_b32 s89, 0xc000\n"
  "s_mov_b32 s68, 0xf0000000\n"
  "s_mov_b32 s69, 0\n"
  "s_sub_u32 s70, s67, s68\n"
  "s_mov_b32 s64, s67\n"
  "s_and_b32 s90, s52, 0xff\n"
  "s_sub_u32 s90, s90, 1\n"
  "s_mul_i32 s91, s90, 6144\n"
  "s_add_u32 s91, s91, s64\n"
  "s_lshr_b32 s62, s53, 16\n"
  "s_mul_i32 s62, s62, 53\n"
  "s_bfe_u32 s63, s53, 0x80008\n"
  "s_add_u32 s62, s62, s63\n"
  "s_mul_i32 s64, s62, 6144\n"
  "s_cmp_eq_u32 s53, 0\n"
  "s_cmov_b32 s64, 0xf0000000\n"
  "s_sub_u32 s91, s64, s91\n"
  "s_mov_b32 s96, s91\n"
  "s_and_b32 s92, s53, 0xff\n"
  "s_sub_u32 s92, s92, 1\n"
  "s_mul_i32 s93, s92, 6144\n"
  "s_add_u32 s93, s93, s64\n"
  "s_lshr_b32 s62, s54, 16\n"
  "s_mul_i32 s62, s62, 53\n"
  "s_bfe_u32 s63, s54, 0x80008\n"
  "s_add_u32 s62, s62, s63\n"
  "s_mul_i32 s64, s62, 6144\n"
  "s_cmp_eq_u32 s54, 0\n"
  "s_cmov_b32 s64, 0xf0000000\n"
  "s_sub_u32 s93, s64, s93\n"
  "s_mov_b32 s97, s93\n"
  "s_and_b32 s94, s54, 0xff\n"
  "s_sub_u32 s94, s94, 1\n"
  "s_mul_i32 s95, s94, 6144\n"
  "s_add_u32 s95, s95, s64\n"
  "s_lshr_b32 s62, s55, 16\n"
  "s_mul_i32 s62, s62, 53\n"
  "s_bfe_u32 s63, s55, 0x80008\n"
  "s_add_u32 s62, s62, s63\n"
  "s_mul_i32 s64, s62, 6144\n"
  "s_cmp_eq_u32 s55, 0\n"
  "s_cmov_b32 s64, 0xf0000000\n"
  "s_sub_u32 s95, s64, s95\n"
  "s_add_u32 s66, s66, 6144\n"
  "s_cmp_ge_u32 s66, 102400\n"
  "s_cselect_b32 s64, 36864, 0\n"
  "s_sub_u32 s66, s66, s64\n"
  "s_sub_u32 s90, s90, 1\n"
  "s_cselect_b32 s63, s91, 6144\n"
  "s_cmov_b64 s[90:91], s[92:93]\n"
  "s_cmov_b64 s[92:93], s[94:95]\n"
  "s_add_u32 s67, s67, s63\n"
  "s_add_u32 s66, s66, 6144\n"
  "s_cmp_ge_u32 s66, 102400\n"
  "s_cselect_b32 s64, 36864, 0\n"
  "s_sub_u32 s66, s66, s64\n"
  "s_sub_u32 s90, s90, 1\n"
  "s_cselect_b32 s63, s91, 6144\n"
  "s_cmov_b64 s[90:91], s[92:93]\n"
  "s_cmov_b64 s[92:93], s[94:95]\n"
  "s_add_u32 s67, s67, s63\n"
  "v_lshrrev_b32 v1, 3, v0\n"
  "v_mul_u32_u24 v114, 0x180, v1\n"
  "v_lshrrev_b32 v1, 4, v0\n"
  "v_and_b32 v2, 7, v0\n"
  "v_xor_b32 v2, v2, v1\n"
  "v_lshl_add_u32 v114, v2, 4, v114\n"
  "s_lshl_b32 s62, s65, 8\n"
  "s_add_u32 s62, s62, 2048\n"
  "v_add_u32 v114, s62, v114\n"
  "v_add_u32 v115, 0x800, v114\n"
  "v_lshrrev_b32 v2, 2, v0\n"
  "v_mul_u32_u24 v116, 0x180, v2\n"
  "v_and_b32 v1, 1, v1\n"
  "v_and_b32 v2, 3, v0\n"
  "v_xor_b32 v2, v2, v1\n"
  "v_lshl_add_u32 v116, v2, 4, v116\n"
  "s_lshl_b32 s62, s65, 6\n"
  "s_add_u32 s62, s62, 128\n"
  "v_add_u32 v116, s62, v116\n"
  "s_mul_i32 s85, s65, 86016\n"
  "s_sub_u32 s57, s57, s85\n"
  "s_add_u32 s85, s85, 18432\n"
  "s_mul_i32 s60, s65, 5120\n"
  "s_add_u32 s60, s60, 12288\n"
  "s_mul_i32 s84, s65, 24576\n"
  "s_sub_u32 s58, 6144, s84\n"
  "s_add_u32 s84, s84, 6144\n"
  "s_cmp_eq_u32 s65, 0\n"
  "s_cselect_b64 s[80:81], s[78:79], s[80:81]\n"
  "s_mov_b32 s82, 100663296\n"
  "s_mov_b32 s83, 0x20000\n"
  "s_mov_b64 s[76:77], s[78:79]\n"
  "s_mov_b32 s78, 100663296\n"
  "s_mov_b32 s79, 0x20000\n"
  "v_lshrrev_b32 v1, 2, v0\n"
  "v_lshlrev_b32 v117, 7, v1\n"
  "v_lshlrev_b32 v121, 6, v1\n"
  "v_and_b32 v2, 1, v0\n"
  "v_lshl_add_u32 v117, v2, 3, v117\n"
  "v_lshl_add_u32 v121, v2, 3, v121\n"
  "v_bfe_u32 v2, v0, 1, 1\n"
  "v_lshl_add_u32 v117, v2, 6, v117\n"
  "v_lshl_add_u32 v121, v2, 5, v121\n"
  "v_bfe_u32 v2, v0, 3, 2\n"
  "v_lshl_add_u32 v117, v2, 4, v117\n"
  "v_bfe_u32 v2, v0, 4, 1\n"
  "v_lshl_add_u32 v121, v2, 4, v121\n"
  "v_add_u32 v117, s59, v117\n"
  "v_add_u32 v121, s59, v121\n"
  "v_xor_b32 v118, 16, v117\n"
  "v_xor_b32 v119, 32, v117\n"
  "v_xor_b32 v120, 48, v117\n"
  "v_xor_b32 v122, 16, v121\n"
  "s_lshl_b32 s62, s65, 10\n"
  "v_lshlrev_b32 v104, 4, v0\n"
  "v_add_u32 v104, s62, v104\n"
  "v_xor_b32 v105, 0x400, v104\n"
  "v_lshrrev_b32 v1, 5, v0\n"
  "v_mul_u32_u24 v1, 0x480, v1\n"
  "v_and_b32 v2, 31, v0\n"
  "v_lshl_add_u32 v106, v2, 3, v1\n"
  "s_mul_i32 s62, s65, 4608\n"
  "s_add_u32 s62, s62, 49152\n"
  "v_add_u32 v106, s62, v106\n"
  "v_and_b32 v2, 7, v0\n"
  "v_lshl_add_u32 v107, v2, 3, v1\n"
  "v_bfe_u32 v2, v0, 3, 1\n"
  "v_lshl_add_u32 v107, v2, 2, v107\n"
  "v_bfe_u32 v2, v0, 4, 1\n"
  "v_lshl_add_u32 v107, v2, 7, v107\n"
  "s_lshl_b32 s62, s65, 6\n"
  "s_add_u32 s62, s62, 51456\n"
  "v_add_u32 v107, s62, v107\n"
  "v_lshrrev_b32 v1, 4, v0\n"
  "v_mul_u32_u24 v108, 0x120, v1\n"
  "v_and_b32 v2, 3, v0\n"
  "v_lshl_add_u32 v108, v2, 6, v108\n"
  "v_bfe_u32 v2, v0, 2, 2\n"
  "v_lshl_add_u32 v108, v2, 3, v108\n"
  "s_mul_i32 s62, s65, 1152\n"
  "s_add_u32 s62, s62, 57344\n"
  "v_add_u32 v108, s62, v108\n"
  "v_bfe_u32 v2, v0, 3, 1\n"
  "v_lshl_add_u32 v2, v2, 2, v1\n"
  "v_mul_u32_u24 v109, 0x180, v2\n"
  "v_and_b32 v2, 7, v0\n"
  "v_lshl_add_u32 v109, v2, 4, v109\n"
  "s_mul_i32 s62, s65, 3072\n"
  "v_add_u32 v109, s62, v109\n"
  "s_mov_b32 s62, 64256\n"
  "v_lshl_add_u32 v111, v1, 4, s62\n"
  "s_lshl_b32 s62, s65, 6\n"
  "s_add_u32 s62, s62, 56064\n"
  "v_lshl_add_u32 v110, v0, 2, s62\n"
  "v_mov_b32 v3, 0x40000\n"
  "v_cmp_gt_u32 vcc, 16, v0\n"
  "s_nop 1\n"
  "v_cndmask_b32 v110, v3, v110, vcc\n"
  "v_mov_b32 v44, 1.0\n"
  "ds_write_b32 v110, v44 offset:8192\n"
  "v_and_b32 v2, 15, v0\n"
  "v_mov_b32 v124, 0x44444444\n"
  "v_mov_b32 v3, 0xeeeeeeee\n"
  "v_cmp_eq_u32 vcc, 1, v2\n"
  "s_nop 1\n"
  "v_cndmask_b32 v124, v124, v3, vcc\n"
  "v_mov_b32 v3, 0x3f803f80\n"
  "v_cmp_gt_u32 vcc, 2, v2\n"
  "v_mov_b32 v80, 0\n"
  "v_mov_b32 v81, 0\n"
  "v_mov_b32 v82, 0\n"
  "v_mov_b32 v83, 0\n"
  "v_cndmask_b32 v88, 0, v3, vcc\n"
  "v_cndmask_b32 v89, 0, v3, vcc\n"
  "v_cndmask_b32 v90, 0, v3, vcc\n"
  "v_cndmask_b32 v91, 0, v3, vcc\n"
  "s_mov_b32 s99, 0\n"
  "s_waitcnt vmcnt(0)\n"
  "s_barrier\n"
  "v_lshlrev_b32 v125, 7, v0\n"
  "v_mov_b32 v126, 0\n"
  "s_mov_b32 s62, 0x28d22090\n"
  "v_writelane_b32 v126, s62, 2\n"
  "s_mov_b32 s62, 0x39163114\n"
  "v_writelane_b32 v126, s62, 3\n"
  "s_mov_b32 s62, 0x41980000\n"
  "v_writelane_b32 v126, s62, 4\n"
  "s_mov_b32 s62, 0x521c49da\n"
  "v_writelane_b32 v126, s62, 5\n"
  "s_mov_b32 s62, 0x5a1e\n"
  "v_writelane_b32 v126, s62, 6\n"
  "s_mov_b32 s62, 0x6ae262a0\n"
  "v_writelane_b32 v126, s62, 7\n"
  "s_mov_b32 s62, 0x7b267324\n"
  "v_writelane_b32 v126, s62, 8\n"
  "s_mov_b32 s62, 0x8ba80000\n"
  "v_writelane_b32 v126, s62, 9\n"
  "s_mov_b32 s62, 0x9c2c93ea\n"
  "v_writelane_b32 v126, s62, 10\n"
  "s_mov_b32 s62, 0xa4afa46e\n"
  "v_writelane_b32 v126, s62, 11\n"
  "s_mov_b32 s62, 0xb52baced\n"
  "v_writelane_b32 v126, s62, 12\n"
  "s_mov_b32 s62, 0xbd29\n"
  "v_writelane_b32 v126, s62, 13\n"
  "s_mov_b32 s62, 0xcde5c5a7\n"
  "v_writelane_b32 v126, s62, 14\n"
  "s_mov_b32 s62, 0xde21d623\n"
  "v_writelane_b32 v126, s62, 15\n"
  "s_mov_b32 s62, 0xe69f0000\n"
  "v_writelane_b32 v126, s62, 16\n"
  "s_mov_b32 s62, 0xf71beedd\n"
  "v_writelane_b32 v126, s62, 17\n"
  "s_mov_b32 s62, 0xff19\n"
  "v_writelane_b32 v126, s62, 18\n"
  "s_mov_b32 s62, 0x7d50797\n"
  "v_writelane_b32 v126, s62, 19\n"
  "s_mov_b32 s62, 0x110013\n"
  "v_writelane_b32 v126, s62, 20\n"
  "s_mov_b32 s62, 0x28f120b0\n"
  "v_writelane_b32 v126, s62, 29\n"
  "s_mov_b32 s62, 0x39333132\n"
  "v_writelane_b32 v126, s62, 30\n"
  "s_mov_b32 s62, 0x41b40000\n"
  "v_writelane_b32 v126, s62, 31\n"
  "s_mov_b32 s62, 0x523649f5\n"
  "v_writelane_b32 v126, s62, 32\n"
  "s_mov_b32 s62, 0x5a37\n"
  "v_writelane_b32 v126, s62, 33\n"
  "s_mov_b32 s62, 0x6af962b8\n"
  "v_writelane_b32 v126, s62, 34\n"
  "s_mov_b32 s62, 0x7b3b733a\n"
  "v_writelane_b32 v126, s62, 35\n"
  "s_mov_b32 s62, 0x8bbc0000\n"
  "v_writelane_b32 v126, s62, 36\n"
  "s_mov_b32 s62, 0x9c3e93fd\n"
  "v_writelane_b32 v126, s62, 37\n"
  "s_mov_b32 s62, 0xa48fa47f\n"
  "v_writelane_b32 v126, s62, 38\n"
  "s_mov_b32 s62, 0xb50dacce\n"
  "v_writelane_b32 v126, s62, 39\n"
  "s_mov_b32 s62, 0xbd0c\n"
  "v_writelane_b32 v126, s62, 40\n"
  "s_mov_b32 s62, 0xcdcac58b\n"
  "v_writelane_b32 v126, s62, 41\n"
  "s_mov_b32 s62, 0xde08d609\n"
  "v_writelane_b32 v126, s62, 42\n"
  "s_mov_b32 s62, 0xe6870000\n"
  "v_writelane_b32 v126, s62, 43\n"
  "s_mov_b32 s62, 0xf705eec6\n"
  "v_writelane_b32 v126, s62, 44\n"
  "s_mov_b32 s62, 0xff04\n"
  "v_writelane_b32 v126, s62, 45\n"
  "s_mov_b32 s62, 0x7c20783\n"
  "v_writelane_b32 v126, s62, 46\n"
  "s_mov_b32 s62, 0x8000001\n"
  "v_writelane_b32 v126, s62, 47\n"
  "s_sub_u32 s62, 31, s87\n"
  "s_bitcmp1_b32 %3, 3\n"
  "s_cselect_b32 s62, s62, s87\n"
  "s_and_b32 s63, %3, 7\n"
  "s_mul_i32 s63, s63, 24\n"
  "v_mov_b32 v3, 0xffff\n"
  "s_mul_i32 s64, s65, 5\n"
  "v_mov_b32 v6, s63\n"
  "s_and_b32 s63, %3, 7\n"
  "s_xor_b32 s63, s63, 1\n"
  "s_mul_i32 s63, s63, 24\n"
  "s_add_u32 s63, s63, 9216\n"
  "v_mov_b32 v7, s63\n"
  "s_add_u32 s64, s64, 6\n"
  "v_bfe_u32 v2, v126, 0, 6\n"
  "v_mul_u32_u24 v4, 192, v2\n"
  "v_lshrrev_b32 v1, s64, v126\n"
  "v_cmp_gt_u32 vcc, 16, v2\n"
  "s_nop 1\n"
  "v_cndmask_b32 v8, v6, v7, vcc\n"
  "v_bfe_u32 v2, v126, 0, 16\n"
  "v_sub_u32 v1, s62, v1\n"
  "v_and_b32 v1, 31, v1\n"
  "v_cmp_gt_u32 vcc, 24, v1\n"
  "v_add3_u32 v4, v4, v1, v8\n"
  "s_nop 0\n"
  "v_cndmask_b32 v4, v3, v4, vcc\n"
  "v_cmp_ne_u32 vcc, 0, v2\n"
  "s_nop 1\n"
  "v_cndmask_b32 v4, v3, v4, vcc\n"
  "s_add_u32 s64, s64, 16\n"
  "v_bfe_u32 v2, v126, 16, 6\n"
  "v_mul_u32_u24 v5, 192, v2\n"
  "v_lshrrev_b32 v1, s64, v126\n"
  "v_cmp_gt_u32 vcc, 16, v2\n"
  "s_nop 1\n"
  "v_cndmask_b32 v8, v6, v7, vcc\n"
  "v_bfe_u32 v2, v126, 16, 16\n"
  "v_sub_u32 v1, s62, v1\n"
  "v_and_b32 v1, 31, v1\n"
  "v_cmp_gt_u32 vcc, 24, v1\n"
  "v_add3_u32 v5, v5, v1, v8\n"
  "s_nop 0\n"
  "v_cndmask_b32 v5, v3, v5, vcc\n"
  "v_cmp_ne_u32 vcc, 0, v2\n"
  "s_nop 1\n"
  "v_cndmask_b32 v5, v3, v5, vcc\n"
  "v_lshl_or_b32 v126, v5, 16, v4\n"
  "s_mul_i32 s72, s65, -3\n"
  "s_add_u32 s72, s72, 1\n"
  "s_mov_b32 s71, 0x53800000\n"
  "s_barrier\n"
  "s_add_u32 m0, s66, 0\n"
  "s_nop 0\n"
  "buffer_load_dwordx4 v112, s[44:47], s67 offen offset:0 nt lds\n"
  "s_add_u32 m0, s66, 1920\n"
  "s_nop 0\n"
  "buffer_load_dwordx4 v112, s[44:47], s67 offen offset:128 nt lds\n"
  "s_add_u32 m0, s66, 3840\n"
  "s_nop 0\n"
  "buffer_load_dwordx4 v112, s[44:47], s67 offen offset:256 nt lds\n"
  "s_add_u32 s66, s66, 6144\n"
  "s_cmp_ge_u32 s66, 102400\n"
  "s_cselect_b32 s64, 36864, 0\n"
  "s_sub_u32 s66, s66, s64\n"
  "s_sub_u32 s90, s90, 1\n"
  "s_cselect_b32 s63, s91, 6144\n"
  "s_cmov_b64 s[90:91], s[92:93]\n"
  "s_cmov_b64 s[92:93], s[94:95]\n"
  "s_add_u32 s67, s67, s63\n"
  "s_add_u32 m0, s66, 0\n"
  "s_nop 0\n"
  "buffer_load_dwordx4 v112, s[44:47], s67 offen offset:0 nt lds\n"
  "s_add_u32 m0, s66, 1920\n"
  "s_nop 0\n"
  "buffer_load_dwordx4 v112, s[44:47], s67 offen offset:128 nt lds\n"
  "s_add_u32 m0, s66, 3840\n"
  "s_nop 0\n"
  "buffer_load_dwordx4 v112, s[44:47], s67 offen offset:256 nt lds\n"
  "s_add_u32 s66, s66, 6144\n"
  "s_cmp_ge_u32 s66, 102400\n"
  "s_cselect_b32 s64, 36864, 0\n"
  "s_sub_u32 s66, s66, s64\n"
  "s_sub_u32 s90, s90, 1\n"
  "s_cselect_b32 s63, s91, 6144\n"
  "s_cmov_b64 s[90:91], s[92:93]\n"
  "s_cmov_b64 s[92:93], s[94:95]\n"
  "s_add_u32 s67, s67, s63\n"
  "s_add_u32 m0, s66, 0\n"
  "s_nop 0\n"
  "buffer_load_dwordx4 v112, s[44:47], s67 offen offset:0 nt lds\n"
  "s_add_u32 m0, s66, 1920\n"
  "s_nop 0\n"
  "buffer_load_dwordx4 v112, s[44:47], s67 offen offset:128 nt lds\n"
  "s_add_u32 m0, s66, 3840\n"
  "s_nop 0\n"
  "buffer_load_dwordx4 v112, s[44:47], s67 offen offset:256 nt lds\n"
  "s_add_u32 s66, s66, 6144\n"
  "s_cmp_ge_u32 s66, 102400\n"
  "s_cselect_b32 s64, 36864, 0\n"
  "s_sub_u32 s66, s66, s64\n"
  "s_sub_u32 s90, s90, 1\n"
  "s_cselect_b32 s63, s91, 6144\n"
  "s_cmov_b64 s[90:91], s[92:93]\n"
  "s_cmov_b64 s[92:93], s[94:95]\n"
  "s_add_u32 s67, s67, s63\n"
  "s_add_u32 m0, s66, 0\n"
  "s_nop 0\n"
  "buffer_load_dwordx4 v112, s[44:47], s67 offen offset:0 nt lds\n"
  "s_add_u32 m0, s66, 1920\n"
  "s_nop 0\n"
  "buffer_load_dwordx4 v112, s[44:47], s67 offen offset:128 nt lds\n"
  "s_add_u32 m0, s66, 3840\n"
  "s_nop 0\n"
  "buffer_load_dwordx4 v112, s[44:47], s67 offen offset:256 nt lds\n"
  "s_add_u32 s66, s66, 6144\n"
  "s_cmp_ge_u32 s66, 102400\n"
  "s_cselect_b32 s64, 36864, 0\n"
  "s_sub_u32 s66, s66, s64\n"
  "s_sub_u32 s90, s90, 1\n"
  "s_cselect_b32 s63, s91, 6144\n"
  "s_cmov_b64 s[90:91], s[92:93]\n"
  "s_cmov_b64 s[92:93], s[94:95]\n"
  "s_add_u32 s67, s67, s63\n"
  "s_add_u32 m0, s59, 21504\n"
  "s_nop 0\n"
  "buffer_load_dwordx4 v114, s[40:43], s57 offen offset:0  lds\n"
  "buffer_load_dwordx4 v115, s[40:43], s57 offen offset:1024  lds\n"
  "buffer_load_dwordx4 v116, s[40:43], s57 offen offset:2048  lds\n"
  "s_add_u32 s57, s57, 6144\n"
  "s_add_u32 m0, s59, 24576\n"
  "s_nop 0\n"
  "buffer_load_dwordx4 v114, s[40:43], s57 offen offset:0  lds\n"
  "buffer_load_dwordx4 v115, s[40:43], s57 offen offset:1024  lds\n"
  "buffer_load_dwordx4 v116, s[40:43], s57 offen offset:2048  lds\n"
  "s_add_u32 s57, s57, s58\n"
  "s_add_u32 m0, s59, 27648\n"
  "s_nop 0\n"
  "buffer_load_dwordx4 v114, s[40:43], s57 offen offset:0  lds\n"
  "buffer_load_dwordx4 v115, s[40:43], s57 offen offset:1024  lds\n"
  "buffer_load_dwordx4 v116, s[40:43], s57 offen offset:2048  lds\n"
  "s_add_u32 s57, s57, 6144\n"
  "s_barrier\n"
  "s_add_u32 m0, s59, 0\n"
  "s_nop 0\n"
  "buffer_load_dwordx4 v114, s[40:43], s57 offen offset:0  lds\n"
  "buffer_load_dwordx4 v115, s[40:43], s57 offen offset:1024  lds\n"
  "buffer_load_dwordx4 v116, s[40:43], s57 offen offset:2048  lds\n"
  "s_add_u32 s57, s57, s84\n"
  "s_add_u32 m0, s59, 3072\n"
  "s_nop 0\n"
  "buffer_load_dwordx4 v114, s[40:43], s57 offen offset:0  lds\n"
  "buffer_load_dwordx4 v115, s[40:43], s57 offen offset:1024  lds\n"
  "buffer_load_dwordx4 v116, s[40:43], s57 offen offset:2048  lds\n"
  "s_add_u32 s57, s57, 6144\n"
  "s_add_u32 m0, s59, 6144\n"
  "s_nop 0\n"
  "buffer_load_dwordx4 v114, s[40:43], s57 offen offset:0  lds\n"
  "buffer_load_dwordx4 v115, s[40:43], s57 offen offset:1024  lds\n"
  "buffer_load_dwordx4 v116, s[40:43], s57 offen offset:2048  lds\n"
  "s_add_u32 s57, s57, s58\n"
  "s_add_u32 m0, s59, 9216\n"
  "s_nop 0\n"
  "buffer_load_dwordx4 v114, s[40:43], s57 offen offset:0  lds\n"
  "buffer_load_dwordx4 v115, s[40:43], s57 offen offset:1024  lds\n"
  "buffer_load_dwordx4 v116, s[40:43], s57 offen offset:2048  lds\n"
  "s_add_u32 s57, s57, 6144\n"
  "s_add_u32 m0, s59, 12288\n"
  "s_nop 0\n"
  "buffer_load_dwordx4 v114, s[40:43], s57 offen offset:0  lds\n"
  "buffer_load_dwordx4 v115, s[40:43], s57 offen offset:1024  lds\n"
  "buffer_load_dwordx4 v116, s[40:43], s57 offen offset:2048  lds\n"
  "s_add_u32 s57, s57, s84\n"
  "s_add_u32 m0, s59, 15360\n"
  "s_nop 0\n"
  "buffer_load_dwordx4 v114, s[40:43], s57 offen offset:0  lds\n"
  "buffer_load_dwordx4 v115, s[40:43], s57 offen offset:1024  lds\n"
  "buffer_load_dwordx4 v116, s[40:43], s57 offen offset:2048  lds\n"
  "s_add_u32 s57, s57, 6144\n"
  "s_add_u32 m0, s59, 18432\n"
  "s_nop 0\n"
  "buffer_load_dwordx4 v114, s[40:43], s57 offen offset:0  lds\n"
  "buffer_load_dwordx4 v115, s[40:43], s57 offen offset:1024  lds\n"
  "buffer_load_dwordx4 v116, s[40:43], s57 offen offset:2048  lds\n"
  "s_add_u32 s57, s57, s58\n"
  "s_waitcnt vmcnt(27)\n"
  "ds_read_b64 v[32:33], v117 offset:21504\n"
  "ds_read_b64 v[34:35], v118 offset:21504\n"
  "s_waitcnt lgkmcnt(0)\n"
  "v_max3_f32 v32, |v32|, |v33|, |v34|\n"
  "v_max3_f32 v32, v32, |v35|, 0\n"
  "v_max_f32 v32, 0, v32\n"
  "v_mul_f32 v33, 0x40000000, v32\n"
  "v_mul_f32 v32, 0x2b800000, v32\n"
  "v_max_f32 v82, v82, v32\n"
  "s_nop 0\n"
  "v_readlane_b32 s98, v33, 0\n"
  "v_readlane_b32 s62, v33, 21\n"
  "v_readlane_b32 s63, v33, 42\n"
  "v_readlane_b32 s64, v33, 63\n"
  "s_nop 1\n"
  "s_max_u32 s98, s98, s62\n"
  "s_max_u32 s63, s63, s64\n"
  "s_max_u32 s98, s98, s63\n"
  "s_and_b32 s98, s98, 0x7f800000\n"
  "s_cselect_b32 s62, 0x400000, 0\n"
  "s_or_b32 s98, s98, s62\n"
  "s_nop 0\n"
  "v_mov_b32 v100, s98\n"
  "v_mov_b32 v101, s98\n"
  "v_mov_b32 v102, s98\n"
  "v_mov_b32 v103, s98\n"
  "ds_read_b64_tr_b16 a[0:1], v117 offset:21504\n"
  "ds_read_b64_tr_b16 a[4:5], v118 offset:21504\n"
  "ds_read_b64_tr_b16 a[8:9], v119 offset:21504\n"
  "ds_read_b64_tr_b16 a[12:13], v120 offset:21504\n"
  "ds_read_b64_tr_b16 a[16:17], v121 offset:23552\n"
  "ds_read_b64_tr_b16 a[20:21], v122 offset:23552\n"
  "s_waitcnt vmcnt(24)\n"
  "ds_read_b64_tr_b16 a[2:3], v117 offset:24576\n"
  "ds_read_b64_tr_b16 a[6:7], v118 offset:24576\n"
  "s_add_u32 m0, s59, 21504\n"
  "ds_read_b64_tr_b16 a[10:11], v119 offset:24576\n"
  "ds_read_b64_tr_b16 a[14:15], v120 offset:24576\n"
  "s_waitcnt lgkmcnt(4)\n"
  "buffer_load_dwordx4 v114, s[40:43], s57 offen offset:0  lds\n"
  "buffer_load_dwordx4 v115, s[40:43], s57 offen offset:1024  lds\n"
  "ds_read_b64_tr_b16 a[18:19], v121 offset:26624\n"
  "ds_read_b64_tr_b16 a[22:23], v122 offset:26624\n"
  "s_waitcnt vmcnt(23)\n"
  "ds_read_b64_tr_b16 a[24:25], v117 offset:27648\n"
  "ds_read_b64_tr_b16 a[28:29], v118 offset:27648\n"
  "buffer_load_dwordx4 v116, s[40:43], s57 offen offset:2048  lds\n"
  "s_add_u32 s57, s57, 6144\n"
  "s_add_u32 m0, s59, 24576\n"
  "ds_read_b64_tr_b16 a[32:33], v119 offset:27648\n"
  "ds_read_b64_tr_b16 a[36:37], v120 offset:27648\n"
  "s_waitcnt lgkmcnt(4)\n"
  "buffer_load_dwordx4 v114, s[40:43], s57 offen offset:0  lds\n"
  "buffer_load_dwordx4 v115, s[40:43], s57 offen offset:1024  lds\n"
  "ds_read_b64_tr_b16 a[40:41], v121 offset:29696\n"
  "ds_read_b64_tr_b16 a[44:45], v122 offset:29696\n"
  "s_waitcnt vmcnt(23)\n"
  "ds_read_b64_tr_b16 a[26:27], v117 offset:0\n"
  "ds_read_b64_tr_b16 a[30:31], v118 offset:0\n"
  "buffer_load_dwordx4 v116, s[40:43], s57 offen offset:2048  lds\n"
  "s_add_u32 s57, s57, s84\n"
  "s_add_u32 m0, s59, 27648\n"
  "ds_read_b64_tr_b16 a[34:35], v119 offset:0\n"
  "ds_read_b64_tr_b16 a[38:39], v120 offset:0\n"
  "s_waitcnt lgkmcnt(4)\n"
  "buffer_load_dwordx4 v114, s[40:43], s57 offen offset:0  lds\n"
  "buffer_load_dwordx4 v115, s[40:43], s57 offen offset:1024  lds\n"
  "ds_read_b64_tr_b16 a[42:43], v121 offset:2048\n"
  "ds_read_b64_tr_b16 a[46:47], v122 offset:2048\n"
  "s_waitcnt vmcnt(23)\n"
  "ds_read_b64_tr_b16 a[48:49], v117 offset:3072\n"
  "ds_read_b64_tr_b16 a[52:53], v118 offset:3072\n"
  "buffer_load_dwordx4 v116, s[40:43], s57 offen offset:2048  lds\n"
  "s_add_u32 s57, s57, 6144\n"
  "s_add_u32 m0, s59, 0\n"
  "ds_read_b64_tr_b16 a[56:57], v119 offset:3072\n"
  "ds_read_b64_tr_b16 a[60:61], v120 offset:3072\n"
  "s_waitcnt lgkmcnt(4)\n"
  "buffer_load_dwordx4 v114, s[40:43], s57 offen offset:0  lds\n"
  "buffer_load_dwordx4 v115, s[40:43], s57 offen offset:1024  lds\n"
  "ds_read_b64_tr_b16 a[64:65], v121 offset:5120\n"
  "ds_read_b64_tr_b16 a[68:69], v122 offset:5120\n"
  "s_waitcnt vmcnt(23)\n"
  "ds_read_b64_tr_b16 a[50:51], v117 offset:6144\n"
  "ds_read_b64_tr_b16 a[54:55], v118 offset:6144\n"
  "buffer_load_dwordx4 v116, s[40:43], s57 offen offset:2048  lds\n"
  "s_add_u32 s57, s57, s58\n"
  "s_add_u32 m0, s59, 3072\n"
  "ds_read_b64_tr_b16 a[58:59], v119 offset:6144\n"
  "ds_read_b64_tr_b16 a[62:63], v120 offset:6144\n"
  "s_waitcnt lgkmcnt(4)\n"
  "buffer_load_dwordx4 v114, s[40:43], s57 offen offset:0  lds\n"
  "buffer_load_dwordx4 v115, s[40:43], s57 offen offset:1024  lds\n"
  "ds_read_b64_tr_b16 a[66:67], v121 offset:8192\n"
  "ds_read_b64_tr_b16 a[70:71], v122 offset:8192\n"
  "s_waitcnt vmcnt(23)\n"
  "ds_read_b64_tr_b16 a[72:73], v117 offset:9216\n"
  "ds_read_b64_tr_b16 a[76:77], v118 offset:9216\n"
  "buffer_load_dwordx4 v116, s[40:43], s57 offen offset:2048  lds\n"
  "s_add_u32 s57, s57, 6144\n"
  "s_add_u32 m0, s59, 6144\n"
  "ds_read_b64_tr_b16 a[80:81], v119 offset:9216\n"
  "ds_read_b64_tr_b16 a[84:85], v120 offset:9216\n"
  "s_waitcnt lgkmcnt(4)\n"
  "buffer_load_dwordx4 v114, s[40:43], s57 offen offset:0  lds\n"
  "buffer_load_dwordx4 v115, s[40:43], s57 offen offset:1024  lds\n"
  "ds_read_b64_tr_b16 a[88:89], v121 offset:11264\n"
  "ds_read_b64_tr_b16 a[92:93], v122 offset:11264\n"
  "s_waitcnt vmcnt(23)\n"
  "ds_read_b64_tr_b16 a[74:75], v117 offset:12288\n"
  "ds_read_b64_tr_b16 a[78:79], v118 offset:12288\n"
  "buffer_load_dwordx4 v116, s[40:43], s57 offen offset:2048  lds\n"
  "s_add_u32 s57, s57, s84\n"
  "s_add_u32 m0, s59, 9216\n"
  "ds_read_b64_tr_b16 a[82:83], v119 offset:12288\n"
  "ds_read_b64_tr_b16 a[86:87], v120 offset:12288\n"
  "s_waitcnt lgkmcnt(0)\n"
  "s_barrier\n"
  "buffer_load_dwordx4 v114, s[40:43], s57 offen offset:0  lds\n"
  "buffer_load_dwordx4 v115, s[40:43], s57 offen offset:1024  lds\n"
  "ds_read_b128 v[48:51], v104 offset:0\n"
  "ds_read_b64_tr_b16 a[90:91], v121 offset:14336\n"
  "ds_read_b64_tr_b16 a[94:95], v122 offset:14336\n"
  "s_waitcnt vmcnt(23)\n"
  "ds_read_b64_tr_b16 a[96:97], v117 offset:15360\n"
  "ds_read_b64_tr_b16 a[100:101], v118 offset:15360\n"
  "ds_read_b128 v[84:87], v111\n"
  "ds_read_b128 v[96:99], v111 offset:64\n"
  "s_xor_b32 s89, s88, s89\n"
  "buffer_load_dwordx4 v116, s[40:43], s57 offen offset:2048  lds\n"
  "ds_read_b128 v[52:55], v104 offset:2048\n"
  "v_xor_b32 v111, 0x2000, v111\n"
  "s_add_u32 s57, s57, 6144\n"
  "s_add_u32 m0, s59, 12288\n"
  "ds_read_b64_tr_b16 a[104:105], v119 offset:15360\n"
  "ds_read_b64_tr_b16 a[108:109], v120 offset:15360\n"
  "s_waitcnt lgkmcnt(7)\n"
  "buffer_load_dwordx4 v114, s[40:43], s57 offen offset:0  lds\n"
  "buffer_load_dwordx4 v115, s[40:43], s57 offen offset:1024  lds\n"
  "ds_read_b64_tr_b16 a[112:113], v121 offset:17408\n"
  "ds_read_b64_tr_b16 a[116:117], v122 offset:17408\n"
  "s_waitcnt vmcnt(23)\n"
  "ds_read_b64_tr_b16 a[98:99], v117 offset:18432\n"
  "ds_read_b64_tr_b16 a[102:103], v118 offset:18432\n"
  "s_sub_u32 s56, s56, 1\n"
  "buffer_load_dwordx4 v116, s[40:43], s57 offen offset:2048  lds\n"
  "ds_read_b128 v[56:59], v105 offset:0\n"
  "s_add_u32 s57, s57, s58\n"
  "s_add_u32 m0, s59, 15360\n"
  "ds_read_b64_tr_b16 a[106:107], v119 offset:18432\n"
  "ds_read_b64_tr_b16 a[110:111], v120 offset:18432\n"
  "s_waitcnt lgkmcnt(5)\n"
  "buffer_load_dwordx4 v114, s[40:43], s57 offen offset:0  lds\n"
  "buffer_load_dwordx4 v115, s[40:43], s57 offen offset:1024  lds\n"
  "ds_read_b64_tr_b16 a[114:115], v121 offset:20480\n"
  "ds_read_b64_tr_b16 a[118:119], v122 offset:20480\n"
  "s_waitcnt vmcnt(23)\n"
  "ds_read_b64_tr_b16 a[120:121], v117 offset:21504\n"
  "ds_read_b64_tr_b16 a[124:125], v118 offset:21504\n"
  "buffer_load_dwordx4 v116, s[40:43], s57 offen offset:2048  lds\n"
  "ds_read_b128 v[60:63], v105 offset:2048\n"
  "v_add_f32 v84, v84, v96\n"
  "s_add_u32 s57, s57, 6144\n"
  "s_add_u32 m0, s59, 18432\n"
  "ds_read_b64_tr_b16 a[128:129], v119 offset:21504\n"
  "ds_read_b64_tr_b16 a[132:133], v120 offset:21504\n"
  "v_add_f32 v85, v85, v97\n"
  "v_add_f32 v86, v86, v98\n"
  "s_waitcnt lgkmcnt(5)\n"
  "buffer_load_dwordx4 v114, s[40:43], s57 offen offset:0  lds\n"
  "buffer_load_dwordx4 v115, s[40:43], s57 offen offset:1024  lds\n"
  "ds_read_b64_tr_b16 a[136:137], v121 offset:23552\n"
  "ds_read_b64_tr_b16 a[140:141], v122 offset:23552\n"
  "s_waitcnt vmcnt(23)\n"
  "ds_read_b64_tr_b16 a[122:123], v117 offset:24576\n"
  "ds_read_b64_tr_b16 a[126:127], v118 offset:24576\n"
  "v_add_f32 v87, v87, v99\n"
  "v_max3_f32 v82, v82, v84, v85\n"
  "s_sub_i32 s61, s56, 5\n"
  "s_max_i32 s61, s61, 0\n"
  "buffer_load_dwordx4 v116, s[40:43], s57 offen offset:2048  lds\n"
  "v_rcp_f32 v84, v84\n"
  "s_add_u32 s57, s57, s84\n"
  "s_add_u32 m0, s59, 21504\n"
  "ds_read_b64_tr_b16 a[130:131], v119 offset:24576\n"
  "ds_read_b64_tr_b16 a[134:135], v120 offset:24576\n"
  "v_rcp_f32 v85, v85\n"
  "s_waitcnt lgkmcnt(4)\n"
  "buffer_load_dwordx4 v114, s[40:43], s57 offen offset:0  lds\n"
  "buffer_load_dwordx4 v115, s[40:43], s57 offen offset:1024  lds\n"
  "ds_read_b64_tr_b16 a[138:139], v121 offset:26624\n"
  "ds_read_b64_tr_b16 a[142:143], v122 offset:26624\n"
  "s_waitcnt vmcnt(23)\n"
  "ds_read_b64_tr_b16 a[144:145], v117 offset:27648\n"
  "ds_read_b64_tr_b16 a[148:149], v118 offset:27648\n"
  "v_max3_f32 v82, v82, v86, v87\n"
  "v_max3_f32 v83, v83, v84, v85\n"
  "buffer_load_dwordx4 v116, s[40:43], s57 offen offset:2048  lds\n"
  "v_rcp_f32 v86, v86\n"
  "s_add_u32 s57, s57, 6144\n"
  "s_add_u32 m0, s59, 24576\n"
  "ds_read_b64_tr_b16 a[152:153], v119 offset:27648\n"
  "ds_read_b64_tr_b16 a[156:157], v120 offset:27648\n"
  "v_rcp_f32 v87, v87\n"
  "s_waitcnt lgkmcnt(4)\n"
  "buffer_load_dwordx4 v114, s[40:43], s57 offen offset:0  lds\n"
  "buffer_load_dwordx4 v115, s[40:43], s57 offen offset:1024  lds\n"
  "ds_read_b64_tr_b16 a[160:161], v121 offset:29696\n"
  "ds_read_b64_tr_b16 a[164:165], v122 offset:29696\n"
  "s_waitcnt vmcnt(23)\n"
  "ds_read_b64_tr_b16 a[146:147], v117 offset:0\n"
  "ds_read_b64_tr_b16 a[150:151], v118 offset:0\n"
  "v_max3_f32 v83, v83, v86, v87\n"
  "v_mul_f32 v96, s99, v84\n"
  "buffer_load_dwordx4 v116, s[40:43], s57 offen offset:2048  lds\n"
  "v_mul_f32 v97, s99, v85\n"
  "v_mul_f32 v98, s99, v86\n"
  "s_add_u32 s57, s57, s58\n"
  "s_add_u32 m0, s59, 27648\n"
  "ds_read_b64_tr_b16 a[154:155], v119 offset:0\n"
  "ds_read_b64_tr_b16 a[158:159], v120 offset:0\n"
  "v_mul_f32 v99, s99, v87\n"
  "v_fma_f32 v40, v40, v84, v96\n"
  "v_mfma_f32_16x16x32_bf16 v[0:3], v[48:51], a[0:3], v[100:103]\n"
  "s_waitcnt lgkmcnt(4)\n"
  "buffer_load_dwordx4 v114, s[40:43], s57 offen offset:0  lds\n"
  "buffer_load_dwordx4 v115, s[40:43], s57 offen offset:1024  lds\n"
  "ds_read_b128 v[64:67], v104 offset:4096\n"
  "ds_read_b64_tr_b16 a[162:163], v121 offset:2048\n"
  "ds_read_b64_tr_b16 a[166:167], v122 offset:2048\n"
  "v_mfma_f32_16x16x32_bf16 v[0:3], v[56:59], a[24:27], v[0:3]\n"
  "s_waitcnt vmcnt(23)\n"
  "ds_read_b64_tr_b16 a[168:169], v117 offset:3072\n"
  "ds_read_b64_tr_b16 a[172:173], v118 offset:3072\n"
  "v_fma_f32 v44, v44, v84, v96\n"
  "v_fma_f32 v41, v41, v85, v97\n"
  "v_mfma_f32_16x16x32_bf16 v[4:7], v[56:59], a[28:31], v[100:103]\n"
  "s_xor_b32 s99, 0x80000000, s98\n"
  "buffer_load_dwordx4 v116, s[40:43], s57 offen offset:2048  lds\n"
  "ds_read_b128 v[68:71], v104 offset:6144\n"
  "v_fma_f32 v45, v45, v85, v97\n"
  "v_mfma_f32_16x16x32_bf16 v[4:7], v[48:51], a[4:7], v[4:7]\n"
  "s_add_u32 s57, s57, 6144\n"
  "s_add_u32 m0, s59, 0\n"
  "ds_read_b64_tr_b16 a[176:177], v119 offset:3072\n"
  "ds_read_b64_tr_b16 a[180:181], v120 offset:3072\n"
  "ds_read_b64_tr_b16 v[92:93], v108 offset:0\n"
  "ds_read_b64_tr_b16 v[94:95], v108 offset:32\n"
  "v_mfma_f32_16x16x32_bf16 v[8:11], v[48:51], a[8:11], v[100:103]\n"
  "s_waitcnt lgkmcnt(7)\n"
  "buffer_load_dwordx4 v114, s[40:43], s57 offen offset:0  lds\n"
  "buffer_load_dwordx4 v115, s[40:43], s57 offen offset:1024  lds\n"
  "ds_read_b64_tr_b16 a[184:185], v121 offset:5120\n"
  "ds_read_b64_tr_b16 a[188:189], v122 offset:5120\n"
  "v_mfma_f32_16x16x32_bf16 v[8:11], v[56:59], a[32:35], v[8:11]\n"
  "s_waitcnt vmcnt(23)\n"
  "ds_read_b64_tr_b16 a[170:171], v117 offset:6144\n"
  "ds_read_b64_tr_b16 a[174:175], v118 offset:6144\n"
  "v_fma_f32 v42, v42, v86, v98\n"
  "v_fma_f32 v46, v46, v86, v98\n"
  "v_mfma_f32_16x16x32_bf16 v[12:15], v[56:59], a[36:39], v[100:103]\n"
  "buffer_load_dwordx4 v116, s[40:43], s57 offen offset:2048  lds\n"
  "ds_read_b128 v[72:75], v105 offset:4096\n"
  "v_fma_f32 v43, v43, v87, v99\n"
  "v_mfma_f32_16x16x32_bf16 v[12:15], v[48:51], a[12:15], v[12:15]\n"
  "s_add_u32 s57, s57, s84\n"
  "s_add_u32 m0, s59, 3072\n"
  "ds_read_b64_tr_b16 a[178:179], v119 offset:6144\n"
  "ds_read_b64_tr_b16 a[182:183], v120 offset:6144\n"
  "v_fma_f32 v47, v47, v87, v99\n"
  "v_fma_f32 v24, v24, v84, v96\n"
  "v_mfma_f32_16x16x32_bf16 v[16:19], v[48:51], a[16:19], v[100:103]\n"
  "s_waitcnt lgkmcnt(5)\n"
  "buffer_load_dwordx4 v114, s[40:43], s57 offen offset:0  lds\n"
  "buffer_load_dwordx4 v115, s[40:43], s57 offen offset:1024  lds\n"
  "ds_read_b64_tr_b16 a[186:187], v121 offset:8192\n"
  "ds_read_b64_tr_b16 a[190:191], v122 offset:8192\n"
  "v_mfma_f32_16x16x32_bf16 v[16:19], v[56:59], a[40:43], v[16:19]\n"
  "s_waitcnt vmcnt(23)\n"
  "ds_read_b64_tr_b16 a[192:193], v117 offset:9216\n"
  "ds_read_b64_tr_b16 a[196:197], v118 offset:9216\n"
  "v_fma_f32 v25, v25, v85, v97\n"
  "v_fma_f32 v28, v28, v84, v96\n"
  "v_mfma_f32_16x16x32_bf16 v[20:23], v[56:59], a[44:47], v[100:103]\n"
  "buffer_load_dwordx4 v116, s[40:43], s57 offen offset:2048  lds\n"
  "ds_read_b128 v[76:79], v105 offset:6144\n"
  "v_fma_f32 v29, v29, v85, v97\n"
  "v_mfma_f32_16x16x32_bf16 v[20:23], v[48:51], a[20:23], v[20:23]\n"
  "s_add_u32 s57, s57, 6144\n"
  "s_add_u32 m0, s59, 6144\n"
  "ds_read_b64_tr_b16 a[200:201], v119 offset:9216\n"
  "ds_read_b64_tr_b16 a[204:205], v120 offset:9216\n"
  "v_fma_f32 v32, v32, v84, v96\n"
  "v_fma_f32 v33, v33, v85, v97\n"
  "v_smfmac_f32_16x16x64_bf16 v[80:83], v[88:91], v[48:55], v124\n"
  "s_waitcnt lgkmcnt(5)\n"
  "buffer_load_dwordx4 v114, s[40:43], s57 offen offset:0  lds\n"
  "buffer_load_dwordx4 v115, s[40:43], s57 offen offset:1024  lds\n"
  "ds_read_b64_tr_b16 a[208:209], v121 offset:11264\n"
  "ds_read_b64_tr_b16 a[212:213], v122 offset:11264\n"
  "v_smfmac_f32_16x16x64_bf16 v[80:83], v[88:91], v[64:71], v124\n"
  "buffer_store_dwordx4 v[92:95], v109, s[48:51], s68 offen offset:0 nt\n"
  "s_waitcnt vmcnt(24)\n"
  "ds_read_b64_tr_b16 a[194:195], v117 offset:12288\n"
  "ds_read_b64_tr_b16 a[198:199], v118 offset:12288\n"
  "v_mfma_f32_16x16x32_bf16 v[20:23], v[52:55], a[68:71], v[20:23]\n"
  "buffer_load_dwordx4 v116, s[40:43], s57 offen offset:2048  lds\n"
  "v_fma_f32 v36, v36, v84, v96\n"
  "v_fma_f32 v37, v37, v85, v97\n"
  "v_mfma_f32_16x16x32_bf16 v[20:23], v[60:63], a[92:95], v[20:23]\n"
  "s_add_u32 s57, s57, s58\n"
  "s_add_u32 m0, s59, 9216\n"
  "ds_read_b64_tr_b16 a[202:203], v119 offset:12288\n"
  "ds_read_b64_tr_b16 a[206:207], v120 offset:12288\n"
  "v_fma_f32 v26, v26, v86, v98\n"
  "v_fma_f32 v27, v27, v87, v99\n"
  "v_mfma_f32_16x16x32_bf16 v[16:19], v[60:63], a[88:91], v[16:19]\n"
  "s_waitcnt lgkmcnt(4)\n"
  "buffer_load_dwordx4 v114, s[40:43], s57 offen offset:0  lds\n"
  "buffer_load_dwordx4 v115, s[40:43], s57 offen offset:1024  lds\n"
  "ds_read_b64_tr_b16 a[210:211], v121 offset:14336\n"
  "ds_read_b64_tr_b16 a[214:215], v122 offset:14336\n"
  "v_mfma_f32_16x16x32_bf16 v[16:19], v[52:55], a[64:67], v[16:19]\n"
  "s_waitcnt vmcnt(24)\n"
  "ds_read_b64_tr_b16 a[216:217], v117 offset:15360\n"
  "ds_read_b64_tr_b16 a[220:221], v118 offset:15360\n"
  "v_fma_f32 v30, v30, v86, v98\n"
  "v_fma_f32 v31, v31, v87, v99\n"
  "v_mfma_f32_16x16x32_bf16 v[12:15], v[52:55], a[60:63], v[12:15]\n"
  "buffer_load_dwordx4 v116, s[40:43], s57 offen offset:2048  lds\n"
  "v_fma_f32 v34, v34, v86, v98\n"
  "v_fma_f32 v35, v35, v87, v99\n"
  "v_mfma_f32_16x16x32_bf16 v[12:15], v[60:63], a[84:87], v[12:15]\n"
  "s_add_u32 s57, s57, 6144\n"
  "s_add_u32 m0, s59, 12288\n"
  "ds_read_b64_tr_b16 a[224:225], v119 offset:15360\n"
  "ds_read_b64_tr_b16 a[228:229], v120 offset:15360\n"
  "v_fma_f32 v38, v38, v86, v98\n"
  "v_fma_f32 v39, v39, v87, v99\n"
  "v_mfma_f32_16x16x32_bf16 v[8:11], v[60:63], a[80:83], v[8:11]\n"
  "s_waitcnt lgkmcnt(4)\n"
  "buffer_load_dwordx4 v114, s[40:43], s57 offen offset:0  lds\n"
  "buffer_load_dwordx4 v115, s[40:43], s57 offen offset:1024  lds\n"
  "ds_read_b64_tr_b16 a[232:233], v121 offset:17408\n"
  "ds_read_b64_tr_b16 a[236:237], v122 offset:17408\n"
  "v_mfma_f32_16x16x32_bf16 v[8:11], v[52:55], a[56:59], v[8:11]\n"
  "s_waitcnt vmcnt(24)\n"
  "ds_read_b64_tr_b16 a[218:219], v117 offset:18432\n"
  "ds_read_b64_tr_b16 a[222:223], v118 offset:18432\n"
  "ds_read_b64_tr_b16 v[96:97], v108 offset:2304\n"
  "ds_read_b64_tr_b16 v[98:99], v108 offset:2336\n"
  "v_mfma_f32_16x16x32_bf16 v[4:7], v[52:55], a[52:55], v[4:7]\n"
  "buffer_load_dwordx4 v116, s[40:43], s57 offen offset:2048  lds\n"
  "v_cvt_pk_bf16_f32 v40, v40, v44\n"
  "v_cvt_pk_bf16_f32 v41, v41, v45\n"
  "v_mfma_f32_16x16x32_bf16 v[4:7], v[60:63], a[76:79], v[4:7]\n"
  "s_add_u32 s57, s57, s84\n"
  "s_add_u32 m0, s59, 15360\n"
  "ds_read_b64_tr_b16 a[226:227], v119 offset:18432\n"
  "ds_read_b64_tr_b16 a[230:231], v120 offset:18432\n"
  "v_cvt_pk_bf16_f32 v42, v42, v46\n"
  "v_cvt_pk_bf16_f32 v43, v43, v47\n"
  "v_mfma_f32_16x16x32_bf16 v[0:3], v[60:63], a[72:75], v[0:3]\n"
  "s_waitcnt lgkmcnt(6)\n"
  "buffer_load_dwordx4 v114, s[40:43], s57 offen offset:0  lds\n"
  "buffer_load_dwordx4 v115, s[40:43], s57 offen offset:1024  lds\n"
  "ds_read_b64_tr_b16 a[234:235], v121 offset:20480\n"
  "ds_read_b64_tr_b16 a[238:239], v122 offset:20480\n"
  "v_mfma_f32_16x16x32_bf16 v[0:3], v[52:55], a[48:51], v[0:3]\n"
  "s_waitcnt vmcnt(24)\n"
  "ds_read_b64_tr_b16 a[240:241], v117 offset:21504\n"
  "ds_read_b64_tr_b16 a[244:245], v118 offset:21504\n"
  "v_cvt_pk_bf16_f32 v24, v24, v28\n"
  "v_cvt_pk_bf16_f32 v28, v25, v29\n"
  "v_mfma_f32_16x16x32_bf16 v[0:3], v[64:67], a[96:99], v[0:3]\n"
  "buffer_load_dwordx4 v116, s[40:43], s57 offen offset:2048  lds\n"
  "ds_read_b128 v[48:51], v104 offset:8192\n"
  "v_cvt_pk_bf16_f32 v25, v32, v36\n"
  "v_mfma_f32_16x16x32_bf16 v[0:3], v[72:75], a[120:123], v[0:3]\n"
  "s_add_u32 s57, s57, 6144\n"
  "s_add_u32 m0, s59, 18432\n"
  "ds_read_b64_tr_b16 a[248:249], v119 offset:21504\n"
  "ds_read_b64_tr_b16 a[252:253], v120 offset:21504\n"
  "v_cvt_pk_bf16_f32 v29, v33, v37\n"
  "v_cvt_pk_bf16_f32 v26, v26, v30\n"
  "v_mfma_f32_16x16x32_bf16 v[4:7], v[72:75], a[124:127], v[4:7]\n"
  "s_waitcnt lgkmcnt(5)\n"
  "buffer_load_dwordx4 v114, s[40:43], s57 offen offset:0  lds\n"
  "buffer_load_dwordx4 v115, s[40:43], s57 offen offset:1024  lds\n"
  "ds_read_b128 v[52:55], v104 offset:10240\n"
  "ds_read_b64_tr_b16 v[128:129], v121 offset:23552\n"
  "ds_read_b64_tr_b16 v[132:133], v122 offset:23552\n"
  "v_mfma_f32_16x16x32_bf16 v[4:7], v[64:67], a[100:103], v[4:7]\n"
  "s_waitcnt vmcnt(24)\n"
  "ds_read_b64_tr_b16 a[242:243], v117 offset:24576\n"
  "ds_read_b64_tr_b16 a[246:247], v118 offset:24576\n"
  "v_cvt_pk_bf16_f32 v30, v27, v31\n"
  "v_cvt_pk_bf16_f32 v27, v34, v38\n"
  "v_mfma_f32_16x16x32_bf16 v[8:11], v[64:67], a[104:107], v[8:11]\n"
  "buffer_load_dwordx4 v116, s[40:43], s57 offen offset:2048  lds\n"
  "v_cvt_pk_bf16_f32 v31, v35, v39\n"
  "v_mfma_f32_16x16x32_bf16 v[8:11], v[72:75], a[128:131], v[8:11]\n"
  "s_add_u32 s57, s57, s58\n"
  "s_add_u32 m0, s59, 21504\n"
  "ds_read_b64_tr_b16 a[250:251], v119 offset:24576\n"
  "ds_read_b64_tr_b16 a[254:255], v120 offset:24576\n"
  "ds_read_b64_tr_b16 v[92:93], v108 offset:4608\n"
  "ds_read_b64_tr_b16 v[94:95], v108 offset:4640\n"
  "v_mfma_f32_16x16x32_bf16 v[12:15], v[72:75], a[132:135], v[12:15]\n"
  "s_waitcnt lgkmcnt(6)\n"
  "buffer_load_dwordx4 v114, s[40:43], s57 offen offset:0  lds\n"
  "buffer_load_dwordx4 v115, s[40:43], s57 offen offset:1024  lds\n"
  "ds_read_b128 v[56:59], v105 offset:8192\n"
  "ds_read_b64_tr_b16 v[130:131], v121 offset:26624\n"
  "ds_read_b64_tr_b16 v[134:135], v122 offset:26624\n"
  "v_mfma_f32_16x16x32_bf16 v[12:15], v[64:67], a[108:111], v[12:15]\n"
  "s_waitcnt vmcnt(24)\n"
  "ds_read_b64_tr_b16 v[136:137], v117 offset:27648\n"
  "ds_read_b64_tr_b16 v[140:141], v118 offset:27648\n"
  "ds_write2_b32 v107, v40, v41 offset0:0 offset1:72\n"
  "v_mfma_f32_16x16x32_bf16 v[16:19], v[64:67], a[112:115], v[16:19]\n"
  "buffer_load_dwordx4 v116, s[40:43], s57 offen offset:2048  lds\n"
  "ds_write2_b32 v107, v42, v43 offset0:144 offset1:216\n"
  "v_mfma_f32_16x16x32_bf16 v[16:19], v[72:75], a[136:139], v[16:19]\n"
  "s_add_u32 s57, s57, 6144\n"
  "s_add_u32 m0, s59, 24576\n"
  "ds_read_b64_tr_b16 v[144:145], v119 offset:27648\n"
  "ds_read_b64_tr_b16 v[148:149], v120 offset:27648\n"
  "ds_write_b64 v106, v[24:25] offset:0\n"
  "v_mfma_f32_16x16x32_bf16 v[20:23], v[72:75], a[140:143], v[20:23]\n"
  "s_waitcnt lgkmcnt(7)\n"
  "buffer_load_dwordx4 v114, s[40:43], s57 offen offset:0  lds\n"
  "buffer_load_dwordx4 v115, s[40:43], s57 offen offset:1024  lds\n"
  "ds_read_b128 v[60:63], v105 offset:10240\n"
  "ds_read_b64_tr_b16 v[152:153], v121 offset:29696\n"
  "ds_read_b64_tr_b16 v[156:157], v122 offset:29696\n"
  "v_mfma_f32_16x16x32_bf16 v[20:23], v[64:67], a[116:119], v[20:23]\n"
  "s_waitcnt vmcnt(24)\n"
  "ds_read_b64_tr_b16 v[138:139], v117 offset:0\n"
  "ds_read_b64_tr_b16 v[142:143], v118 offset:0\n"
  "ds_write_b64 v106, v[28:29] offset:288\n"
  "v_mfma_f32_16x16x32_bf16 v[20:23], v[68:71], a[164:167], v[20:23]\n"
  "buffer_load_dwordx4 v116, s[40:43], s57 offen offset:2048  lds\n"
  "ds_write_b64 v106, v[26:27] offset:576\n"
  "v_mfma_f32_16x16x32_bf16 v[20:23], v[76:79], a[188:191], v[20:23]\n"
  "s_add_u32 s57, s57, s84\n"
  "ds_read_b64_tr_b16 v[146:147], v119 offset:0\n"
  "ds_read_b64_tr_b16 v[150:151], v120 offset:0\n"
  "ds_write_b64 v106, v[30:31] offset:864\n"
  "v_mfma_f32_16x16x32_bf16 v[16:19], v[76:79], a[184:187], v[16:19]\n"
  "buffer_store_dwordx4 v[96:99], v109, s[48:51], s68 offen offset:128 nt\n"
  "ds_read_b64_tr_b16 v[154:155], v121 offset:2048\n"
  "ds_read_b64_tr_b16 v[158:159], v122 offset:2048\n"
  "v_xor_b32 v108, 0x2000, v108\n"
  "v_mfma_f32_16x16x32_bf16 v[16:19], v[68:71], a[160:163], v[16:19]\n"
  "s_waitcnt vmcnt(23)\n"
  "ds_read_b64_tr_b16 v[160:161], v117 offset:3072\n"
  "ds_read_b64_tr_b16 v[164:165], v118 offset:3072\n"
  "v_mfma_f32_16x16x32_bf16 v[12:15], v[68:71], a[156:159], v[12:15]\n"
  "v_mfma_f32_16x16x32_bf16 v[12:15], v[76:79], a[180:183], v[12:15]\n"
  "ds_read_b64_tr_b16 v[168:169], v119 offset:3072\n"
  "ds_read_b64_tr_b16 v[172:173], v120 offset:3072\n"
  "v_mfma_f32_16x16x32_bf16 v[8:11], v[76:79], a[176:179], v[8:11]\n"
  "ds_read_b64_tr_b16 v[176:177], v121 offset:5120\n"
  "ds_read_b64_tr_b16 v[180:181], v122 offset:5120\n"
  "v_mfma_f32_16x16x32_bf16 v[8:11], v[68:71], a[152:155], v[8:11]\n"
  "s_waitcnt vmcnt(19)\n"
  "ds_read_b64_tr_b16 v[162:163], v117 offset:6144\n"
  "ds_read_b64_tr_b16 v[166:167], v118 offset:6144\n"
  "v_mfma_f32_16x16x32_bf16 v[4:7], v[68:71], a[148:151], v[4:7]\n"
  "v_mfma_f32_16x16x32_bf16 v[4:7], v[76:79], a[172:175], v[4:7]\n"
  "ds_read_b64_tr_b16 v[170:171], v119 offset:6144\n"
  "ds_read_b64_tr_b16 v[174:175], v120 offset:6144\n"
  "v_mfma_f32_16x16x32_bf16 v[0:3], v[76:79], a[168:171], v[0:3]\n"
  "ds_read_b64_tr_b16 v[178:179], v121 offset:8192\n"
  "ds_read_b64_tr_b16 v[182:183], v122 offset:8192\n"
  "v_mfma_f32_16x16x32_bf16 v[0:3], v[68:71], a[144:147], v[0:3]\n"
  "s_waitcnt lgkmcnt(15)\n"
  "s_waitcnt vmcnt(16)\n"
  "ds_read_b64_tr_b16 v[184:185], v117 offset:9216\n"
  "ds_read_b64_tr_b16 v[188:189], v118 offset:9216\n"
  "v_mfma_f32_16x16x32_bf16 v[0:3], v[48:51], a[192:195], v[0:3]\n"
  "s_add_u32 s72, s72, 1\n"
  "ds_read_b128 v[64:67], v104 offset:12288\n"
  "v_mfma_f32_16x16x32_bf16 v[0:3], v[56:59], a[216:219], v[0:3]\n"
  "s_lshr_b32 s74, s72, 1\n"
  "ds_read_b64_tr_b16 v[192:193], v119 offset:9216\n"
  "ds_read_b64_tr_b16 v[196:197], v120 offset:9216\n"
  "v_readlane_b32 s73, v126, s74\n"
  "v_mfma_f32_16x16x32_bf16 v[4:7], v[56:59], a[220:223], v[4:7]\n"
  "s_lshl_b32 s74, s72, 4\n"
  "buffer_store_dwordx4 v[92:95], v109, s[48:51], s68 offen offset:256 nt\n"
  "ds_read_b128 v[68:71], v104 offset:14336\n"
  "ds_read_b64_tr_b16 v[200:201], v121 offset:11264\n"
  "ds_read_b64_tr_b16 v[204:205], v122 offset:11264\n"
  "v_mfma_f32_16x16x32_bf16 v[4:7], v[48:51], a[196:199], v[4:7]\n"
  "s_waitcnt vmcnt(14)\n"
  "ds_read_b64_tr_b16 v[186:187], v117 offset:12288\n"
  "ds_read_b64_tr_b16 v[190:191], v118 offset:12288\n"
  "v_xor_b32 v104, s88, v104\n"
  "v_mfma_f32_16x16x32_bf16 v[8:11], v[48:51], a[200:203], v[8:11]\n"
  "s_lshr_b32 s73, s73, s74\n"
  "v_mfma_f32_16x16x32_bf16 v[8:11], v[56:59], a[224:227], v[8:11]\n"
  "s_and_b32 s73, s73, 0xffff\n"
  "ds_read_b64_tr_b16 v[194:195], v119 offset:12288\n"
  "ds_read_b64_tr_b16 v[198:199], v120 offset:12288\n"
  "v_mfma_f32_16x16x32_bf16 v[12:15], v[56:59], a[228:231], v[12:15]\n"
  "s_lshl_b32 s73, s73, 13\n"
  "ds_read_b128 v[72:75], v105 offset:12288\n"
  "ds_read_b64_tr_b16 v[202:203], v121 offset:14336\n"
  "ds_read_b64_tr_b16 v[206:207], v122 offset:14336\n"
  "v_mfma_f32_16x16x32_bf16 v[12:15], v[48:51], a[204:207], v[12:15]\n"
  "s_waitcnt vmcnt(11)\n"
  "ds_read_b64_tr_b16 v[208:209], v117 offset:15360\n"
  "ds_read_b64_tr_b16 v[212:213], v118 offset:15360\n"
  "v_mfma_f32_16x16x32_bf16 v[16:19], v[48:51], a[208:211], v[16:19]\n"
  "s_add_u32 s68, s68, s69\n"
  "v_mfma_f32_16x16x32_bf16 v[16:19], v[56:59], a[232:235], v[16:19]\n"
  "s_mov_b32 s69, s70\n"
  "ds_read_b64_tr_b16 v[216:217], v119 offset:15360\n"
  "ds_read_b64_tr_b16 v[220:221], v120 offset:15360\n"
  "v_mfma_f32_16x16x32_bf16 v[20:23], v[56:59], a[236:239], v[20:23]\n"
  "s_movk_i32 s70, 6144\n"
  "ds_read_b128 v[76:79], v105 offset:14336\n"
  "ds_read_b64_tr_b16 v[224:225], v121 offset:17408\n"
  "ds_read_b64_tr_b16 v[228:229], v122 offset:17408\n"
  "v_xor_b32 v105, s88, v105\n"
  "v_mfma_f32_16x16x32_bf16 v[20:23], v[48:51], a[212:215], v[20:23]\n"
  "s_waitcnt vmcnt(8)\n"
  "ds_read_b64_tr_b16 v[210:211], v117 offset:18432\n"
  "ds_read_b64_tr_b16 v[214:215], v118 offset:18432\n"
  "v_smfmac_f32_16x16x64_bf16 v[80:83], v[88:91], v[48:55], v124\n"
  "s_waitcnt lgkmcnt(15)\n"
  "v_smfmac_f32_16x16x64_bf16 v[80:83], v[88:91], v[64:71], v124\n"
  "ds_read_b64_tr_b16 v[218:219], v119 offset:18432\n"
  "ds_read_b64_tr_b16 v[222:223], v120 offset:18432\n"
  "v_mfma_f32_16x16x32_bf16 v[20:23], v[52:55], v[132:135], v[20:23]\n"
  "s_xor_b32 s88, s88, s89\n"
  "ds_read_b64_tr_b16 v[226:227], v121 offset:20480\n"
  "ds_read_b64_tr_b16 v[230:231], v122 offset:20480\n"
  "v_mfma_f32_16x16x32_bf16 v[20:23], v[60:63], v[156:159], v[20:23]\n"
  "s_waitcnt vmcnt(5)\n"
  "ds_read_b64_tr_b16 v[232:233], v117 offset:21504\n"
  "ds_read_b64_tr_b16 v[236:237], v118 offset:21504\n"
  "v_mfma_f32_16x16x32_bf16 v[16:19], v[60:63], v[152:155], v[16:19]\n"
  "v_add_f32 v44, v80, v81\n"
  "v_mov_b32 v80, 0\n"
  "v_mov_b32 v81, 0\n"
  "v_mfma_f32_16x16x32_bf16 v[16:19], v[52:55], v[128:131], v[16:19]\n"
  "ds_read_b64_tr_b16 v[240:241], v119 offset:21504\n"
  "ds_read_b64_tr_b16 v[244:245], v120 offset:21504\n"
  "ds_write_b32 v110, v44\n"
  "v_mfma_f32_16x16x32_bf16 v[12:15], v[52:55], a[252:255], v[12:15]\n"
  "ds_read_b64_tr_b16 v[248:249], v121 offset:23552\n"
  "ds_read_b64_tr_b16 v[252:253], v122 offset:23552\n"
  "v_mfma_f32_16x16x32_bf16 v[12:15], v[60:63], v[148:151], v[12:15]\n"
  "s_waitcnt vmcnt(2)\n"
  "ds_read_b64_tr_b16 v[234:235], v117 offset:24576\n"
  "ds_read_b64_tr_b16 v[238:239], v118 offset:24576\n"
  "v_mfma_f32_16x16x32_bf16 v[8:11], v[60:63], v[144:147], v[8:11]\n"
  "v_mfma_f32_16x16x32_bf16 v[8:11], v[52:55], a[248:251], v[8:11]\n"
  "ds_read_b64_tr_b16 v[242:243], v119 offset:24576\n"
  "ds_read_b64_tr_b16 v[246:247], v120 offset:24576\n"
  "v_xor_b32 v106, 0x2000, v106\n"
  "v_xor_b32 v107, 0x2000, v107\n"
  "v_mfma_f32_16x16x32_bf16 v[4:7], v[52:55], a[244:247], v[4:7]\n"
  "ds_read_b64_tr_b16 v[250:251], v121 offset:26624\n"
  "ds_read_b64_tr_b16 v[254:255], v122 offset:26624\n"
  "v_xor_b32 v110, 0x2000, v110\n"
  "v_mfma_f32_16x16x32_bf16 v[4:7], v[60:63], v[140:143], v[4:7]\n"
  "v_mfma_f32_16x16x32_bf16 v[0:3], v[60:63], v[136:139], v[0:3]\n"
  "v_mfma_f32_16x16x32_bf16 v[0:3], v[52:55], a[240:243], v[0:3]\n"
  "s_add_u32 m0, s66, 0\n"
  "s_nop 0\n"
  "buffer_load_dwordx4 v112, s[44:47], s67 offen offset:0 nt lds\n"
  "s_add_u32 m0, s66, 1920\n"
  "s_nop 0\n"
  "buffer_load_dwordx4 v112, s[44:47], s67 offen offset:128 nt lds\n"
  "s_add_u32 m0, s66, 3840\n"
  "s_nop 0\n"
  "buffer_load_dwordx4 v112, s[44:47], s67 offen offset:256 nt lds\n"
  "s_add_u32 s66, s66, 6144\n"
  "s_cmp_ge_u32 s66, 102400\n"
  "s_cselect_b32 s64, 36864, 0\n"
  "s_sub_u32 s66, s66, s64\n"
  "s_sub_u32 s90, s90, 1\n"
  "s_cselect_b32 s63, s91, 6144\n"
  "s_cmov_b64 s[90:91], s[92:93]\n"
  "s_cmov_b64 s[92:93], s[94:95]\n"
  "s_add_u32 s67, s67, s63\n"
  "s_add_u32 m0, s66, 0\n"
  "s_nop 0\n"
  "buffer_load_dwordx4 v112, s[44:47], s67 offen offset:0 nt lds\n"
  "s_add_u32 m0, s66, 1920\n"
  "s_nop 0\n"
  "buffer_load_dwordx4 v112, s[44:47], s67 offen offset:128 nt lds\n"
  "s_add_u32 m0, s66, 3840\n"
  "s_nop 0\n"
  "buffer_load_dwordx4 v112, s[44:47], s67 offen offset:256 nt lds\n"
  "s_add_u32 s66, s66, 6144\n"
  "s_cmp_ge_u32 s66, 102400\n"
  "s_cselect_b32 s64, 36864, 0\n"
  "s_sub_u32 s66, s66, s64\n"
  "s_sub_u32 s90, s90, 1\n"
  "s_cselect_b32 s63, s91, 6144\n"
  "s_cmov_b64 s[90:91], s[92:93]\n"
  "s_cmov_b64 s[92:93], s[94:95]\n"
  "s_add_u32 s67, s67, s63\n"
  ".p2align 6\n"
  "s_nop 0\n"
  "B_H1_%=:\n"
  "s_waitcnt vmcnt(17)\n"
  "B_H_%=:\n"
  "s_waitcnt vmcnt(25) lgkmcnt(0)\n"
  "s_barrier\n"
  "v_mfma_f32_16x16x32_bf16 v[20:23], v[64:67], v[180:183], v[20:23]\n"
  "s_add_u32 m0, s66, 0\n"
  "ds_read_b128 v[48:51], v104 offset:0\n"
  "ds_read_b128 v[84:87], v111\n"
  "v_mfma_f32_16x16x32_bf16 v[20:23], v[72:75], v[204:207], v[20:23]\n"
  "s_xor_b32 s89, s88, s89\n"
  "ds_read_b128 v[96:99], v111 offset:64\n"
  "v_xor_b32 v111, 0x2000, v111\n"
  "v_mfma_f32_16x16x32_bf16 v[20:23], v[68:71], v[228:231], v[20:23]\n"
  "s_sub_u32 s56, s56, 1\n"
  "ds_read_b128 v[52:55], v104 offset:2048\n"
  "v_mfma_f32_16x16x32_bf16 v[44:47], v[76:79], v[252:255], v[20:23]\n"
  "buffer_load_dwordx4 v112, s[44:47], s67 offen offset:0 nt lds\n"
  "v_mfma_f32_16x16x32_bf16 v[16:19], v[76:79], v[248:251], v[16:19]\n"
  "s_add_u32 m0, s66, 1920\n"
  "v_mfma_f32_16x16x32_bf16 v[16:19], v[68:71], v[224:227], v[16:19]\n"
  "v_mfma_f32_16x16x32_bf16 v[16:19], v[72:75], v[200:203], v[16:19]\n"
  "ds_read_b128 v[56:59], v105 offset:0\n"
  "v_mfma_f32_16x16x32_bf16 v[40:43], v[64:67], v[176:179], v[16:19]\n"
  "buffer_load_dwordx4 v112, s[44:47], s67 offen offset:128 nt lds\n"
  "v_mfma_f32_16x16x32_bf16 v[12:15], v[64:67], v[172:175], v[12:15]\n"
  "s_add_u32 m0, s66, 3840\n"
  "v_mfma_f32_16x16x32_bf16 v[12:15], v[72:75], v[196:199], v[12:15]\n"
  "v_mfma_f32_16x16x32_bf16 v[12:15], v[68:71], v[220:223], v[12:15]\n"
  "s_waitcnt lgkmcnt(2)\n"
  "ds_read_b128 v[60:63], v105 offset:2048\n"
  "v_add_f32 v84, v84, v96\n"
  "v_mfma_f32_16x16x32_bf16 v[36:39], v[76:79], v[244:247], v[12:15]\n"
  "buffer_load_dwordx4 v112, s[44:47], s67 offen offset:256 nt lds\n"
  "v_mfma_f32_16x16x32_bf16 v[8:11], v[76:79], v[240:243], v[8:11]\n"
  "s_mov_b32 m0, 56320\n"
  "v_add_f32 v85, v85, v97\n"
  "v_add_f32 v86, v86, v98\n"
  "v_mfma_f32_16x16x32_bf16 v[8:11], v[68:71], v[216:219], v[8:11]\n"
  "v_add_f32 v87, v87, v99\n"
  "v_max3_f32 v82, v82, v84, v85\n"
  "v_mfma_f32_16x16x32_bf16 v[8:11], v[72:75], v[192:195], v[8:11]\n"
  "v_rcp_f32 v84, v84\n"
  "v_mfma_f32_16x16x32_bf16 v[32:35], v[64:67], v[168:171], v[8:11]\n"
  "buffer_load_dword v125, s[80:83], s73 offen  lds\n"
  "v_mfma_f32_16x16x32_bf16 v[4:7], v[64:67], v[164:167], v[4:7]\n"
  "v_rcp_f32 v85, v85\n"
  "v_mfma_f32_16x16x32_bf16 v[4:7], v[72:75], v[188:191], v[4:7]\n"
  "v_max3_f32 v82, v82, v86, v87\n"
  "v_max3_f32 v83, v83, v84, v85\n"
  "v_mfma_f32_16x16x32_bf16 v[4:7], v[68:71], v[212:215], v[4:7]\n"
  "v_rcp_f32 v86, v86\n"
  "v_mfma_f32_16x16x32_bf16 v[28:31], v[76:79], v[236:239], v[4:7]\n"
  "v_rcp_f32 v87, v87\n"
  "v_mfma_f32_16x16x32_bf16 v[0:3], v[76:79], v[232:235], v[0:3]\n"
  "v_max3_f32 v83, v83, v86, v87\n"
  "v_mul_f32 v96, s99, v84\n"
  "v_mfma_f32_16x16x32_bf16 v[0:3], v[68:71], v[208:211], v[0:3]\n"
  "v_mul_f32 v97, s99, v85\n"
  "v_fma_f32 v40, v40, v84, v96\n"
  "v_mfma_f32_16x16x32_bf16 v[0:3], v[72:75], v[184:187], v[0:3]\n"
  "v_fma_f32 v44, v44, v84, v96\n"
  "v_cvt_pk_bf16_f32 v40, v40, v44\n"
  "v_mfma_f32_16x16x32_bf16 v[24:27], v[64:67], v[160:163], v[0:3]\n"
  "s_waitcnt lgkmcnt(0)\n"
  "v_fma_f32 v41, v41, v85, v97\n"
  "v_fma_f32 v45, v45, v85, v97\n"
  "v_mfma_f32_16x16x32_bf16 v[0:3], v[48:51], a[0:3], v[100:103]\n"
  "ds_read_b128 v[64:67], v104 offset:4096\n"
  "v_cvt_pk_bf16_f32 v41, v41, v45\n"
  "v_mfma_f32_16x16x32_bf16 v[0:3], v[56:59], a[24:27], v[0:3]\n"
  "v_mul_f32 v98, s99, v86\n"
  "v_mul_f32 v99, s99, v87\n"
  "v_mfma_f32_16x16x32_bf16 v[0:3], v[52:55], a[48:51], v[0:3]\n"
  "ds_read_b128 v[68:71], v104 offset:6144\n"
  "v_fma_f32 v42, v42, v86, v98\n"
  "v_mfma_f32_16x16x32_bf16 v[0:3], v[60:63], a[72:75], v[0:3]\n"
  "ds_read_b64_tr_b16 v[92:93], v108 offset:0\n"
  "ds_read_b64_tr_b16 v[94:95], v108 offset:32\n"
  "v_mfma_f32_16x16x32_bf16 v[4:7], v[60:63], a[76:79], v[100:103]\n"
  "v_fma_f32 v46, v46, v86, v98\n"
  "v_cvt_pk_bf16_f32 v42, v42, v46\n"
  "v_mfma_f32_16x16x32_bf16 v[4:7], v[52:55], a[52:55], v[4:7]\n"
  "v_fma_f32 v43, v43, v87, v99\n"
  "v_fma_f32 v47, v47, v87, v99\n"
  "v_mfma_f32_16x16x32_bf16 v[4:7], v[56:59], a[28:31], v[4:7]\n"
  "ds_read_b128 v[72:75], v105 offset:4096\n"
  "v_cvt_pk_bf16_f32 v43, v43, v47\n"
  "v_mfma_f32_16x16x32_bf16 v[4:7], v[48:51], a[4:7], v[4:7]\n"
  "v_fma_f32 v24, v24, v84, v96\n"
  "v_fma_f32 v25, v25, v85, v97\n"
  "v_mfma_f32_16x16x32_bf16 v[8:11], v[48:51], a[8:11], v[100:103]\n"
  "v_fma_f32 v28, v28, v84, v96\n"
  "v_fma_f32 v29, v29, v85, v97\n"
  "v_mfma_f32_16x16x32_bf16 v[8:11], v[56:59], a[32:35], v[8:11]\n"
  "v_fma_f32 v32, v32, v84, v96\n"
  "v_fma_f32 v33, v33, v85, v97\n"
  "v_mfma_f32_16x16x32_bf16 v[8:11], v[52:55], a[56:59], v[8:11]\n"
  "ds_read_b128 v[76:79], v105 offset:6144\n"
  "v_fma_f32 v36, v36, v84, v96\n"
  "v_mfma_f32_16x16x32_bf16 v[8:11], v[60:63], a[80:83], v[8:11]\n"
  "v_fma_f32 v37, v37, v85, v97\n"
  "v_fma_f32 v26, v26, v86, v98\n"
  "v_smfmac_f32_16x16x64_bf16 v[80:83], v[88:91], v[48:55], v124\n"
  "s_waitcnt lgkmcnt(4)\n"
  "v_fma_f32 v27, v27, v87, v99\n"
  "v_smfmac_f32_16x16x64_bf16 v[80:83], v[88:91], v[64:71], v124\n"
  "s_waitcnt lgkmcnt(2)\n"
  "buffer_store_dwordx4 v[92:95], v109, s[48:51], s68 offen offset:0 nt\n"
  "ds_write2_b32 v107, v40, v41 offset0:0 offset1:72\n"
  "v_mfma_f32_16x16x32_bf16 v[12:15], v[60:63], a[84:87], v[100:103]\n"
  "v_fma_f32 v30, v30, v86, v98\n"
  "v_fma_f32 v31, v31, v87, v99\n"
  "v_mfma_f32_16x16x32_bf16 v[12:15], v[52:55], a[60:63], v[12:15]\n"
  "v_fma_f32 v34, v34, v86, v98\n"
  "v_fma_f32 v35, v35, v87, v99\n"
  "v_mfma_f32_16x16x32_bf16 v[12:15], v[56:59], a[36:39], v[12:15]\n"
  "v_fma_f32 v38, v38, v86, v98\n"
  "v_fma_f32 v39, v39, v87, v99\n"
  "v_mfma_f32_16x16x32_bf16 v[12:15], v[48:51], a[12:15], v[12:15]\n"
  "ds_write2_b32 v107, v42, v43 offset0:144 offset1:216\n"
  "v_mfma_f32_16x16x32_bf16 v[16:19], v[48:51], a[16:19], v[100:103]\n"
  "v_cvt_pk_bf16_f32 v24, v24, v28\n"
  "v_cvt_pk_bf16_f32 v28, v25, v29\n"
  "v_mfma_f32_16x16x32_bf16 v[16:19], v[56:59], a[40:43], v[16:19]\n"
  "ds_read_b64_tr_b16 v[96:97], v108 offset:2304\n"
  "ds_read_b64_tr_b16 v[98:99], v108 offset:2336\n"
  "v_mfma_f32_16x16x32_bf16 v[16:19], v[52:55], a[64:67], v[16:19]\n"
  "v_cvt_pk_bf16_f32 v25, v32, v36\n"
  "v_cvt_pk_bf16_f32 v29, v33, v37\n"
  "v_mfma_f32_16x16x32_bf16 v[16:19], v[60:63], a[88:91], v[16:19]\n"
  "v_cvt_pk_bf16_f32 v26, v26, v30\n"
  "v_cvt_pk_bf16_f32 v30, v27, v31\n"
  "v_mfma_f32_16x16x32_bf16 v[20:23], v[60:63], a[92:95], v[100:103]\n"
  "v_cvt_pk_bf16_f32 v27, v34, v38\n"
  "v_cvt_pk_bf16_f32 v31, v35, v39\n"
  "v_mfma_f32_16x16x32_bf16 v[20:23], v[52:55], a[68:71], v[20:23]\n"
  "v_mfma_f32_16x16x32_bf16 v[20:23], v[56:59], a[44:47], v[20:23]\n"
  "v_mfma_f32_16x16x32_bf16 v[20:23], v[48:51], a[20:23], v[20:23]\n"
  "s_waitcnt lgkmcnt(4)\n"
  "v_mfma_f32_16x16x32_bf16 v[20:23], v[64:67], a[116:119], v[20:23]\n"
  "s_add_u32 s66, s66, 6144\n"
  "ds_read_b128 v[48:51], v104 offset:8192\n"
  "v_mfma_f32_16x16x32_bf16 v[20:23], v[72:75], a[140:143], v[20:23]\n"
  "s_cmp_ge_u32 s66, 102400\n"
  "s_cselect_b32 s64, 36864, 0\n"
  "v_mfma_f32_16x16x32_bf16 v[20:23], v[68:71], a[164:167], v[20:23]\n"
  "s_sub_u32 s66, s66, s64\n"
  "ds_read_b128 v[52:55], v104 offset:10240\n"
  "v_mfma_f32_16x16x32_bf16 v[20:23], v[76:79], a[188:191], v[20:23]\n"
  "s_sub_u32 s90, s90, 1\n"
  "s_cselect_b32 s63, s91, 6144\n"
  "s_cmov_b64 s[90:91], s[92:93]\n"
  "s_cmov_b64 s[92:93], s[94:95]\n"
  "s_waitcnt lgkmcnt(2)\n"
  "buffer_store_dwordx4 v[96:99], v109, s[48:51], s68 offen offset:128 nt\n"
  "ds_write_b64 v106, v[24:25] offset:0\n"
  "v_mfma_f32_16x16x32_bf16 v[16:19], v[76:79], a[184:187], v[16:19]\n"
  "s_add_u32 s67, s67, s63\n"
  "v_mfma_f32_16x16x32_bf16 v[16:19], v[68:71], a[160:163], v[16:19]\n"
  "ds_read_b64_tr_b16 v[92:93], v108 offset:4608\n"
  "ds_read_b64_tr_b16 v[94:95], v108 offset:4640\n"
  "v_mfma_f32_16x16x32_bf16 v[16:19], v[72:75], a[136:139], v[16:19]\n"
  "ds_read_b128 v[56:59], v105 offset:8192\n"
  "v_xor_b32 v108, 0x2000, v108\n"
  "v_mfma_f32_16x16x32_bf16 v[16:19], v[64:67], a[112:115], v[16:19]\n"
  "v_mfma_f32_16x16x32_bf16 v[12:15], v[64:67], a[108:111], v[12:15]\n"
  "v_mfma_f32_16x16x32_bf16 v[12:15], v[72:75], a[132:135], v[12:15]\n"
  "v_mfma_f32_16x16x32_bf16 v[12:15], v[68:71], a[156:159], v[12:15]\n"
  "ds_read_b128 v[60:63], v105 offset:10240\n"
  "v_mfma_f32_16x16x32_bf16 v[12:15], v[76:79], a[180:183], v[12:15]\n"
  "v_mfma_f32_16x16x32_bf16 v[8:11], v[76:79], a[176:179], v[8:11]\n"
  "v_mfma_f32_16x16x32_bf16 v[8:11], v[68:71], a[152:155], v[8:11]\n"
  "v_mfma_f32_16x16x32_bf16 v[8:11], v[72:75], a[128:131], v[8:11]\n"
  "v_mfma_f32_16x16x32_bf16 v[8:11], v[64:67], a[104:107], v[8:11]\n"
  "s_waitcnt lgkmcnt(2)\n"
  "buffer_store_dwordx4 v[92:95], v109, s[48:51], s68 offen offset:256 nt\n"
  "ds_write_b64 v106, v[28:29] offset:288\n"
  "v_mfma_f32_16x16x32_bf16 v[4:7], v[64:67], a[100:103], v[4:7]\n"
  "s_add_u32 s68, s68, s69\n"
  "v_mfma_f32_16x16x32_bf16 v[4:7], v[72:75], a[124:127], v[4:7]\n"
  "s_mov_b32 s69, s70\n"
  "v_mfma_f32_16x16x32_bf16 v[4:7], v[68:71], a[148:151], v[4:7]\n"
  "s_movk_i32 s70, 6144\n"
  "v_mfma_f32_16x16x32_bf16 v[4:7], v[76:79], a[172:175], v[4:7]\n"
  "v_mfma_f32_16x16x32_bf16 v[0:3], v[76:79], a[168:171], v[0:3]\n"
  "v_mfma_f32_16x16x32_bf16 v[0:3], v[68:71], a[144:147], v[0:3]\n"
  "v_mfma_f32_16x16x32_bf16 v[0:3], v[72:75], a[120:123], v[0:3]\n"
  "v_mfma_f32_16x16x32_bf16 v[0:3], v[64:67], a[96:99], v[0:3]\n"
  "s_waitcnt lgkmcnt(1)\n"
  "v_mfma_f32_16x16x32_bf16 v[0:3], v[48:51], a[192:195], v[0:3]\n"
  "s_add_u32 s72, s72, 1\n"
  "ds_read_b128 v[64:67], v104 offset:12288\n"
  "v_mfma_f32_16x16x32_bf16 v[0:3], v[56:59], a[216:219], v[0:3]\n"
  "s_lshr_b32 s74, s72, 1\n"
  "v_readlane_b32 s73, v126, s74\n"
  "v_mfma_f32_16x16x32_bf16 v[0:3], v[52:55], a[240:243], v[0:3]\n"
  "s_lshl_b32 s74, s72, 4\n"
  "ds_read_b128 v[68:71], v104 offset:14336\n"
  "v_xor_b32 v104, s88, v104\n"
  "v_mfma_f32_16x16x32_bf16 v[0:3], v[60:63], v[136:139], v[0:3]\n"
  "ds_write_b64 v106, v[26:27] offset:576\n"
  "v_mfma_f32_16x16x32_bf16 v[4:7], v[60:63], v[140:143], v[4:7]\n"
  "s_lshr_b32 s73, s73, s74\n"
  "v_mfma_f32_16x16x32_bf16 v[4:7], v[52:55], a[244:247], v[4:7]\n"
  "s_and_b32 s73, s73, 0xffff\n"
  "v_mfma_f32_16x16x32_bf16 v[4:7], v[56:59], a[220:223], v[4:7]\n"
  "s_lshl_b32 s73, s73, 13\n"
  "ds_read_b128 v[72:75], v105 offset:12288\n"
  "v_mfma_f32_16x16x32_bf16 v[4:7], v[48:51], a[196:199], v[4:7]\n"
  "ds_write_b64 v106, v[30:31] offset:864\n"
  "v_mfma_f32_16x16x32_bf16 v[8:11], v[48:51], a[200:203], v[8:11]\n"
  "v_mfma_f32_16x16x32_bf16 v[8:11], v[56:59], a[224:227], v[8:11]\n"
  "v_mfma_f32_16x16x32_bf16 v[8:11], v[52:55], a[248:251], v[8:11]\n"
  "ds_read_b128 v[76:79], v105 offset:14336\n"
  "v_xor_b32 v105, s88, v105\n"
  "v_mfma_f32_16x16x32_bf16 v[8:11], v[60:63], v[144:147], v[8:11]\n"
  "s_xor_b32 s88, s88, s89\n"
  "v_smfmac_f32_16x16x64_bf16 v[80:83], v[88:91], v[48:55], v124\n"
  "s_waitcnt lgkmcnt(4)\n"
  "v_smfmac_f32_16x16x64_bf16 v[80:83], v[88:91], v[64:71], v124\n"
  "v_mfma_f32_16x16x32_bf16 v[12:15], v[60:63], v[148:151], v[12:15]\n"
  "v_mfma_f32_16x16x32_bf16 v[12:15], v[52:55], a[252:255], v[12:15]\n"
  "v_mfma_f32_16x16x32_bf16 v[12:15], v[56:59], a[228:231], v[12:15]\n"
  "v_add_f32 v44, v80, v81\n"
  "v_mov_b32 v80, 0\n"
  "v_mfma_f32_16x16x32_bf16 v[12:15], v[48:51], a[204:207], v[12:15]\n"
  "ds_write_b32 v110, v44\n"
  "v_mfma_f32_16x16x32_bf16 v[16:19], v[48:51], a[208:211], v[16:19]\n"
  "v_mov_b32 v81, 0\n"
  "v_mfma_f32_16x16x32_bf16 v[16:19], v[56:59], a[232:235], v[16:19]\n"
  "v_mfma_f32_16x16x32_bf16 v[16:19], v[52:55], v[128:131], v[16:19]\n"
  "v_mfma_f32_16x16x32_bf16 v[16:19], v[60:63], v[152:155], v[16:19]\n"
  "v_xor_b32 v106, 0x2000, v106\n"
  "v_xor_b32 v107, 0x2000, v107\n"
  "v_mfma_f32_16x16x32_bf16 v[20:23], v[60:63], v[156:159], v[20:23]\n"
  "v_xor_b32 v110, 0x2000, v110\n"
  "v_mfma_f32_16x16x32_bf16 v[20:23], v[52:55], v[132:135], v[20:23]\n"
  "v_mfma_f32_16x16x32_bf16 v[20:23], v[56:59], a[236:239], v[20:23]\n"
  "s_sub_u32 s62, s56, 1\n"
  "s_cmp_lt_u32 s62, s61\n"
  "v_mfma_f32_16x16x32_bf16 v[20:23], v[48:51], a[212:215], v[20:23]\n"
  "s_cbranch_scc1 B_H_%=\n"
  "s_cmp_lg_u32 s56, 0\n"
  "s_cbranch_scc1 B_H1_%=\n"
  "s_mov_b32 s52, s53\n"
  "s_mov_b32 s53, s54\n"
  "s_mov_b32 s54, s55\n"
  "s_cmp_eq_u32 s52, 0\n"
  "s_cbranch_scc1 B_LAST_%=\n"
  "s_lshr_b32 s62, s52, 16\n"
  "s_lshl1_add_u32 s62, s62, s65\n"
  "s_mul_i32 s57, s62, 98304\n"
  "s_and_b32 s56, s52, 0xff\n"
  "s_cmp_lg_u32 s53, 0\n"
  "s_cselect_b32 s62, 6, 0\n"
  "s_sub_u32 s56, s56, s62\n"
  "s_waitcnt vmcnt(17) lgkmcnt(0)\n"
  "s_barrier\n"
  "v_mfma_f32_16x16x32_bf16 v[20:23], v[64:67], v[180:183], v[20:23]\n"
  "s_add_u32 m0, s66, 0\n"
  "ds_read_b128 v[48:51], v104 offset:0\n"
  "ds_read_b128 v[84:87], v111\n"
  "v_mfma_f32_16x16x32_bf16 v[20:23], v[72:75], v[204:207], v[20:23]\n"
  "s_xor_b32 s89, s88, s89\n"
  "ds_read_b128 v[96:99], v111 offset:64\n"
  "v_xor_b32 v111, 0x2000, v111\n"
  "v_mfma_f32_16x16x32_bf16 v[20:23], v[68:71], v[228:231], v[20:23]\n"
  "ds_read_b128 v[52:55], v104 offset:2048\n"
  "v_mfma_f32_16x16x32_bf16 v[44:47], v[76:79], v[252:255], v[20:23]\n"
  "buffer_load_dwordx4 v112, s[44:47], s67 offen offset:0 nt lds\n"
  "v_mfma_f32_16x16x32_bf16 v[16:19], v[76:79], v[248:251], v[16:19]\n"
  "s_add_u32 m0, s66, 1920\n"
  "v_mfma_f32_16x16x32_bf16 v[16:19], v[68:71], v[224:227], v[16:19]\n"
  "v_mfma_f32_16x16x32_bf16 v[16:19], v[72:75], v[200:203], v[16:19]\n"
  "ds_read_b128 v[56:59], v105 offset:0\n"
  "v_mfma_f32_16x16x32_bf16 v[40:43], v[64:67], v[176:179], v[16:19]\n"
  "buffer_load_dwordx4 v112, s[44:47], s67 offen offset:128 nt lds\n"
  "v_mfma_f32_16x16x32_bf16 v[12:15], v[64:67], v[172:175], v[12:15]\n"
  "s_add_u32 m0, s66, 3840\n"
  "v_mfma_f32_16x16x32_bf16 v[12:15], v[72:75], v[196:199], v[12:15]\n"
  "v_mfma_f32_16x16x32_bf16 v[12:15], v[68:71], v[220:223], v[12:15]\n"
  "s_waitcnt lgkmcnt(2)\n"
  "ds_read_b128 v[60:63], v105 offset:2048\n"
  "v_add_f32 v84, v84, v96\n"
  "v_mfma_f32_16x16x32_bf16 v[36:39], v[76:79], v[244:247], v[12:15]\n"
  "buffer_load_dwordx4 v112, s[44:47], s67 offen offset:256 nt lds\n"
  "v_mfma_f32_16x16x32_bf16 v[8:11], v[76:79], v[240:243], v[8:11]\n"
  "v_add_f32 v85, v85, v97\n"
  "v_add_f32 v86, v86, v98\n"
  "v_mfma_f32_16x16x32_bf16 v[8:11], v[68:71], v[216:219], v[8:11]\n"
  "v_add_f32 v87, v87, v99\n"
  "v_max3_f32 v82, v82, v84, v85\n"
  "v_mfma_f32_16x16x32_bf16 v[8:11], v[72:75], v[192:195], v[8:11]\n"
  "v_rcp_f32 v84, v84\n"
  "v_mfma_f32_16x16x32_bf16 v[32:35], v[64:67], v[168:171], v[8:11]\n"
  "s_add_u32 s73, s67, s60\n"
  "s_mov_b32 m0, 56320\n"
  "s_nop 0\n"
  "buffer_load_dword v125, s[44:47], s73 offen lds\n"
  "v_mfma_f32_16x16x32_bf16 v[4:7], v[64:67], v[164:167], v[4:7]\n"
  "v_rcp_f32 v85, v85\n"
  "v_mfma_f32_16x16x32_bf16 v[4:7], v[72:75], v[188:191], v[4:7]\n"
  "v_max3_f32 v82, v82, v86, v87\n"
  "v_max3_f32 v83, v83, v84, v85\n"
  "v_mfma_f32_16x16x32_bf16 v[4:7], v[68:71], v[212:215], v[4:7]\n"
  "v_rcp_f32 v86, v86\n"
  "v_mfma_f32_16x16x32_bf16 v[28:31], v[76:79], v[236:239], v[4:7]\n"
  "v_rcp_f32 v87, v87\n"
  "v_mfma_f32_16x16x32_bf16 v[0:3], v[76:79], v[232:235], v[0:3]\n"
  "s_add_u32 m0, s59, 0\n"
  "v_max3_f32 v83, v83, v86, v87\n"
  "v_mul_f32 v96, s99, v84\n"
  "v_mfma_f32_16x16x32_bf16 v[0:3], v[68:71], v[208:211], v[0:3]\n"
  "buffer_load_dwordx4 v112, s[76:79], s57 offen offset:0  lds\n"
  "v_mfma_f32_16x16x32_bf16 v[0:3], v[72:75], v[184:187], v[0:3]\n"
  "v_mul_f32 v97, s99, v85\n"
  "v_mul_f32 v98, s99, v86\n"
  "v_mfma_f32_16x16x32_bf16 v[24:27], v[64:67], v[160:163], v[0:3]\n"
  "s_waitcnt lgkmcnt(0)\n"
  "v_mul_f32 v99, s99, v87\n"
  "v_fma_f32 v40, v40, v84, v96\n"
  "v_mfma_f32_16x16x32_bf16 v[0:3], v[48:51], a[0:3], v[100:103]\n"
  "ds_read_b128 v[64:67], v104 offset:4096\n"
  "v_fma_f32 v44, v44, v84, v96\n"
  "v_mfma_f32_16x16x32_bf16 v[0:3], v[56:59], a[24:27], v[0:3]\n"
  "v_fma_f32 v41, v41, v85, v97\n"
  "v_fma_f32 v45, v45, v85, v97\n"
  "v_mfma_f32_16x16x32_bf16 v[0:3], v[52:55], a[48:51], v[0:3]\n"
  "ds_read_b128 v[68:71], v104 offset:6144\n"
  "v_fma_f32 v42, v42, v86, v98\n"
  "v_mfma_f32_16x16x32_bf16 v[0:3], v[60:63], a[72:75], v[0:3]\n"
  "ds_read_b64_tr_b16 v[92:93], v108 offset:0\n"
  "ds_read_b64_tr_b16 v[94:95], v108 offset:32\n"
  "v_mfma_f32_16x16x32_bf16 v[4:7], v[60:63], a[76:79], v[100:103]\n"
  "buffer_load_dwordx4 v113, s[76:79], s57 offen offset:1024  lds\n"
  "v_mfma_f32_16x16x32_bf16 v[4:7], v[52:55], a[52:55], v[4:7]\n"
  "v_fma_f32 v46, v46, v86, v98\n"
  "v_fma_f32 v43, v43, v87, v99\n"
  "v_mfma_f32_16x16x32_bf16 v[4:7], v[56:59], a[28:31], v[4:7]\n"
  "ds_read_b128 v[72:75], v105 offset:4096\n"
  "v_fma_f32 v47, v47, v87, v99\n"
  "v_mfma_f32_16x16x32_bf16 v[4:7], v[48:51], a[4:7], v[4:7]\n"
  "v_fma_f32 v24, v24, v84, v96\n"
  "v_fma_f32 v25, v25, v85, v97\n"
  "v_mfma_f32_16x16x32_bf16 v[8:11], v[48:51], a[8:11], v[100:103]\n"
  "v_fma_f32 v28, v28, v84, v96\n"
  "v_fma_f32 v29, v29, v85, v97\n"
  "v_mfma_f32_16x16x32_bf16 v[8:11], v[56:59], a[32:35], v[8:11]\n"
  "v_fma_f32 v32, v32, v84, v96\n"
  "v_fma_f32 v33, v33, v85, v97\n"
  "v_mfma_f32_16x16x32_bf16 v[8:11], v[52:55], a[56:59], v[8:11]\n"
  "s_add_u32 m0, s59, 1920\n"
  "ds_read_b128 v[76:79], v105 offset:6144\n"
  "v_fma_f32 v36, v36, v84, v96\n"
  "v_mfma_f32_16x16x32_bf16 v[8:11], v[60:63], a[80:83], v[8:11]\n"
  "buffer_load_dwordx4 v112, s[76:79], s57 offen offset:128  lds\n"
  "v_smfmac_f32_16x16x64_bf16 v[80:83], v[88:91], v[48:55], v124\n"
  "s_waitcnt lgkmcnt(4)\n"
  "v_fma_f32 v37, v37, v85, v97\n"
  "v_smfmac_f32_16x16x64_bf16 v[80:83], v[88:91], v[64:71], v124\n"
  "s_waitcnt lgkmcnt(2)\n"
  "buffer_store_dwordx4 v[92:95], v109, s[48:51], s68 offen offset:0 nt\n"
  "v_fma_f32 v26, v26, v86, v98\n"
  "v_fma_f32 v27, v27, v87, v99\n"
  "v_mfma_f32_16x16x32_bf16 v[12:15], v[60:63], a[84:87], v[100:103]\n"
  "v_fma_f32 v30, v30, v86, v98\n"
  "v_fma_f32 v31, v31, v87, v99\n"
  "v_mfma_f32_16x16x32_bf16 v[12:15], v[52:55], a[60:63], v[12:15]\n"
  "v_fma_f32 v34, v34, v86, v98\n"
  "v_fma_f32 v35, v35, v87, v99\n"
  "v_mfma_f32_16x16x32_bf16 v[12:15], v[56:59], a[36:39], v[12:15]\n"
  "v_fma_f32 v38, v38, v86, v98\n"
  "v_fma_f32 v39, v39, v87, v99\n"
  "v_mfma_f32_16x16x32_bf16 v[12:15], v[48:51], a[12:15], v[12:15]\n"
  "v_cvt_pk_bf16_f32 v40, v40, v44\n"
  "v_cvt_pk_bf16_f32 v41, v41, v45\n"
  "v_mfma_f32_16x16x32_bf16 v[16:19], v[48:51], a[16:19], v[100:103]\n"
  "buffer_load_dwordx4 v113, s[76:79], s57 offen offset:1152  lds\n"
  "v_mfma_f32_16x16x32_bf16 v[16:19], v[56:59], a[40:43], v[16:19]\n"
  "ds_read_b64_tr_b16 v[96:97], v108 offset:2304\n"
  "ds_read_b64_tr_b16 v[98:99], v108 offset:2336\n"
  "v_mfma_f32_16x16x32_bf16 v[16:19], v[52:55], a[64:67], v[16:19]\n"
  "v_cvt_pk_bf16_f32 v42, v42, v46\n"
  "v_cvt_pk_bf16_f32 v43, v43, v47\n"
  "v_mfma_f32_16x16x32_bf16 v[16:19], v[60:63], a[88:91], v[16:19]\n"
  "v_cvt_pk_bf16_f32 v24, v24, v28\n"
  "v_cvt_pk_bf16_f32 v28, v25, v29\n"
  "v_mfma_f32_16x16x32_bf16 v[20:23], v[60:63], a[92:95], v[100:103]\n"
  "v_cvt_pk_bf16_f32 v25, v32, v36\n"
  "v_cvt_pk_bf16_f32 v29, v33, v37\n"
  "v_mfma_f32_16x16x32_bf16 v[20:23], v[52:55], a[68:71], v[20:23]\n"
  "v_cvt_pk_bf16_f32 v26, v26, v30\n"
  "v_cvt_pk_bf16_f32 v30, v27, v31\n"
  "v_mfma_f32_16x16x32_bf16 v[20:23], v[56:59], a[44:47], v[20:23]\n"
  "s_add_u32 m0, s59, 3840\n"
  "v_cvt_pk_bf16_f32 v27, v34, v38\n"
  "v_cvt_pk_bf16_f32 v31, v35, v39\n"
  "v_mfma_f32_16x16x32_bf16 v[20:23], v[48:51], a[20:23], v[20:23]\n"
  "s_waitcnt lgkmcnt(2)\n"
  "buffer_load_dwordx4 v112, s[76:79], s57 offen offset:256  lds\n"
  "v_mfma_f32_16x16x32_bf16 v[20:23], v[64:67], a[116:119], v[20:23]\n"
  "s_add_u32 s66, s66, 6144\n"
  "ds_read_b128 v[48:51], v104 offset:8192\n"
  "v_mfma_f32_16x16x32_bf16 v[20:23], v[72:75], a[140:143], v[20:23]\n"
  "s_cmp_ge_u32 s66, 102400\n"
  "s_cselect_b32 s64, 36864, 0\n"
  "v_mfma_f32_16x16x32_bf16 v[20:23], v[68:71], a[164:167], v[20:23]\n"
  "s_sub_u32 s66, s66, s64\n"
  "ds_read_b128 v[52:55], v104 offset:10240\n"
  "v_mfma_f32_16x16x32_bf16 v[20:23], v[76:79], a[188:191], v[20:23]\n"
  "s_sub_u32 s90, s90, 1\n"
  "s_cselect_b32 s63, s91, 6144\n"
  "s_cmov_b64 s[90:91], s[92:93]\n"
  "s_cmov_b64 s[92:93], s[94:95]\n"
  "s_waitcnt lgkmcnt(2)\n"
  "buffer_store_dwordx4 v[96:99], v109, s[48:51], s68 offen offset:128 nt\n"
  "ds_write2_b32 v107, v40, v41 offset0:0 offset1:72\n"
  "v_mfma_f32_16x16x32_bf16 v[16:19], v[76:79], a[184:187], v[16:19]\n"
  "s_add_u32 s67, s67, s63\n"
  "v_mfma_f32_16x16x32_bf16 v[16:19], v[68:71], a[160:163], v[16:19]\n"
  "ds_read_b64_tr_b16 v[92:93], v108 offset:4608\n"
  "ds_read_b64_tr_b16 v[94:95], v108 offset:4640\n"
  "v_mfma_f32_16x16x32_bf16 v[16:19], v[72:75], a[136:139], v[16:19]\n"
  "buffer_load_dwordx4 v113, s[76:79], s57 offen offset:1280  lds\n"
  "ds_read_b128 v[56:59], v105 offset:8192\n"
  "v_mfma_f32_16x16x32_bf16 v[16:19], v[64:67], a[112:115], v[16:19]\n"
  "s_add_u32 s57, s57, 6144\n"
  "ds_write2_b32 v107, v42, v43 offset0:144 offset1:216\n"
  "v_mfma_f32_16x16x32_bf16 v[12:15], v[64:67], a[108:111], v[12:15]\n"
  "v_xor_b32 v108, 0x2000, v108\n"
  "v_mfma_f32_16x16x32_bf16 v[12:15], v[72:75], a[132:135], v[12:15]\n"
  "v_mfma_f32_16x16x32_bf16 v[12:15], v[68:71], a[156:159], v[12:15]\n"
  "ds_read_b128 v[60:63], v105 offset:10240\n"
  "v_mfma_f32_16x16x32_bf16 v[12:15], v[76:79], a[180:183], v[12:15]\n"
  "ds_write_b64 v106, v[24:25] offset:0\n"
  "v_mfma_f32_16x16x32_bf16 v[8:11], v[76:79], a[176:179], v[8:11]\n"
  "s_add_u32 m0, s59, 5888\n"
  "v_mfma_f32_16x16x32_bf16 v[8:11], v[68:71], a[152:155], v[8:11]\n"
  "buffer_load_dwordx4 v112, s[76:79], s57 offen offset:256  lds\n"
  "v_mfma_f32_16x16x32_bf16 v[8:11], v[72:75], a[128:131], v[8:11]\n"
  "v_mfma_f32_16x16x32_bf16 v[8:11], v[64:67], a[104:107], v[8:11]\n"
  "s_waitcnt lgkmcnt(4)\n"
  "buffer_store_dwordx4 v[92:95], v109, s[48:51], s68 offen offset:256 nt\n"
  "ds_write_b64 v106, v[28:29] offset:288\n"
  "v_mfma_f32_16x16x32_bf16 v[4:7], v[64:67], a[100:103], v[4:7]\n"
  "s_add_u32 s68, s68, s69\n"
  "v_mfma_f32_16x16x32_bf16 v[4:7], v[72:75], a[124:127], v[4:7]\n"
  "s_mov_b32 s69, s70\n"
  "v_mfma_f32_16x16x32_bf16 v[4:7], v[68:71], a[148:151], v[4:7]\n"
  "s_movk_i32 s70, 6144\n"
  "v_mfma_f32_16x16x32_bf16 v[4:7], v[76:79], a[172:175], v[4:7]\n"
  "ds_write_b64 v106, v[26:27] offset:576\n"
  "v_mfma_f32_16x16x32_bf16 v[0:3], v[76:79], a[168:171], v[0:3]\n"
  "buffer_load_dwordx4 v113, s[76:79], s57 offen offset:1280  lds\n"
  "v_mfma_f32_16x16x32_bf16 v[0:3], v[68:71], a[144:147], v[0:3]\n"
  "v_mfma_f32_16x16x32_bf16 v[0:3], v[72:75], a[120:123], v[0:3]\n"
  "v_mfma_f32_16x16x32_bf16 v[0:3], v[64:67], a[96:99], v[0:3]\n"
  "s_waitcnt lgkmcnt(3)\n"
  "v_mfma_f32_16x16x32_bf16 v[0:3], v[48:51], a[192:195], v[0:3]\n"
  "ds_read_b128 v[64:67], v104 offset:12288\n"
  "v_mfma_f32_16x16x32_bf16 v[0:3], v[56:59], a[216:219], v[0:3]\n"
  "v_mfma_f32_16x16x32_bf16 v[0:3], v[52:55], a[240:243], v[0:3]\n"
  "s_add_u32 m0, s59, 8064\n"
  "ds_read_b128 v[68:71], v104 offset:14336\n"
  "v_xor_b32 v104, s88, v104\n"
  "v_mfma_f32_16x16x32_bf16 v[0:3], v[60:63], v[136:139], v[0:3]\n"
  "buffer_load_dwordx4 v112, s[76:79], s57 offen offset:128  lds\n"
  "v_mfma_f32_16x16x32_bf16 v[4:7], v[60:63], v[140:143], v[4:7]\n"
  "v_mfma_f32_16x16x32_bf16 v[4:7], v[52:55], a[244:247], v[4:7]\n"
  "v_mfma_f32_16x16x32_bf16 v[4:7], v[56:59], a[220:223], v[4:7]\n"
  "ds_read_b128 v[72:75], v105 offset:12288\n"
  "v_mfma_f32_16x16x32_bf16 v[4:7], v[48:51], a[196:199], v[4:7]\n"
  "ds_write_b64 v106, v[30:31] offset:864\n"
  "v_mfma_f32_16x16x32_bf16 v[8:11], v[48:51], a[200:203], v[8:11]\n"
  "v_mfma_f32_16x16x32_bf16 v[8:11], v[56:59], a[224:227], v[8:11]\n"
  "v_mfma_f32_16x16x32_bf16 v[8:11], v[52:55], a[248:251], v[8:11]\n"
  "buffer_load_dwordx4 v113, s[76:79], s57 offen offset:1152  lds\n"
  "ds_read_b128 v[76:79], v105 offset:14336\n"
  "v_mfma_f32_16x16x32_bf16 v[8:11], v[60:63], v[144:147], v[8:11]\n"
  "v_xor_b32 v105, s88, v105\n"
  "v_smfmac_f32_16x16x64_bf16 v[80:83], v[88:91], v[48:55], v124\n"
  "s_waitcnt lgkmcnt(3)\n"
  "v_smfmac_f32_16x16x64_bf16 v[80:83], v[88:91], v[64:71], v124\n"
  "s_xor_b32 s88, s88, s89\n"
  "v_mfma_f32_16x16x32_bf16 v[12:15], v[60:63], v[148:151], v[12:15]\n"
  "v_mfma_f32_16x16x32_bf16 v[12:15], v[52:55], a[252:255], v[12:15]\n"
  "v_mfma_f32_16x16x32_bf16 v[12:15], v[56:59], a[228:231], v[12:15]\n"
  "v_add_f32 v44, v80, v81\n"
  "v_mov_b32 v80, 0\n"
  "v_mfma_f32_16x16x32_bf16 v[12:15], v[48:51], a[204:207], v[12:15]\n"
  "ds_write_b32 v110, v44\n"
  "v_mfma_f32_16x16x32_bf16 v[16:19], v[48:51], a[208:211], v[16:19]\n"
  "v_mov_b32 v81, 0\n"
  "v_mfma_f32_16x16x32_bf16 v[16:19], v[56:59], a[232:235], v[16:19]\n"
  "v_mfma_f32_16x16x32_bf16 v[16:19], v[52:55], v[128:131], v[16:19]\n"
  "v_mfma_f32_16x16x32_bf16 v[16:19], v[60:63], v[152:155], v[16:19]\n"
  "v_xor_b32 v106, 0x2000, v106\n"
  "v_xor_b32 v107, 0x2000, v107\n"
  "v_mfma_f32_16x16x32_bf16 v[20:23], v[60:63], v[156:159], v[20:23]\n"
  "v_xor_b32 v110, 0x2000, v110\n"
  "v_mfma_f32_16x16x32_bf16 v[20:23], v[52:55], v[132:135], v[20:23]\n"
  "v_mfma_f32_16x16x32_bf16 v[20:23], v[56:59], a[236:239], v[20:23]\n"
  "v_mfma_f32_16x16x32_bf16 v[20:23], v[48:51], a[212:215], v[20:23]\n"
  "s_waitcnt vmcnt(27) lgkmcnt(0)\n"
  "s_barrier\n"
  "v_mfma_f32_16x16x32_bf16 v[20:23], v[64:67], v[180:183], v[20:23]\n"
  "s_add_u32 m0, s66, 0\n"
  "ds_read_b128 v[48:51], v104 offset:0\n"
  "ds_read_b128 v[84:87], v111\n"
  "v_mfma_f32_16x16x32_bf16 v[20:23], v[72:75], v[204:207], v[20:23]\n"
  "s_xor_b32 s89, s88, s89\n"
  "ds_read_b128 v[96:99], v111 offset:64\n"
  "v_xor_b32 v111, 0x2000, v111\n"
  "v_mfma_f32_16x16x32_bf16 v[20:23], v[68:71], v[228:231], v[20:23]\n"
  "ds_read_b128 v[52:55], v104 offset:2048\n"
  "v_mfma_f32_16x16x32_bf16 v[44:47], v[76:79], v[252:255], v[20:23]\n"
  "buffer_load_dwordx4 v112, s[44:47], s67 offen offset:0 nt lds\n"
  "v_mfma_f32_16x16x32_bf16 v[16:19], v[76:79], v[248:251], v[16:19]\n"
  "s_add_u32 m0, s66, 1920\n"
  "v_mfma_f32_16x16x32_bf16 v[16:19], v[68:71], v[224:227], v[16:19]\n"
  "v_mfma_f32_16x16x32_bf16 v[16:19], v[72:75], v[200:203], v[16:19]\n"
  "ds_read_b128 v[56:59], v105 offset:0\n"
  "v_mfma_f32_16x16x32_bf16 v[40:43], v[64:67], v[176:179], v[16:19]\n"
  "buffer_load_dwordx4 v112, s[44:47], s67 offen offset:128 nt lds\n"
  "v_mfma_f32_16x16x32_bf16 v[12:15], v[64:67], v[172:175], v[12:15]\n"
  "s_add_u32 m0, s66, 3840\n"
  "v_mfma_f32_16x16x32_bf16 v[12:15], v[72:75], v[196:199], v[12:15]\n"
  "v_mfma_f32_16x16x32_bf16 v[12:15], v[68:71], v[220:223], v[12:15]\n"
  "s_waitcnt lgkmcnt(2)\n"
  "ds_read_b128 v[60:63], v105 offset:2048\n"
  "v_add_f32 v84, v84, v96\n"
  "v_mfma_f32_16x16x32_bf16 v[36:39], v[76:79], v[244:247], v[12:15]\n"
  "buffer_load_dwordx4 v112, s[44:47], s67 offen offset:256 nt lds\n"
  "v_mfma_f32_16x16x32_bf16 v[8:11], v[76:79], v[240:243], v[8:11]\n"
  "v_add_f32 v85, v85, v97\n"
  "v_add_f32 v86, v86, v98\n"
  "v_mfma_f32_16x16x32_bf16 v[8:11], v[68:71], v[216:219], v[8:11]\n"
  "v_add_f32 v87, v87, v99\n"
  "v_max3_f32 v82, v82, v84, v85\n"
  "v_mfma_f32_16x16x32_bf16 v[8:11], v[72:75], v[192:195], v[8:11]\n"
  "v_rcp_f32 v84, v84\n"
  "v_mfma_f32_16x16x32_bf16 v[32:35], v[64:67], v[168:171], v[8:11]\n"
  "v_rcp_f32 v85, v85\n"
  "v_mfma_f32_16x16x32_bf16 v[4:7], v[64:67], v[164:167], v[4:7]\n"
  "v_max3_f32 v82, v82, v86, v87\n"
  "v_max3_f32 v83, v83, v84, v85\n"
  "v_mfma_f32_16x16x32_bf16 v[4:7], v[72:75], v[188:191], v[4:7]\n"
  "s_add_u32 s73, s73, 14336\n"
  "s_mov_b32 m0, 56320\n"
  "s_nop 0\n"
  "buffer_load_dword v125, s[44:47], s73 offen lds\n"
  "v_mfma_f32_16x16x32_bf16 v[4:7], v[68:71], v[212:215], v[4:7]\n"
  "v_rcp_f32 v86, v86\n"
  "v_mfma_f32_16x16x32_bf16 v[28:31], v[76:79], v[236:239], v[4:7]\n"
  "v_rcp_f32 v87, v87\n"
  "v_mfma_f32_16x16x32_bf16 v[0:3], v[76:79], v[232:235], v[0:3]\n"
  "s_add_u32 m0, s59, 10240\n"
  "v_max3_f32 v83, v83, v86, v87\n"
  "v_mul_f32 v96, s99, v84\n"
  "v_mfma_f32_16x16x32_bf16 v[0:3], v[68:71], v[208:211], v[0:3]\n"
  "buffer_load_dwordx4 v112, s[76:79], s57 offen offset:0  lds\n"
  "v_mfma_f32_16x16x32_bf16 v[0:3], v[72:75], v[184:187], v[0:3]\n"
  "v_mul_f32 v97, s99, v85\n"
  "v_mul_f32 v98, s99, v86\n"
  "v_mfma_f32_16x16x32_bf16 v[24:27], v[64:67], v[160:163], v[0:3]\n"
  "s_waitcnt lgkmcnt(0)\n"
  "v_mul_f32 v99, s99, v87\n"
  "v_fma_f32 v40, v40, v84, v96\n"
  "v_mfma_f32_16x16x32_bf16 v[0:3], v[48:51], a[0:3], v[100:103]\n"
  "ds_read_b128 v[64:67], v104 offset:4096\n"
  "v_fma_f32 v44, v44, v84, v96\n"
  "v_mfma_f32_16x16x32_bf16 v[0:3], v[56:59], a[24:27], v[0:3]\n"
  "v_fma_f32 v41, v41, v85, v97\n"
  "v_fma_f32 v45, v45, v85, v97\n"
  "v_mfma_f32_16x16x32_bf16 v[0:3], v[52:55], a[48:51], v[0:3]\n"
  "ds_read_b128 v[68:71], v104 offset:6144\n"
  "v_fma_f32 v42, v42, v86, v98\n"
  "v_mfma_f32_16x16x32_bf16 v[0:3], v[60:63], a[72:75], v[0:3]\n"
  "ds_read_b64_tr_b16 v[92:93], v108 offset:0\n"
  "ds_read_b64_tr_b16 v[94:95], v108 offset:32\n"
  "v_mfma_f32_16x16x32_bf16 v[4:7], v[60:63], a[76:79], v[100:103]\n"
  "buffer_load_dwordx4 v113, s[76:79], s57 offen offset:1024  lds\n"
  "v_mfma_f32_16x16x32_bf16 v[4:7], v[52:55], a[52:55], v[4:7]\n"
  "s_add_u32 s57, s57, 6144\n"
  "v_fma_f32 v46, v46, v86, v98\n"
  "v_fma_f32 v43, v43, v87, v99\n"
  "v_mfma_f32_16x16x32_bf16 v[4:7], v[56:59], a[28:31], v[4:7]\n"
  "ds_read_b128 v[72:75], v105 offset:4096\n"
  "v_fma_f32 v47, v47, v87, v99\n"
  "v_mfma_f32_16x16x32_bf16 v[4:7], v[48:51], a[4:7], v[4:7]\n"
  "v_fma_f32 v24, v24, v84, v96\n"
  "v_fma_f32 v25, v25, v85, v97\n"
  "v_mfma_f32_16x16x32_bf16 v[8:11], v[48:51], a[8:11], v[100:103]\n"
  "v_fma_f32 v28, v28, v84, v96\n"
  "v_fma_f32 v29, v29, v85, v97\n"
  "v_mfma_f32_16x16x32_bf16 v[8:11], v[56:59], a[32:35], v[8:11]\n"
  "v_fma_f32 v32, v32, v84, v96\n"
  "v_fma_f32 v33, v33, v85, v97\n"
  "v_mfma_f32_16x16x32_bf16 v[8:11], v[52:55], a[56:59], v[8:11]\n"
  "s_add_u32 m0, s59, 12288\n"
  "ds_read_b128 v[76:79], v105 offset:6144\n"
  "v_fma_f32 v36, v36, v84, v96\n"
  "v_mfma_f32_16x16x32_bf16 v[8:11], v[60:63], a[80:83], v[8:11]\n"
  "buffer_load_dwordx4 v112, s[76:79], s57 offen offset:0  lds\n"
  "v_smfmac_f32_16x16x64_bf16 v[80:83], v[88:91], v[48:55], v124\n"
  "s_waitcnt lgkmcnt(4)\n"
  "v_fma_f32 v37, v37, v85, v97\n"
  "v_smfmac_f32_16x16x64_bf16 v[80:83], v[88:91], v[64:71], v124\n"
  "s_waitcnt lgkmcnt(2)\n"
  "buffer_store_dwordx4 v[92:95], v109, s[48:51], s68 offen offset:0 nt\n"
  "v_fma_f32 v26, v26, v86, v98\n"
  "v_fma_f32 v27, v27, v87, v99\n"
  "v_mfma_f32_16x16x32_bf16 v[12:15], v[60:63], a[84:87], v[100:103]\n"
  "v_fma_f32 v30, v30, v86, v98\n"
  "v_fma_f32 v31, v31, v87, v99\n"
  "v_mfma_f32_16x16x32_bf16 v[12:15], v[52:55], a[60:63], v[12:15]\n"
  "v_fma_f32 v34, v34, v86, v98\n"
  "v_fma_f32 v35, v35, v87, v99\n"
  "v_mfma_f32_16x16x32_bf16 v[12:15], v[56:59], a[36:39], v[12:15]\n"
  "v_fma_f32 v38, v38, v86, v98\n"
  "v_fma_f32 v39, v39, v87, v99\n"
  "v_mfma_f32_16x16x32_bf16 v[12:15], v[48:51], a[12:15], v[12:15]\n"
  "v_cvt_pk_bf16_f32 v40, v40, v44\n"
  "v_cvt_pk_bf16_f32 v41, v41, v45\n"
  "v_mfma_f32_16x16x32_bf16 v[16:19], v[48:51], a[16:19], v[100:103]\n"
  "buffer_load_dwordx4 v113, s[76:79], s57 offen offset:1024  lds\n"
  "v_mfma_f32_16x16x32_bf16 v[16:19], v[56:59], a[40:43], v[16:19]\n"
  "ds_read_b64_tr_b16 v[96:97], v108 offset:2304\n"
  "ds_read_b64_tr_b16 v[98:99], v108 offset:2336\n"
  "v_mfma_f32_16x16x32_bf16 v[16:19], v[52:55], a[64:67], v[16:19]\n"
  "v_cvt_pk_bf16_f32 v42, v42, v46\n"
  "v_cvt_pk_bf16_f32 v43, v43, v47\n"
  "v_mfma_f32_16x16x32_bf16 v[16:19], v[60:63], a[88:91], v[16:19]\n"
  "v_cvt_pk_bf16_f32 v24, v24, v28\n"
  "v_cvt_pk_bf16_f32 v28, v25, v29\n"
  "v_mfma_f32_16x16x32_bf16 v[20:23], v[60:63], a[92:95], v[100:103]\n"
  "v_cvt_pk_bf16_f32 v25, v32, v36\n"
  "v_cvt_pk_bf16_f32 v29, v33, v37\n"
  "v_mfma_f32_16x16x32_bf16 v[20:23], v[52:55], a[68:71], v[20:23]\n"
  "v_cvt_pk_bf16_f32 v26, v26, v30\n"
  "v_cvt_pk_bf16_f32 v30, v27, v31\n"
  "v_mfma_f32_16x16x32_bf16 v[20:23], v[56:59], a[44:47], v[20:23]\n"
  "s_add_u32 m0, s59, 14208\n"
  "v_cvt_pk_bf16_f32 v27, v34, v38\n"
  "v_cvt_pk_bf16_f32 v31, v35, v39\n"
  "v_mfma_f32_16x16x32_bf16 v[20:23], v[48:51], a[20:23], v[20:23]\n"
  "s_waitcnt lgkmcnt(2)\n"
  "buffer_load_dwordx4 v112, s[76:79], s57 offen offset:128  lds\n"
  "v_mfma_f32_16x16x32_bf16 v[20:23], v[64:67], a[116:119], v[20:23]\n"
  "s_add_u32 s66, s66, 6144\n"
  "ds_read_b128 v[48:51], v104 offset:8192\n"
  "v_mfma_f32_16x16x32_bf16 v[20:23], v[72:75], a[140:143], v[20:23]\n"
  "s_cmp_ge_u32 s66, 102400\n"
  "s_cselect_b32 s64, 36864, 0\n"
  "v_mfma_f32_16x16x32_bf16 v[20:23], v[68:71], a[164:167], v[20:23]\n"
  "s_sub_u32 s66, s66, s64\n"
  "ds_read_b128 v[52:55], v104 offset:10240\n"
  "v_mfma_f32_16x16x32_bf16 v[20:23], v[76:79], a[188:191], v[20:23]\n"
  "s_sub_u32 s90, s90, 1\n"
  "s_cselect_b32 s63, s91, 6144\n"
  "s_cmov_b64 s[90:91], s[92:93]\n"
  "s_cmov_b64 s[92:93], s[94:95]\n"
  "s_waitcnt lgkmcnt(2)\n"
  "buffer_store_dwordx4 v[96:99], v109, s[48:51], s68 offen offset:128 nt\n"
  "ds_write2_b32 v107, v40, v41 offset0:0 offset1:72\n"
  "v_mfma_f32_16x16x32_bf16 v[16:19], v[76:79], a[184:187], v[16:19]\n"
  "s_add_u32 s67, s67, s63\n"
  "v_mfma_f32_16x16x32_bf16 v[16:19], v[68:71], a[160:163], v[16:19]\n"
  "ds_read_b64_tr_b16 v[92:93], v108 offset:4608\n"
  "ds_read_b64_tr_b16 v[94:95], v108 offset:4640\n"
  "v_mfma_f32_16x16x32_bf16 v[16:19], v[72:75], a[136:139], v[16:19]\n"
  "buffer_load_dwordx4 v113, s[76:79], s57 offen offset:1152  lds\n"
  "ds_read_b128 v[56:59], v105 offset:8192\n"
  "v_mfma_f32_16x16x32_bf16 v[16:19], v[64:67], a[112:115], v[16:19]\n"
  "ds_write2_b32 v107, v42, v43 offset0:144 offset1:216\n"
  "v_mfma_f32_16x16x32_bf16 v[12:15], v[64:67], a[108:111], v[12:15]\n"
  "v_xor_b32 v108, 0x2000, v108\n"
  "v_mfma_f32_16x16x32_bf16 v[12:15], v[72:75], a[132:135], v[12:15]\n"
  "v_mfma_f32_16x16x32_bf16 v[12:15], v[68:71], a[156:159], v[12:15]\n"
  "ds_read_b128 v[60:63], v105 offset:10240\n"
  "v_mfma_f32_16x16x32_bf16 v[12:15], v[76:79], a[180:183], v[12:15]\n"
  "ds_write_b64 v106, v[24:25] offset:0\n"
  "v_mfma_f32_16x16x32_bf16 v[8:11], v[76:79], a[176:179], v[8:11]\n"
  "s_add_u32 m0, s59, 16128\n"
  "v_mfma_f32_16x16x32_bf16 v[8:11], v[68:71], a[152:155], v[8:11]\n"
  "buffer_load_dwordx4 v112, s[76:79], s57 offen offset:256  lds\n"
  "v_mfma_f32_16x16x32_bf16 v[8:11], v[72:75], a[128:131], v[8:11]\n"
  "v_mfma_f32_16x16x32_bf16 v[8:11], v[64:67], a[104:107], v[8:11]\n"
  "s_waitcnt lgkmcnt(4)\n"
  "buffer_store_dwordx4 v[92:95], v109, s[48:51], s68 offen offset:256 nt\n"
  "ds_write_b64 v106, v[28:29] offset:288\n"
  "v_mfma_f32_16x16x32_bf16 v[4:7], v[64:67], a[100:103], v[4:7]\n"
  "s_add_u32 s68, s68, s69\n"
  "v_mfma_f32_16x16x32_bf16 v[4:7], v[72:75], a[124:127], v[4:7]\n"
  "s_mov_b32 s69, s70\n"
  "v_mfma_f32_16x16x32_bf16 v[4:7], v[68:71], a[148:151], v[4:7]\n"
  "s_movk_i32 s70, 6144\n"
  "v_mfma_f32_16x16x32_bf16 v[4:7], v[76:79], a[172:175], v[4:7]\n"
  "ds_write_b64 v106, v[26:27] offset:576\n"
  "v_mfma_f32_16x16x32_bf16 v[0:3], v[76:79], a[168:171], v[0:3]\n"
  "buffer_load_dwordx4 v113, s[76:79], s57 offen offset:1280  lds\n"
  "v_mfma_f32_16x16x32_bf16 v[0:3], v[68:71], a[144:147], v[0:3]\n"
  "s_add_u32 s57, s57, 6144\n"
  "v_mfma_f32_16x16x32_bf16 v[0:3], v[72:75], a[120:123], v[0:3]\n"
  "v_mfma_f32_16x16x32_bf16 v[0:3], v[64:67], a[96:99], v[0:3]\n"
  "s_waitcnt lgkmcnt(3)\n"
  "v_mfma_f32_16x16x32_bf16 v[0:3], v[48:51], a[192:195], v[0:3]\n"
  "ds_read_b128 v[64:67], v104 offset:12288\n"
  "v_mfma_f32_16x16x32_bf16 v[0:3], v[56:59], a[216:219], v[0:3]\n"
  "v_mfma_f32_16x16x32_bf16 v[0:3], v[52:55], a[240:243], v[0:3]\n"
  "s_add_u32 m0, s59, 18176\n"
  "ds_read_b128 v[68:71], v104 offset:14336\n"
  "v_xor_b32 v104, s88, v104\n"
  "v_mfma_f32_16x16x32_bf16 v[0:3], v[60:63], v[136:139], v[0:3]\n"
  "buffer_load_dwordx4 v112, s[76:79], s57 offen offset:256  lds\n"
  "v_mfma_f32_16x16x32_bf16 v[4:7], v[60:63], v[140:143], v[4:7]\n"
  "v_mfma_f32_16x16x32_bf16 v[4:7], v[52:55], a[244:247], v[4:7]\n"
  "v_mfma_f32_16x16x32_bf16 v[4:7], v[56:59], a[220:223], v[4:7]\n"
  "ds_read_b128 v[72:75], v105 offset:12288\n"
  "v_mfma_f32_16x16x32_bf16 v[4:7], v[48:51], a[196:199], v[4:7]\n"
  "ds_write_b64 v106, v[30:31] offset:864\n"
  "v_mfma_f32_16x16x32_bf16 v[8:11], v[48:51], a[200:203], v[8:11]\n"
  "v_mfma_f32_16x16x32_bf16 v[8:11], v[56:59], a[224:227], v[8:11]\n"
  "v_mfma_f32_16x16x32_bf16 v[8:11], v[52:55], a[248:251], v[8:11]\n"
  "buffer_load_dwordx4 v113, s[76:79], s57 offen offset:1280  lds\n"
  "ds_read_b128 v[76:79], v105 offset:14336\n"
  "v_mfma_f32_16x16x32_bf16 v[8:11], v[60:63], v[144:147], v[8:11]\n"
  "s_sub_u32 s57, s57, s85\n"
  "v_xor_b32 v105, s88, v105\n"
  "v_smfmac_f32_16x16x64_bf16 v[80:83], v[88:91], v[48:55], v124\n"
  "s_waitcnt lgkmcnt(3)\n"
  "v_smfmac_f32_16x16x64_bf16 v[80:83], v[88:91], v[64:71], v124\n"
  "s_xor_b32 s88, s88, s89\n"
  "v_mfma_f32_16x16x32_bf16 v[12:15], v[60:63], v[148:151], v[12:15]\n"
  "v_mfma_f32_16x16x32_bf16 v[12:15], v[52:55], a[252:255], v[12:15]\n"
  "v_mfma_f32_16x16x32_bf16 v[12:15], v[56:59], a[228:231], v[12:15]\n"
  "v_add_f32 v44, v80, v81\n"
  "v_mov_b32 v80, 0\n"
  "v_mfma_f32_16x16x32_bf16 v[12:15], v[48:51], a[204:207], v[12:15]\n"
  "ds_write_b32 v110, v44\n"
  "v_mfma_f32_16x16x32_bf16 v[16:19], v[48:51], a[208:211], v[16:19]\n"
  "v_mov_b32 v81, 0\n"
  "v_mfma_f32_16x16x32_bf16 v[16:19], v[56:59], a[232:235], v[16:19]\n"
  "v_mfma_f32_16x16x32_bf16 v[16:19], v[52:55], v[128:131], v[16:19]\n"
  "v_mfma_f32_16x16x32_bf16 v[16:19], v[60:63], v[152:155], v[16:19]\n"
  "v_xor_b32 v106, 0x2000, v106\n"
  "v_xor_b32 v107, 0x2000, v107\n"
  "v_mfma_f32_16x16x32_bf16 v[20:23], v[60:63], v[156:159], v[20:23]\n"
  "v_xor_b32 v110, 0x2000, v110\n"
  "v_mfma_f32_16x16x32_bf16 v[20:23], v[52:55], v[132:135], v[20:23]\n"
  "v_mfma_f32_16x16x32_bf16 v[20:23], v[56:59], a[236:239], v[20:23]\n"
  "v_mfma_f32_16x16x32_bf16 v[20:23], v[48:51], a[212:215], v[20:23]\n"
  "s_waitcnt vmcnt(17) lgkmcnt(0)\n"
  "s_barrier\n"
  "v_mfma_f32_16x16x32_bf16 v[20:23], v[64:67], v[180:183], v[20:23]\n"
  "s_add_u32 m0, s66, 0\n"
  "ds_read_b128 v[48:51], v104 offset:0\n"
  "ds_read_b128 v[84:87], v111\n"
  "v_mfma_f32_16x16x32_bf16 v[20:23], v[72:75], v[204:207], v[20:23]\n"
  "s_xor_b32 s89, s88, s89\n"
  "ds_read_b128 v[96:99], v111 offset:64\n"
  "v_xor_b32 v111, 0x2000, v111\n"
  "v_mfma_f32_16x16x32_bf16 v[20:23], v[68:71], v[228:231], v[20:23]\n"
  "ds_read_b128 v[52:55], v104 offset:2048\n"
  "v_mfma_f32_16x16x32_bf16 v[44:47], v[76:79], v[252:255], v[20:23]\n"
  "buffer_load_dwordx4 v112, s[44:47], s67 offen offset:0 nt lds\n"
  "v_mfma_f32_16x16x32_bf16 v[16:19], v[76:79], v[248:251], v[16:19]\n"
  "s_add_u32 m0, s66, 1920\n"
  "v_mfma_f32_16x16x32_bf16 v[16:19], v[68:71], v[224:227], v[16:19]\n"
  "v_mfma_f32_16x16x32_bf16 v[16:19], v[72:75], v[200:203], v[16:19]\n"
  "ds_read_b128 v[56:59], v105 offset:0\n"
  "v_mfma_f32_16x16x32_bf16 v[40:43], v[64:67], v[176:179], v[16:19]\n"
  "buffer_load_dwordx4 v112, s[44:47], s67 offen offset:128 nt lds\n"
  "v_mfma_f32_16x16x32_bf16 v[12:15], v[64:67], v[172:175], v[12:15]\n"
  "s_add_u32 m0, s66, 3840\n"
  "v_mfma_f32_16x16x32_bf16 v[12:15], v[72:75], v[196:199], v[12:15]\n"
  "v_mfma_f32_16x16x32_bf16 v[12:15], v[68:71], v[220:223], v[12:15]\n"
  "s_waitcnt lgkmcnt(2)\n"
  "ds_read_b128 v[60:63], v105 offset:2048\n"
  "v_add_f32 v84, v84, v96\n"
  "v_mfma_f32_16x16x32_bf16 v[36:39], v[76:79], v[244:247], v[12:15]\n"
  "buffer_load_dwordx4 v112, s[44:47], s67 offen offset:256 nt lds\n"
  "v_mfma_f32_16x16x32_bf16 v[8:11], v[76:79], v[240:243], v[8:11]\n"
  "v_add_f32 v85, v85, v97\n"
  "v_add_f32 v86, v86, v98\n"
  "v_mfma_f32_16x16x32_bf16 v[8:11], v[68:71], v[216:219], v[8:11]\n"
  "v_add_f32 v87, v87, v99\n"
  "v_max3_f32 v82, v82, v84, v85\n"
  "v_mfma_f32_16x16x32_bf16 v[8:11], v[72:75], v[192:195], v[8:11]\n"
  "s_add_u32 m0, s59, 21504\n"
  "v_rcp_f32 v84, v84\n"
  "v_mfma_f32_16x16x32_bf16 v[32:35], v[64:67], v[168:171], v[8:11]\n"
  "buffer_load_dwordx4 v114, s[40:43], s57 offen offset:0  lds\n"
  "v_mfma_f32_16x16x32_bf16 v[4:7], v[64:67], v[164:167], v[4:7]\n"
  "v_rcp_f32 v85, v85\n"
  "v_mfma_f32_16x16x32_bf16 v[4:7], v[72:75], v[188:191], v[4:7]\n"
  "v_max3_f32 v82, v82, v86, v87\n"
  "v_max3_f32 v83, v83, v84, v85\n"
  "v_mfma_f32_16x16x32_bf16 v[4:7], v[68:71], v[212:215], v[4:7]\n"
  "v_rcp_f32 v86, v86\n"
  "v_mfma_f32_16x16x32_bf16 v[28:31], v[76:79], v[236:239], v[4:7]\n"
  "buffer_load_dwordx4 v115, s[40:43], s57 offen offset:1024  lds\n"
  "v_mfma_f32_16x16x32_bf16 v[0:3], v[76:79], v[232:235], v[0:3]\n"
  "v_rcp_f32 v87, v87\n"
  "v_mfma_f32_16x16x32_bf16 v[0:3], v[68:71], v[208:211], v[0:3]\n"
  "v_max3_f32 v83, v83, v86, v87\n"
  "v_mul_f32 v96, s99, v84\n"
  "v_mfma_f32_16x16x32_bf16 v[0:3], v[72:75], v[184:187], v[0:3]\n"
  "v_mul_f32 v97, s99, v85\n"
  "v_mul_f32 v98, s99, v86\n"
  "v_mfma_f32_16x16x32_bf16 v[24:27], v[64:67], v[160:163], v[0:3]\n"
  "s_waitcnt lgkmcnt(0)\n"
  "buffer_load_dwordx4 v116, s[40:43], s57 offen offset:2048  lds\n"
  "v_mfma_f32_16x16x32_bf16 v[0:3], v[48:51], a[0:3], v[100:103]\n"
  "s_add_u32 s57, s57, 6144\n"
  "ds_read_b128 v[64:67], v104 offset:4096\n"
  "v_mul_f32 v99, s99, v87\n"
  "v_mfma_f32_16x16x32_bf16 v[0:3], v[56:59], a[24:27], v[0:3]\n"
  "v_fma_f32 v40, v40, v84, v96\n"
  "v_fma_f32 v44, v44, v84, v96\n"
  "v_mfma_f32_16x16x32_bf16 v[0:3], v[52:55], a[48:51], v[0:3]\n"
  "s_add_u32 m0, s59, 24576\n"
  "ds_read_b128 v[68:71], v104 offset:6144\n"
  "v_fma_f32 v41, v41, v85, v97\n"
  "v_mfma_f32_16x16x32_bf16 v[0:3], v[60:63], a[72:75], v[0:3]\n"
  "buffer_load_dwordx4 v114, s[40:43], s57 offen offset:0  lds\n"
  "v_mfma_f32_16x16x32_bf16 v[4:7], v[60:63], a[76:79], v[100:103]\n"
  "ds_read_b64_tr_b16 v[92:93], v108 offset:0\n"
  "ds_read_b64_tr_b16 v[94:95], v108 offset:32\n"
  "v_mfma_f32_16x16x32_bf16 v[4:7], v[52:55], a[52:55], v[4:7]\n"
  "v_fma_f32 v45, v45, v85, v97\n"
  "v_fma_f32 v42, v42, v86, v98\n"
  "v_mfma_f32_16x16x32_bf16 v[4:7], v[56:59], a[28:31], v[4:7]\n"
  "ds_read_b128 v[72:75], v105 offset:4096\n"
  "v_fma_f32 v46, v46, v86, v98\n"
  "v_mfma_f32_16x16x32_bf16 v[4:7], v[48:51], a[4:7], v[4:7]\n"
  "buffer_load_dwordx4 v115, s[40:43], s57 offen offset:1024  lds\n"
  "v_mfma_f32_16x16x32_bf16 v[8:11], v[48:51], a[8:11], v[100:103]\n"
  "v_fma_f32 v43, v43, v87, v99\n"
  "v_fma_f32 v47, v47, v87, v99\n"
  "v_mfma_f32_16x16x32_bf16 v[8:11], v[56:59], a[32:35], v[8:11]\n"
  "v_fma_f32 v24, v24, v84, v96\n"
  "v_fma_f32 v25, v25, v85, v97\n"
  "v_mfma_f32_16x16x32_bf16 v[8:11], v[52:55], a[56:59], v[8:11]\n"
  "ds_read_b128 v[76:79], v105 offset:6144\n"
  "v_fma_f32 v28, v28, v84, v96\n"
  "v_mfma_f32_16x16x32_bf16 v[8:11], v[60:63], a[80:83], v[8:11]\n"
  "buffer_load_dwordx4 v116, s[40:43], s57 offen offset:2048  lds\n"
  "v_smfmac_f32_16x16x64_bf16 v[80:83], v[88:91], v[48:55], v124\n"
  "s_waitcnt lgkmcnt(4)\n"
  "s_add_u32 s57, s57, s58\n"
  "v_fma_f32 v29, v29, v85, v97\n"
  "v_smfmac_f32_16x16x64_bf16 v[80:83], v[88:91], v[64:71], v124\n"
  "s_waitcnt lgkmcnt(2)\n"
  "buffer_store_dwordx4 v[92:95], v109, s[48:51], s68 offen offset:0 nt\n"
  "v_fma_f32 v32, v32, v84, v96\n"
  "v_fma_f32 v33, v33, v85, v97\n"
  "v_mfma_f32_16x16x32_bf16 v[12:15], v[60:63], a[84:87], v[100:103]\n"
  "s_add_u32 m0, s59, 27648\n"
  "v_fma_f32 v36, v36, v84, v96\n"
  "v_fma_f32 v37, v37, v85, v97\n"
  "v_mfma_f32_16x16x32_bf16 v[12:15], v[52:55], a[60:63], v[12:15]\n"
  "buffer_load_dwordx4 v114, s[40:43], s57 offen offset:0  lds\n"
  "v_mfma_f32_16x16x32_bf16 v[12:15], v[56:59], a[36:39], v[12:15]\n"
  "v_fma_f32 v26, v26, v86, v98\n"
  "v_fma_f32 v27, v27, v87, v99\n"
  "v_mfma_f32_16x16x32_bf16 v[12:15], v[48:51], a[12:15], v[12:15]\n"
  "v_fma_f32 v30, v30, v86, v98\n"
  "v_fma_f32 v31, v31, v87, v99\n"
  "v_mfma_f32_16x16x32_bf16 v[16:19], v[48:51], a[16:19], v[100:103]\n"
  "v_fma_f32 v34, v34, v86, v98\n"
  "v_fma_f32 v35, v35, v87, v99\n"
  "v_mfma_f32_16x16x32_bf16 v[16:19], v[56:59], a[40:43], v[16:19]\n"
  "buffer_load_dwordx4 v115, s[40:43], s57 offen offset:1024  lds\n"
  "v_mfma_f32_16x16x32_bf16 v[16:19], v[52:55], a[64:67], v[16:19]\n"
  "v_fma_f32 v38, v38, v86, v98\n"
  "v_fma_f32 v39, v39, v87, v99\n"
  "v_mfma_f32_16x16x32_bf16 v[16:19], v[60:63], a[88:91], v[16:19]\n"
  "ds_read_b64_tr_b16 v[96:97], v108 offset:2304\n"
  "ds_read_b64_tr_b16 v[98:99], v108 offset:2336\n"
  "v_mfma_f32_16x16x32_bf16 v[20:23], v[60:63], a[92:95], v[100:103]\n"
  "v_cvt_pk_bf16_f32 v40, v40, v44\n"
  "v_cvt_pk_bf16_f32 v41, v41, v45\n"
  "v_mfma_f32_16x16x32_bf16 v[20:23], v[52:55], a[68:71], v[20:23]\n"
  "buffer_load_dwordx4 v116, s[40:43], s57 offen offset:2048  lds\n"
  "v_mfma_f32_16x16x32_bf16 v[20:23], v[56:59], a[44:47], v[20:23]\n"
  "s_add_u32 s57, s57, 6144\n"
  "v_cvt_pk_bf16_f32 v42, v42, v46\n"
  "v_cvt_pk_bf16_f32 v43, v43, v47\n"
  "v_mfma_f32_16x16x32_bf16 v[20:23], v[48:51], a[20:23], v[20:23]\n"
  "s_waitcnt lgkmcnt(2)\n"
  "v_cvt_pk_bf16_f32 v24, v24, v28\n"
  "v_cvt_pk_bf16_f32 v28, v25, v29\n"
  "v_mfma_f32_16x16x32_bf16 v[20:23], v[64:67], a[116:119], v[20:23]\n"
  "s_add_u32 s66, s66, 6144\n"
  "ds_read_b128 v[48:51], v104 offset:8192\n"
  "v_cvt_pk_bf16_f32 v25, v32, v36\n"
  "v_mfma_f32_16x16x32_bf16 v[20:23], v[72:75], a[140:143], v[20:23]\n"
  "s_cmp_ge_u32 s66, 102400\n"
  "s_cselect_b32 s64, 36864, 0\n"
  "v_cvt_pk_bf16_f32 v29, v33, v37\n"
  "v_cvt_pk_bf16_f32 v26, v26, v30\n"
  "v_mfma_f32_16x16x32_bf16 v[20:23], v[68:71], a[164:167], v[20:23]\n"
  "s_sub_u32 s66, s66, s64\n"
  "ds_read_b128 v[52:55], v104 offset:10240\n"
  "v_cvt_pk_bf16_f32 v30, v27, v31\n"
  "v_mfma_f32_16x16x32_bf16 v[20:23], v[76:79], a[188:191], v[20:23]\n"
  "s_sub_u32 s90, s90, 1\n"
  "s_cselect_b32 s63, s91, 6144\n"
  "s_cmov_b64 s[90:91], s[92:93]\n"
  "s_cmov_b64 s[92:93], s[94:95]\n"
  "s_waitcnt lgkmcnt(2)\n"
  "buffer_store_dwordx4 v[96:99], v109, s[48:51], s68 offen offset:128 nt\n"
  "ds_write2_b32 v107, v40, v41 offset0:0 offset1:72\n"
  "v_mfma_f32_16x16x32_bf16 v[16:19], v[76:79], a[184:187], v[16:19]\n"
  "s_add_u32 s67, s67, s63\n"
  "v_cvt_pk_bf16_f32 v27, v34, v38\n"
  "v_cvt_pk_bf16_f32 v31, v35, v39\n"
  "v_mfma_f32_16x16x32_bf16 v[16:19], v[68:71], a[160:163], v[16:19]\n"
  "ds_read_b64_tr_b16 v[92:93], v108 offset:4608\n"
  "ds_read_b64_tr_b16 v[94:95], v108 offset:4640\n"
  "v_mfma_f32_16x16x32_bf16 v[16:19], v[72:75], a[136:139], v[16:19]\n"
  "ds_read_b128 v[56:59], v105 offset:8192\n"
  "v_xor_b32 v108, 0x2000, v108\n"
  "v_mfma_f32_16x16x32_bf16 v[16:19], v[64:67], a[112:115], v[16:19]\n"
  "ds_write2_b32 v107, v42, v43 offset0:144 offset1:216\n"
  "v_mfma_f32_16x16x32_bf16 v[12:15], v[64:67], a[108:111], v[12:15]\n"
  "v_mfma_f32_16x16x32_bf16 v[12:15], v[72:75], a[132:135], v[12:15]\n"
  "v_mfma_f32_16x16x32_bf16 v[12:15], v[68:71], a[156:159], v[12:15]\n"
  "ds_read_b128 v[60:63], v105 offset:10240\n"
  "v_mfma_f32_16x16x32_bf16 v[12:15], v[76:79], a[180:183], v[12:15]\n"
  "ds_write_b64 v106, v[24:25] offset:0\n"
  "v_mfma_f32_16x16x32_bf16 v[8:11], v[76:79], a[176:179], v[8:11]\n"
  "v_mfma_f32_16x16x32_bf16 v[8:11], v[68:71], a[152:155], v[8:11]\n"
  "v_mfma_f32_16x16x32_bf16 v[8:11], v[72:75], a[128:131], v[8:11]\n"
  "v_mfma_f32_16x16x32_bf16 v[8:11], v[64:67], a[104:107], v[8:11]\n"
  "s_waitcnt lgkmcnt(4)\n"
  "buffer_store_dwordx4 v[92:95], v109, s[48:51], s68 offen offset:256 nt\n"
  "ds_write_b64 v106, v[28:29] offset:288\n"
  "v_mfma_f32_16x16x32_bf16 v[4:7], v[64:67], a[100:103], v[4:7]\n"
  "s_add_u32 s68, s68, s69\n"
  "v_mfma_f32_16x16x32_bf16 v[4:7], v[72:75], a[124:127], v[4:7]\n"
  "s_mov_b32 s69, s70\n"
  "v_mfma_f32_16x16x32_bf16 v[4:7], v[68:71], a[148:151], v[4:7]\n"
  "s_movk_i32 s70, 6144\n"
  "v_mfma_f32_16x16x32_bf16 v[4:7], v[76:79], a[172:175], v[4:7]\n"
  "ds_write_b64 v106, v[26:27] offset:576\n"
  "v_mfma_f32_16x16x32_bf16 v[0:3], v[76:79], a[168:171], v[0:3]\n"
  "v_mfma_f32_16x16x32_bf16 v[0:3], v[68:71], a[144:147], v[0:3]\n"
  "v_mfma_f32_16x16x32_bf16 v[0:3], v[72:75], a[120:123], v[0:3]\n"
  "v_mfma_f32_16x16x32_bf16 v[0:3], v[64:67], a[96:99], v[0:3]\n"
  "s_waitcnt lgkmcnt(3)\n"
  "v_mfma_f32_16x16x32_bf16 v[0:3], v[48:51], a[192:195], v[0:3]\n"
  "ds_read_b128 v[64:67], v104 offset:12288\n"
  "v_mfma_f32_16x16x32_bf16 v[0:3], v[56:59], a[216:219], v[0:3]\n"
  "v_mfma_f32_16x16x32_bf16 v[0:3], v[52:55], a[240:243], v[0:3]\n"
  "ds_read_b128 v[68:71], v104 offset:14336\n"
  "v_xor_b32 v104, s88, v104\n"
  "v_mfma_f32_16x16x32_bf16 v[0:3], v[60:63], v[136:139], v[0:3]\n"
  "ds_write_b64 v106, v[30:31] offset:864\n"
  "v_mfma_f32_16x16x32_bf16 v[4:7], v[60:63], v[140:143], v[4:7]\n"
  "v_mfma_f32_16x16x32_bf16 v[4:7], v[52:55], a[244:247], v[4:7]\n"
  "v_mfma_f32_16x16x32_bf16 v[4:7], v[56:59], a[220:223], v[4:7]\n"
  "ds_read_b128 v[72:75], v105 offset:12288\n"
  "v_mfma_f32_16x16x32_bf16 v[4:7], v[48:51], a[196:199], v[4:7]\n"
  "v_mfma_f32_16x16x32_bf16 v[8:11], v[48:51], a[200:203], v[8:11]\n"
  "v_mfma_f32_16x16x32_bf16 v[8:11], v[56:59], a[224:227], v[8:11]\n"
  "v_mfma_f32_16x16x32_bf16 v[8:11], v[52:55], a[248:251], v[8:11]\n"
  "ds_read_b128 v[76:79], v105 offset:14336\n"
  "v_xor_b32 v105, s88, v105\n"
  "v_mfma_f32_16x16x32_bf16 v[8:11], v[60:63], v[144:147], v[8:11]\n"
  "s_xor_b32 s88, s88, s89\n"
  "v_smfmac_f32_16x16x64_bf16 v[80:83], v[88:91], v[48:55], v124\n"
  "s_waitcnt lgkmcnt(3)\n"
  "v_smfmac_f32_16x16x64_bf16 v[80:83], v[88:91], v[64:71], v124\n"
  "v_mfma_f32_16x16x32_bf16 v[12:15], v[60:63], v[148:151], v[12:15]\n"
  "v_mfma_f32_16x16x32_bf16 v[12:15], v[52:55], a[252:255], v[12:15]\n"
  "v_mfma_f32_16x16x32_bf16 v[12:15], v[56:59], a[228:231], v[12:15]\n"
  "v_add_f32 v44, v80, v81\n"
  "v_mov_b32 v80, 0\n"
  "v_mfma_f32_16x16x32_bf16 v[12:15], v[48:51], a[204:207], v[12:15]\n"
  "ds_write_b32 v110, v44\n"
  "v_mfma_f32_16x16x32_bf16 v[16:19], v[48:51], a[208:211], v[16:19]\n"
  "v_mov_b32 v81, 0\n"
  "v_mfma_f32_16x16x32_bf16 v[16:19], v[56:59], a[232:235], v[16:19]\n"
  "v_mfma_f32_16x16x32_bf16 v[16:19], v[52:55], v[128:131], v[16:19]\n"
  "v_mfma_f32_16x16x32_bf16 v[16:19], v[60:63], v[152:155], v[16:19]\n"
  "v_xor_b32 v106, 0x2000, v106\n"
  "v_xor_b32 v107, 0x2000, v107\n"
  "v_mfma_f32_16x16x32_bf16 v[20:23], v[60:63], v[156:159], v[20:23]\n"
  "v_xor_b32 v110, 0x2000, v110\n"
  "v_mfma_f32_16x16x32_bf16 v[20:23], v[52:55], v[132:135], v[20:23]\n"
  "v_mfma_f32_16x16x32_bf16 v[20:23], v[56:59], a[236:239], v[20:23]\n"
  "v_mfma_f32_16x16x32_bf16 v[20:23], v[48:51], a[212:215], v[20:23]\n"
  "s_waitcnt vmcnt(15) lgkmcnt(0)\n"
  "s_barrier\n"
  "v_mfma_f32_16x16x32_bf16 v[20:23], v[64:67], v[180:183], v[20:23]\n"
  "s_add_u32 m0, s66, 0\n"
  "ds_read_b128 v[48:51], v104 offset:0\n"
  "ds_read_b128 v[84:87], v111\n"
  "v_mfma_f32_16x16x32_bf16 v[20:23], v[72:75], v[204:207], v[20:23]\n"
  "s_xor_b32 s89, s88, s89\n"
  "ds_read_b128 v[96:99], v111 offset:64\n"
  "v_xor_b32 v111, 0x2000, v111\n"
  "v_mfma_f32_16x16x32_bf16 v[20:23], v[68:71], v[228:231], v[20:23]\n"
  "ds_read_b128 v[52:55], v104 offset:2048\n"
  "v_mfma_f32_16x16x32_bf16 v[44:47], v[76:79], v[252:255], v[20:23]\n"
  "buffer_load_dwordx4 v112, s[44:47], s67 offen offset:0 nt lds\n"
  "v_mfma_f32_16x16x32_bf16 v[16:19], v[76:79], v[248:251], v[16:19]\n"
  "s_add_u32 m0, s66, 1920\n"
  "v_mfma_f32_16x16x32_bf16 v[16:19], v[68:71], v[224:227], v[16:19]\n"
  "v_mfma_f32_16x16x32_bf16 v[16:19], v[72:75], v[200:203], v[16:19]\n"
  "ds_read_b128 v[56:59], v105 offset:0\n"
  "v_mfma_f32_16x16x32_bf16 v[40:43], v[64:67], v[176:179], v[16:19]\n"
  "buffer_load_dwordx4 v112, s[44:47], s67 offen offset:128 nt lds\n"
  "v_mfma_f32_16x16x32_bf16 v[12:15], v[64:67], v[172:175], v[12:15]\n"
  "s_add_u32 m0, s66, 3840\n"
  "v_mfma_f32_16x16x32_bf16 v[12:15], v[72:75], v[196:199], v[12:15]\n"
  "v_mfma_f32_16x16x32_bf16 v[12:15], v[68:71], v[220:223], v[12:15]\n"
  "s_waitcnt lgkmcnt(2)\n"
  "ds_read_b128 v[60:63], v105 offset:2048\n"
  "v_add_f32 v84, v84, v96\n"
  "v_mfma_f32_16x16x32_bf16 v[36:39], v[76:79], v[244:247], v[12:15]\n"
  "buffer_load_dwordx4 v112, s[44:47], s67 offen offset:256 nt lds\n"
  "v_mfma_f32_16x16x32_bf16 v[8:11], v[76:79], v[240:243], v[8:11]\n"
  "v_add_f32 v85, v85, v97\n"
  "v_add_f32 v86, v86, v98\n"
  "v_mfma_f32_16x16x32_bf16 v[8:11], v[68:71], v[216:219], v[8:11]\n"
  "v_add_f32 v87, v87, v99\n"
  "v_max3_f32 v82, v82, v84, v85\n"
  "v_mfma_f32_16x16x32_bf16 v[8:11], v[72:75], v[192:195], v[8:11]\n"
  "v_rcp_f32 v84, v84\n"
  "v_mfma_f32_16x16x32_bf16 v[32:35], v[64:67], v[168:171], v[8:11]\n"
  "v_rcp_f32 v85, v85\n"
  "v_mfma_f32_16x16x32_bf16 v[4:7], v[64:67], v[164:167], v[4:7]\n"
  "v_max3_f32 v82, v82, v86, v87\n"
  "v_max3_f32 v83, v83, v84, v85\n"
  "v_mfma_f32_16x16x32_bf16 v[4:7], v[72:75], v[188:191], v[4:7]\n"
  "v_rcp_f32 v86, v86\n"
  "v_mfma_f32_16x16x32_bf16 v[4:7], v[68:71], v[212:215], v[4:7]\n"
  "v_rcp_f32 v87, v87\n"
  "v_mfma_f32_16x16x32_bf16 v[28:31], v[76:79], v[236:239], v[4:7]\n"
  "v_max3_f32 v83, v83, v86, v87\n"
  "v_mul_f32 v96, s99, v84\n"
  "v_mfma_f32_16x16x32_bf16 v[0:3], v[76:79], v[232:235], v[0:3]\n"
  "v_mul_f32 v97, s99, v85\n"
  "v_fma_f32 v40, v40, v84, v96\n"
  "v_mfma_f32_16x16x32_bf16 v[0:3], v[68:71], v[208:211], v[0:3]\n"
  "v_fma_f32 v44, v44, v84, v96\n"
  "v_cvt_pk_bf16_f32 v40, v40, v44\n"
  "v_mfma_f32_16x16x32_bf16 v[0:3], v[72:75], v[184:187], v[0:3]\n"
  "v_fma_f32 v41, v41, v85, v97\n"
  "v_fma_f32 v45, v45, v85, v97\n"
  "v_mfma_f32_16x16x32_bf16 v[24:27], v[64:67], v[160:163], v[0:3]\n"
  "s_waitcnt lgkmcnt(0)\n"
  "v_cvt_pk_bf16_f32 v41, v41, v45\n"
  "v_mul_f32 v98, s99, v86\n"
  "v_mfma_f32_16x16x32_bf16 v[0:3], v[48:51], a[0:3], v[100:103]\n"
  "ds_read_b128 v[64:67], v104 offset:4096\n"
  "v_mul_f32 v99, s99, v87\n"
  "v_mfma_f32_16x16x32_bf16 v[0:3], v[56:59], a[24:27], v[0:3]\n"
  "v_fma_f32 v42, v42, v86, v98\n"
  "v_fma_f32 v46, v46, v86, v98\n"
  "v_mfma_f32_16x16x32_bf16 v[0:3], v[52:55], a[48:51], v[0:3]\n"
  "ds_read_b128 v[68:71], v104 offset:6144\n"
  "v_cvt_pk_bf16_f32 v42, v42, v46\n"
  "v_mfma_f32_16x16x32_bf16 v[0:3], v[60:63], a[72:75], v[0:3]\n"
  "ds_read_b64_tr_b16 v[92:93], v108 offset:0\n"
  "ds_read_b64_tr_b16 v[94:95], v108 offset:32\n"
  "v_mfma_f32_16x16x32_bf16 v[4:7], v[60:63], a[76:79], v[100:103]\n"
  "v_fma_f32 v43, v43, v87, v99\n"
  "v_fma_f32 v47, v47, v87, v99\n"
  "v_mfma_f32_16x16x32_bf16 v[4:7], v[52:55], a[52:55], v[4:7]\n"
  "v_cvt_pk_bf16_f32 v43, v43, v47\n"
  "v_fma_f32 v24, v24, v84, v96\n"
  "v_mfma_f32_16x16x32_bf16 v[4:7], v[56:59], a[28:31], v[4:7]\n"
  "ds_read_b128 v[72:75], v105 offset:4096\n"
  "v_fma_f32 v25, v25, v85, v97\n"
  "v_mfma_f32_16x16x32_bf16 v[4:7], v[48:51], a[4:7], v[4:7]\n"
  "v_fma_f32 v28, v28, v84, v96\n"
  "v_fma_f32 v29, v29, v85, v97\n"
  "v_mfma_f32_16x16x32_bf16 v[8:11], v[48:51], a[8:11], v[100:103]\n"
  "v_fma_f32 v32, v32, v84, v96\n"
  "v_fma_f32 v33, v33, v85, v97\n"
  "v_mfma_f32_16x16x32_bf16 v[8:11], v[56:59], a[32:35], v[8:11]\n"
  "v_fma_f32 v36, v36, v84, v96\n"
  "v_fma_f32 v37, v37, v85, v97\n"
  "v_mfma_f32_16x16x32_bf16 v[8:11], v[52:55], a[56:59], v[8:11]\n"
  "ds_read_b128 v[76:79], v105 offset:6144\n"
  "v_fma_f32 v26, v26, v86, v98\n"
  "v_mfma_f32_16x16x32_bf16 v[8:11], v[60:63], a[80:83], v[8:11]\n"
  "v_fma_f32 v27, v27, v87, v99\n"
  "v_fma_f32 v30, v30, v86, v98\n"
  "v_smfmac_f32_16x16x64_bf16 v[80:83], v[88:91], v[48:55], v124\n"
  "s_waitcnt lgkmcnt(4)\n"
  "v_fma_f32 v31, v31, v87, v99\n"
  "v_smfmac_f32_16x16x64_bf16 v[80:83], v[88:91], v[64:71], v124\n"
  "s_waitcnt lgkmcnt(2)\n"
  "buffer_store_dwordx4 v[92:95], v109, s[48:51], s68 offen offset:0 nt\n"
  "ds_write2_b32 v107, v40, v41 offset0:0 offset1:72\n"
  "v_mfma_f32_16x16x32_bf16 v[12:15], v[60:63], a[84:87], v[100:103]\n"
  "v_fma_f32 v34, v34, v86, v98\n"
  "v_fma_f32 v35, v35, v87, v99\n"
  "v_mfma_f32_16x16x32_bf16 v[12:15], v[52:55], a[60:63], v[12:15]\n"
  "v_fma_f32 v38, v38, v86, v98\n"
  "v_fma_f32 v39, v39, v87, v99\n"
  "v_mfma_f32_16x16x32_bf16 v[12:15], v[56:59], a[36:39], v[12:15]\n"
  "v_cvt_pk_bf16_f32 v24, v24, v28\n"
  "v_cvt_pk_bf16_f32 v28, v25, v29\n"
  "v_mfma_f32_16x16x32_bf16 v[12:15], v[48:51], a[12:15], v[12:15]\n"
  "ds_write2_b32 v107, v42, v43 offset0:144 offset1:216\n"
  "v_mfma_f32_16x16x32_bf16 v[16:19], v[48:51], a[16:19], v[100:103]\n"
  "v_cvt_pk_bf16_f32 v25, v32, v36\n"
  "v_cvt_pk_bf16_f32 v29, v33, v37\n"
  "v_mfma_f32_16x16x32_bf16 v[16:19], v[56:59], a[40:43], v[16:19]\n"
  "ds_read_b64_tr_b16 v[96:97], v108 offset:2304\n"
  "ds_read_b64_tr_b16 v[98:99], v108 offset:2336\n"
  "v_mfma_f32_16x16x32_bf16 v[16:19], v[52:55], a[64:67], v[16:19]\n"
  "v_cvt_pk_bf16_f32 v26, v26, v30\n"
  "v_cvt_pk_bf16_f32 v30, v27, v31\n"
  "v_mfma_f32_16x16x32_bf16 v[16:19], v[60:63], a[88:91], v[16:19]\n"
  "v_cvt_pk_bf16_f32 v27, v34, v38\n"
  "v_cvt_pk_bf16_f32 v31, v35, v39\n"
  "v_mfma_f32_16x16x32_bf16 v[20:23], v[60:63], a[92:95], v[100:103]\n"
  "v_mfma_f32_16x16x32_bf16 v[20:23], v[52:55], a[68:71], v[20:23]\n"
  "v_mfma_f32_16x16x32_bf16 v[20:23], v[56:59], a[44:47], v[20:23]\n"
  "v_mfma_f32_16x16x32_bf16 v[20:23], v[48:51], a[20:23], v[20:23]\n"
  "s_waitcnt lgkmcnt(4)\n"
  "v_mfma_f32_16x16x32_bf16 v[20:23], v[64:67], a[116:119], v[20:23]\n"
  "s_add_u32 s66, s66, 6144\n"
  "ds_read_b128 v[48:51], v104 offset:8192\n"
  "v_mfma_f32_16x16x32_bf16 v[20:23], v[72:75], a[140:143], v[20:23]\n"
  "s_cmp_ge_u32 s66, 102400\n"
  "s_cselect_b32 s64, 36864, 0\n"
  "v_mfma_f32_16x16x32_bf16 v[20:23], v[68:71], a[164:167], v[20:23]\n"
  "s_sub_u32 s66, s66, s64\n"
  "ds_read_b128 v[52:55], v104 offset:10240\n"
  "v_mfma_f32_16x16x32_bf16 v[20:23], v[76:79], a[188:191], v[20:23]\n"
  "s_sub_u32 s90, s90, 1\n"
  "s_cselect_b32 s63, s91, 6144\n"
  "s_cmov_b64 s[90:91], s[92:93]\n"
  "s_cmov_b64 s[92:93], s[94:95]\n"
  "s_waitcnt lgkmcnt(2)\n"
  "buffer_store_dwordx4 v[96:99], v109, s[48:51], s68 offen offset:128 nt\n"
  "ds_write_b64 v106, v[24:25] offset:0\n"
  "v_mfma_f32_16x16x32_bf16 v[16:19], v[76:79], a[184:187], v[16:19]\n"
  "s_add_u32 s67, s67, s63\n"
  "v_mfma_f32_16x16x32_bf16 v[16:19], v[68:71], a[160:163], v[16:19]\n"
  "ds_read_b64_tr_b16 v[92:93], v108 offset:4608\n"
  "ds_read_b64_tr_b16 v[94:95], v108 offset:4640\n"
  "v_mfma_f32_16x16x32_bf16 v[16:19], v[72:75], a[136:139], v[16:19]\n"
  "ds_read_b128 v[56:59], v105 offset:8192\n"
  "v_xor_b32 v108, 0x2000, v108\n"
  "v_mfma_f32_16x16x32_bf16 v[16:19], v[64:67], a[112:115], v[16:19]\n"
  "v_mfma_f32_16x16x32_bf16 v[12:15], v[64:67], a[108:111], v[12:15]\n"
  "v_mfma_f32_16x16x32_bf16 v[12:15], v[72:75], a[132:135], v[12:15]\n"
  "v_mfma_f32_16x16x32_bf16 v[12:15], v[68:71], a[156:159], v[12:15]\n"
  "ds_read_b128 v[60:63], v105 offset:10240\n"
  "v_mfma_f32_16x16x32_bf16 v[12:15], v[76:79], a[180:183], v[12:15]\n"
  "v_mfma_f32_16x16x32_bf16 v[8:11], v[76:79], a[176:179], v[8:11]\n"
  "v_mfma_f32_16x16x32_bf16 v[8:11], v[68:71], a[152:155], v[8:11]\n"
  "v_mfma_f32_16x16x32_bf16 v[8:11], v[72:75], a[128:131], v[8:11]\n"
  "v_mfma_f32_16x16x32_bf16 v[8:11], v[64:67], a[104:107], v[8:11]\n"
  "s_waitcnt lgkmcnt(2)\n"
  "buffer_store_dwordx4 v[92:95], v109, s[48:51], s68 offen offset:256 nt\n"
  "ds_write_b64 v106, v[28:29] offset:288\n"
  "v_mfma_f32_16x16x32_bf16 v[4:7], v[64:67], a[100:103], v[4:7]\n"
  "s_add_u32 s68, s68, s69\n"
  "v_mfma_f32_16x16x32_bf16 v[4:7], v[72:75], a[124:127], v[4:7]\n"
  "s_mov_b32 s69, s70\n"
  "v_mfma_f32_16x16x32_bf16 v[4:7], v[68:71], a[148:151], v[4:7]\n"
  "s_movk_i32 s70, 6144\n"
  "v_mfma_f32_16x16x32_bf16 v[4:7], v[76:79], a[172:175], v[4:7]\n"
  "v_mfma_f32_16x16x32_bf16 v[0:3], v[76:79], a[168:171], v[0:3]\n"
  "v_mfma_f32_16x16x32_bf16 v[0:3], v[68:71], a[144:147], v[0:3]\n"
  "v_mfma_f32_16x16x32_bf16 v[0:3], v[72:75], a[120:123], v[0:3]\n"
  "v_mfma_f32_16x16x32_bf16 v[0:3], v[64:67], a[96:99], v[0:3]\n"
  "s_waitcnt lgkmcnt(1)\n"
  "v_mfma_f32_16x16x32_bf16 v[0:3], v[48:51], a[192:195], v[0:3]\n"
  "ds_read_b128 v[64:67], v104 offset:12288\n"
  "v_mfma_f32_16x16x32_bf16 v[0:3], v[56:59], a[216:219], v[0:3]\n"
  "v_mfma_f32_16x16x32_bf16 v[0:3], v[52:55], a[240:243], v[0:3]\n"
  "ds_read_b128 v[68:71], v104 offset:14336\n"
  "v_xor_b32 v104, s88, v104\n"
  "v_mfma_f32_16x16x32_bf16 v[0:3], v[60:63], v[136:139], v[0:3]\n"
  "ds_write_b64 v106, v[26:27] offset:576\n"
  "v_mfma_f32_16x16x32_bf16 v[4:7], v[60:63], v[140:143], v[4:7]\n"
  "v_mfma_f32_16x16x32_bf16 v[4:7], v[52:55], a[244:247], v[4:7]\n"
  "v_mfma_f32_16x16x32_bf16 v[4:7], v[56:59], a[220:223], v[4:7]\n"
  "ds_read_b128 v[72:75], v105 offset:12288\n"
  "v_mfma_f32_16x16x32_bf16 v[4:7], v[48:51], a[196:199], v[4:7]\n"
  "ds_write_b64 v106, v[30:31] offset:864\n"
  "v_mfma_f32_16x16x32_bf16 v[8:11], v[48:51], a[200:203], v[8:11]\n"
  "v_mfma_f32_16x16x32_bf16 v[8:11], v[56:59], a[224:227], v[8:11]\n"
  "v_mfma_f32_16x16x32_bf16 v[8:11], v[52:55], a[248:251], v[8:11]\n"
  "ds_read_b128 v[76:79], v105 offset:14336\n"
  "v_xor_b32 v105, s88, v105\n"
  "v_mfma_f32_16x16x32_bf16 v[8:11], v[60:63], v[144:147], v[8:11]\n"
  "s_xor_b32 s88, s88, s89\n"
  "v_smfmac_f32_16x16x64_bf16 v[80:83], v[88:91], v[48:55], v124\n"
  "s_waitcnt lgkmcnt(4)\n"
  "v_smfmac_f32_16x16x64_bf16 v[80:83], v[88:91], v[64:71], v124\n"
  "v_mfma_f32_16x16x32_bf16 v[12:15], v[60:63], v[148:151], v[12:15]\n"
  "v_mfma_f32_16x16x32_bf16 v[12:15], v[52:55], a[252:255], v[12:15]\n"
  "v_mfma_f32_16x16x32_bf16 v[12:15], v[56:59], a[228:231], v[12:15]\n"
  "v_add_f32 v44, v80, v81\n"
  "v_mov_b32 v80, 0\n"
  "v_mfma_f32_16x16x32_bf16 v[12:15], v[48:51], a[204:207], v[12:15]\n"
  "ds_write_b32 v110, v44\n"
  "v_mfma_f32_16x16x32_bf16 v[16:19], v[48:51], a[208:211], v[16:19]\n"
  "v_mov_b32 v81, 0\n"
  "v_mfma_f32_16x16x32_bf16 v[16:19], v[56:59], a[232:235], v[16:19]\n"
  "v_mfma_f32_16x16x32_bf16 v[16:19], v[52:55], v[128:131], v[16:19]\n"
  "v_mfma_f32_16x16x32_bf16 v[16:19], v[60:63], v[152:155], v[16:19]\n"
  "v_xor_b32 v106, 0x2000, v106\n"
  "v_xor_b32 v107, 0x2000, v107\n"
  "v_mfma_f32_16x16x32_bf16 v[20:23], v[60:63], v[156:159], v[20:23]\n"
  "v_xor_b32 v110, 0x2000, v110\n"
  "v_mfma_f32_16x16x32_bf16 v[20:23], v[52:55], v[132:135], v[20:23]\n"
  "v_mfma_f32_16x16x32_bf16 v[20:23], v[56:59], a[236:239], v[20:23]\n"
  "v_mfma_f32_16x16x32_bf16 v[20:23], v[48:51], a[212:215], v[20:23]\n"
  "s_waitcnt lgkmcnt(0)\n"
  "s_barrier\n"
  "v_mfma_f32_16x16x32_bf16 v[20:23], v[64:67], v[180:183], v[20:23]\n"
  "s_add_u32 m0, s66, 0\n"
  "ds_read_b128 v[48:51], v104 offset:0\n"
  "ds_read_b128 v[84:87], v111\n"
  "v_mfma_f32_16x16x32_bf16 v[20:23], v[72:75], v[204:207], v[20:23]\n"
  "s_xor_b32 s89, s88, s89\n"
  "ds_read_b128 v[96:99], v111 offset:64\n"
  "v_xor_b32 v111, 0x2000, v111\n"
  "v_mfma_f32_16x16x32_bf16 v[20:23], v[68:71], v[228:231], v[20:23]\n"
  "ds_read_b128 v[52:55], v104 offset:2048\n"
  "v_mfma_f32_16x16x32_bf16 v[44:47], v[76:79], v[252:255], v[20:23]\n"
  "buffer_load_dwordx4 v112, s[44:47], s67 offen offset:0 nt lds\n"
  "v_mfma_f32_16x16x32_bf16 v[16:19], v[76:79], v[248:251], v[16:19]\n"
  "s_add_u32 m0, s66, 1920\n"
  "v_mfma_f32_16x16x32_bf16 v[16:19], v[68:71], v[224:227], v[16:19]\n"
  "v_mfma_f32_16x16x32_bf16 v[16:19], v[72:75], v[200:203], v[16:19]\n"
  "ds_read_b128 v[56:59], v105 offset:0\n"
  "v_mfma_f32_16x16x32_bf16 v[40:43], v[64:67], v[176:179], v[16:19]\n"
  "buffer_load_dwordx4 v112, s[44:47], s67 offen offset:128 nt lds\n"
  "v_mfma_f32_16x16x32_bf16 v[12:15], v[64:67], v[172:175], v[12:15]\n"
  "s_add_u32 m0, s66, 3840\n"
  "v_mfma_f32_16x16x32_bf16 v[12:15], v[72:75], v[196:199], v[12:15]\n"
  "v_mfma_f32_16x16x32_bf16 v[12:15], v[68:71], v[220:223], v[12:15]\n"
  "s_waitcnt lgkmcnt(2)\n"
  "ds_read_b128 v[60:63], v105 offset:2048\n"
  "v_add_f32 v84, v84, v96\n"
  "v_mfma_f32_16x16x32_bf16 v[36:39], v[76:79], v[244:247], v[12:15]\n"
  "buffer_load_dwordx4 v112, s[44:47], s67 offen offset:256 nt lds\n"
  "v_mfma_f32_16x16x32_bf16 v[8:11], v[76:79], v[240:243], v[8:11]\n"
  "v_add_f32 v85, v85, v97\n"
  "v_add_f32 v86, v86, v98\n"
  "v_mfma_f32_16x16x32_bf16 v[8:11], v[68:71], v[216:219], v[8:11]\n"
  "v_add_f32 v87, v87, v99\n"
  "v_max3_f32 v82, v82, v84, v85\n"
  "v_mfma_f32_16x16x32_bf16 v[8:11], v[72:75], v[192:195], v[8:11]\n"
  "v_rcp_f32 v84, v84\n"
  "v_mfma_f32_16x16x32_bf16 v[32:35], v[64:67], v[168:171], v[8:11]\n"
  "v_rcp_f32 v85, v85\n"
  "v_mfma_f32_16x16x32_bf16 v[4:7], v[64:67], v[164:167], v[4:7]\n"
  "v_max3_f32 v82, v82, v86, v87\n"
  "v_max3_f32 v83, v83, v84, v85\n"
  "v_mfma_f32_16x16x32_bf16 v[4:7], v[72:75], v[188:191], v[4:7]\n"
  "v_rcp_f32 v86, v86\n"
  "v_mfma_f32_16x16x32_bf16 v[4:7], v[68:71], v[212:215], v[4:7]\n"
  "v_rcp_f32 v87, v87\n"
  "v_mfma_f32_16x16x32_bf16 v[28:31], v[76:79], v[236:239], v[4:7]\n"
  "v_max3_f32 v83, v83, v86, v87\n"
  "v_mul_f32 v96, s99, v84\n"
  "v_mfma_f32_16x16x32_bf16 v[0:3], v[76:79], v[232:235], v[0:3]\n"
  "v_mul_f32 v97, s99, v85\n"
  "v_fma_f32 v40, v40, v84, v96\n"
  "v_mfma_f32_16x16x32_bf16 v[0:3], v[68:71], v[208:211], v[0:3]\n"
  "v_fma_f32 v44, v44, v84, v96\n"
  "v_cvt_pk_bf16_f32 v40, v40, v44\n"
  "v_mfma_f32_16x16x32_bf16 v[0:3], v[72:75], v[184:187], v[0:3]\n"
  "v_fma_f32 v41, v41, v85, v97\n"
  "v_fma_f32 v45, v45, v85, v97\n"
  "v_mfma_f32_16x16x32_bf16 v[24:27], v[64:67], v[160:163], v[0:3]\n"
  "s_waitcnt lgkmcnt(0)\n"
  "v_cvt_pk_bf16_f32 v41, v41, v45\n"
  "v_mul_f32 v98, s99, v86\n"
  "v_mfma_f32_16x16x32_bf16 v[0:3], v[48:51], a[0:3], v[100:103]\n"
  "ds_read_b128 v[64:67], v104 offset:4096\n"
  "v_mul_f32 v99, s99, v87\n"
  "v_mfma_f32_16x16x32_bf16 v[0:3], v[56:59], a[24:27], v[0:3]\n"
  "v_fma_f32 v42, v42, v86, v98\n"
  "v_fma_f32 v46, v46, v86, v98\n"
  "v_mfma_f32_16x16x32_bf16 v[0:3], v[52:55], a[48:51], v[0:3]\n"
  "ds_read_b128 v[68:71], v104 offset:6144\n"
  "v_cvt_pk_bf16_f32 v42, v42, v46\n"
  "v_mfma_f32_16x16x32_bf16 v[0:3], v[60:63], a[72:75], v[0:3]\n"
  "ds_read_b64_tr_b16 v[92:93], v108 offset:0\n"
  "ds_read_b64_tr_b16 v[94:95], v108 offset:32\n"
  "v_mfma_f32_16x16x32_bf16 v[4:7], v[60:63], a[76:79], v[100:103]\n"
  "v_fma_f32 v43, v43, v87, v99\n"
  "v_fma_f32 v47, v47, v87, v99\n"
  "v_mfma_f32_16x16x32_bf16 v[4:7], v[52:55], a[52:55], v[4:7]\n"
  "v_cvt_pk_bf16_f32 v43, v43, v47\n"
  "v_fma_f32 v24, v24, v84, v96\n"
  "v_mfma_f32_16x16x32_bf16 v[4:7], v[56:59], a[28:31], v[4:7]\n"
  "ds_read_b128 v[72:75], v105 offset:4096\n"
  "v_fma_f32 v25, v25, v85, v97\n"
  "v_mfma_f32_16x16x32_bf16 v[4:7], v[48:51], a[4:7], v[4:7]\n"
  "v_fma_f32 v28, v28, v84, v96\n"
  "v_fma_f32 v29, v29, v85, v97\n"
  "v_mfma_f32_16x16x32_bf16 v[8:11], v[48:51], a[8:11], v[100:103]\n"
  "v_fma_f32 v32, v32, v84, v96\n"
  "v_fma_f32 v33, v33, v85, v97\n"
  "v_mfma_f32_16x16x32_bf16 v[8:11], v[56:59], a[32:35], v[8:11]\n"
  "v_fma_f32 v36, v36, v84, v96\n"
  "v_fma_f32 v37, v37, v85, v97\n"
  "v_mfma_f32_16x16x32_bf16 v[8:11], v[52:55], a[56:59], v[8:11]\n"
  "ds_read_b128 v[76:79], v105 offset:6144\n"
  "v_fma_f32 v26, v26, v86, v98\n"
  "v_mfma_f32_16x16x32_bf16 v[8:11], v[60:63], a[80:83], v[8:11]\n"
  "v_fma_f32 v27, v27, v87, v99\n"
  "v_fma_f32 v30, v30, v86, v98\n"
  "v_smfmac_f32_16x16x64_bf16 v[80:83], v[88:91], v[48:55], v124\n"
  "s_waitcnt lgkmcnt(4)\n"
  "v_fma_f32 v31, v31, v87, v99\n"
  "v_smfmac_f32_16x16x64_bf16 v[80:83], v[88:91], v[64:71], v124\n"
  "s_waitcnt lgkmcnt(2)\n"
  "buffer_store_dwordx4 v[92:95], v109, s[48:51], s68 offen offset:0 nt\n"
  "ds_write2_b32 v107, v40, v41 offset0:0 offset1:72\n"
  "v_mfma_f32_16x16x32_bf16 v[12:15], v[60:63], a[84:87], v[100:103]\n"
  "v_fma_f32 v34, v34, v86, v98\n"
  "v_fma_f32 v35, v35, v87, v99\n"
  "v_mfma_f32_16x16x32_bf16 v[12:15], v[52:55], a[60:63], v[12:15]\n"
  "v_fma_f32 v38, v38, v86, v98\n"
  "v_fma_f32 v39, v39, v87, v99\n"
  "v_mfma_f32_16x16x32_bf16 v[12:15], v[56:59], a[36:39], v[12:15]\n"
  "v_cvt_pk_bf16_f32 v24, v24, v28\n"
  "v_cvt_pk_bf16_f32 v28, v25, v29\n"
  "v_mfma_f32_16x16x32_bf16 v[12:15], v[48:51], a[12:15], v[12:15]\n"
  "ds_write2_b32 v107, v42, v43 offset0:144 offset1:216\n"
  "v_mfma_f32_16x16x32_bf16 v[16:19], v[48:51], a[16:19], v[100:103]\n"
  "v_cvt_pk_bf16_f32 v25, v32, v36\n"
  "v_cvt_pk_bf16_f32 v29, v33, v37\n"
  "v_mfma_f32_16x16x32_bf16 v[16:19], v[56:59], a[40:43], v[16:19]\n"
  "ds_read_b64_tr_b16 v[96:97], v108 offset:2304\n"
  "ds_read_b64_tr_b16 v[98:99], v108 offset:2336\n"
  "v_mfma_f32_16x16x32_bf16 v[16:19], v[52:55], a[64:67], v[16:19]\n"
  "v_cvt_pk_bf16_f32 v26, v26, v30\n"
  "v_cvt_pk_bf16_f32 v30, v27, v31\n"
  "v_mfma_f32_16x16x32_bf16 v[16:19], v[60:63], a[88:91], v[16:19]\n"
  "v_cvt_pk_bf16_f32 v27, v34, v38\n"
  "v_cvt_pk_bf16_f32 v31, v35, v39\n"
  "v_mfma_f32_16x16x32_bf16 v[20:23], v[60:63], a[92:95], v[100:103]\n"
  "v_mfma_f32_16x16x32_bf16 v[20:23], v[52:55], a[68:71], v[20:23]\n"
  "v_mfma_f32_16x16x32_bf16 v[20:23], v[56:59], a[44:47], v[20:23]\n"
  "v_mfma_f32_16x16x32_bf16 v[20:23], v[48:51], a[20:23], v[20:23]\n"
  "s_waitcnt lgkmcnt(4)\n"
  "v_mfma_f32_16x16x32_bf16 v[20:23], v[64:67], a[116:119], v[20:23]\n"
  "s_add_u32 s66, s66, 6144\n"
  "ds_read_b128 v[48:51], v104 offset:8192\n"
  "v_mfma_f32_16x16x32_bf16 v[20:23], v[72:75], a[140:143], v[20:23]\n"
  "s_cmp_ge_u32 s66, 102400\n"
  "s_cselect_b32 s64, 36864, 0\n"
  "v_mfma_f32_16x16x32_bf16 v[20:23], v[68:71], a[164:167], v[20:23]\n"
  "s_sub_u32 s66, s66, s64\n"
  "ds_read_b128 v[52:55], v104 offset:10240\n"
  "v_mfma_f32_16x16x32_bf16 v[20:23], v[76:79], a[188:191], v[20:23]\n"
  "s_sub_u32 s90, s90, 1\n"
  "s_cselect_b32 s63, s91, 6144\n"
  "s_cmov_b64 s[90:91], s[92:93]\n"
  "s_cmov_b64 s[92:93], s[94:95]\n"
  "s_waitcnt lgkmcnt(2)\n"
  "buffer_store_dwordx4 v[96:99], v109, s[48:51], s68 offen offset:128 nt\n"
  "ds_write_b64 v106, v[24:25] offset:0\n"
  "v_mfma_f32_16x16x32_bf16 v[16:19], v[76:79], a[184:187], v[16:19]\n"
  "s_add_u32 s67, s67, s63\n"
  "v_mfma_f32_16x16x32_bf16 v[16:19], v[68:71], a[160:163], v[16:19]\n"
  "ds_read_b64_tr_b16 v[92:93], v108 offset:4608\n"
  "ds_read_b64_tr_b16 v[94:95], v108 offset:4640\n"
  "v_mfma_f32_16x16x32_bf16 v[16:19], v[72:75], a[136:139], v[16:19]\n"
  "ds_read_b128 v[56:59], v105 offset:8192\n"
  "v_xor_b32 v108, 0x2000, v108\n"
  "v_mfma_f32_16x16x32_bf16 v[16:19], v[64:67], a[112:115], v[16:19]\n"
  "v_mfma_f32_16x16x32_bf16 v[12:15], v[64:67], a[108:111], v[12:15]\n"
  "v_mfma_f32_16x16x32_bf16 v[12:15], v[72:75], a[132:135], v[12:15]\n"
  "v_mfma_f32_16x16x32_bf16 v[12:15], v[68:71], a[156:159], v[12:15]\n"
  "ds_read_b128 v[60:63], v105 offset:10240\n"
  "v_mfma_f32_16x16x32_bf16 v[12:15], v[76:79], a[180:183], v[12:15]\n"
  "v_mfma_f32_16x16x32_bf16 v[8:11], v[76:79], a[176:179], v[8:11]\n"
  "v_mfma_f32_16x16x32_bf16 v[8:11], v[68:71], a[152:155], v[8:11]\n"
  "v_mfma_f32_16x16x32_bf16 v[8:11], v[72:75], a[128:131], v[8:11]\n"
  "v_mfma_f32_16x16x32_bf16 v[8:11], v[64:67], a[104:107], v[8:11]\n"
  "s_waitcnt lgkmcnt(2)\n"
  "buffer_store_dwordx4 v[92:95], v109, s[48:51], s68 offen offset:256 nt\n"
  "ds_write_b64 v106, v[28:29] offset:288\n"
  "v_mfma_f32_16x16x32_bf16 v[4:7], v[64:67], a[100:103], v[4:7]\n"
  "s_add_u32 s68, s68, s69\n"
  "v_mfma_f32_16x16x32_bf16 v[4:7], v[72:75], a[124:127], v[4:7]\n"
  "s_mov_b32 s69, s70\n"
  "v_mfma_f32_16x16x32_bf16 v[4:7], v[68:71], a[148:151], v[4:7]\n"
  "s_movk_i32 s70, 6144\n"
  "v_mfma_f32_16x16x32_bf16 v[4:7], v[76:79], a[172:175], v[4:7]\n"
  "v_mfma_f32_16x16x32_bf16 v[0:3], v[76:79], a[168:171], v[0:3]\n"
  "v_mfma_f32_16x16x32_bf16 v[0:3], v[68:71], a[144:147], v[0:3]\n"
  "v_mfma_f32_16x16x32_bf16 v[0:3], v[72:75], a[120:123], v[0:3]\n"
  "v_mfma_f32_16x16x32_bf16 v[0:3], v[64:67], a[96:99], v[0:3]\n"
  "s_waitcnt lgkmcnt(1)\n"
  "v_mfma_f32_16x16x32_bf16 v[0:3], v[48:51], a[192:195], v[0:3]\n"
  "ds_read_b128 v[64:67], v104 offset:12288\n"
  "v_mfma_f32_16x16x32_bf16 v[0:3], v[56:59], a[216:219], v[0:3]\n"
  "v_mfma_f32_16x16x32_bf16 v[0:3], v[52:55], a[240:243], v[0:3]\n"
  "ds_read_b128 v[68:71], v104 offset:14336\n"
  "v_xor_b32 v104, s88, v104\n"
  "v_mfma_f32_16x16x32_bf16 v[0:3], v[60:63], v[136:139], v[0:3]\n"
  "ds_write_b64 v106, v[26:27] offset:576\n"
  "v_mfma_f32_16x16x32_bf16 v[4:7], v[60:63], v[140:143], v[4:7]\n"
  "v_mfma_f32_16x16x32_bf16 v[4:7], v[52:55], a[244:247], v[4:7]\n"
  "v_mfma_f32_16x16x32_bf16 v[4:7], v[56:59], a[220:223], v[4:7]\n"
  "ds_read_b128 v[72:75], v105 offset:12288\n"
  "v_mfma_f32_16x16x32_bf16 v[4:7], v[48:51], a[196:199], v[4:7]\n"
  "ds_write_b64 v106, v[30:31] offset:864\n"
  "v_mfma_f32_16x16x32_bf16 v[8:11], v[48:51], a[200:203], v[8:11]\n"
  "v_mfma_f32_16x16x32_bf16 v[8:11], v[56:59], a[224:227], v[8:11]\n"
  "v_mfma_f32_16x16x32_bf16 v[8:11], v[52:55], a[248:251], v[8:11]\n"
  "ds_read_b128 v[76:79], v105 offset:14336\n"
  "v_xor_b32 v105, s88, v105\n"
  "v_mfma_f32_16x16x32_bf16 v[8:11], v[60:63], v[144:147], v[8:11]\n"
  "s_xor_b32 s88, s88, s89\n"
  "v_smfmac_f32_16x16x64_bf16 v[80:83], v[88:91], v[48:55], v124\n"
  "s_waitcnt lgkmcnt(4)\n"
  "v_smfmac_f32_16x16x64_bf16 v[80:83], v[88:91], v[64:71], v124\n"
  "v_mfma_f32_16x16x32_bf16 v[12:15], v[60:63], v[148:151], v[12:15]\n"
  "v_mfma_f32_16x16x32_bf16 v[12:15], v[52:55], a[252:255], v[12:15]\n"
  "v_mfma_f32_16x16x32_bf16 v[12:15], v[56:59], a[228:231], v[12:15]\n"
  "v_add_f32 v44, v80, v81\n"
  "v_mov_b32 v80, 0\n"
  "v_mfma_f32_16x16x32_bf16 v[12:15], v[48:51], a[204:207], v[12:15]\n"
  "ds_write_b32 v110, v44\n"
  "v_mfma_f32_16x16x32_bf16 v[16:19], v[48:51], a[208:211], v[16:19]\n"
  "v_mov_b32 v81, 0\n"
  "v_mfma_f32_16x16x32_bf16 v[16:19], v[56:59], a[232:235], v[16:19]\n"
  "v_mfma_f32_16x16x32_bf16 v[16:19], v[52:55], v[128:131], v[16:19]\n"
  "v_mfma_f32_16x16x32_bf16 v[16:19], v[60:63], v[152:155], v[16:19]\n"
  "v_xor_b32 v106, 0x2000, v106\n"
  "v_xor_b32 v107, 0x2000, v107\n"
  "v_mfma_f32_16x16x32_bf16 v[20:23], v[60:63], v[156:159], v[20:23]\n"
  "v_xor_b32 v110, 0x2000, v110\n"
  "v_mfma_f32_16x16x32_bf16 v[20:23], v[52:55], v[132:135], v[20:23]\n"
  "v_mfma_f32_16x16x32_bf16 v[20:23], v[56:59], a[236:239], v[20:23]\n"
  "v_mfma_f32_16x16x32_bf16 v[20:23], v[48:51], a[212:215], v[20:23]\n"
  "s_waitcnt lgkmcnt(0)\n"
  "s_barrier\n"
  "v_mfma_f32_16x16x32_bf16 v[20:23], v[64:67], v[180:183], v[20:23]\n"
  "s_xor_b32 s89, s88, s89\n"
  "ds_read_b128 v[48:51], v104 offset:0\n"
  "ds_read_b128 v[84:87], v111\n"
  "ds_read_b128 v[96:99], v111 offset:64\n"
  "v_xor_b32 v111, 0x2000, v111\n"
  "v_mfma_f32_16x16x32_bf16 v[20:23], v[72:75], v[204:207], v[20:23]\n"
  "s_add_u32 m0, s59, 0\n"
  "v_mfma_f32_16x16x32_bf16 v[20:23], v[68:71], v[228:231], v[20:23]\n"
  "buffer_load_dwordx4 v114, s[40:43], s57 offen offset:0 nt lds\n"
  "buffer_load_dwordx4 v115, s[40:43], s57 offen offset:1024 nt lds\n"
  "ds_read_b128 v[52:55], v104 offset:2048\n"
  "v_mfma_f32_16x16x32_bf16 v[44:47], v[76:79], v[252:255], v[20:23]\n"
  "v_mfma_f32_16x16x32_bf16 v[16:19], v[76:79], v[248:251], v[16:19]\n"
  "buffer_load_dwordx4 v116, s[40:43], s57 offen offset:2048  lds\n"
  "v_mfma_f32_16x16x32_bf16 v[16:19], v[68:71], v[224:227], v[16:19]\n"
  "s_add_u32 s57, s57, s84\n"
  "v_mfma_f32_16x16x32_bf16 v[16:19], v[72:75], v[200:203], v[16:19]\n"
  "ds_read_b128 v[56:59], v105 offset:0\n"
  "v_mfma_f32_16x16x32_bf16 v[40:43], v[64:67], v[176:179], v[16:19]\n"
  "s_add_u32 m0, s66, 0\n"
  "s_nop 0\n"
  "buffer_load_dwordx4 v112, s[44:47], s67 offen offset:0 nt lds\n"
  "s_add_u32 m0, s59, 3072\n"
  "v_mfma_f32_16x16x32_bf16 v[12:15], v[64:67], v[172:175], v[12:15]\n"
  "buffer_load_dwordx4 v114, s[40:43], s57 offen offset:0 nt lds\n"
  "buffer_load_dwordx4 v115, s[40:43], s57 offen offset:1024 nt lds\n"
  "v_mfma_f32_16x16x32_bf16 v[12:15], v[72:75], v[196:199], v[12:15]\n"
  "v_mfma_f32_16x16x32_bf16 v[12:15], v[68:71], v[220:223], v[12:15]\n"
  "s_waitcnt lgkmcnt(2)\n"
  "buffer_load_dwordx4 v116, s[40:43], s57 offen offset:2048  lds\n"
  "ds_read_b128 v[60:63], v105 offset:2048\n"
  "v_add_f32 v84, v84, v96\n"
  "v_mfma_f32_16x16x32_bf16 v[36:39], v[76:79], v[244:247], v[12:15]\n"
  "s_add_u32 s57, s57, 6144\n"
  "v_add_f32 v85, v85, v97\n"
  "v_add_f32 v86, v86, v98\n"
  "v_add_f32 v87, v87, v99\n"
  "v_max3_f32 v82, v82, v84, v85\n"
  "v_mfma_f32_16x16x32_bf16 v[8:11], v[76:79], v[240:243], v[8:11]\n"
  "v_rcp_f32 v84, v84\n"
  "v_rcp_f32 v85, v85\n"
  "v_mfma_f32_16x16x32_bf16 v[8:11], v[68:71], v[216:219], v[8:11]\n"
  "s_add_u32 m0, s66, 1920\n"
  "s_nop 0\n"
  "buffer_load_dwordx4 v112, s[44:47], s67 offen offset:128 nt lds\n"
  "s_add_u32 m0, s59, 6144\n"
  "v_max3_f32 v82, v82, v86, v87\n"
  "v_max3_f32 v83, v83, v84, v85\n"
  "v_mfma_f32_16x16x32_bf16 v[8:11], v[72:75], v[192:195], v[8:11]\n"
  "buffer_load_dwordx4 v114, s[40:43], s57 offen offset:0 nt lds\n"
  "buffer_load_dwordx4 v115, s[40:43], s57 offen offset:1024 nt lds\n"
  "v_mfma_f32_16x16x32_bf16 v[32:35], v[64:67], v[168:171], v[8:11]\n"
  "v_rcp_f32 v86, v86\n"
  "v_rcp_f32 v87, v87\n"
  "v_mfma_f32_16x16x32_bf16 v[4:7], v[64:67], v[164:167], v[4:7]\n"
  "buffer_load_dwordx4 v116, s[40:43], s57 offen offset:2048  lds\n"
  "v_max3_f32 v83, v83, v86, v87\n"
  "v_mul_f32 v96, s99, v84\n"
  "v_mfma_f32_16x16x32_bf16 v[4:7], v[72:75], v[188:191], v[4:7]\n"
  "s_add_u32 s57, s57, s58\n"
  "v_mul_f32 v97, s99, v85\n"
  "v_mul_f32 v98, s99, v86\n"
  "v_mul_f32 v99, s99, v87\n"
  "v_fma_f32 v36, v36, v84, v96\n"
  "v_mfma_f32_16x16x32_bf16 v[4:7], v[68:71], v[212:215], v[4:7]\n"
  "v_fma_f32 v32, v32, v84, v96\n"
  "v_fma_f32 v33, v33, v85, v97\n"
  "v_fma_f32 v37, v37, v85, v97\n"
  "v_fma_f32 v34, v34, v86, v98\n"
  "v_mfma_f32_16x16x32_bf16 v[28:31], v[76:79], v[236:239], v[4:7]\n"
  "s_add_u32 m0, s66, 3840\n"
  "s_nop 0\n"
  "buffer_load_dwordx4 v112, s[44:47], s67 offen offset:256 nt lds\n"
  "s_add_u32 m0, s59, 9216\n"
  "v_fma_f32 v35, v35, v87, v99\n"
  "v_fma_f32 v38, v38, v86, v98\n"
  "v_mfma_f32_16x16x32_bf16 v[0:3], v[76:79], v[232:235], v[0:3]\n"
  "s_add_u32 s66, s66, 6144\n"
  "s_cmp_ge_u32 s66, 102400\n"
  "s_cselect_b32 s64, 36864, 0\n"
  "s_sub_u32 s66, s66, s64\n"
  "s_sub_u32 s90, s90, 1\n"
  "s_cselect_b32 s63, s91, 6144\n"
  "s_cmov_b64 s[90:91], s[92:93]\n"
  "s_cmov_b64 s[92:93], s[94:95]\n"
  "s_add_u32 s67, s67, s63\n"
  "buffer_load_dwordx4 v114, s[40:43], s57 offen offset:0 nt lds\n"
  "buffer_load_dwordx4 v115, s[40:43], s57 offen offset:1024 nt lds\n"
  "v_mfma_f32_16x16x32_bf16 v[0:3], v[68:71], v[208:211], v[0:3]\n"
  "v_fma_f32 v39, v39, v87, v99\n"
  "v_fma_f32 v40, v40, v84, v96\n"
  "v_fma_f32 v44, v44, v84, v96\n"
  "v_fma_f32 v41, v41, v85, v97\n"
  "v_mfma_f32_16x16x32_bf16 v[0:3], v[72:75], v[184:187], v[0:3]\n"
  "buffer_load_dwordx4 v116, s[40:43], s57 offen offset:2048  lds\n"
  "v_fma_f32 v28, v28, v84, v96\n"
  "v_fma_f32 v29, v29, v85, v97\n"
  "v_mfma_f32_16x16x32_bf16 v[24:27], v[64:67], v[160:163], v[0:3]\n"
  "s_waitcnt lgkmcnt(0)\n"
  "s_add_u32 s57, s57, 6144\n"
  "v_fma_f32 v30, v30, v86, v98\n"
  "v_fma_f32 v31, v31, v87, v99\n"
  "v_fma_f32 v45, v45, v85, v97\n"
  "v_fma_f32 v42, v42, v86, v98\n"
  "v_mfma_f32_16x16x32_bf16 v[0:3], v[48:51], a[0:3], v[100:103]\n"
  "ds_read_b128 v[64:67], v104 offset:4096\n"
  "v_fma_f32 v46, v46, v86, v98\n"
  "v_fma_f32 v43, v43, v87, v99\n"
  "v_fma_f32 v47, v47, v87, v99\n"
  "v_mfma_f32_16x16x32_bf16 v[0:3], v[56:59], a[24:27], v[0:3]\n"
  "s_add_u32 m0, s59, 12288\n"
  "v_cvt_pk_bf16_f32 v40, v40, v44\n"
  "v_cvt_pk_bf16_f32 v41, v41, v45\n"
  "v_cvt_pk_bf16_f32 v42, v42, v46\n"
  "v_cvt_pk_bf16_f32 v43, v43, v47\n"
  "v_mfma_f32_16x16x32_bf16 v[4:7], v[56:59], a[28:31], v[100:103]\n"
  "buffer_load_dwordx4 v114, s[40:43], s57 offen offset:0 nt lds\n"
  "buffer_load_dwordx4 v115, s[40:43], s57 offen offset:1024 nt lds\n"
  "ds_read_b128 v[68:71], v104 offset:6144\n"
  "v_mfma_f32_16x16x32_bf16 v[4:7], v[48:51], a[4:7], v[4:7]\n"
  "ds_write2_b32 v107, v40, v41 offset0:0 offset1:72\n"
  "v_fma_f32 v24, v24, v84, v96\n"
  "v_mfma_f32_16x16x32_bf16 v[8:11], v[48:51], a[8:11], v[100:103]\n"
  "buffer_load_dwordx4 v116, s[40:43], s57 offen offset:2048  lds\n"
  "v_fma_f32 v25, v25, v85, v97\n"
  "v_cvt_pk_bf16_f32 v24, v24, v28\n"
  "v_mfma_f32_16x16x32_bf16 v[8:11], v[56:59], a[32:35], v[8:11]\n"
  "s_add_u32 s57, s57, s84\n"
  "ds_write2_b32 v107, v42, v43 offset0:144 offset1:216\n"
  "v_cvt_pk_bf16_f32 v28, v25, v29\n"
  "v_mfma_f32_16x16x32_bf16 v[12:15], v[56:59], a[36:39], v[100:103]\n"
  "ds_read_b128 v[72:75], v105 offset:4096\n"
  "v_cvt_pk_bf16_f32 v25, v32, v36\n"
  "v_cvt_pk_bf16_f32 v29, v33, v37\n"
  "v_fma_f32 v26, v26, v86, v98\n"
  "v_mfma_f32_16x16x32_bf16 v[12:15], v[48:51], a[12:15], v[12:15]\n"
  "s_add_u32 m0, s59, 15360\n"
  "ds_write_b64 v106, v[24:25] offset:0\n"
  "v_fma_f32 v27, v27, v87, v99\n"
  "v_mfma_f32_16x16x32_bf16 v[16:19], v[48:51], a[16:19], v[100:103]\n"
  "buffer_load_dwordx4 v114, s[40:43], s57 offen offset:0 nt lds\n"
  "buffer_load_dwordx4 v115, s[40:43], s57 offen offset:1024 nt lds\n"
  "v_mfma_f32_16x16x32_bf16 v[16:19], v[56:59], a[40:43], v[16:19]\n"
  "ds_write_b64 v106, v[28:29] offset:288\n"
  "v_cvt_pk_bf16_f32 v26, v26, v30\n"
  "v_mfma_f32_16x16x32_bf16 v[20:23], v[56:59], a[44:47], v[100:103]\n"
  "buffer_load_dwordx4 v116, s[40:43], s57 offen offset:2048  lds\n"
  "ds_read_b128 v[76:79], v105 offset:6144\n"
  "v_cvt_pk_bf16_f32 v30, v27, v31\n"
  "v_mfma_f32_16x16x32_bf16 v[20:23], v[48:51], a[20:23], v[20:23]\n"
  "s_add_u32 s57, s57, 6144\n"
  "ds_read_b64_tr_b16 v[92:93], v108 offset:0\n"
  "ds_read_b64_tr_b16 v[94:95], v108 offset:32\n"
  "v_cvt_pk_bf16_f32 v27, v34, v38\n"
  "v_cvt_pk_bf16_f32 v31, v35, v39\n"
  "v_smfmac_f32_16x16x64_bf16 v[80:83], v[88:91], v[48:55], v124\n"
  "s_waitcnt lgkmcnt(8)\n"
  "s_waitcnt vmcnt(42)\n"
  "ds_read_b64_tr_b16 a[0:1], v117 offset:21504\n"
  "ds_read_b64_tr_b16 a[4:5], v118 offset:21504\n"
  "v_smfmac_f32_16x16x64_bf16 v[80:83], v[88:91], v[64:71], v124\n"
  "s_add_u32 m0, s59, 18432\n"
  "ds_write_b64 v106, v[26:27] offset:576\n"
  "v_mfma_f32_16x16x32_bf16 v[20:23], v[52:55], a[68:71], v[20:23]\n"
  "buffer_load_dwordx4 v114, s[40:43], s57 offen offset:0 nt lds\n"
  "buffer_load_dwordx4 v115, s[40:43], s57 offen offset:1024 nt lds\n"
  "ds_read_b64_tr_b16 a[8:9], v119 offset:21504\n"
  "ds_read_b64_tr_b16 a[12:13], v120 offset:21504\n"
  "v_mfma_f32_16x16x32_bf16 v[20:23], v[60:63], a[92:95], v[20:23]\n"
  "ds_read_b64_tr_b16 a[16:17], v121 offset:23552\n"
  "ds_read_b64_tr_b16 a[20:21], v122 offset:23552\n"
  "ds_write_b64 v106, v[30:31] offset:864\n"
  "v_mfma_f32_16x16x32_bf16 v[16:19], v[60:63], a[88:91], v[16:19]\n"
  "buffer_load_dwordx4 v116, s[40:43], s57 offen offset:2048  lds\n"
  "s_waitcnt vmcnt(38)\n"
  "ds_read_b64 v[32:33], v117 offset:27648\n"
  "ds_read_b64 v[34:35], v118 offset:27648\n"
  "v_mfma_f32_16x16x32_bf16 v[16:19], v[52:55], a[64:67], v[16:19]\n"
  "s_add_u32 s57, s57, s58\n"
  "ds_read_b64_tr_b16 a[2:3], v117 offset:24576\n"
  "ds_read_b64_tr_b16 a[6:7], v118 offset:24576\n"
  "v_mfma_f32_16x16x32_bf16 v[12:15], v[52:55], a[60:63], v[12:15]\n"
  "s_waitcnt lgkmcnt(12)\n"
  "buffer_store_dwordx4 v[92:95], v109, s[48:51], s68 offen offset:0 nt\n"
  "v_mfma_f32_16x16x32_bf16 v[12:15], v[60:63], a[84:87], v[12:15]\n"
  "ds_read_b64_tr_b16 a[10:11], v119 offset:24576\n"
  "ds_read_b64_tr_b16 a[14:15], v120 offset:24576\n"
  "ds_read_b64_tr_b16 v[96:97], v108 offset:2304\n"
  "ds_read_b64_tr_b16 v[98:99], v108 offset:2336\n"
  "v_mfma_f32_16x16x32_bf16 v[8:11], v[60:63], a[80:83], v[8:11]\n"
  "s_add_u32 m0, s59, 21504\n"
  "ds_read_b64_tr_b16 a[18:19], v121 offset:26624\n"
  "ds_read_b64_tr_b16 a[22:23], v122 offset:26624\n"
  "v_mfma_f32_16x16x32_bf16 v[8:11], v[52:55], a[56:59], v[8:11]\n"
  "s_waitcnt lgkmcnt(11)\n"
  "buffer_load_dwordx4 v114, s[40:43], s57 offen offset:0 nt lds\n"
  "buffer_load_dwordx4 v115, s[40:43], s57 offen offset:1024 nt lds\n"
  "v_mfma_f32_16x16x32_bf16 v[4:7], v[52:55], a[52:55], v[4:7]\n"
  "ds_read_b64_tr_b16 a[24:25], v117 offset:27648\n"
  "ds_read_b64_tr_b16 a[28:29], v118 offset:27648\n"
  "s_waitcnt lgkmcnt(10)\n"
  "v_max3_f32 v32, |v32|, |v33|, |v34|\n"
  "v_max3_f32 v32, v32, |v35|, 0\n"
  "v_mfma_f32_16x16x32_bf16 v[4:7], v[60:63], a[76:79], v[4:7]\n"
  "buffer_load_dwordx4 v116, s[40:43], s57 offen offset:2048  lds\n"
  "v_max_f32 v32, 0, v32\n"
  "v_mul_f32 v33, 0x40000000, v32\n"
  "v_mfma_f32_16x16x32_bf16 v[0:3], v[60:63], a[72:75], v[0:3]\n"
  "s_add_u32 s57, s57, 6144\n"
  "ds_read_b64_tr_b16 a[32:33], v119 offset:27648\n"
  "ds_read_b64_tr_b16 a[36:37], v120 offset:27648\n"
  "v_mul_f32 v32, 0x2b800000, v32\n"
  "v_max_f32 v82, v82, v32\n"
  "v_mfma_f32_16x16x32_bf16 v[0:3], v[52:55], a[48:51], v[0:3]\n"
  "ds_read_b64_tr_b16 a[40:41], v121 offset:29696\n"
  "ds_read_b64_tr_b16 a[44:45], v122 offset:29696\n"
  "v_readlane_b32 s98, v33, 0\n"
  "v_mfma_f32_16x16x32_bf16 v[0:3], v[64:67], a[96:99], v[0:3]\n"
  "s_add_u32 m0, s59, 24576\n"
  "ds_read_b128 v[48:51], v104 offset:8192\n"
  "v_readlane_b32 s62, v33, 21\n"
  "v_mfma_f32_16x16x32_bf16 v[0:3], v[72:75], a[120:123], v[0:3]\n"
  "s_waitcnt lgkmcnt(7)\n"
  "buffer_load_dwordx4 v114, s[40:43], s57 offen offset:0 nt lds\n"
  "buffer_load_dwordx4 v115, s[40:43], s57 offen offset:1024 nt lds\n"
  "s_waitcnt vmcnt(27)\n"
  "ds_read_b64_tr_b16 a[26:27], v117 offset:0\n"
  "ds_read_b64_tr_b16 a[30:31], v118 offset:0\n"
  "v_mfma_f32_16x16x32_bf16 v[4:7], v[72:75], a[124:127], v[4:7]\n"
  "ds_read_b128 v[52:55], v104 offset:10240\n"
  "v_readlane_b32 s63, v33, 42\n"
  "v_mfma_f32_16x16x32_bf16 v[4:7], v[64:67], a[100:103], v[4:7]\n"
  "buffer_load_dwordx4 v116, s[40:43], s57 offen offset:2048  lds\n"
  "ds_read_b64_tr_b16 a[34:35], v119 offset:0\n"
  "ds_read_b64_tr_b16 a[38:39], v120 offset:0\n"
  "v_mfma_f32_16x16x32_bf16 v[8:11], v[64:67], a[104:107], v[8:11]\n"
  "s_add_u32 s57, s57, s84\n"
  "ds_read_b64_tr_b16 a[42:43], v121 offset:2048\n"
  "ds_read_b64_tr_b16 a[46:47], v122 offset:2048\n"
  "v_readlane_b32 s64, v33, 63\n"
  "v_mfma_f32_16x16x32_bf16 v[8:11], v[72:75], a[128:131], v[8:11]\n"
  "buffer_store_dwordx4 v[96:99], v109, s[48:51], s68 offen offset:128 nt\n"
  "ds_read_b64_tr_b16 v[92:93], v108 offset:4608\n"
  "ds_read_b64_tr_b16 v[94:95], v108 offset:4640\n"
  "v_xor_b32 v108, 0x2000, v108\n"
  "v_mfma_f32_16x16x32_bf16 v[12:15], v[72:75], a[132:135], v[12:15]\n"
  "s_add_u32 m0, s59, 27648\n"
  "ds_read_b128 v[56:59], v105 offset:8192\n"
  "s_waitcnt vmcnt(25)\n"
  "ds_read_b64_tr_b16 a[48:49], v117 offset:3072\n"
  "ds_read_b64_tr_b16 a[52:53], v118 offset:3072\n"
  "v_mfma_f32_16x16x32_bf16 v[12:15], v[64:67], a[108:111], v[12:15]\n"
  "s_waitcnt lgkmcnt(13)\n"
  "buffer_load_dwordx4 v114, s[40:43], s57 offen offset:0 nt lds\n"
  "buffer_load_dwordx4 v115, s[40:43], s57 offen offset:1024 nt lds\n"
  "v_mfma_f32_16x16x32_bf16 v[16:19], v[64:67], a[112:115], v[16:19]\n"
  "s_nop 1\n"
  "s_max_u32 s98, s98, s62\n"
  "s_max_u32 s63, s63, s64\n"
  "s_max_u32 s98, s98, s63\n"
  "s_and_b32 s98, s98, 0x7f800000\n"
  "s_cselect_b32 s62, 0x400000, 0\n"
  "s_or_b32 s98, s98, s62\n"
  "s_nop 0\n"
  "ds_read_b64_tr_b16 a[56:57], v119 offset:3072\n"
  "ds_read_b64_tr_b16 a[60:61], v120 offset:3072\n"
  "v_mov_b32 v100, s98\n"
  "v_mov_b32 v101, s98\n"
  "v_mfma_f32_16x16x32_bf16 v[16:19], v[72:75], a[136:139], v[16:19]\n"
  "buffer_load_dwordx4 v116, s[40:43], s57 offen offset:2048  lds\n"
  "ds_read_b64_tr_b16 a[64:65], v121 offset:5120\n"
  "ds_read_b64_tr_b16 a[68:69], v122 offset:5120\n"
  "v_mfma_f32_16x16x32_bf16 v[20:23], v[72:75], a[140:143], v[20:23]\n"
  "s_add_u32 s57, s57, 6144\n"
  "ds_read_b128 v[60:63], v105 offset:10240\n"
  "v_mov_b32 v102, s98\n"
  "v_mov_b32 v103, s98\n"
  "v_mfma_f32_16x16x32_bf16 v[20:23], v[64:67], a[116:119], v[20:23]\n"
  "s_waitcnt vmcnt(24)\n"
  "ds_read_b64_tr_b16 a[50:51], v117 offset:6144\n"
  "ds_read_b64_tr_b16 a[54:55], v118 offset:6144\n"
  "v_mfma_f32_16x16x32_bf16 v[20:23], v[68:71], a[164:167], v[20:23]\n"
  "s_add_u32 m0, s59, 0\n"
  "v_mfma_f32_16x16x32_bf16 v[20:23], v[76:79], a[188:191], v[20:23]\n"
  "s_waitcnt lgkmcnt(12)\n"
  "buffer_load_dwordx4 v114, s[40:43], s57 offen offset:0 nt lds\n"
  "buffer_load_dwordx4 v115, s[40:43], s57 offen offset:1024 nt lds\n"
  "ds_read_b64_tr_b16 a[58:59], v119 offset:6144\n"
  "ds_read_b64_tr_b16 a[62:63], v120 offset:6144\n"
  "v_mfma_f32_16x16x32_bf16 v[16:19], v[76:79], a[184:187], v[16:19]\n"
  "ds_read_b64_tr_b16 a[66:67], v121 offset:8192\n"
  "ds_read_b64_tr_b16 a[70:71], v122 offset:8192\n"
  "v_mfma_f32_16x16x32_bf16 v[16:19], v[68:71], a[160:163], v[16:19]\n"
  "buffer_load_dwordx4 v116, s[40:43], s57 offen offset:2048  lds\n"
  "v_mfma_f32_16x16x32_bf16 v[12:15], v[68:71], a[156:159], v[12:15]\n"
  "s_add_u32 s57, s57, s58\n"
  "s_waitcnt vmcnt(23)\n"
  "ds_read_b64_tr_b16 a[72:73], v117 offset:9216\n"
  "ds_read_b64_tr_b16 a[76:77], v118 offset:9216\n"
  "v_mfma_f32_16x16x32_bf16 v[12:15], v[76:79], a[180:183], v[12:15]\n"
  "s_waitcnt lgkmcnt(15)\n"
  "buffer_store_dwordx4 v[92:95], v109, s[48:51], s68 offen offset:256 nt\n"
  "v_mfma_f32_16x16x32_bf16 v[8:11], v[76:79], a[176:179], v[8:11]\n"
  "s_add_u32 m0, s59, 3072\n"
  "ds_read_b64_tr_b16 a[80:81], v119 offset:9216\n"
  "ds_read_b64_tr_b16 a[84:85], v120 offset:9216\n"
  "v_mfma_f32_16x16x32_bf16 v[8:11], v[68:71], a[152:155], v[8:11]\n"
  "s_waitcnt lgkmcnt(11)\n"
  "buffer_load_dwordx4 v114, s[40:43], s57 offen offset:0 nt lds\n"
  "buffer_load_dwordx4 v115, s[40:43], s57 offen offset:1024 nt lds\n"
  "ds_read_b64_tr_b16 a[88:89], v121 offset:11264\n"
  "ds_read_b64_tr_b16 a[92:93], v122 offset:11264\n"
  "v_mfma_f32_16x16x32_bf16 v[4:7], v[68:71], a[148:151], v[4:7]\n"
  "s_add_u32 s68, s68, s69\n"
  "v_mfma_f32_16x16x32_bf16 v[4:7], v[76:79], a[172:175], v[4:7]\n"
  "buffer_load_dwordx4 v116, s[40:43], s57 offen offset:2048  lds\n"
  "s_waitcnt vmcnt(24)\n"
  "ds_read_b64_tr_b16 a[74:75], v117 offset:12288\n"
  "ds_read_b64_tr_b16 a[78:79], v118 offset:12288\n"
  "v_mfma_f32_16x16x32_bf16 v[0:3], v[76:79], a[168:171], v[0:3]\n"
  "s_add_u32 s57, s57, 6144\n"
  "v_mfma_f32_16x16x32_bf16 v[0:3], v[68:71], a[144:147], v[0:3]\n"
  "s_waitcnt lgkmcnt(14)\n"
  "ds_read_b64_tr_b16 a[82:83], v119 offset:12288\n"
  "ds_read_b64_tr_b16 a[86:87], v120 offset:12288\n"
  "v_mfma_f32_16x16x32_bf16 v[0:3], v[48:51], a[192:195], v[0:3]\n"
  "s_add_u32 m0, s59, 6144\n"
  "ds_read_b128 v[64:67], v104 offset:12288\n"
  "ds_read_b64_tr_b16 a[90:91], v121 offset:14336\n"
  "ds_read_b64_tr_b16 a[94:95], v122 offset:14336\n"
  "v_mfma_f32_16x16x32_bf16 v[0:3], v[56:59], a[216:219], v[0:3]\n"
  "s_waitcnt lgkmcnt(13)\n"
  "buffer_load_dwordx4 v114, s[40:43], s57 offen offset:0 nt lds\n"
  "buffer_load_dwordx4 v115, s[40:43], s57 offen offset:1024 nt lds\n"
  "v_mfma_f32_16x16x32_bf16 v[4:7], v[56:59], a[220:223], v[4:7]\n"
  "ds_read_b128 v[68:71], v104 offset:14336\n"
  "s_waitcnt vmcnt(23)\n"
  "ds_read_b64_tr_b16 a[96:97], v117 offset:15360\n"
  "ds_read_b64_tr_b16 a[100:101], v118 offset:15360\n"
  "v_xor_b32 v104, s88, v104\n"
  "v_mfma_f32_16x16x32_bf16 v[4:7], v[48:51], a[196:199], v[4:7]\n"
  "s_mov_b32 s69, s70\n"
  "buffer_load_dwordx4 v116, s[40:43], s57 offen offset:2048  lds\n"
  "v_mfma_f32_16x16x32_bf16 v[8:11], v[48:51], a[200:203], v[8:11]\n"
  "s_add_u32 s57, s57, s84\n"
  "ds_read_b64_tr_b16 a[104:105], v119 offset:15360\n"
  "ds_read_b64_tr_b16 a[108:109], v120 offset:15360\n"
  "v_mfma_f32_16x16x32_bf16 v[8:11], v[56:59], a[224:227], v[8:11]\n"
  "s_mov_b32 s70, s96\n"
  "ds_read_b64_tr_b16 a[112:113], v121 offset:17408\n"
  "ds_read_b64_tr_b16 a[116:117], v122 offset:17408\n"
  "v_mfma_f32_16x16x32_bf16 v[12:15], v[56:59], a[228:231], v[12:15]\n"
  "s_add_u32 m0, s59, 9216\n"
  "ds_read_b128 v[72:75], v105 offset:12288\n"
  "v_mfma_f32_16x16x32_bf16 v[12:15], v[48:51], a[204:207], v[12:15]\n"
  "s_waitcnt lgkmcnt(15)\n"
  "buffer_load_dwordx4 v114, s[40:43], s57 offen offset:0 nt lds\n"
  "buffer_load_dwordx4 v115, s[40:43], s57 offen offset:1024 nt lds\n"
  "s_waitcnt vmcnt(23)\n"
  "ds_read_b64_tr_b16 a[98:99], v117 offset:18432\n"
  "ds_read_b64_tr_b16 a[102:103], v118 offset:18432\n"
  "v_mfma_f32_16x16x32_bf16 v[16:19], v[48:51], a[208:211], v[16:19]\n"
  "s_mov_b32 s96, s97\n"
  "v_mfma_f32_16x16x32_bf16 v[16:19], v[56:59], a[232:235], v[16:19]\n"
  "buffer_load_dwordx4 v116, s[40:43], s57 offen offset:2048  lds\n"
  "ds_read_b64_tr_b16 a[106:107], v119 offset:18432\n"
  "ds_read_b64_tr_b16 a[110:111], v120 offset:18432\n"
  "v_mfma_f32_16x16x32_bf16 v[20:23], v[56:59], a[236:239], v[20:23]\n"
  "s_add_u32 s57, s57, 6144\n"
  "ds_read_b128 v[76:79], v105 offset:14336\n"
  "ds_read_b64_tr_b16 a[114:115], v121 offset:20480\n"
  "ds_read_b64_tr_b16 a[118:119], v122 offset:20480\n"
  "v_xor_b32 v105, s88, v105\n"
  "v_mfma_f32_16x16x32_bf16 v[20:23], v[48:51], a[212:215], v[20:23]\n"
  "s_xor_b32 s88, s88, s89\n"
  "v_smfmac_f32_16x16x64_bf16 v[80:83], v[88:91], v[48:55], v124\n"
  "s_waitcnt lgkmcnt(14)\n"
  "s_add_u32 m0, s59, 12288\n"
  "s_waitcnt vmcnt(20)\n"
  "ds_read_b64_tr_b16 a[120:121], v117 offset:21504\n"
  "ds_read_b64_tr_b16 a[124:125], v118 offset:21504\n"
  "v_smfmac_f32_16x16x64_bf16 v[80:83], v[88:91], v[64:71], v124\n"
  "buffer_load_dwordx4 v114, s[40:43], s57 offen offset:0 nt lds\n"
  "buffer_load_dwordx4 v115, s[40:43], s57 offen offset:1024 nt lds\n"
  "v_mfma_f32_16x16x32_bf16 v[20:23], v[52:55], v[132:135], v[20:23]\n"
  "ds_read_b64_tr_b16 a[128:129], v119 offset:21504\n"
  "ds_read_b64_tr_b16 a[132:133], v120 offset:21504\n"
  "v_mfma_f32_16x16x32_bf16 v[20:23], v[60:63], v[156:159], v[20:23]\n"
  "buffer_load_dwordx4 v116, s[40:43], s57 offen offset:2048  lds\n"
  "ds_read_b64_tr_b16 a[136:137], v121 offset:23552\n"
  "ds_read_b64_tr_b16 a[140:141], v122 offset:23552\n"
  "v_mfma_f32_16x16x32_bf16 v[16:19], v[60:63], v[152:155], v[16:19]\n"
  "s_add_u32 s57, s57, s58\n"
  "v_add_f32 v44, v80, v81\n"
  "v_mov_b32 v80, 0\n"
  "v_mov_b32 v81, 0\n"
  "v_mfma_f32_16x16x32_bf16 v[16:19], v[52:55], v[128:131], v[16:19]\n"
  "s_waitcnt vmcnt(20)\n"
  "ds_read_b64_tr_b16 a[122:123], v117 offset:24576\n"
  "ds_read_b64_tr_b16 a[126:127], v118 offset:24576\n"
  "ds_write_b32 v110, v44\n"
  "v_mfma_f32_16x16x32_bf16 v[12:15], v[52:55], a[252:255], v[12:15]\n"
  "s_add_u32 m0, s59, 15360\n"
  "v_mfma_f32_16x16x32_bf16 v[12:15], v[60:63], v[148:151], v[12:15]\n"
  "s_waitcnt lgkmcnt(15)\n"
  "buffer_load_dwordx4 v114, s[40:43], s57 offen offset:0 nt lds\n"
  "buffer_load_dwordx4 v115, s[40:43], s57 offen offset:1024 nt lds\n"
  "ds_read_b64_tr_b16 a[130:131], v119 offset:24576\n"
  "ds_read_b64_tr_b16 a[134:135], v120 offset:24576\n"
  "v_mfma_f32_16x16x32_bf16 v[8:11], v[60:63], v[144:147], v[8:11]\n"
  "ds_read_b64_tr_b16 a[138:139], v121 offset:26624\n"
  "ds_read_b64_tr_b16 a[142:143], v122 offset:26624\n"
  "v_mfma_f32_16x16x32_bf16 v[8:11], v[52:55], a[248:251], v[8:11]\n"
  "buffer_load_dwordx4 v116, s[40:43], s57 offen offset:2048  lds\n"
  "v_xor_b32 v106, 0x2000, v106\n"
  "v_xor_b32 v107, 0x2000, v107\n"
  "v_mfma_f32_16x16x32_bf16 v[4:7], v[52:55], a[244:247], v[4:7]\n"
  "s_add_u32 s57, s57, 6144\n"
  "s_waitcnt vmcnt(19)\n"
  "ds_read_b64_tr_b16 a[144:145], v117 offset:27648\n"
  "ds_read_b64_tr_b16 a[148:149], v118 offset:27648\n"
  "v_xor_b32 v110, 0x2000, v110\n"
  "v_mfma_f32_16x16x32_bf16 v[4:7], v[60:63], v[140:143], v[4:7]\n"
  "v_mfma_f32_16x16x32_bf16 v[0:3], v[60:63], v[136:139], v[0:3]\n"
  "s_add_u32 m0, s59, 18432\n"
  "ds_read_b64_tr_b16 a[152:153], v119 offset:27648\n"
  "ds_read_b64_tr_b16 a[156:157], v120 offset:27648\n"
  "v_mfma_f32_16x16x32_bf16 v[0:3], v[52:55], a[240:243], v[0:3]\n"
  "s_waitcnt lgkmcnt(15)\n"
  "buffer_load_dwordx4 v114, s[40:43], s57 offen offset:0 nt lds\n"
  "buffer_load_dwordx4 v115, s[40:43], s57 offen offset:1024 nt lds\n"
  "ds_read_b64_tr_b16 a[160:161], v121 offset:29696\n"
  "ds_read_b64_tr_b16 a[164:165], v122 offset:29696\n"
  "s_waitcnt lgkmcnt(0)\n"
  "s_barrier\n"
  "v_mfma_f32_16x16x32_bf16 v[0:3], v[64:67], v[160:163], v[0:3]\n"
  "s_add_u32 s72, s72, 7\n"
  "ds_read_b128 v[48:51], v104 offset:0\n"
  "ds_read_b128 v[84:87], v111\n"
  "ds_read_b128 v[96:99], v111 offset:64\n"
  "v_xor_b32 v111, 0x2000, v111\n"
  "v_mfma_f32_16x16x32_bf16 v[0:3], v[72:75], v[184:187], v[0:3]\n"
  "buffer_load_dwordx4 v116, s[40:43], s57 offen offset:2048  lds\n"
  "s_waitcnt vmcnt(19)\n"
  "ds_read_b64_tr_b16 a[146:147], v117 offset:0\n"
  "ds_read_b64_tr_b16 a[150:151], v118 offset:0\n"
  "v_mfma_f32_16x16x32_bf16 v[4:7], v[72:75], v[188:191], v[4:7]\n"
  "s_add_u32 s57, s57, s84\n"
  "ds_read_b128 v[52:55], v104 offset:2048\n"
  "v_mfma_f32_16x16x32_bf16 v[4:7], v[64:67], v[164:167], v[4:7]\n"
  "s_xor_b32 s89, s88, s89\n"
  "ds_read_b64_tr_b16 a[154:155], v119 offset:0\n"
  "ds_read_b64_tr_b16 a[158:159], v120 offset:0\n"
  "v_mfma_f32_16x16x32_bf16 v[8:11], v[64:67], v[168:171], v[8:11]\n"
  "s_add_u32 m0, s59, 21504\n"
  "ds_read_b64_tr_b16 a[162:163], v121 offset:2048\n"
  "ds_read_b64_tr_b16 a[166:167], v122 offset:2048\n"
  "v_mfma_f32_16x16x32_bf16 v[8:11], v[72:75], v[192:195], v[8:11]\n"
  "buffer_load_dwordx4 v114, s[40:43], s57 offen offset:0 nt lds\n"
  "buffer_load_dwordx4 v115, s[40:43], s57 offen offset:1024 nt lds\n"
  "v_mfma_f32_16x16x32_bf16 v[12:15], v[72:75], v[196:199], v[12:15]\n"
  "ds_read_b128 v[56:59], v105 offset:0\n"
  "s_waitcnt vmcnt(17)\n"
  "ds_read_b64_tr_b16 a[168:169], v117 offset:3072\n"
  "ds_read_b64_tr_b16 a[172:173], v118 offset:3072\n"
  "v_mfma_f32_16x16x32_bf16 v[12:15], v[64:67], v[172:175], v[12:15]\n"
  "s_sub_u32 s56, s56, 1\n"
  "buffer_load_dwordx4 v116, s[40:43], s57 offen offset:2048  lds\n"
  "v_mfma_f32_16x16x32_bf16 v[16:19], v[64:67], v[176:179], v[16:19]\n"
  "s_add_u32 s57, s57, 6144\n"
  "ds_read_b64_tr_b16 a[176:177], v119 offset:3072\n"
  "ds_read_b64_tr_b16 a[180:181], v120 offset:3072\n"
  "v_mfma_f32_16x16x32_bf16 v[16:19], v[72:75], v[200:203], v[16:19]\n"
  "s_mov_b32 s61, 99\n"
  "ds_read_b64_tr_b16 a[184:185], v121 offset:5120\n"
  "ds_read_b64_tr_b16 a[188:189], v122 offset:5120\n"
  "v_mfma_f32_16x16x32_bf16 v[20:23], v[72:75], v[204:207], v[20:23]\n"
  "s_add_u32 m0, s66, 0\n"
  "s_nop 0\n"
  "buffer_load_dwordx4 v112, s[44:47], s67 offen offset:0 nt lds\n"
  "s_add_u32 m0, s59, 24576\n"
  "ds_read_b128 v[60:63], v105 offset:2048\n"
  "v_mfma_f32_16x16x32_bf16 v[20:23], v[64:67], v[180:183], v[20:23]\n"
  "buffer_load_dwordx4 v114, s[40:43], s57 offen offset:0 nt lds\n"
  "buffer_load_dwordx4 v115, s[40:43], s57 offen offset:1024 nt lds\n"
  "s_waitcnt vmcnt(18)\n"
  "ds_read_b64_tr_b16 a[170:171], v117 offset:6144\n"
  "ds_read_b64_tr_b16 a[174:175], v118 offset:6144\n"
  "v_mfma_f32_16x16x32_bf16 v[20:23], v[68:71], v[228:231], v[20:23]\n"
  "s_waitcnt lgkmcnt(15)\n"
  "v_add_f32 v84, v84, v96\n"
  "v_add_f32 v85, v85, v97\n"
  "v_add_f32 v86, v86, v98\n"
  "v_add_f32 v87, v87, v99\n"
  "v_mfma_f32_16x16x32_bf16 v[44:47], v[76:79], v[252:255], v[20:23]\n"
  "buffer_load_dwordx4 v116, s[40:43], s57 offen offset:2048  lds\n"
  "ds_read_b64_tr_b16 a[178:179], v119 offset:6144\n"
  "ds_read_b64_tr_b16 a[182:183], v120 offset:6144\n"
  "v_mfma_f32_16x16x32_bf16 v[16:19], v[76:79], v[248:251], v[16:19]\n"
  "s_add_u32 s57, s57, s58\n"
  "ds_read_b64_tr_b16 a[186:187], v121 offset:8192\n"
  "ds_read_b64_tr_b16 a[190:191], v122 offset:8192\n"
  "v_max3_f32 v82, v82, v84, v85\n"
  "v_max3_f32 v82, v82, v86, v87\n"
  "v_mfma_f32_16x16x32_bf16 v[40:43], v[68:71], v[224:227], v[16:19]\n"
  "v_rcp_f32 v84, v84\n"
  "v_rcp_f32 v85, v85\n"
  "v_mfma_f32_16x16x32_bf16 v[12:15], v[68:71], v[220:223], v[12:15]\n"
  "s_add_u32 m0, s66, 1920\n"
  "s_nop 0\n"
  "buffer_load_dwordx4 v112, s[44:47], s67 offen offset:128 nt lds\n"
  "s_add_u32 m0, s59, 27648\n"
  "s_waitcnt vmcnt(17)\n"
  "ds_read_b64_tr_b16 a[192:193], v117 offset:9216\n"
  "ds_read_b64_tr_b16 a[196:197], v118 offset:9216\n"
  "v_mfma_f32_16x16x32_bf16 v[36:39], v[76:79], v[244:247], v[12:15]\n"
  "buffer_load_dwordx4 v114, s[40:43], s57 offen offset:0 nt lds\n"
  "buffer_load_dwordx4 v115, s[40:43], s57 offen offset:1024 nt lds\n"
  "v_mfma_f32_16x16x32_bf16 v[8:11], v[76:79], v[240:243], v[8:11]\n"
  "ds_read_b64_tr_b16 a[200:201], v119 offset:9216\n"
  "ds_read_b64_tr_b16 a[204:205], v120 offset:9216\n"
  "v_rcp_f32 v86, v86\n"
  "v_mfma_f32_16x16x32_bf16 v[32:35], v[68:71], v[216:219], v[8:11]\n"
  "buffer_load_dwordx4 v116, s[40:43], s57 offen offset:2048  lds\n"
  "ds_read_b64_tr_b16 a[208:209], v121 offset:11264\n"
  "ds_read_b64_tr_b16 a[212:213], v122 offset:11264\n"
  "v_mfma_f32_16x16x32_bf16 v[4:7], v[68:71], v[212:215], v[4:7]\n"
  "s_add_u32 s57, s57, 6144\n"
  "v_rcp_f32 v87, v87\n"
  "v_max3_f32 v83, v83, v84, v85\n"
  "v_mul_f32 v96, s99, v84\n"
  "v_mfma_f32_16x16x32_bf16 v[28:31], v[76:79], v[236:239], v[4:7]\n"
  "s_waitcnt vmcnt(17)\n"
  "ds_read_b64_tr_b16 a[194:195], v117 offset:12288\n"
  "ds_read_b64_tr_b16 a[198:199], v118 offset:12288\n"
  "v_max3_f32 v83, v83, v86, v87\n"
  "v_mul_f32 v97, s99, v85\n"
  "v_mfma_f32_16x16x32_bf16 v[0:3], v[76:79], v[232:235], v[0:3]\n"
  "s_add_u32 m0, s66, 3840\n"
  "s_nop 0\n"
  "buffer_load_dwordx4 v112, s[44:47], s67 offen offset:256 nt lds\n"
  "s_add_u32 m0, s59, 0\n"
  "v_mul_f32 v98, s99, v86\n"
  "v_mul_f32 v99, s99, v87\n"
  "v_mfma_f32_16x16x32_bf16 v[24:27], v[68:71], v[208:211], v[0:3]\n"
  "s_waitcnt lgkmcnt(14)\n"
  "s_add_u32 s66, s66, 6144\n"
  "s_cmp_ge_u32 s66, 102400\n"
  "s_cselect_b32 s64, 36864, 0\n"
  "s_sub_u32 s66, s66, s64\n"
  "s_sub_u32 s90, s90, 1\n"
  "s_cselect_b32 s63, s91, 6144\n"
  "s_cmov_b64 s[90:91], s[92:93]\n"
  "s_cmov_b64 s[92:93], s[94:95]\n"
  "s_add_u32 s67, s67, s63\n"
  "buffer_load_dwordx4 v114, s[40:43], s57 offen offset:0 nt lds\n"
  "buffer_load_dwordx4 v115, s[40:43], s57 offen offset:1024 nt lds\n"
  "ds_read_b64_tr_b16 a[202:203], v119 offset:12288\n"
  "ds_read_b64_tr_b16 a[206:207], v120 offset:12288\n"
  "v_mfma_f32_16x16x32_bf16 v[0:3], v[48:51], a[0:3], v[100:103]\n"
  "s_xor_b32 s99, 0x80000000, s98\n"
  "ds_read_b128 v[64:67], v104 offset:4096\n"
  "ds_read_b64_tr_b16 a[210:211], v121 offset:14336\n"
  "ds_read_b64_tr_b16 a[214:215], v122 offset:14336\n"
  "v_fma_f32 v40, v40, v84, v96\n"
  "v_mfma_f32_16x16x32_bf16 v[0:3], v[56:59], a[24:27], v[0:3]\n"
  "buffer_load_dwordx4 v116, s[40:43], s57 offen offset:2048  lds\n"
  "v_fma_f32 v44, v44, v84, v96\n"
  "v_fma_f32 v41, v41, v85, v97\n"
  "v_mfma_f32_16x16x32_bf16 v[4:7], v[56:59], a[28:31], v[100:103]\n"
  "s_add_u32 s57, s57, s84\n"
  "ds_read_b128 v[68:71], v104 offset:6144\n"
  "s_waitcnt vmcnt(18)\n"
  "ds_read_b64_tr_b16 a[216:217], v117 offset:15360\n"
  "ds_read_b64_tr_b16 a[220:221], v118 offset:15360\n"
  "v_fma_f32 v45, v45, v85, v97\n"
  "v_mfma_f32_16x16x32_bf16 v[4:7], v[48:51], a[4:7], v[4:7]\n"
  "ds_read_b64_tr_b16 v[92:93], v108 offset:0\n"
  "ds_read_b64_tr_b16 v[94:95], v108 offset:32\n"
  "v_fma_f32 v42, v42, v86, v98\n"
  "v_fma_f32 v46, v46, v86, v98\n"
  "v_mfma_f32_16x16x32_bf16 v[8:11], v[48:51], a[8:11], v[100:103]\n"
  "s_add_u32 m0, s59, 3072\n"
  "ds_read_b64_tr_b16 a[224:225], v119 offset:15360\n"
  "ds_read_b64_tr_b16 a[228:229], v120 offset:15360\n"
  "v_fma_f32 v43, v43, v87, v99\n"
  "v_fma_f32 v47, v47, v87, v99\n"
  "v_mfma_f32_16x16x32_bf16 v[8:11], v[56:59], a[32:35], v[8:11]\n"
  "buffer_load_dwordx4 v114, s[40:43], s57 offen offset:0 nt lds\n"
  "buffer_load_dwordx4 v115, s[40:43], s57 offen offset:1024 nt lds\n"
  "ds_read_b64_tr_b16 a[232:233], v121 offset:17408\n"
  "ds_read_b64_tr_b16 a[236:237], v122 offset:17408\n"
  "v_mfma_f32_16x16x32_bf16 v[12:15], v[56:59], a[36:39], v[100:103]\n"
  "ds_read_b128 v[72:75], v105 offset:4096\n"
  "v_fma_f32 v24, v24, v84, v96\n"
  "v_fma_f32 v25, v25, v85, v97\n"
  "v_fma_f32 v28, v28, v84, v96\n"
  "v_mfma_f32_16x16x32_bf16 v[12:15], v[48:51], a[12:15], v[12:15]\n"
  "buffer_load_dwordx4 v116, s[40:43], s57 offen offset:2048  lds\n"
  "s_waitcnt vmcnt(18)\n"
  "ds_read_b64_tr_b16 a[218:219], v117 offset:18432\n"
  "ds_read_b64_tr_b16 a[222:223], v118 offset:18432\n"
  "v_mfma_f32_16x16x32_bf16 v[16:19], v[48:51], a[16:19], v[100:103]\n"
  "s_add_u32 s57, s57, 6144\n"
  "v_fma_f32 v29, v29, v85, v97\n"
  "v_fma_f32 v32, v32, v84, v96\n"
  "v_fma_f32 v33, v33, v85, v97\n"
  "v_fma_f32 v36, v36, v84, v96\n"
  "v_mfma_f32_16x16x32_bf16 v[16:19], v[56:59], a[40:43], v[16:19]\n"
  "ds_read_b64_tr_b16 a[226:227], v119 offset:18432\n"
  "ds_read_b64_tr_b16 a[230:231], v120 offset:18432\n"
  "v_fma_f32 v37, v37, v85, v97\n"
  "v_fma_f32 v26, v26, v86, v98\n"
  "v_mfma_f32_16x16x32_bf16 v[20:23], v[56:59], a[44:47], v[100:103]\n"
  "s_add_u32 m0, s59, 6144\n"
  "ds_read_b128 v[76:79], v105 offset:6144\n"
  "ds_read_b64_tr_b16 a[234:235], v121 offset:20480\n"
  "ds_read_b64_tr_b16 a[238:239], v122 offset:20480\n"
  "v_fma_f32 v27, v27, v87, v99\n"
  "v_mfma_f32_16x16x32_bf16 v[20:23], v[48:51], a[20:23], v[20:23]\n"
  "s_waitcnt lgkmcnt(15)\n"
  "buffer_load_dwordx4 v114, s[40:43], s57 offen offset:0 nt lds\n"
  "buffer_load_dwordx4 v115, s[40:43], s57 offen offset:1024 nt lds\n"
  "v_smfmac_f32_16x16x64_bf16 v[80:83], v[88:91], v[48:55], v124\n"
  "s_waitcnt vmcnt(17)\n"
  "ds_read_b64_tr_b16 a[240:241], v117 offset:21504\n"
  "ds_read_b64_tr_b16 a[244:245], v118 offset:21504\n"
  "v_fma_f32 v30, v30, v86, v98\n"
  "v_smfmac_f32_16x16x64_bf16 v[80:83], v[88:91], v[64:71], v124\n"
  "buffer_load_dwordx4 v116, s[40:43], s57 offen offset:2048  lds\n"
  "v_fma_f32 v31, v31, v87, v99\n"
  "v_mfma_f32_16x16x32_bf16 v[20:23], v[52:55], a[68:71], v[20:23]\n"
  "s_add_u32 s57, s57, s58\n"
  "ds_read_b64_tr_b16 a[248:249], v119 offset:21504\n"
  "ds_read_b64_tr_b16 a[252:253], v120 offset:21504\n"
  "v_fma_f32 v34, v34, v86, v98\n"
  "v_fma_f32 v35, v35, v87, v99\n"
  "v_mfma_f32_16x16x32_bf16 v[20:23], v[60:63], a[92:95], v[20:23]\n"
  "s_waitcnt lgkmcnt(15)\n"
  "buffer_store_dwordx4 v[92:95], v109, s[48:51], s68 offen offset:0 nt\n"
  "ds_read_b64_tr_b16 v[128:129], v121 offset:23552\n"
  "ds_read_b64_tr_b16 v[132:133], v122 offset:23552\n"
  "v_fma_f32 v38, v38, v86, v98\n"
  "v_mfma_f32_16x16x32_bf16 v[16:19], v[60:63], a[88:91], v[16:19]\n"
  "s_add_u32 m0, s59, 9216\n"
  "v_fma_f32 v39, v39, v87, v99\n"
  "v_cvt_pk_bf16_f32 v40, v40, v44\n"
  "v_cvt_pk_bf16_f32 v41, v41, v45\n"
  "v_cvt_pk_bf16_f32 v42, v42, v46\n"
  "v_mfma_f32_16x16x32_bf16 v[16:19], v[52:55], a[64:67], v[16:19]\n"
  "buffer_load_dwordx4 v114, s[40:43], s57 offen offset:0 nt lds\n"
  "buffer_load_dwordx4 v115, s[40:43], s57 offen offset:1024 nt lds\n"
  "s_waitcnt vmcnt(17)\n"
  "ds_read_b64_tr_b16 a[242:243], v117 offset:24576\n"
  "ds_read_b64_tr_b16 a[246:247], v118 offset:24576\n"
  "v_mfma_f32_16x16x32_bf16 v[12:15], v[52:55], a[60:63], v[12:15]\n"
  "ds_write2_b32 v107, v40, v41 offset0:0 offset1:72\n"
  "v_cvt_pk_bf16_f32 v43, v43, v47\n"
  "v_mfma_f32_16x16x32_bf16 v[12:15], v[60:63], a[84:87], v[12:15]\n"
  "buffer_load_dwordx4 v116, s[40:43], s57 offen offset:2048  lds\n"
  "ds_read_b64_tr_b16 a[250:251], v119 offset:24576\n"
  "ds_read_b64_tr_b16 a[254:255], v120 offset:24576\n"
  "v_mfma_f32_16x16x32_bf16 v[8:11], v[60:63], a[80:83], v[8:11]\n"
  "s_add_u32 s57, s57, 6144\n"
  "ds_read_b64_tr_b16 v[130:131], v121 offset:26624\n"
  "ds_read_b64_tr_b16 v[134:135], v122 offset:26624\n"
  "ds_read_b64_tr_b16 v[96:97], v108 offset:2304\n"
  "ds_read_b64_tr_b16 v[98:99], v108 offset:2336\n"
  "v_mfma_f32_16x16x32_bf16 v[8:11], v[52:55], a[56:59], v[8:11]\n"
  "ds_write2_b32 v107, v42, v43 offset0:144 offset1:216\n"
  "v_cvt_pk_bf16_f32 v24, v24, v28\n"
  "v_mfma_f32_16x16x32_bf16 v[4:7], v[52:55], a[52:55], v[4:7]\n"
  "s_add_u32 m0, s59, 12288\n"
  "s_waitcnt vmcnt(14)\n"
  "ds_read_b64_tr_b16 v[136:137], v117 offset:27648\n"
  "ds_read_b64_tr_b16 v[140:141], v118 offset:27648\n"
  "v_cvt_pk_bf16_f32 v28, v25, v29\n"
  "v_cvt_pk_bf16_f32 v25, v32, v36\n"
  "v_mfma_f32_16x16x32_bf16 v[4:7], v[60:63], a[76:79], v[4:7]\n"
  "buffer_load_dwordx4 v114, s[40:43], s57 offen offset:0 nt lds\n"
  "buffer_load_dwordx4 v115, s[40:43], s57 offen offset:1024 nt lds\n"
  "v_mfma_f32_16x16x32_bf16 v[0:3], v[60:63], a[72:75], v[0:3]\n"
  "ds_read_b64_tr_b16 v[144:145], v119 offset:27648\n"
  "ds_read_b64_tr_b16 v[148:149], v120 offset:27648\n"
  "v_cvt_pk_bf16_f32 v29, v33, v37\n"
  "v_cvt_pk_bf16_f32 v26, v26, v30\n"
  "v_mfma_f32_16x16x32_bf16 v[0:3], v[52:55], a[48:51], v[0:3]\n"
  "s_waitcnt lgkmcnt(15)\n"
  "buffer_load_dwordx4 v116, s[40:43], s57 offen offset:2048  lds\n"
  "ds_read_b64_tr_b16 v[152:153], v121 offset:29696\n"
  "ds_read_b64_tr_b16 v[156:157], v122 offset:29696\n"
  "v_mfma_f32_16x16x32_bf16 v[0:3], v[64:67], a[96:99], v[0:3]\n"
  "s_add_u32 s57, s57, s84\n"
  "ds_read_b128 v[48:51], v104 offset:8192\n"
  "ds_write_b64 v106, v[24:25] offset:0\n"
  "v_mfma_f32_16x16x32_bf16 v[0:3], v[72:75], a[120:123], v[0:3]\n"
  "s_waitcnt vmcnt(13)\n"
  "ds_read_b64_tr_b16 v[138:139], v117 offset:0\n"
  "ds_read_b64_tr_b16 v[142:143], v118 offset:0\n"
  "v_cvt_pk_bf16_f32 v30, v27, v31\n"
  "v_cvt_pk_bf16_f32 v27, v34, v38\n"
  "v_mfma_f32_16x16x32_bf16 v[4:7], v[72:75], a[124:127], v[4:7]\n"
  "s_add_u32 m0, s59, 15360\n"
  "ds_read_b128 v[52:55], v104 offset:10240\n"
  "ds_write_b64 v106, v[28:29] offset:288\n"
  "v_mfma_f32_16x16x32_bf16 v[4:7], v[64:67], a[100:103], v[4:7]\n"
  "buffer_load_dwordx4 v114, s[40:43], s57 offen offset:0 nt lds\n"
  "buffer_load_dwordx4 v115, s[40:43], s57 offen offset:1024 nt lds\n"
  "ds_read_b64_tr_b16 v[146:147], v119 offset:0\n"
  "ds_read_b64_tr_b16 v[150:151], v120 offset:0\n"
  "v_mfma_f32_16x16x32_bf16 v[8:11], v[64:67], a[104:107], v[8:11]\n"
  "ds_read_b64_tr_b16 v[154:155], v121 offset:2048\n"
  "ds_read_b64_tr_b16 v[158:159], v122 offset:2048\n"
  "v_cvt_pk_bf16_f32 v31, v35, v39\n"
  "v_mfma_f32_16x16x32_bf16 v[8:11], v[72:75], a[128:131], v[8:11]\n"
  "buffer_load_dwordx4 v116, s[40:43], s57 offen offset:2048  lds\n"
  "ds_read_b64_tr_b16 v[92:93], v108 offset:4608\n"
  "ds_read_b64_tr_b16 v[94:95], v108 offset:4640\n"
  "v_mfma_f32_16x16x32_bf16 v[12:15], v[72:75], a[132:135], v[12:15]\n"
  "s_add_u32 s57, s57, 6144\n"
  "ds_read_b128 v[56:59], v105 offset:8192\n"
  "s_waitcnt vmcnt(13)\n"
  "ds_read_b64_tr_b16 v[160:161], v117 offset:3072\n"
  "ds_read_b64_tr_b16 v[164:165], v118 offset:3072\n"
  "v_xor_b32 v108, 0x2000, v108\n"
  "v_mfma_f32_16x16x32_bf16 v[12:15], v[64:67], a[108:111], v[12:15]\n"
  "s_waitcnt lgkmcnt(15)\n"
  "buffer_store_dwordx4 v[96:99], v109, s[48:51], s68 offen offset:128 nt\n"
  "ds_write_b64 v106, v[26:27] offset:576\n"
  "v_mfma_f32_16x16x32_bf16 v[16:19], v[64:67], a[112:115], v[16:19]\n"
  "s_add_u32 m0, s59, 18432\n"
  "ds_read_b64_tr_b16 v[168:169], v119 offset:3072\n"
  "ds_read_b64_tr_b16 v[172:173], v120 offset:3072\n"
  "ds_write_b64 v106, v[30:31] offset:864\n"
  "v_mfma_f32_16x16x32_bf16 v[16:19], v[72:75], a[136:139], v[16:19]\n"
  "buffer_load_dwordx4 v114, s[40:43], s57 offen offset:0 nt lds\n"
  "buffer_load_dwordx4 v115, s[40:43], s57 offen offset:1024 nt lds\n"
  "ds_read_b64_tr_b16 v[176:177], v121 offset:5120\n"
  "ds_read_b64_tr_b16 v[180:181], v122 offset:5120\n"
  "v_mfma_f32_16x16x32_bf16 v[20:23], v[72:75], a[140:143], v[20:23]\n"
  "ds_read_b128 v[60:63], v105 offset:10240\n"
  "v_mfma_f32_16x16x32_bf16 v[20:23], v[64:67], a[116:119], v[20:23]\n"
  "buffer_load_dwordx4 v116, s[40:43], s57 offen offset:2048  lds\n"
  "s_waitcnt vmcnt(14)\n"
  "ds_read_b64_tr_b16 v[162:163], v117 offset:6144\n"
  "ds_read_b64_tr_b16 v[166:167], v118 offset:6144\n"
  "v_mfma_f32_16x16x32_bf16 v[20:23], v[68:71], a[164:167], v[20:23]\n"
  "s_add_u32 s57, s57, s58\n"
  "v_mfma_f32_16x16x32_bf16 v[20:23], v[76:79], a[188:191], v[20:23]\n"
  "ds_read_b64_tr_b16 v[170:171], v119 offset:6144\n"
  "ds_read_b64_tr_b16 v[174:175], v120 offset:6144\n"
  "v_mfma_f32_16x16x32_bf16 v[16:19], v[76:79], a[184:187], v[16:19]\n"
  "s_add_u32 m0, s59, 21504\n"
  "ds_read_b64_tr_b16 v[178:179], v121 offset:8192\n"
  "ds_read_b64_tr_b16 v[182:183], v122 offset:8192\n"
  "v_mfma_f32_16x16x32_bf16 v[16:19], v[68:71], a[160:163], v[16:19]\n"
  "buffer_load_dwordx4 v114, s[40:43], s57 offen offset:0 nt lds\n"
  "buffer_load_dwordx4 v115, s[40:43], s57 offen offset:1024 nt lds\n"
  "v_mfma_f32_16x16x32_bf16 v[12:15], v[68:71], a[156:159], v[12:15]\n"
  "s_waitcnt vmcnt(12)\n"
  "ds_read_b64_tr_b16 v[184:185], v117 offset:9216\n"
  "ds_read_b64_tr_b16 v[188:189], v118 offset:9216\n"
  "v_mfma_f32_16x16x32_bf16 v[12:15], v[76:79], a[180:183], v[12:15]\n"
  "buffer_load_dwordx4 v116, s[40:43], s57 offen offset:2048  lds\n"
  "v_mfma_f32_16x16x32_bf16 v[8:11], v[76:79], a[176:179], v[8:11]\n"
  "s_add_u32 s57, s57, 6144\n"
  "ds_read_b64_tr_b16 v[192:193], v119 offset:9216\n"
  "ds_read_b64_tr_b16 v[196:197], v120 offset:9216\n"
  "v_mfma_f32_16x16x32_bf16 v[8:11], v[68:71], a[152:155], v[8:11]\n"
  "s_add_u32 m0, s59, 24576\n"
  "s_waitcnt lgkmcnt(15)\n"
  "buffer_store_dwordx4 v[92:95], v109, s[48:51], s68 offen offset:256 nt\n"
  "ds_read_b64_tr_b16 v[200:201], v121 offset:11264\n"
  "ds_read_b64_tr_b16 v[204:205], v122 offset:11264\n"
  "v_mfma_f32_16x16x32_bf16 v[4:7], v[68:71], a[148:151], v[4:7]\n"
  "buffer_load_dwordx4 v114, s[40:43], s57 offen offset:0 nt lds\n"
  "buffer_load_dwordx4 v115, s[40:43], s57 offen offset:1024 nt lds\n"
  "v_mfma_f32_16x16x32_bf16 v[4:7], v[76:79], a[172:175], v[4:7]\n"
  "s_waitcnt vmcnt(13)\n"
  "ds_read_b64_tr_b16 v[186:187], v117 offset:12288\n"
  "ds_read_b64_tr_b16 v[190:191], v118 offset:12288\n"
  "v_mfma_f32_16x16x32_bf16 v[0:3], v[76:79], a[168:171], v[0:3]\n"
  "s_add_u32 s68, s68, s69\n"
  "buffer_load_dwordx4 v116, s[40:43], s57 offen offset:2048  lds\n"
  "v_mfma_f32_16x16x32_bf16 v[0:3], v[68:71], a[144:147], v[0:3]\n"
  "s_waitcnt lgkmcnt(14)\n"
  "s_add_u32 s57, s57, s84\n"
  "ds_read_b64_tr_b16 v[194:195], v119 offset:12288\n"
  "ds_read_b64_tr_b16 v[198:199], v120 offset:12288\n"
  "v_mfma_f32_16x16x32_bf16 v[0:3], v[48:51], a[192:195], v[0:3]\n"
  "s_add_u32 s72, s72, 1\n"
  "ds_read_b128 v[64:67], v104 offset:12288\n"
  "ds_read_b64_tr_b16 v[202:203], v121 offset:14336\n"
  "ds_read_b64_tr_b16 v[206:207], v122 offset:14336\n"
  "v_mfma_f32_16x16x32_bf16 v[0:3], v[56:59], a[216:219], v[0:3]\n"
  "s_lshr_b32 s74, s72, 1\n"
  "v_readlane_b32 s73, v126, s74\n"
  "v_mfma_f32_16x16x32_bf16 v[4:7], v[56:59], a[220:223], v[4:7]\n"
  "ds_read_b128 v[68:71], v104 offset:14336\n"
  "s_waitcnt vmcnt(11)\n"
  "ds_read_b64_tr_b16 v[208:209], v117 offset:15360\n"
  "ds_read_b64_tr_b16 v[212:213], v118 offset:15360\n"
  "v_xor_b32 v104, s88, v104\n"
  "v_mfma_f32_16x16x32_bf16 v[4:7], v[48:51], a[196:199], v[4:7]\n"
  "s_lshl_b32 s74, s72, 4\n"
  "v_mfma_f32_16x16x32_bf16 v[8:11], v[48:51], a[200:203], v[8:11]\n"
  "s_lshr_b32 s73, s73, s74\n"
  "ds_read_b64_tr_b16 v[216:217], v119 offset:15360\n"
  "ds_read_b64_tr_b16 v[220:221], v120 offset:15360\n"
  "v_mfma_f32_16x16x32_bf16 v[8:11], v[56:59], a[224:227], v[8:11]\n"
  "s_and_b32 s73, s73, 0xffff\n"
  "ds_read_b64_tr_b16 v[224:225], v121 offset:17408\n"
  "ds_read_b64_tr_b16 v[228:229], v122 offset:17408\n"
  "v_mfma_f32_16x16x32_bf16 v[12:15], v[56:59], a[228:231], v[12:15]\n"
  "s_lshl_b32 s73, s73, 13\n"
  "ds_read_b128 v[72:75], v105 offset:12288\n"
  "v_mfma_f32_16x16x32_bf16 v[12:15], v[48:51], a[204:207], v[12:15]\n"
  "s_waitcnt vmcnt(7)\n"
  "ds_read_b64_tr_b16 v[210:211], v117 offset:18432\n"
  "ds_read_b64_tr_b16 v[214:215], v118 offset:18432\n"
  "v_mfma_f32_16x16x32_bf16 v[16:19], v[48:51], a[208:211], v[16:19]\n"
  "s_mov_b32 s69, s70\n"
  "v_mfma_f32_16x16x32_bf16 v[16:19], v[56:59], a[232:235], v[16:19]\n"
  "s_movk_i32 s70, 6144\n"
  "ds_read_b64_tr_b16 v[218:219], v119 offset:18432\n"
  "ds_read_b64_tr_b16 v[222:223], v120 offset:18432\n"
  "v_mfma_f32_16x16x32_bf16 v[20:23], v[56:59], a[236:239], v[20:23]\n"
  "ds_read_b128 v[76:79], v105 offset:14336\n"
  "ds_read_b64_tr_b16 v[226:227], v121 offset:20480\n"
  "ds_read_b64_tr_b16 v[230:231], v122 offset:20480\n"
  "v_xor_b32 v105, s88, v105\n"
  "v_mfma_f32_16x16x32_bf16 v[20:23], v[48:51], a[212:215], v[20:23]\n"
  "s_xor_b32 s88, s88, s89\n"
  "v_smfmac_f32_16x16x64_bf16 v[80:83], v[88:91], v[48:55], v124\n"
  "s_waitcnt lgkmcnt(14)\n"
  "s_waitcnt vmcnt(4)\n"
  "ds_read_b64_tr_b16 v[232:233], v117 offset:21504\n"
  "ds_read_b64_tr_b16 v[236:237], v118 offset:21504\n"
  "v_smfmac_f32_16x16x64_bf16 v[80:83], v[88:91], v[64:71], v124\n"
  "v_mfma_f32_16x16x32_bf16 v[20:23], v[52:55], v[132:135], v[20:23]\n"
  "ds_read_b64_tr_b16 v[240:241], v119 offset:21504\n"
  "ds_read_b64_tr_b16 v[244:245], v120 offset:21504\n"
  "v_mfma_f32_16x16x32_bf16 v[20:23], v[60:63], v[156:159], v[20:23]\n"
  "ds_read_b64_tr_b16 v[248:249], v121 offset:23552\n"
  "ds_read_b64_tr_b16 v[252:253], v122 offset:23552\n"
  "v_mfma_f32_16x16x32_bf16 v[16:19], v[60:63], v[152:155], v[16:19]\n"
  "v_add_f32 v44, v80, v81\n"
  "v_mov_b32 v80, 0\n"
  "v_mov_b32 v81, 0\n"
  "v_mfma_f32_16x16x32_bf16 v[16:19], v[52:55], v[128:131], v[16:19]\n"
  "s_waitcnt vmcnt(0)\n"
  "ds_read_b64_tr_b16 v[234:235], v117 offset:24576\n"
  "ds_read_b64_tr_b16 v[238:239], v118 offset:24576\n"
  "ds_write_b32 v110, v44\n"
  "v_mfma_f32_16x16x32_bf16 v[12:15], v[52:55], a[252:255], v[12:15]\n"
  "v_mfma_f32_16x16x32_bf16 v[12:15], v[60:63], v[148:151], v[12:15]\n"
  "ds_read_b64_tr_b16 v[242:243], v119 offset:24576\n"
  "ds_read_b64_tr_b16 v[246:247], v120 offset:24576\n"
  "v_mfma_f32_16x16x32_bf16 v[8:11], v[60:63], v[144:147], v[8:11]\n"
  "ds_read_b64_tr_b16 v[250:251], v121 offset:26624\n"
  "ds_read_b64_tr_b16 v[254:255], v122 offset:26624\n"
  "v_mfma_f32_16x16x32_bf16 v[8:11], v[52:55], a[248:251], v[8:11]\n"
  "v_xor_b32 v106, 0x2000, v106\n"
  "v_xor_b32 v107, 0x2000, v107\n"
  "v_mfma_f32_16x16x32_bf16 v[4:7], v[52:55], a[244:247], v[4:7]\n"
  "v_xor_b32 v110, 0x2000, v110\n"
  "v_mfma_f32_16x16x32_bf16 v[4:7], v[60:63], v[140:143], v[4:7]\n"
  "v_mfma_f32_16x16x32_bf16 v[0:3], v[60:63], v[136:139], v[0:3]\n"
  "v_mfma_f32_16x16x32_bf16 v[0:3], v[52:55], a[240:243], v[0:3]\n"
  "s_branch B_H_%=\n"
  "B_LAST_%=:\n"
  "s_waitcnt lgkmcnt(0)\n"
  "s_barrier\n"
  "v_mfma_f32_16x16x32_bf16 v[20:23], v[64:67], v[180:183], v[20:23]\n"
  "ds_read_b64_tr_b16 v[48:49], v108 offset:0\n"
  "ds_read_b64_tr_b16 v[50:51], v108 offset:32\n"
  "ds_read_b128 v[84:87], v111\n"
  "v_mfma_f32_16x16x32_bf16 v[20:23], v[72:75], v[204:207], v[20:23]\n"
  "ds_read_b64_tr_b16 v[52:53], v108 offset:2304\n"
  "ds_read_b64_tr_b16 v[54:55], v108 offset:2336\n"
  "ds_read_b128 v[96:99], v111 offset:64\n"
  "v_mfma_f32_16x16x32_bf16 v[20:23], v[68:71], v[228:231], v[20:23]\n"
  "v_xor_b32 v111, 0x2000, v111\n"
  "v_mfma_f32_16x16x32_bf16 v[44:47], v[76:79], v[252:255], v[20:23]\n"
  "v_mfma_f32_16x16x32_bf16 v[16:19], v[76:79], v[248:251], v[16:19]\n"
  "v_mfma_f32_16x16x32_bf16 v[16:19], v[68:71], v[224:227], v[16:19]\n"
  "ds_read_b64_tr_b16 v[56:57], v108 offset:4608\n"
  "ds_read_b64_tr_b16 v[58:59], v108 offset:4640\n"
  "v_xor_b32 v108, 0x2000, v108\n"
  "v_mfma_f32_16x16x32_bf16 v[16:19], v[72:75], v[200:203], v[16:19]\n"
  "v_mfma_f32_16x16x32_bf16 v[40:43], v[64:67], v[176:179], v[16:19]\n"
  "s_waitcnt lgkmcnt(2)\n"
  "buffer_store_dwordx4 v[48:51], v109, s[48:51], s68 offen offset:0 nt\n"
  "v_add_f32 v84, v84, v96\n"
  "v_add_f32 v85, v85, v97\n"
  "v_add_f32 v86, v86, v98\n"
  "v_mfma_f32_16x16x32_bf16 v[12:15], v[64:67], v[172:175], v[12:15]\n"
  "v_add_f32 v87, v87, v99\n"
  "v_max3_f32 v82, v82, v84, v85\n"
  "v_max3_f32 v82, v82, v86, v87\n"
  "v_mfma_f32_16x16x32_bf16 v[12:15], v[72:75], v[196:199], v[12:15]\n"
  "v_rcp_f32 v84, v84\n"
  "v_mfma_f32_16x16x32_bf16 v[12:15], v[68:71], v[220:223], v[12:15]\n"
  "v_rcp_f32 v85, v85\n"
  "v_mul_f32 v96, s99, v84\n"
  "v_mfma_f32_16x16x32_bf16 v[36:39], v[76:79], v[244:247], v[12:15]\n"
  "buffer_store_dwordx4 v[52:55], v109, s[48:51], s68 offen offset:128 nt\n"
  "v_rcp_f32 v86, v86\n"
  "v_mul_f32 v97, s99, v85\n"
  "v_mfma_f32_16x16x32_bf16 v[8:11], v[76:79], v[240:243], v[8:11]\n"
  "v_rcp_f32 v87, v87\n"
  "v_mul_f32 v98, s99, v86\n"
  "v_mfma_f32_16x16x32_bf16 v[8:11], v[68:71], v[216:219], v[8:11]\n"
  "v_mul_f32 v99, s99, v87\n"
  "v_fma_f32 v40, v40, v84, v96\n"
  "v_fma_f32 v44, v44, v84, v96\n"
  "v_mfma_f32_16x16x32_bf16 v[8:11], v[72:75], v[192:195], v[8:11]\n"
  "v_cvt_pk_bf16_f32 v40, v40, v44\n"
  "v_fma_f32 v41, v41, v85, v97\n"
  "v_fma_f32 v45, v45, v85, v97\n"
  "v_mfma_f32_16x16x32_bf16 v[32:35], v[64:67], v[168:171], v[8:11]\n"
  "s_waitcnt lgkmcnt(0)\n"
  "buffer_store_dwordx4 v[56:59], v109, s[48:51], s68 offen offset:256 nt\n"
  "v_cvt_pk_bf16_f32 v41, v41, v45\n"
  "v_fma_f32 v42, v42, v86, v98\n"
  "v_fma_f32 v46, v46, v86, v98\n"
  "v_mfma_f32_16x16x32_bf16 v[4:7], v[64:67], v[164:167], v[4:7]\n"
  "s_add_u32 s68, s68, s69\n"
  "ds_write2_b32 v107, v40, v41 offset0:0 offset1:72\n"
  "v_mfma_f32_16x16x32_bf16 v[4:7], v[72:75], v[188:191], v[4:7]\n"
  "s_mov_b32 s69, s70\n"
  "v_cvt_pk_bf16_f32 v42, v42, v46\n"
  "v_fma_f32 v43, v43, v87, v99\n"
  "v_fma_f32 v47, v47, v87, v99\n"
  "v_mfma_f32_16x16x32_bf16 v[4:7], v[68:71], v[212:215], v[4:7]\n"
  "s_movk_i32 s70, 6144\n"
  "v_cvt_pk_bf16_f32 v43, v43, v47\n"
  "v_fma_f32 v32, v32, v84, v96\n"
  "v_fma_f32 v33, v33, v85, v97\n"
  "v_mfma_f32_16x16x32_bf16 v[28:31], v[76:79], v[236:239], v[4:7]\n"
  "ds_write2_b32 v107, v42, v43 offset0:144 offset1:216\n"
  "v_mfma_f32_16x16x32_bf16 v[0:3], v[76:79], v[232:235], v[0:3]\n"
  "v_fma_f32 v36, v36, v84, v96\n"
  "v_fma_f32 v37, v37, v85, v97\n"
  "v_fma_f32 v34, v34, v86, v98\n"
  "v_mfma_f32_16x16x32_bf16 v[0:3], v[68:71], v[208:211], v[0:3]\n"
  "v_fma_f32 v35, v35, v87, v99\n"
  "v_fma_f32 v38, v38, v86, v98\n"
  "v_fma_f32 v39, v39, v87, v99\n"
  "v_mfma_f32_16x16x32_bf16 v[0:3], v[72:75], v[184:187], v[0:3]\n"
  "v_fma_f32 v28, v28, v84, v96\n"
  "v_fma_f32 v29, v29, v85, v97\n"
  "v_fma_f32 v30, v30, v86, v98\n"
  "v_mfma_f32_16x16x32_bf16 v[24:27], v[64:67], v[160:163], v[0:3]\n"
  "v_fma_f32 v31, v31, v87, v99\n"
  "v_max3_f32 v83, v83, v84, v85\n"
  "v_max3_f32 v83, v83, v86, v87\n"
  "s_nop 4\n"
  "v_fma_f32 v24, v24, v84, v96\n"
  "v_fma_f32 v25, v25, v85, v97\n"
  "v_cvt_pk_bf16_f32 v24, v24, v28\n"
  "v_cvt_pk_bf16_f32 v28, v25, v29\n"
  "v_cvt_pk_bf16_f32 v25, v32, v36\n"
  "v_cvt_pk_bf16_f32 v29, v33, v37\n"
  "ds_write_b64 v106, v[24:25] offset:0\n"
  "ds_write_b64 v106, v[28:29] offset:288\n"
  "v_fma_f32 v26, v26, v86, v98\n"
  "v_fma_f32 v27, v27, v87, v99\n"
  "v_cvt_pk_bf16_f32 v26, v26, v30\n"
  "v_cvt_pk_bf16_f32 v30, v27, v31\n"
  "v_cvt_pk_bf16_f32 v27, v34, v38\n"
  "v_cvt_pk_bf16_f32 v31, v35, v39\n"
  "ds_write_b64 v106, v[26:27] offset:576\n"
  "ds_write_b64 v106, v[30:31] offset:864\n"
  "v_mul_f32 v83, 0x51800000, v83\n"
  "v_add_f32 v82, v82, v83\n"
  "ds_write_b32 v104, v82\n"
  "s_waitcnt lgkmcnt(0)\n"
  "s_barrier\n"
  "ds_read_b32 v83, v105\n"
  "ds_read_b64_tr_b16 v[48:49], v108 offset:0\n"
  "ds_read_b64_tr_b16 v[50:51], v108 offset:32\n"
  "ds_read_b64_tr_b16 v[52:53], v108 offset:2304\n"
  "ds_read_b64_tr_b16 v[54:55], v108 offset:2336\n"
  "ds_read_b64_tr_b16 v[56:57], v108 offset:4608\n"
  "ds_read_b64_tr_b16 v[58:59], v108 offset:4640\n"
  "s_waitcnt lgkmcnt(4)\n"
  "buffer_store_dwordx4 v[48:51], v109, s[48:51], s68 offen offset:0 nt\n"
  "s_waitcnt lgkmcnt(2)\n"
  "buffer_store_dwordx4 v[52:55], v109, s[48:51], s68 offen offset:128 nt\n"
  "s_waitcnt lgkmcnt(0)\n"
  "buffer_store_dwordx4 v[56:59], v109, s[48:51], s68 offen offset:256 nt\n"
  "v_add_f32 v82, v82, v83\n"
  "v_cmp_ngt_f32 vcc, s71, v82\n"
  "s_nop 4\n"
  "s_cmp_lg_u64 vcc, 0\n"
  "s_cselect_b32 %0, 1, 0\n"
  "L_END_%=:\n"
  : "=&s"(bad)
  : "s"((unsigned long long)__builtin_amdgcn_kernarg_segment_ptr()), "s"(wid), "s"(wg)
  : "v0", "v1", "v2", "v3", "v4", "v5", "v6", "v7", "v8", "v9", "v10", "v11", "v12", "v13", "v14", "v15", "v16", "v17", "v18", "v19", "v20", "v21", "v22", "v23", "v24", "v25", "v26", "v27", "v28", "v29", "v30", "v31", "v32", "v33", "v34", "v35", "v36", "v37", "v38", "v39", "v40", "v41", "v42", "v43", "v44", "v45", "v46", "v47", "v48", "v49", "v50", "v51", "v52", "v53", "v54", "v55", "v56", "v57", "v58", "v59", "v60", "v61", "v62", "v63", "v64", "v65", "v66", "v67", "v68", "v69", "v70", "v71", "v72", "v73", "v74", "v75", "v76", "v77", "v78", "v79", "v80", "v81", "v82", "v83", "v84", "v85", "v86", "v87", "v88", "v89", "v90", "v91", "v92", "v93", "v94", "v95", "v96", "v97", "v98", "v99", "v100", "v101", "v102", "v103", "v104", "v105", "v106", "v107", "v108", "v109", "v110", "v111", "v112", "v113", "v114", "v115", "v116", "v117", "v118", "v119", "v120", "v121", "v122", "v123", "v124", "v125", "v126", "v127", "v128", "v129", "v130", "v131", "v132", "v133", "v134", "v135", "v136", "v137", "v138", "v139", "v140", "v141", "v142", "v143", "v144", "v145", "v146", "v147", "v148", "v149", "v150", "v151", "v152", "v153", "v154", "v155", "v156", "v157", "v158", "v159", "v160", "v161", "v162", "v163", "v164", "v165", "v166", "v167", "v168", "v169", "v170", "v171", "v172", "v173", "v174", "v175", "v176", "v177", "v178", "v179", "v180", "v181", "v182", "v183", "v184", "v185", "v186", "v187", "v188", "v189", "v190", "v191", "v192", "v193", "v194", "v195", "v196", "v197", "v198", "v199", "v200", "v201", "v202", "v203", "v204", "v205", "v206", "v207", "v208", "v209", "v210", "v211", "v212", "v213", "v214", "v215", "v216", "v217", "v218", "v219", "v220", "v221", "v222", "v223", "v224", "v225", "v226", "v227", "v228", "v229", "v230", "v231", "v232", "v233", "v234", "v235", "v236", "v237", "v238", "v239", "v240", "v241", "v242", "v243", "v244", "v245", "v246", "v247", "v248", "v249", "v250", "v251", "v252", "v253", "v254", "v255", "a0", "a1", "a2", "a3", "a4", "a5", "a6", "a7", "a8", "a9", "a10", "a11", "a12", "a13", "a14", "a15", "a16", "a17", "a18", "a19", "a20", "a21", "a22", "a23", "a24", "a25", "a26", "a27", "a28", "a29", "a30", "a31", "a32", "a33", "a34", "a35", "a36", "a37", "a38", "a39", "a40", "a41", "a42", "a43", "a44", "a45", "a46", "a47", "a48", "a49", "a50", "a51", "a52", "a53", "a54", "a55", "a56", "a57", "a58", "a59", "a60", "a61", "a62", "a63", "a64", "a65", "a66", "a67", "a68", "a69", "a70", "a71", "a72", "a73", "a74", "a75", "a76", "a77", "a78", "a79", "a80", "a81", "a82", "a83", "a84", "a85", "a86", "a87", "a88", "a89", "a90", "a91", "a92", "a93", "a94", "a95", "a96", "a97", "a98", "a99", "a100", "a101", "a102", "a103", "a104", "a105", "a106", "a107", "a108", "a109", "a110", "a111", "a112", "a113", "a114", "a115", "a116", "a117", "a118", "a119", "a120", "a121", "a122", "a123", "a124", "a125", "a126", "a127", "a128", "a129", "a130", "a131", "a132", "a133", "a134", "a135", "a136", "a137", "a138", "a139", "a140", "a141", "a142", "a143", "a144", "a145", "a146", "a147", "a148", "a149", "a150", "a151", "a152", "a153", "a154", "a155", "a156", "a157", "a158", "a159", "a160", "a161", "a162", "a163", "a164", "a165", "a166", "a167", "a168", "a169", "a170", "a171", "a172", "a173", "a174", "a175", "a176", "a177", "a178", "a179", "a180", "a181", "a182", "a183", "a184", "a185", "a186", "a187", "a188", "a189", "a190", "a191", "a192", "a193", "a194", "a195", "a196", "a197", "a198", "a199", "a200", "a201", "a202", "a203", "a204", "a205", "a206", "a207", "a208", "a209", "a210", "a211", "a212", "a213", "a214", "a215", "a216", "a217", "a218", "a219", "a220", "a221", "a222", "a223", "a224", "a225", "a226", "a227", "a228", "a229", "a230", "a231", "a232", "a233", "a234", "a235", "a236", "a237", "a238", "a239", "a240", "a241", "a242", "a243", "a244", "a245", "a246", "a247", "a248", "a249", "a250", "a251", "a252", "a253", "a254", "a255", "s40", "s41", "s42", "s43", "s44", "s45", "s46", "s47", "s48", "s49", "s50", "s51", "s52", "s53", "s54", "s55", "s56", "s57", "s58", "s59", "s60", "s61", "s62", "s63", "s64", "s65", "s66", "s67", "s68", "s69", "s70", "s71", "s72", "s73", "s74", "s75", "s76", "s77", "s78", "s79", "s80", "s81", "s82", "s83", "s84", "s85", "s86", "s87", "s88", "s89", "s90", "s91", "s92", "s93", "s94", "s95", "s96", "s97", "s98", "s99", "vcc", "scc", "memory");
  if (bad) {
    unsigned long long ka = (unsigned long long)__builtin_amdgcn_kernarg_segment_ptr();
    asm volatile("s_waitcnt vmcnt(0)\n\ts_barrier" : "+s"(ka) :: "memory");
    const unsigned short* const* arg = (const unsigned short* const*)ka;
    const int p = wg >> 4, side = (wg >> 3) & 1, x = 8 + (p * 20 >> 4), h0 = 8 * p + (wg & 7), n0 = side ? 53 - x : x;
    const int y = 53 - n0 + (wg & 1 ? -(2) : 2);
#pragma unroll 1
    for (int i = 0; i < 3; ++i) {
      const int head = i == 0 ? h0 : i == 1 ? 128 + wg : 384 + (h0 ^ side), n = i == 0 ? n0 : i == 1 ? 53 : y, t0 = side ? 53 - n : 0;
#pragma unroll 1
      for (int t = wid & 1; t < n; t += 2) exact_tile(arg[0], arg[1], arg[2], (unsigned short*)arg[3], head, 16 * (t0 + t));
    }
  }
}

extern "C" void kernel_launch(void* const* d_in, const int* in_sizes, int n_in,
                              void* d_out, int out_size, void* d_ws, size_t ws_size,
                              hipStream_t stream) {
  (void)hipFuncSetAttribute((const void*)attn_fwd_g, hipFuncAttributeMaxDynamicSharedMemorySize, LDS_BYTES);
  hipLaunchKernelGGL(attn_fwd_g, dim3(256), dim3(256), LDS_BYTES, stream, (const unsigned short*)d_in[0], (const unsigned short*)d_in[1],
                     (const unsigned short*)d_in[2], (unsigned short*)d_out);
}
```

```cpp
#include <hip/hip_runtime.h>
#include <hip/hip_bf16.h>

typedef __bf16   bf16x8 __attribute__((ext_vector_type(8)));
typedef __bf16   bf16x2 __attribute__((ext_vector_type(2)));
typedef float    f32x4  __attribute__((ext_vector_type(4)));
typedef float    f32x2  __attribute__((ext_vector_type(2)));
typedef unsigned u32x4  __attribute__((ext_vector_type(4)));
typedef unsigned u32x2  __attribute__((ext_vector_type(2)));
typedef unsigned short u16x8 __attribute__((ext_vector_type(8)));

constexpr int SQ = 848, SK = 512, DH = 192, ROWB = 384;
constexpr int LDS_BYTES = 163840;
constexpr float CSC = 0.07216878364870322f * 1.4426950408889634f;

#define DI __device__ __forceinline__
DI unsigned cvtpk(float lo, float hi) { f32x2 v = {lo, hi}; bf16x2 b = __builtin_convertvector(v, bf16x2); return __builtin_bit_cast(unsigned, b); }
DI float xmax(float x) {
  auto r = __builtin_amdgcn_permlane32_swap(__float_as_uint(x), __float_as_uint(x), false, false);
  x = fmaxf(__uint_as_float(r[0]), __uint_as_float(r[1]));
  auto q = __builtin_amdgcn_permlane16_swap(__float_as_uint(x), __float_as_uint(x), false, false);
  return fmaxf(__uint_as_float(q[0]), __uint_as_float(q[1]));
}
DI float xsum(float x) {
  auto r = __builtin_amdgcn_permlane32_swap(__float_as_uint(x), __float_as_uint(x), false, false);
  x = __uint_as_float(r[0]) + __uint_as_float(r[1]);
  auto q = __builtin_amdgcn_permlane16_swap(__float_as_uint(x), __float_as_uint(x), false, false);
  return __uint_as_float(q[0]) + __uint_as_float(q[1]);
}
DI f32x4 score_tile(const unsigned short* krow, const bf16x8 (&qf)[6]) {
  f32x4 acc = {0.f, 0.f, 0.f, 0.f};
#pragma unroll
  for (int s = 0; s < 6; ++s) acc = __builtin_amdgcn_mfma_f32_16x16x32_bf16(*(const bf16x8*)(krow + 32 * s), qf[s], acc, 0, 0, 0);
  return acc;
}
DI void exact_tile(const unsigned short* Q, const unsigned short* K, const unsigned short* V, unsigned short* O, int head, int row0) {
  const unsigned lane = __builtin_amdgcn_mbcnt_hi(~0u, __builtin_amdgcn_mbcnt_lo(~0u, 0u));
  const int c = lane & 15, g4 = lane >> 4;
  const int qrow = min(row0 + c, SQ - 1);
  bf16x8 qf[6];
#pragma unroll
  for (int s = 0; s < 6; ++s) qf[s] = *(const bf16x8*)(Q + ((size_t)head * SQ + qrow) * DH + 32 * s + 8 * g4);
  const unsigned short* Kh = K + ((size_t)head * SK + c) * DH + 8 * g4;
  float m = -__builtin_huge_valf();
#pragma unroll 1
  for (int kb = 0; kb < SK / 16; ++kb) {
    const f32x4 a = score_tile(Kh + (size_t)kb * 16 * DH, qf);
    m = fmaxf(fmaxf(m, fmaxf(a[0], a[1])), fmaxf(a[2], a[3]));
  }
  const float mc = -xmax(m) * CSC;
  float l = 0.f;
  f32x4 o[12];
#pragma unroll
  for (int dt = 0; dt < 12; ++dt) o[dt] = f32x4{0.f, 0.f, 0.f, 0.f};
#pragma unroll 1
  for (int j = 0; j < SK / 32; ++j) {
    u32x4 p;
#pragma unroll
    for (int kt = 0; kt < 2; ++kt) {
      f32x4 a = score_tile(Kh + (size_t)(32 * j + 16 * kt) * DH, qf);
#pragma unroll
      for (int r = 0; r < 4; ++r) { a[r] = __builtin_amdgcn_exp2f(fmaf(a[r], CSC, mc)); l += a[r]; }
      p[2 * kt] = cvtpk(a[0], a[1]); p[2 * kt + 1] = cvtpk(a[2], a[3]);
    }
    const unsigned short* Vj = V + ((size_t)head * SK + 32 * j + 4 * g4) * DH + c;
#pragma unroll
    for (int dt = 0; dt < 12; ++dt) {
      u16x8 vf;
#pragma unroll
      for (int i = 0; i < 8; ++i) vf[i] = Vj[(16 * (i >> 2) + (i & 3)) * DH + 16 * dt];
      o[dt] = __builtin_amdgcn_mfma_f32_16x16x32_bf16(__builtin_bit_cast(bf16x8, vf), __builtin_bit_cast(bf16x8, p), o[dt], 0, 0, 0);
    }
  }
  const float inv = 1.0f / xsum(l);
  if (row0 + c < SQ) {
#pragma unroll
    for (int dt = 0; dt < 12; ++dt)
      *(u32x2*)(O + ((size_t)head * SQ + qrow) * DH + 16 * dt + 4 * g4) = u32x2{cvtpk(o[dt][0] * inv, o[dt][1] * inv), cvtpk(o[dt][2] * inv, o[dt][3] * inv)};
  }
}

__global__ __launch_bounds__(256, 1)
void attn_fwd_g(const unsigned short* __restrict__ Qg, const unsigned short* __restrict__ Kg,
                const unsigned short* __restrict__ Vg, unsigned short* __restrict__ Og) {
  const int wid = __builtin_amdgcn_readfirstlane(threadIdx.x >> 6);
  const int wg = blockIdx.x;
  unsigned bad;
  asm volatile(
  "s_load_dwordx8 s[76:83], %1, 0\n"
  "L_PC_%=:\n"
  "s_getpc_b64 s[84:85]\n"
  "v_mbcnt_lo_u32_b32 v0, -1, 0\n"
  "v_mbcnt_hi_u32_b32 v0, -1, v0\n"
  "v_lshrrev_b32 v2, 3, v0\n"
  "v_and_b32 v1, 7, v0\n"
  "v_xor_b32 v1, v1, v2\n"
  "v_mul_u32_u24 v123, 0x180, v2\n"
  "v_lshl_add_u32 v123, v1, 4, v123\n"
  "s_lshr_b32 s87, %3, 4\n"
  "s_and_b32 s63, %3, 7\n"
  "s_lshl3_add_u32 s86, s87, s63\n"
  "s_and_b32 s65, %2, 1\n"
  "s_mul_i32 s59, s65, 30720\n"
  "s_add_u32 s59, s59, 102400\n"
  "s_cmp_gt_u32 %2, 1\n"
  "s_cbranch_scc0 A_START_%=\n"
  "s_lshr_b32 s62, %3, 3\n"
  "s_lshl1_add_u32 s62, s62, s65\n"
  "s_lshl_b32 s62, s62, 13\n"
  "s_add_u32 s48, s84, s62\n"
  "s_addc_u32 s49, s85, 0\n"
  "s_sub_i32 s50, L_END_%=-L_PC_%=, s62\n"
  "s_max_i32 s50, s50, 0\n"
  "s_mov_b32 s51, 0x20000\n"
  "s_mov_b32 m0, 7168\n"
  "v_lshlrev_b32 v125, 7, v0\n"
  "buffer_load_dword v125, s[48:51], 0 offen lds\n"
  "s_mul_i32 s75, s87, 20\n"
  "s_lshr_b32 s75, s75, 4\n"
  "s_add_u32 s75, s75, 8\n"
  "s_bitcmp1_b32 %3, 3\n"
  "s_cselect_b32 s62, s75, 0\n"
  "s_mul_i32 s63, s86, 53\n"
  "s_add_u32 s62, s62, s63\n"
  "s_mul_i32 s67, s62, 6144\n"
  "s_add_u32 s64, s67, 6144\n"
  "s_lshl_b32 s66, s65, 10\n"
  "s_add_u32 s66, s66, 65536\n"
  "s_mul_i32 s62, s65, 3072\n"
  "s_sub_u32 s46, 166723584, s62\n"
  "s_mov_b32 s47, 0x20000\n"
  "s_waitcnt lgkmcnt(0)\n"
  "s_add_u32 s44, s76, s62\n"
  "s_addc_u32 s45, s77, 0\n"
  "s_add_u32 m0, s66, 0\n"
  "s_nop 0\n"
  "buffer_load_dwordx4 v123, s[44:47], s67 offen offset:0 nt lds\n"
  "s_add_u32 m0, s66, 1920\n"
  "s_nop 0\n"
  "buffer_load_dwordx4 v123, s[44:47], s67 offen offset:128 nt lds\n"
  "s_add_u32 m0, s66, 3840\n"
  "s_nop 0\n"
  "buffer_load_dwordx4 v123, s[44:47], s67 offen offset:256 nt lds\n"
  "s_add_u32 m0, s66, 6144\n"
  "s_nop 0\n"
  "buffer_load_dwordx4 v123, s[44:47], s64 offen offset:0 nt lds\n"
  "s_add_u32 m0, s66, 8064\n"
  "s_nop 0\n"
  "buffer_load_dwordx4 v123, s[44:47], s64 offen offset:128 nt lds\n"
  "s_add_u32 m0, s66, 9984\n"
  "s_nop 0\n"
  "buffer_load_dwordx4 v123, s[44:47], s64 offen offset:256 nt lds\n"
  "s_branch B_START_%=\n"
  "A_START_%=:\n"
  "s_lshl1_add_u32 s62, s86, s65\n"
  "s_mul_i32 s57, s62, 98304\n"
  "v_and_b32 v3, 2, v2\n"
  "v_and_b32 v112, 4, v2\n"
  "v_lshl_add_u32 v112, v3, 3, v112\n"
  "v_and_b32 v3, 1, v2\n"
  "v_lshl_add_u32 v112, v3, 1, v112\n"
  "v_mul_u32_u24 v112, 0x180, v112\n"
  "v_lshl_add_u32 v112, v1, 4, v112\n"
  "v_add_u32 v113, 0x800, v112\n"
  "s_mov_b32 s42, 100663296\n"
  "s_mov_b32 s43, 0x20000\n"
  "s_waitcnt lgkmcnt(0)\n"
  "s_mov_b64 s[40:41], s[78:79]\n"
  "s_add_u32 m0, s59, 0\n"
  "s_nop 0\n"
  "buffer_load_dwordx4 v112, s[40:43], s57 offen offset:0  lds\n"
  "buffer_load_dwordx4 v113, s[40:43], s57 offen offset:1024  lds\n"
  "s_add_u32 m0, s59, 1920\n"
  "s_nop 0\n"
  "buffer_load_dwordx4 v112, s[40:43], s57 offen offset:128  lds\n"
  "buffer_load_dwordx4 v113, s[40:43], s57 offen offset:1152  lds\n"
  "s_add_u32 m0, s59, 3840\n"
  "s_nop 0\n"
  "buffer_load_dwordx4 v112, s[40:43], s57 offen offset:256  lds\n"
  "buffer_load_dwordx4 v113, s[40:43], s57 offen offset:1280  lds\n"
  "s_add_u32 s57, s57, 384\n"
  "s_add_u32 m0, s59, 5888\n"
  "s_nop 0\n"
  "buffer_load_dwordx4 v112, s[40:43], s57 offen offset:256  lds\n"
  "buffer_load_dwordx4 v113, s[40:43], s57 offen offset:1280  lds\n"
  "s_add_u32 m0, s59, 8064\n"
  "s_nop 0\n"
  "buffer_load_dwordx4 v112, s[40:43], s57 offen offset:128  lds\n"
  "buffer_load_dwordx4 v113, s[40:43], s57 offen offset:1152  lds\n"
  "s_add_u32 m0, s59, 10240\n"
  "s_nop 0\n"
  "buffer_load_dwordx4 v112, s[40:43], s57 offen offset:0  lds\n"
  "buffer_load_dwordx4 v113, s[40:43], s57 offen offset:1024  lds\n"
  "s_add_u32 s57, s57, 11904\n"
  "s_add_u32 m0, s59, 12288\n"
  "s_nop 0\n"
  "buffer_load_dwordx4 v112, s[40:43], s57 offen offset:0  lds\n"
  "buffer_load_dwordx4 v113, s[40:43], s57 offen offset:1024  lds\n"
  "s_add_u32 m0, s59, 14208\n"
  "s_nop 0\n"
  "buffer_load_dwordx4 v112, s[40:43], s57 offen offset:128  lds\n"
  "buffer_load_dwordx4 v113, s[40:43], s57 offen offset:1152  lds\n"
  "s_add_u32 m0, s59, 16128\n"
  "s_nop 0\n"
  "buffer_load_dwordx4 v112, s[40:43], s57 offen offset:256  lds\n"
  "buffer_load_dwordx4 v113, s[40:43], s57 offen offset:1280  lds\n"
  "s_add_u32 s57, s57, 384\n"
  "s_add_u32 m0, s59, 18176\n"
  "s_nop 0\n"
  "buffer_load_dwordx4 v112, s[40:43], s57 offen offset:256  lds\n"
  "buffer_load_dwordx4 v113, s[40:43], s57 offen offset:1280  lds\n"
  "s_mul_i32 s75, s87, 20\n"
  "s_lshr_b32 s75, s75, 4\n"
  "s_add_u32 s75, s75, 8\n"
  "s_sub_u32 s62, 53, s75\n"
  "s_bitcmp1_b32 %3, 3\n"
  "s_cselect_b32 s52, s62, s75\n"
  "s_cselect_b32 s54, s75, s62\n"
  "s_cselect_b32 s62, s75, 0\n"
  "s_lshl_b32 s62, s62, 8\n"
  "s_or_b32 s52, s52, s62\n"
  "s_pack_ll_b32_b16 s52, s52, s86\n"
  "s_add_u32 s62, %3, 128\n"
  "s_pack_ll_b32_b16 s53, 53, s62\n"
  "s_bitcmp1_b32 %3, 0\n"
  "s_cselect_b32 s62, -2, 2\n"
  "s_add_u32 s54, s54, s62\n"
  "s_sub_u32 s62, 53, s54\n"
  "s_lshl_b32 s62, s62, 8\n"
  "s_bfe_u32 s63, %3, 0x10003\n"
  "s_cselect_b32 s62, s62, 0\n"
  "s_or_b32 s54, s54, s62\n"
  "s_xor_b32 s63, s63, s86\n"
  "s_add_u32 s63, s63, 384\n"
  "s_pack_ll_b32_b16 s54, s54, s63\n"
  "s_mov_b32 s55, 0\n"
  "s_and_b32 s56, s52, 0xff\n"
  "s_cmp_lg_u32 s53, 0\n"
  "s_cselect_b32 s62, 1, 0\n"
  "s_sub_u32 s56, s56, s62\n"
  "v_and_b32 v2, 7, v0\n"
  "v_lshrrev_b32 v83, 4, v0\n"
  "v_xor_b32 v83, v83, v2\n"
  "v_lshlrev_b32 v83, 4, v83\n"
  "v_lshl_add_u32 v83, v2, 7, v83\n"
  "v_bfe_u32 v1, v0, 3, 1\n"
  "v_lshl_add_u32 v83, v1, 10, v83\n"
  "v_xor_b32 v84, 64, v83\n"
  "v_add_u32 v87, s59, v83\n"
  "v_add_u32 v88, s59, v84\n"
  "s_mov_b32 s66, 65536\n"
  "v_add_u32 v81, s66, v83\n"
  "v_add_u32 v82, s66, v84\n"
  "s_lshl_b32 s89, s65, 11\n"
  "s_add_u32 s89, s89, 16128\n"
  "s_mov_b32 s88, s89\n"
  "s_add_u32 s68, s89, 49344\n"
  "s_mov_b32 s60, 0x3dd53b94\n"
  "s_mov_b32 s61, 0x409fecaf\n"
  "v_mov_b32 v92, 0xc2400000\n"
  "v_mov_b32 v93, 0xc2400000\n"
  "v_mov_b32 v94, 0xc2400000\n"
  "v_mov_b32 v95, 0xc2400000\n"
  "s_waitcnt vmcnt(18)\n"
  "ds_read_b128 a[0:3], v87 offset:0\n"
  "ds_read_b128 a[4:7], v88 offset:0\n"
  "s_waitcnt vmcnt(16)\n"
  "ds_read_b128 a[8:11], v87 offset:2048\n"
  "ds_read_b128 a[12:15], v88 offset:2048\n"
  "s_add_u32 m0, s59, -128\n"
  "s_waitcnt lgkmcnt(2)\n"
  "buffer_load_dwordx4 v112, s[40:43], s57 offen offset:128  lds\n"
  "buffer_load_dwordx4 v113, s[40:43], s57 offen offset:1152  lds\n"
  "s_mov_b32 m0, s88\n"
  "s_waitcnt vmcnt(16)\n"
  "ds_read_b128 a[16:19], v87 offset:4096\n"
  "ds_read_b128 a[20:23], v88 offset:4096\n"
  "s_add_u32 m0, s59, 2048\n"
  "s_waitcnt lgkmcnt(2)\n"
  "buffer_load_dwordx4 v112, s[40:43], s57 offen offset:0  lds\n"
  "buffer_load_dwordx4 v113, s[40:43], s57 offen offset:1024  lds\n"
  "s_mov_b32 m0, s88\n"
  "s_add_u32 s57, s57, 11904\n"
  "s_waitcnt vmcnt(16)\n"
  "ds_read_b128 a[40:43], v87 offset:6144\n"
  "ds_read_b128 a[44:47], v88 offset:6144\n"
  "s_add_u32 m0, s59, 4096\n"
  "s_waitcnt lgkmcnt(2)\n"
  "buffer_load_dwordx4 v112, s[40:43], s57 offen offset:0  lds\n"
  "buffer_load_dwordx4 v113, s[40:43], s57 offen offset:1024  lds\n"
  "s_mov_b32 m0, s88\n"
  "s_waitcnt vmcnt(16)\n"
  "ds_read_b128 a[32:35], v87 offset:8192\n"
  "ds_read_b128 a[36:39], v88 offset:8192\n"
  "s_add_u32 m0, s59, 6016\n"
  "s_waitcnt lgkmcnt(2)\n"
  "buffer_load_dwordx4 v112, s[40:43], s57 offen offset:128  lds\n"
  "buffer_load_dwordx4 v113, s[40:43], s57 offen offset:1152  lds\n"
  "s_mov_b32 m0, s88\n"
  "s_waitcnt vmcnt(16)\n"
  "ds_read_b128 a[24:27], v87 offset:10240\n"
  "ds_read_b128 a[28:31], v88 offset:10240\n"
  "s_add_u32 m0, s59, 7936\n"
  "s_waitcnt lgkmcnt(2)\n"
  "buffer_load_dwordx4 v112, s[40:43], s57 offen offset:256  lds\n"
  "buffer_load_dwordx4 v113, s[40:43], s57 offen offset:1280  lds\n"
  "s_mov_b32 m0, s88\n"
  "s_add_u32 s57, s57, 384\n"
  "s_waitcnt vmcnt(16)\n"
  "ds_read_b128 a[48:51], v87 offset:12288\n"
  "ds_read_b128 a[52:55], v88 offset:12288\n"
  "s_add_u32 m0, s59, 9984\n"
  "s_waitcnt lgkmcnt(2)\n"
  "buffer_load_dwordx4 v112, s[40:43], s57 offen offset:256  lds\n"
  "buffer_load_dwordx4 v113, s[40:43], s57 offen offset:1280  lds\n"
  "s_mov_b32 m0, s88\n"
  "s_waitcnt vmcnt(16)\n"
  "ds_read_b128 a[56:59], v87 offset:14336\n"
  "ds_read_b128 a[60:63], v88 offset:14336\n"
  "s_add_u32 m0, s59, 12160\n"
  "s_waitcnt lgkmcnt(2)\n"
  "buffer_load_dwordx4 v112, s[40:43], s57 offen offset:128  lds\n"
  "buffer_load_dwordx4 v113, s[40:43], s57 offen offset:1152  lds\n"
  "s_mov_b32 m0, s88\n"
  "s_waitcnt vmcnt(16)\n"
  "ds_read_b128 a[64:67], v87 offset:16384\n"
  "ds_read_b128 a[68:71], v88 offset:16384\n"
  "s_add_u32 m0, s59, 14336\n"
  "s_waitcnt lgkmcnt(2)\n"
  "buffer_load_dwordx4 v112, s[40:43], s57 offen offset:0  lds\n"
  "buffer_load_dwordx4 v113, s[40:43], s57 offen offset:1024  lds\n"
  "s_mov_b32 m0, s88\n"
  "s_add_u32 s57, s57, 11904\n"
  "s_waitcnt vmcnt(16)\n"
  "ds_read_b128 a[88:91], v87 offset:18432\n"
  "ds_read_b128 a[92:95], v88 offset:18432\n"
  "s_add_u32 m0, s59, 16384\n"
  "s_waitcnt lgkmcnt(2)\n"
  "buffer_load_dwordx4 v112, s[40:43], s57 offen offset:0  lds\n"
  "buffer_load_dwordx4 v113, s[40:43], s57 offen offset:1024  lds\n"
  "s_mov_b32 m0, s88\n"
  "s_waitcnt vmcnt(16)\n"
  "ds_read_b128 a[80:83], v87 offset:0\n"
  "ds_read_b128 a[84:87], v88 offset:0\n"
  "s_barrier\n"
  "ds_read_b128 v[32:35], v81 offset:0\n"
  "ds_read_b128 v[36:39], v82 offset:0\n"
  "ds_read_b128 v[40:43], v81 offset:2048\n"
  "ds_read_b128 v[44:47], v82 offset:2048\n"
  "ds_read_b128 v[48:51], v81 offset:4096\n"
  "ds_read_b128 v[52:55], v82 offset:4096\n"
  "s_waitcnt lgkmcnt(0)\n"
  ".p2align 3\n"
  "v_mfma_f32_16x16x32_bf16 v[0:3], v[32:35], a[0:3], v[92:95]\n"
  "s_add_u32 s66, s66, 6144\n"
  "s_cmp_eq_u32 s66, 102400\n"
  "s_cselect_b32 s66, 65536, s66\n"
  "s_add_u32 m0, s59, 18304\n"
  "s_nop 0\n"
  "buffer_load_dwordx4 v112, s[40:43], s57 offen offset:128  lds\n"
  "buffer_load_dwordx4 v113, s[40:43], s57 offen offset:1152  lds\n"
  "s_mov_b32 m0, s88\n"
  "v_mfma_f32_16x16x32_bf16 v[0:3], v[36:39], a[4:7], v[0:3]\n"
  "s_waitcnt vmcnt(16)\n"
  "ds_read_b128 a[72:75], v87 offset:2048\n"
  "ds_read_b128 a[76:79], v88 offset:2048\n"
  "v_mfma_f32_16x16x32_bf16 v[0:3], v[40:43], a[8:11], v[0:3]\n"
  "s_add_u32 m0, s59, -256\n"
  "s_nop 0\n"
  "buffer_load_dwordx4 v112, s[40:43], s57 offen offset:256  lds\n"
  "buffer_load_dwordx4 v113, s[40:43], s57 offen offset:1280  lds\n"
  "s_mov_b32 m0, s88\n"
  "s_add_u32 s57, s57, 384\n"
  "v_mfma_f32_16x16x32_bf16 v[0:3], v[44:47], a[12:15], v[0:3]\n"
  "s_waitcnt vmcnt(16)\n"
  "ds_read_b128 a[96:99], v87 offset:4096\n"
  "ds_read_b128 a[100:103], v88 offset:4096\n"
  "v_mfma_f32_16x16x32_bf16 v[0:3], v[48:51], a[16:19], v[0:3]\n"
  "s_add_u32 m0, s59, 1792\n"
  "s_waitcnt lgkmcnt(2)\n"
  "buffer_load_dwordx4 v112, s[40:43], s57 offen offset:256  lds\n"
  "buffer_load_dwordx4 v113, s[40:43], s57 offen offset:1280  lds\n"
  "s_mov_b32 m0, s88\n"
  "v_mfma_f32_16x16x32_bf16 v[0:3], v[52:55], a[20:23], v[0:3]\n"
  "s_waitcnt vmcnt(16)\n"
  "ds_read_b128 a[104:107], v87 offset:6144\n"
  "ds_read_b128 a[108:111], v88 offset:6144\n"
  "v_add_u32 v81, s66, v83\n"
  "v_add_u32 v82, s66, v84\n"
  "v_mfma_f32_16x16x32_bf16 v[4:7], v[52:55], a[44:47], v[92:95]\n"
  "s_add_u32 m0, s59, 3968\n"
  "s_waitcnt lgkmcnt(2)\n"
  "buffer_load_dwordx4 v112, s[40:43], s57 offen offset:128  lds\n"
  "buffer_load_dwordx4 v113, s[40:43], s57 offen offset:1152  lds\n"
  "s_mov_b32 m0, s88\n"
  "v_mfma_f32_16x16x32_bf16 v[4:7], v[48:51], a[40:43], v[4:7]\n"
  "s_waitcnt vmcnt(16)\n"
  "ds_read_b128 a[112:115], v87 offset:8192\n"
  "ds_read_b128 a[116:119], v88 offset:8192\n"
  "v_mfma_f32_16x16x32_bf16 v[4:7], v[44:47], a[36:39], v[4:7]\n"
  "s_add_u32 m0, s59, 6144\n"
  "s_waitcnt lgkmcnt(2)\n"
  "buffer_load_dwordx4 v112, s[40:43], s57 offen offset:0  lds\n"
  "buffer_load_dwordx4 v113, s[40:43], s57 offen offset:1024  lds\n"
  "s_mov_b32 m0, s88\n"
  "s_add_u32 s57, s57, 11904\n"
  "v_mfma_f32_16x16x32_bf16 v[4:7], v[40:43], a[32:35], v[4:7]\n"
  "s_waitcnt vmcnt(16)\n"
  "ds_read_b128 a[136:139], v87 offset:10240\n"
  "ds_read_b128 a[140:143], v88 offset:10240\n"
  "v_mfma_f32_16x16x32_bf16 v[4:7], v[36:39], a[28:31], v[4:7]\n"
  "s_add_u32 m0, s59, 8192\n"
  "s_waitcnt lgkmcnt(2)\n"
  "buffer_load_dwordx4 v112, s[40:43], s57 offen offset:0  lds\n"
  "buffer_load_dwordx4 v113, s[40:43], s57 offen offset:1024  lds\n"
  "s_mov_b32 m0, s88\n"
  "v_mfma_f32_16x16x32_bf16 v[4:7], v[32:35], a[24:27], v[4:7]\n"
  "s_waitcnt vmcnt(16)\n"
  "ds_read_b128 a[128:131], v87 offset:12288\n"
  "ds_read_b128 a[132:135], v88 offset:12288\n"
  "v_mfma_f32_16x16x32_bf16 v[8:11], v[32:35], a[48:51], v[92:95]\n"
  "s_add_u32 m0, s59, 10112\n"
  "s_waitcnt lgkmcnt(2)\n"
  "buffer_load_dwordx4 v112, s[40:43], s57 offen offset:128  lds\n"
  "buffer_load_dwordx4 v113, s[40:43], s57 offen offset:1152  lds\n"
  "s_mov_b32 m0, s88\n"
  "v_mfma_f32_16x16x32_bf16 v[8:11], v[36:39], a[52:55], v[8:11]\n"
  "s_waitcnt vmcnt(16)\n"
  "ds_read_b128 a[120:123], v87 offset:14336\n"
  "ds_read_b128 a[124:127], v88 offset:14336\n"
  "v_mfma_f32_16x16x32_bf16 v[8:11], v[40:43], a[56:59], v[8:11]\n"
  "s_add_u32 m0, s59, 12032\n"
  "s_waitcnt lgkmcnt(2)\n"
  "buffer_load_dwordx4 v112, s[40:43], s57 offen offset:256  lds\n"
  "buffer_load_dwordx4 v113, s[40:43], s57 offen offset:1280  lds\n"
  "s_mov_b32 m0, s88\n"
  "s_add_u32 s57, s57, 384\n"
  "v_mfma_f32_16x16x32_bf16 v[8:11], v[44:47], a[60:63], v[8:11]\n"
  "s_waitcnt vmcnt(16)\n"
  "ds_read_b128 a[144:147], v87 offset:16384\n"
  "ds_read_b128 a[148:151], v88 offset:16384\n"
  "v_mfma_f32_16x16x32_bf16 v[8:11], v[48:51], a[64:67], v[8:11]\n"
  "s_add_u32 m0, s59, 14080\n"
  "s_waitcnt lgkmcnt(2)\n"
  "buffer_load_dwordx4 v112, s[40:43], s57 offen offset:256  lds\n"
  "buffer_load_dwordx4 v113, s[40:43], s57 offen offset:1280  lds\n"
  "s_mov_b32 m0, s88\n"
  "v_mfma_f32_16x16x32_bf16 v[8:11], v[52:55], a[68:71], v[8:11]\n"
  "s_waitcnt vmcnt(16)\n"
  "ds_read_b128 a[152:155], v87 offset:18432\n"
  "ds_read_b128 a[156:159], v88 offset:18432\n"
  "v_mfma_f32_16x16x32_bf16 v[12:15], v[52:55], a[92:95], v[92:95]\n"
  "s_add_u32 m0, s59, 16256\n"
  "s_waitcnt lgkmcnt(2)\n"
  "buffer_load_dwordx4 v112, s[40:43], s57 offen offset:128  lds\n"
  "buffer_load_dwordx4 v113, s[40:43], s57 offen offset:1152  lds\n"
  "s_mov_b32 m0, s88\n"
  "v_mfma_f32_16x16x32_bf16 v[12:15], v[48:51], a[88:91], v[12:15]\n"
  "s_waitcnt vmcnt(16)\n"
  "ds_read_b128 a[160:163], v87 offset:0\n"
  "ds_read_b128 a[164:167], v88 offset:0\n"
  "v_mfma_f32_16x16x32_bf16 v[12:15], v[44:47], a[84:87], v[12:15]\n"
  "s_add_u32 m0, s59, 18432\n"
  "s_waitcnt lgkmcnt(2)\n"
  "buffer_load_dwordx4 v112, s[40:43], s57 offen offset:0  lds\n"
  "buffer_load_dwordx4 v113, s[40:43], s57 offen offset:1024  lds\n"
  "s_mov_b32 m0, s88\n"
  "s_add_u32 s57, s57, 11904\n"
  "v_mfma_f32_16x16x32_bf16 v[12:15], v[40:43], a[80:83], v[12:15]\n"
  "s_waitcnt vmcnt(16)\n"
  "ds_read_b128 a[184:187], v87 offset:2048\n"
  "ds_read_b128 a[188:191], v88 offset:2048\n"
  "v_mfma_f32_16x16x32_bf16 v[12:15], v[36:39], a[76:79], v[12:15]\n"
  "s_add_u32 m0, s59, 0\n"
  "s_waitcnt lgkmcnt(2)\n"
  "buffer_load_dwordx4 v112, s[40:43], s57 offen offset:0  lds\n"
  "buffer_load_dwordx4 v113, s[40:43], s57 offen offset:1024  lds\n"
  "s_mov_b32 m0, s88\n"
  "v_mfma_f32_16x16x32_bf16 v[12:15], v[32:35], a[72:75], v[12:15]\n"
  "s_waitcnt vmcnt(16)\n"
  "ds_read_b128 a[176:179], v87 offset:4096\n"
  "ds_read_b128 a[180:183], v88 offset:4096\n"
  "v_mfma_f32_16x16x32_bf16 v[16:19], v[32:35], a[96:99], v[92:95]\n"
  "s_add_u32 m0, s59, 1920\n"
  "s_waitcnt lgkmcnt(2)\n"
  "buffer_load_dwordx4 v112, s[40:43], s57 offen offset:128  lds\n"
  "buffer_load_dwordx4 v113, s[40:43], s57 offen offset:1152  lds\n"
  "s_mov_b32 m0, s88\n"
  "v_mfma_f32_16x16x32_bf16 v[16:19], v[36:39], a[100:103], v[16:19]\n"
  "s_waitcnt vmcnt(16)\n"
  "ds_read_b128 a[168:171], v87 offset:6144\n"
  "ds_read_b128 a[172:175], v88 offset:6144\n"
  "v_mfma_f32_16x16x32_bf16 v[16:19], v[40:43], a[104:107], v[16:19]\n"
  "s_add_u32 m0, s59, 3840\n"
  "s_waitcnt lgkmcnt(2)\n"
  "buffer_load_dwordx4 v112, s[40:43], s57 offen offset:256  lds\n"
  "buffer_load_dwordx4 v113, s[40:43], s57 offen offset:1280  lds\n"
  "s_mov_b32 m0, s88\n"
  "s_add_u32 s57, s57, 384\n"
  "v_mfma_f32_16x16x32_bf16 v[16:19], v[44:47], a[108:111], v[16:19]\n"
  "s_waitcnt vmcnt(16)\n"
  "ds_read_b128 a[192:195], v87 offset:8192\n"
  "ds_read_b128 a[196:199], v88 offset:8192\n"
  "v_mfma_f32_16x16x32_bf16 v[16:19], v[48:51], a[112:115], v[16:19]\n"
  "s_add_u32 m0, s59, 5888\n"
  "s_waitcnt lgkmcnt(2)\n"
  "buffer_load_dwordx4 v112, s[40:43], s57 offen offset:256  lds\n"
  "buffer_load_dwordx4 v113, s[40:43], s57 offen offset:1280  lds\n"
  "s_mov_b32 m0, s88\n"
  "v_mfma_f32_16x16x32_bf16 v[16:19], v[52:55], a[116:119], v[16:19]\n"
  "s_waitcnt vmcnt(16)\n"
  "ds_read_b128 a[200:203], v87 offset:10240\n"
  "ds_read_b128 a[204:207], v88 offset:10240\n"
  "v_pk_mul_f32 v[0:1], v[0:1], s[60:61] op_sel_hi:[1,0]\n"
  "v_pk_mul_f32 v[2:3], v[2:3], s[60:61] op_sel_hi:[1,0]\n"
  "v_pk_mul_f32 v[4:5], v[4:5], s[60:61] op_sel_hi:[1,0]\n"
  "v_pk_mul_f32 v[6:7], v[6:7], s[60:61] op_sel_hi:[1,0]\n"
  "v_pk_mul_f32 v[8:9], v[8:9], s[60:61] op_sel_hi:[1,0]\n"
  "v_pk_mul_f32 v[10:11], v[10:11], s[60:61] op_sel_hi:[1,0]\n"
  "v_pk_mul_f32 v[12:13], v[12:13], s[60:61] op_sel_hi:[1,0]\n"
  "v_pk_mul_f32 v[14:15], v[14:15], s[60:61] op_sel_hi:[1,0]\n"
  "v_mfma_f32_16x16x32_bf16 v[20:23], v[52:55], a[140:143], v[92:95]\n"
  "s_add_u32 m0, s59, 8064\n"
  "s_waitcnt lgkmcnt(2)\n"
  "buffer_load_dwordx4 v112, s[40:43], s57 offen offset:128  lds\n"
  "buffer_load_dwordx4 v113, s[40:43], s57 offen offset:1152  lds\n"
  "s_mov_b32 m0, s88\n"
  "v_exp_f32 v0, v0\n"
  "v_mfma_f32_16x16x32_bf16 v[20:23], v[48:51], a[136:139], v[20:23]\n"
  "s_waitcnt vmcnt(16)\n"
  "ds_read_b128 a[208:211], v87 offset:12288\n"
  "ds_read_b128 a[212:215], v88 offset:12288\n"
  "v_exp_f32 v4, v4\n"
  "v_mfma_f32_16x16x32_bf16 v[20:23], v[44:47], a[132:135], v[20:23]\n"
  "s_add_u32 m0, s59, 10240\n"
  "s_waitcnt lgkmcnt(2)\n"
  "buffer_load_dwordx4 v112, s[40:43], s57 offen offset:0  lds\n"
  "buffer_load_dwordx4 v113, s[40:43], s57 offen offset:1024  lds\n"
  "s_mov_b32 m0, s88\n"
  "s_add_u32 s57, s57, 11904\n"
  "v_exp_f32 v1, v1\n"
  "v_mfma_f32_16x16x32_bf16 v[20:23], v[40:43], a[128:131], v[20:23]\n"
  "s_waitcnt vmcnt(16)\n"
  "ds_read_b128 a[232:235], v87 offset:14336\n"
  "ds_read_b128 a[236:239], v88 offset:14336\n"
  "v_exp_f32 v5, v5\n"
  "v_mfma_f32_16x16x32_bf16 v[20:23], v[36:39], a[124:127], v[20:23]\n"
  "s_add_u32 m0, s59, 12288\n"
  "s_waitcnt lgkmcnt(2)\n"
  "buffer_load_dwordx4 v112, s[40:43], s57 offen offset:0  lds\n"
  "buffer_load_dwordx4 v113, s[40:43], s57 offen offset:1024  lds\n"
  "s_mov_b32 m0, s88\n"
  "v_cvt_pk_bf16_f32 v0, v0, v4\n"
  "v_cvt_pk_bf16_f32 v1, v1, v5\n"
  "v_mfma_f32_16x16x32_bf16 v[20:23], v[32:35], a[120:123], v[20:23]\n"
  "s_waitcnt vmcnt(16)\n"
  "ds_read_b128 a[224:227], v87 offset:16384\n"
  "ds_read_b128 a[228:231], v88 offset:16384\n"
  "ds_write_addtid_b32 v0 offset:0\n"
  "ds_write_addtid_b32 v1 offset:4096\n"
  "v_mfma_f32_16x16x32_bf16 v[24:27], v[32:35], a[144:147], v[92:95]\n"
  "s_add_u32 m0, s59, 14208\n"
  "s_waitcnt lgkmcnt(4)\n"
  "buffer_load_dwordx4 v112, s[40:43], s57 offen offset:128  lds\n"
  "buffer_load_dwordx4 v113, s[40:43], s57 offen offset:1152  lds\n"
  "s_mov_b32 m0, s88\n"
  "v_exp_f32 v2, v2\n"
  "v_mfma_f32_16x16x32_bf16 v[24:27], v[36:39], a[148:151], v[24:27]\n"
  "s_waitcnt vmcnt(16)\n"
  "ds_read_b128 a[216:219], v87 offset:18432\n"
  "ds_read_b128 a[220:223], v88 offset:18432\n"
  "v_exp_f32 v6, v6\n"
  "v_mfma_f32_16x16x32_bf16 v[24:27], v[40:43], a[152:155], v[24:27]\n"
  "s_add_u32 m0, s59, 16128\n"
  "s_waitcnt lgkmcnt(4)\n"
  "buffer_load_dwordx4 v112, s[40:43], s57 offen offset:256  lds\n"
  "buffer_load_dwordx4 v113, s[40:43], s57 offen offset:1280  lds\n"
  "s_mov_b32 m0, s88\n"
  "s_add_u32 s57, s57, 384\n"
  "v_exp_f32 v3, v3\n"
  "v_mfma_f32_16x16x32_bf16 v[24:27], v[44:47], a[156:159], v[24:27]\n"
  "s_waitcnt vmcnt(16)\n"
  "ds_read_b128 a[240:243], v87 offset:0\n"
  "ds_read_b128 a[244:247], v88 offset:0\n"
  "v_exp_f32_e64 v7, v7\n"
  "v_mfma_f32_16x16x32_bf16 v[24:27], v[48:51], a[160:163], v[24:27]\n"
  "s_add_u32 m0, s59, 18176\n"
  "s_waitcnt lgkmcnt(2)\n"
  "buffer_load_dwordx4 v112, s[40:43], s57 offen offset:256  lds\n"
  "buffer_load_dwordx4 v113, s[40:43], s57 offen offset:1280  lds\n"
  "s_mov_b32 m0, s88\n"
  "v_cvt_pk_bf16_f32 v2, v2, v6\n"
  "v_cvt_pk_bf16_f32 v3, v3, v7\n"
  "v_mfma_f32_16x16x32_bf16 v[24:27], v[52:55], a[164:167], v[24:27]\n"
  "s_waitcnt vmcnt(16)\n"
  "ds_read_b128 a[248:251], v87 offset:2048\n"
  "ds_read_b128 a[252:255], v88 offset:2048\n"
  "ds_write_addtid_b32 v2 offset:8224\n"
  "ds_write_addtid_b32 v3 offset:12320\n"
  "v_mfma_f32_16x16x32_bf16 v[28:31], v[52:55], a[188:191], v[92:95]\n"
  "s_add_u32 m0, s59, -128\n"
  "s_waitcnt lgkmcnt(4)\n"
  "buffer_load_dwordx4 v112, s[40:43], s57 offen offset:128  lds\n"
  "buffer_load_dwordx4 v113, s[40:43], s57 offen offset:1152  lds\n"
  "s_mov_b32 m0, s88\n"
  "v_exp_f32 v8, v8\n"
  "v_mfma_f32_16x16x32_bf16 v[28:31], v[48:51], a[184:187], v[28:31]\n"
  "s_waitcnt vmcnt(16)\n"
  "ds_read_b128 v[128:131], v87 offset:4096\n"
  "ds_read_b128 v[132:135], v88 offset:4096\n"
  "v_exp_f32 v12, v12\n"
  "v_mfma_f32_16x16x32_bf16 v[28:31], v[44:47], a[180:183], v[28:31]\n"
  "s_add_u32 m0, s59, 2048\n"
  "s_waitcnt lgkmcnt(4)\n"
  "buffer_load_dwordx4 v112, s[40:43], s57 offen offset:0  lds\n"
  "buffer_load_dwordx4 v113, s[40:43], s57 offen offset:1024  lds\n"
  "s_mov_b32 m0, s88\n"
  "s_add_u32 s57, s57, 11904\n"
  "v_exp_f32 v9, v9\n"
  "v_mfma_f32_16x16x32_bf16 v[28:31], v[40:43], a[176:179], v[28:31]\n"
  "s_waitcnt vmcnt(16)\n"
  "ds_read_b128 v[152:155], v87 offset:6144\n"
  "ds_read_b128 v[156:159], v88 offset:6144\n"
  "v_exp_f32_e64 v13, v13\n"
  "v_mfma_f32_16x16x32_bf16 v[28:31], v[36:39], a[172:175], v[28:31]\n"
  "s_add_u32 m0, s59, 4096\n"
  "s_waitcnt lgkmcnt(2)\n"
  "buffer_load_dwordx4 v112, s[40:43], s57 offen offset:0  lds\n"
  "buffer_load_dwordx4 v113, s[40:43], s57 offen offset:1024  lds\n"
  "s_mov_b32 m0, s88\n"
  "v_cvt_pk_bf16_f32 v8, v8, v12\n"
  "v_cvt_pk_bf16_f32 v9, v9, v13\n"
  "v_mfma_f32_16x16x32_bf16 v[28:31], v[32:35], a[168:171], v[28:31]\n"
  "s_waitcnt vmcnt(16)\n"
  "ds_read_b128 v[144:147], v87 offset:8192\n"
  "ds_read_b128 v[148:151], v88 offset:8192\n"
  "ds_write_addtid_b32 v8 offset:256\n"
  "ds_write_addtid_b32 v9 offset:4352\n"
  "v_mfma_f32_16x16x32_bf16 v[0:3], v[32:35], a[192:195], v[92:95]\n"
  "s_add_u32 m0, s59, 6016\n"
  "s_waitcnt lgkmcnt(4)\n"
  "buffer_load_dwordx4 v112, s[40:43], s57 offen offset:128  lds\n"
  "buffer_load_dwordx4 v113, s[40:43], s57 offen offset:1152  lds\n"
  "s_mov_b32 m0, s88\n"
  "v_exp_f32 v10, v10\n"
  "v_mfma_f32_16x16x32_bf16 v[0:3], v[36:39], a[196:199], v[0:3]\n"
  "s_waitcnt vmcnt(16)\n"
  "ds_read_b128 v[136:139], v87 offset:10240\n"
  "ds_read_b128 v[140:143], v88 offset:10240\n"
  "v_exp_f32 v14, v14\n"
  "v_mfma_f32_16x16x32_bf16 v[0:3], v[40:43], a[200:203], v[0:3]\n"
  "s_add_u32 m0, s59, 7936\n"
  "s_waitcnt lgkmcnt(4)\n"
  "buffer_load_dwordx4 v112, s[40:43], s57 offen offset:256  lds\n"
  "buffer_load_dwordx4 v113, s[40:43], s57 offen offset:1280  lds\n"
  "s_mov_b32 m0, s88\n"
  "s_add_u32 s57, s57, 384\n"
  "v_exp_f32 v11, v11\n"
  "v_mfma_f32_16x16x32_bf16 v[0:3], v[44:47], a[204:207], v[0:3]\n"
  "s_waitcnt vmcnt(16)\n"
  "ds_read_b128 v[160:163], v87 offset:12288\n"
  "ds_read_b128 v[164:167], v88 offset:12288\n"
  "v_exp_f32_e64 v15, v15\n"
  "v_mfma_f32_16x16x32_bf16 v[0:3], v[48:51], a[208:211], v[0:3]\n"
  "s_add_u32 m0, s59, 9984\n"
  "s_waitcnt lgkmcnt(2)\n"
  "buffer_load_dwordx4 v112, s[40:43], s57 offen offset:256  lds\n"
  "buffer_load_dwordx4 v113, s[40:43], s57 offen offset:1280  lds\n"
  "s_mov_b32 m0, s88\n"
  "v_cvt_pk_bf16_f32 v10, v10, v14\n"
  "v_cvt_pk_bf16_f32 v11, v11, v15\n"
  "v_mfma_f32_16x16x32_bf16 v[0:3], v[52:55], a[212:215], v[0:3]\n"
  "s_waitcnt vmcnt(16)\n"
  "ds_read_b128 v[168:171], v87 offset:14336\n"
  "ds_read_b128 v[172:175], v88 offset:14336\n"
  "ds_read_b128 v[56:59], v81 offset:0\n"
  "ds_read_b128 v[60:63], v82 offset:0\n"
  "ds_read_b128 v[64:67], v81 offset:2048\n"
  "ds_write_addtid_b32 v10 offset:8480\n"
  "ds_write_addtid_b32 v11 offset:12576\n"
  "v_pk_mul_f32 v[16:17], v[16:17], s[60:61] op_sel_hi:[1,0]\n"
  "v_pk_mul_f32 v[18:19], v[18:19], s[60:61] op_sel_hi:[1,0]\n"
  "v_pk_mul_f32 v[20:21], v[20:21], s[60:61] op_sel_hi:[1,0]\n"
  "v_pk_mul_f32 v[22:23], v[22:23], s[60:61] op_sel_hi:[1,0]\n"
  "v_pk_mul_f32 v[24:25], v[24:25], s[60:61] op_sel_hi:[1,0]\n"
  "v_pk_mul_f32 v[26:27], v[26:27], s[60:61] op_sel_hi:[1,0]\n"
  "v_pk_mul_f32 v[28:29], v[28:29], s[60:61] op_sel_hi:[1,0]\n"
  "v_pk_mul_f32 v[30:31], v[30:31], s[60:61] op_sel_hi:[1,0]\n"
  "v_mfma_f32_16x16x32_bf16 v[4:7], v[52:55], a[236:239], v[92:95]\n"
  "s_add_u32 m0, s59, 12160\n"
  "s_waitcnt lgkmcnt(7)\n"
  "buffer_load_dwordx4 v112, s[40:43], s57 offen offset:128  lds\n"
  "buffer_load_dwordx4 v113, s[40:43], s57 offen offset:1152  lds\n"
  "s_mov_b32 m0, s88\n"
  "v_exp_f32 v16, v16\n"
  "v_mfma_f32_16x16x32_bf16 v[4:7], v[48:51], a[232:235], v[4:7]\n"
  "s_waitcnt vmcnt(16)\n"
  "ds_read_b128 v[176:179], v87 offset:16384\n"
  "ds_read_b128 v[180:183], v88 offset:16384\n"
  "v_exp_f32 v20, v20\n"
  "v_mfma_f32_16x16x32_bf16 v[4:7], v[44:47], a[228:231], v[4:7]\n"
  "s_add_u32 m0, s59, 14336\n"
  "s_waitcnt lgkmcnt(7)\n"
  "buffer_load_dwordx4 v112, s[40:43], s57 offen offset:0  lds\n"
  "buffer_load_dwordx4 v113, s[40:43], s57 offen offset:1024  lds\n"
  "s_mov_b32 m0, s88\n"
  "s_add_u32 s57, s57, 11904\n"
  "v_exp_f32 v17, v17\n"
  "v_mfma_f32_16x16x32_bf16 v[4:7], v[40:43], a[224:227], v[4:7]\n"
  "s_waitcnt vmcnt(16)\n"
  "ds_read_b128 v[200:203], v87 offset:18432\n"
  "ds_read_b128 v[204:207], v88 offset:18432\n"
  "v_exp_f32_e64 v21, v21\n"
  "v_mfma_f32_16x16x32_bf16 v[4:7], v[36:39], a[220:223], v[4:7]\n"
  "v_cvt_pk_bf16_f32 v16, v16, v20\n"
  "v_cvt_pk_bf16_f32 v17, v17, v21\n"
  "v_mfma_f32_16x16x32_bf16 v[4:7], v[32:35], a[216:219], v[4:7]\n"
  "s_waitcnt vmcnt(14)\n"
  "ds_read_b128 v[192:195], v87 offset:0\n"
  "ds_read_b128 v[196:199], v88 offset:0\n"
  "ds_write_addtid_b32 v16 offset:512\n"
  "ds_write_addtid_b32 v17 offset:4608\n"
  "v_mfma_f32_16x16x32_bf16 v[8:11], v[32:35], a[240:243], v[92:95]\n"
  "s_barrier\n"
  "v_exp_f32 v18, v18\n"
  "v_mfma_f32_16x16x32_bf16 v[8:11], v[36:39], a[244:247], v[8:11]\n"
  "s_waitcnt vmcnt(12)\n"
  "ds_read_b128 v[184:187], v87 offset:2048\n"
  "ds_read_b128 v[188:191], v88 offset:2048\n"
  "v_exp_f32 v22, v22\n"
  "v_mfma_f32_16x16x32_bf16 v[8:11], v[40:43], a[248:251], v[8:11]\n"
  "v_exp_f32 v19, v19\n"
  "v_mfma_f32_16x16x32_bf16 v[8:11], v[44:47], a[252:255], v[8:11]\n"
  "s_waitcnt vmcnt(10)\n"
  "ds_read_b128 v[208:211], v87 offset:4096\n"
  "ds_read_b128 v[212:215], v88 offset:4096\n"
  "v_exp_f32 v23, v23\n"
  "v_mfma_f32_16x16x32_bf16 v[8:11], v[48:51], v[128:131], v[8:11]\n"
  "v_cvt_pk_bf16_f32 v18, v18, v22\n"
  "v_cvt_pk_bf16_f32 v19, v19, v23\n"
  "v_mfma_f32_16x16x32_bf16 v[8:11], v[52:55], v[132:135], v[8:11]\n"
  "s_waitcnt vmcnt(8)\n"
  "ds_read_b128 v[216:219], v87 offset:6144\n"
  "ds_read_b128 v[220:223], v88 offset:6144\n"
  "ds_write_addtid_b32 v18 offset:8736\n"
  "ds_write_addtid_b32 v19 offset:12832\n"
  "v_mfma_f32_16x16x32_bf16 v[12:15], v[52:55], v[156:159], v[92:95]\n"
  "v_exp_f32 v24, v24\n"
  "v_mfma_f32_16x16x32_bf16 v[12:15], v[48:51], v[152:155], v[12:15]\n"
  "s_waitcnt vmcnt(6)\n"
  "ds_read_b128 v[224:227], v87 offset:8192\n"
  "ds_read_b128 v[228:231], v88 offset:8192\n"
  "v_exp_f32 v28, v28\n"
  "v_mfma_f32_16x16x32_bf16 v[12:15], v[44:47], v[148:151], v[12:15]\n"
  "v_exp_f32 v25, v25\n"
  "v_mfma_f32_16x16x32_bf16 v[12:15], v[40:43], v[144:147], v[12:15]\n"
  "s_waitcnt vmcnt(4)\n"
  "ds_read_b128 v[248:251], v87 offset:10240\n"
  "ds_read_b128 v[252:255], v88 offset:10240\n"
  "v_exp_f32_e64 v29, v29\n"
  "v_mfma_f32_16x16x32_bf16 v[12:15], v[36:39], v[140:143], v[12:15]\n"
  "v_cvt_pk_bf16_f32 v24, v24, v28\n"
  "v_cvt_pk_bf16_f32 v25, v25, v29\n"
  "v_mfma_f32_16x16x32_bf16 v[12:15], v[32:35], v[136:139], v[12:15]\n"
  "s_waitcnt vmcnt(2)\n"
  "ds_read_b128 v[240:243], v87 offset:12288\n"
  "ds_read_b128 v[244:247], v88 offset:12288\n"
  "ds_write_addtid_b32 v24 offset:768\n"
  "ds_write_addtid_b32 v25 offset:4864\n"
  "v_mfma_f32_16x16x32_bf16 v[16:19], v[32:35], v[160:163], v[92:95]\n"
  "v_exp_f32 v26, v26\n"
  "v_mfma_f32_16x16x32_bf16 v[16:19], v[36:39], v[164:167], v[16:19]\n"
  "s_waitcnt vmcnt(0)\n"
  "ds_read_b128 v[232:235], v87 offset:14336\n"
  "ds_read_b128 v[236:239], v88 offset:14336\n"
  "v_exp_f32 v30, v30\n"
  "v_mfma_f32_16x16x32_bf16 v[16:19], v[40:43], v[168:171], v[16:19]\n"
  "v_exp_f32 v27, v27\n"
  "v_mfma_f32_16x16x32_bf16 v[16:19], v[44:47], v[172:175], v[16:19]\n"
  "s_waitcnt lgkmcnt(0)\n"
  "s_barrier\n"
  "v_exp_f32 v31, v31\n"
  "v_mfma_f32_16x16x32_bf16 v[16:19], v[48:51], v[176:179], v[16:19]\n"
  "v_cvt_pk_bf16_f32 v26, v26, v30\n"
  "v_cvt_pk_bf16_f32 v27, v27, v31\n"
  "v_mfma_f32_16x16x32_bf16 v[16:19], v[52:55], v[180:183], v[16:19]\n"
  "ds_read_b128 v[68:71], v82 offset:2048\n"
  "ds_read_b128 v[72:75], v81 offset:4096\n"
  "ds_read_b128 v[76:79], v82 offset:4096\n"
  "ds_write_addtid_b32 v26 offset:8992\n"
  "ds_write_addtid_b32 v27 offset:13088\n"
  "v_pk_mul_f32 v[0:1], v[0:1], s[60:61] op_sel_hi:[1,0]\n"
  "v_pk_mul_f32 v[2:3], v[2:3], s[60:61] op_sel_hi:[1,0]\n"
  "v_pk_mul_f32 v[4:5], v[4:5], s[60:61] op_sel_hi:[1,0]\n"
  "v_pk_mul_f32 v[6:7], v[6:7], s[60:61] op_sel_hi:[1,0]\n"
  "v_pk_mul_f32 v[8:9], v[8:9], s[60:61] op_sel_hi:[1,0]\n"
  "v_pk_mul_f32 v[10:11], v[10:11], s[60:61] op_sel_hi:[1,0]\n"
  "v_pk_mul_f32 v[12:13], v[12:13], s[60:61] op_sel_hi:[1,0]\n"
  "v_pk_mul_f32 v[14:15], v[14:15], s[60:61] op_sel_hi:[1,0]\n"
  "v_mfma_f32_16x16x32_bf16 v[20:23], v[52:55], v[204:207], v[92:95]\n"
  "v_exp_f32 v0, v0\n"
  "v_mfma_f32_16x16x32_bf16 v[20:23], v[48:51], v[200:203], v[20:23]\n"
  "v_exp_f32 v4, v4\n"
  "v_mfma_f32_16x16x32_bf16 v[20:23], v[44:47], v[196:199], v[20:23]\n"
  "v_exp_f32 v1, v1\n"
  "v_mfma_f32_16x16x32_bf16 v[20:23], v[40:43], v[192:195], v[20:23]\n"
  "v_exp_f32 v5, v5\n"
  "v_mfma_f32_16x16x32_bf16 v[20:23], v[36:39], v[188:191], v[20:23]\n"
  "v_cvt_pk_bf16_f32 v0, v0, v4\n"
  "v_cvt_pk_bf16_f32 v1, v1, v5\n"
  "v_mfma_f32_16x16x32_bf16 v[20:23], v[32:35], v[184:187], v[20:23]\n"
  "ds_write_addtid_b32 v0 offset:1024\n"
  "ds_write_addtid_b32 v1 offset:5120\n"
  "v_mfma_f32_16x16x32_bf16 v[24:27], v[32:35], v[208:211], v[92:95]\n"
  "v_exp_f32 v2, v2\n"
  "v_mfma_f32_16x16x32_bf16 v[24:27], v[36:39], v[212:215], v[24:27]\n"
  "v_exp_f32 v6, v6\n"
  "v_mfma_f32_16x16x32_bf16 v[24:27], v[40:43], v[216:219], v[24:27]\n"
  "v_exp_f32 v3, v3\n"
  "v_mfma_f32_16x16x32_bf16 v[24:27], v[44:47], v[220:223], v[24:27]\n"
  "v_exp_f32 v7, v7\n"
  "v_mfma_f32_16x16x32_bf16 v[24:27], v[48:51], v[224:227], v[24:27]\n"
  "v_cvt_pk_bf16_f32 v2, v2, v6\n"
  "v_cvt_pk_bf16_f32 v3, v3, v7\n"
  "v_mfma_f32_16x16x32_bf16 v[24:27], v[52:55], v[228:231], v[24:27]\n"
  "ds_write_addtid_b32 v2 offset:9248\n"
  "ds_write_addtid_b32 v3 offset:13344\n"
  "v_mfma_f32_16x16x32_bf16 v[28:31], v[52:55], v[252:255], v[92:95]\n"
  "v_exp_f32 v8, v8\n"
  "v_mfma_f32_16x16x32_bf16 v[28:31], v[48:51], v[248:251], v[28:31]\n"
  "v_exp_f32 v12, v12\n"
  "v_mfma_f32_16x16x32_bf16 v[28:31], v[44:47], v[244:247], v[28:31]\n"
  "v_exp_f32 v9, v9\n"
  "v_mfma_f32_16x16x32_bf16 v[28:31], v[40:43], v[240:243], v[28:31]\n"
  "s_sub_u32 s56, s56, 1\n"
  "v_exp_f32 v13, v13\n"
  "v_mfma_f32_16x16x32_bf16 v[28:31], v[36:39], v[236:239], v[28:31]\n"
  "v_cvt_pk_bf16_f32 v8, v8, v12\n"
  "v_cvt_pk_bf16_f32 v9, v9, v13\n"
  "v_mfma_f32_16x16x32_bf16 v[28:31], v[32:35], v[232:235], v[28:31]\n"
  "s_waitcnt lgkmcnt(6)\n"
  "ds_write_addtid_b32 v8 offset:1280\n"
  "ds_write_addtid_b32 v9 offset:5376\n"
  ".p2align 3\n"
  "v_mfma_f32_16x16x32_bf16 v[0:3], v[56:59], a[0:3], v[92:95]\n"
  "s_add_u32 s66, s66, 6144\n"
  "v_exp_f32 v10, v10\n"
  "v_mfma_f32_16x16x32_bf16 v[0:3], v[60:63], a[4:7], v[0:3]\n"
  "s_cmp_eq_u32 s66, 102400\n"
  "v_exp_f32 v14, v14\n"
  "v_mfma_f32_16x16x32_bf16 v[0:3], v[64:67], a[8:11], v[0:3]\n"
  "s_cselect_b32 s66, 65536, s66\n"
  "v_exp_f32 v11, v11\n"
  "v_mfma_f32_16x16x32_bf16 v[0:3], v[68:71], a[12:15], v[0:3]\n"
  "v_exp_f32 v15, v15\n"
  "v_mfma_f32_16x16x32_bf16 v[0:3], v[72:75], a[16:19], v[0:3]\n"
  "v_cvt_pk_bf16_f32 v10, v10, v14\n"
  "v_cvt_pk_bf16_f32 v11, v11, v15\n"
  "v_mfma_f32_16x16x32_bf16 v[0:3], v[76:79], a[20:23], v[0:3]\n"
  "ds_write_addtid_b32 v10 offset:9504\n"
  "ds_write_addtid_b32 v11 offset:13600\n"
  "v_add_u32 v81, s66, v83\n"
  "v_add_u32 v82, s66, v84\n"
  "v_pk_mul_f32 v[16:17], v[16:17], s[60:61] op_sel_hi:[1,0]\n"
  "v_pk_mul_f32 v[18:19], v[18:19], s[60:61] op_sel_hi:[1,0]\n"
  "v_pk_mul_f32 v[20:21], v[20:21], s[60:61] op_sel_hi:[1,0]\n"
  "v_pk_mul_f32 v[22:23], v[22:23], s[60:61] op_sel_hi:[1,0]\n"
  "v_pk_mul_f32 v[24:25], v[24:25], s[60:61] op_sel_hi:[1,0]\n"
  "v_pk_mul_f32 v[26:27], v[26:27], s[60:61] op_sel_hi:[1,0]\n"
  "v_pk_mul_f32 v[28:29], v[28:29], s[60:61] op_sel_hi:[1,0]\n"
  "v_pk_mul_f32 v[30:31], v[30:31], s[60:61] op_sel_hi:[1,0]\n"
  "v_mfma_f32_16x16x32_bf16 v[4:7], v[76:79], a[44:47], v[92:95]\n"
  "v_exp_f32 v16, v16\n"
  "v_mfma_f32_16x16x32_bf16 v[4:7], v[72:75], a[40:43], v[4:7]\n"
  "v_exp_f32 v20, v20\n"
  "v_mfma_f32_16x16x32_bf16 v[4:7], v[68:71], a[36:39], v[4:7]\n"
  "v_exp_f32 v17, v17\n"
  "v_mfma_f32_16x16x32_bf16 v[4:7], v[64:67], a[32:35], v[4:7]\n"
  "v_exp_f32 v21, v21\n"
  "v_mfma_f32_16x16x32_bf16 v[4:7], v[60:63], a[28:31], v[4:7]\n"
  "v_cvt_pk_bf16_f32 v16, v16, v20\n"
  "v_cvt_pk_bf16_f32 v17, v17, v21\n"
  "v_mfma_f32_16x16x32_bf16 v[4:7], v[56:59], a[24:27], v[4:7]\n"
  "ds_write_addtid_b32 v16 offset:1536\n"
  "ds_write_addtid_b32 v17 offset:5632\n"
  "v_mfma_f32_16x16x32_bf16 v[8:11], v[56:59], a[48:51], v[92:95]\n"
  "v_exp_f32 v18, v18\n"
  "v_mfma_f32_16x16x32_bf16 v[8:11], v[60:63], a[52:55], v[8:11]\n"
  "v_exp_f32 v22, v22\n"
  "v_mfma_f32_16x16x32_bf16 v[8:11], v[64:67], a[56:59], v[8:11]\n"
  "v_exp_f32 v19, v19\n"
  "v_mfma_f32_16x16x32_bf16 v[8:11], v[68:71], a[60:63], v[8:11]\n"
  "v_exp_f32 v23, v23\n"
  "v_mfma_f32_16x16x32_bf16 v[8:11], v[72:75], a[64:67], v[8:11]\n"
  "v_cvt_pk_bf16_f32 v18, v18, v22\n"
  "v_cvt_pk_bf16_f32 v19, v19, v23\n"
  "v_mfma_f32_16x16x32_bf16 v[8:11], v[76:79], a[68:71], v[8:11]\n"
  "ds_write_addtid_b32 v18 offset:9760\n"
  "ds_write_addtid_b32 v19 offset:13856\n"
  "v_mfma_f32_16x16x32_bf16 v[12:15], v[76:79], a[92:95], v[92:95]\n"
  "v_exp_f32 v24, v24\n"
  "v_mfma_f32_16x16x32_bf16 v[12:15], v[72:75], a[88:91], v[12:15]\n"
  "v_exp_f32 v28, v28\n"
  "v_mfma_f32_16x16x32_bf16 v[12:15], v[68:71], a[84:87], v[12:15]\n"
  "v_exp_f32 v25, v25\n"
  "v_mfma_f32_16x16x32_bf16 v[12:15], v[64:67], a[80:83], v[12:15]\n"
  "v_exp_f32 v29, v29\n"
  "v_mfma_f32_16x16x32_bf16 v[12:15], v[60:63], a[76:79], v[12:15]\n"
  "v_cvt_pk_bf16_f32 v24, v24, v28\n"
  "v_cvt_pk_bf16_f32 v25, v25, v29\n"
  "v_mfma_f32_16x16x32_bf16 v[12:15], v[56:59], a[72:75], v[12:15]\n"
  "ds_write_addtid_b32 v24 offset:1792\n"
  "ds_write_addtid_b32 v25 offset:5888\n"
  "v_mfma_f32_16x16x32_bf16 v[16:19], v[56:59], a[96:99], v[92:95]\n"
  "v_exp_f32 v26, v26\n"
  "v_mfma_f32_16x16x32_bf16 v[16:19], v[60:63], a[100:103], v[16:19]\n"
  "v_exp_f32 v30, v30\n"
  "v_mfma_f32_16x16x32_bf16 v[16:19], v[64:67], a[104:107], v[16:19]\n"
  "v_exp_f32 v27, v27\n"
  "v_mfma_f32_16x16x32_bf16 v[16:19], v[68:71], a[108:111], v[16:19]\n"
  "v_exp_f32 v31, v31\n"
  "v_mfma_f32_16x16x32_bf16 v[16:19], v[72:75], a[112:115], v[16:19]\n"
  "v_cvt_pk_bf16_f32 v26, v26, v30\n"
  "v_cvt_pk_bf16_f32 v27, v27, v31\n"
  "v_mfma_f32_16x16x32_bf16 v[16:19], v[76:79], a[116:119], v[16:19]\n"
  "ds_write_addtid_b32 v26 offset:10016\n"
  "ds_write_addtid_b32 v27 offset:14112\n"
  "v_pk_mul_f32 v[0:1], v[0:1], s[60:61] op_sel_hi:[1,0]\n"
  "v_pk_mul_f32 v[2:3], v[2:3], s[60:61] op_sel_hi:[1,0]\n"
  "v_pk_mul_f32 v[4:5], v[4:5], s[60:61] op_sel_hi:[1,0]\n"
  "v_pk_mul_f32 v[6:7], v[6:7], s[60:61] op_sel_hi:[1,0]\n"
  "v_pk_mul_f32 v[8:9], v[8:9], s[60:61] op_sel_hi:[1,0]\n"
  "v_pk_mul_f32 v[10:11], v[10:11], s[60:61] op_sel_hi:[1,0]\n"
  "v_pk_mul_f32 v[12:13], v[12:13], s[60:61] op_sel_hi:[1,0]\n"
  "v_pk_mul_f32 v[14:15], v[14:15], s[60:61] op_sel_hi:[1,0]\n"
  "v_mfma_f32_16x16x32_bf16 v[20:23], v[76:79], a[140:143], v[92:95]\n"
  "s_add_u32 s88, s88, 16448\n"
  "v_exp_f32 v0, v0\n"
  "v_mfma_f32_16x16x32_bf16 v[20:23], v[72:75], a[136:139], v[20:23]\n"
  "s_cmp_eq_u32 s88, s68\n"
  "v_exp_f32 v4, v4\n"
  "v_mfma_f32_16x16x32_bf16 v[20:23], v[68:71], a[132:135], v[20:23]\n"
  "s_cselect_b32 s88, s89, s88\n"
  "v_exp_f32 v1, v1\n"
  "v_mfma_f32_16x16x32_bf16 v[20:23], v[64:67], a[128:131], v[20:23]\n"
  "s_waitcnt lgkmcnt(0)\n"
  "s_barrier\n"
  "v_exp_f32_e64 v5, v5\n"
  "v_mfma_f32_16x16x32_bf16 v[20:23], v[60:63], a[124:127], v[20:23]\n"
  "s_mov_b32 m0, s88\n"
  "v_cvt_pk_bf16_f32 v0, v0, v4\n"
  "v_cvt_pk_bf16_f32 v1, v1, v5\n"
  "v_mfma_f32_16x16x32_bf16 v[20:23], v[56:59], a[120:123], v[20:23]\n"
  "ds_write_addtid_b32 v0 offset:0\n"
  "ds_write_addtid_b32 v1 offset:4096\n"
  "v_mfma_f32_16x16x32_bf16 v[24:27], v[56:59], a[144:147], v[92:95]\n"
  "v_exp_f32 v2, v2\n"
  "v_mfma_f32_16x16x32_bf16 v[24:27], v[60:63], a[148:151], v[24:27]\n"
  "v_exp_f32 v6, v6\n"
  "v_mfma_f32_16x16x32_bf16 v[24:27], v[64:67], a[152:155], v[24:27]\n"
  "v_exp_f32 v3, v3\n"
  "v_mfma_f32_16x16x32_bf16 v[24:27], v[68:71], a[156:159], v[24:27]\n"
  "v_exp_f32 v7, v7\n"
  "v_mfma_f32_16x16x32_bf16 v[24:27], v[72:75], a[160:163], v[24:27]\n"
  "v_cvt_pk_bf16_f32 v2, v2, v6\n"
  "v_cvt_pk_bf16_f32 v3, v3, v7\n"
  "v_mfma_f32_16x16x32_bf16 v[24:27], v[76:79], a[164:167], v[24:27]\n"
  "ds_write_addtid_b32 v2 offset:8224\n"
  "ds_write_addtid_b32 v3 offset:12320\n"
  "v_mfma_f32_16x16x32_bf16 v[28:31], v[76:79], a[188:191], v[92:95]\n"
  "v_exp_f32 v8, v8\n"
  "v_mfma_f32_16x16x32_bf16 v[28:31], v[72:75], a[184:187], v[28:31]\n"
  "v_exp_f32 v12, v12\n"
  "v_mfma_f32_16x16x32_bf16 v[28:31], v[68:71], a[180:183], v[28:31]\n"
  "v_exp_f32 v9, v9\n"
  "v_mfma_f32_16x16x32_bf16 v[28:31], v[64:67], a[176:179], v[28:31]\n"
  "v_exp_f32 v13, v13\n"
  "v_mfma_f32_16x16x32_bf16 v[28:31], v[60:63], a[172:175], v[28:31]\n"
  "v_cvt_pk_bf16_f32 v8, v8, v12\n"
  "v_cvt_pk_bf16_f32 v9, v9, v13\n"
  "v_mfma_f32_16x16x32_bf16 v[28:31], v[56:59], a[168:171], v[28:31]\n"
  "ds_write_addtid_b32 v8 offset:256\n"
  "ds_write_addtid_b32 v9 offset:4352\n"
  "v_mfma_f32_16x16x32_bf16 v[0:3], v[56:59], a[192:195], v[92:95]\n"
  "v_exp_f32 v10, v10\n"
  "v_mfma_f32_16x16x32_bf16 v[0:3], v[60:63], a[196:199], v[0:3]\n"
  "v_exp_f32 v14, v14\n"
  "v_mfma_f32_16x16x32_bf16 v[0:3], v[64:67], a[200:203], v[0:3]\n"
  "v_exp_f32 v11, v11\n"
  "v_mfma_f32_16x16x32_bf16 v[0:3], v[68:71], a[204:207], v[0:3]\n"
  "v_exp_f32 v15, v15\n"
  "v_mfma_f32_16x16x32_bf16 v[0:3], v[72:75], a[208:211], v[0:3]\n"
  "v_cvt_pk_bf16_f32 v10, v10, v14\n"
  "v_cvt_pk_bf16_f32 v11, v11, v15\n"
  "v_mfma_f32_16x16x32_bf16 v[0:3], v[76:79], a[212:215], v[0:3]\n"
  "ds_read_b128 v[32:35], v81 offset:0\n"
  "ds_read_b128 v[36:39], v82 offset:0\n"
  "ds_read_b128 v[40:43], v81 offset:2048\n"
  "ds_write_addtid_b32 v10 offset:8480\n"
  "ds_write_addtid_b32 v11 offset:12576\n"
  "v_pk_mul_f32 v[16:17], v[16:17], s[60:61] op_sel_hi:[1,0]\n"
  "v_pk_mul_f32 v[18:19], v[18:19], s[60:61] op_sel_hi:[1,0]\n"
  "v_pk_mul_f32 v[20:21], v[20:21], s[60:61] op_sel_hi:[1,0]\n"
  "v_pk_mul_f32 v[22:23], v[22:23], s[60:61] op_sel_hi:[1,0]\n"
  "v_pk_mul_f32 v[24:25], v[24:25], s[60:61] op_sel_hi:[1,0]\n"
  "v_pk_mul_f32 v[26:27], v[26:27], s[60:61] op_sel_hi:[1,0]\n"
  "v_pk_mul_f32 v[28:29], v[28:29], s[60:61] op_sel_hi:[1,0]\n"
  "v_pk_mul_f32 v[30:31], v[30:31], s[60:61] op_sel_hi:[1,0]\n"
  "v_mfma_f32_16x16x32_bf16 v[4:7], v[76:79], a[236:239], v[92:95]\n"
  "v_exp_f32 v16, v16\n"
  "v_mfma_f32_16x16x32_bf16 v[4:7], v[72:75], a[232:235], v[4:7]\n"
  "v_exp_f32 v20, v20\n"
  "v_mfma_f32_16x16x32_bf16 v[4:7], v[68:71], a[228:231], v[4:7]\n"
  "v_exp_f32 v17, v17\n"
  "v_mfma_f32_16x16x32_bf16 v[4:7], v[64:67], a[224:227], v[4:7]\n"
  "v_exp_f32 v21, v21\n"
  "v_mfma_f32_16x16x32_bf16 v[4:7], v[60:63], a[220:223], v[4:7]\n"
  "v_cvt_pk_bf16_f32 v16, v16, v20\n"
  "v_cvt_pk_bf16_f32 v17, v17, v21\n"
  "v_mfma_f32_16x16x32_bf16 v[4:7], v[56:59], a[216:219], v[4:7]\n"
  "ds_write_addtid_b32 v16 offset:512\n"
  "ds_write_addtid_b32 v17 offset:4608\n"
  "v_mfma_f32_16x16x32_bf16 v[8:11], v[56:59], a[240:243], v[92:95]\n"
  "v_exp_f32 v18, v18\n"
  "v_mfma_f32_16x16x32_bf16 v[8:11], v[60:63], a[244:247], v[8:11]\n"
  "v_exp_f32 v22, v22\n"
  "v_mfma_f32_16x16x32_bf16 v[8:11], v[64:67], a[248:251], v[8:11]\n"
  "v_exp_f32 v19, v19\n"
  "v_mfma_f32_16x16x32_bf16 v[8:11], v[68:71], a[252:255], v[8:11]\n"
  "v_exp_f32 v23, v23\n"
  "v_mfma_f32_16x16x32_bf16 v[8:11], v[72:75], v[128:131], v[8:11]\n"
  "v_cvt_pk_bf16_f32 v18, v18, v22\n"
  "v_cvt_pk_bf16_f32 v19, v19, v23\n"
  "v_mfma_f32_16x16x32_bf16 v[8:11], v[76:79], v[132:135], v[8:11]\n"
  "ds_write_addtid_b32 v18 offset:8736\n"
  "ds_write_addtid_b32 v19 offset:12832\n"
  "v_mfma_f32_16x16x32_bf16 v[12:15], v[76:79], v[156:159], v[92:95]\n"
  "v_exp_f32 v24, v24\n"
  "v_mfma_f32_16x16x32_bf16 v[12:15], v[72:75], v[152:155], v[12:15]\n"
  "v_exp_f32 v28, v28\n"
  "v_mfma_f32_16x16x32_bf16 v[12:15], v[68:71], v[148:151], v[12:15]\n"
  "v_exp_f32 v25, v25\n"
  "v_mfma_f32_16x16x32_bf16 v[12:15], v[64:67], v[144:147], v[12:15]\n"
  "v_exp_f32 v29, v29\n"
  "v_mfma_f32_16x16x32_bf16 v[12:15], v[60:63], v[140:143], v[12:15]\n"
  "v_cvt_pk_bf16_f32 v24, v24, v28\n"
  "v_cvt_pk_bf16_f32 v25, v25, v29\n"
  "v_mfma_f32_16x16x32_bf16 v[12:15], v[56:59], v[136:139], v[12:15]\n"
  "ds_write_addtid_b32 v24 offset:768\n"
  "ds_write_addtid_b32 v25 offset:4864\n"
  "v_mfma_f32_16x16x32_bf16 v[16:19], v[56:59], v[160:163], v[92:95]\n"
  "v_exp_f32 v26, v26\n"
  "v_mfma_f32_16x16x32_bf16 v[16:19], v[60:63], v[164:167], v[16:19]\n"
  "v_exp_f32 v30, v30\n"
  "v_mfma_f32_16x16x32_bf16 v[16:19], v[64:67], v[168:171], v[16:19]\n"
  "v_exp_f32 v27, v27\n"
  "v_mfma_f32_16x16x32_bf16 v[16:19], v[68:71], v[172:175], v[16:19]\n"
  "v_exp_f32 v31, v31\n"
  "v_mfma_f32_16x16x32_bf16 v[16:19], v[72:75], v[176:179], v[16:19]\n"
  "v_cvt_pk_bf16_f32 v26, v26, v30\n"
  "v_cvt_pk_bf16_f32 v27, v27, v31\n"
  "v_mfma_f32_16x16x32_bf16 v[16:19], v[76:79], v[180:183], v[16:19]\n"
  "ds_read_b128 v[44:47], v82 offset:2048\n"
  "ds_read_b128 v[48:51], v81 offset:4096\n"
  "ds_read_b128 v[52:55], v82 offset:4096\n"
  "ds_write_addtid_b32 v26 offset:8992\n"
  "ds_write_addtid_b32 v27 offset:13088\n"
  "v_pk_mul_f32 v[0:1], v[0:1], s[60:61] op_sel_hi:[1,0]\n"
  "v_pk_mul_f32 v[2:3], v[2:3], s[60:61] op_sel_hi:[1,0]\n"
  "v_pk_mul_f32 v[4:5], v[4:5], s[60:61] op_sel_hi:[1,0]\n"
  "v_pk_mul_f32 v[6:7], v[6:7], s[60:61] op_sel_hi:[1,0]\n"
  "v_pk_mul_f32 v[8:9], v[8:9], s[60:61] op_sel_hi:[1,0]\n"
  "v_pk_mul_f32 v[10:11], v[10:11], s[60:61] op_sel_hi:[1,0]\n"
  "v_pk_mul_f32 v[12:13], v[12:13], s[60:61] op_sel_hi:[1,0]\n"
  "v_pk_mul_f32 v[14:15], v[14:15], s[60:61] op_sel_hi:[1,0]\n"
  "v_mfma_f32_16x16x32_bf16 v[20:23], v[76:79], v[204:207], v[92:95]\n"
  "v_exp_f32 v0, v0\n"
  "v_mfma_f32_16x16x32_bf16 v[20:23], v[72:75], v[200:203], v[20:23]\n"
  "v_exp_f32 v4, v4\n"
  "v_mfma_f32_16x16x32_bf16 v[20:23], v[68:71], v[196:199], v[20:23]\n"
  "v_exp_f32 v1, v1\n"
  "v_mfma_f32_16x16x32_bf16 v[20:23], v[64:67], v[192:195], v[20:23]\n"
  "v_exp_f32 v5, v5\n"
  "v_mfma_f32_16x16x32_bf16 v[20:23], v[60:63], v[188:191], v[20:23]\n"
  "v_cvt_pk_bf16_f32 v0, v0, v4\n"
  "v_cvt_pk_bf16_f32 v1, v1, v5\n"
  "v_mfma_f32_16x16x32_bf16 v[20:23], v[56:59], v[184:187], v[20:23]\n"
  "ds_write_addtid_b32 v0 offset:1024\n"
  "ds_write_addtid_b32 v1 offset:5120\n"
  "v_mfma_f32_16x16x32_bf16 v[24:27], v[56:59], v[208:211], v[92:95]\n"
  "v_exp_f32 v2, v2\n"
  "v_mfma_f32_16x16x32_bf16 v[24:27], v[60:63], v[212:215], v[24:27]\n"
  "v_exp_f32 v6, v6\n"
  "v_mfma_f32_16x16x32_bf16 v[24:27], v[64:67], v[216:219], v[24:27]\n"
  "v_exp_f32 v3, v3\n"
  "v_mfma_f32_16x16x32_bf16 v[24:27], v[68:71], v[220:223], v[24:27]\n"
  "v_exp_f32 v7, v7\n"
  "v_mfma_f32_16x16x32_bf16 v[24:27], v[72:75], v[224:227], v[24:27]\n"
  "v_cvt_pk_bf16_f32 v2, v2, v6\n"
  "v_cvt_pk_bf16_f32 v3, v3, v7\n"
  "v_mfma_f32_16x16x32_bf16 v[24:27], v[76:79], v[228:231], v[24:27]\n"
  "ds_write_addtid_b32 v2 offset:9248\n"
  "ds_write_addtid_b32 v3 offset:13344\n"
  "v_mfma_f32_16x16x32_bf16 v[28:31], v[76:79], v[252:255], v[92:95]\n"
  "v_exp_f32 v8, v8\n"
  "v_mfma_f32_16x16x32_bf16 v[28:31], v[72:75], v[248:251], v[28:31]\n"
  "v_exp_f32 v12, v12\n"
  "v_mfma_f32_16x16x32_bf16 v[28:31], v[68:71], v[244:247], v[28:31]\n"
  "v_exp_f32 v9, v9\n"
  "v_mfma_f32_16x16x32_bf16 v[28:31], v[64:67], v[240:243], v[28:31]\n"
  "s_sub_u32 s56, s56, 1\n"
  "v_exp_f32_e64 v13, v13\n"
  "v_mfma_f32_16x16x32_bf16 v[28:31], v[60:63], v[236:239], v[28:31]\n"
  "v_cvt_pk_bf16_f32 v8, v8, v12\n"
  "v_cvt_pk_bf16_f32 v9, v9, v13\n"
  "v_mfma_f32_16x16x32_bf16 v[28:31], v[56:59], v[232:235], v[28:31]\n"
  "s_waitcnt lgkmcnt(6)\n"
  "ds_write_addtid_b32 v8 offset:1280\n"
  "ds_write_addtid_b32 v9 offset:5376\n"
  "v_mfma_f32_16x16x32_bf16 v[0:3], v[32:35], a[0:3], v[92:95]\n"
  "s_add_u32 s66, s66, 6144\n"
  "v_exp_f32 v10, v10\n"
  "v_mfma_f32_16x16x32_bf16 v[0:3], v[36:39], a[4:7], v[0:3]\n"
  "s_cmp_eq_u32 s66, 102400\n"
  "v_exp_f32 v14, v14\n"
  "v_mfma_f32_16x16x32_bf16 v[0:3], v[40:43], a[8:11], v[0:3]\n"
  "s_cselect_b32 s66, 65536, s66\n"
  "v_exp_f32 v11, v11\n"
  "v_mfma_f32_16x16x32_bf16 v[0:3], v[44:47], a[12:15], v[0:3]\n"
  "v_exp_f32_e64 v15, v15\n"
  "v_mfma_f32_16x16x32_bf16 v[0:3], v[48:51], a[16:19], v[0:3]\n"
  "v_cvt_pk_bf16_f32 v10, v10, v14\n"
  "v_cvt_pk_bf16_f32 v11, v11, v15\n"
  "v_mfma_f32_16x16x32_bf16 v[0:3], v[52:55], a[20:23], v[0:3]\n"
  "ds_write_addtid_b32 v10 offset:9504\n"
  "ds_write_addtid_b32 v11 offset:13600\n"
  "v_add_u32 v81, s66, v83\n"
  "v_add_u32 v82, s66, v84\n"
  "v_pk_mul_f32 v[16:17], v[16:17], s[60:61] op_sel_hi:[1,0]\n"
  "v_pk_mul_f32 v[18:19], v[18:19], s[60:61] op_sel_hi:[1,0]\n"
  "v_pk_mul_f32 v[20:21], v[20:21], s[60:61] op_sel_hi:[1,0]\n"
  "v_pk_mul_f32 v[22:23], v[22:23], s[60:61] op_sel_hi:[1,0]\n"
  "v_pk_mul_f32 v[24:25], v[24:25], s[60:61] op_sel_hi:[1,0]\n"
  "v_pk_mul_f32 v[26:27], v[26:27], s[60:61] op_sel_hi:[1,0]\n"
  "v_pk_mul_f32 v[28:29], v[28:29], s[60:61] op_sel_hi:[1,0]\n"
  "v_pk_mul_f32 v[30:31], v[30:31], s[60:61] op_sel_hi:[1,0]\n"
  "v_mfma_f32_16x16x32_bf16 v[4:7], v[52:55], a[44:47], v[92:95]\n"
  "v_exp_f32 v16, v16\n"
  "v_mfma_f32_16x16x32_bf16 v[4:7], v[48:51], a[40:43], v[4:7]\n"
  "v_exp_f32 v20, v20\n"
  "v_mfma_f32_16x16x32_bf16 v[4:7], v[44:47], a[36:39], v[4:7]\n"
  "v_exp_f32 v17, v17\n"
  "v_mfma_f32_16x16x32_bf16 v[4:7], v[40:43], a[32:35], v[4:7]\n"
  "v_exp_f32 v21, v21\n"
  "v_mfma_f32_16x16x32_bf16 v[4:7], v[36:39], a[28:31], v[4:7]\n"
  "v_cvt_pk_bf16_f32 v16, v16, v20\n"
  "v_cvt_pk_bf16_f32 v17, v17, v21\n"
  "v_mfma_f32_16x16x32_bf16 v[4:7], v[32:35], a[24:27], v[4:7]\n"
  "ds_write_addtid_b32 v16 offset:1536\n"
  "ds_write_addtid_b32 v17 offset:5632\n"
  "v_mfma_f32_16x16x32_bf16 v[8:11], v[32:35], a[48:51], v[92:95]\n"
  "v_exp_f32 v18, v18\n"
  "v_mfma_f32_16x16x32_bf16 v[8:11], v[36:39], a[52:55], v[8:11]\n"
  "v_exp_f32 v22, v22\n"
  "v_mfma_f32_16x16x32_bf16 v[8:11], v[40:43], a[56:59], v[8:11]\n"
  "v_exp_f32 v19, v19\n"
  "v_mfma_f32_16x16x32_bf16 v[8:11], v[44:47], a[60:63], v[8:11]\n"
  "v_exp_f32 v23, v23\n"
  "v_mfma_f32_16x16x32_bf16 v[8:11], v[48:51], a[64:67], v[8:11]\n"
  "v_cvt_pk_bf16_f32 v18, v18, v22\n"
  "v_cvt_pk_bf16_f32 v19, v19, v23\n"
  "v_mfma_f32_16x16x32_bf16 v[8:11], v[52:55], a[68:71], v[8:11]\n"
  "ds_write_addtid_b32 v18 offset:9760\n"
  "ds_write_addtid_b32 v19 offset:13856\n"
  "v_mfma_f32_16x16x32_bf16 v[12:15], v[52:55], a[92:95], v[92:95]\n"
  "v_exp_f32 v24, v24\n"
  "v_mfma_f32_16x16x32_bf16 v[12:15], v[48:51], a[88:91], v[12:15]\n"
  "v_exp_f32 v28, v28\n"
  "v_mfma_f32_16x16x32_bf16 v[12:15], v[44:47], a[84:87], v[12:15]\n"
  "v_exp_f32 v25, v25\n"
  "v_mfma_f32_16x16x32_bf16 v[12:15], v[40:43], a[80:83], v[12:15]\n"
  "v_exp_f32 v29, v29\n"
  "v_mfma_f32_16x16x32_bf16 v[12:15], v[36:39], a[76:79], v[12:15]\n"
  "v_cvt_pk_bf16_f32 v24, v24, v28\n"
  "v_cvt_pk_bf16_f32 v25, v25, v29\n"
  "v_mfma_f32_16x16x32_bf16 v[12:15], v[32:35], a[72:75], v[12:15]\n"
  "ds_write_addtid_b32 v24 offset:1792\n"
  "ds_write_addtid_b32 v25 offset:5888\n"
  "v_mfma_f32_16x16x32_bf16 v[16:19], v[32:35], a[96:99], v[92:95]\n"
  "v_exp_f32 v26, v26\n"
  "v_mfma_f32_16x16x32_bf16 v[16:19], v[36:39], a[100:103], v[16:19]\n"
  "v_exp_f32 v30, v30\n"
  "v_mfma_f32_16x16x32_bf16 v[16:19], v[40:43], a[104:107], v[16:19]\n"
  "v_exp_f32 v27, v27\n"
  "v_mfma_f32_16x16x32_bf16 v[16:19], v[44:47], a[108:111], v[16:19]\n"
  "v_exp_f32 v31, v31\n"
  "v_mfma_f32_16x16x32_bf16 v[16:19], v[48:51], a[112:115], v[16:19]\n"
  "v_cvt_pk_bf16_f32 v26, v26, v30\n"
  "v_cvt_pk_bf16_f32 v27, v27, v31\n"
  "v_mfma_f32_16x16x32_bf16 v[16:19], v[52:55], a[116:119], v[16:19]\n"
  "ds_write_addtid_b32 v26 offset:10016\n"
  "ds_write_addtid_b32 v27 offset:14112\n"
  "v_pk_mul_f32 v[0:1], v[0:1], s[60:61] op_sel_hi:[1,0]\n"
  "v_pk_mul_f32 v[2:3], v[2:3], s[60:61] op_sel_hi:[1,0]\n"
  "v_pk_mul_f32 v[4:5], v[4:5], s[60:61] op_sel_hi:[1,0]\n"
  "v_pk_mul_f32 v[6:7], v[6:7], s[60:61] op_sel_hi:[1,0]\n"
  "v_pk_mul_f32 v[8:9], v[8:9], s[60:61] op_sel_hi:[1,0]\n"
  "v_pk_mul_f32 v[10:11], v[10:11], s[60:61] op_sel_hi:[1,0]\n"
  "v_pk_mul_f32 v[12:13], v[12:13], s[60:61] op_sel_hi:[1,0]\n"
  "v_pk_mul_f32 v[14:15], v[14:15], s[60:61] op_sel_hi:[1,0]\n"
  "v_mfma_f32_16x16x32_bf16 v[20:23], v[52:55], a[140:143], v[92:95]\n"
  "s_add_u32 s88, s88, 16448\n"
  "v_exp_f32 v0, v0\n"
  "v_mfma_f32_16x16x32_bf16 v[20:23], v[48:51], a[136:139], v[20:23]\n"
  "s_cmp_eq_u32 s88, s68\n"
  "v_exp_f32 v4, v4\n"
  "v_mfma_f32_16x16x32_bf16 v[20:23], v[44:47], a[132:135], v[20:23]\n"
  "s_cselect_b32 s88, s89, s88\n"
  "v_exp_f32 v1, v1\n"
  "v_mfma_f32_16x16x32_bf16 v[20:23], v[40:43], a[128:131], v[20:23]\n"
  "v_exp_f32_e64 v5, v5\n"
  "v_mfma_f32_16x16x32_bf16 v[20:23], v[36:39], a[124:127], v[20:23]\n"
  "s_mov_b32 m0, s88\n"
  "v_cvt_pk_bf16_f32 v0, v0, v4\n"
  "v_cvt_pk_bf16_f32 v1, v1, v5\n"
  "v_mfma_f32_16x16x32_bf16 v[20:23], v[32:35], a[120:123], v[20:23]\n"
  "ds_write_addtid_b32 v0 offset:0\n"
  "ds_write_addtid_b32 v1 offset:4096\n"
  "v_mfma_f32_16x16x32_bf16 v[24:27], v[32:35], a[144:147], v[92:95]\n"
  "v_exp_f32 v2, v2\n"
  "v_mfma_f32_16x16x32_bf16 v[24:27], v[36:39], a[148:151], v[24:27]\n"
  "v_exp_f32 v6, v6\n"
  "v_mfma_f32_16x16x32_bf16 v[24:27], v[40:43], a[152:155], v[24:27]\n"
  "v_exp_f32 v3, v3\n"
  "v_mfma_f32_16x16x32_bf16 v[24:27], v[44:47], a[156:159], v[24:27]\n"
  "v_exp_f32 v7, v7\n"
  "v_mfma_f32_16x16x32_bf16 v[24:27], v[48:51], a[160:163], v[24:27]\n"
  "v_cvt_pk_bf16_f32 v2, v2, v6\n"
  "v_cvt_pk_bf16_f32 v3, v3, v7\n"
  "v_mfma_f32_16x16x32_bf16 v[24:27], v[52:55], a[164:167], v[24:27]\n"
  "ds_write_addtid_b32 v2 offset:8224\n"
  "ds_write_addtid_b32 v3 offset:12320\n"
  "v_mfma_f32_16x16x32_bf16 v[28:31], v[52:55], a[188:191], v[92:95]\n"
  "v_exp_f32 v8, v8\n"
  "v_mfma_f32_16x16x32_bf16 v[28:31], v[48:51], a[184:187], v[28:31]\n"
  "v_exp_f32 v12, v12\n"
  "v_mfma_f32_16x16x32_bf16 v[28:31], v[44:47], a[180:183], v[28:31]\n"
  "v_exp_f32 v9, v9\n"
  "v_mfma_f32_16x16x32_bf16 v[28:31], v[40:43], a[176:179], v[28:31]\n"
  "v_exp_f32 v13, v13\n"
  "v_mfma_f32_16x16x32_bf16 v[28:31], v[36:39], a[172:175], v[28:31]\n"
  "v_cvt_pk_bf16_f32 v8, v8, v12\n"
  "v_cvt_pk_bf16_f32 v9, v9, v13\n"
  "v_mfma_f32_16x16x32_bf16 v[28:31], v[32:35], a[168:171], v[28:31]\n"
  "ds_write_addtid_b32 v8 offset:256\n"
  "ds_write_addtid_b32 v9 offset:4352\n"
  "v_mfma_f32_16x16x32_bf16 v[0:3], v[32:35], a[192:195], v[92:95]\n"
  "v_exp_f32 v10, v10\n"
  "v_mfma_f32_16x16x32_bf16 v[0:3], v[36:39], a[196:199], v[0:3]\n"
  "v_exp_f32 v14, v14\n"
  "v_mfma_f32_16x16x32_bf16 v[0:3], v[40:43], a[200:203], v[0:3]\n"
  "v_exp_f32 v11, v11\n"
  "v_mfma_f32_16x16x32_bf16 v[0:3], v[44:47], a[204:207], v[0:3]\n"
  "v_exp_f32 v15, v15\n"
  "v_mfma_f32_16x16x32_bf16 v[0:3], v[48:51], a[208:211], v[0:3]\n"
  "v_cvt_pk_bf16_f32 v10, v10, v14\n"
  "v_cvt_pk_bf16_f32 v11, v11, v15\n"
  "v_mfma_f32_16x16x32_bf16 v[0:3], v[52:55], a[212:215], v[0:3]\n"
  "ds_read_b128 v[56:59], v81 offset:0\n"
  "ds_read_b128 v[60:63], v82 offset:0\n"
  "ds_read_b128 v[64:67], v81 offset:2048\n"
  "ds_write_addtid_b32 v10 offset:8480\n"
  "ds_write_addtid_b32 v11 offset:12576\n"
  "v_pk_mul_f32 v[16:17], v[16:17], s[60:61] op_sel_hi:[1,0]\n"
  "v_pk_mul_f32 v[18:19], v[18:19], s[60:61] op_sel_hi:[1,0]\n"
  "v_pk_mul_f32 v[20:21], v[20:21], s[60:61] op_sel_hi:[1,0]\n"
  "v_pk_mul_f32 v[22:23], v[22:23], s[60:61] op_sel_hi:[1,0]\n"
  "v_pk_mul_f32 v[24:25], v[24:25], s[60:61] op_sel_hi:[1,0]\n"
  "v_pk_mul_f32 v[26:27], v[26:27], s[60:61] op_sel_hi:[1,0]\n"
  "v_pk_mul_f32 v[28:29], v[28:29], s[60:61] op_sel_hi:[1,0]\n"
  "v_pk_mul_f32 v[30:31], v[30:31], s[60:61] op_sel_hi:[1,0]\n"
  "v_mfma_f32_16x16x32_bf16 v[4:7], v[52:55], a[236:239], v[92:95]\n"
  "v_exp_f32 v16, v16\n"
  "v_mfma_f32_16x16x32_bf16 v[4:7], v[48:51], a[232:235], v[4:7]\n"
  "v_exp_f32 v20, v20\n"
  "v_mfma_f32_16x16x32_bf16 v[4:7], v[44:47], a[228:231], v[4:7]\n"
  "v_exp_f32 v17, v17\n"
  "v_mfma_f32_16x16x32_bf16 v[4:7], v[40:43], a[224:227], v[4:7]\n"
  "v_exp_f32 v21, v21\n"
  "v_mfma_f32_16x16x32_bf16 v[4:7], v[36:39], a[220:223], v[4:7]\n"
  "v_cvt_pk_bf16_f32 v16, v16, v20\n"
  "v_cvt_pk_bf16_f32 v17, v17, v21\n"
  "v_mfma_f32_16x16x32_bf16 v[4:7], v[32:35], a[216:219], v[4:7]\n"
  "ds_write_addtid_b32 v16 offset:512\n"
  "ds_write_addtid_b32 v17 offset:4608\n"
  "v_mfma_f32_16x16x32_bf16 v[8:11], v[32:35], a[240:243], v[92:95]\n"
  "v_exp_f32 v18, v18\n"
  "v_mfma_f32_16x16x32_bf16 v[8:11], v[36:39], a[244:247], v[8:11]\n"
  "v_exp_f32 v22, v22\n"
  "v_mfma_f32_16x16x32_bf16 v[8:11], v[40:43], a[248:251], v[8:11]\n"
  "v_exp_f32 v19, v19\n"
  "v_mfma_f32_16x16x32_bf16 v[8:11], v[44:47], a[252:255], v[8:11]\n"
  "v_exp_f32 v23, v23\n"
  "v_mfma_f32_16x16x32_bf16 v[8:11], v[48:51], v[128:131], v[8:11]\n"
  "v_cvt_pk_bf16_f32 v18, v18, v22\n"
  "v_cvt_pk_bf16_f32 v19, v19, v23\n"
  "v_mfma_f32_16x16x32_bf16 v[8:11], v[52:55], v[132:135], v[8:11]\n"
  "ds_write_addtid_b32 v18 offset:8736\n"
  "ds_write_addtid_b32 v19 offset:12832\n"
  "v_mfma_f32_16x16x32_bf16 v[12:15], v[52:55], v[156:159], v[92:95]\n"
  "v_exp_f32 v24, v24\n"
  "v_mfma_f32_16x16x32_bf16 v[12:15], v[48:51], v[152:155], v[12:15]\n"
  "v_exp_f32 v28, v28\n"
  "v_mfma_f32_16x16x32_bf16 v[12:15], v[44:47], v[148:151], v[12:15]\n"
  "v_exp_f32 v25, v25\n"
  "v_mfma_f32_16x16x32_bf16 v[12:15], v[40:43], v[144:147], v[12:15]\n"
  "v_exp_f32 v29, v29\n"
  "v_mfma_f32_16x16x32_bf16 v[12:15], v[36:39], v[140:143], v[12:15]\n"
  "v_cvt_pk_bf16_f32 v24, v24, v28\n"
  "v_cvt_pk_bf16_f32 v25, v25, v29\n"
  "v_mfma_f32_16x16x32_bf16 v[12:15], v[32:35], v[136:139], v[12:15]\n"
  "ds_write_addtid_b32 v24 offset:768\n"
  "ds_write_addtid_b32 v25 offset:4864\n"
  "v_mfma_f32_16x16x32_bf16 v[16:19], v[32:35], v[160:163], v[92:95]\n"
  "v_exp_f32 v26, v26\n"
  "v_mfma_f32_16x16x32_bf16 v[16:19], v[36:39], v[164:167], v[16:19]\n"
  "v_exp_f32 v30, v30\n"
  "v_mfma_f32_16x16x32_bf16 v[16:19], v[40:43], v[168:171], v[16:19]\n"
  "v_exp_f32 v27, v27\n"
  "v_mfma_f32_16x16x32_bf16 v[16:19], v[44:47], v[172:175], v[16:19]\n"
  "v_exp_f32 v31, v31\n"
  "v_mfma_f32_16x16x32_bf16 v[16:19], v[48:51], v[176:179], v[16:19]\n"
  "v_cvt_pk_bf16_f32 v26, v26, v30\n"
  "v_cvt_pk_bf16_f32 v27, v27, v31\n"
  "v_mfma_f32_16x16x32_bf16 v[16:19], v[52:55], v[180:183], v[16:19]\n"
  "ds_read_b128 v[68:71], v82 offset:2048\n"
  "ds_read_b128 v[72:75], v81 offset:4096\n"
  "ds_read_b128 v[76:79], v82 offset:4096\n"
  "ds_write_addtid_b32 v26 offset:8992\n"
  "ds_write_addtid_b32 v27 offset:13088\n"
  "v_pk_mul_f32 v[0:1], v[0:1], s[60:61] op_sel_hi:[1,0]\n"
  "v_pk_mul_f32 v[2:3], v[2:3], s[60:61] op_sel_hi:[1,0]\n"
  "v_pk_mul_f32 v[4:5], v[4:5], s[60:61] op_sel_hi:[1,0]\n"
  "v_pk_mul_f32 v[6:7], v[6:7], s[60:61] op_sel_hi:[1,0]\n"
  "v_pk_mul_f32 v[8:9], v[8:9], s[60:61] op_sel_hi:[1,0]\n"
  "v_pk_mul_f32 v[10:11], v[10:11], s[60:61] op_sel_hi:[1,0]\n"
  "v_pk_mul_f32 v[12:13], v[12:13], s[60:61] op_sel_hi:[1,0]\n"
  "v_pk_mul_f32 v[14:15], v[14:15], s[60:61] op_sel_hi:[1,0]\n"
  "v_mfma_f32_16x16x32_bf16 v[20:23], v[52:55], v[204:207], v[92:95]\n"
  "v_exp_f32 v0, v0\n"
  "v_mfma_f32_16x16x32_bf16 v[20:23], v[48:51], v[200:203], v[20:23]\n"
  "v_exp_f32 v4, v4\n"
  "v_mfma_f32_16x16x32_bf16 v[20:23], v[44:47], v[196:199], v[20:23]\n"
  "v_exp_f32 v1, v1\n"
  "v_mfma_f32_16x16x32_bf16 v[20:23], v[40:43], v[192:195], v[20:23]\n"
  "v_exp_f32 v5, v5\n"
  "v_mfma_f32_16x16x32_bf16 v[20:23], v[36:39], v[188:191], v[20:23]\n"
  "v_cvt_pk_bf16_f32 v0, v0, v4\n"
  "v_cvt_pk_bf16_f32 v1, v1, v5\n"
  "v_mfma_f32_16x16x32_bf16 v[20:23], v[32:35], v[184:187], v[20:23]\n"
  "ds_write_addtid_b32 v0 offset:1024\n"
  "ds_write_addtid_b32 v1 offset:5120\n"
  "v_mfma_f32_16x16x32_bf16 v[24:27], v[32:35], v[208:211], v[92:95]\n"
  "v_exp_f32 v2, v2\n"
  "v_mfma_f32_16x16x32_bf16 v[24:27], v[36:39], v[212:215], v[24:27]\n"
  "v_exp_f32 v6, v6\n"
  "v_mfma_f32_16x16x32_bf16 v[24:27], v[40:43], v[216:219], v[24:27]\n"
  "v_exp_f32 v3, v3\n"
  "v_mfma_f32_16x16x32_bf16 v[24:27], v[44:47], v[220:223], v[24:27]\n"
  "v_exp_f32 v7, v7\n"
  "v_mfma_f32_16x16x32_bf16 v[24:27], v[48:51], v[224:227], v[24:27]\n"
  "v_cvt_pk_bf16_f32 v2, v2, v6\n"
  "v_cvt_pk_bf16_f32 v3, v3, v7\n"
  "v_mfma_f32_16x16x32_bf16 v[24:27], v[52:55], v[228:231], v[24:27]\n"
  "ds_write_addtid_b32 v2 offset:9248\n"
  "ds_write_addtid_b32 v3 offset:13344\n"
  "v_mfma_f32_16x16x32_bf16 v[28:31], v[52:55], v[252:255], v[92:95]\n"
  "v_exp_f32 v8, v8\n"
  "v_mfma_f32_16x16x32_bf16 v[28:31], v[48:51], v[248:251], v[28:31]\n"
  "v_exp_f32 v12, v12\n"
  "v_mfma_f32_16x16x32_bf16 v[28:31], v[44:47], v[244:247], v[28:31]\n"
  "v_exp_f32 v9, v9\n"
  "v_mfma_f32_16x16x32_bf16 v[28:31], v[40:43], v[240:243], v[28:31]\n"
  "s_sub_u32 s56, s56, 1\n"
  "v_exp_f32 v13, v13\n"
  "v_mfma_f32_16x16x32_bf16 v[28:31], v[36:39], v[236:239], v[28:31]\n"
  "v_cvt_pk_bf16_f32 v8, v8, v12\n"
  "v_cvt_pk_bf16_f32 v9, v9, v13\n"
  "v_mfma_f32_16x16x32_bf16 v[28:31], v[32:35], v[232:235], v[28:31]\n"
  "s_waitcnt lgkmcnt(6)\n"
  "ds_write_addtid_b32 v8 offset:1280\n"
  "ds_write_addtid_b32 v9 offset:5376\n"
  ".p2align 3\n"
  ".p2align 6\n"
  "A_H1_%=:\n"
  "v_mfma_f32_16x16x32_bf16 v[0:3], v[56:59], a[0:3], v[92:95]\n"
  "s_add_u32 s66, s66, 6144\n"
  "v_exp_f32 v10, v10\n"
  "v_mfma_f32_16x16x32_bf16 v[0:3], v[60:63], a[4:7], v[0:3]\n"
  "s_cmp_eq_u32 s66, 102400\n"
  "v_exp_f32 v14, v14\n"
  "v_mfma_f32_16x16x32_bf16 v[0:3], v[64:67], a[8:11], v[0:3]\n"
  "s_cselect_b32 s66, 65536, s66\n"
  "v_exp_f32 v11, v11\n"
  "v_mfma_f32_16x16x32_bf16 v[0:3], v[68:71], a[12:15], v[0:3]\n"
  "v_exp_f32 v15, v15\n"
  "v_mfma_f32_16x16x32_bf16 v[0:3], v[72:75], a[16:19], v[0:3]\n"
  "v_cvt_pk_bf16_f32 v10, v10, v14\n"
  "v_cvt_pk_bf16_f32 v11, v11, v15\n"
  "v_mfma_f32_16x16x32_bf16 v[0:3], v[76:79], a[20:23], v[0:3]\n"
  "ds_write_addtid_b32 v10 offset:9504\n"
  "ds_write_addtid_b32 v11 offset:13600\n"
  "v_add_u32 v81, s66, v83\n"
  "v_add_u32 v82, s66, v84\n"
  "v_pk_mul_f32 v[16:17], v[16:17], s[60:61] op_sel_hi:[1,0]\n"
  "v_pk_mul_f32 v[18:19], v[18:19], s[60:61] op_sel_hi:[1,0]\n"
  "v_pk_mul_f32 v[20:21], v[20:21], s[60:61] op_sel_hi:[1,0]\n"
  "v_pk_mul_f32 v[22:23], v[22:23], s[60:61] op_sel_hi:[1,0]\n"
  "v_pk_mul_f32 v[24:25], v[24:25], s[60:61] op_sel_hi:[1,0]\n"
  "v_pk_mul_f32 v[26:27], v[26:27], s[60:61] op_sel_hi:[1,0]\n"
  "v_pk_mul_f32 v[28:29], v[28:29], s[60:61] op_sel_hi:[1,0]\n"
  "v_pk_mul_f32 v[30:31], v[30:31], s[60:61] op_sel_hi:[1,0]\n"
  "v_mfma_f32_16x16x32_bf16 v[4:7], v[76:79], a[44:47], v[92:95]\n"
  "v_exp_f32 v16, v16\n"
  "v_mfma_f32_16x16x32_bf16 v[4:7], v[72:75], a[40:43], v[4:7]\n"
  "v_exp_f32 v20, v20\n"
  "v_mfma_f32_16x16x32_bf16 v[4:7], v[68:71], a[36:39], v[4:7]\n"
  "v_exp_f32 v17, v17\n"
  "v_mfma_f32_16x16x32_bf16 v[4:7], v[64:67], a[32:35], v[4:7]\n"
  "v_exp_f32 v21, v21\n"
  "v_mfma_f32_16x16x32_bf16 v[4:7], v[60:63], a[28:31], v[4:7]\n"
  "v_cvt_pk_bf16_f32 v16, v16, v20\n"
  "v_cvt_pk_bf16_f32 v17, v17, v21\n"
  "v_mfma_f32_16x16x32_bf16 v[4:7], v[56:59], a[24:27], v[4:7]\n"
  "ds_write_addtid_b32 v16 offset:1536\n"
  "ds_write_addtid_b32 v17 offset:5632\n"
  "v_mfma_f32_16x16x32_bf16 v[8:11], v[56:59], a[48:51], v[92:95]\n"
  "v_exp_f32 v18, v18\n"
  "v_mfma_f32_16x16x32_bf16 v[8:11], v[60:63], a[52:55], v[8:11]\n"
  "v_exp_f32 v22, v22\n"
  "v_mfma_f32_16x16x32_bf16 v[8:11], v[64:67], a[56:59], v[8:11]\n"
  "v_exp_f32 v19, v19\n"
  "v_mfma_f32_16x16x32_bf16 v[8:11], v[68:71], a[60:63], v[8:11]\n"
  "v_exp_f32 v23, v23\n"
  "v_mfma_f32_16x16x32_bf16 v[8:11], v[72:75], a[64:67], v[8:11]\n"
  "v_cvt_pk_bf16_f32 v18, v18, v22\n"
  "v_cvt_pk_bf16_f32 v19, v19, v23\n"
  "v_mfma_f32_16x16x32_bf16 v[8:11], v[76:79], a[68:71], v[8:11]\n"
  "ds_write_addtid_b32 v18 offset:9760\n"
  "ds_write_addtid_b32 v19 offset:13856\n"
  "v_mfma_f32_16x16x32_bf16 v[12:15], v[76:79], a[92:95], v[92:95]\n"
  "v_exp_f32 v24, v24\n"
  "v_mfma_f32_16x16x32_bf16 v[12:15], v[72:75], a[88:91], v[12:15]\n"
  "v_exp_f32 v28, v28\n"
  "v_mfma_f32_16x16x32_bf16 v[12:15], v[68:71], a[84:87], v[12:15]\n"
  "v_exp_f32 v25, v25\n"
  "v_mfma_f32_16x16x32_bf16 v[12:15], v[64:67], a[80:83], v[12:15]\n"
  "v_exp_f32 v29, v29\n"
  "v_mfma_f32_16x16x32_bf16 v[12:15], v[60:63], a[76:79], v[12:15]\n"
  "v_cvt_pk_bf16_f32 v24, v24, v28\n"
  "v_cvt_pk_bf16_f32 v25, v25, v29\n"
  "v_mfma_f32_16x16x32_bf16 v[12:15], v[56:59], a[72:75], v[12:15]\n"
  "ds_write_addtid_b32 v24 offset:1792\n"
  "ds_write_addtid_b32 v25 offset:5888\n"
  "v_mfma_f32_16x16x32_bf16 v[16:19], v[56:59], a[96:99], v[92:95]\n"
  "v_exp_f32 v26, v26\n"
  "v_mfma_f32_16x16x32_bf16 v[16:19], v[60:63], a[100:103], v[16:19]\n"
  "v_exp_f32 v30, v30\n"
  "v_mfma_f32_16x16x32_bf16 v[16:19], v[64:67], a[104:107], v[16:19]\n"
  "v_exp_f32 v27, v27\n"
  "v_mfma_f32_16x16x32_bf16 v[16:19], v[68:71], a[108:111], v[16:19]\n"
  "v_exp_f32_e64 v31, v31\n"
  "v_mfma_f32_16x16x32_bf16 v[16:19], v[72:75], a[112:115], v[16:19]\n"
  "v_cvt_pk_bf16_f32 v26, v26, v30\n"
  "v_cvt_pk_bf16_f32 v27, v27, v31\n"
  "v_mfma_f32_16x16x32_bf16 v[16:19], v[76:79], a[116:119], v[16:19]\n"
  "s_barrier\n"
  "ds_write_addtid_b32 v26 offset:10016\n"
  "ds_write_addtid_b32 v27 offset:14112\n"
  "v_pk_mul_f32 v[0:1], v[0:1], s[60:61] op_sel_hi:[1,0]\n"
  "v_pk_mul_f32 v[2:3], v[2:3], s[60:61] op_sel_hi:[1,0]\n"
  "v_pk_mul_f32 v[4:5], v[4:5], s[60:61] op_sel_hi:[1,0]\n"
  "v_pk_mul_f32 v[6:7], v[6:7], s[60:61] op_sel_hi:[1,0]\n"
  "v_pk_mul_f32 v[8:9], v[8:9], s[60:61] op_sel_hi:[1,0]\n"
  "v_pk_mul_f32 v[10:11], v[10:11], s[60:61] op_sel_hi:[1,0]\n"
  "v_pk_mul_f32 v[12:13], v[12:13], s[60:61] op_sel_hi:[1,0]\n"
  "v_pk_mul_f32 v[14:15], v[14:15], s[60:61] op_sel_hi:[1,0]\n"
  "v_mfma_f32_16x16x32_bf16 v[20:23], v[76:79], a[140:143], v[92:95]\n"
  "s_add_u32 s88, s88, 16448\n"
  "v_exp_f32 v0, v0\n"
  "v_mfma_f32_16x16x32_bf16 v[20:23], v[72:75], a[136:139], v[20:23]\n"
  "s_cmp_eq_u32 s88, s68\n"
  "v_exp_f32 v4, v4\n"
  "v_mfma_f32_16x16x32_bf16 v[20:23], v[68:71], a[132:135], v[20:23]\n"
  "s_cselect_b32 s88, s89, s88\n"
  "v_exp_f32 v1, v1\n"
  "v_mfma_f32_16x16x32_bf16 v[20:23], v[64:67], a[128:131], v[20:23]\n"
  "v_exp_f32_e64 v5, v5\n"
  "v_mfma_f32_16x16x32_bf16 v[20:23], v[60:63], a[124:127], v[20:23]\n"
  "s_mov_b32 m0, s88\n"
  "v_cvt_pk_bf16_f32 v0, v0, v4\n"
  "v_cvt_pk_bf16_f32 v1, v1, v5\n"
  "v_mfma_f32_16x16x32_bf16 v[20:23], v[56:59], a[120:123], v[20:23]\n"
  "ds_write_addtid_b32 v0 offset:0\n"
  "ds_write_addtid_b32 v1 offset:4096\n"
  "v_mfma_f32_16x16x32_bf16 v[24:27], v[56:59], a[144:147], v[92:95]\n"
  "v_exp_f32 v2, v2\n"
  "v_mfma_f32_16x16x32_bf16 v[24:27], v[60:63], a[148:151], v[24:27]\n"
  "v_exp_f32 v6, v6\n"
  "v_mfma_f32_16x16x32_bf16 v[24:27], v[64:67], a[152:155], v[24:27]\n"
  "v_exp_f32 v3, v3\n"
  "v_mfma_f32_16x16x32_bf16 v[24:27], v[68:71], a[156:159], v[24:27]\n"
  "v_exp_f32 v7, v7\n"
  "v_mfma_f32_16x16x32_bf16 v[24:27], v[72:75], a[160:163], v[24:27]\n"
  "v_cvt_pk_bf16_f32 v2, v2, v6\n"
  "v_cvt_pk_bf16_f32 v3, v3, v7\n"
  "v_mfma_f32_16x16x32_bf16 v[24:27], v[76:79], a[164:167], v[24:27]\n"
  "ds_write_addtid_b32 v2 offset:8224\n"
  "ds_write_addtid_b32 v3 offset:12320\n"
  "v_mfma_f32_16x16x32_bf16 v[28:31], v[76:79], a[188:191], v[92:95]\n"
  "v_exp_f32 v8, v8\n"
  "v_mfma_f32_16x16x32_bf16 v[28:31], v[72:75], a[184:187], v[28:31]\n"
  "v_exp_f32 v12, v12\n"
  "v_mfma_f32_16x16x32_bf16 v[28:31], v[68:71], a[180:183], v[28:31]\n"
  "v_exp_f32 v9, v9\n"
  "v_mfma_f32_16x16x32_bf16 v[28:31], v[64:67], a[176:179], v[28:31]\n"
  "v_exp_f32 v13, v13\n"
  "v_mfma_f32_16x16x32_bf16 v[28:31], v[60:63], a[172:175], v[28:31]\n"
  "v_cvt_pk_bf16_f32 v8, v8, v12\n"
  "v_cvt_pk_bf16_f32 v9, v9, v13\n"
  "v_mfma_f32_16x16x32_bf16 v[28:31], v[56:59], a[168:171], v[28:31]\n"
  "ds_write_addtid_b32 v8 offset:256\n"
  "ds_write_addtid_b32 v9 offset:4352\n"
  "v_mfma_f32_16x16x32_bf16 v[0:3], v[56:59], a[192:195], v[92:95]\n"
  "v_exp_f32 v10, v10\n"
  "v_mfma_f32_16x16x32_bf16 v[0:3], v[60:63], a[196:199], v[0:3]\n"
  "v_exp_f32 v14, v14\n"
  "v_mfma_f32_16x16x32_bf16 v[0:3], v[64:67], a[200:203], v[0:3]\n"
  "v_exp_f32 v11, v11\n"
  "v_mfma_f32_16x16x32_bf16 v[0:3], v[68:71], a[204:207], v[0:3]\n"
  "v_exp_f32 v15, v15\n"
  "v_mfma_f32_16x16x32_bf16 v[0:3], v[72:75], a[208:211], v[0:3]\n"
  "v_cvt_pk_bf16_f32 v10, v10, v14\n"
  "v_cvt_pk_bf16_f32 v11, v11, v15\n"
  "v_mfma_f32_16x16x32_bf16 v[0:3], v[76:79], a[212:215], v[0:3]\n"
  "ds_read_b128 v[32:35], v81 offset:0\n"
  "ds_read_b128 v[36:39], v82 offset:0\n"
  "ds_read_b128 v[40:43], v81 offset:2048\n"
  "ds_write_addtid_b32 v10 offset:8480\n"
  "ds_write_addtid_b32 v11 offset:12576\n"
  "v_pk_mul_f32 v[16:17], v[16:17], s[60:61] op_sel_hi:[1,0]\n"
  "v_pk_mul_f32 v[18:19], v[18:19], s[60:61] op_sel_hi:[1,0]\n"
  "v_pk_mul_f32 v[20:21], v[20:21], s[60:61] op_sel_hi:[1,0]\n"
  "v_pk_mul_f32 v[22:23], v[22:23], s[60:61] op_sel_hi:[1,0]\n"
  "v_pk_mul_f32 v[24:25], v[24:25], s[60:61] op_sel_hi:[1,0]\n"
  "v_pk_mul_f32 v[26:27], v[26:27], s[60:61] op_sel_hi:[1,0]\n"
  "v_pk_mul_f32 v[28:29], v[28:29], s[60:61] op_sel_hi:[1,0]\n"
  "v_pk_mul_f32 v[30:31], v[30:31], s[60:61] op_sel_hi:[1,0]\n"
  "v_mfma_f32_16x16x32_bf16 v[4:7], v[76:79], a[236:239], v[92:95]\n"
  "v_exp_f32 v16, v16\n"
  "v_mfma_f32_16x16x32_bf16 v[4:7], v[72:75], a[232:235], v[4:7]\n"
  "v_exp_f32 v20, v20\n"
  "v_mfma_f32_16x16x32_bf16 v[4:7], v[68:71], a[228:231], v[4:7]\n"
  "v_exp_f32 v17, v17\n"
  "v_mfma_f32_16x16x32_bf16 v[4:7], v[64:67], a[224:227], v[4:7]\n"
  "v_exp_f32 v21, v21\n"
  "v_mfma_f32_16x16x32_bf16 v[4:7], v[60:63], a[220:223], v[4:7]\n"
  "v_cvt_pk_bf16_f32 v16, v16, v20\n"
  "v_cvt_pk_bf16_f32 v17, v17, v21\n"
  "v_mfma_f32_16x16x32_bf16 v[4:7], v[56:59], a[216:219], v[4:7]\n"
  "ds_write_addtid_b32 v16 offset:512\n"
  "ds_write_addtid_b32 v17 offset:4608\n"
  "v_mfma_f32_16x16x32_bf16 v[8:11], v[56:59], a[240:243], v[92:95]\n"
  "v_exp_f32 v18, v18\n"
  "v_mfma_f32_16x16x32_bf16 v[8:11], v[60:63], a[244:247], v[8:11]\n"
  "v_exp_f32 v22, v22\n"
  "v_mfma_f32_16x16x32_bf16 v[8:11], v[64:67], a[248:251], v[8:11]\n"
  "v_exp_f32 v19, v19\n"
  "v_mfma_f32_16x16x32_bf16 v[8:11], v[68:71], a[252:255], v[8:11]\n"
  "v_exp_f32 v23, v23\n"
  "v_mfma_f32_16x16x32_bf16 v[8:11], v[72:75], v[128:131], v[8:11]\n"
  "v_cvt_pk_bf16_f32 v18, v18, v22\n"
  "v_cvt_pk_bf16_f32 v19, v19, v23\n"
  "v_mfma_f32_16x16x32_bf16 v[8:11], v[76:79], v[132:135], v[8:11]\n"
  "ds_write_addtid_b32 v18 offset:8736\n"
  "ds_write_addtid_b32 v19 offset:12832\n"
  "v_mfma_f32_16x16x32_bf16 v[12:15], v[76:79], v[156:159], v[92:95]\n"
  "v_exp_f32 v24, v24\n"
  "v_mfma_f32_16x16x32_bf16 v[12:15], v[72:75], v[152:155], v[12:15]\n"
  "v_exp_f32 v28, v28\n"
  "v_mfma_f32_16x16x32_bf16 v[12:15], v[68:71], v[148:151], v[12:15]\n"
  "v_exp_f32 v25, v25\n"
  "v_mfma_f32_16x16x32_bf16 v[12:15], v[64:67], v[144:147], v[12:15]\n"
  "v_exp_f32 v29, v29\n"
  "v_mfma_f32_16x16x32_bf16 v[12:15], v[60:63], v[140:143], v[12:15]\n"
  "v_cvt_pk_bf16_f32 v24, v24, v28\n"
  "v_cvt_pk_bf16_f32 v25, v25, v29\n"
  "v_mfma_f32_16x16x32_bf16 v[12:15], v[56:59], v[136:139], v[12:15]\n"
  "ds_write_addtid_b32 v24 offset:768\n"
  "ds_write_addtid_b32 v25 offset:4864\n"
  "v_mfma_f32_16x16x32_bf16 v[16:19], v[56:59], v[160:163], v[92:95]\n"
  "v_exp_f32 v26, v26\n"
  "v_mfma_f32_16x16x32_bf16 v[16:19], v[60:63], v[164:167], v[16:19]\n"
  "v_exp_f32 v30, v30\n"
  "v_mfma_f32_16x16x32_bf16 v[16:19], v[64:67], v[168:171], v[16:19]\n"
  "v_exp_f32 v27, v27\n"
  "v_mfma_f32_16x16x32_bf16 v[16:19], v[68:71], v[172:175], v[16:19]\n"
  "v_exp_f32 v31, v31\n"
  "v_mfma_f32_16x16x32_bf16 v[16:19], v[72:75], v[176:179], v[16:19]\n"
  "v_cvt_pk_bf16_f32 v26, v26, v30\n"
  "v_cvt_pk_bf16_f32 v27, v27, v31\n"
  "v_mfma_f32_16x16x32_bf16 v[16:19], v[76:79], v[180:183], v[16:19]\n"
  "ds_read_b128 v[44:47], v82 offset:2048\n"
  "ds_read_b128 v[48:51], v81 offset:4096\n"
  "ds_read_b128 v[52:55], v82 offset:4096\n"
  "ds_write_addtid_b32 v26 offset:8992\n"
  "ds_write_addtid_b32 v27 offset:13088\n"
  "v_pk_mul_f32 v[0:1], v[0:1], s[60:61] op_sel_hi:[1,0]\n"
  "v_pk_mul_f32 v[2:3], v[2:3], s[60:61] op_sel_hi:[1,0]\n"
  "v_pk_mul_f32 v[4:5], v[4:5], s[60:61] op_sel_hi:[1,0]\n"
  "v_pk_mul_f32 v[6:7], v[6:7], s[60:61] op_sel_hi:[1,0]\n"
  "v_pk_mul_f32 v[8:9], v[8:9], s[60:61] op_sel_hi:[1,0]\n"
  "v_pk_mul_f32 v[10:11], v[10:11], s[60:61] op_sel_hi:[1,0]\n"
  "v_pk_mul_f32 v[12:13], v[12:13], s[60:61] op_sel_hi:[1,0]\n"
  "v_pk_mul_f32 v[14:15], v[14:15], s[60:61] op_sel_hi:[1,0]\n"
  "v_mfma_f32_16x16x32_bf16 v[20:23], v[76:79], v[204:207], v[92:95]\n"
  "v_exp_f32 v0, v0\n"
  "v_mfma_f32_16x16x32_bf16 v[20:23], v[72:75], v[200:203], v[20:23]\n"
  "v_exp_f32 v4, v4\n"
  "v_mfma_f32_16x16x32_bf16 v[20:23], v[68:71], v[196:199], v[20:23]\n"
  "v_exp_f32 v1, v1\n"
  "v_mfma_f32_16x16x32_bf16 v[20:23], v[64:67], v[192:195], v[20:23]\n"
  "v_exp_f32 v5, v5\n"
  "v_mfma_f32_16x16x32_bf16 v[20:23], v[60:63], v[188:191], v[20:23]\n"
  "v_cvt_pk_bf16_f32 v0, v0, v4\n"
  "v_cvt_pk_bf16_f32 v1, v1, v5\n"
  "v_mfma_f32_16x16x32_bf16 v[20:23], v[56:59], v[184:187], v[20:23]\n"
  "ds_write_addtid_b32 v0 offset:1024\n"
  "ds_write_addtid_b32 v1 offset:5120\n"
  "v_mfma_f32_16x16x32_bf16 v[24:27], v[56:59], v[208:211], v[92:95]\n"
  "v_exp_f32 v2, v2\n"
  "v_mfma_f32_16x16x32_bf16 v[24:27], v[60:63], v[212:215], v[24:27]\n"
  "v_exp_f32 v6, v6\n"
  "v_mfma_f32_16x16x32_bf16 v[24:27], v[64:67], v[216:219], v[24:27]\n"
  "v_exp_f32 v3, v3\n"
  "v_mfma_f32_16x16x32_bf16 v[24:27], v[68:71], v[220:223], v[24:27]\n"
  "v_exp_f32 v7, v7\n"
  "v_mfma_f32_16x16x32_bf16 v[24:27], v[72:75], v[224:227], v[24:27]\n"
  "v_cvt_pk_bf16_f32 v2, v2, v6\n"
  "v_cvt_pk_bf16_f32 v3, v3, v7\n"
  "v_mfma_f32_16x16x32_bf16 v[24:27], v[76:79], v[228:231], v[24:27]\n"
  "ds_write_addtid_b32 v2 offset:9248\n"
  "ds_write_addtid_b32 v3 offset:13344\n"
  "v_mfma_f32_16x16x32_bf16 v[28:31], v[76:79], v[252:255], v[92:95]\n"
  "v_exp_f32 v8, v8\n"
  "v_mfma_f32_16x16x32_bf16 v[28:31], v[72:75], v[248:251], v[28:31]\n"
  "v_exp_f32 v12, v12\n"
  "v_mfma_f32_16x16x32_bf16 v[28:31], v[68:71], v[244:247], v[28:31]\n"
  "v_exp_f32 v9, v9\n"
  "v_mfma_f32_16x16x32_bf16 v[28:31], v[64:67], v[240:243], v[28:31]\n"
  "s_sub_u32 s56, s56, 1\n"
  "v_exp_f32 v13, v13\n"
  "v_mfma_f32_16x16x32_bf16 v[28:31], v[60:63], v[236:239], v[28:31]\n"
  "s_cmp_eq_u32 s56, 0\n"
  "v_cvt_pk_bf16_f32 v8, v8, v12\n"
  "v_cvt_pk_bf16_f32 v9, v9, v13\n"
  "v_mfma_f32_16x16x32_bf16 v[28:31], v[56:59], v[232:235], v[28:31]\n"
  "s_waitcnt lgkmcnt(6)\n"
  "ds_write_addtid_b32 v8 offset:1280\n"
  "ds_write_addtid_b32 v9 offset:5376\n"
  "s_cbranch_scc1 A_X0_%=\n"
  ".p2align 3\n"
  "A_H0_%=:\n"
  "v_mfma_f32_16x16x32_bf16 v[0:3], v[32:35], a[0:3], v[92:95]\n"
  "s_add_u32 s66, s66, 6144\n"
  "v_exp_f32 v10, v10\n"
  "v_mfma_f32_16x16x32_bf16 v[0:3], v[36:39], a[4:7], v[0:3]\n"
  "s_cmp_eq_u32 s66, 102400\n"
  "v_exp_f32 v14, v14\n"
  "v_mfma_f32_16x16x32_bf16 v[0:3], v[40:43], a[8:11], v[0:3]\n"
  "s_cselect_b32 s66, 65536, s66\n"
  "v_exp_f32 v11, v11\n"
  "v_mfma_f32_16x16x32_bf16 v[0:3], v[44:47], a[12:15], v[0:3]\n"
  "v_exp_f32 v15, v15\n"
  "v_mfma_f32_16x16x32_bf16 v[0:3], v[48:51], a[16:19], v[0:3]\n"
  "v_cvt_pk_bf16_f32 v10, v10, v14\n"
  "v_cvt_pk_bf16_f32 v11, v11, v15\n"
  "v_mfma_f32_16x16x32_bf16 v[0:3], v[52:55], a[20:23], v[0:3]\n"
  "ds_write_addtid_b32 v10 offset:9504\n"
  "ds_write_addtid_b32 v11 offset:13600\n"
  "v_add_u32 v81, s66, v83\n"
  "v_add_u32 v82, s66, v84\n"
  "v_pk_mul_f32 v[16:17], v[16:17], s[60:61] op_sel_hi:[1,0]\n"
  "v_pk_mul_f32 v[18:19], v[18:19], s[60:61] op_sel_hi:[1,0]\n"
  "v_pk_mul_f32 v[20:21], v[20:21], s[60:61] op_sel_hi:[1,0]\n"
  "v_pk_mul_f32 v[22:23], v[22:23], s[60:61] op_sel_hi:[1,0]\n"
  "v_pk_mul_f32 v[24:25], v[24:25], s[60:61] op_sel_hi:[1,0]\n"
  "v_pk_mul_f32 v[26:27], v[26:27], s[60:61] op_sel_hi:[1,0]\n"
  "v_pk_mul_f32 v[28:29], v[28:29], s[60:61] op_sel_hi:[1,0]\n"
  "v_pk_mul_f32 v[30:31], v[30:31], s[60:61] op_sel_hi:[1,0]\n"
  "v_mfma_f32_16x16x32_bf16 v[4:7], v[52:55], a[44:47], v[92:95]\n"
  "v_exp_f32 v16, v16\n"
  "v_mfma_f32_16x16x32_bf16 v[4:7], v[48:51], a[40:43], v[4:7]\n"
  "v_exp_f32 v20, v20\n"
  "v_mfma_f32_16x16x32_bf16 v[4:7], v[44:47], a[36:39], v[4:7]\n"
  "v_exp_f32 v17, v17\n"
  "v_mfma_f32_16x16x32_bf16 v[4:7], v[40:43], a[32:35], v[4:7]\n"
  "v_exp_f32 v21, v21\n"
  "v_mfma_f32_16x16x32_bf16 v[4:7], v[36:39], a[28:31], v[4:7]\n"
  "v_cvt_pk_bf16_f32 v16, v16, v20\n"
  "v_cvt_pk_bf16_f32 v17, v17, v21\n"
  "v_mfma_f32_16x16x32_bf16 v[4:7], v[32:35], a[24:27], v[4:7]\n"
  "ds_write_addtid_b32 v16 offset:1536\n"
  "ds_write_addtid_b32 v17 offset:5632\n"
  "v_mfma_f32_16x16x32_bf16 v[8:11], v[32:35], a[48:51], v[92:95]\n"
  "v_exp_f32 v18, v18\n"
  "v_mfma_f32_16x16x32_bf16 v[8:11], v[36:39], a[52:55], v[8:11]\n"
  "v_exp_f32 v22, v22\n"
  "v_mfma_f32_16x16x32_bf16 v[8:11], v[40:43], a[56:59], v[8:11]\n"
  "v_exp_f32 v19, v19\n"
  "v_mfma_f32_16x16x32_bf16 v[8:11], v[44:47], a[60:63], v[8:11]\n"
  "v_exp_f32 v23, v23\n"
  "v_mfma_f32_16x16x32_bf16 v[8:11], v[48:51], a[64:67], v[8:11]\n"
  "v_cvt_pk_bf16_f32 v18, v18, v22\n"
  "v_cvt_pk_bf16_f32 v19, v19, v23\n"
  "v_mfma_f32_16x16x32_bf16 v[8:11], v[52:55], a[68:71], v[8:11]\n"
  "ds_write_addtid_b32 v18 offset:9760\n"
  "ds_write_addtid_b32 v19 offset:13856\n"
  "v_mfma_f32_16x16x32_bf16 v[12:15], v[52:55], a[92:95], v[92:95]\n"
  "v_exp_f32 v24, v24\n"
  "v_mfma_f32_16x16x32_bf16 v[12:15], v[48:51], a[88:91], v[12:15]\n"
  "v_exp_f32 v28, v28\n"
  "v_mfma_f32_16x16x32_bf16 v[12:15], v[44:47], a[84:87], v[12:15]\n"
  "v_exp_f32 v25, v25\n"
  "v_mfma_f32_16x16x32_bf16 v[12:15], v[40:43], a[80:83], v[12:15]\n"
  "v_exp_f32 v29, v29\n"
  "v_mfma_f32_16x16x32_bf16 v[12:15], v[36:39], a[76:79], v[12:15]\n"
  "v_cvt_pk_bf16_f32 v24, v24, v28\n"
  "v_cvt_pk_bf16_f32 v25, v25, v29\n"
  "v_mfma_f32_16x16x32_bf16 v[12:15], v[32:35], a[72:75], v[12:15]\n"
  "ds_write_addtid_b32 v24 offset:1792\n"
  "ds_write_addtid_b32 v25 offset:5888\n"
  "v_mfma_f32_16x16x32_bf16 v[16:19], v[32:35], a[96:99], v[92:95]\n"
  "v_exp_f32 v26, v26\n"
  "v_mfma_f32_16x16x32_bf16 v[16:19], v[36:39], a[100:103], v[16:19]\n"
  "v_exp_f32 v30, v30\n"
  "v_mfma_f32_16x16x32_bf16 v[16:19], v[40:43], a[104:107], v[16:19]\n"
  "v_exp_f32 v27, v27\n"
  "v_mfma_f32_16x16x32_bf16 v[16:19], v[44:47], a[108:111], v[16:19]\n"
  "v_exp_f32_e64 v31, v31\n"
  "v_mfma_f32_16x16x32_bf16 v[16:19], v[48:51], a[112:115], v[16:19]\n"
  "v_cvt_pk_bf16_f32 v26, v26, v30\n"
  "v_cvt_pk_bf16_f32 v27, v27, v31\n"
  "v_mfma_f32_16x16x32_bf16 v[16:19], v[52:55], a[116:119], v[16:19]\n"
  "s_barrier\n"
  "ds_write_addtid_b32 v26 offset:10016\n"
  "ds_write_addtid_b32 v27 offset:14112\n"
  "v_pk_mul_f32 v[0:1], v[0:1], s[60:61] op_sel_hi:[1,0]\n"
  "v_pk_mul_f32 v[2:3], v[2:3], s[60:61] op_sel_hi:[1,0]\n"
  "v_pk_mul_f32 v[4:5], v[4:5], s[60:61] op_sel_hi:[1,0]\n"
  "v_pk_mul_f32 v[6:7], v[6:7], s[60:61] op_sel_hi:[1,0]\n"
  "v_pk_mul_f32 v[8:9], v[8:9], s[60:61] op_sel_hi:[1,0]\n"
  "v_pk_mul_f32 v[10:11], v[10:11], s[60:61] op_sel_hi:[1,0]\n"
  "v_pk_mul_f32 v[12:13], v[12:13], s[60:61] op_sel_hi:[1,0]\n"
  "v_pk_mul_f32 v[14:15], v[14:15], s[60:61] op_sel_hi:[1,0]\n"
  "v_mfma_f32_16x16x32_bf16 v[20:23], v[52:55], a[140:143], v[92:95]\n"
  "s_add_u32 s88, s88, 16448\n"
  "v_exp_f32 v0, v0\n"
  "v_mfma_f32_16x16x32_bf16 v[20:23], v[48:51], a[136:139], v[20:23]\n"
  "s_cmp_eq_u32 s88, s68\n"
  "v_exp_f32 v4, v4\n"
  "v_mfma_f32_16x16x32_bf16 v[20:23], v[44:47], a[132:135], v[20:23]\n"
  "s_cselect_b32 s88, s89, s88\n"
  "v_exp_f32 v1, v1\n"
  "v_mfma_f32_16x16x32_bf16 v[20:23], v[40:43], a[128:131], v[20:23]\n"
  "v_exp_f32_e64 v5, v5\n"
  "v_mfma_f32_16x16x32_bf16 v[20:23], v[36:39], a[124:127], v[20:23]\n"
  "s_mov_b32 m0, s88\n"
  "v_cvt_pk_bf16_f32 v0, v0, v4\n"
  "v_cvt_pk_bf16_f32 v1, v1, v5\n"
  "v_mfma_f32_16x16x32_bf16 v[20:23], v[32:35], a[120:123], v[20:23]\n"
  "ds_write_addtid_b32 v0 offset:0\n"
  "ds_write_addtid_b32 v1 offset:4096\n"
  "v_mfma_f32_16x16x32_bf16 v[24:27], v[32:35], a[144:147], v[92:95]\n"
  "v_exp_f32 v2, v2\n"
  "v_mfma_f32_16x16x32_bf16 v[24:27], v[36:39], a[148:151], v[24:27]\n"
  "v_exp_f32 v6, v6\n"
  "v_mfma_f32_16x16x32_bf16 v[24:27], v[40:43], a[152:155], v[24:27]\n"
  "v_exp_f32 v3, v3\n"
  "v_mfma_f32_16x16x32_bf16 v[24:27], v[44:47], a[156:159], v[24:27]\n"
  "v_exp_f32 v7, v7\n"
  "v_mfma_f32_16x16x32_bf16 v[24:27], v[48:51], a[160:163], v[24:27]\n"
  "v_cvt_pk_bf16_f32 v2, v2, v6\n"
  "v_cvt_pk_bf16_f32 v3, v3, v7\n"
  "v_mfma_f32_16x16x32_bf16 v[24:27], v[52:55], a[164:167], v[24:27]\n"
  "ds_write_addtid_b32 v2 offset:8224\n"
  "ds_write_addtid_b32 v3 offset:12320\n"
  "v_mfma_f32_16x16x32_bf16 v[28:31], v[52:55], a[188:191], v[92:95]\n"
  "v_exp_f32 v8, v8\n"
  "v_mfma_f32_16x16x32_bf16 v[28:31], v[48:51], a[184:187], v[28:31]\n"
  "v_exp_f32 v12, v12\n"
  "v_mfma_f32_16x16x32_bf16 v[28:31], v[44:47], a[180:183], v[28:31]\n"
  "v_exp_f32 v9, v9\n"
  "v_mfma_f32_16x16x32_bf16 v[28:31], v[40:43], a[176:179], v[28:31]\n"
  "v_exp_f32 v13, v13\n"
  "v_mfma_f32_16x16x32_bf16 v[28:31], v[36:39], a[172:175], v[28:31]\n"
  "v_cvt_pk_bf16_f32 v8, v8, v12\n"
  "v_cvt_pk_bf16_f32 v9, v9, v13\n"
  "v_mfma_f32_16x16x32_bf16 v[28:31], v[32:35], a[168:171], v[28:31]\n"
  "ds_write_addtid_b32 v8 offset:256\n"
  "ds_write_addtid_b32 v9 offset:4352\n"
  "v_mfma_f32_16x16x32_bf16 v[0:3], v[32:35], a[192:195], v[92:95]\n"
  "v_exp_f32 v10, v10\n"
  "v_mfma_f32_16x16x32_bf16 v[0:3], v[36:39], a[196:199], v[0:3]\n"
  "v_exp_f32 v14, v14\n"
  "v_mfma_f32_16x16x32_bf16 v[0:3], v[40:43], a[200:203], v[0:3]\n"
  "v_exp_f32 v11, v11\n"
  "v_mfma_f32_16x16x32_bf16 v[0:3], v[44:47], a[204:207], v[0:3]\n"
  "v_exp_f32 v15, v15\n"
  "v_mfma_f32_16x16x32_bf16 v[0:3], v[48:51], a[208:211], v[0:3]\n"
  "v_cvt_pk_bf16_f32 v10, v10, v14\n"
  "v_cvt_pk_bf16_f32 v11, v11, v15\n"
  "v_mfma_f32_16x16x32_bf16 v[0:3], v[52:55], a[212:215], v[0:3]\n"
  "ds_read_b128 v[56:59], v81 offset:0\n"
  "ds_read_b128 v[60:63], v82 offset:0\n"
  "ds_read_b128 v[64:67], v81 offset:2048\n"
  "ds_write_addtid_b32 v10 offset:8480\n"
  "ds_write_addtid_b32 v11 offset:12576\n"
  "v_pk_mul_f32 v[16:17], v[16:17], s[60:61] op_sel_hi:[1,0]\n"
  "v_pk_mul_f32 v[18:19], v[18:19], s[60:61] op_sel_hi:[1,0]\n"
  "v_pk_mul_f32 v[20:21], v[20:21], s[60:61] op_sel_hi:[1,0]\n"
  "v_pk_mul_f32 v[22:23], v[22:23], s[60:61] op_sel_hi:[1,0]\n"
  "v_pk_mul_f32 v[24:25], v[24:25], s[60:61] op_sel_hi:[1,0]\n"
  "v_pk_mul_f32 v[26:27], v[26:27], s[60:61] op_sel_hi:[1,0]\n"
  "v_pk_mul_f32 v[28:29], v[28:29], s[60:61] op_sel_hi:[1,0]\n"
  "v_pk_mul_f32 v[30:31], v[30:31], s[60:61] op_sel_hi:[1,0]\n"
  "v_mfma_f32_16x16x32_bf16 v[4:7], v[52:55], a[236:239], v[92:95]\n"
  "v_exp_f32 v16, v16\n"
  "v_mfma_f32_16x16x32_bf16 v[4:7], v[48:51], a[232:235], v[4:7]\n"
  "v_exp_f32 v20, v20\n"
  "v_mfma_f32_16x16x32_bf16 v[4:7], v[44:47], a[228:231], v[4:7]\n"
  "v_exp_f32 v17, v17\n"
  "v_mfma_f32_16x16x32_bf16 v[4:7], v[40:43], a[224:227], v[4:7]\n"
  "v_exp_f32 v21, v21\n"
  "v_mfma_f32_16x16x32_bf16 v[4:7], v[36:39], a[220:223], v[4:7]\n"
  "v_cvt_pk_bf16_f32 v16, v16, v20\n"
  "v_cvt_pk_bf16_f32 v17, v17, v21\n"
  "v_mfma_f32_16x16x32_bf16 v[4:7], v[32:35], a[216:219], v[4:7]\n"
  "ds_write_addtid_b32 v16 offset:512\n"
  "ds_write_addtid_b32 v17 offset:4608\n"
  "v_mfma_f32_16x16x32_bf16 v[8:11], v[32:35], a[240:243], v[92:95]\n"
  "v_exp_f32 v18, v18\n"
  "v_mfma_f32_16x16x32_bf16 v[8:11], v[36:39], a[244:247], v[8:11]\n"
  "v_exp_f32 v22, v22\n"
  "v_mfma_f32_16x16x32_bf16 v[8:11], v[40:43], a[248:251], v[8:11]\n"
  "v_exp_f32 v19, v19\n"
  "v_mfma_f32_16x16x32_bf16 v[8:11], v[44:47], a[252:255], v[8:11]\n"
  "v_exp_f32 v23, v23\n"
  "v_mfma_f32_16x16x32_bf16 v[8:11], v[48:51], v[128:131], v[8:11]\n"
  "v_cvt_pk_bf16_f32 v18, v18, v22\n"
  "v_cvt_pk_bf16_f32 v19, v19, v23\n"
  "v_mfma_f32_16x16x32_bf16 v[8:11], v[52:55], v[132:135], v[8:11]\n"
  "ds_write_addtid_b32 v18 offset:8736\n"
  "ds_write_addtid_b32 v19 offset:12832\n"
  "v_mfma_f32_16x16x32_bf16 v[12:15], v[52:55], v[156:159], v[92:95]\n"
  "v_exp_f32 v24, v24\n"
  "v_mfma_f32_16x16x32_bf16 v[12:15], v[48:51], v[152:155], v[12:15]\n"
  "v_exp_f32 v28, v28\n"
  "v_mfma_f32_16x16x32_bf16 v[12:15], v[44:47], v[148:151], v[12:15]\n"
  "v_exp_f32 v25, v25\n"
  "v_mfma_f32_16x16x32_bf16 v[12:15], v[40:43], v[144:147], v[12:15]\n"
  "v_exp_f32 v29, v29\n"
  "v_mfma_f32_16x16x32_bf16 v[12:15], v[36:39], v[140:143], v[12:15]\n"
  "v_cvt_pk_bf16_f32 v24, v24, v28\n"
  "v_cvt_pk_bf16_f32 v25, v25, v29\n"
  "v_mfma_f32_16x16x32_bf16 v[12:15], v[32:35], v[136:139], v[12:15]\n"
  "ds_write_addtid_b32 v24 offset:768\n"
  "ds_write_addtid_b32 v25 offset:4864\n"
  "v_mfma_f32_16x16x32_bf16 v[16:19], v[32:35], v[160:163], v[92:95]\n"
  "v_exp_f32 v26, v26\n"
  "v_mfma_f32_16x16x32_bf16 v[16:19], v[36:39], v[164:167], v[16:19]\n"
  "v_exp_f32 v30, v30\n"
  "v_mfma_f32_16x16x32_bf16 v[16:19], v[40:43], v[168:171], v[16:19]\n"
  "v_exp_f32 v27, v27\n"
  "v_mfma_f32_16x16x32_bf16 v[16:19], v[44:47], v[172:175], v[16:19]\n"
  "v_exp_f32 v31, v31\n"
  "v_mfma_f32_16x16x32_bf16 v[16:19], v[48:51], v[176:179], v[16:19]\n"
  "v_cvt_pk_bf16_f32 v26, v26, v30\n"
  "v_cvt_pk_bf16_f32 v27, v27, v31\n"
  "v_mfma_f32_16x16x32_bf16 v[16:19], v[52:55], v[180:183], v[16:19]\n"
  "ds_read_b128 v[68:71], v82 offset:2048\n"
  "ds_read_b128 v[72:75], v81 offset:4096\n"
  "ds_read_b128 v[76:79], v82 offset:4096\n"
  "ds_write_addtid_b32 v26 offset:8992\n"
  "ds_write_addtid_b32 v27 offset:13088\n"
  "v_pk_mul_f32 v[0:1], v[0:1], s[60:61] op_sel_hi:[1,0]\n"
  "v_pk_mul_f32 v[2:3], v[2:3], s[60:61] op_sel_hi:[1,0]\n"
  "v_pk_mul_f32 v[4:5], v[4:5], s[60:61] op_sel_hi:[1,0]\n"
  "v_pk_mul_f32 v[6:7], v[6:7], s[60:61] op_sel_hi:[1,0]\n"
  "v_pk_mul_f32 v[8:9], v[8:9], s[60:61] op_sel_hi:[1,0]\n"
  "v_pk_mul_f32 v[10:11], v[10:11], s[60:61] op_sel_hi:[1,0]\n"
  "v_pk_mul_f32 v[12:13], v[12:13], s[60:61] op_sel_hi:[1,0]\n"
  "v_pk_mul_f32 v[14:15], v[14:15], s[60:61] op_sel_hi:[1,0]\n"
  "v_mfma_f32_16x16x32_bf16 v[20:23], v[52:55], v[204:207], v[92:95]\n"
  "v_exp_f32 v0, v0\n"
  "v_mfma_f32_16x16x32_bf16 v[20:23], v[48:51], v[200:203], v[20:23]\n"
  "v_exp_f32 v4, v4\n"
  "v_mfma_f32_16x16x32_bf16 v[20:23], v[44:47], v[196:199], v[20:23]\n"
  "v_exp_f32 v1, v1\n"
  "v_mfma_f32_16x16x32_bf16 v[20:23], v[40:43], v[192:195], v[20:23]\n"
  "v_exp_f32 v5, v5\n"
  "v_mfma_f32_16x16x32_bf16 v[20:23], v[36:39], v[188:191], v[20:23]\n"
  "v_cvt_pk_bf16_f32 v0, v0, v4\n"
  "v_cvt_pk_bf16_f32 v1, v1, v5\n"
  "v_mfma_f32_16x16x32_bf16 v[20:23], v[32:35], v[184:187], v[20:23]\n"
  "ds_write_addtid_b32 v0 offset:1024\n"
  "ds_write_addtid_b32 v1 offset:5120\n"
  "v_mfma_f32_16x16x32_bf16 v[24:27], v[32:35], v[208:211], v[92:95]\n"
  "v_exp_f32 v2, v2\n"
  "v_mfma_f32_16x16x32_bf16 v[24:27], v[36:39], v[212:215], v[24:27]\n"
  "v_exp_f32 v6, v6\n"
  "v_mfma_f32_16x16x32_bf16 v[24:27], v[40:43], v[216:219], v[24:27]\n"
  "v_exp_f32 v3, v3\n"
  "v_mfma_f32_16x16x32_bf16 v[24:27], v[44:47], v[220:223], v[24:27]\n"
  "v_exp_f32 v7, v7\n"
  "v_mfma_f32_16x16x32_bf16 v[24:27], v[48:51], v[224:227], v[24:27]\n"
  "v_cvt_pk_bf16_f32 v2, v2, v6\n"
  "v_cvt_pk_bf16_f32 v3, v3, v7\n"
  "v_mfma_f32_16x16x32_bf16 v[24:27], v[52:55], v[228:231], v[24:27]\n"
  "ds_write_addtid_b32 v2 offset:9248\n"
  "ds_write_addtid_b32 v3 offset:13344\n"
  "v_mfma_f32_16x16x32_bf16 v[28:31], v[52:55], v[252:255], v[92:95]\n"
  "v_exp_f32 v8, v8\n"
  "v_mfma_f32_16x16x32_bf16 v[28:31], v[48:51], v[248:251], v[28:31]\n"
  "v_exp_f32 v12, v12\n"
  "v_mfma_f32_16x16x32_bf16 v[28:31], v[44:47], v[244:247], v[28:31]\n"
  "v_exp_f32 v9, v9\n"
  "v_mfma_f32_16x16x32_bf16 v[28:31], v[40:43], v[240:243], v[28:31]\n"
  "s_sub_u32 s56, s56, 1\n"
  "v_exp_f32 v13, v13\n"
  "v_mfma_f32_16x16x32_bf16 v[28:31], v[36:39], v[236:239], v[28:31]\n"
  "s_cmp_eq_u32 s56, 0\n"
  "v_cvt_pk_bf16_f32 v8, v8, v12\n"
  "v_cvt_pk_bf16_f32 v9, v9, v13\n"
  "v_mfma_f32_16x16x32_bf16 v[28:31], v[32:35], v[232:235], v[28:31]\n"
  "s_waitcnt lgkmcnt(6)\n"
  "ds_write_addtid_b32 v8 offset:1280\n"
  "ds_write_addtid_b32 v9 offset:5376\n"
  "s_cbranch_scc0 A_H1_%=\n"
  ".p2align 3\n"
  "A_X1_%=:\n"
  "s_mov_b32 s52, s53\n"
  "s_mov_b32 s53, s54\n"
  "s_mov_b32 s54, s55\n"
  "s_cmp_eq_u32 s52, 0\n"
  "s_cbranch_scc1 A_LAST_%=\n"
  "s_lshr_b32 s62, s52, 16\n"
  "s_lshl1_add_u32 s62, s62, s65\n"
  "s_mul_i32 s57, s62, 98304\n"
  "s_and_b32 s56, s52, 0xff\n"
  "s_cmp_lg_u32 s53, 0\n"
  "s_cselect_b32 s62, 1, 0\n"
  "s_sub_u32 s56, s56, s62\n"
  "s_add_u32 s57, s57, 12672\n"
  "v_mfma_f32_16x16x32_bf16 v[0:3], v[56:59], a[0:3], v[92:95]\n"
  "s_add_u32 s66, s66, 6144\n"
  "s_cmp_eq_u32 s66, 102400\n"
  "s_cselect_b32 s66, 65536, s66\n"
  "v_exp_f32 v10, v10\n"
  "v_mfma_f32_16x16x32_bf16 v[0:3], v[60:63], a[4:7], v[0:3]\n"
  "v_exp_f32 v14, v14\n"
  "v_mfma_f32_16x16x32_bf16 v[0:3], v[64:67], a[8:11], v[0:3]\n"
  "ds_read_b128 a[0:3], v87 offset:0\n"
  "ds_read_b128 a[4:7], v88 offset:0\n"
  "v_exp_f32 v11, v11\n"
  "v_mfma_f32_16x16x32_bf16 v[0:3], v[68:71], a[12:15], v[0:3]\n"
  "v_exp_f32 v15, v15\n"
  "v_mfma_f32_16x16x32_bf16 v[0:3], v[72:75], a[16:19], v[0:3]\n"
  "ds_read_b128 a[8:11], v87 offset:2048\n"
  "ds_read_b128 a[12:15], v88 offset:2048\n"
  "v_cvt_pk_bf16_f32 v10, v10, v14\n"
  "v_cvt_pk_bf16_f32 v11, v11, v15\n"
  "v_mfma_f32_16x16x32_bf16 v[0:3], v[76:79], a[20:23], v[0:3]\n"
  "ds_write_addtid_b32 v10 offset:9504\n"
  "ds_write_addtid_b32 v11 offset:13600\n"
  "v_add_u32 v81, s66, v83\n"
  "v_add_u32 v82, s66, v84\n"
  "v_pk_mul_f32 v[16:17], v[16:17], s[60:61] op_sel_hi:[1,0]\n"
  "v_pk_mul_f32 v[18:19], v[18:19], s[60:61] op_sel_hi:[1,0]\n"
  "v_pk_mul_f32 v[20:21], v[20:21], s[60:61] op_sel_hi:[1,0]\n"
  "v_pk_mul_f32 v[22:23], v[22:23], s[60:61] op_sel_hi:[1,0]\n"
  "v_pk_mul_f32 v[24:25], v[24:25], s[60:61] op_sel_hi:[1,0]\n"
  "v_pk_mul_f32 v[26:27], v[26:27], s[60:61] op_sel_hi:[1,0]\n"
  "v_pk_mul_f32 v[28:29], v[28:29], s[60:61] op_sel_hi:[1,0]\n"
  "v_pk_mul_f32 v[30:31], v[30:31], s[60:61] op_sel_hi:[1,0]\n"
  "v_mfma_f32_16x16x32_bf16 v[4:7], v[76:79], a[44:47], v[92:95]\n"
  "s_add_u32 m0, s59, -128\n"
  "v_exp_f32 v16, v16\n"
  "v_mfma_f32_16x16x32_bf16 v[4:7], v[72:75], a[40:43], v[4:7]\n"
  "s_waitcnt lgkmcnt(4)\n"
  "buffer_load_dwordx4 v112, s[40:43], s57 offen offset:128 nt lds\n"
  "buffer_load_dwordx4 v113, s[40:43], s57 offen offset:1152 nt lds\n"
  "v_exp_f32 v20, v20\n"
  "v_mfma_f32_16x16x32_bf16 v[4:7], v[68:71], a[36:39], v[4:7]\n"
  "s_add_u32 m0, s59, 2048\n"
  "ds_read_b128 a[16:19], v87 offset:4096\n"
  "ds_read_b128 a[20:23], v88 offset:4096\n"
  "v_exp_f32 v17, v17\n"
  "v_mfma_f32_16x16x32_bf16 v[4:7], v[64:67], a[32:35], v[4:7]\n"
  "s_waitcnt lgkmcnt(4)\n"
  "buffer_load_dwordx4 v112, s[40:43], s57 offen offset:0 nt lds\n"
  "buffer_load_dwordx4 v113, s[40:43], s57 offen offset:1024 nt lds\n"
  "s_add_u32 s57, s57, 11904\n"
  "v_exp_f32_e64 v21, v21\n"
  "v_mfma_f32_16x16x32_bf16 v[4:7], v[60:63], a[28:31], v[4:7]\n"
  "s_mov_b32 m0, s88\n"
  "ds_read_b128 a[40:43], v87 offset:6144\n"
  "ds_read_b128 a[44:47], v88 offset:6144\n"
  "v_cvt_pk_bf16_f32 v16, v16, v20\n"
  "v_cvt_pk_bf16_f32 v17, v17, v21\n"
  "v_mfma_f32_16x16x32_bf16 v[4:7], v[56:59], a[24:27], v[4:7]\n"
  "ds_write_addtid_b32 v16 offset:1536\n"
  "ds_write_addtid_b32 v17 offset:5632\n"
  "v_mfma_f32_16x16x32_bf16 v[8:11], v[56:59], a[48:51], v[92:95]\n"
  "s_add_u32 m0, s59, 4096\n"
  "v_exp_f32 v18, v18\n"
  "v_mfma_f32_16x16x32_bf16 v[8:11], v[60:63], a[52:55], v[8:11]\n"
  "s_waitcnt lgkmcnt(4)\n"
  "buffer_load_dwordx4 v112, s[40:43], s57 offen offset:0 nt lds\n"
  "buffer_load_dwordx4 v113, s[40:43], s57 offen offset:1024 nt lds\n"
  "v_exp_f32 v22, v22\n"
  "v_mfma_f32_16x16x32_bf16 v[8:11], v[64:67], a[56:59], v[8:11]\n"
  "ds_read_b128 a[32:35], v87 offset:8192\n"
  "ds_read_b128 a[36:39], v88 offset:8192\n"
  "v_exp_f32 v19, v19\n"
  "v_mfma_f32_16x16x32_bf16 v[8:11], v[68:71], a[60:63], v[8:11]\n"
  "v_exp_f32 v23, v23\n"
  "v_mfma_f32_16x16x32_bf16 v[8:11], v[72:75], a[64:67], v[8:11]\n"
  "s_mov_b32 m0, s88\n"
  "v_cvt_pk_bf16_f32 v18, v18, v22\n"
  "v_cvt_pk_bf16_f32 v19, v19, v23\n"
  "v_mfma_f32_16x16x32_bf16 v[8:11], v[76:79], a[68:71], v[8:11]\n"
  "s_barrier\n"
  "ds_write_addtid_b32 v18 offset:9760\n"
  "ds_write_addtid_b32 v19 offset:13856\n"
  "v_mfma_f32_16x16x32_bf16 v[12:15], v[76:79], a[92:95], v[92:95]\n"
  "s_add_u32 m0, s59, 6016\n"
  "v_exp_f32 v24, v24\n"
  "v_mfma_f32_16x16x32_bf16 v[12:15], v[72:75], a[88:91], v[12:15]\n"
  "s_waitcnt lgkmcnt(6)\n"
  "buffer_load_dwordx4 v112, s[40:43], s57 offen offset:128 nt lds\n"
  "buffer_load_dwordx4 v113, s[40:43], s57 offen offset:1152 nt lds\n"
  "v_exp_f32 v28, v28\n"
  "v_mfma_f32_16x16x32_bf16 v[12:15], v[68:71], a[84:87], v[12:15]\n"
  "s_add_u32 m0, s59, 7936\n"
  "ds_read_b128 a[24:27], v87 offset:10240\n"
  "ds_read_b128 a[28:31], v88 offset:10240\n"
  "v_exp_f32 v25, v25\n"
  "v_mfma_f32_16x16x32_bf16 v[12:15], v[64:67], a[80:83], v[12:15]\n"
  "s_waitcnt lgkmcnt(4)\n"
  "buffer_load_dwordx4 v112, s[40:43], s57 offen offset:256 nt lds\n"
  "buffer_load_dwordx4 v113, s[40:43], s57 offen offset:1280 nt lds\n"
  "s_add_u32 s57, s57, 384\n"
  "v_exp_f32 v29, v29\n"
  "v_mfma_f32_16x16x32_bf16 v[12:15], v[60:63], a[76:79], v[12:15]\n"
  "s_mov_b32 m0, s88\n"
  "ds_read_b128 a[48:51], v87 offset:12288\n"
  "ds_read_b128 a[52:55], v88 offset:12288\n"
  "v_cvt_pk_bf16_f32 v24, v24, v28\n"
  "v_cvt_pk_bf16_f32 v25, v25, v29\n"
  "v_mfma_f32_16x16x32_bf16 v[12:15], v[56:59], a[72:75], v[12:15]\n"
  "ds_write_addtid_b32 v24 offset:1792\n"
  "ds_write_addtid_b32 v25 offset:5888\n"
  "v_mfma_f32_16x16x32_bf16 v[16:19], v[56:59], a[96:99], v[92:95]\n"
  "s_add_u32 m0, s59, 9984\n"
  "v_exp_f32 v26, v26\n"
  "v_mfma_f32_16x16x32_bf16 v[16:19], v[60:63], a[100:103], v[16:19]\n"
  "s_waitcnt lgkmcnt(4)\n"
  "buffer_load_dwordx4 v112, s[40:43], s57 offen offset:256 nt lds\n"
  "buffer_load_dwordx4 v113, s[40:43], s57 offen offset:1280 nt lds\n"
  "v_exp_f32 v30, v30\n"
  "v_mfma_f32_16x16x32_bf16 v[16:19], v[64:67], a[104:107], v[16:19]\n"
  "s_add_u32 m0, s59, 12160\n"
  "ds_read_b128 a[56:59], v87 offset:14336\n"
  "ds_read_b128 a[60:63], v88 offset:14336\n"
  "s_waitcnt vmcnt(10)\n"
  "ds_read_b128 a[80:83], v87 offset:0\n"
  "ds_read_b128 a[84:87], v88 offset:0\n"
  "v_exp_f32 v27, v27\n"
  "v_mfma_f32_16x16x32_bf16 v[16:19], v[68:71], a[108:111], v[16:19]\n"
  "s_waitcnt lgkmcnt(6)\n"
  "buffer_load_dwordx4 v112, s[40:43], s57 offen offset:128 nt lds\n"
  "buffer_load_dwordx4 v113, s[40:43], s57 offen offset:1152 nt lds\n"
  "v_exp_f32_e64 v31, v31\n"
  "v_mfma_f32_16x16x32_bf16 v[16:19], v[72:75], a[112:115], v[16:19]\n"
  "s_mov_b32 m0, s88\n"
  "ds_read_b128 a[64:67], v87 offset:16384\n"
  "ds_read_b128 a[68:71], v88 offset:16384\n"
  "s_waitcnt vmcnt(10)\n"
  "ds_read_b128 a[72:75], v87 offset:2048\n"
  "ds_read_b128 a[76:79], v88 offset:2048\n"
  "v_cvt_pk_bf16_f32 v26, v26, v30\n"
  "v_cvt_pk_bf16_f32 v27, v27, v31\n"
  "v_mfma_f32_16x16x32_bf16 v[16:19], v[76:79], a[116:119], v[16:19]\n"
  "ds_write_addtid_b32 v26 offset:10016\n"
  "ds_write_addtid_b32 v27 offset:14112\n"
  "v_pk_mul_f32 v[0:1], v[0:1], s[60:61] op_sel_hi:[1,0]\n"
  "v_pk_mul_f32 v[2:3], v[2:3], s[60:61] op_sel_hi:[1,0]\n"
  "v_pk_mul_f32 v[4:5], v[4:5], s[60:61] op_sel_hi:[1,0]\n"
  "v_pk_mul_f32 v[6:7], v[6:7], s[60:61] op_sel_hi:[1,0]\n"
  "v_pk_mul_f32 v[8:9], v[8:9], s[60:61] op_sel_hi:[1,0]\n"
  "v_pk_mul_f32 v[10:11], v[10:11], s[60:61] op_sel_hi:[1,0]\n"
  "v_pk_mul_f32 v[12:13], v[12:13], s[60:61] op_sel_hi:[1,0]\n"
  "v_pk_mul_f32 v[14:15], v[14:15], s[60:61] op_sel_hi:[1,0]\n"
  "v_mfma_f32_16x16x32_bf16 v[20:23], v[76:79], a[140:143], v[92:95]\n"
  "s_add_u32 m0, s59, 14336\n"
  "v_exp_f32 v0, v0\n"
  "v_mfma_f32_16x16x32_bf16 v[20:23], v[72:75], a[136:139], v[20:23]\n"
  "s_waitcnt lgkmcnt(8)\n"
  "buffer_load_dwordx4 v112, s[40:43], s57 offen offset:0 nt lds\n"
  "buffer_load_dwordx4 v113, s[40:43], s57 offen offset:1024 nt lds\n"
  "s_add_u32 s57, s57, 11904\n"
  "v_exp_f32 v4, v4\n"
  "v_mfma_f32_16x16x32_bf16 v[20:23], v[68:71], a[132:135], v[20:23]\n"
  "s_add_u32 m0, s59, 16384\n"
  "ds_read_b128 a[88:91], v87 offset:18432\n"
  "ds_read_b128 a[92:95], v88 offset:18432\n"
  "s_waitcnt vmcnt(10)\n"
  "ds_read_b128 a[96:99], v87 offset:4096\n"
  "ds_read_b128 a[100:103], v88 offset:4096\n"
  "v_exp_f32 v1, v1\n"
  "v_mfma_f32_16x16x32_bf16 v[20:23], v[64:67], a[128:131], v[20:23]\n"
  "s_waitcnt lgkmcnt(8)\n"
  "buffer_load_dwordx4 v112, s[40:43], s57 offen offset:0 nt lds\n"
  "buffer_load_dwordx4 v113, s[40:43], s57 offen offset:1024 nt lds\n"
  "v_exp_f32 v5, v5\n"
  "v_mfma_f32_16x16x32_bf16 v[20:23], v[60:63], a[124:127], v[20:23]\n"
  "s_add_u32 s88, s88, 16448\n"
  "s_cmp_eq_u32 s88, s68\n"
  "s_cselect_b32 s88, s89, s88\n"
  "s_mov_b32 m0, s88\n"
  "s_mov_b32 m0, s88\n"
  "s_waitcnt vmcnt(10)\n"
  "ds_read_b128 a[104:107], v87 offset:6144\n"
  "ds_read_b128 a[108:111], v88 offset:6144\n"
  "v_cvt_pk_bf16_f32 v0, v0, v4\n"
  "v_cvt_pk_bf16_f32 v1, v1, v5\n"
  "v_mfma_f32_16x16x32_bf16 v[20:23], v[56:59], a[120:123], v[20:23]\n"
  "ds_write_addtid_b32 v0 offset:0\n"
  "ds_write_addtid_b32 v1 offset:4096\n"
  "v_mfma_f32_16x16x32_bf16 v[24:27], v[56:59], a[144:147], v[92:95]\n"
  "s_add_u32 m0, s59, 18304\n"
  "v_exp_f32 v2, v2\n"
  "v_mfma_f32_16x16x32_bf16 v[24:27], v[60:63], a[148:151], v[24:27]\n"
  "s_waitcnt lgkmcnt(6)\n"
  "buffer_load_dwordx4 v112, s[40:43], s57 offen offset:128 nt lds\n"
  "buffer_load_dwordx4 v113, s[40:43], s57 offen offset:1152 nt lds\n"
  "v_exp_f32 v6, v6\n"
  "v_mfma_f32_16x16x32_bf16 v[24:27], v[64:67], a[152:155], v[24:27]\n"
  "s_waitcnt vmcnt(10)\n"
  "ds_read_b128 a[112:115], v87 offset:8192\n"
  "ds_read_b128 a[116:119], v88 offset:8192\n"
  "v_exp_f32 v3, v3\n"
  "v_mfma_f32_16x16x32_bf16 v[24:27], v[68:71], a[156:159], v[24:27]\n"
  "v_exp_f32_e64 v7, v7\n"
  "v_mfma_f32_16x16x32_bf16 v[24:27], v[72:75], a[160:163], v[24:27]\n"
  "s_mov_b32 m0, s88\n"
  "v_cvt_pk_bf16_f32 v2, v2, v6\n"
  "v_cvt_pk_bf16_f32 v3, v3, v7\n"
  "v_mfma_f32_16x16x32_bf16 v[24:27], v[76:79], a[164:167], v[24:27]\n"
  "ds_write_addtid_b32 v2 offset:8224\n"
  "ds_write_addtid_b32 v3 offset:12320\n"
  "v_mfma_f32_16x16x32_bf16 v[28:31], v[76:79], a[188:191], v[92:95]\n"
  "s_add_u32 m0, s59, -256\n"
  "v_exp_f32 v8, v8\n"
  "v_mfma_f32_16x16x32_bf16 v[28:31], v[72:75], a[184:187], v[28:31]\n"
  "buffer_load_dwordx4 v112, s[40:43], s57 offen offset:256 nt lds\n"
  "buffer_load_dwordx4 v113, s[40:43], s57 offen offset:1280 nt lds\n"
  "s_add_u32 s57, s57, 384\n"
  "v_exp_f32 v12, v12\n"
  "v_mfma_f32_16x16x32_bf16 v[28:31], v[68:71], a[180:183], v[28:31]\n"
  "s_add_u32 m0, s59, 1792\n"
  "s_waitcnt vmcnt(10)\n"
  "ds_read_b128 a[136:139], v87 offset:10240\n"
  "ds_read_b128 a[140:143], v88 offset:10240\n"
  "v_exp_f32 v9, v9\n"
  "v_mfma_f32_16x16x32_bf16 v[28:31], v[64:67], a[176:179], v[28:31]\n"
  "buffer_load_dwordx4 v112, s[40:43], s57 offen offset:256 nt lds\n"
  "buffer_load_dwordx4 v113, s[40:43], s57 offen offset:1280 nt lds\n"
  "v_exp_f32_e64 v13, v13\n"
  "v_mfma_f32_16x16x32_bf16 v[28:31], v[60:63], a[172:175], v[28:31]\n"
  "s_mov_b32 m0, s88\n"
  "s_waitcnt vmcnt(10)\n"
  "ds_read_b128 a[128:131], v87 offset:12288\n"
  "ds_read_b128 a[132:135], v88 offset:12288\n"
  "v_cvt_pk_bf16_f32 v8, v8, v12\n"
  "v_cvt_pk_bf16_f32 v9, v9, v13\n"
  "v_mfma_f32_16x16x32_bf16 v[28:31], v[56:59], a[168:171], v[28:31]\n"
  "ds_write_addtid_b32 v8 offset:256\n"
  "ds_write_addtid_b32 v9 offset:4352\n"
  "v_mfma_f32_16x16x32_bf16 v[0:3], v[56:59], a[192:195], v[92:95]\n"
  "s_add_u32 m0, s59, 3968\n"
  "v_exp_f32 v10, v10\n"
  "v_mfma_f32_16x16x32_bf16 v[0:3], v[60:63], a[196:199], v[0:3]\n"
  "s_waitcnt lgkmcnt(14)\n"
  "buffer_load_dwordx4 v112, s[40:43], s57 offen offset:128 nt lds\n"
  "buffer_load_dwordx4 v113, s[40:43], s57 offen offset:1152 nt lds\n"
  "v_exp_f32 v14, v14\n"
  "v_mfma_f32_16x16x32_bf16 v[0:3], v[64:67], a[200:203], v[0:3]\n"
  "s_add_u32 m0, s59, 6144\n"
  "s_waitcnt vmcnt(10)\n"
  "ds_read_b128 a[120:123], v87 offset:14336\n"
  "ds_read_b128 a[124:127], v88 offset:14336\n"
  "v_exp_f32 v11, v11\n"
  "v_mfma_f32_16x16x32_bf16 v[0:3], v[68:71], a[204:207], v[0:3]\n"
  "s_waitcnt lgkmcnt(14)\n"
  "buffer_load_dwordx4 v112, s[40:43], s57 offen offset:0 nt lds\n"
  "buffer_load_dwordx4 v113, s[40:43], s57 offen offset:1024 nt lds\n"
  "s_add_u32 s57, s57, 11904\n"
  "v_exp_f32_e64 v15, v15\n"
  "v_mfma_f32_16x16x32_bf16 v[0:3], v[72:75], a[208:211], v[0:3]\n"
  "s_mov_b32 m0, s88\n"
  "s_waitcnt vmcnt(10)\n"
  "ds_read_b128 a[144:147], v87 offset:16384\n"
  "ds_read_b128 a[148:151], v88 offset:16384\n"
  "v_cvt_pk_bf16_f32 v10, v10, v14\n"
  "v_cvt_pk_bf16_f32 v11, v11, v15\n"
  "v_mfma_f32_16x16x32_bf16 v[0:3], v[76:79], a[212:215], v[0:3]\n"
  "ds_read_b128 v[32:35], v81 offset:0\n"
  "ds_read_b128 v[36:39], v82 offset:0\n"
  "ds_read_b128 v[40:43], v81 offset:2048\n"
  "ds_write_addtid_b32 v10 offset:8480\n"
  "ds_write_addtid_b32 v11 offset:12576\n"
  "v_pk_mul_f32 v[16:17], v[16:17], s[60:61] op_sel_hi:[1,0]\n"
  "v_pk_mul_f32 v[18:19], v[18:19], s[60:61] op_sel_hi:[1,0]\n"
  "v_pk_mul_f32 v[20:21], v[20:21], s[60:61] op_sel_hi:[1,0]\n"
  "v_pk_mul_f32 v[22:23], v[22:23], s[60:61] op_sel_hi:[1,0]\n"
  "v_pk_mul_f32 v[24:25], v[24:25], s[60:61] op_sel_hi:[1,0]\n"
  "v_pk_mul_f32 v[26:27], v[26:27], s[60:61] op_sel_hi:[1,0]\n"
  "v_pk_mul_f32 v[28:29], v[28:29], s[60:61] op_sel_hi:[1,0]\n"
  "v_pk_mul_f32 v[30:31], v[30:31], s[60:61] op_sel_hi:[1,0]\n"
  "v_mfma_f32_16x16x32_bf16 v[4:7], v[76:79], a[236:239], v[92:95]\n"
  "s_add_u32 m0, s59, 8192\n"
  "v_exp_f32 v16, v16\n"
  "v_mfma_f32_16x16x32_bf16 v[4:7], v[72:75], a[232:235], v[4:7]\n"
  "s_waitcnt lgkmcnt(15)\n"
  "buffer_load_dwordx4 v112, s[40:43], s57 offen offset:0 nt lds\n"
  "buffer_load_dwordx4 v113, s[40:43], s57 offen offset:1024 nt lds\n"
  "v_exp_f32 v20, v20\n"
  "v_mfma_f32_16x16x32_bf16 v[4:7], v[68:71], a[228:231], v[4:7]\n"
  "s_add_u32 m0, s59, 10112\n"
  "s_waitcnt vmcnt(10)\n"
  "ds_read_b128 a[152:155], v87 offset:18432\n"
  "ds_read_b128 a[156:159], v88 offset:18432\n"
  "v_exp_f32 v17, v17\n"
  "v_mfma_f32_16x16x32_bf16 v[4:7], v[64:67], a[224:227], v[4:7]\n"
  "s_waitcnt lgkmcnt(15)\n"
  "buffer_load_dwordx4 v112, s[40:43], s57 offen offset:128 nt lds\n"
  "buffer_load_dwordx4 v113, s[40:43], s57 offen offset:1152 nt lds\n"
  "v_exp_f32_e64 v21, v21\n"
  "v_mfma_f32_16x16x32_bf16 v[4:7], v[60:63], a[220:223], v[4:7]\n"
  "s_mov_b32 m0, s88\n"
  "s_waitcnt vmcnt(10)\n"
  "ds_read_b128 a[160:163], v87 offset:0\n"
  "ds_read_b128 a[164:167], v88 offset:0\n"
  "v_cvt_pk_bf16_f32 v16, v16, v20\n"
  "v_cvt_pk_bf16_f32 v17, v17, v21\n"
  "v_mfma_f32_16x16x32_bf16 v[4:7], v[56:59], a[216:219], v[4:7]\n"
  "ds_write_addtid_b32 v16 offset:512\n"
  "ds_write_addtid_b32 v17 offset:4608\n"
  "v_mfma_f32_16x16x32_bf16 v[8:11], v[56:59], a[240:243], v[92:95]\n"
  "s_add_u32 m0, s59, 12032\n"
  "v_exp_f32 v18, v18\n"
  "v_mfma_f32_16x16x32_bf16 v[8:11], v[60:63], a[244:247], v[8:11]\n"
  "s_waitcnt lgkmcnt(15)\n"
  "buffer_load_dwordx4 v112, s[40:43], s57 offen offset:256 nt lds\n"
  "buffer_load_dwordx4 v113, s[40:43], s57 offen offset:1280 nt lds\n"
  "s_add_u32 s57, s57, 384\n"
  "v_exp_f32 v22, v22\n"
  "v_mfma_f32_16x16x32_bf16 v[8:11], v[64:67], a[248:251], v[8:11]\n"
  "s_add_u32 m0, s59, 14080\n"
  "s_waitcnt vmcnt(10)\n"
  "ds_read_b128 a[184:187], v87 offset:2048\n"
  "ds_read_b128 a[188:191], v88 offset:2048\n"
  "v_exp_f32 v19, v19\n"
  "v_mfma_f32_16x16x32_bf16 v[8:11], v[68:71], a[252:255], v[8:11]\n"
  "s_waitcnt lgkmcnt(15)\n"
  "buffer_load_dwordx4 v112, s[40:43], s57 offen offset:256 nt lds\n"
  "buffer_load_dwordx4 v113, s[40:43], s57 offen offset:1280 nt lds\n"
  "v_exp_f32_e64 v23, v23\n"
  "v_mfma_f32_16x16x32_bf16 v[8:11], v[72:75], v[128:131], v[8:11]\n"
  "s_mov_b32 m0, s88\n"
  "s_waitcnt vmcnt(10)\n"
  "ds_read_b128 a[176:179], v87 offset:4096\n"
  "ds_read_b128 a[180:183], v88 offset:4096\n"
  "v_cvt_pk_bf16_f32 v18, v18, v22\n"
  "v_cvt_pk_bf16_f32 v19, v19, v23\n"
  "v_mfma_f32_16x16x32_bf16 v[8:11], v[76:79], v[132:135], v[8:11]\n"
  "ds_write_addtid_b32 v18 offset:8736\n"
  "ds_write_addtid_b32 v19 offset:12832\n"
  "v_mfma_f32_16x16x32_bf16 v[12:15], v[76:79], v[156:159], v[92:95]\n"
  "v_exp_f32 v24, v24\n"
  "v_mfma_f32_16x16x32_bf16 v[12:15], v[72:75], v[152:155], v[12:15]\n"
  "v_exp_f32 v28, v28\n"
  "v_mfma_f32_16x16x32_bf16 v[12:15], v[68:71], v[148:151], v[12:15]\n"
  "s_add_u32 m0, s59, 16256\n"
  "v_exp_f32 v25, v25\n"
  "v_mfma_f32_16x16x32_bf16 v[12:15], v[64:67], v[144:147], v[12:15]\n"
  "s_waitcnt lgkmcnt(15)\n"
  "buffer_load_dwordx4 v112, s[40:43], s57 offen offset:128 nt lds\n"
  "buffer_load_dwordx4 v113, s[40:43], s57 offen offset:1152 nt lds\n"
  "v_exp_f32_e64 v29, v29\n"
  "v_mfma_f32_16x16x32_bf16 v[12:15], v[60:63], v[140:143], v[12:15]\n"
  "s_mov_b32 m0, s88\n"
  "s_waitcnt vmcnt(10)\n"
  "ds_read_b128 a[168:171], v87 offset:6144\n"
  "ds_read_b128 a[172:175], v88 offset:6144\n"
  "v_cvt_pk_bf16_f32 v24, v24, v28\n"
  "v_cvt_pk_bf16_f32 v25, v25, v29\n"
  "v_mfma_f32_16x16x32_bf16 v[12:15], v[56:59], v[136:139], v[12:15]\n"
  "ds_write_addtid_b32 v24 offset:768\n"
  "ds_write_addtid_b32 v25 offset:4864\n"
  "v_mfma_f32_16x16x32_bf16 v[16:19], v[56:59], v[160:163], v[92:95]\n"
  "s_add_u32 m0, s59, 18432\n"
  "v_exp_f32 v26, v26\n"
  "v_mfma_f32_16x16x32_bf16 v[16:19], v[60:63], v[164:167], v[16:19]\n"
  "s_waitcnt lgkmcnt(14)\n"
  "buffer_load_dwordx4 v112, s[40:43], s57 offen offset:0 nt lds\n"
  "buffer_load_dwordx4 v113, s[40:43], s57 offen offset:1024 nt lds\n"
  "s_add_u32 s57, s57, 11904\n"
  "v_exp_f32 v30, v30\n"
  "v_mfma_f32_16x16x32_bf16 v[16:19], v[64:67], v[168:171], v[16:19]\n"
  "s_add_u32 m0, s59, 0\n"
  "s_waitcnt vmcnt(10)\n"
  "ds_read_b128 a[192:195], v87 offset:8192\n"
  "ds_read_b128 a[196:199], v88 offset:8192\n"
  "v_exp_f32 v27, v27\n"
  "v_mfma_f32_16x16x32_bf16 v[16:19], v[68:71], v[172:175], v[16:19]\n"
  "s_waitcnt lgkmcnt(14)\n"
  "buffer_load_dwordx4 v112, s[40:43], s57 offen offset:0 nt lds\n"
  "buffer_load_dwordx4 v113, s[40:43], s57 offen offset:1024 nt lds\n"
  "v_exp_f32 v31, v31\n"
  "v_mfma_f32_16x16x32_bf16 v[16:19], v[72:75], v[176:179], v[16:19]\n"
  "s_mov_b32 m0, s88\n"
  "s_waitcnt vmcnt(10)\n"
  "ds_read_b128 a[200:203], v87 offset:10240\n"
  "ds_read_b128 a[204:207], v88 offset:10240\n"
  "v_cvt_pk_bf16_f32 v26, v26, v30\n"
  "v_cvt_pk_bf16_f32 v27, v27, v31\n"
  "v_mfma_f32_16x16x32_bf16 v[16:19], v[76:79], v[180:183], v[16:19]\n"
  "ds_read_b128 v[44:47], v82 offset:2048\n"
  "ds_read_b128 v[48:51], v81 offset:4096\n"
  "ds_read_b128 v[52:55], v82 offset:4096\n"
  "ds_write_addtid_b32 v26 offset:8992\n"
  "ds_write_addtid_b32 v27 offset:13088\n"
  "v_pk_mul_f32 v[0:1], v[0:1], s[60:61] op_sel_hi:[1,0]\n"
  "v_pk_mul_f32 v[2:3], v[2:3], s[60:61] op_sel_hi:[1,0]\n"
  "v_pk_mul_f32 v[4:5], v[4:5], s[60:61] op_sel_hi:[1,0]\n"
  "v_pk_mul_f32 v[6:7], v[6:7], s[60:61] op_sel_hi:[1,0]\n"
  "v_pk_mul_f32 v[8:9], v[8:9], s[60:61] op_sel_hi:[1,0]\n"
  "v_pk_mul_f32 v[10:11], v[10:11], s[60:61] op_sel_hi:[1,0]\n"
  "v_pk_mul_f32 v[12:13], v[12:13], s[60:61] op_sel_hi:[1,0]\n"
  "v_pk_mul_f32 v[14:15], v[14:15], s[60:61] op_sel_hi:[1,0]\n"
  "v_mfma_f32_16x16x32_bf16 v[20:23], v[76:79], v[204:207], v[92:95]\n"
  "s_add_u32 m0, s59, 1920\n"
  "v_exp_f32 v0, v0\n"
  "v_mfma_f32_16x16x32_bf16 v[20:23], v[72:75], v[200:203], v[20:23]\n"
  "s_waitcnt lgkmcnt(15)\n"
  "buffer_load_dwordx4 v112, s[40:43], s57 offen offset:128 nt lds\n"
  "buffer_load_dwordx4 v113, s[40:43], s57 offen offset:1152 nt lds\n"
  "v_exp_f32 v4, v4\n"
  "v_mfma_f32_16x16x32_bf16 v[20:23], v[68:71], v[196:199], v[20:23]\n"
  "s_add_u32 m0, s59, 3840\n"
  "s_waitcnt vmcnt(10)\n"
  "ds_read_b128 a[208:211], v87 offset:12288\n"
  "ds_read_b128 a[212:215], v88 offset:12288\n"
  "v_exp_f32 v1, v1\n"
  "v_mfma_f32_16x16x32_bf16 v[20:23], v[64:67], v[192:195], v[20:23]\n"
  "buffer_load_dwordx4 v112, s[40:43], s57 offen offset:256 nt lds\n"
  "buffer_load_dwordx4 v113, s[40:43], s57 offen offset:1280 nt lds\n"
  "s_add_u32 s57, s57, 384\n"
  "v_exp_f32 v5, v5\n"
  "v_mfma_f32_16x16x32_bf16 v[20:23], v[60:63], v[188:191], v[20:23]\n"
  "s_mov_b32 m0, s88\n"
  "s_waitcnt vmcnt(10)\n"
  "ds_read_b128 a[232:235], v87 offset:14336\n"
  "ds_read_b128 a[236:239], v88 offset:14336\n"
  "v_cvt_pk_bf16_f32 v0, v0, v4\n"
  "v_cvt_pk_bf16_f32 v1, v1, v5\n"
  "v_mfma_f32_16x16x32_bf16 v[20:23], v[56:59], v[184:187], v[20:23]\n"
  "ds_write_addtid_b32 v0 offset:1024\n"
  "ds_write_addtid_b32 v1 offset:5120\n"
  "v_mfma_f32_16x16x32_bf16 v[24:27], v[56:59], v[208:211], v[92:95]\n"
  "s_add_u32 m0, s59, 5888\n"
  "v_exp_f32 v2, v2\n"
  "v_mfma_f32_16x16x32_bf16 v[24:27], v[60:63], v[212:215], v[24:27]\n"
  "s_waitcnt lgkmcnt(15)\n"
  "buffer_load_dwordx4 v112, s[40:43], s57 offen offset:256 nt lds\n"
  "buffer_load_dwordx4 v113, s[40:43], s57 offen offset:1280 nt lds\n"
  "v_exp_f32 v6, v6\n"
  "v_mfma_f32_16x16x32_bf16 v[24:27], v[64:67], v[216:219], v[24:27]\n"
  "s_add_u32 m0, s59, 8064\n"
  "s_waitcnt vmcnt(10)\n"
  "ds_read_b128 a[224:227], v87 offset:16384\n"
  "ds_read_b128 a[228:231], v88 offset:16384\n"
  "v_exp_f32 v3, v3\n"
  "v_mfma_f32_16x16x32_bf16 v[24:27], v[68:71], v[220:223], v[24:27]\n"
  "s_waitcnt lgkmcnt(15)\n"
  "buffer_load_dwordx4 v112, s[40:43], s57 offen offset:128 nt lds\n"
  "buffer_load_dwordx4 v113, s[40:43], s57 offen offset:1152 nt lds\n"
  "v_exp_f32_e64 v7, v7\n"
  "v_mfma_f32_16x16x32_bf16 v[24:27], v[72:75], v[224:227], v[24:27]\n"
  "s_mov_b32 m0, s88\n"
  "s_waitcnt vmcnt(10)\n"
  "ds_read_b128 a[216:219], v87 offset:18432\n"
  "ds_read_b128 a[220:223], v88 offset:18432\n"
  "v_cvt_pk_bf16_f32 v2, v2, v6\n"
  "v_cvt_pk_bf16_f32 v3, v3, v7\n"
  "v_mfma_f32_16x16x32_bf16 v[24:27], v[76:79], v[228:231], v[24:27]\n"
  "s_barrier\n"
  "ds_write_addtid_b32 v2 offset:9248\n"
  "ds_write_addtid_b32 v3 offset:13344\n"
  "v_mfma_f32_16x16x32_bf16 v[28:31], v[76:79], v[252:255], v[92:95]\n"
  "v_exp_f32 v8, v8\n"
  "v_mfma_f32_16x16x32_bf16 v[28:31], v[72:75], v[248:251], v[28:31]\n"
  "v_exp_f32 v12, v12\n"
  "v_mfma_f32_16x16x32_bf16 v[28:31], v[68:71], v[244:247], v[28:31]\n"
  "s_add_u32 m0, s59, 10240\n"
  "v_exp_f32 v9, v9\n"
  "v_mfma_f32_16x16x32_bf16 v[28:31], v[64:67], v[240:243], v[28:31]\n"
  "s_waitcnt lgkmcnt(15)\n"
  "buffer_load_dwordx4 v112, s[40:43], s57 offen offset:0 nt lds\n"
  "buffer_load_dwordx4 v113, s[40:43], s57 offen offset:1024 nt lds\n"
  "s_add_u32 s57, s57, 11904\n"
  "v_exp_f32_e64 v13, v13\n"
  "v_mfma_f32_16x16x32_bf16 v[28:31], v[60:63], v[236:239], v[28:31]\n"
  "s_mov_b32 m0, s88\n"
  "s_waitcnt vmcnt(10)\n"
  "ds_read_b128 a[240:243], v87 offset:0\n"
  "ds_read_b128 a[244:247], v88 offset:0\n"
  "v_cvt_pk_bf16_f32 v8, v8, v12\n"
  "v_cvt_pk_bf16_f32 v9, v9, v13\n"
  "v_mfma_f32_16x16x32_bf16 v[28:31], v[56:59], v[232:235], v[28:31]\n"
  "s_waitcnt lgkmcnt(15)\n"
  "ds_write_addtid_b32 v8 offset:1280\n"
  "ds_write_addtid_b32 v9 offset:5376\n"
  ".p2align 3\n"
  "v_mfma_f32_16x16x32_bf16 v[0:3], v[32:35], a[0:3], v[92:95]\n"
  "s_add_u32 s66, s66, 6144\n"
  "s_cmp_eq_u32 s66, 102400\n"
  "s_cselect_b32 s66, 65536, s66\n"
  "s_add_u32 m0, s59, 12288\n"
  "v_exp_f32 v10, v10\n"
  "v_mfma_f32_16x16x32_bf16 v[0:3], v[36:39], a[4:7], v[0:3]\n"
  "s_waitcnt lgkmcnt(14)\n"
  "buffer_load_dwordx4 v112, s[40:43], s57 offen offset:0 nt lds\n"
  "buffer_load_dwordx4 v113, s[40:43], s57 offen offset:1024 nt lds\n"
  "v_exp_f32 v14, v14\n"
  "v_mfma_f32_16x16x32_bf16 v[0:3], v[40:43], a[8:11], v[0:3]\n"
  "s_add_u32 m0, s59, 14208\n"
  "s_waitcnt vmcnt(10)\n"
  "ds_read_b128 a[248:251], v87 offset:2048\n"
  "ds_read_b128 a[252:255], v88 offset:2048\n"
  "v_exp_f32 v11, v11\n"
  "v_mfma_f32_16x16x32_bf16 v[0:3], v[44:47], a[12:15], v[0:3]\n"
  "s_waitcnt lgkmcnt(14)\n"
  "buffer_load_dwordx4 v112, s[40:43], s57 offen offset:128 nt lds\n"
  "buffer_load_dwordx4 v113, s[40:43], s57 offen offset:1152 nt lds\n"
  "v_exp_f32_e64 v15, v15\n"
  "v_mfma_f32_16x16x32_bf16 v[0:3], v[48:51], a[16:19], v[0:3]\n"
  "s_mov_b32 m0, s88\n"
  "s_waitcnt vmcnt(10)\n"
  "ds_read_b128 v[128:131], v87 offset:4096\n"
  "ds_read_b128 v[132:135], v88 offset:4096\n"
  "v_cvt_pk_bf16_f32 v10, v10, v14\n"
  "v_cvt_pk_bf16_f32 v11, v11, v15\n"
  "v_mfma_f32_16x16x32_bf16 v[0:3], v[52:55], a[20:23], v[0:3]\n"
  "ds_write_addtid_b32 v10 offset:9504\n"
  "ds_write_addtid_b32 v11 offset:13600\n"
  "v_add_u32 v81, s66, v83\n"
  "v_add_u32 v82, s66, v84\n"
  "v_pk_mul_f32 v[16:17], v[16:17], s[60:61] op_sel_hi:[1,0]\n"
  "v_pk_mul_f32 v[18:19], v[18:19], s[60:61] op_sel_hi:[1,0]\n"
  "v_pk_mul_f32 v[20:21], v[20:21], s[60:61] op_sel_hi:[1,0]\n"
  "v_pk_mul_f32 v[22:23], v[22:23], s[60:61] op_sel_hi:[1,0]\n"
  "v_pk_mul_f32 v[24:25], v[24:25], s[60:61] op_sel_hi:[1,0]\n"
  "v_pk_mul_f32 v[26:27], v[26:27], s[60:61] op_sel_hi:[1,0]\n"
  "v_pk_mul_f32 v[28:29], v[28:29], s[60:61] op_sel_hi:[1,0]\n"
  "v_pk_mul_f32 v[30:31], v[30:31], s[60:61] op_sel_hi:[1,0]\n"
  "v_mfma_f32_16x16x32_bf16 v[4:7], v[52:55], a[44:47], v[92:95]\n"
  "s_add_u32 m0, s59, 16128\n"
  "v_exp_f32 v16, v16\n"
  "v_mfma_f32_16x16x32_bf16 v[4:7], v[48:51], a[40:43], v[4:7]\n"
  "s_waitcnt lgkmcnt(14)\n"
  "buffer_load_dwordx4 v112, s[40:43], s57 offen offset:256 nt lds\n"
  "buffer_load_dwordx4 v113, s[40:43], s57 offen offset:1280 nt lds\n"
  "s_add_u32 s57, s57, 384\n"
  "v_exp_f32 v20, v20\n"
  "v_mfma_f32_16x16x32_bf16 v[4:7], v[44:47], a[36:39], v[4:7]\n"
  "s_add_u32 m0, s59, 18176\n"
  "s_waitcnt vmcnt(10)\n"
  "ds_read_b128 v[152:155], v87 offset:6144\n"
  "ds_read_b128 v[156:159], v88 offset:6144\n"
  "v_exp_f32 v17, v17\n"
  "v_mfma_f32_16x16x32_bf16 v[4:7], v[40:43], a[32:35], v[4:7]\n"
  "s_waitcnt lgkmcnt(14)\n"
  "buffer_load_dwordx4 v112, s[40:43], s57 offen offset:256 nt lds\n"
  "buffer_load_dwordx4 v113, s[40:43], s57 offen offset:1280 nt lds\n"
  "v_exp_f32_e64 v21, v21\n"
  "v_mfma_f32_16x16x32_bf16 v[4:7], v[36:39], a[28:31], v[4:7]\n"
  "s_mov_b32 m0, s88\n"
  "s_waitcnt vmcnt(10)\n"
  "ds_read_b128 v[144:147], v87 offset:8192\n"
  "ds_read_b128 v[148:151], v88 offset:8192\n"
  "v_cvt_pk_bf16_f32 v16, v16, v20\n"
  "v_cvt_pk_bf16_f32 v17, v17, v21\n"
  "v_mfma_f32_16x16x32_bf16 v[4:7], v[32:35], a[24:27], v[4:7]\n"
  "ds_write_addtid_b32 v16 offset:1536\n"
  "ds_write_addtid_b32 v17 offset:5632\n"
  "v_mfma_f32_16x16x32_bf16 v[8:11], v[32:35], a[48:51], v[92:95]\n"
  "s_add_u32 m0, s59, -128\n"
  "v_exp_f32 v18, v18\n"
  "v_mfma_f32_16x16x32_bf16 v[8:11], v[36:39], a[52:55], v[8:11]\n"
  "s_waitcnt lgkmcnt(14)\n"
  "buffer_load_dwordx4 v112, s[40:43], s57 offen offset:128 nt lds\n"
  "buffer_load_dwordx4 v113, s[40:43], s57 offen offset:1152 nt lds\n"
  "v_exp_f32 v22, v22\n"
  "v_mfma_f32_16x16x32_bf16 v[8:11], v[40:43], a[56:59], v[8:11]\n"
  "s_add_u32 m0, s59, 2048\n"
  "s_waitcnt vmcnt(10)\n"
  "ds_read_b128 v[136:139], v87 offset:10240\n"
  "ds_read_b128 v[140:143], v88 offset:10240\n"
  "v_exp_f32 v19, v19\n"
  "v_mfma_f32_16x16x32_bf16 v[8:11], v[44:47], a[60:63], v[8:11]\n"
  "s_waitcnt lgkmcnt(12)\n"
  "buffer_load_dwordx4 v112, s[40:43], s57 offen offset:0 nt lds\n"
  "buffer_load_dwordx4 v113, s[40:43], s57 offen offset:1024 nt lds\n"
  "s_add_u32 s57, s57, 11904\n"
  "v_exp_f32_e64 v23, v23\n"
  "v_mfma_f32_16x16x32_bf16 v[8:11], v[48:51], a[64:67], v[8:11]\n"
  "s_mov_b32 m0, s88\n"
  "s_waitcnt vmcnt(10)\n"
  "ds_read_b128 v[160:163], v87 offset:12288\n"
  "ds_read_b128 v[164:167], v88 offset:12288\n"
  "v_cvt_pk_bf16_f32 v18, v18, v22\n"
  "v_cvt_pk_bf16_f32 v19, v19, v23\n"
  "v_mfma_f32_16x16x32_bf16 v[8:11], v[52:55], a[68:71], v[8:11]\n"
  "ds_write_addtid_b32 v18 offset:9760\n"
  "ds_write_addtid_b32 v19 offset:13856\n"
  "v_mfma_f32_16x16x32_bf16 v[12:15], v[52:55], a[92:95], v[92:95]\n"
  "s_add_u32 m0, s59, 4096\n"
  "v_exp_f32 v24, v24\n"
  "v_mfma_f32_16x16x32_bf16 v[12:15], v[48:51], a[88:91], v[12:15]\n"
  "s_waitcnt lgkmcnt(14)\n"
  "buffer_load_dwordx4 v112, s[40:43], s57 offen offset:0 nt lds\n"
  "buffer_load_dwordx4 v113, s[40:43], s57 offen offset:1024 nt lds\n"
  "v_exp_f32 v28, v28\n"
  "v_mfma_f32_16x16x32_bf16 v[12:15], v[44:47], a[84:87], v[12:15]\n"
  "s_waitcnt vmcnt(10)\n"
  "ds_read_b128 v[168:171], v87 offset:14336\n"
  "ds_read_b128 v[172:175], v88 offset:14336\n"
  "v_exp_f32 v25, v25\n"
  "v_mfma_f32_16x16x32_bf16 v[12:15], v[40:43], a[80:83], v[12:15]\n"
  "v_exp_f32_e64 v29, v29\n"
  "v_mfma_f32_16x16x32_bf16 v[12:15], v[36:39], a[76:79], v[12:15]\n"
  "s_mov_b32 m0, s88\n"
  "v_cvt_pk_bf16_f32 v24, v24, v28\n"
  "v_cvt_pk_bf16_f32 v25, v25, v29\n"
  "v_mfma_f32_16x16x32_bf16 v[12:15], v[32:35], a[72:75], v[12:15]\n"
  "ds_write_addtid_b32 v24 offset:1792\n"
  "ds_write_addtid_b32 v25 offset:5888\n"
  "v_mfma_f32_16x16x32_bf16 v[16:19], v[32:35], a[96:99], v[92:95]\n"
  "s_add_u32 m0, s59, 6016\n"
  "v_exp_f32 v26, v26\n"
  "v_mfma_f32_16x16x32_bf16 v[16:19], v[36:39], a[100:103], v[16:19]\n"
  "s_waitcnt lgkmcnt(14)\n"
  "buffer_load_dwordx4 v112, s[40:43], s57 offen offset:128 nt lds\n"
  "buffer_load_dwordx4 v113, s[40:43], s57 offen offset:1152 nt lds\n"
  "v_exp_f32 v30, v30\n"
  "v_mfma_f32_16x16x32_bf16 v[16:19], v[40:43], a[104:107], v[16:19]\n"
  "s_add_u32 m0, s59, 7936\n"
  "s_waitcnt vmcnt(10)\n"
  "ds_read_b128 v[176:179], v87 offset:16384\n"
  "ds_read_b128 v[180:183], v88 offset:16384\n"
  "v_exp_f32 v27, v27\n"
  "v_mfma_f32_16x16x32_bf16 v[16:19], v[44:47], a[108:111], v[16:19]\n"
  "s_waitcnt lgkmcnt(14)\n"
  "buffer_load_dwordx4 v112, s[40:43], s57 offen offset:256 nt lds\n"
  "buffer_load_dwordx4 v113, s[40:43], s57 offen offset:1280 nt lds\n"
  "s_add_u32 s57, s57, 384\n"
  "v_exp_f32_e64 v31, v31\n"
  "v_mfma_f32_16x16x32_bf16 v[16:19], v[48:51], a[112:115], v[16:19]\n"
  "s_mov_b32 m0, s88\n"
  "s_waitcnt vmcnt(10)\n"
  "ds_read_b128 v[200:203], v87 offset:18432\n"
  "ds_read_b128 v[204:207], v88 offset:18432\n"
  "v_cvt_pk_bf16_f32 v26, v26, v30\n"
  "v_cvt_pk_bf16_f32 v27, v27, v31\n"
  "v_mfma_f32_16x16x32_bf16 v[16:19], v[52:55], a[116:119], v[16:19]\n"
  "ds_write_addtid_b32 v26 offset:10016\n"
  "ds_write_addtid_b32 v27 offset:14112\n"
  "v_pk_mul_f32 v[0:1], v[0:1], s[60:61] op_sel_hi:[1,0]\n"
  "v_pk_mul_f32 v[2:3], v[2:3], s[60:61] op_sel_hi:[1,0]\n"
  "v_pk_mul_f32 v[4:5], v[4:5], s[60:61] op_sel_hi:[1,0]\n"
  "v_pk_mul_f32 v[6:7], v[6:7], s[60:61] op_sel_hi:[1,0]\n"
  "v_pk_mul_f32 v[8:9], v[8:9], s[60:61] op_sel_hi:[1,0]\n"
  "v_pk_mul_f32 v[10:11], v[10:11], s[60:61] op_sel_hi:[1,0]\n"
  "v_pk_mul_f32 v[12:13], v[12:13], s[60:61] op_sel_hi:[1,0]\n"
  "v_pk_mul_f32 v[14:15], v[14:15], s[60:61] op_sel_hi:[1,0]\n"
  "v_mfma_f32_16x16x32_bf16 v[20:23], v[52:55], a[140:143], v[92:95]\n"
  "s_add_u32 m0, s59, 9984\n"
  "v_exp_f32 v0, v0\n"
  "v_mfma_f32_16x16x32_bf16 v[20:23], v[48:51], a[136:139], v[20:23]\n"
  "s_waitcnt lgkmcnt(14)\n"
  "buffer_load_dwordx4 v112, s[40:43], s57 offen offset:256 nt lds\n"
  "buffer_load_dwordx4 v113, s[40:43], s57 offen offset:1280 nt lds\n"
  "v_exp_f32 v4, v4\n"
  "v_mfma_f32_16x16x32_bf16 v[20:23], v[44:47], a[132:135], v[20:23]\n"
  "s_add_u32 m0, s59, 12160\n"
  "s_waitcnt vmcnt(10)\n"
  "ds_read_b128 v[192:195], v87 offset:0\n"
  "ds_read_b128 v[196:199], v88 offset:0\n"
  "v_exp_f32 v1, v1\n"
  "v_mfma_f32_16x16x32_bf16 v[20:23], v[40:43], a[128:131], v[20:23]\n"
  "s_waitcnt lgkmcnt(14)\n"
  "buffer_load_dwordx4 v112, s[40:43], s57 offen offset:128 nt lds\n"
  "buffer_load_dwordx4 v113, s[40:43], s57 offen offset:1152 nt lds\n"
  "v_exp_f32 v5, v5\n"
  "v_mfma_f32_16x16x32_bf16 v[20:23], v[36:39], a[124:127], v[20:23]\n"
  "s_add_u32 s88, s88, 16448\n"
  "s_cmp_eq_u32 s88, s68\n"
  "s_cselect_b32 s88, s89, s88\n"
  "s_mov_b32 m0, s88\n"
  "s_mov_b32 m0, s88\n"
  "s_waitcnt vmcnt(10)\n"
  "ds_read_b128 v[184:187], v87 offset:2048\n"
  "ds_read_b128 v[188:191], v88 offset:2048\n"
  "v_cvt_pk_bf16_f32 v0, v0, v4\n"
  "v_cvt_pk_bf16_f32 v1, v1, v5\n"
  "v_mfma_f32_16x16x32_bf16 v[20:23], v[32:35], a[120:123], v[20:23]\n"
  "ds_write_addtid_b32 v0 offset:0\n"
  "ds_write_addtid_b32 v1 offset:4096\n"
  "v_mfma_f32_16x16x32_bf16 v[24:27], v[32:35], a[144:147], v[92:95]\n"
  "s_add_u32 m0, s59, 14336\n"
  "v_exp_f32 v2, v2\n"
  "v_mfma_f32_16x16x32_bf16 v[24:27], v[36:39], a[148:151], v[24:27]\n"
  "s_waitcnt lgkmcnt(14)\n"
  "buffer_load_dwordx4 v112, s[40:43], s57 offen offset:0 nt lds\n"
  "buffer_load_dwordx4 v113, s[40:43], s57 offen offset:1024 nt lds\n"
  "s_add_u32 s57, s57, 11904\n"
  "v_exp_f32 v6, v6\n"
  "v_mfma_f32_16x16x32_bf16 v[24:27], v[40:43], a[152:155], v[24:27]\n"
  "s_waitcnt vmcnt(10)\n"
  "ds_read_b128 v[208:211], v87 offset:4096\n"
  "ds_read_b128 v[212:215], v88 offset:4096\n"
  "v_exp_f32 v3, v3\n"
  "v_mfma_f32_16x16x32_bf16 v[24:27], v[44:47], a[156:159], v[24:27]\n"
  "v_exp_f32 v7, v7\n"
  "v_mfma_f32_16x16x32_bf16 v[24:27], v[48:51], a[160:163], v[24:27]\n"
  "s_mov_b32 m0, s88\n"
  "s_waitcnt vmcnt(8)\n"
  "ds_read_b128 v[216:219], v87 offset:6144\n"
  "ds_read_b128 v[220:223], v88 offset:6144\n"
  "v_cvt_pk_bf16_f32 v2, v2, v6\n"
  "v_cvt_pk_bf16_f32 v3, v3, v7\n"
  "v_mfma_f32_16x16x32_bf16 v[24:27], v[52:55], a[164:167], v[24:27]\n"
  "ds_write_addtid_b32 v2 offset:8224\n"
  "ds_write_addtid_b32 v3 offset:12320\n"
  "v_mfma_f32_16x16x32_bf16 v[28:31], v[52:55], a[188:191], v[92:95]\n"
  "v_exp_f32 v8, v8\n"
  "v_mfma_f32_16x16x32_bf16 v[28:31], v[48:51], a[184:187], v[28:31]\n"
  "v_exp_f32 v12, v12\n"
  "v_mfma_f32_16x16x32_bf16 v[28:31], v[44:47], a[180:183], v[28:31]\n"
  "s_waitcnt vmcnt(6)\n"
  "ds_read_b128 v[224:227], v87 offset:8192\n"
  "ds_read_b128 v[228:231], v88 offset:8192\n"
  "v_exp_f32 v9, v9\n"
  "v_mfma_f32_16x16x32_bf16 v[28:31], v[40:43], a[176:179], v[28:31]\n"
  "v_exp_f32_e64 v13, v13\n"
  "v_mfma_f32_16x16x32_bf16 v[28:31], v[36:39], a[172:175], v[28:31]\n"
  "v_cvt_pk_bf16_f32 v8, v8, v12\n"
  "v_cvt_pk_bf16_f32 v9, v9, v13\n"
  "v_mfma_f32_16x16x32_bf16 v[28:31], v[32:35], a[168:171], v[28:31]\n"
  "ds_write_addtid_b32 v8 offset:256\n"
  "ds_write_addtid_b32 v9 offset:4352\n"
  "v_mfma_f32_16x16x32_bf16 v[0:3], v[32:35], a[192:195], v[92:95]\n"
  "v_exp_f32 v10, v10\n"
  "v_mfma_f32_16x16x32_bf16 v[0:3], v[36:39], a[196:199], v[0:3]\n"
  "v_exp_f32 v14, v14\n"
  "v_mfma_f32_16x16x32_bf16 v[0:3], v[40:43], a[200:203], v[0:3]\n"
  "s_waitcnt vmcnt(4)\n"
  "ds_read_b128 v[248:251], v87 offset:10240\n"
  "ds_read_b128 v[252:255], v88 offset:10240\n"
  "v_exp_f32 v11, v11\n"
  "v_mfma_f32_16x16x32_bf16 v[0:3], v[44:47], a[204:207], v[0:3]\n"
  "v_exp_f32 v15, v15\n"
  "v_mfma_f32_16x16x32_bf16 v[0:3], v[48:51], a[208:211], v[0:3]\n"
  "s_waitcnt vmcnt(2)\n"
  "ds_read_b128 v[240:243], v87 offset:12288\n"
  "ds_read_b128 v[244:247], v88 offset:12288\n"
  "v_cvt_pk_bf16_f32 v10, v10, v14\n"
  "v_cvt_pk_bf16_f32 v11, v11, v15\n"
  "v_mfma_f32_16x16x32_bf16 v[0:3], v[52:55], a[212:215], v[0:3]\n"
  "ds_read_b128 v[56:59], v81 offset:0\n"
  "ds_read_b128 v[60:63], v82 offset:0\n"
  "ds_read_b128 v[64:67], v81 offset:2048\n"
  "ds_write_addtid_b32 v10 offset:8480\n"
  "ds_write_addtid_b32 v11 offset:12576\n"
  "v_pk_mul_f32 v[16:17], v[16:17], s[60:61] op_sel_hi:[1,0]\n"
  "v_pk_mul_f32 v[18:19], v[18:19], s[60:61] op_sel_hi:[1,0]\n"
  "v_pk_mul_f32 v[20:21], v[20:21], s[60:61] op_sel_hi:[1,0]\n"
  "v_pk_mul_f32 v[22:23], v[22:23], s[60:61] op_sel_hi:[1,0]\n"
  "v_pk_mul_f32 v[24:25], v[24:25], s[60:61] op_sel_hi:[1,0]\n"
  "v_pk_mul_f32 v[26:27], v[26:27], s[60:61] op_sel_hi:[1,0]\n"
  "v_pk_mul_f32 v[28:29], v[28:29], s[60:61] op_sel_hi:[1,0]\n"
  "v_pk_mul_f32 v[30:31], v[30:31], s[60:61] op_sel_hi:[1,0]\n"
  "v_mfma_f32_16x16x32_bf16 v[4:7], v[52:55], a[236:239], v[92:95]\n"
  "v_exp_f32 v16, v16\n"
  "v_mfma_f32_16x16x32_bf16 v[4:7], v[48:51], a[232:235], v[4:7]\n"
  "v_exp_f32 v20, v20\n"
  "v_mfma_f32_16x16x32_bf16 v[4:7], v[44:47], a[228:231], v[4:7]\n"
  "s_waitcnt vmcnt(0)\n"
  "ds_read_b128 v[232:235], v87 offset:14336\n"
  "ds_read_b128 v[236:239], v88 offset:14336\n"
  "v_exp_f32 v17, v17\n"
  "v_mfma_f32_16x16x32_bf16 v[4:7], v[40:43], a[224:227], v[4:7]\n"
  "v_exp_f32_e64 v21, v21\n"
  "v_mfma_f32_16x16x32_bf16 v[4:7], v[36:39], a[220:223], v[4:7]\n"
  "v_cvt_pk_bf16_f32 v16, v16, v20\n"
  "v_cvt_pk_bf16_f32 v17, v17, v21\n"
  "v_mfma_f32_16x16x32_bf16 v[4:7], v[32:35], a[216:219], v[4:7]\n"
  "s_waitcnt lgkmcnt(0)\n"
  "s_barrier\n"
  "ds_write_addtid_b32 v16 offset:512\n"
  "ds_write_addtid_b32 v17 offset:4608\n"
  "v_mfma_f32_16x16x32_bf16 v[8:11], v[32:35], a[240:243], v[92:95]\n"
  "v_exp_f32 v18, v18\n"
  "v_mfma_f32_16x16x32_bf16 v[8:11], v[36:39], a[244:247], v[8:11]\n"
  "v_exp_f32 v22, v22\n"
  "v_mfma_f32_16x16x32_bf16 v[8:11], v[40:43], a[248:251], v[8:11]\n"
  "v_exp_f32 v19, v19\n"
  "v_mfma_f32_16x16x32_bf16 v[8:11], v[44:47], a[252:255], v[8:11]\n"
  "v_exp_f32 v23, v23\n"
  "v_mfma_f32_16x16x32_bf16 v[8:11], v[48:51], v[128:131], v[8:11]\n"
  "v_cvt_pk_bf16_f32 v18, v18, v22\n"
  "v_cvt_pk_bf16_f32 v19, v19, v23\n"
  "v_mfma_f32_16x16x32_bf16 v[8:11], v[52:55], v[132:135], v[8:11]\n"
  "ds_write_addtid_b32 v18 offset:8736\n"
  "ds_write_addtid_b32 v19 offset:12832\n"
  "v_mfma_f32_16x16x32_bf16 v[12:15], v[52:55], v[156:159], v[92:95]\n"
  "v_exp_f32 v24, v24\n"
  "v_mfma_f32_16x16x32_bf16 v[12:15], v[48:51], v[152:155], v[12:15]\n"
  "v_exp_f32 v28, v28\n"
  "v_mfma_f32_16x16x32_bf16 v[12:15], v[44:47], v[148:151], v[12:15]\n"
  "v_exp_f32 v25, v25\n"
  "v_mfma_f32_16x16x32_bf16 v[12:15], v[40:43], v[144:147], v[12:15]\n"
  "v_exp_f32 v29, v29\n"
  "v_mfma_f32_16x16x32_bf16 v[12:15], v[36:39], v[140:143], v[12:15]\n"
  "v_cvt_pk_bf16_f32 v24, v24, v28\n"
  "v_cvt_pk_bf16_f32 v25, v25, v29\n"
  "v_mfma_f32_16x16x32_bf16 v[12:15], v[32:35], v[136:139], v[12:15]\n"
  "ds_write_addtid_b32 v24 offset:768\n"
  "ds_write_addtid_b32 v25 offset:4864\n"
  "v_mfma_f32_16x16x32_bf16 v[16:19], v[32:35], v[160:163], v[92:95]\n"
  "v_exp_f32 v26, v26\n"
  "v_mfma_f32_16x16x32_bf16 v[16:19], v[36:39], v[164:167], v[16:19]\n"
  "v_exp_f32 v30, v30\n"
  "v_mfma_f32_16x16x32_bf16 v[16:19], v[40:43], v[168:171], v[16:19]\n"
  "v_exp_f32 v27, v27\n"
  "v_mfma_f32_16x16x32_bf16 v[16:19], v[44:47], v[172:175], v[16:19]\n"
  "v_exp_f32 v31, v31\n"
  "v_mfma_f32_16x16x32_bf16 v[16:19], v[48:51], v[176:179], v[16:19]\n"
  "v_cvt_pk_bf16_f32 v26, v26, v30\n"
  "v_cvt_pk_bf16_f32 v27, v27, v31\n"
  "v_mfma_f32_16x16x32_bf16 v[16:19], v[52:55], v[180:183], v[16:19]\n"
  "ds_read_b128 v[68:71], v82 offset:2048\n"
  "ds_read_b128 v[72:75], v81 offset:4096\n"
  "ds_read_b128 v[76:79], v82 offset:4096\n"
  "ds_write_addtid_b32 v26 offset:8992\n"
  "ds_write_addtid_b32 v27 offset:13088\n"
  "v_pk_mul_f32 v[0:1], v[0:1], s[60:61] op_sel_hi:[1,0]\n"
  "v_pk_mul_f32 v[2:3], v[2:3], s[60:61] op_sel_hi:[1,0]\n"
  "v_pk_mul_f32 v[4:5], v[4:5], s[60:61] op_sel_hi:[1,0]\n"
  "v_pk_mul_f32 v[6:7], v[6:7], s[60:61] op_sel_hi:[1,0]\n"
  "v_pk_mul_f32 v[8:9], v[8:9], s[60:61] op_sel_hi:[1,0]\n"
  "v_pk_mul_f32 v[10:11], v[10:11], s[60:61] op_sel_hi:[1,0]\n"
  "v_pk_mul_f32 v[12:13], v[12:13], s[60:61] op_sel_hi:[1,0]\n"
  "v_pk_mul_f32 v[14:15], v[14:15], s[60:61] op_sel_hi:[1,0]\n"
  "v_mfma_f32_16x16x32_bf16 v[20:23], v[52:55], v[204:207], v[92:95]\n"
  "v_exp_f32 v0, v0\n"
  "v_mfma_f32_16x16x32_bf16 v[20:23], v[48:51], v[200:203], v[20:23]\n"
  "v_exp_f32 v4, v4\n"
  "v_mfma_f32_16x16x32_bf16 v[20:23], v[44:47], v[196:199], v[20:23]\n"
  "v_exp_f32 v1, v1\n"
  "v_mfma_f32_16x16x32_bf16 v[20:23], v[40:43], v[192:195], v[20:23]\n"
  "v_exp_f32 v5, v5\n"
  "v_mfma_f32_16x16x32_bf16 v[20:23], v[36:39], v[188:191], v[20:23]\n"
  "v_cvt_pk_bf16_f32 v0, v0, v4\n"
  "v_cvt_pk_bf16_f32 v1, v1, v5\n"
  "v_mfma_f32_16x16x32_bf16 v[20:23], v[32:35], v[184:187], v[20:23]\n"
  "ds_write_addtid_b32 v0 offset:1024\n"
  "ds_write_addtid_b32 v1 offset:5120\n"
  "v_mfma_f32_16x16x32_bf16 v[24:27], v[32:35], v[208:211], v[92:95]\n"
  "v_exp_f32 v2, v2\n"
  "v_mfma_f32_16x16x32_bf16 v[24:27], v[36:39], v[212:215], v[24:27]\n"
  "v_exp_f32 v6, v6\n"
  "v_mfma_f32_16x16x32_bf16 v[24:27], v[40:43], v[216:219], v[24:27]\n"
  "v_exp_f32 v3, v3\n"
  "v_mfma_f32_16x16x32_bf16 v[24:27], v[44:47], v[220:223], v[24:27]\n"
  "v_exp_f32 v7, v7\n"
  "v_mfma_f32_16x16x32_bf16 v[24:27], v[48:51], v[224:227], v[24:27]\n"
  "v_cvt_pk_bf16_f32 v2, v2, v6\n"
  "v_cvt_pk_bf16_f32 v3, v3, v7\n"
  "v_mfma_f32_16x16x32_bf16 v[24:27], v[52:55], v[228:231], v[24:27]\n"
  "ds_write_addtid_b32 v2 offset:9248\n"
  "ds_write_addtid_b32 v3 offset:13344\n"
  "v_mfma_f32_16x16x32_bf16 v[28:31], v[52:55], v[252:255], v[92:95]\n"
  "v_exp_f32 v8, v8\n"
  "v_mfma_f32_16x16x32_bf16 v[28:31], v[48:51], v[248:251], v[28:31]\n"
  "v_exp_f32 v12, v12\n"
  "v_mfma_f32_16x16x32_bf16 v[28:31], v[44:47], v[244:247], v[28:31]\n"
  "v_exp_f32 v9, v9\n"
  "v_mfma_f32_16x16x32_bf16 v[28:31], v[40:43], v[240:243], v[28:31]\n"
  "s_sub_u32 s56, s56, 1\n"
  "v_exp_f32 v13, v13\n"
  "v_mfma_f32_16x16x32_bf16 v[28:31], v[36:39], v[236:239], v[28:31]\n"
  "v_cvt_pk_bf16_f32 v8, v8, v12\n"
  "v_cvt_pk_bf16_f32 v9, v9, v13\n"
  "v_mfma_f32_16x16x32_bf16 v[28:31], v[32:35], v[232:235], v[28:31]\n"
  "s_waitcnt lgkmcnt(6)\n"
  "ds_write_addtid_b32 v8 offset:1280\n"
  "ds_write_addtid_b32 v9 offset:5376\n"
  ".p2align 3\n"
  "v_mfma_f32_16x16x32_bf16 v[0:3], v[56:59], a[0:3], v[92:95]\n"
  "s_add_u32 s66, s66, 6144\n"
  "v_exp_f32 v10, v10\n"
  "v_mfma_f32_16x16x32_bf16 v[0:3], v[60:63], a[4:7], v[0:3]\n"
  "s_cmp_eq_u32 s66, 102400\n"
  "v_exp_f32 v14, v14\n"
  "v_mfma_f32_16x16x32_bf16 v[0:3], v[64:67], a[8:11], v[0:3]\n"
  "s_cselect_b32 s66, 65536, s66\n"
  "v_exp_f32 v11, v11\n"
  "v_mfma_f32_16x16x32_bf16 v[0:3], v[68:71], a[12:15], v[0:3]\n"
  "v_exp_f32 v15, v15\n"
  "v_mfma_f32_16x16x32_bf16 v[0:3], v[72:75], a[16:19], v[0:3]\n"
  "v_cvt_pk_bf16_f32 v10, v10, v14\n"
  "v_cvt_pk_bf16_f32 v11, v11, v15\n"
  "v_mfma_f32_16x16x32_bf16 v[0:3], v[76:79], a[20:23], v[0:3]\n"
  "ds_write_addtid_b32 v10 offset:9504\n"
  "ds_write_addtid_b32 v11 offset:13600\n"
  "v_add_u32 v81, s66, v83\n"
  "v_add_u32 v82, s66, v84\n"
  "v_pk_mul_f32 v[16:17], v[16:17], s[60:61] op_sel_hi:[1,0]\n"
  "v_pk_mul_f32 v[18:19], v[18:19], s[60:61] op_sel_hi:[1,0]\n"
  "v_pk_mul_f32 v[20:21], v[20:21], s[60:61] op_sel_hi:[1,0]\n"
  "v_pk_mul_f32 v[22:23], v[22:23], s[60:61] op_sel_hi:[1,0]\n"
  "v_pk_mul_f32 v[24:25], v[24:25], s[60:61] op_sel_hi:[1,0]\n"
  "v_pk_mul_f32 v[26:27], v[26:27], s[60:61] op_sel_hi:[1,0]\n"
  "v_pk_mul_f32 v[28:29], v[28:29], s[60:61] op_sel_hi:[1,0]\n"
  "v_pk_mul_f32 v[30:31], v[30:31], s[60:61] op_sel_hi:[1,0]\n"
  "v_mfma_f32_16x16x32_bf16 v[4:7], v[76:79], a[44:47], v[92:95]\n"
  "v_exp_f32 v16, v16\n"
  "v_mfma_f32_16x16x32_bf16 v[4:7], v[72:75], a[40:43], v[4:7]\n"
  "v_exp_f32 v20, v20\n"
  "v_mfma_f32_16x16x32_bf16 v[4:7], v[68:71], a[36:39], v[4:7]\n"
  "v_exp_f32 v17, v17\n"
  "v_mfma_f32_16x16x32_bf16 v[4:7], v[64:67], a[32:35], v[4:7]\n"
  "v_exp_f32 v21, v21\n"
  "v_mfma_f32_16x16x32_bf16 v[4:7], v[60:63], a[28:31], v[4:7]\n"
  "v_cvt_pk_bf16_f32 v16, v16, v20\n"
  "v_cvt_pk_bf16_f32 v17, v17, v21\n"
  "v_mfma_f32_16x16x32_bf16 v[4:7], v[56:59], a[24:27], v[4:7]\n"
  "ds_write_addtid_b32 v16 offset:1536\n"
  "ds_write_addtid_b32 v17 offset:5632\n"
  "v_mfma_f32_16x16x32_bf16 v[8:11], v[56:59], a[48:51], v[92:95]\n"
  "v_exp_f32 v18, v18\n"
  "v_mfma_f32_16x16x32_bf16 v[8:11], v[60:63], a[52:55], v[8:11]\n"
  "v_exp_f32 v22, v22\n"
  "v_mfma_f32_16x16x32_bf16 v[8:11], v[64:67], a[56:59], v[8:11]\n"
  "v_exp_f32 v19, v19\n"
  "v_mfma_f32_16x16x32_bf16 v[8:11], v[68:71], a[60:63], v[8:11]\n"
  "v_exp_f32 v23, v23\n"
  "v_mfma_f32_16x16x32_bf16 v[8:11], v[72:75], a[64:67], v[8:11]\n"
  "v_cvt_pk_bf16_f32 v18, v18, v22\n"
  "v_cvt_pk_bf16_f32 v19, v19, v23\n"
  "v_mfma_f32_16x16x32_bf16 v[8:11], v[76:79], a[68:71], v[8:11]\n"
  "ds_write_addtid_b32 v18 offset:9760\n"
  "ds_write_addtid_b32 v19 offset:13856\n"
  "v_mfma_f32_16x16x32_bf16 v[12:15], v[76:79], a[92:95], v[92:95]\n"
  "v_exp_f32 v24, v24\n"
  "v_mfma_f32_16x16x32_bf16 v[12:15], v[72:75], a[88:91], v[12:15]\n"
  "v_exp_f32 v28, v28\n"
  "v_mfma_f32_16x16x32_bf16 v[12:15], v[68:71], a[84:87], v[12:15]\n"
  "v_exp_f32 v25, v25\n"
  "v_mfma_f32_16x16x32_bf16 v[12:15], v[64:67], a[80:83], v[12:15]\n"
  "v_exp_f32 v29, v29\n"
  "v_mfma_f32_16x16x32_bf16 v[12:15], v[60:63], a[76:79], v[12:15]\n"
  "v_cvt_pk_bf16_f32 v24, v24, v28\n"
  "v_cvt_pk_bf16_f32 v25, v25, v29\n"
  "v_mfma_f32_16x16x32_bf16 v[12:15], v[56:59], a[72:75], v[12:15]\n"
  "ds_write_addtid_b32 v24 offset:1792\n"
  "ds_write_addtid_b32 v25 offset:5888\n"
  "v_mfma_f32_16x16x32_bf16 v[16:19], v[56:59], a[96:99], v[92:95]\n"
  "v_exp_f32 v26, v26\n"
  "v_mfma_f32_16x16x32_bf16 v[16:19], v[60:63], a[100:103], v[16:19]\n"
  "v_exp_f32 v30, v30\n"
  "v_mfma_f32_16x16x32_bf16 v[16:19], v[64:67], a[104:107], v[16:19]\n"
  "v_exp_f32 v27, v27\n"
  "v_mfma_f32_16x16x32_bf16 v[16:19], v[68:71], a[108:111], v[16:19]\n"
  "v_exp_f32 v31, v31\n"
  "v_mfma_f32_16x16x32_bf16 v[16:19], v[72:75], a[112:115], v[16:19]\n"
  "v_cvt_pk_bf16_f32 v26, v26, v30\n"
  "v_cvt_pk_bf16_f32 v27, v27, v31\n"
  "v_mfma_f32_16x16x32_bf16 v[16:19], v[76:79], a[116:119], v[16:19]\n"
  "ds_write_addtid_b32 v26 offset:10016\n"
  "ds_write_addtid_b32 v27 offset:14112\n"
  "v_pk_mul_f32 v[0:1], v[0:1], s[60:61] op_sel_hi:[1,0]\n"
  "v_pk_mul_f32 v[2:3], v[2:3], s[60:61] op_sel_hi:[1,0]\n"
  "v_pk_mul_f32 v[4:5], v[4:5], s[60:61] op_sel_hi:[1,0]\n"
  "v_pk_mul_f32 v[6:7], v[6:7], s[60:61] op_sel_hi:[1,0]\n"
  "v_pk_mul_f32 v[8:9], v[8:9], s[60:61] op_sel_hi:[1,0]\n"
  "v_pk_mul_f32 v[10:11], v[10:11], s[60:61] op_sel_hi:[1,0]\n"
  "v_pk_mul_f32 v[12:13], v[12:13], s[60:61] op_sel_hi:[1,0]\n"
  "v_pk_mul_f32 v[14:15], v[14:15], s[60:61] op_sel_hi:[1,0]\n"
  "v_mfma_f32_16x16x32_bf16 v[20:23], v[76:79], a[140:143], v[92:95]\n"
  "s_add_u32 s88, s88, 16448\n"
  "v_exp_f32 v0, v0\n"
  "v_mfma_f32_16x16x32_bf16 v[20:23], v[72:75], a[136:139], v[20:23]\n"
  "s_cmp_eq_u32 s88, s68\n"
  "v_exp_f32 v4, v4\n"
  "v_mfma_f32_16x16x32_bf16 v[20:23], v[68:71], a[132:135], v[20:23]\n"
  "s_cselect_b32 s88, s89, s88\n"
  "v_exp_f32 v1, v1\n"
  "v_mfma_f32_16x16x32_bf16 v[20:23], v[64:67], a[128:131], v[20:23]\n"
  "v_exp_f32_e64 v5, v5\n"
  "v_mfma_f32_16x16x32_bf16 v[20:23], v[60:63], a[124:127], v[20:23]\n"
  "s_mov_b32 m0, s88\n"
  "v_cvt_pk_bf16_f32 v0, v0, v4\n"
  "v_cvt_pk_bf16_f32 v1, v1, v5\n"
  "v_mfma_f32_16x16x32_bf16 v[20:23], v[56:59], a[120:123], v[20:23]\n"
  "ds_write_addtid_b32 v0 offset:0\n"
  "ds_write_addtid_b32 v1 offset:4096\n"
  "v_mfma_f32_16x16x32_bf16 v[24:27], v[56:59], a[144:147], v[92:95]\n"
  "v_exp_f32 v2, v2\n"
  "v_mfma_f32_16x16x32_bf16 v[24:27], v[60:63], a[148:151], v[24:27]\n"
  "v_exp_f32 v6, v6\n"
  "v_mfma_f32_16x16x32_bf16 v[24:27], v[64:67], a[152:155], v[24:27]\n"
  "v_exp_f32 v3, v3\n"
  "v_mfma_f32_16x16x32_bf16 v[24:27], v[68:71], a[156:159], v[24:27]\n"
  "v_exp_f32 v7, v7\n"
  "v_mfma_f32_16x16x32_bf16 v[24:27], v[72:75], a[160:163], v[24:27]\n"
  "v_cvt_pk_bf16_f32 v2, v2, v6\n"
  "v_cvt_pk_bf16_f32 v3, v3, v7\n"
  "v_mfma_f32_16x16x32_bf16 v[24:27], v[76:79], a[164:167], v[24:27]\n"
  "ds_write_addtid_b32 v2 offset:8224\n"
  "ds_write_addtid_b32 v3 offset:12320\n"
  "v_mfma_f32_16x16x32_bf16 v[28:31], v[76:79], a[188:191], v[92:95]\n"
  "v_exp_f32 v8, v8\n"
  "v_mfma_f32_16x16x32_bf16 v[28:31], v[72:75], a[184:187], v[28:31]\n"
  "v_exp_f32 v12, v12\n"
  "v_mfma_f32_16x16x32_bf16 v[28:31], v[68:71], a[180:183], v[28:31]\n"
  "v_exp_f32 v9, v9\n"
  "v_mfma_f32_16x16x32_bf16 v[28:31], v[64:67], a[176:179], v[28:31]\n"
  "v_exp_f32 v13, v13\n"
  "v_mfma_f32_16x16x32_bf16 v[28:31], v[60:63], a[172:175], v[28:31]\n"
  "v_cvt_pk_bf16_f32 v8, v8, v12\n"
  "v_cvt_pk_bf16_f32 v9, v9, v13\n"
  "v_mfma_f32_16x16x32_bf16 v[28:31], v[56:59], a[168:171], v[28:31]\n"
  "ds_write_addtid_b32 v8 offset:256\n"
  "ds_write_addtid_b32 v9 offset:4352\n"
  "v_mfma_f32_16x16x32_bf16 v[0:3], v[56:59], a[192:195], v[92:95]\n"
  "v_exp_f32 v10, v10\n"
  "v_mfma_f32_16x16x32_bf16 v[0:3], v[60:63], a[196:199], v[0:3]\n"
  "v_exp_f32 v14, v14\n"
  "v_mfma_f32_16x16x32_bf16 v[0:3], v[64:67], a[200:203], v[0:3]\n"
  "v_exp_f32 v11, v11\n"
  "v_mfma_f32_16x16x32_bf16 v[0:3], v[68:71], a[204:207], v[0:3]\n"
  "v_exp_f32 v15, v15\n"
  "v_mfma_f32_16x16x32_bf16 v[0:3], v[72:75], a[208:211], v[0:3]\n"
  "v_cvt_pk_bf16_f32 v10, v10, v14\n"
  "v_cvt_pk_bf16_f32 v11, v11, v15\n"
  "v_mfma_f32_16x16x32_bf16 v[0:3], v[76:79], a[212:215], v[0:3]\n"
  "ds_read_b128 v[32:35], v81 offset:0\n"
  "ds_read_b128 v[36:39], v82 offset:0\n"
  "ds_read_b128 v[40:43], v81 offset:2048\n"
  "ds_write_addtid_b32 v10 offset:8480\n"
  "ds_write_addtid_b32 v11 offset:12576\n"
  "v_pk_mul_f32 v[16:17], v[16:17], s[60:61] op_sel_hi:[1,0]\n"
  "v_pk_mul_f32 v[18:19], v[18:19], s[60:61] op_sel_hi:[1,0]\n"
  "v_pk_mul_f32 v[20:21], v[20:21], s[60:61] op_sel_hi:[1,0]\n"
  "v_pk_mul_f32 v[22:23], v[22:23], s[60:61] op_sel_hi:[1,0]\n"
  "v_pk_mul_f32 v[24:25], v[24:25], s[60:61] op_sel_hi:[1,0]\n"
  "v_pk_mul_f32 v[26:27], v[26:27], s[60:61] op_sel_hi:[1,0]\n"
  "v_pk_mul_f32 v[28:29], v[28:29], s[60:61] op_sel_hi:[1,0]\n"
  "v_pk_mul_f32 v[30:31], v[30:31], s[60:61] op_sel_hi:[1,0]\n"
  "v_mfma_f32_16x16x32_bf16 v[4:7], v[76:79], a[236:239], v[92:95]\n"
  "v_exp_f32 v16, v16\n"
  "v_mfma_f32_16x16x32_bf16 v[4:7], v[72:75], a[232:235], v[4:7]\n"
  "v_exp_f32 v20, v20\n"
  "v_mfma_f32_16x16x32_bf16 v[4:7], v[68:71], a[228:231], v[4:7]\n"
  "v_exp_f32 v17, v17\n"
  "v_mfma_f32_16x16x32_bf16 v[4:7], v[64:67], a[224:227], v[4:7]\n"
  "v_exp_f32 v21, v21\n"
  "v_mfma_f32_16x16x32_bf16 v[4:7], v[60:63], a[220:223], v[4:7]\n"
  "v_cvt_pk_bf16_f32 v16, v16, v20\n"
  "v_cvt_pk_bf16_f32 v17, v17, v21\n"
  "v_mfma_f32_16x16x32_bf16 v[4:7], v[56:59], a[216:219], v[4:7]\n"
  "ds_write_addtid_b32 v16 offset:512\n"
  "ds_write_addtid_b32 v17 offset:4608\n"
  "v_mfma_f32_16x16x32_bf16 v[8:11], v[56:59], a[240:243], v[92:95]\n"
  "v_exp_f32 v18, v18\n"
  "v_mfma_f32_16x16x32_bf16 v[8:11], v[60:63], a[244:247], v[8:11]\n"
  "v_exp_f32 v22, v22\n"
  "v_mfma_f32_16x16x32_bf16 v[8:11], v[64:67], a[248:251], v[8:11]\n"
  "v_exp_f32 v19, v19\n"
  "v_mfma_f32_16x16x32_bf16 v[8:11], v[68:71], a[252:255], v[8:11]\n"
  "v_exp_f32 v23, v23\n"
  "v_mfma_f32_16x16x32_bf16 v[8:11], v[72:75], v[128:131], v[8:11]\n"
  "v_cvt_pk_bf16_f32 v18, v18, v22\n"
  "v_cvt_pk_bf16_f32 v19, v19, v23\n"
  "v_mfma_f32_16x16x32_bf16 v[8:11], v[76:79], v[132:135], v[8:11]\n"
  "ds_write_addtid_b32 v18 offset:8736\n"
  "ds_write_addtid_b32 v19 offset:12832\n"
  "v_mfma_f32_16x16x32_bf16 v[12:15], v[76:79], v[156:159], v[92:95]\n"
  "v_exp_f32 v24, v24\n"
  "v_mfma_f32_16x16x32_bf16 v[12:15], v[72:75], v[152:155], v[12:15]\n"
  "v_exp_f32 v28, v28\n"
  "v_mfma_f32_16x16x32_bf16 v[12:15], v[68:71], v[148:151], v[12:15]\n"
  "v_exp_f32 v25, v25\n"
  "v_mfma_f32_16x16x32_bf16 v[12:15], v[64:67], v[144:147], v[12:15]\n"
  "v_exp_f32 v29, v29\n"
  "v_mfma_f32_16x16x32_bf16 v[12:15], v[60:63], v[140:143], v[12:15]\n"
  "v_cvt_pk_bf16_f32 v24, v24, v28\n"
  "v_cvt_pk_bf16_f32 v25, v25, v29\n"
  "v_mfma_f32_16x16x32_bf16 v[12:15], v[56:59], v[136:139], v[12:15]\n"
  "ds_write_addtid_b32 v24 offset:768\n"
  "ds_write_addtid_b32 v25 offset:4864\n"
  "v_mfma_f32_16x16x32_bf16 v[16:19], v[56:59], v[160:163], v[92:95]\n"
  "v_exp_f32 v26, v26\n"
  "v_mfma_f32_16x16x32_bf16 v[16:19], v[60:63], v[164:167], v[16:19]\n"
  "v_exp_f32 v30, v30\n"
  "v_mfma_f32_16x16x32_bf16 v[16:19], v[64:67], v[168:171], v[16:19]\n"
  "v_exp_f32 v27, v27\n"
  "v_mfma_f32_16x16x32_bf16 v[16:19], v[68:71], v[172:175], v[16:19]\n"
  "v_exp_f32 v31, v31\n"
  "v_mfma_f32_16x16x32_bf16 v[16:19], v[72:75], v[176:179], v[16:19]\n"
  "v_cvt_pk_bf16_f32 v26, v26, v30\n"
  "v_cvt_pk_bf16_f32 v27, v27, v31\n"
  "v_mfma_f32_16x16x32_bf16 v[16:19], v[76:79], v[180:183], v[16:19]\n"
  "s_waitcnt lgkmcnt(15)\n"
  "s_barrier\n"
  "ds_read_b128 v[44:47], v82 offset:2048\n"
  "ds_read_b128 v[48:51], v81 offset:4096\n"
  "ds_read_b128 v[52:55], v82 offset:4096\n"
  "ds_write_addtid_b32 v26 offset:8992\n"
  "ds_write_addtid_b32 v27 offset:13088\n"
  "v_pk_mul_f32 v[0:1], v[0:1], s[60:61] op_sel_hi:[1,0]\n"
  "v_pk_mul_f32 v[2:3], v[2:3], s[60:61] op_sel_hi:[1,0]\n"
  "v_pk_mul_f32 v[4:5], v[4:5], s[60:61] op_sel_hi:[1,0]\n"
  "v_pk_mul_f32 v[6:7], v[6:7], s[60:61] op_sel_hi:[1,0]\n"
  "v_pk_mul_f32 v[8:9], v[8:9], s[60:61] op_sel_hi:[1,0]\n"
  "v_pk_mul_f32 v[10:11], v[10:11], s[60:61] op_sel_hi:[1,0]\n"
  "v_pk_mul_f32 v[12:13], v[12:13], s[60:61] op_sel_hi:[1,0]\n"
  "v_pk_mul_f32 v[14:15], v[14:15], s[60:61] op_sel_hi:[1,0]\n"
  "v_mfma_f32_16x16x32_bf16 v[20:23], v[76:79], v[204:207], v[92:95]\n"
  "v_exp_f32 v0, v0\n"
  "v_mfma_f32_16x16x32_bf16 v[20:23], v[72:75], v[200:203], v[20:23]\n"
  "v_exp_f32 v4, v4\n"
  "v_mfma_f32_16x16x32_bf16 v[20:23], v[68:71], v[196:199], v[20:23]\n"
  "v_exp_f32 v1, v1\n"
  "v_mfma_f32_16x16x32_bf16 v[20:23], v[64:67], v[192:195], v[20:23]\n"
  "v_exp_f32 v5, v5\n"
  "v_mfma_f32_16x16x32_bf16 v[20:23], v[60:63], v[188:191], v[20:23]\n"
  "v_cvt_pk_bf16_f32 v0, v0, v4\n"
  "v_cvt_pk_bf16_f32 v1, v1, v5\n"
  "v_mfma_f32_16x16x32_bf16 v[20:23], v[56:59], v[184:187], v[20:23]\n"
  "ds_write_addtid_b32 v0 offset:1024\n"
  "ds_write_addtid_b32 v1 offset:5120\n"
  "v_mfma_f32_16x16x32_bf16 v[24:27], v[56:59], v[208:211], v[92:95]\n"
  "v_exp_f32 v2, v2\n"
  "v_mfma_f32_16x16x32_bf16 v[24:27], v[60:63], v[212:215], v[24:27]\n"
  "v_exp_f32 v6, v6\n"
  "v_mfma_f32_16x16x32_bf16 v[24:27], v[64:67], v[216:219], v[24:27]\n"
  "v_exp_f32 v3, v3\n"
  "v_mfma_f32_16x16x32_bf16 v[24:27], v[68:71], v[220:223], v[24:27]\n"
  "v_exp_f32 v7, v7\n"
  "v_mfma_f32_16x16x32_bf16 v[24:27], v[72:75], v[224:227], v[24:27]\n"
  "v_cvt_pk_bf16_f32 v2, v2, v6\n"
  "v_cvt_pk_bf16_f32 v3, v3, v7\n"
  "v_mfma_f32_16x16x32_bf16 v[24:27], v[76:79], v[228:231], v[24:27]\n"
  "ds_write_addtid_b32 v2 offset:9248\n"
  "ds_write_addtid_b32 v3 offset:13344\n"
  "v_mfma_f32_16x16x32_bf16 v[28:31], v[76:79], v[252:255], v[92:95]\n"
  "v_exp_f32 v8, v8\n"
  "v_mfma_f32_16x16x32_bf16 v[28:31], v[72:75], v[248:251], v[28:31]\n"
  "v_exp_f32 v12, v12\n"
  "v_mfma_f32_16x16x32_bf16 v[28:31], v[68:71], v[244:247], v[28:31]\n"
  "v_exp_f32 v9, v9\n"
  "v_mfma_f32_16x16x32_bf16 v[28:31], v[64:67], v[240:243], v[28:31]\n"
  "s_sub_u32 s56, s56, 1\n"
  "v_exp_f32 v13, v13\n"
  "v_mfma_f32_16x16x32_bf16 v[28:31], v[60:63], v[236:239], v[28:31]\n"
  "v_cvt_pk_bf16_f32 v8, v8, v12\n"
  "v_cvt_pk_bf16_f32 v9, v9, v13\n"
  "v_mfma_f32_16x16x32_bf16 v[28:31], v[56:59], v[232:235], v[28:31]\n"
  "s_waitcnt lgkmcnt(6)\n"
  "ds_write_addtid_b32 v8 offset:1280\n"
  "ds_write_addtid_b32 v9 offset:5376\n"
  ".p2align 3\n"
  "A_N0_%=:\n"
  "v_mfma_f32_16x16x32_bf16 v[0:3], v[32:35], a[0:3], v[92:95]\n"
  "s_add_u32 s66, s66, 6144\n"
  "v_exp_f32 v10, v10\n"
  "v_mfma_f32_16x16x32_bf16 v[0:3], v[36:39], a[4:7], v[0:3]\n"
  "s_cmp_eq_u32 s66, 102400\n"
  "v_exp_f32 v14, v14\n"
  "v_mfma_f32_16x16x32_bf16 v[0:3], v[40:43], a[8:11], v[0:3]\n"
  "s_cselect_b32 s66, 65536, s66\n"
  "v_exp_f32 v11, v11\n"
  "v_mfma_f32_16x16x32_bf16 v[0:3], v[44:47], a[12:15], v[0:3]\n"
  "v_exp_f32 v15, v15\n"
  "v_mfma_f32_16x16x32_bf16 v[0:3], v[48:51], a[16:19], v[0:3]\n"
  "v_cvt_pk_bf16_f32 v10, v10, v14\n"
  "v_cvt_pk_bf16_f32 v11, v11, v15\n"
  "v_mfma_f32_16x16x32_bf16 v[0:3], v[52:55], a[20:23], v[0:3]\n"
  "ds_write_addtid_b32 v10 offset:9504\n"
  "ds_write_addtid_b32 v11 offset:13600\n"
  "v_add_u32 v81, s66, v83\n"
  "v_add_u32 v82, s66, v84\n"
  "v_pk_mul_f32 v[16:17], v[16:17], s[60:61] op_sel_hi:[1,0]\n"
  "v_pk_mul_f32 v[18:19], v[18:19], s[60:61] op_sel_hi:[1,0]\n"
  "v_pk_mul_f32 v[20:21], v[20:21], s[60:61] op_sel_hi:[1,0]\n"
  "v_pk_mul_f32 v[22:23], v[22:23], s[60:61] op_sel_hi:[1,0]\n"
  "v_pk_mul_f32 v[24:25], v[24:25], s[60:61] op_sel_hi:[1,0]\n"
  "v_pk_mul_f32 v[26:27], v[26:27], s[60:61] op_sel_hi:[1,0]\n"
  "v_pk_mul_f32 v[28:29], v[28:29], s[60:61] op_sel_hi:[1,0]\n"
  "v_pk_mul_f32 v[30:31], v[30:31], s[60:61] op_sel_hi:[1,0]\n"
  "v_mfma_f32_16x16x32_bf16 v[4:7], v[52:55], a[44:47], v[92:95]\n"
  "v_exp_f32 v16, v16\n"
  "v_mfma_f32_16x16x32_bf16 v[4:7], v[48:51], a[40:43], v[4:7]\n"
  "v_exp_f32 v20, v20\n"
  "v_mfma_f32_16x16x32_bf16 v[4:7], v[44:47], a[36:39], v[4:7]\n"
  "v_exp_f32 v17, v17\n"
  "v_mfma_f32_16x16x32_bf16 v[4:7], v[40:43], a[32:35], v[4:7]\n"
  "v_exp_f32 v21, v21\n"
  "v_mfma_f32_16x16x32_bf16 v[4:7], v[36:39], a[28:31], v[4:7]\n"
  "v_cvt_pk_bf16_f32 v16, v16, v20\n"
  "v_cvt_pk_bf16_f32 v17, v17, v21\n"
  "v_mfma_f32_16x16x32_bf16 v[4:7], v[32:35], a[24:27], v[4:7]\n"
  "ds_write_addtid_b32 v16 offset:1536\n"
  "ds_write_addtid_b32 v17 offset:5632\n"
  "v_mfma_f32_16x16x32_bf16 v[8:11], v[32:35], a[48:51], v[92:95]\n"
  "v_exp_f32 v18, v18\n"
  "v_mfma_f32_16x16x32_bf16 v[8:11], v[36:39], a[52:55], v[8:11]\n"
  "v_exp_f32 v22, v22\n"
  "v_mfma_f32_16x16x32_bf16 v[8:11], v[40:43], a[56:59], v[8:11]\n"
  "v_exp_f32 v19, v19\n"
  "v_mfma_f32_16x16x32_bf16 v[8:11], v[44:47], a[60:63], v[8:11]\n"
  "v_exp_f32 v23, v23\n"
  "v_mfma_f32_16x16x32_bf16 v[8:11], v[48:51], a[64:67], v[8:11]\n"
  "v_cvt_pk_bf16_f32 v18, v18, v22\n"
  "v_cvt_pk_bf16_f32 v19, v19, v23\n"
  "v_mfma_f32_16x16x32_bf16 v[8:11], v[52:55], a[68:71], v[8:11]\n"
  "ds_write_addtid_b32 v18 offset:9760\n"
  "ds_write_addtid_b32 v19 offset:13856\n"
  "v_mfma_f32_16x16x32_bf16 v[12:15], v[52:55], a[92:95], v[92:95]\n"
  "v_exp_f32 v24, v24\n"
  "v_mfma_f32_16x16x32_bf16 v[12:15], v[48:51], a[88:91], v[12:15]\n"
  "v_exp_f32 v28, v28\n"
  "v_mfma_f32_16x16x32_bf16 v[12:15], v[44:47], a[84:87], v[12:15]\n"
  "v_exp_f32 v25, v25\n"
  "v_mfma_f32_16x16x32_bf16 v[12:15], v[40:43], a[80:83], v[12:15]\n"
  "v_exp_f32 v29, v29\n"
  "v_mfma_f32_16x16x32_bf16 v[12:15], v[36:39], a[76:79], v[12:15]\n"
  "v_cvt_pk_bf16_f32 v24, v24, v28\n"
  "v_cvt_pk_bf16_f32 v25, v25, v29\n"
  "v_mfma_f32_16x16x32_bf16 v[12:15], v[32:35], a[72:75], v[12:15]\n"
  "ds_write_addtid_b32 v24 offset:1792\n"
  "ds_write_addtid_b32 v25 offset:5888\n"
  "v_mfma_f32_16x16x32_bf16 v[16:19], v[32:35], a[96:99], v[92:95]\n"
  "v_exp_f32 v26, v26\n"
  "v_mfma_f32_16x16x32_bf16 v[16:19], v[36:39], a[100:103], v[16:19]\n"
  "v_exp_f32 v30, v30\n"
  "v_mfma_f32_16x16x32_bf16 v[16:19], v[40:43], a[104:107], v[16:19]\n"
  "v_exp_f32 v27, v27\n"
  "v_mfma_f32_16x16x32_bf16 v[16:19], v[44:47], a[108:111], v[16:19]\n"
  "v_exp_f32 v31, v31\n"
  "v_mfma_f32_16x16x32_bf16 v[16:19], v[48:51], a[112:115], v[16:19]\n"
  "v_cvt_pk_bf16_f32 v26, v26, v30\n"
  "v_cvt_pk_bf16_f32 v27, v27, v31\n"
  "v_mfma_f32_16x16x32_bf16 v[16:19], v[52:55], a[116:119], v[16:19]\n"
  "ds_write_addtid_b32 v26 offset:10016\n"
  "ds_write_addtid_b32 v27 offset:14112\n"
  "v_pk_mul_f32 v[0:1], v[0:1], s[60:61] op_sel_hi:[1,0]\n"
  "v_pk_mul_f32 v[2:3], v[2:3], s[60:61] op_sel_hi:[1,0]\n"
  "v_pk_mul_f32 v[4:5], v[4:5], s[60:61] op_sel_hi:[1,0]\n"
  "v_pk_mul_f32 v[6:7], v[6:7], s[60:61] op_sel_hi:[1,0]\n"
  "v_pk_mul_f32 v[8:9], v[8:9], s[60:61] op_sel_hi:[1,0]\n"
  "v_pk_mul_f32 v[10:11], v[10:11], s[60:61] op_sel_hi:[1,0]\n"
  "v_pk_mul_f32 v[12:13], v[12:13], s[60:61] op_sel_hi:[1,0]\n"
  "v_pk_mul_f32 v[14:15], v[14:15], s[60:61] op_sel_hi:[1,0]\n"
  "v_mfma_f32_16x16x32_bf16 v[20:23], v[52:55], a[140:143], v[92:95]\n"
  "s_add_u32 s88, s88, 16448\n"
  "v_exp_f32 v0, v0\n"
  "v_mfma_f32_16x16x32_bf16 v[20:23], v[48:51], a[136:139], v[20:23]\n"
  "s_cmp_eq_u32 s88, s68\n"
  "v_exp_f32 v4, v4\n"
  "v_mfma_f32_16x16x32_bf16 v[20:23], v[44:47], a[132:135], v[20:23]\n"
  "s_cselect_b32 s88, s89, s88\n"
  "v_exp_f32 v1, v1\n"
  "v_mfma_f32_16x16x32_bf16 v[20:23], v[40:43], a[128:131], v[20:23]\n"
  "v_exp_f32_e64 v5, v5\n"
  "v_mfma_f32_16x16x32_bf16 v[20:23], v[36:39], a[124:127], v[20:23]\n"
  "s_mov_b32 m0, s88\n"
  "v_cvt_pk_bf16_f32 v0, v0, v4\n"
  "v_cvt_pk_bf16_f32 v1, v1, v5\n"
  "v_mfma_f32_16x16x32_bf16 v[20:23], v[32:35], a[120:123], v[20:23]\n"
  "ds_write_addtid_b32 v0 offset:0\n"
  "ds_write_addtid_b32 v1 offset:4096\n"
  "v_mfma_f32_16x16x32_bf16 v[24:27], v[32:35], a[144:147], v[92:95]\n"
  "v_exp_f32 v2, v2\n"
  "v_mfma_f32_16x16x32_bf16 v[24:27], v[36:39], a[148:151], v[24:27]\n"
  "v_exp_f32 v6, v6\n"
  "v_mfma_f32_16x16x32_bf16 v[24:27], v[40:43], a[152:155], v[24:27]\n"
  "v_exp_f32 v3, v3\n"
  "v_mfma_f32_16x16x32_bf16 v[24:27], v[44:47], a[156:159], v[24:27]\n"
  "v_exp_f32 v7, v7\n"
  "v_mfma_f32_16x16x32_bf16 v[24:27], v[48:51], a[160:163], v[24:27]\n"
  "v_cvt_pk_bf16_f32 v2, v2, v6\n"
  "v_cvt_pk_bf16_f32 v3, v3, v7\n"
  "v_mfma_f32_16x16x32_bf16 v[24:27], v[52:55], a[164:167], v[24:27]\n"
  "ds_write_addtid_b32 v2 offset:8224\n"
  "ds_write_addtid_b32 v3 offset:12320\n"
  "v_mfma_f32_16x16x32_bf16 v[28:31], v[52:55], a[188:191], v[92:95]\n"
  "v_exp_f32 v8, v8\n"
  "v_mfma_f32_16x16x32_bf16 v[28:31], v[48:51], a[184:187], v[28:31]\n"
  "v_exp_f32 v12, v12\n"
  "v_mfma_f32_16x16x32_bf16 v[28:31], v[44:47], a[180:183], v[28:31]\n"
  "v_exp_f32 v9, v9\n"
  "v_mfma_f32_16x16x32_bf16 v[28:31], v[40:43], a[176:179], v[28:31]\n"
  "v_exp_f32 v13, v13\n"
  "v_mfma_f32_16x16x32_bf16 v[28:31], v[36:39], a[172:175], v[28:31]\n"
  "v_cvt_pk_bf16_f32 v8, v8, v12\n"
  "v_cvt_pk_bf16_f32 v9, v9, v13\n"
  "v_mfma_f32_16x16x32_bf16 v[28:31], v[32:35], a[168:171], v[28:31]\n"
  "ds_write_addtid_b32 v8 offset:256\n"
  "ds_write_addtid_b32 v9 offset:4352\n"
  "v_mfma_f32_16x16x32_bf16 v[0:3], v[32:35], a[192:195], v[92:95]\n"
  "v_exp_f32 v10, v10\n"
  "v_mfma_f32_16x16x32_bf16 v[0:3], v[36:39], a[196:199], v[0:3]\n"
  "v_exp_f32 v14, v14\n"
  "v_mfma_f32_16x16x32_bf16 v[0:3], v[40:43], a[200:203], v[0:3]\n"
  "v_exp_f32 v11, v11\n"
  "v_mfma_f32_16x16x32_bf16 v[0:3], v[44:47], a[204:207], v[0:3]\n"
  "v_exp_f32 v15, v15\n"
  "v_mfma_f32_16x16x32_bf16 v[0:3], v[48:51], a[208:211], v[0:3]\n"
  "v_cvt_pk_bf16_f32 v10, v10, v14\n"
  "v_cvt_pk_bf16_f32 v11, v11, v15\n"
  "v_mfma_f32_16x16x32_bf16 v[0:3], v[52:55], a[212:215], v[0:3]\n"
  "ds_read_b128 v[56:59], v81 offset:0\n"
  "ds_read_b128 v[60:63], v82 offset:0\n"
  "ds_read_b128 v[64:67], v81 offset:2048\n"
  "ds_write_addtid_b32 v10 offset:8480\n"
  "ds_write_addtid_b32 v11 offset:12576\n"
  "v_pk_mul_f32 v[16:17], v[16:17], s[60:61] op_sel_hi:[1,0]\n"
  "v_pk_mul_f32 v[18:19], v[18:19], s[60:61] op_sel_hi:[1,0]\n"
  "v_pk_mul_f32 v[20:21], v[20:21], s[60:61] op_sel_hi:[1,0]\n"
  "v_pk_mul_f32 v[22:23], v[22:23], s[60:61] op_sel_hi:[1,0]\n"
  "v_pk_mul_f32 v[24:25], v[24:25], s[60:61] op_sel_hi:[1,0]\n"
  "v_pk_mul_f32 v[26:27], v[26:27], s[60:61] op_sel_hi:[1,0]\n"
  "v_pk_mul_f32 v[28:29], v[28:29], s[60:61] op_sel_hi:[1,0]\n"
  "v_pk_mul_f32 v[30:31], v[30:31], s[60:61] op_sel_hi:[1,0]\n"
  "v_mfma_f32_16x16x32_bf16 v[4:7], v[52:55], a[236:239], v[92:95]\n"
  "v_exp_f32 v16, v16\n"
  "v_mfma_f32_16x16x32_bf16 v[4:7], v[48:51], a[232:235], v[4:7]\n"
  "v_exp_f32 v20, v20\n"
  "v_mfma_f32_16x16x32_bf16 v[4:7], v[44:47], a[228:231], v[4:7]\n"
  "v_exp_f32 v17, v17\n"
  "v_mfma_f32_16x16x32_bf16 v[4:7], v[40:43], a[224:227], v[4:7]\n"
  "v_exp_f32 v21, v21\n"
  "v_mfma_f32_16x16x32_bf16 v[4:7], v[36:39], a[220:223], v[4:7]\n"
  "v_cvt_pk_bf16_f32 v16, v16, v20\n"
  "v_cvt_pk_bf16_f32 v17, v17, v21\n"
  "v_mfma_f32_16x16x32_bf16 v[4:7], v[32:35], a[216:219], v[4:7]\n"
  "ds_write_addtid_b32 v16 offset:512\n"
  "ds_write_addtid_b32 v17 offset:4608\n"
  "v_mfma_f32_16x16x32_bf16 v[8:11], v[32:35], a[240:243], v[92:95]\n"
  "v_exp_f32 v18, v18\n"
  "v_mfma_f32_16x16x32_bf16 v[8:11], v[36:39], a[244:247], v[8:11]\n"
  "v_exp_f32 v22, v22\n"
  "v_mfma_f32_16x16x32_bf16 v[8:11], v[40:43], a[248:251], v[8:11]\n"
  "v_exp_f32 v19, v19\n"
  "v_mfma_f32_16x16x32_bf16 v[8:11], v[44:47], a[252:255], v[8:11]\n"
  "v_exp_f32 v23, v23\n"
  "v_mfma_f32_16x16x32_bf16 v[8:11], v[48:51], v[128:131], v[8:11]\n"
  "v_cvt_pk_bf16_f32 v18, v18, v22\n"
  "v_cvt_pk_bf16_f32 v19, v19, v23\n"
  "v_mfma_f32_16x16x32_bf16 v[8:11], v[52:55], v[132:135], v[8:11]\n"
  "ds_write_addtid_b32 v18 offset:8736\n"
  "ds_write_addtid_b32 v19 offset:12832\n"
  "v_mfma_f32_16x16x32_bf16 v[12:15], v[52:55], v[156:159], v[92:95]\n"
  "v_exp_f32 v24, v24\n"
  "v_mfma_f32_16x16x32_bf16 v[12:15], v[48:51], v[152:155], v[12:15]\n"
  "v_exp_f32 v28, v28\n"
  "v_mfma_f32_16x16x32_bf16 v[12:15], v[44:47], v[148:151], v[12:15]\n"
  "v_exp_f32 v25, v25\n"
  "v_mfma_f32_16x16x32_bf16 v[12:15], v[40:43], v[144:147], v[12:15]\n"
  "v_exp_f32 v29, v29\n"
  "v_mfma_f32_16x16x32_bf16 v[12:15], v[36:39], v[140:143], v[12:15]\n"
  "v_cvt_pk_bf16_f32 v24, v24, v28\n"
  "v_cvt_pk_bf16_f32 v25, v25, v29\n"
  "v_mfma_f32_16x16x32_bf16 v[12:15], v[32:35], v[136:139], v[12:15]\n"
  "ds_write_addtid_b32 v24 offset:768\n"
  "ds_write_addtid_b32 v25 offset:4864\n"
  "v_mfma_f32_16x16x32_bf16 v[16:19], v[32:35], v[160:163], v[92:95]\n"
  "v_exp_f32 v26, v26\n"
  "v_mfma_f32_16x16x32_bf16 v[16:19], v[36:39], v[164:167], v[16:19]\n"
  "v_exp_f32 v30, v30\n"
  "v_mfma_f32_16x16x32_bf16 v[16:19], v[40:43], v[168:171], v[16:19]\n"
  "v_exp_f32 v27, v27\n"
  "v_mfma_f32_16x16x32_bf16 v[16:19], v[44:47], v[172:175], v[16:19]\n"
  "v_exp_f32 v31, v31\n"
  "v_mfma_f32_16x16x32_bf16 v[16:19], v[48:51], v[176:179], v[16:19]\n"
  "v_cvt_pk_bf16_f32 v26, v26, v30\n"
  "v_cvt_pk_bf16_f32 v27, v27, v31\n"
  "v_mfma_f32_16x16x32_bf16 v[16:19], v[52:55], v[180:183], v[16:19]\n"
  "ds_read_b128 v[68:71], v82 offset:2048\n"
  "ds_read_b128 v[72:75], v81 offset:4096\n"
  "ds_read_b128 v[76:79], v82 offset:4096\n"
  "ds_write_addtid_b32 v26 offset:8992\n"
  "ds_write_addtid_b32 v27 offset:13088\n"
  "v_pk_mul_f32 v[0:1], v[0:1], s[60:61] op_sel_hi:[1,0]\n"
  "v_pk_mul_f32 v[2:3], v[2:3], s[60:61] op_sel_hi:[1,0]\n"
  "v_pk_mul_f32 v[4:5], v[4:5], s[60:61] op_sel_hi:[1,0]\n"
  "v_pk_mul_f32 v[6:7], v[6:7], s[60:61] op_sel_hi:[1,0]\n"
  "v_pk_mul_f32 v[8:9], v[8:9], s[60:61] op_sel_hi:[1,0]\n"
  "v_pk_mul_f32 v[10:11], v[10:11], s[60:61] op_sel_hi:[1,0]\n"
  "v_pk_mul_f32 v[12:13], v[12:13], s[60:61] op_sel_hi:[1,0]\n"
  "v_pk_mul_f32 v[14:15], v[14:15], s[60:61] op_sel_hi:[1,0]\n"
  "v_mfma_f32_16x16x32_bf16 v[20:23], v[52:55], v[204:207], v[92:95]\n"
  "v_exp_f32 v0, v0\n"
  "v_mfma_f32_16x16x32_bf16 v[20:23], v[48:51], v[200:203], v[20:23]\n"
  "v_exp_f32 v4, v4\n"
  "v_mfma_f32_16x16x32_bf16 v[20:23], v[44:47], v[196:199], v[20:23]\n"
  "v_exp_f32 v1, v1\n"
  "v_mfma_f32_16x16x32_bf16 v[20:23], v[40:43], v[192:195], v[20:23]\n"
  "v_exp_f32 v5, v5\n"
  "v_mfma_f32_16x16x32_bf16 v[20:23], v[36:39], v[188:191], v[20:23]\n"
  "v_cvt_pk_bf16_f32 v0, v0, v4\n"
  "v_cvt_pk_bf16_f32 v1, v1, v5\n"
  "v_mfma_f32_16x16x32_bf16 v[20:23], v[32:35], v[184:187], v[20:23]\n"
  "ds_write_addtid_b32 v0 offset:1024\n"
  "ds_write_addtid_b32 v1 offset:5120\n"
  "v_mfma_f32_16x16x32_bf16 v[24:27], v[32:35], v[208:211], v[92:95]\n"
  "v_exp_f32 v2, v2\n"
  "v_mfma_f32_16x16x32_bf16 v[24:27], v[36:39], v[212:215], v[24:27]\n"
  "v_exp_f32 v6, v6\n"
  "v_mfma_f32_16x16x32_bf16 v[24:27], v[40:43], v[216:219], v[24:27]\n"
  "v_exp_f32 v3, v3\n"
  "v_mfma_f32_16x16x32_bf16 v[24:27], v[44:47], v[220:223], v[24:27]\n"
  "v_exp_f32 v7, v7\n"
  "v_mfma_f32_16x16x32_bf16 v[24:27], v[48:51], v[224:227], v[24:27]\n"
  "v_cvt_pk_bf16_f32 v2, v2, v6\n"
  "v_cvt_pk_bf16_f32 v3, v3, v7\n"
  "v_mfma_f32_16x16x32_bf16 v[24:27], v[52:55], v[228:231], v[24:27]\n"
  "ds_write_addtid_b32 v2 offset:9248\n"
  "ds_write_addtid_b32 v3 offset:13344\n"
  "v_mfma_f32_16x16x32_bf16 v[28:31], v[52:55], v[252:255], v[92:95]\n"
  "v_exp_f32 v8, v8\n"
  "v_mfma_f32_16x16x32_bf16 v[28:31], v[48:51], v[248:251], v[28:31]\n"
  "v_exp_f32 v12, v12\n"
  "v_mfma_f32_16x16x32_bf16 v[28:31], v[44:47], v[244:247], v[28:31]\n"
  "v_exp_f32 v9, v9\n"
  "v_mfma_f32_16x16x32_bf16 v[28:31], v[40:43], v[240:243], v[28:31]\n"
  "s_sub_u32 s56, s56, 1\n"
  "v_exp_f32_e64 v13, v13\n"
  "v_mfma_f32_16x16x32_bf16 v[28:31], v[36:39], v[236:239], v[28:31]\n"
  "v_cvt_pk_bf16_f32 v8, v8, v12\n"
  "v_cvt_pk_bf16_f32 v9, v9, v13\n"
  "v_mfma_f32_16x16x32_bf16 v[28:31], v[32:35], v[232:235], v[28:31]\n"
  "s_waitcnt lgkmcnt(6)\n"
  "ds_write_addtid_b32 v8 offset:1280\n"
  "ds_write_addtid_b32 v9 offset:5376\n"
  "s_branch A_H1_%=\n"
  ".p2align 3\n"
  "A_X0_%=:\n"
  "s_mov_b32 s52, s53\n"
  "s_mov_b32 s53, s54\n"
  "s_mov_b32 s54, s55\n"
  "s_cmp_eq_u32 s52, 0\n"
  "s_cbranch_scc1 A_LAST_%=\n"
  "s_lshr_b32 s62, s52, 16\n"
  "s_lshl1_add_u32 s62, s62, s65\n"
  "s_mul_i32 s57, s62, 98304\n"
  "s_and_b32 s56, s52, 0xff\n"
  "s_cmp_lg_u32 s53, 0\n"
  "s_cselect_b32 s62, 1, 0\n"
  "s_sub_u32 s56, s56, s62\n"
  "s_add_u32 s57, s57, 12672\n"
  "v_mfma_f32_16x16x32_bf16 v[0:3], v[32:35], a[0:3], v[92:95]\n"
  "s_add_u32 s66, s66, 6144\n"
  "s_cmp_eq_u32 s66, 102400\n"
  "s_cselect_b32 s66, 65536, s66\n"
  "v_exp_f32 v10, v10\n"
  "v_mfma_f32_16x16x32_bf16 v[0:3], v[36:39], a[4:7], v[0:3]\n"
  "v_exp_f32 v14, v14\n"
  "v_mfma_f32_16x16x32_bf16 v[0:3], v[40:43], a[8:11], v[0:3]\n"
  "ds_read_b128 a[0:3], v87 offset:0\n"
  "ds_read_b128 a[4:7], v88 offset:0\n"
  "v_exp_f32 v11, v11\n"
  "v_mfma_f32_16x16x32_bf16 v[0:3], v[44:47], a[12:15], v[0:3]\n"
  "v_exp_f32 v15, v15\n"
  "v_mfma_f32_16x16x32_bf16 v[0:3], v[48:51], a[16:19], v[0:3]\n"
  "ds_read_b128 a[8:11], v87 offset:2048\n"
  "ds_read_b128 a[12:15], v88 offset:2048\n"
  "v_cvt_pk_bf16_f32 v10, v10, v14\n"
  "v_cvt_pk_bf16_f32 v11, v11, v15\n"
  "v_mfma_f32_16x16x32_bf16 v[0:3], v[52:55], a[20:23], v[0:3]\n"
  "ds_write_addtid_b32 v10 offset:9504\n"
  "ds_write_addtid_b32 v11 offset:13600\n"
  "v_add_u32 v81, s66, v83\n"
  "v_add_u32 v82, s66, v84\n"
  "v_pk_mul_f32 v[16:17], v[16:17], s[60:61] op_sel_hi:[1,0]\n"
  "v_pk_mul_f32 v[18:19], v[18:19], s[60:61] op_sel_hi:[1,0]\n"
  "v_pk_mul_f32 v[20:21], v[20:21], s[60:61] op_sel_hi:[1,0]\n"
  "v_pk_mul_f32 v[22:23], v[22:23], s[60:61] op_sel_hi:[1,0]\n"
  "v_pk_mul_f32 v[24:25], v[24:25], s[60:61] op_sel_hi:[1,0]\n"
  "v_pk_mul_f32 v[26:27], v[26:27], s[60:61] op_sel_hi:[1,0]\n"
  "v_pk_mul_f32 v[28:29], v[28:29], s[60:61] op_sel_hi:[1,0]\n"
  "v_pk_mul_f32 v[30:31], v[30:31], s[60:61] op_sel_hi:[1,0]\n"
  "v_mfma_f32_16x16x32_bf16 v[4:7], v[52:55], a[44:47], v[92:95]\n"
  "s_add_u32 m0, s59, -128\n"
  "v_exp_f32 v16, v16\n"
  "v_mfma_f32_16x16x32_bf16 v[4:7], v[48:51], a[40:43], v[4:7]\n"
  "s_waitcnt lgkmcnt(4)\n"
  "buffer_load_dwordx4 v112, s[40:43], s57 offen offset:128 nt lds\n"
  "buffer_load_dwordx4 v113, s[40:43], s57 offen offset:1152 nt lds\n"
  "v_exp_f32 v20, v20\n"
  "v_mfma_f32_16x16x32_bf16 v[4:7], v[44:47], a[36:39], v[4:7]\n"
  "s_add_u32 m0, s59, 2048\n"
  "ds_read_b128 a[16:19], v87 offset:4096\n"
  "ds_read_b128 a[20:23], v88 offset:4096\n"
  "v_exp_f32 v17, v17\n"
  "v_mfma_f32_16x16x32_bf16 v[4:7], v[40:43], a[32:35], v[4:7]\n"
  "s_waitcnt lgkmcnt(4)\n"
  "buffer_load_dwordx4 v112, s[40:43], s57 offen offset:0 nt lds\n"
  "buffer_load_dwordx4 v113, s[40:43], s57 offen offset:1024 nt lds\n"
  "s_add_u32 s57, s57, 11904\n"
  "v_exp_f32_e64 v21, v21\n"
  "v_mfma_f32_16x16x32_bf16 v[4:7], v[36:39], a[28:31], v[4:7]\n"
  "s_mov_b32 m0, s88\n"
  "ds_read_b128 a[40:43], v87 offset:6144\n"
  "ds_read_b128 a[44:47], v88 offset:6144\n"
  "v_cvt_pk_bf16_f32 v16, v16, v20\n"
  "v_cvt_pk_bf16_f32 v17, v17, v21\n"
  "v_mfma_f32_16x16x32_bf16 v[4:7], v[32:35], a[24:27], v[4:7]\n"
  "ds_write_addtid_b32 v16 offset:1536\n"
  "ds_write_addtid_b32 v17 offset:5632\n"
  "v_mfma_f32_16x16x32_bf16 v[8:11], v[32:35], a[48:51], v[92:95]\n"
  "s_add_u32 m0, s59, 4096\n"
  "v_exp_f32 v18, v18\n"
  "v_mfma_f32_16x16x32_bf16 v[8:11], v[36:39], a[52:55], v[8:11]\n"
  "s_waitcnt lgkmcnt(4)\n"
  "buffer_load_dwordx4 v112, s[40:43], s57 offen offset:0 nt lds\n"
  "buffer_load_dwordx4 v113, s[40:43], s57 offen offset:1024 nt lds\n"
  "v_exp_f32 v22, v22\n"
  "v_mfma_f32_16x16x32_bf16 v[8:11], v[40:43], a[56:59], v[8:11]\n"
  "ds_read_b128 a[32:35], v87 offset:8192\n"
  "ds_read_b128 a[36:39], v88 offset:8192\n"
  "v_exp_f32 v19, v19\n"
  "v_mfma_f32_16x16x32_bf16 v[8:11], v[44:47], a[60:63], v[8:11]\n"
  "v_exp_f32 v23, v23\n"
  "v_mfma_f32_16x16x32_bf16 v[8:11], v[48:51], a[64:67], v[8:11]\n"
  "s_mov_b32 m0, s88\n"
  "v_cvt_pk_bf16_f32 v18, v18, v22\n"
  "v_cvt_pk_bf16_f32 v19, v19, v23\n"
  "v_mfma_f32_16x16x32_bf16 v[8:11], v[52:55], a[68:71], v[8:11]\n"
  "s_barrier\n"
  "ds_write_addtid_b32 v18 offset:9760\n"
  "ds_write_addtid_b32 v19 offset:13856\n"
  "v_mfma_f32_16x16x32_bf16 v[12:15], v[52:55], a[92:95], v[92:95]\n"
  "s_add_u32 m0, s59, 6016\n"
  "v_exp_f32 v24, v24\n"
  "v_mfma_f32_16x16x32_bf16 v[12:15], v[48:51], a[88:91], v[12:15]\n"
  "s_waitcnt lgkmcnt(6)\n"
  "buffer_load_dwordx4 v112, s[40:43], s57 offen offset:128 nt lds\n"
  "buffer_load_dwordx4 v113, s[40:43], s57 offen offset:1152 nt lds\n"
  "v_exp_f32 v28, v28\n"
  "v_mfma_f32_16x16x32_bf16 v[12:15], v[44:47], a[84:87], v[12:15]\n"
  "s_add_u32 m0, s59, 7936\n"
  "ds_read_b128 a[24:27], v87 offset:10240\n"
  "ds_read_b128 a[28:31], v88 offset:10240\n"
  "v_exp_f32 v25, v25\n"
  "v_mfma_f32_16x16x32_bf16 v[12:15], v[40:43], a[80:83], v[12:15]\n"
  "s_waitcnt lgkmcnt(4)\n"
  "buffer_load_dwordx4 v112, s[40:43], s57 offen offset:256 nt lds\n"
  "buffer_load_dwordx4 v113, s[40:43], s57 offen offset:1280 nt lds\n"
  "s_add_u32 s57, s57, 384\n"
  "v_exp_f32 v29, v29\n"
  "v_mfma_f32_16x16x32_bf16 v[12:15], v[36:39], a[76:79], v[12:15]\n"
  "s_mov_b32 m0, s88\n"
  "ds_read_b128 a[48:51], v87 offset:12288\n"
  "ds_read_b128 a[52:55], v88 offset:12288\n"
  "v_cvt_pk_bf16_f32 v24, v24, v28\n"
  "v_cvt_pk_bf16_f32 v25, v25, v29\n"
  "v_mfma_f32_16x16x32_bf16 v[12:15], v[32:35], a[72:75], v[12:15]\n"
  "ds_write_addtid_b32 v24 offset:1792\n"
  "ds_write_addtid_b32 v25 offset:5888\n"
  "v_mfma_f32_16x16x32_bf16 v[16:19], v[32:35], a[96:99], v[92:95]\n"
  "s_add_u32 m0, s59, 9984\n"
  "v_exp_f32 v26, v26\n"
  "v_mfma_f32_16x16x32_bf16 v[16:19], v[36:39], a[100:103], v[16:19]\n"
  "s_waitcnt lgkmcnt(4)\n"
  "buffer_load_dwordx4 v112, s[40:43], s57 offen offset:256 nt lds\n"
  "buffer_load_dwordx4 v113, s[40:43], s57 offen offset:1280 nt lds\n"
  "v_exp_f32 v30, v30\n"
  "v_mfma_f32_16x16x32_bf16 v[16:19], v[40:43], a[104:107], v[16:19]\n"
  "s_add_u32 m0, s59, 12160\n"
  "ds_read_b128 a[56:59], v87 offset:14336\n"
  "ds_read_b128 a[60:63], v88 offset:14336\n"
  "s_waitcnt vmcnt(10)\n"
  "ds_read_b128 a[80:83], v87 offset:0\n"
  "ds_read_b128 a[84:87], v88 offset:0\n"
  "v_exp_f32 v27, v27\n"
  "v_mfma_f32_16x16x32_bf16 v[16:19], v[44:47], a[108:111], v[16:19]\n"
  "s_waitcnt lgkmcnt(6)\n"
  "buffer_load_dwordx4 v112, s[40:43], s57 offen offset:128 nt lds\n"
  "buffer_load_dwordx4 v113, s[40:43], s57 offen offset:1152 nt lds\n"
  "v_exp_f32_e64 v31, v31\n"
  "v_mfma_f32_16x16x32_bf16 v[16:19], v[48:51], a[112:115], v[16:19]\n"
  "s_mov_b32 m0, s88\n"
  "ds_read_b128 a[64:67], v87 offset:16384\n"
  "ds_read_b128 a[68:71], v88 offset:16384\n"
  "s_waitcnt vmcnt(10)\n"
  "ds_read_b128 a[72:75], v87 offset:2048\n"
  "ds_read_b128 a[76:79], v88 offset:2048\n"
  "v_cvt_pk_bf16_f32 v26, v26, v30\n"
  "v_cvt_pk_bf16_f32 v27, v27, v31\n"
  "v_mfma_f32_16x16x32_bf16 v[16:19], v[52:55], a[116:119], v[16:19]\n"
  "ds_write_addtid_b32 v26 offset:10016\n"
  "ds_write_addtid_b32 v27 offset:14112\n"
  "v_pk_mul_f32 v[0:1], v[0:1], s[60:61] op_sel_hi:[1,0]\n"
  "v_pk_mul_f32 v[2:3], v[2:3], s[60:61] op_sel_hi:[1,0]\n"
  "v_pk_mul_f32 v[4:5], v[4:5], s[60:61] op_sel_hi:[1,0]\n"
  "v_pk_mul_f32 v[6:7], v[6:7], s[60:61] op_sel_hi:[1,0]\n"
  "v_pk_mul_f32 v[8:9], v[8:9], s[60:61] op_sel_hi:[1,0]\n"
  "v_pk_mul_f32 v[10:11], v[10:11], s[60:61] op_sel_hi:[1,0]\n"
  "v_pk_mul_f32 v[12:13], v[12:13], s[60:61] op_sel_hi:[1,0]\n"
  "v_pk_mul_f32 v[14:15], v[14:15], s[60:61] op_sel_hi:[1,0]\n"
  "v_mfma_f32_16x16x32_bf16 v[20:23], v[52:55], a[140:143], v[92:95]\n"
  "s_add_u32 m0, s59, 14336\n"
  "v_exp_f32 v0, v0\n"
  "v_mfma_f32_16x16x32_bf16 v[20:23], v[48:51], a[136:139], v[20:23]\n"
  "s_waitcnt lgkmcnt(8)\n"
  "buffer_load_dwordx4 v112, s[40:43], s57 offen offset:0 nt lds\n"
  "buffer_load_dwordx4 v113, s[40:43], s57 offen offset:1024 nt lds\n"
  "s_add_u32 s57, s57, 11904\n"
  "v_exp_f32 v4, v4\n"
  "v_mfma_f32_16x16x32_bf16 v[20:23], v[44:47], a[132:135], v[20:23]\n"
  "s_add_u32 m0, s59, 16384\n"
  "ds_read_b128 a[88:91], v87 offset:18432\n"
  "ds_read_b128 a[92:95], v88 offset:18432\n"
  "s_waitcnt vmcnt(10)\n"
  "ds_read_b128 a[96:99], v87 offset:4096\n"
  "ds_read_b128 a[100:103], v88 offset:4096\n"
  "v_exp_f32 v1, v1\n"
  "v_mfma_f32_16x16x32_bf16 v[20:23], v[40:43], a[128:131], v[20:23]\n"
  "s_waitcnt lgkmcnt(8)\n"
  "buffer_load_dwordx4 v112, s[40:43], s57 offen offset:0 nt lds\n"
  "buffer_load_dwordx4 v113, s[40:43], s57 offen offset:1024 nt lds\n"
  "v_exp_f32 v5, v5\n"
  "v_mfma_f32_16x16x32_bf16 v[20:23], v[36:39], a[124:127], v[20:23]\n"
  "s_add_u32 s88, s88, 16448\n"
  "s_cmp_eq_u32 s88, s68\n"
  "s_cselect_b32 s88, s89, s88\n"
  "s_mov_b32 m0, s88\n"
  "s_mov_b32 m0, s88\n"
  "s_waitcnt vmcnt(10)\n"
  "ds_read_b128 a[104:107], v87 offset:6144\n"
  "ds_read_b128 a[108:111], v88 offset:6144\n"
  "v_cvt_pk_bf16_f32 v0, v0, v4\n"
  "v_cvt_pk_bf16_f32 v1, v1, v5\n"
  "v_mfma_f32_16x16x32_bf16 v[20:23], v[32:35], a[120:123], v[20:23]\n"
  "ds_write_addtid_b32 v0 offset:0\n"
  "ds_write_addtid_b32 v1 offset:4096\n"
  "v_mfma_f32_16x16x32_bf16 v[24:27], v[32:35], a[144:147], v[92:95]\n"
  "s_add_u32 m0, s59, 18304\n"
  "v_exp_f32 v2, v2\n"
  "v_mfma_f32_16x16x32_bf16 v[24:27], v[36:39], a[148:151], v[24:27]\n"
  "s_waitcnt lgkmcnt(6)\n"
  "buffer_load_dwordx4 v112, s[40:43], s57 offen offset:128 nt lds\n"
  "buffer_load_dwordx4 v113, s[40:43], s57 offen offset:1152 nt lds\n"
  "v_exp_f32 v6, v6\n"
  "v_mfma_f32_16x16x32_bf16 v[24:27], v[40:43], a[152:155], v[24:27]\n"
  "s_waitcnt vmcnt(10)\n"
  "ds_read_b128 a[112:115], v87 offset:8192\n"
  "ds_read_b128 a[116:119], v88 offset:8192\n"
  "v_exp_f32 v3, v3\n"
  "v_mfma_f32_16x16x32_bf16 v[24:27], v[44:47], a[156:159], v[24:27]\n"
  "v_exp_f32_e64 v7, v7\n"
  "v_mfma_f32_16x16x32_bf16 v[24:27], v[48:51], a[160:163], v[24:27]\n"
  "s_mov_b32 m0, s88\n"
  "v_cvt_pk_bf16_f32 v2, v2, v6\n"
  "v_cvt_pk_bf16_f32 v3, v3, v7\n"
  "v_mfma_f32_16x16x32_bf16 v[24:27], v[52:55], a[164:167], v[24:27]\n"
  "ds_write_addtid_b32 v2 offset:8224\n"
  "ds_write_addtid_b32 v3 offset:12320\n"
  "v_mfma_f32_16x16x32_bf16 v[28:31], v[52:55], a[188:191], v[92:95]\n"
  "s_add_u32 m0, s59, -256\n"
  "v_exp_f32 v8, v8\n"
  "v_mfma_f32_16x16x32_bf16 v[28:31], v[48:51], a[184:187], v[28:31]\n"
  "buffer_load_dwordx4 v112, s[40:43], s57 offen offset:256 nt lds\n"
  "buffer_load_dwordx4 v113, s[40:43], s57 offen offset:1280 nt lds\n"
  "s_add_u32 s57, s57, 384\n"
  "v_exp_f32 v12, v12\n"
  "v_mfma_f32_16x16x32_bf16 v[28:31], v[44:47], a[180:183], v[28:31]\n"
  "s_add_u32 m0, s59, 1792\n"
  "s_waitcnt vmcnt(10)\n"
  "ds_read_b128 a[136:139], v87 offset:10240\n"
  "ds_read_b128 a[140:143], v88 offset:10240\n"
  "v_exp_f32 v9, v9\n"
  "v_mfma_f32_16x16x32_bf16 v[28:31], v[40:43], a[176:179], v[28:31]\n"
  "buffer_load_dwordx4 v112, s[40:43], s57 offen offset:256 nt lds\n"
  "buffer_load_dwordx4 v113, s[40:43], s57 offen offset:1280 nt lds\n"
  "v_exp_f32_e64 v13, v13\n"
  "v_mfma_f32_16x16x32_bf16 v[28:31], v[36:39], a[172:175], v[28:31]\n"
  "s_mov_b32 m0, s88\n"
  "s_waitcnt vmcnt(10)\n"
  "ds_read_b128 a[128:131], v87 offset:12288\n"
  "ds_read_b128 a[132:135], v88 offset:12288\n"
  "v_cvt_pk_bf16_f32 v8, v8, v12\n"
  "v_cvt_pk_bf16_f32 v9, v9, v13\n"
  "v_mfma_f32_16x16x32_bf16 v[28:31], v[32:35], a[168:171], v[28:31]\n"
  "ds_write_addtid_b32 v8 offset:256\n"
  "ds_write_addtid_b32 v9 offset:4352\n"
  "v_mfma_f32_16x16x32_bf16 v[0:3], v[32:35], a[192:195], v[92:95]\n"
  "s_add_u32 m0, s59, 3968\n"
  "v_exp_f32 v10, v10\n"
  "v_mfma_f32_16x16x32_bf16 v[0:3], v[36:39], a[196:199], v[0:3]\n"
  "s_waitcnt lgkmcnt(14)\n"
  "buffer_load_dwordx4 v112, s[40:43], s57 offen offset:128 nt lds\n"
  "buffer_load_dwordx4 v113, s[40:43], s57 offen offset:1152 nt lds\n"
  "v_exp_f32 v14, v14\n"
  "v_mfma_f32_16x16x32_bf16 v[0:3], v[40:43], a[200:203], v[0:3]\n"
  "s_add_u32 m0, s59, 6144\n"
  "s_waitcnt vmcnt(10)\n"
  "ds_read_b128 a[120:123], v87 offset:14336\n"
  "ds_read_b128 a[124:127], v88 offset:14336\n"
  "v_exp_f32 v11, v11\n"
  "v_mfma_f32_16x16x32_bf16 v[0:3], v[44:47], a[204:207], v[0:3]\n"
  "s_waitcnt lgkmcnt(14)\n"
  "buffer_load_dwordx4 v112, s[40:43], s57 offen offset:0 nt lds\n"
  "buffer_load_dwordx4 v113, s[40:43], s57 offen offset:1024 nt lds\n"
  "s_add_u32 s57, s57, 11904\n"
  "v_exp_f32_e64 v15, v15\n"
  "v_mfma_f32_16x16x32_bf16 v[0:3], v[48:51], a[208:211], v[0:3]\n"
  "s_mov_b32 m0, s88\n"
  "s_waitcnt vmcnt(10)\n"
  "ds_read_b128 a[144:147], v87 offset:16384\n"
  "ds_read_b128 a[148:151], v88 offset:16384\n"
  "v_cvt_pk_bf16_f32 v10, v10, v14\n"
  "v_cvt_pk_bf16_f32 v11, v11, v15\n"
  "v_mfma_f32_16x16x32_bf16 v[0:3], v[52:55], a[212:215], v[0:3]\n"
  "ds_read_b128 v[56:59], v81 offset:0\n"
  "ds_read_b128 v[60:63], v82 offset:0\n"
  "ds_read_b128 v[64:67], v81 offset:2048\n"
  "ds_write_addtid_b32 v10 offset:8480\n"
  "ds_write_addtid_b32 v11 offset:12576\n"
  "v_pk_mul_f32 v[16:17], v[16:17], s[60:61] op_sel_hi:[1,0]\n"
  "v_pk_mul_f32 v[18:19], v[18:19], s[60:61] op_sel_hi:[1,0]\n"
  "v_pk_mul_f32 v[20:21], v[20:21], s[60:61] op_sel_hi:[1,0]\n"
  "v_pk_mul_f32 v[22:23], v[22:23], s[60:61] op_sel_hi:[1,0]\n"
  "v_pk_mul_f32 v[24:25], v[24:25], s[60:61] op_sel_hi:[1,0]\n"
  "v_pk_mul_f32 v[26:27], v[26:27], s[60:61] op_sel_hi:[1,0]\n"
  "v_pk_mul_f32 v[28:29], v[28:29], s[60:61] op_sel_hi:[1,0]\n"
  "v_pk_mul_f32 v[30:31], v[30:31], s[60:61] op_sel_hi:[1,0]\n"
  "v_mfma_f32_16x16x32_bf16 v[4:7], v[52:55], a[236:239], v[92:95]\n"
  "s_add_u32 m0, s59, 8192\n"
  "v_exp_f32 v16, v16\n"
  "v_mfma_f32_16x16x32_bf16 v[4:7], v[48:51], a[232:235], v[4:7]\n"
  "s_waitcnt lgkmcnt(15)\n"
  "buffer_load_dwordx4 v112, s[40:43], s57 offen offset:0 nt lds\n"
  "buffer_load_dwordx4 v113, s[40:43], s57 offen offset:1024 nt lds\n"
  "v_exp_f32 v20, v20\n"
  "v_mfma_f32_16x16x32_bf16 v[4:7], v[44:47], a[228:231], v[4:7]\n"
  "s_add_u32 m0, s59, 10112\n"
  "s_waitcnt vmcnt(10)\n"
  "ds_read_b128 a[152:155], v87 offset:18432\n"
  "ds_read_b128 a[156:159], v88 offset:18432\n"
  "v_exp_f32 v17, v17\n"
  "v_mfma_f32_16x16x32_bf16 v[4:7], v[40:43], a[224:227], v[4:7]\n"
  "s_waitcnt lgkmcnt(15)\n"
  "buffer_load_dwordx4 v112, s[40:43], s57 offen offset:128 nt lds\n"
  "buffer_load_dwordx4 v113, s[40:43], s57 offen offset:1152 nt lds\n"
  "v_exp_f32_e64 v21, v21\n"
  "v_mfma_f32_16x16x32_bf16 v[4:7], v[36:39], a[220:223], v[4:7]\n"
  "s_mov_b32 m0, s88\n"
  "s_waitcnt vmcnt(10)\n"
  "ds_read_b128 a[160:163], v87 offset:0\n"
  "ds_read_b128 a[164:167], v88 offset:0\n"
  "v_cvt_pk_bf16_f32 v16, v16, v20\n"
  "v_cvt_pk_bf16_f32 v17, v17, v21\n"
  "v_mfma_f32_16x16x32_bf16 v[4:7], v[32:35], a[216:219], v[4:7]\n"
  "ds_write_addtid_b32 v16 offset:512\n"
  "ds_write_addtid_b32 v17 offset:4608\n"
  "v_mfma_f32_16x16x32_bf16 v[8:11], v[32:35], a[240:243], v[92:95]\n"
  "s_add_u32 m0, s59, 12032\n"
  "v_exp_f32 v18, v18\n"
  "v_mfma_f32_16x16x32_bf16 v[8:11], v[36:39], a[244:247], v[8:11]\n"
  "s_waitcnt lgkmcnt(15)\n"
  "buffer_load_dwordx4 v112, s[40:43], s57 offen offset:256 nt lds\n"
  "buffer_load_dwordx4 v113, s[40:43], s57 offen offset:1280 nt lds\n"
  "s_add_u32 s57, s57, 384\n"
  "v_exp_f32 v22, v22\n"
  "v_mfma_f32_16x16x32_bf16 v[8:11], v[40:43], a[248:251], v[8:11]\n"
  "s_add_u32 m0, s59, 14080\n"
  "s_waitcnt vmcnt(10)\n"
  "ds_read_b128 a[184:187], v87 offset:2048\n"
  "ds_read_b128 a[188:191], v88 offset:2048\n"
  "v_exp_f32 v19, v19\n"
  "v_mfma_f32_16x16x32_bf16 v[8:11], v[44:47], a[252:255], v[8:11]\n"
  "s_waitcnt lgkmcnt(15)\n"
  "buffer_load_dwordx4 v112, s[40:43], s57 offen offset:256 nt lds\n"
  "buffer_load_dwordx4 v113, s[40:43], s57 offen offset:1280 nt lds\n"
  "v_exp_f32_e64 v23, v23\n"
  "v_mfma_f32_16x16x32_bf16 v[8:11], v[48:51], v[128:131], v[8:11]\n"
  "s_mov_b32 m0, s88\n"
  "s_waitcnt vmcnt(10)\n"
  "ds_read_b128 a[176:179], v87 offset:4096\n"
  "ds_read_b128 a[180:183], v88 offset:4096\n"
  "v_cvt_pk_bf16_f32 v18, v18, v22\n"
  "v_cvt_pk_bf16_f32 v19, v19, v23\n"
  "v_mfma_f32_16x16x32_bf16 v[8:11], v[52:55], v[132:135], v[8:11]\n"
  "ds_write_addtid_b32 v18 offset:8736\n"
  "ds_write_addtid_b32 v19 offset:12832\n"
  "v_mfma_f32_16x16x32_bf16 v[12:15], v[52:55], v[156:159], v[92:95]\n"
  "v_exp_f32 v24, v24\n"
  "v_mfma_f32_16x16x32_bf16 v[12:15], v[48:51], v[152:155], v[12:15]\n"
  "v_exp_f32 v28, v28\n"
  "v_mfma_f32_16x16x32_bf16 v[12:15], v[44:47], v[148:151], v[12:15]\n"
  "s_add_u32 m0, s59, 16256\n"
  "v_exp_f32 v25, v25\n"
  "v_mfma_f32_16x16x32_bf16 v[12:15], v[40:43], v[144:147], v[12:15]\n"
  "s_waitcnt lgkmcnt(15)\n"
  "buffer_load_dwordx4 v112, s[40:43], s57 offen offset:128 nt lds\n"
  "buffer_load_dwordx4 v113, s[40:43], s57 offen offset:1152 nt lds\n"
  "v_exp_f32_e64 v29, v29\n"
  "v_mfma_f32_16x16x32_bf16 v[12:15], v[36:39], v[140:143], v[12:15]\n"
  "s_mov_b32 m0, s88\n"
  "s_waitcnt vmcnt(10)\n"
  "ds_read_b128 a[168:171], v87 offset:6144\n"
  "ds_read_b128 a[172:175], v88 offset:6144\n"
  "v_cvt_pk_bf16_f32 v24, v24, v28\n"
  "v_cvt_pk_bf16_f32 v25, v25, v29\n"
  "v_mfma_f32_16x16x32_bf16 v[12:15], v[32:35], v[136:139], v[12:15]\n"
  "ds_write_addtid_b32 v24 offset:768\n"
  "ds_write_addtid_b32 v25 offset:4864\n"
  "v_mfma_f32_16x16x32_bf16 v[16:19], v[32:35], v[160:163], v[92:95]\n"
  "s_add_u32 m0, s59, 18432\n"
  "v_exp_f32 v26, v26\n"
  "v_mfma_f32_16x16x32_bf16 v[16:19], v[36:39], v[164:167], v[16:19]\n"
  "s_waitcnt lgkmcnt(14)\n"
  "buffer_load_dwordx4 v112, s[40:43], s57 offen offset:0 nt lds\n"
  "buffer_load_dwordx4 v113, s[40:43], s57 offen offset:1024 nt lds\n"
  "s_add_u32 s57, s57, 11904\n"
  "v_exp_f32 v30, v30\n"
  "v_mfma_f32_16x16x32_bf16 v[16:19], v[40:43], v[168:171], v[16:19]\n"
  "s_add_u32 m0, s59, 0\n"
  "s_waitcnt vmcnt(10)\n"
  "ds_read_b128 a[192:195], v87 offset:8192\n"
  "ds_read_b128 a[196:199], v88 offset:8192\n"
  "v_exp_f32 v27, v27\n"
  "v_mfma_f32_16x16x32_bf16 v[16:19], v[44:47], v[172:175], v[16:19]\n"
  "s_waitcnt lgkmcnt(14)\n"
  "buffer_load_dwordx4 v112, s[40:43], s57 offen offset:0 nt lds\n"
  "buffer_load_dwordx4 v113, s[40:43], s57 offen offset:1024 nt lds\n"
  "v_exp_f32 v31, v31\n"
  "v_mfma_f32_16x16x32_bf16 v[16:19], v[48:51], v[176:179], v[16:19]\n"
  "s_mov_b32 m0, s88\n"
  "s_waitcnt vmcnt(10)\n"
  "ds_read_b128 a[200:203], v87 offset:10240\n"
  "ds_read_b128 a[204:207], v88 offset:10240\n"
  "v_cvt_pk_bf16_f32 v26, v26, v30\n"
  "v_cvt_pk_bf16_f32 v27, v27, v31\n"
  "v_mfma_f32_16x16x32_bf16 v[16:19], v[52:55], v[180:183], v[16:19]\n"
  "ds_read_b128 v[68:71], v82 offset:2048\n"
  "ds_read_b128 v[72:75], v81 offset:4096\n"
  "ds_read_b128 v[76:79], v82 offset:4096\n"
  "ds_write_addtid_b32 v26 offset:8992\n"
  "ds_write_addtid_b32 v27 offset:13088\n"
  "v_pk_mul_f32 v[0:1], v[0:1], s[60:61] op_sel_hi:[1,0]\n"
  "v_pk_mul_f32 v[2:3], v[2:3], s[60:61] op_sel_hi:[1,0]\n"
  "v_pk_mul_f32 v[4:5], v[4:5], s[60:61] op_sel_hi:[1,0]\n"
  "v_pk_mul_f32 v[6:7], v[6:7], s[60:61] op_sel_hi:[1,0]\n"
  "v_pk_mul_f32 v[8:9], v[8:9], s[60:61] op_sel_hi:[1,0]\n"
  "v_pk_mul_f32 v[10:11], v[10:11], s[60:61] op_sel_hi:[1,0]\n"
  "v_pk_mul_f32 v[12:13], v[12:13], s[60:61] op_sel_hi:[1,0]\n"
  "v_pk_mul_f32 v[14:15], v[14:15], s[60:61] op_sel_hi:[1,0]\n"
  "v_mfma_f32_16x16x32_bf16 v[20:23], v[52:55], v[204:207], v[92:95]\n"
  "s_add_u32 m0, s59, 1920\n"
  "v_exp_f32 v0, v0\n"
  "v_mfma_f32_16x16x32_bf16 v[20:23], v[48:51], v[200:203], v[20:23]\n"
  "s_waitcnt lgkmcnt(15)\n"
  "buffer_load_dwordx4 v112, s[40:43], s57 offen offset:128 nt lds\n"
  "buffer_load_dwordx4 v113, s[40:43], s57 offen offset:1152 nt lds\n"
  "v_exp_f32 v4, v4\n"
  "v_mfma_f32_16x16x32_bf16 v[20:23], v[44:47], v[196:199], v[20:23]\n"
  "s_add_u32 m0, s59, 3840\n"
  "s_waitcnt vmcnt(10)\n"
  "ds_read_b128 a[208:211], v87 offset:12288\n"
  "ds_read_b128 a[212:215], v88 offset:12288\n"
  "v_exp_f32 v1, v1\n"
  "v_mfma_f32_16x16x32_bf16 v[20:23], v[40:43], v[192:195], v[20:23]\n"
  "buffer_load_dwordx4 v112, s[40:43], s57 offen offset:256 nt lds\n"
  "buffer_load_dwordx4 v113, s[40:43], s57 offen offset:1280 nt lds\n"
  "s_add_u32 s57, s57, 384\n"
  "v_exp_f32 v5, v5\n"
  "v_mfma_f32_16x16x32_bf16 v[20:23], v[36:39], v[188:191], v[20:23]\n"
  "s_mov_b32 m0, s88\n"
  "s_waitcnt vmcnt(10)\n"
  "ds_read_b128 a[232:235], v87 offset:14336\n"
  "ds_read_b128 a[236:239], v88 offset:14336\n"
  "v_cvt_pk_bf16_f32 v0, v0, v4\n"
  "v_cvt_pk_bf16_f32 v1, v1, v5\n"
  "v_mfma_f32_16x16x32_bf16 v[20:23], v[32:35], v[184:187], v[20:23]\n"
  "ds_write_addtid_b32 v0 offset:1024\n"
  "ds_write_addtid_b32 v1 offset:5120\n"
  "v_mfma_f32_16x16x32_bf16 v[24:27], v[32:35], v[208:211], v[92:95]\n"
  "s_add_u32 m0, s59, 5888\n"
  "v_exp_f32 v2, v2\n"
  "v_mfma_f32_16x16x32_bf16 v[24:27], v[36:39], v[212:215], v[24:27]\n"
  "s_waitcnt lgkmcnt(15)\n"
  "buffer_load_dwordx4 v112, s[40:43], s57 offen offset:256 nt lds\n"
  "buffer_load_dwordx4 v113, s[40:43], s57 offen offset:1280 nt lds\n"
  "v_exp_f32 v6, v6\n"
  "v_mfma_f32_16x16x32_bf16 v[24:27], v[40:43], v[216:219], v[24:27]\n"
  "s_add_u32 m0, s59, 8064\n"
  "s_waitcnt vmcnt(10)\n"
  "ds_read_b128 a[224:227], v87 offset:16384\n"
  "ds_read_b128 a[228:231], v88 offset:16384\n"
  "v_exp_f32 v3, v3\n"
  "v_mfma_f32_16x16x32_bf16 v[24:27], v[44:47], v[220:223], v[24:27]\n"
  "s_waitcnt lgkmcnt(15)\n"
  "buffer_load_dwordx4 v112, s[40:43], s57 offen offset:128 nt lds\n"
  "buffer_load_dwordx4 v113, s[40:43], s57 offen offset:1152 nt lds\n"
  "v_exp_f32_e64 v7, v7\n"
  "v_mfma_f32_16x16x32_bf16 v[24:27], v[48:51], v[224:227], v[24:27]\n"
  "s_mov_b32 m0, s88\n"
  "s_waitcnt vmcnt(10)\n"
  "ds_read_b128 a[216:219], v87 offset:18432\n"
  "ds_read_b128 a[220:223], v88 offset:18432\n"
  "v_cvt_pk_bf16_f32 v2, v2, v6\n"
  "v_cvt_pk_bf16_f32 v3, v3, v7\n"
  "v_mfma_f32_16x16x32_bf16 v[24:27], v[52:55], v[228:231], v[24:27]\n"
  "s_barrier\n"
  "ds_write_addtid_b32 v2 offset:9248\n"
  "ds_write_addtid_b32 v3 offset:13344\n"
  "v_mfma_f32_16x16x32_bf16 v[28:31], v[52:55], v[252:255], v[92:95]\n"
  "v_exp_f32 v8, v8\n"
  "v_mfma_f32_16x16x32_bf16 v[28:31], v[48:51], v[248:251], v[28:31]\n"
  "v_exp_f32 v12, v12\n"
  "v_mfma_f32_16x16x32_bf16 v[28:31], v[44:47], v[244:247], v[28:31]\n"
  "s_add_u32 m0, s59, 10240\n"
  "v_exp_f32 v9, v9\n"
  "v_mfma_f32_16x16x32_bf16 v[28:31], v[40:43], v[240:243], v[28:31]\n"
  "s_waitcnt lgkmcnt(15)\n"
  "buffer_load_dwordx4 v112, s[40:43], s57 offen offset:0 nt lds\n"
  "buffer_load_dwordx4 v113, s[40:43], s57 offen offset:1024 nt lds\n"
  "s_add_u32 s57, s57, 11904\n"
  "v_exp_f32_e64 v13, v13\n"
  "v_mfma_f32_16x16x32_bf16 v[28:31], v[36:39], v[236:239], v[28:31]\n"
  "s_mov_b32 m0, s88\n"
  "s_waitcnt vmcnt(10)\n"
  "ds_read_b128 a[240:243], v87 offset:0\n"
  "ds_read_b128 a[244:247], v88 offset:0\n"
  "v_cvt_pk_bf16_f32 v8, v8, v12\n"
  "v_cvt_pk_bf16_f32 v9, v9, v13\n"
  "v_mfma_f32_16x16x32_bf16 v[28:31], v[32:35], v[232:235], v[28:31]\n"
  "s_waitcnt lgkmcnt(15)\n"
  "ds_write_addtid_b32 v8 offset:1280\n"
  "ds_write_addtid_b32 v9 offset:5376\n"
  ".p2align 3\n"
  "v_mfma_f32_16x16x32_bf16 v[0:3], v[56:59], a[0:3], v[92:95]\n"
  "s_add_u32 s66, s66, 6144\n"
  "s_cmp_eq_u32 s66, 102400\n"
  "s_cselect_b32 s66, 65536, s66\n"
  "s_add_u32 m0, s59, 12288\n"
  "v_exp_f32 v10, v10\n"
  "v_mfma_f32_16x16x32_bf16 v[0:3], v[60:63], a[4:7], v[0:3]\n"
  "s_waitcnt lgkmcnt(14)\n"
  "buffer_load_dwordx4 v112, s[40:43], s57 offen offset:0 nt lds\n"
  "buffer_load_dwordx4 v113, s[40:43], s57 offen offset:1024 nt lds\n"
  "v_exp_f32 v14, v14\n"
  "v_mfma_f32_16x16x32_bf16 v[0:3], v[64:67], a[8:11], v[0:3]\n"
  "s_add_u32 m0, s59, 14208\n"
  "s_waitcnt vmcnt(10)\n"
  "ds_read_b128 a[248:251], v87 offset:2048\n"
  "ds_read_b128 a[252:255], v88 offset:2048\n"
  "v_exp_f32 v11, v11\n"
  "v_mfma_f32_16x16x32_bf16 v[0:3], v[68:71], a[12:15], v[0:3]\n"
  "s_waitcnt lgkmcnt(14)\n"
  "buffer_load_dwordx4 v112, s[40:43], s57 offen offset:128 nt lds\n"
  "buffer_load_dwordx4 v113, s[40:43], s57 offen offset:1152 nt lds\n"
  "v_exp_f32_e64 v15, v15\n"
  "v_mfma_f32_16x16x32_bf16 v[0:3], v[72:75], a[16:19], v[0:3]\n"
  "s_mov_b32 m0, s88\n"
  "s_waitcnt vmcnt(10)\n"
  "ds_read_b128 v[128:131], v87 offset:4096\n"
  "ds_read_b128 v[132:135], v88 offset:4096\n"
  "v_cvt_pk_bf16_f32 v10, v10, v14\n"
  "v_cvt_pk_bf16_f32 v11, v11, v15\n"
  "v_mfma_f32_16x16x32_bf16 v[0:3], v[76:79], a[20:23], v[0:3]\n"
  "ds_write_addtid_b32 v10 offset:9504\n"
  "ds_write_addtid_b32 v11 offset:13600\n"
  "v_add_u32 v81, s66, v83\n"
  "v_add_u32 v82, s66, v84\n"
  "v_pk_mul_f32 v[16:17], v[16:17], s[60:61] op_sel_hi:[1,0]\n"
  "v_pk_mul_f32 v[18:19], v[18:19], s[60:61] op_sel_hi:[1,0]\n"
  "v_pk_mul_f32 v[20:21], v[20:21], s[60:61] op_sel_hi:[1,0]\n"
  "v_pk_mul_f32 v[22:23], v[22:23], s[60:61] op_sel_hi:[1,0]\n"
  "v_pk_mul_f32 v[24:25], v[24:25], s[60:61] op_sel_hi:[1,0]\n"
  "v_pk_mul_f32 v[26:27], v[26:27], s[60:61] op_sel_hi:[1,0]\n"
  "v_pk_mul_f32 v[28:29], v[28:29], s[60:61] op_sel_hi:[1,0]\n"
  "v_pk_mul_f32 v[30:31], v[30:31], s[60:61] op_sel_hi:[1,0]\n"
  "v_mfma_f32_16x16x32_bf16 v[4:7], v[76:79], a[44:47], v[92:95]\n"
  "s_add_u32 m0, s59, 16128\n"
  "v_exp_f32 v16, v16\n"
  "v_mfma_f32_16x16x32_bf16 v[4:7], v[72:75], a[40:43], v[4:7]\n"
  "s_waitcnt lgkmcnt(14)\n"
  "buffer_load_dwordx4 v112, s[40:43], s57 offen offset:256 nt lds\n"
  "buffer_load_dwordx4 v113, s[40:43], s57 offen offset:1280 nt lds\n"
  "s_add_u32 s57, s57, 384\n"
  "v_exp_f32 v20, v20\n"
  "v_mfma_f32_16x16x32_bf16 v[4:7], v[68:71], a[36:39], v[4:7]\n"
  "s_add_u32 m0, s59, 18176\n"
  "s_waitcnt vmcnt(10)\n"
  "ds_read_b128 v[152:155], v87 offset:6144\n"
  "ds_read_b128 v[156:159], v88 offset:6144\n"
  "v_exp_f32 v17, v17\n"
  "v_mfma_f32_16x16x32_bf16 v[4:7], v[64:67], a[32:35], v[4:7]\n"
  "s_waitcnt lgkmcnt(14)\n"
  "buffer_load_dwordx4 v112, s[40:43], s57 offen offset:256 nt lds\n"
  "buffer_load_dwordx4 v113, s[40:43], s57 offen offset:1280 nt lds\n"
  "v_exp_f32_e64 v21, v21\n"
  "v_mfma_f32_16x16x32_bf16 v[4:7], v[60:63], a[28:31], v[4:7]\n"
  "s_mov_b32 m0, s88\n"
  "s_waitcnt vmcnt(10)\n"
  "ds_read_b128 v[144:147], v87 offset:8192\n"
  "ds_read_b128 v[148:151], v88 offset:8192\n"
  "v_cvt_pk_bf16_f32 v16, v16, v20\n"
  "v_cvt_pk_bf16_f32 v17, v17, v21\n"
  "v_mfma_f32_16x16x32_bf16 v[4:7], v[56:59], a[24:27], v[4:7]\n"
  "ds_write_addtid_b32 v16 offset:1536\n"
  "ds_write_addtid_b32 v17 offset:5632\n"
  "v_mfma_f32_16x16x32_bf16 v[8:11], v[56:59], a[48:51], v[92:95]\n"
  "s_add_u32 m0, s59, -128\n"
  "v_exp_f32 v18, v18\n"
  "v_mfma_f32_16x16x32_bf16 v[8:11], v[60:63], a[52:55], v[8:11]\n"
  "s_waitcnt lgkmcnt(14)\n"
  "buffer_load_dwordx4 v112, s[40:43], s57 offen offset:128 nt lds\n"
  "buffer_load_dwordx4 v113, s[40:43], s57 offen offset:1152 nt lds\n"
  "v_exp_f32 v22, v22\n"
  "v_mfma_f32_16x16x32_bf16 v[8:11], v[64:67], a[56:59], v[8:11]\n"
  "s_add_u32 m0, s59, 2048\n"
  "s_waitcnt vmcnt(10)\n"
  "ds_read_b128 v[136:139], v87 offset:10240\n"
  "ds_read_b128 v[140:143], v88 offset:10240\n"
  "v_exp_f32 v19, v19\n"
  "v_mfma_f32_16x16x32_bf16 v[8:11], v[68:71], a[60:63], v[8:11]\n"
  "s_waitcnt lgkmcnt(12)\n"
  "buffer_load_dwordx4 v112, s[40:43], s57 offen offset:0 nt lds\n"
  "buffer_load_dwordx4 v113, s[40:43], s57 offen offset:1024 nt lds\n"
  "s_add_u32 s57, s57, 11904\n"
  "v_exp_f32_e64 v23, v23\n"
  "v_mfma_f32_16x16x32_bf16 v[8:11], v[72:75], a[64:67], v[8:11]\n"
  "s_mov_b32 m0, s88\n"
  "s_waitcnt vmcnt(10)\n"
  "ds_read_b128 v[160:163], v87 offset:12288\n"
  "ds_read_b128 v[164:167], v88 offset:12288\n"
  "v_cvt_pk_bf16_f32 v18, v18, v22\n"
  "v_cvt_pk_bf16_f32 v19, v19, v23\n"
  "v_mfma_f32_16x16x32_bf16 v[8:11], v[76:79], a[68:71], v[8:11]\n"
  "ds_write_addtid_b32 v18 offset:9760\n"
  "ds_write_addtid_b32 v19 offset:13856\n"
  "v_mfma_f32_16x16x32_bf16 v[12:15], v[76:79], a[92:95], v[92:95]\n"
  "s_add_u32 m0, s59, 4096\n"
  "v_exp_f32 v24, v24\n"
  "v_mfma_f32_16x16x32_bf16 v[12:15], v[72:75], a[88:91], v[12:15]\n"
  "s_waitcnt lgkmcnt(14)\n"
  "buffer_load_dwordx4 v112, s[40:43], s57 offen offset:0 nt lds\n"
  "buffer_load_dwordx4 v113, s[40:43], s57 offen offset:1024 nt lds\n"
  "v_exp_f32 v28, v28\n"
  "v_mfma_f32_16x16x32_bf16 v[12:15], v[68:71], a[84:87], v[12:15]\n"
  "s_waitcnt vmcnt(10)\n"
  "ds_read_b128 v[168:171], v87 offset:14336\n"
  "ds_read_b128 v[172:175], v88 offset:14336\n"
  "v_exp_f32 v25, v25\n"
  "v_mfma_f32_16x16x32_bf16 v[12:15], v[64:67], a[80:83], v[12:15]\n"
  "v_exp_f32_e64 v29, v29\n"
  "v_mfma_f32_16x16x32_bf16 v[12:15], v[60:63], a[76:79], v[12:15]\n"
  "s_mov_b32 m0, s88\n"
  "v_cvt_pk_bf16_f32 v24, v24, v28\n"
  "v_cvt_pk_bf16_f32 v25, v25, v29\n"
  "v_mfma_f32_16x16x32_bf16 v[12:15], v[56:59], a[72:75], v[12:15]\n"
  "ds_write_addtid_b32 v24 offset:1792\n"
  "ds_write_addtid_b32 v25 offset:5888\n"
  "v_mfma_f32_16x16x32_bf16 v[16:19], v[56:59], a[96:99], v[92:95]\n"
  "s_add_u32 m0, s59, 6016\n"
  "v_exp_f32 v26, v26\n"
  "v_mfma_f32_16x16x32_bf16 v[16:19], v[60:63], a[100:103], v[16:19]\n"
  "s_waitcnt lgkmcnt(14)\n"
  "buffer_load_dwordx4 v112, s[40:43], s57 offen offset:128 nt lds\n"
  "buffer_load_dwordx4 v113, s[40:43], s57 offen offset:1152 nt lds\n"
  "v_exp_f32 v30, v30\n"
  "v_mfma_f32_16x16x32_bf16 v[16:19], v[64:67], a[104:107], v[16:19]\n"
  "s_add_u32 m0, s59, 7936\n"
  "s_waitcnt vmcnt(10)\n"
  "ds_read_b128 v[176:179], v87 offset:16384\n"
  "ds_read_b128 v[180:183], v88 offset:16384\n"
  "v_exp_f32 v27, v27\n"
  "v_mfma_f32_16x16x32_bf16 v[16:19], v[68:71], a[108:111], v[16:19]\n"
  "s_waitcnt lgkmcnt(14)\n"
  "buffer_load_dwordx4 v112, s[40:43], s57 offen offset:256 nt lds\n"
  "buffer_load_dwordx4 v113, s[40:43], s57 offen offset:1280 nt lds\n"
  "s_add_u32 s57, s57, 384\n"
  "v_exp_f32_e64 v31, v31\n"
  "v_mfma_f32_16x16x32_bf16 v[16:19], v[72:75], a[112:115], v[16:19]\n"
  "s_mov_b32 m0, s88\n"
  "s_waitcnt vmcnt(10)\n"
  "ds_read_b128 v[200:203], v87 offset:18432\n"
  "ds_read_b128 v[204:207], v88 offset:18432\n"
  "v_cvt_pk_bf16_f32 v26, v26, v30\n"
  "v_cvt_pk_bf16_f32 v27, v27, v31\n"
  "v_mfma_f32_16x16x32_bf16 v[16:19], v[76:79], a[116:119], v[16:19]\n"
  "ds_write_addtid_b32 v26 offset:10016\n"
  "ds_write_addtid_b32 v27 offset:14112\n"
  "v_pk_mul_f32 v[0:1], v[0:1], s[60:61] op_sel_hi:[1,0]\n"
  "v_pk_mul_f32 v[2:3], v[2:3], s[60:61] op_sel_hi:[1,0]\n"
  "v_pk_mul_f32 v[4:5], v[4:5], s[60:61] op_sel_hi:[1,0]\n"
  "v_pk_mul_f32 v[6:7], v[6:7], s[60:61] op_sel_hi:[1,0]\n"
  "v_pk_mul_f32 v[8:9], v[8:9], s[60:61] op_sel_hi:[1,0]\n"
  "v_pk_mul_f32 v[10:11], v[10:11], s[60:61] op_sel_hi:[1,0]\n"
  "v_pk_mul_f32 v[12:13], v[12:13], s[60:61] op_sel_hi:[1,0]\n"
  "v_pk_mul_f32 v[14:15], v[14:15], s[60:61] op_sel_hi:[1,0]\n"
  "v_mfma_f32_16x16x32_bf16 v[20:23], v[76:79], a[140:143], v[92:95]\n"
  "s_add_u32 m0, s59, 9984\n"
  "v_exp_f32 v0, v0\n"
  "v_mfma_f32_16x16x32_bf16 v[20:23], v[72:75], a[136:139], v[20:23]\n"
  "s_waitcnt lgkmcnt(14)\n"
  "buffer_load_dwordx4 v112, s[40:43], s57 offen offset:256 nt lds\n"
  "buffer_load_dwordx4 v113, s[40:43], s57 offen offset:1280 nt lds\n"
  "v_exp_f32 v4, v4\n"
  "v_mfma_f32_16x16x32_bf16 v[20:23], v[68:71], a[132:135], v[20:23]\n"
  "s_add_u32 m0, s59, 12160\n"
  "s_waitcnt vmcnt(10)\n"
  "ds_read_b128 v[192:195], v87 offset:0\n"
  "ds_read_b128 v[196:199], v88 offset:0\n"
  "v_exp_f32 v1, v1\n"
  "v_mfma_f32_16x16x32_bf16 v[20:23], v[64:67], a[128:131], v[20:23]\n"
  "s_waitcnt lgkmcnt(14)\n"
  "buffer_load_dwordx4 v112, s[40:43], s57 offen offset:128 nt lds\n"
  "buffer_load_dwordx4 v113, s[40:43], s57 offen offset:1152 nt lds\n"
  "v_exp_f32 v5, v5\n"
  "v_mfma_f32_16x16x32_bf16 v[20:23], v[60:63], a[124:127], v[20:23]\n"
  "s_add_u32 s88, s88, 16448\n"
  "s_cmp_eq_u32 s88, s68\n"
  "s_cselect_b32 s88, s89, s88\n"
  "s_mov_b32 m0, s88\n"
  "s_mov_b32 m0, s88\n"
  "s_waitcnt vmcnt(10)\n"
  "ds_read_b128 v[184:187], v87 offset:2048\n"
  "ds_read_b128 v[188:191], v88 offset:2048\n"
  "v_cvt_pk_bf16_f32 v0, v0, v4\n"
  "v_cvt_pk_bf16_f32 v1, v1, v5\n"
  "v_mfma_f32_16x16x32_bf16 v[20:23], v[56:59], a[120:123], v[20:23]\n"
  "ds_write_addtid_b32 v0 offset:0\n"
  "ds_write_addtid_b32 v1 offset:4096\n"
  "v_mfma_f32_16x16x32_bf16 v[24:27], v[56:59], a[144:147], v[92:95]\n"
  "s_add_u32 m0, s59, 14336\n"
  "v_exp_f32 v2, v2\n"
  "v_mfma_f32_16x16x32_bf16 v[24:27], v[60:63], a[148:151], v[24:27]\n"
  "s_waitcnt lgkmcnt(14)\n"
  "buffer_load_dwordx4 v112, s[40:43], s57 offen offset:0 nt lds\n"
  "buffer_load_dwordx4 v113, s[40:43], s57 offen offset:1024 nt lds\n"
  "s_add_u32 s57, s57, 11904\n"
  "v_exp_f32 v6, v6\n"
  "v_mfma_f32_16x16x32_bf16 v[24:27], v[64:67], a[152:155], v[24:27]\n"
  "s_waitcnt vmcnt(10)\n"
  "ds_read_b128 v[208:211], v87 offset:4096\n"
  "ds_read_b128 v[212:215], v88 offset:4096\n"
  "v_exp_f32 v3, v3\n"
  "v_mfma_f32_16x16x32_bf16 v[24:27], v[68:71], a[156:159], v[24:27]\n"
  "v_exp_f32 v7, v7\n"
  "v_mfma_f32_16x16x32_bf16 v[24:27], v[72:75], a[160:163], v[24:27]\n"
  "s_mov_b32 m0, s88\n"
  "s_waitcnt vmcnt(8)\n"
  "ds_read_b128 v[216:219], v87 offset:6144\n"
  "ds_read_b128 v[220:223], v88 offset:6144\n"
  "v_cvt_pk_bf16_f32 v2, v2, v6\n"
  "v_cvt_pk_bf16_f32 v3, v3, v7\n"
  "v_mfma_f32_16x16x32_bf16 v[24:27], v[76:79], a[164:167], v[24:27]\n"
  "ds_write_addtid_b32 v2 offset:8224\n"
  "ds_write_addtid_b32 v3 offset:12320\n"
  "v_mfma_f32_16x16x32_bf16 v[28:31], v[76:79], a[188:191], v[92:95]\n"
  "v_exp_f32 v8, v8\n"
  "v_mfma_f32_16x16x32_bf16 v[28:31], v[72:75], a[184:187], v[28:31]\n"
  "v_exp_f32 v12, v12\n"
  "v_mfma_f32_16x16x32_bf16 v[28:31], v[68:71], a[180:183], v[28:31]\n"
  "s_waitcnt vmcnt(6)\n"
  "ds_read_b128 v[224:227], v87 offset:8192\n"
  "ds_read_b128 v[228:231], v88 offset:8192\n"
  "v_exp_f32 v9, v9\n"
  "v_mfma_f32_16x16x32_bf16 v[28:31], v[64:67], a[176:179], v[28:31]\n"
  "v_exp_f32_e64 v13, v13\n"
  "v_mfma_f32_16x16x32_bf16 v[28:31], v[60:63], a[172:175], v[28:31]\n"
  "v_cvt_pk_bf16_f32 v8, v8, v12\n"
  "v_cvt_pk_bf16_f32 v9, v9, v13\n"
  "v_mfma_f32_16x16x32_bf16 v[28:31], v[56:59], a[168:171], v[28:31]\n"
  "ds_write_addtid_b32 v8 offset:256\n"
  "ds_write_addtid_b32 v9 offset:4352\n"
  "v_mfma_f32_16x16x32_bf16 v[0:3], v[56:59], a[192:195], v[92:95]\n"
  "v_exp_f32 v10, v10\n"
  "v_mfma_f32_16x16x32_bf16 v[0:3], v[60:63], a[196:199], v[0:3]\n"
  "v_exp_f32 v14, v14\n"
  "v_mfma_f32_16x16x32_bf16 v[0:3], v[64:67], a[200:203], v[0:3]\n"
  "s_waitcnt vmcnt(4)\n"
  "ds_read_b128 v[248:251], v87 offset:10240\n"
  "ds_read_b128 v[252:255], v88 offset:10240\n"
  "v_exp_f32 v11, v11\n"
  "v_mfma_f32_16x16x32_bf16 v[0:3], v[68:71], a[204:207], v[0:3]\n"
  "v_exp_f32 v15, v15\n"
  "v_mfma_f32_16x16x32_bf16 v[0:3], v[72:75], a[208:211], v[0:3]\n"
  "s_waitcnt vmcnt(2)\n"
  "ds_read_b128 v[240:243], v87 offset:12288\n"
  "ds_read_b128 v[244:247], v88 offset:12288\n"
  "v_cvt_pk_bf16_f32 v10, v10, v14\n"
  "v_cvt_pk_bf16_f32 v11, v11, v15\n"
  "v_mfma_f32_16x16x32_bf16 v[0:3], v[76:79], a[212:215], v[0:3]\n"
  "ds_read_b128 v[32:35], v81 offset:0\n"
  "ds_read_b128 v[36:39], v82 offset:0\n"
  "ds_read_b128 v[40:43], v81 offset:2048\n"
  "ds_write_addtid_b32 v10 offset:8480\n"
  "ds_write_addtid_b32 v11 offset:12576\n"
  "v_pk_mul_f32 v[16:17], v[16:17], s[60:61] op_sel_hi:[1,0]\n"
  "v_pk_mul_f32 v[18:19], v[18:19], s[60:61] op_sel_hi:[1,0]\n"
  "v_pk_mul_f32 v[20:21], v[20:21], s[60:61] op_sel_hi:[1,0]\n"
  "v_pk_mul_f32 v[22:23], v[22:23], s[60:61] op_sel_hi:[1,0]\n"
  "v_pk_mul_f32 v[24:25], v[24:25], s[60:61] op_sel_hi:[1,0]\n"
  "v_pk_mul_f32 v[26:27], v[26:27], s[60:61] op_sel_hi:[1,0]\n"
  "v_pk_mul_f32 v[28:29], v[28:29], s[60:61] op_sel_hi:[1,0]\n"
  "v_pk_mul_f32 v[30:31], v[30:31], s[60:61] op_sel_hi:[1,0]\n"
  "v_mfma_f32_16x16x32_bf16 v[4:7], v[76:79], a[236:239], v[92:95]\n"
  "v_exp_f32 v16, v16\n"
  "v_mfma_f32_16x16x32_bf16 v[4:7], v[72:75], a[232:235], v[4:7]\n"
  "v_exp_f32 v20, v20\n"
  "v_mfma_f32_16x16x32_bf16 v[4:7], v[68:71], a[228:231], v[4:7]\n"
  "s_waitcnt vmcnt(0)\n"
  "ds_read_b128 v[232:235], v87 offset:14336\n"
  "ds_read_b128 v[236:239], v88 offset:14336\n"
  "v_exp_f32 v17, v17\n"
  "v_mfma_f32_16x16x32_bf16 v[4:7], v[64:67], a[224:227], v[4:7]\n"
  "v_exp_f32_e64 v21, v21\n"
  "v_mfma_f32_16x16x32_bf16 v[4:7], v[60:63], a[220:223], v[4:7]\n"
  "v_cvt_pk_bf16_f32 v16, v16, v20\n"
  "v_cvt_pk_bf16_f32 v17, v17, v21\n"
  "v_mfma_f32_16x16x32_bf16 v[4:7], v[56:59], a[216:219], v[4:7]\n"
  "s_waitcnt lgkmcnt(0)\n"
  "s_barrier\n"
  "ds_write_addtid_b32 v16 offset:512\n"
  "ds_write_addtid_b32 v17 offset:4608\n"
  "v_mfma_f32_16x16x32_bf16 v[8:11], v[56:59], a[240:243], v[92:95]\n"
  "v_exp_f32 v18, v18\n"
  "v_mfma_f32_16x16x32_bf16 v[8:11], v[60:63], a[244:247], v[8:11]\n"
  "v_exp_f32 v22, v22\n"
  "v_mfma_f32_16x16x32_bf16 v[8:11], v[64:67], a[248:251], v[8:11]\n"
  "v_exp_f32 v19, v19\n"
  "v_mfma_f32_16x16x32_bf16 v[8:11], v[68:71], a[252:255], v[8:11]\n"
  "v_exp_f32 v23, v23\n"
  "v_mfma_f32_16x16x32_bf16 v[8:11], v[72:75], v[128:131], v[8:11]\n"
  "v_cvt_pk_bf16_f32 v18, v18, v22\n"
  "v_cvt_pk_bf16_f32 v19, v19, v23\n"
  "v_mfma_f32_16x16x32_bf16 v[8:11], v[76:79], v[132:135], v[8:11]\n"
  "ds_write_addtid_b32 v18 offset:8736\n"
  "ds_write_addtid_b32 v19 offset:12832\n"
  "v_mfma_f32_16x16x32_bf16 v[12:15], v[76:79], v[156:159], v[92:95]\n"
  "v_exp_f32 v24, v24\n"
  "v_mfma_f32_16x16x32_bf16 v[12:15], v[72:75], v[152:155], v[12:15]\n"
  "v_exp_f32 v28, v28\n"
  "v_mfma_f32_16x16x32_bf16 v[12:15], v[68:71], v[148:151], v[12:15]\n"
  "v_exp_f32 v25, v25\n"
  "v_mfma_f32_16x16x32_bf16 v[12:15], v[64:67], v[144:147], v[12:15]\n"
  "v_exp_f32 v29, v29\n"
  "v_mfma_f32_16x16x32_bf16 v[12:15], v[60:63], v[140:143], v[12:15]\n"
  "v_cvt_pk_bf16_f32 v24, v24, v28\n"
  "v_cvt_pk_bf16_f32 v25, v25, v29\n"
  "v_mfma_f32_16x16x32_bf16 v[12:15], v[56:59], v[136:139], v[12:15]\n"
  "ds_write_addtid_b32 v24 offset:768\n"
  "ds_write_addtid_b32 v25 offset:4864\n"
  "v_mfma_f32_16x16x32_bf16 v[16:19], v[56:59], v[160:163], v[92:95]\n"
  "v_exp_f32 v26, v26\n"
  "v_mfma_f32_16x16x32_bf16 v[16:19], v[60:63], v[164:167], v[16:19]\n"
  "v_exp_f32 v30, v30\n"
  "v_mfma_f32_16x16x32_bf16 v[16:19], v[64:67], v[168:171], v[16:19]\n"
  "v_exp_f32 v27, v27\n"
  "v_mfma_f32_16x16x32_bf16 v[16:19], v[68:71], v[172:175], v[16:19]\n"
  "v_exp_f32 v31, v31\n"
  "v_mfma_f32_16x16x32_bf16 v[16:19], v[72:75], v[176:179], v[16:19]\n"
  "v_cvt_pk_bf16_f32 v26, v26, v30\n"
  "v_cvt_pk_bf16_f32 v27, v27, v31\n"
  "v_mfma_f32_16x16x32_bf16 v[16:19], v[76:79], v[180:183], v[16:19]\n"
  "ds_read_b128 v[44:47], v82 offset:2048\n"
  "ds_read_b128 v[48:51], v81 offset:4096\n"
  "ds_read_b128 v[52:55], v82 offset:4096\n"
  "ds_write_addtid_b32 v26 offset:8992\n"
  "ds_write_addtid_b32 v27 offset:13088\n"
  "v_pk_mul_f32 v[0:1], v[0:1], s[60:61] op_sel_hi:[1,0]\n"
  "v_pk_mul_f32 v[2:3], v[2:3], s[60:61] op_sel_hi:[1,0]\n"
  "v_pk_mul_f32 v[4:5], v[4:5], s[60:61] op_sel_hi:[1,0]\n"
  "v_pk_mul_f32 v[6:7], v[6:7], s[60:61] op_sel_hi:[1,0]\n"
  "v_pk_mul_f32 v[8:9], v[8:9], s[60:61] op_sel_hi:[1,0]\n"
  "v_pk_mul_f32 v[10:11], v[10:11], s[60:61] op_sel_hi:[1,0]\n"
  "v_pk_mul_f32 v[12:13], v[12:13], s[60:61] op_sel_hi:[1,0]\n"
  "v_pk_mul_f32 v[14:15], v[14:15], s[60:61] op_sel_hi:[1,0]\n"
  "v_mfma_f32_16x16x32_bf16 v[20:23], v[76:79], v[204:207], v[92:95]\n"
  "v_exp_f32 v0, v0\n"
  "v_mfma_f32_16x16x32_bf16 v[20:23], v[72:75], v[200:203], v[20:23]\n"
  "v_exp_f32 v4, v4\n"
  "v_mfma_f32_16x16x32_bf16 v[20:23], v[68:71], v[196:199], v[20:23]\n"
  "v_exp_f32 v1, v1\n"
  "v_mfma_f32_16x16x32_bf16 v[20:23], v[64:67], v[192:195], v[20:23]\n"
  "v_exp_f32 v5, v5\n"
  "v_mfma_f32_16x16x32_bf16 v[20:23], v[60:63], v[188:191], v[20:23]\n"
  "v_cvt_pk_bf16_f32 v0, v0, v4\n"
  "v_cvt_pk_bf16_f32 v1, v1, v5\n"
  "v_mfma_f32_16x16x32_bf16 v[20:23], v[56:59], v[184:187], v[20:23]\n"
  "ds_write_addtid_b32 v0 offset:1024\n"
  "ds_write_addtid_b32 v1 offset:5120\n"
  "v_mfma_f32_16x16x32_bf16 v[24:27], v[56:59], v[208:211], v[92:95]\n"
  "v_exp_f32 v2, v2\n"
  "v_mfma_f32_16x16x32_bf16 v[24:27], v[60:63], v[212:215], v[24:27]\n"
  "v_exp_f32 v6, v6\n"
  "v_mfma_f32_16x16x32_bf16 v[24:27], v[64:67], v[216:219], v[24:27]\n"
  "v_exp_f32 v3, v3\n"
  "v_mfma_f32_16x16x32_bf16 v[24:27], v[68:71], v[220:223], v[24:27]\n"
  "v_exp_f32 v7, v7\n"
  "v_mfma_f32_16x16x32_bf16 v[24:27], v[72:75], v[224:227], v[24:27]\n"
  "v_cvt_pk_bf16_f32 v2, v2, v6\n"
  "v_cvt_pk_bf16_f32 v3, v3, v7\n"
  "v_mfma_f32_16x16x32_bf16 v[24:27], v[76:79], v[228:231], v[24:27]\n"
  "ds_write_addtid_b32 v2 offset:9248\n"
  "ds_write_addtid_b32 v3 offset:13344\n"
  "v_mfma_f32_16x16x32_bf16 v[28:31], v[76:79], v[252:255], v[92:95]\n"
  "v_exp_f32 v8, v8\n"
  "v_mfma_f32_16x16x32_bf16 v[28:31], v[72:75], v[248:251], v[28:31]\n"
  "v_exp_f32 v12, v12\n"
  "v_mfma_f32_16x16x32_bf16 v[28:31], v[68:71], v[244:247], v[28:31]\n"
  "v_exp_f32 v9, v9\n"
  "v_mfma_f32_16x16x32_bf16 v[28:31], v[64:67], v[240:243], v[28:31]\n"
  "s_sub_u32 s56, s56, 1\n"
  "v_exp_f32 v13, v13\n"
  "v_mfma_f32_16x16x32_bf16 v[28:31], v[60:63], v[236:239], v[28:31]\n"
  "v_cvt_pk_bf16_f32 v8, v8, v12\n"
  "v_cvt_pk_bf16_f32 v9, v9, v13\n"
  "v_mfma_f32_16x16x32_bf16 v[28:31], v[56:59], v[232:235], v[28:31]\n"
  "s_waitcnt lgkmcnt(6)\n"
  "ds_write_addtid_b32 v8 offset:1280\n"
  "ds_write_addtid_b32 v9 offset:5376\n"
  ".p2align 3\n"
  "v_mfma_f32_16x16x32_bf16 v[0:3], v[32:35], a[0:3], v[92:95]\n"
  "s_add_u32 s66, s66, 6144\n"
  "v_exp_f32 v10, v10\n"
  "v_mfma_f32_16x16x32_bf16 v[0:3], v[36:39], a[4:7], v[0:3]\n"
  "s_cmp_eq_u32 s66, 102400\n"
  "v_exp_f32 v14, v14\n"
  "v_mfma_f32_16x16x32_bf16 v[0:3], v[40:43], a[8:11], v[0:3]\n"
  "s_cselect_b32 s66, 65536, s66\n"
  "v_exp_f32 v11, v11\n"
  "v_mfma_f32_16x16x32_bf16 v[0:3], v[44:47], a[12:15], v[0:3]\n"
  "v_exp_f32 v15, v15\n"
  "v_mfma_f32_16x16x32_bf16 v[0:3], v[48:51], a[16:19], v[0:3]\n"
  "v_cvt_pk_bf16_f32 v10, v10, v14\n"
  "v_cvt_pk_bf16_f32 v11, v11, v15\n"
  "v_mfma_f32_16x16x32_bf16 v[0:3], v[52:55], a[20:23], v[0:3]\n"
  "ds_write_addtid_b32 v10 offset:9504\n"
  "ds_write_addtid_b32 v11 offset:13600\n"
  "v_add_u32 v81, s66, v83\n"
  "v_add_u32 v82, s66, v84\n"
  "v_pk_mul_f32 v[16:17], v[16:17], s[60:61] op_sel_hi:[1,0]\n"
  "v_pk_mul_f32 v[18:19], v[18:19], s[60:61] op_sel_hi:[1,0]\n"
  "v_pk_mul_f32 v[20:21], v[20:21], s[60:61] op_sel_hi:[1,0]\n"
  "v_pk_mul_f32 v[22:23], v[22:23], s[60:61] op_sel_hi:[1,0]\n"
  "v_pk_mul_f32 v[24:25], v[24:25], s[60:61] op_sel_hi:[1,0]\n"
  "v_pk_mul_f32 v[26:27], v[26:27], s[60:61] op_sel_hi:[1,0]\n"
  "v_pk_mul_f32 v[28:29], v[28:29], s[60:61] op_sel_hi:[1,0]\n"
  "v_pk_mul_f32 v[30:31], v[30:31], s[60:61] op_sel_hi:[1,0]\n"
  "v_mfma_f32_16x16x32_bf16 v[4:7], v[52:55], a[44:47], v[92:95]\n"
  "v_exp_f32 v16, v16\n"
  "v_mfma_f32_16x16x32_bf16 v[4:7], v[48:51], a[40:43], v[4:7]\n"
  "v_exp_f32 v20, v20\n"
  "v_mfma_f32_16x16x32_bf16 v[4:7], v[44:47], a[36:39], v[4:7]\n"
  "v_exp_f32 v17, v17\n"
  "v_mfma_f32_16x16x32_bf16 v[4:7], v[40:43], a[32:35], v[4:7]\n"
  "v_exp_f32 v21, v21\n"
  "v_mfma_f32_16x16x32_bf16 v[4:7], v[36:39], a[28:31], v[4:7]\n"
  "v_cvt_pk_bf16_f32 v16, v16, v20\n"
  "v_cvt_pk_bf16_f32 v17, v17, v21\n"
  "v_mfma_f32_16x16x32_bf16 v[4:7], v[32:35], a[24:27], v[4:7]\n"
  "ds_write_addtid_b32 v16 offset:1536\n"
  "ds_write_addtid_b32 v17 offset:5632\n"
  "v_mfma_f32_16x16x32_bf16 v[8:11], v[32:35], a[48:51], v[92:95]\n"
  "v_exp_f32 v18, v18\n"
  "v_mfma_f32_16x16x32_bf16 v[8:11], v[36:39], a[52:55], v[8:11]\n"
  "v_exp_f32 v22, v22\n"
  "v_mfma_f32_16x16x32_bf16 v[8:11], v[40:43], a[56:59], v[8:11]\n"
  "v_exp_f32 v19, v19\n"
  "v_mfma_f32_16x16x32_bf16 v[8:11], v[44:47], a[60:63], v[8:11]\n"
  "v_exp_f32 v23, v23\n"
  "v_mfma_f32_16x16x32_bf16 v[8:11], v[48:51], a[64:67], v[8:11]\n"
  "v_cvt_pk_bf16_f32 v18, v18, v22\n"
  "v_cvt_pk_bf16_f32 v19, v19, v23\n"
  "v_mfma_f32_16x16x32_bf16 v[8:11], v[52:55], a[68:71], v[8:11]\n"
  "ds_write_addtid_b32 v18 offset:9760\n"
  "ds_write_addtid_b32 v19 offset:13856\n"
  "v_mfma_f32_16x16x32_bf16 v[12:15], v[52:55], a[92:95], v[92:95]\n"
  "v_exp_f32 v24, v24\n"
  "v_mfma_f32_16x16x32_bf16 v[12:15], v[48:51], a[88:91], v[12:15]\n"
  "v_exp_f32 v28, v28\n"
  "v_mfma_f32_16x16x32_bf16 v[12:15], v[44:47], a[84:87], v[12:15]\n"
  "v_exp_f32 v25, v25\n"
  "v_mfma_f32_16x16x32_bf16 v[12:15], v[40:43], a[80:83], v[12:15]\n"
  "v_exp_f32 v29, v29\n"
  "v_mfma_f32_16x16x32_bf16 v[12:15], v[36:39], a[76:79], v[12:15]\n"
  "v_cvt_pk_bf16_f32 v24, v24, v28\n"
  "v_cvt_pk_bf16_f32 v25, v25, v29\n"
  "v_mfma_f32_16x16x32_bf16 v[12:15], v[32:35], a[72:75], v[12:15]\n"
  "ds_write_addtid_b32 v24 offset:1792\n"
  "ds_write_addtid_b32 v25 offset:5888\n"
  "v_mfma_f32_16x16x32_bf16 v[16:19], v[32:35], a[96:99], v[92:95]\n"
  "v_exp_f32 v26, v26\n"
  "v_mfma_f32_16x16x32_bf16 v[16:19], v[36:39], a[100:103], v[16:19]\n"
  "v_exp_f32 v30, v30\n"
  "v_mfma_f32_16x16x32_bf16 v[16:19], v[40:43], a[104:107], v[16:19]\n"
  "v_exp_f32 v27, v27\n"
  "v_mfma_f32_16x16x32_bf16 v[16:19], v[44:47], a[108:111], v[16:19]\n"
  "v_exp_f32 v31, v31\n"
  "v_mfma_f32_16x16x32_bf16 v[16:19], v[48:51], a[112:115], v[16:19]\n"
  "v_cvt_pk_bf16_f32 v26, v26, v30\n"
  "v_cvt_pk_bf16_f32 v27, v27, v31\n"
  "v_mfma_f32_16x16x32_bf16 v[16:19], v[52:55], a[116:119], v[16:19]\n"
  "ds_write_addtid_b32 v26 offset:10016\n"
  "ds_write_addtid_b32 v27 offset:14112\n"
  "v_pk_mul_f32 v[0:1], v[0:1], s[60:61] op_sel_hi:[1,0]\n"
  "v_pk_mul_f32 v[2:3], v[2:3], s[60:61] op_sel_hi:[1,0]\n"
  "v_pk_mul_f32 v[4:5], v[4:5], s[60:61] op_sel_hi:[1,0]\n"
  "v_pk_mul_f32 v[6:7], v[6:7], s[60:61] op_sel_hi:[1,0]\n"
  "v_pk_mul_f32 v[8:9], v[8:9], s[60:61] op_sel_hi:[1,0]\n"
  "v_pk_mul_f32 v[10:11], v[10:11], s[60:61] op_sel_hi:[1,0]\n"
  "v_pk_mul_f32 v[12:13], v[12:13], s[60:61] op_sel_hi:[1,0]\n"
  "v_pk_mul_f32 v[14:15], v[14:15], s[60:61] op_sel_hi:[1,0]\n"
  "v_mfma_f32_16x16x32_bf16 v[20:23], v[52:55], a[140:143], v[92:95]\n"
  "s_add_u32 s88, s88, 16448\n"
  "v_exp_f32 v0, v0\n"
  "v_mfma_f32_16x16x32_bf16 v[20:23], v[48:51], a[136:139], v[20:23]\n"
  "s_cmp_eq_u32 s88, s68\n"
  "v_exp_f32 v4, v4\n"
  "v_mfma_f32_16x16x32_bf16 v[20:23], v[44:47], a[132:135], v[20:23]\n"
  "s_cselect_b32 s88, s89, s88\n"
  "v_exp_f32 v1, v1\n"
  "v_mfma_f32_16x16x32_bf16 v[20:23], v[40:43], a[128:131], v[20:23]\n"
  "v_exp_f32_e64 v5, v5\n"
  "v_mfma_f32_16x16x32_bf16 v[20:23], v[36:39], a[124:127], v[20:23]\n"
  "s_mov_b32 m0, s88\n"
  "v_cvt_pk_bf16_f32 v0, v0, v4\n"
  "v_cvt_pk_bf16_f32 v1, v1, v5\n"
  "v_mfma_f32_16x16x32_bf16 v[20:23], v[32:35], a[120:123], v[20:23]\n"
  "ds_write_addtid_b32 v0 offset:0\n"
  "ds_write_addtid_b32 v1 offset:4096\n"
  "v_mfma_f32_16x16x32_bf16 v[24:27], v[32:35], a[144:147], v[92:95]\n"
  "v_exp_f32 v2, v2\n"
  "v_mfma_f32_16x16x32_bf16 v[24:27], v[36:39], a[148:151], v[24:27]\n"
  "v_exp_f32 v6, v6\n"
  "v_mfma_f32_16x16x32_bf16 v[24:27], v[40:43], a[152:155], v[24:27]\n"
  "v_exp_f32 v3, v3\n"
  "v_mfma_f32_16x16x32_bf16 v[24:27], v[44:47], a[156:159], v[24:27]\n"
  "v_exp_f32 v7, v7\n"
  "v_mfma_f32_16x16x32_bf16 v[24:27], v[48:51], a[160:163], v[24:27]\n"
  "v_cvt_pk_bf16_f32 v2, v2, v6\n"
  "v_cvt_pk_bf16_f32 v3, v3, v7\n"
  "v_mfma_f32_16x16x32_bf16 v[24:27], v[52:55], a[164:167], v[24:27]\n"
  "ds_write_addtid_b32 v2 offset:8224\n"
  "ds_write_addtid_b32 v3 offset:12320\n"
  "v_mfma_f32_16x16x32_bf16 v[28:31], v[52:55], a[188:191], v[92:95]\n"
  "v_exp_f32 v8, v8\n"
  "v_mfma_f32_16x16x32_bf16 v[28:31], v[48:51], a[184:187], v[28:31]\n"
  "v_exp_f32 v12, v12\n"
  "v_mfma_f32_16x16x32_bf16 v[28:31], v[44:47], a[180:183], v[28:31]\n"
  "v_exp_f32 v9, v9\n"
  "v_mfma_f32_16x16x32_bf16 v[28:31], v[40:43], a[176:179], v[28:31]\n"
  "v_exp_f32 v13, v13\n"
  "v_mfma_f32_16x16x32_bf16 v[28:31], v[36:39], a[172:175], v[28:31]\n"
  "v_cvt_pk_bf16_f32 v8, v8, v12\n"
  "v_cvt_pk_bf16_f32 v9, v9, v13\n"
  "v_mfma_f32_16x16x32_bf16 v[28:31], v[32:35], a[168:171], v[28:31]\n"
  "ds_write_addtid_b32 v8 offset:256\n"
  "ds_write_addtid_b32 v9 offset:4352\n"
  "v_mfma_f32_16x16x32_bf16 v[0:3], v[32:35], a[192:195], v[92:95]\n"
  "v_exp_f32 v10, v10\n"
  "v_mfma_f32_16x16x32_bf16 v[0:3], v[36:39], a[196:199], v[0:3]\n"
  "v_exp_f32 v14, v14\n"
  "v_mfma_f32_16x16x32_bf16 v[0:3], v[40:43], a[200:203], v[0:3]\n"
  "v_exp_f32 v11, v11\n"
  "v_mfma_f32_16x16x32_bf16 v[0:3], v[44:47], a[204:207], v[0:3]\n"
  "v_exp_f32 v15, v15\n"
  "v_mfma_f32_16x16x32_bf16 v[0:3], v[48:51], a[208:211], v[0:3]\n"
  "v_cvt_pk_bf16_f32 v10, v10, v14\n"
  "v_cvt_pk_bf16_f32 v11, v11, v15\n"
  "v_mfma_f32_16x16x32_bf16 v[0:3], v[52:55], a[212:215], v[0:3]\n"
  "ds_read_b128 v[56:59], v81 offset:0\n"
  "ds_read_b128 v[60:63], v82 offset:0\n"
  "ds_read_b128 v[64:67], v81 offset:2048\n"
  "ds_write_addtid_b32 v10 offset:8480\n"
  "ds_write_addtid_b32 v11 offset:12576\n"
  "v_pk_mul_f32 v[16:17], v[16:17], s[60:61] op_sel_hi:[1,0]\n"
  "v_pk_mul_f32 v[18:19], v[18:19], s[60:61] op_sel_hi:[1,0]\n"
  "v_pk_mul_f32 v[20:21], v[20:21], s[60:61] op_sel_hi:[1,0]\n"
  "v_pk_mul_f32 v[22:23], v[22:23], s[60:61] op_sel_hi:[1,0]\n"
  "v_pk_mul_f32 v[24:25], v[24:25], s[60:61] op_sel_hi:[1,0]\n"
  "v_pk_mul_f32 v[26:27], v[26:27], s[60:61] op_sel_hi:[1,0]\n"
  "v_pk_mul_f32 v[28:29], v[28:29], s[60:61] op_sel_hi:[1,0]\n"
  "v_pk_mul_f32 v[30:31], v[30:31], s[60:61] op_sel_hi:[1,0]\n"
  "v_mfma_f32_16x16x32_bf16 v[4:7], v[52:55], a[236:239], v[92:95]\n"
  "v_exp_f32 v16, v16\n"
  "v_mfma_f32_16x16x32_bf16 v[4:7], v[48:51], a[232:235], v[4:7]\n"
  "v_exp_f32 v20, v20\n"
  "v_mfma_f32_16x16x32_bf16 v[4:7], v[44:47], a[228:231], v[4:7]\n"
  "v_exp_f32 v17, v17\n"
  "v_mfma_f32_16x16x32_bf16 v[4:7], v[40:43], a[224:227], v[4:7]\n"
  "v_exp_f32 v21, v21\n"
  "v_mfma_f32_16x16x32_bf16 v[4:7], v[36:39], a[220:223], v[4:7]\n"
  "v_cvt_pk_bf16_f32 v16, v16, v20\n"
  "v_cvt_pk_bf16_f32 v17, v17, v21\n"
  "v_mfma_f32_16x16x32_bf16 v[4:7], v[32:35], a[216:219], v[4:7]\n"
  "ds_write_addtid_b32 v16 offset:512\n"
  "ds_write_addtid_b32 v17 offset:4608\n"
  "v_mfma_f32_16x16x32_bf16 v[8:11], v[32:35], a[240:243], v[92:95]\n"
  "v_exp_f32 v18, v18\n"
  "v_mfma_f32_16x16x32_bf16 v[8:11], v[36:39], a[244:247], v[8:11]\n"
  "v_exp_f32 v22, v22\n"
  "v_mfma_f32_16x16x32_bf16 v[8:11], v[40:43], a[248:251], v[8:11]\n"
  "v_exp_f32 v19, v19\n"
  "v_mfma_f32_16x16x32_bf16 v[8:11], v[44:47], a[252:255], v[8:11]\n"
  "v_exp_f32 v23, v23\n"
  "v_mfma_f32_16x16x32_bf16 v[8:11], v[48:51], v[128:131], v[8:11]\n"
  "v_cvt_pk_bf16_f32 v18, v18, v22\n"
  "v_cvt_pk_bf16_f32 v19, v19, v23\n"
  "v_mfma_f32_16x16x32_bf16 v[8:11], v[52:55], v[132:135], v[8:11]\n"
  "ds_write_addtid_b32 v18 offset:8736\n"
  "ds_write_addtid_b32 v19 offset:12832\n"
  "v_mfma_f32_16x16x32_bf16 v[12:15], v[52:55], v[156:159], v[92:95]\n"
  "v_exp_f32 v24, v24\n"
  "v_mfma_f32_16x16x32_bf16 v[12:15], v[48:51], v[152:155], v[12:15]\n"
  "v_exp_f32 v28, v28\n"
  "v_mfma_f32_16x16x32_bf16 v[12:15], v[44:47], v[148:151], v[12:15]\n"
  "v_exp_f32 v25, v25\n"
  "v_mfma_f32_16x16x32_bf16 v[12:15], v[40:43], v[144:147], v[12:15]\n"
  "v_exp_f32 v29, v29\n"
  "v_mfma_f32_16x16x32_bf16 v[12:15], v[36:39], v[140:143], v[12:15]\n"
  "v_cvt_pk_bf16_f32 v24, v24, v28\n"
  "v_cvt_pk_bf16_f32 v25, v25, v29\n"
  "v_mfma_f32_16x16x32_bf16 v[12:15], v[32:35], v[136:139], v[12:15]\n"
  "ds_write_addtid_b32 v24 offset:768\n"
  "ds_write_addtid_b32 v25 offset:4864\n"
  "v_mfma_f32_16x16x32_bf16 v[16:19], v[32:35], v[160:163], v[92:95]\n"
  "v_exp_f32 v26, v26\n"
  "v_mfma_f32_16x16x32_bf16 v[16:19], v[36:39], v[164:167], v[16:19]\n"
  "v_exp_f32 v30, v30\n"
  "v_mfma_f32_16x16x32_bf16 v[16:19], v[40:43], v[168:171], v[16:19]\n"
  "v_exp_f32 v27, v27\n"
  "v_mfma_f32_16x16x32_bf16 v[16:19], v[44:47], v[172:175], v[16:19]\n"
  "v_exp_f32 v31, v31\n"
  "v_mfma_f32_16x16x32_bf16 v[16:19], v[48:51], v[176:179], v[16:19]\n"
  "v_cvt_pk_bf16_f32 v26, v26, v30\n"
  "v_cvt_pk_bf16_f32 v27, v27, v31\n"
  "v_mfma_f32_16x16x32_bf16 v[16:19], v[52:55], v[180:183], v[16:19]\n"
  "s_waitcnt lgkmcnt(15)\n"
  "s_barrier\n"
  "ds_read_b128 v[68:71], v82 offset:2048\n"
  "ds_read_b128 v[72:75], v81 offset:4096\n"
  "ds_read_b128 v[76:79], v82 offset:4096\n"
  "ds_write_addtid_b32 v26 offset:8992\n"
  "ds_write_addtid_b32 v27 offset:13088\n"
  "v_pk_mul_f32 v[0:1], v[0:1], s[60:61] op_sel_hi:[1,0]\n"
  "v_pk_mul_f32 v[2:3], v[2:3], s[60:61] op_sel_hi:[1,0]\n"
  "v_pk_mul_f32 v[4:5], v[4:5], s[60:61] op_sel_hi:[1,0]\n"
  "v_pk_mul_f32 v[6:7], v[6:7], s[60:61] op_sel_hi:[1,0]\n"
  "v_pk_mul_f32 v[8:9], v[8:9], s[60:61] op_sel_hi:[1,0]\n"
  "v_pk_mul_f32 v[10:11], v[10:11], s[60:61] op_sel_hi:[1,0]\n"
  "v_pk_mul_f32 v[12:13], v[12:13], s[60:61] op_sel_hi:[1,0]\n"
  "v_pk_mul_f32 v[14:15], v[14:15], s[60:61] op_sel_hi:[1,0]\n"
  "v_mfma_f32_16x16x32_bf16 v[20:23], v[52:55], v[204:207], v[92:95]\n"
  "v_exp_f32 v0, v0\n"
  "v_mfma_f32_16x16x32_bf16 v[20:23], v[48:51], v[200:203], v[20:23]\n"
  "v_exp_f32 v4, v4\n"
  "v_mfma_f32_16x16x32_bf16 v[20:23], v[44:47], v[196:199], v[20:23]\n"
  "v_exp_f32 v1, v1\n"
  "v_mfma_f32_16x16x32_bf16 v[20:23], v[40:43], v[192:195], v[20:23]\n"
  "v_exp_f32 v5, v5\n"
  "v_mfma_f32_16x16x32_bf16 v[20:23], v[36:39], v[188:191], v[20:23]\n"
  "v_cvt_pk_bf16_f32 v0, v0, v4\n"
  "v_cvt_pk_bf16_f32 v1, v1, v5\n"
  "v_mfma_f32_16x16x32_bf16 v[20:23], v[32:35], v[184:187], v[20:23]\n"
  "ds_write_addtid_b32 v0 offset:1024\n"
  "ds_write_addtid_b32 v1 offset:5120\n"
  "v_mfma_f32_16x16x32_bf16 v[24:27], v[32:35], v[208:211], v[92:95]\n"
  "v_exp_f32 v2, v2\n"
  "v_mfma_f32_16x16x32_bf16 v[24:27], v[36:39], v[212:215], v[24:27]\n"
  "v_exp_f32 v6, v6\n"
  "v_mfma_f32_16x16x32_bf16 v[24:27], v[40:43], v[216:219], v[24:27]\n"
  "v_exp_f32 v3, v3\n"
  "v_mfma_f32_16x16x32_bf16 v[24:27], v[44:47], v[220:223], v[24:27]\n"
  "v_exp_f32 v7, v7\n"
  "v_mfma_f32_16x16x32_bf16 v[24:27], v[48:51], v[224:227], v[24:27]\n"
  "v_cvt_pk_bf16_f32 v2, v2, v6\n"
  "v_cvt_pk_bf16_f32 v3, v3, v7\n"
  "v_mfma_f32_16x16x32_bf16 v[24:27], v[52:55], v[228:231], v[24:27]\n"
  "ds_write_addtid_b32 v2 offset:9248\n"
  "ds_write_addtid_b32 v3 offset:13344\n"
  "v_mfma_f32_16x16x32_bf16 v[28:31], v[52:55], v[252:255], v[92:95]\n"
  "v_exp_f32 v8, v8\n"
  "v_mfma_f32_16x16x32_bf16 v[28:31], v[48:51], v[248:251], v[28:31]\n"
  "v_exp_f32 v12, v12\n"
  "v_mfma_f32_16x16x32_bf16 v[28:31], v[44:47], v[244:247], v[28:31]\n"
  "v_exp_f32 v9, v9\n"
  "v_mfma_f32_16x16x32_bf16 v[28:31], v[40:43], v[240:243], v[28:31]\n"
  "s_sub_u32 s56, s56, 1\n"
  "v_exp_f32 v13, v13\n"
  "v_mfma_f32_16x16x32_bf16 v[28:31], v[36:39], v[236:239], v[28:31]\n"
  "v_cvt_pk_bf16_f32 v8, v8, v12\n"
  "v_cvt_pk_bf16_f32 v9, v9, v13\n"
  "v_mfma_f32_16x16x32_bf16 v[28:31], v[32:35], v[232:235], v[28:31]\n"
  "s_waitcnt lgkmcnt(6)\n"
  "ds_write_addtid_b32 v8 offset:1280\n"
  "ds_write_addtid_b32 v9 offset:5376\n"
  ".p2align 3\n"
  "A_N1_%=:\n"
  "v_mfma_f32_16x16x32_bf16 v[0:3], v[56:59], a[0:3], v[92:95]\n"
  "s_add_u32 s66, s66, 6144\n"
  "v_exp_f32 v10, v10\n"
  "v_mfma_f32_16x16x32_bf16 v[0:3], v[60:63], a[4:7], v[0:3]\n"
  "s_cmp_eq_u32 s66, 102400\n"
  "v_exp_f32 v14, v14\n"
  "v_mfma_f32_16x16x32_bf16 v[0:3], v[64:67], a[8:11], v[0:3]\n"
  "s_cselect_b32 s66, 65536, s66\n"
  "v_exp_f32 v11, v11\n"
  "v_mfma_f32_16x16x32_bf16 v[0:3], v[68:71], a[12:15], v[0:3]\n"
  "v_exp_f32 v15, v15\n"
  "v_mfma_f32_16x16x32_bf16 v[0:3], v[72:75], a[16:19], v[0:3]\n"
  "v_cvt_pk_bf16_f32 v10, v10, v14\n"
  "v_cvt_pk_bf16_f32 v11, v11, v15\n"
  "v_mfma_f32_16x16x32_bf16 v[0:3], v[76:79], a[20:23], v[0:3]\n"
  "ds_write_addtid_b32 v10 offset:9504\n"
  "ds_write_addtid_b32 v11 offset:13600\n"
  "v_add_u32 v81, s66, v83\n"
  "v_add_u32 v82, s66, v84\n"
  "v_pk_mul_f32 v[16:17], v[16:17], s[60:61] op_sel_hi:[1,0]\n"
  "v_pk_mul_f32 v[18:19], v[18:19], s[60:61] op_sel_hi:[1,0]\n"
  "v_pk_mul_f32 v[20:21], v[20:21], s[60:61] op_sel_hi:[1,0]\n"
  "v_pk_mul_f32 v[22:23], v[22:23], s[60:61] op_sel_hi:[1,0]\n"
  "v_pk_mul_f32 v[24:25], v[24:25], s[60:61] op_sel_hi:[1,0]\n"
  "v_pk_mul_f32 v[26:27], v[26:27], s[60:61] op_sel_hi:[1,0]\n"
  "v_pk_mul_f32 v[28:29], v[28:29], s[60:61] op_sel_hi:[1,0]\n"
  "v_pk_mul_f32 v[30:31], v[30:31], s[60:61] op_sel_hi:[1,0]\n"
  "v_mfma_f32_16x16x32_bf16 v[4:7], v[76:79], a[44:47], v[92:95]\n"
  "v_exp_f32 v16, v16\n"
  "v_mfma_f32_16x16x32_bf16 v[4:7], v[72:75], a[40:43], v[4:7]\n"
  "v_exp_f32 v20, v20\n"
  "v_mfma_f32_16x16x32_bf16 v[4:7], v[68:71], a[36:39], v[4:7]\n"
  "v_exp_f32 v17, v17\n"
  "v_mfma_f32_16x16x32_bf16 v[4:7], v[64:67], a[32:35], v[4:7]\n"
  "v_exp_f32 v21, v21\n"
  "v_mfma_f32_16x16x32_bf16 v[4:7], v[60:63], a[28:31], v[4:7]\n"
  "v_cvt_pk_bf16_f32 v16, v16, v20\n"
  "v_cvt_pk_bf16_f32 v17, v17, v21\n"
  "v_mfma_f32_16x16x32_bf16 v[4:7], v[56:59], a[24:27], v[4:7]\n"
  "ds_write_addtid_b32 v16 offset:1536\n"
  "ds_write_addtid_b32 v17 offset:5632\n"
  "v_mfma_f32_16x16x32_bf16 v[8:11], v[56:59], a[48:51], v[92:95]\n"
  "v_exp_f32 v18, v18\n"
  "v_mfma_f32_16x16x32_bf16 v[8:11], v[60:63], a[52:55], v[8:11]\n"
  "v_exp_f32 v22, v22\n"
  "v_mfma_f32_16x16x32_bf16 v[8:11], v[64:67], a[56:59], v[8:11]\n"
  "v_exp_f32 v19, v19\n"
  "v_mfma_f32_16x16x32_bf16 v[8:11], v[68:71], a[60:63], v[8:11]\n"
  "v_exp_f32 v23, v23\n"
  "v_mfma_f32_16x16x32_bf16 v[8:11], v[72:75], a[64:67], v[8:11]\n"
  "v_cvt_pk_bf16_f32 v18, v18, v22\n"
  "v_cvt_pk_bf16_f32 v19, v19, v23\n"
  "v_mfma_f32_16x16x32_bf16 v[8:11], v[76:79], a[68:71], v[8:11]\n"
  "ds_write_addtid_b32 v18 offset:9760\n"
  "ds_write_addtid_b32 v19 offset:13856\n"
  "v_mfma_f32_16x16x32_bf16 v[12:15], v[76:79], a[92:95], v[92:95]\n"
  "v_exp_f32 v24, v24\n"
  "v_mfma_f32_16x16x32_bf16 v[12:15], v[72:75], a[88:91], v[12:15]\n"
  "v_exp_f32 v28, v28\n"
  "v_mfma_f32_16x16x32_bf16 v[12:15], v[68:71], a[84:87], v[12:15]\n"
  "v_exp_f32 v25, v25\n"
  "v_mfma_f32_16x16x32_bf16 v[12:15], v[64:67], a[80:83], v[12:15]\n"
  "v_exp_f32 v29, v29\n"
  "v_mfma_f32_16x16x32_bf16 v[12:15], v[60:63], a[76:79], v[12:15]\n"
  "v_cvt_pk_bf16_f32 v24, v24, v28\n"
  "v_cvt_pk_bf16_f32 v25, v25, v29\n"
  "v_mfma_f32_16x16x32_bf16 v[12:15], v[56:59], a[72:75], v[12:15]\n"
  "ds_write_addtid_b32 v24 offset:1792\n"
  "ds_write_addtid_b32 v25 offset:5888\n"
  "v_mfma_f32_16x16x32_bf16 v[16:19], v[56:59], a[96:99], v[92:95]\n"
  "v_exp_f32 v26, v26\n"
  "v_mfma_f32_16x16x32_bf16 v[16:19], v[60:63], a[100:103], v[16:19]\n"
  "v_exp_f32 v30, v30\n"
  "v_mfma_f32_16x16x32_bf16 v[16:19], v[64:67], a[104:107], v[16:19]\n"
  "v_exp_f32 v27, v27\n"
  "v_mfma_f32_16x16x32_bf16 v[16:19], v[68:71], a[108:111], v[16:19]\n"
  "v_exp_f32 v31, v31\n"
  "v_mfma_f32_16x16x32_bf16 v[16:19], v[72:75], a[112:115], v[16:19]\n"
  "v_cvt_pk_bf16_f32 v26, v26, v30\n"
  "v_cvt_pk_bf16_f32 v27, v27, v31\n"
  "v_mfma_f32_16x16x32_bf16 v[16:19], v[76:79], a[116:119], v[16:19]\n"
  "ds_write_addtid_b32 v26 offset:10016\n"
  "ds_write_addtid_b32 v27 offset:14112\n"
  "v_pk_mul_f32 v[0:1], v[0:1], s[60:61] op_sel_hi:[1,0]\n"
  "v_pk_mul_f32 v[2:3], v[2:3], s[60:61] op_sel_hi:[1,0]\n"
  "v_pk_mul_f32 v[4:5], v[4:5], s[60:61] op_sel_hi:[1,0]\n"
  "v_pk_mul_f32 v[6:7], v[6:7], s[60:61] op_sel_hi:[1,0]\n"
  "v_pk_mul_f32 v[8:9], v[8:9], s[60:61] op_sel_hi:[1,0]\n"
  "v_pk_mul_f32 v[10:11], v[10:11], s[60:61] op_sel_hi:[1,0]\n"
  "v_pk_mul_f32 v[12:13], v[12:13], s[60:61] op_sel_hi:[1,0]\n"
  "v_pk_mul_f32 v[14:15], v[14:15], s[60:61] op_sel_hi:[1,0]\n"
  "v_mfma_f32_16x16x32_bf16 v[20:23], v[76:79], a[140:143], v[92:95]\n"
  "s_add_u32 s88, s88, 16448\n"
  "v_exp_f32 v0, v0\n"
  "v_mfma_f32_16x16x32_bf16 v[20:23], v[72:75], a[136:139], v[20:23]\n"
  "s_cmp_eq_u32 s88, s68\n"
  "v_exp_f32 v4, v4\n"
  "v_mfma_f32_16x16x32_bf16 v[20:23], v[68:71], a[132:135], v[20:23]\n"
  "s_cselect_b32 s88, s89, s88\n"
  "v_exp_f32 v1, v1\n"
  "v_mfma_f32_16x16x32_bf16 v[20:23], v[64:67], a[128:131], v[20:23]\n"
  "v_exp_f32_e64 v5, v5\n"
  "v_mfma_f32_16x16x32_bf16 v[20:23], v[60:63], a[124:127], v[20:23]\n"
  "s_mov_b32 m0, s88\n"
  "v_cvt_pk_bf16_f32 v0, v0, v4\n"
  "v_cvt_pk_bf16_f32 v1, v1, v5\n"
  "v_mfma_f32_16x16x32_bf16 v[20:23], v[56:59], a[120:123], v[20:23]\n"
  "ds_write_addtid_b32 v0 offset:0\n"
  "ds_write_addtid_b32 v1 offset:4096\n"
  "v_mfma_f32_16x16x32_bf16 v[24:27], v[56:59], a[144:147], v[92:95]\n"
  "v_exp_f32 v2, v2\n"
  "v_mfma_f32_16x16x32_bf16 v[24:27], v[60:63], a[148:151], v[24:27]\n"
  "v_exp_f32 v6, v6\n"
  "v_mfma_f32_16x16x32_bf16 v[24:27], v[64:67], a[152:155], v[24:27]\n"
  "v_exp_f32 v3, v3\n"
  "v_mfma_f32_16x16x32_bf16 v[24:27], v[68:71], a[156:159], v[24:27]\n"
  "v_exp_f32 v7, v7\n"
  "v_mfma_f32_16x16x32_bf16 v[24:27], v[72:75], a[160:163], v[24:27]\n"
  "v_cvt_pk_bf16_f32 v2, v2, v6\n"
  "v_cvt_pk_bf16_f32 v3, v3, v7\n"
  "v_mfma_f32_16x16x32_bf16 v[24:27], v[76:79], a[164:167], v[24:27]\n"
  "ds_write_addtid_b32 v2 offset:8224\n"
  "ds_write_addtid_b32 v3 offset:12320\n"
  "v_mfma_f32_16x16x32_bf16 v[28:31], v[76:79], a[188:191], v[92:95]\n"
  "v_exp_f32 v8, v8\n"
  "v_mfma_f32_16x16x32_bf16 v[28:31], v[72:75], a[184:187], v[28:31]\n"
  "v_exp_f32 v12, v12\n"
  "v_mfma_f32_16x16x32_bf16 v[28:31], v[68:71], a[180:183], v[28:31]\n"
  "v_exp_f32 v9, v9\n"
  "v_mfma_f32_16x16x32_bf16 v[28:31], v[64:67], a[176:179], v[28:31]\n"
  "v_exp_f32 v13, v13\n"
  "v_mfma_f32_16x16x32_bf16 v[28:31], v[60:63], a[172:175], v[28:31]\n"
  "v_cvt_pk_bf16_f32 v8, v8, v12\n"
  "v_cvt_pk_bf16_f32 v9, v9, v13\n"
  "v_mfma_f32_16x16x32_bf16 v[28:31], v[56:59], a[168:171], v[28:31]\n"
  "ds_write_addtid_b32 v8 offset:256\n"
  "ds_write_addtid_b32 v9 offset:4352\n"
  "v_mfma_f32_16x16x32_bf16 v[0:3], v[56:59], a[192:195], v[92:95]\n"
  "v_exp_f32 v10, v10\n"
  "v_mfma_f32_16x16x32_bf16 v[0:3], v[60:63], a[196:199], v[0:3]\n"
  "v_exp_f32 v14, v14\n"
  "v_mfma_f32_16x16x32_bf16 v[0:3], v[64:67], a[200:203], v[0:3]\n"
  "v_exp_f32 v11, v11\n"
  "v_mfma_f32_16x16x32_bf16 v[0:3], v[68:71], a[204:207], v[0:3]\n"
  "v_exp_f32 v15, v15\n"
  "v_mfma_f32_16x16x32_bf16 v[0:3], v[72:75], a[208:211], v[0:3]\n"
  "v_cvt_pk_bf16_f32 v10, v10, v14\n"
  "v_cvt_pk_bf16_f32 v11, v11, v15\n"
  "v_mfma_f32_16x16x32_bf16 v[0:3], v[76:79], a[212:215], v[0:3]\n"
  "ds_read_b128 v[32:35], v81 offset:0\n"
  "ds_read_b128 v[36:39], v82 offset:0\n"
  "ds_read_b128 v[40:43], v81 offset:2048\n"
  "ds_write_addtid_b32 v10 offset:8480\n"
  "ds_write_addtid_b32 v11 offset:12576\n"
  "v_pk_mul_f32 v[16:17], v[16:17], s[60:61] op_sel_hi:[1,0]\n"
  "v_pk_mul_f32 v[18:19], v[18:19], s[60:61] op_sel_hi:[1,0]\n"
  "v_pk_mul_f32 v[20:21], v[20:21], s[60:61] op_sel_hi:[1,0]\n"
  "v_pk_mul_f32 v[22:23], v[22:23], s[60:61] op_sel_hi:[1,0]\n"
  "v_pk_mul_f32 v[24:25], v[24:25], s[60:61] op_sel_hi:[1,0]\n"
  "v_pk_mul_f32 v[26:27], v[26:27], s[60:61] op_sel_hi:[1,0]\n"
  "v_pk_mul_f32 v[28:29], v[28:29], s[60:61] op_sel_hi:[1,0]\n"
  "v_pk_mul_f32 v[30:31], v[30:31], s[60:61] op_sel_hi:[1,0]\n"
  "v_mfma_f32_16x16x32_bf16 v[4:7], v[76:79], a[236:239], v[92:95]\n"
  "v_exp_f32 v16, v16\n"
  "v_mfma_f32_16x16x32_bf16 v[4:7], v[72:75], a[232:235], v[4:7]\n"
  "v_exp_f32 v20, v20\n"
  "v_mfma_f32_16x16x32_bf16 v[4:7], v[68:71], a[228:231], v[4:7]\n"
  "v_exp_f32 v17, v17\n"
  "v_mfma_f32_16x16x32_bf16 v[4:7], v[64:67], a[224:227], v[4:7]\n"
  "v_exp_f32 v21, v21\n"
  "v_mfma_f32_16x16x32_bf16 v[4:7], v[60:63], a[220:223], v[4:7]\n"
  "v_cvt_pk_bf16_f32 v16, v16, v20\n"
  "v_cvt_pk_bf16_f32 v17, v17, v21\n"
  "v_mfma_f32_16x16x32_bf16 v[4:7], v[56:59], a[216:219], v[4:7]\n"
  "ds_write_addtid_b32 v16 offset:512\n"
  "ds_write_addtid_b32 v17 offset:4608\n"
  "v_mfma_f32_16x16x32_bf16 v[8:11], v[56:59], a[240:243], v[92:95]\n"
  "v_exp_f32 v18, v18\n"
  "v_mfma_f32_16x16x32_bf16 v[8:11], v[60:63], a[244:247], v[8:11]\n"
  "v_exp_f32 v22, v22\n"
  "v_mfma_f32_16x16x32_bf16 v[8:11], v[64:67], a[248:251], v[8:11]\n"
  "v_exp_f32 v19, v19\n"
  "v_mfma_f32_16x16x32_bf16 v[8:11], v[68:71], a[252:255], v[8:11]\n"
  "v_exp_f32 v23, v23\n"
  "v_mfma_f32_16x16x32_bf16 v[8:11], v[72:75], v[128:131], v[8:11]\n"
  "v_cvt_pk_bf16_f32 v18, v18, v22\n"
  "v_cvt_pk_bf16_f32 v19, v19, v23\n"
  "v_mfma_f32_16x16x32_bf16 v[8:11], v[76:79], v[132:135], v[8:11]\n"
  "ds_write_addtid_b32 v18 offset:8736\n"
  "ds_write_addtid_b32 v19 offset:12832\n"
  "v_mfma_f32_16x16x32_bf16 v[12:15], v[76:79], v[156:159], v[92:95]\n"
  "v_exp_f32 v24, v24\n"
  "v_mfma_f32_16x16x32_bf16 v[12:15], v[72:75], v[152:155], v[12:15]\n"
  "v_exp_f32 v28, v28\n"
  "v_mfma_f32_16x16x32_bf16 v[12:15], v[68:71], v[148:151], v[12:15]\n"
  "v_exp_f32 v25, v25\n"
  "v_mfma_f32_16x16x32_bf16 v[12:15], v[64:67], v[144:147], v[12:15]\n"
  "v_exp_f32 v29, v29\n"
  "v_mfma_f32_16x16x32_bf16 v[12:15], v[60:63], v[140:143], v[12:15]\n"
  "v_cvt_pk_bf16_f32 v24, v24, v28\n"
  "v_cvt_pk_bf16_f32 v25, v25, v29\n"
  "v_mfma_f32_16x16x32_bf16 v[12:15], v[56:59], v[136:139], v[12:15]\n"
  "ds_write_addtid_b32 v24 offset:768\n"
  "ds_write_addtid_b32 v25 offset:4864\n"
  "v_mfma_f32_16x16x32_bf16 v[16:19], v[56:59], v[160:163], v[92:95]\n"
  "v_exp_f32 v26, v26\n"
  "v_mfma_f32_16x16x32_bf16 v[16:19], v[60:63], v[164:167], v[16:19]\n"
  "v_exp_f32 v30, v30\n"
  "v_mfma_f32_16x16x32_bf16 v[16:19], v[64:67], v[168:171], v[16:19]\n"
  "v_exp_f32 v27, v27\n"
  "v_mfma_f32_16x16x32_bf16 v[16:19], v[68:71], v[172:175], v[16:19]\n"
  "v_exp_f32 v31, v31\n"
  "v_mfma_f32_16x16x32_bf16 v[16:19], v[72:75], v[176:179], v[16:19]\n"
  "v_cvt_pk_bf16_f32 v26, v26, v30\n"
  "v_cvt_pk_bf16_f32 v27, v27, v31\n"
  "v_mfma_f32_16x16x32_bf16 v[16:19], v[76:79], v[180:183], v[16:19]\n"
  "ds_read_b128 v[44:47], v82 offset:2048\n"
  "ds_read_b128 v[48:51], v81 offset:4096\n"
  "ds_read_b128 v[52:55], v82 offset:4096\n"
  "ds_write_addtid_b32 v26 offset:8992\n"
  "ds_write_addtid_b32 v27 offset:13088\n"
  "v_pk_mul_f32 v[0:1], v[0:1], s[60:61] op_sel_hi:[1,0]\n"
  "v_pk_mul_f32 v[2:3], v[2:3], s[60:61] op_sel_hi:[1,0]\n"
  "v_pk_mul_f32 v[4:5], v[4:5], s[60:61] op_sel_hi:[1,0]\n"
  "v_pk_mul_f32 v[6:7], v[6:7], s[60:61] op_sel_hi:[1,0]\n"
  "v_pk_mul_f32 v[8:9], v[8:9], s[60:61] op_sel_hi:[1,0]\n"
  "v_pk_mul_f32 v[10:11], v[10:11], s[60:61] op_sel_hi:[1,0]\n"
  "v_pk_mul_f32 v[12:13], v[12:13], s[60:61] op_sel_hi:[1,0]\n"
  "v_pk_mul_f32 v[14:15], v[14:15], s[60:61] op_sel_hi:[1,0]\n"
  "v_mfma_f32_16x16x32_bf16 v[20:23], v[76:79], v[204:207], v[92:95]\n"
  "v_exp_f32 v0, v0\n"
  "v_mfma_f32_16x16x32_bf16 v[20:23], v[72:75], v[200:203], v[20:23]\n"
  "v_exp_f32 v4, v4\n"
  "v_mfma_f32_16x16x32_bf16 v[20:23], v[68:71], v[196:199], v[20:23]\n"
  "v_exp_f32 v1, v1\n"
  "v_mfma_f32_16x16x32_bf16 v[20:23], v[64:67], v[192:195], v[20:23]\n"
  "v_exp_f32 v5, v5\n"
  "v_mfma_f32_16x16x32_bf16 v[20:23], v[60:63], v[188:191], v[20:23]\n"
  "v_cvt_pk_bf16_f32 v0, v0, v4\n"
  "v_cvt_pk_bf16_f32 v1, v1, v5\n"
  "v_mfma_f32_16x16x32_bf16 v[20:23], v[56:59], v[184:187], v[20:23]\n"
  "ds_write_addtid_b32 v0 offset:1024\n"
  "ds_write_addtid_b32 v1 offset:5120\n"
  "v_mfma_f32_16x16x32_bf16 v[24:27], v[56:59], v[208:211], v[92:95]\n"
  "v_exp_f32 v2, v2\n"
  "v_mfma_f32_16x16x32_bf16 v[24:27], v[60:63], v[212:215], v[24:27]\n"
  "v_exp_f32 v6, v6\n"
  "v_mfma_f32_16x16x32_bf16 v[24:27], v[64:67], v[216:219], v[24:27]\n"
  "v_exp_f32 v3, v3\n"
  "v_mfma_f32_16x16x32_bf16 v[24:27], v[68:71], v[220:223], v[24:27]\n"
  "v_exp_f32 v7, v7\n"
  "v_mfma_f32_16x16x32_bf16 v[24:27], v[72:75], v[224:227], v[24:27]\n"
  "v_cvt_pk_bf16_f32 v2, v2, v6\n"
  "v_cvt_pk_bf16_f32 v3, v3, v7\n"
  "v_mfma_f32_16x16x32_bf16 v[24:27], v[76:79], v[228:231], v[24:27]\n"
  "ds_write_addtid_b32 v2 offset:9248\n"
  "ds_write_addtid_b32 v3 offset:13344\n"
  "v_mfma_f32_16x16x32_bf16 v[28:31], v[76:79], v[252:255], v[92:95]\n"
  "v_exp_f32 v8, v8\n"
  "v_mfma_f32_16x16x32_bf16 v[28:31], v[72:75], v[248:251], v[28:31]\n"
  "v_exp_f32 v12, v12\n"
  "v_mfma_f32_16x16x32_bf16 v[28:31], v[68:71], v[244:247], v[28:31]\n"
  "v_exp_f32 v9, v9\n"
  "v_mfma_f32_16x16x32_bf16 v[28:31], v[64:67], v[240:243], v[28:31]\n"
  "s_sub_u32 s56, s56, 1\n"
  "v_exp_f32_e64 v13, v13\n"
  "v_mfma_f32_16x16x32_bf16 v[28:31], v[60:63], v[236:239], v[28:31]\n"
  "v_cvt_pk_bf16_f32 v8, v8, v12\n"
  "v_cvt_pk_bf16_f32 v9, v9, v13\n"
  "v_mfma_f32_16x16x32_bf16 v[28:31], v[56:59], v[232:235], v[28:31]\n"
  "s_waitcnt lgkmcnt(6)\n"
  "ds_write_addtid_b32 v8 offset:1280\n"
  "ds_write_addtid_b32 v9 offset:5376\n"
  "s_branch A_H0_%=\n"
  "A_LAST_%=:\n"
  "v_exp_f32 v10, v10\n"
  "v_exp_f32 v14, v14\n"
  "s_nop 0\n"
  "v_cvt_pk_bf16_f32 v10, v10, v14\n"
  "v_exp_f32 v11, v11\n"
  "v_exp_f32 v15, v15\n"
  "ds_write_addtid_b32 v10 offset:9504\n"
  "v_cvt_pk_bf16_f32 v11, v11, v15\n"
  "v_pk_mul_f32 v[16:17], v[16:17], s[60:61] op_sel_hi:[1,0]\n"
  "v_pk_mul_f32 v[18:19], v[18:19], s[60:61] op_sel_hi:[1,0]\n"
  "v_pk_mul_f32 v[20:21], v[20:21], s[60:61] op_sel_hi:[1,0]\n"
  "v_pk_mul_f32 v[22:23], v[22:23], s[60:61] op_sel_hi:[1,0]\n"
  "v_pk_mul_f32 v[24:25], v[24:25], s[60:61] op_sel_hi:[1,0]\n"
  "v_pk_mul_f32 v[26:27], v[26:27], s[60:61] op_sel_hi:[1,0]\n"
  "v_pk_mul_f32 v[28:29], v[28:29], s[60:61] op_sel_hi:[1,0]\n"
  "v_pk_mul_f32 v[30:31], v[30:31], s[60:61] op_sel_hi:[1,0]\n"
  "v_exp_f32 v16, v16\n"
  "v_exp_f32 v20, v20\n"
  "ds_write_addtid_b32 v11 offset:13600\n"
  "v_cvt_pk_bf16_f32 v16, v16, v20\n"
  "v_exp_f32 v17, v17\n"
  "v_exp_f32 v21, v21\n"
  "ds_write_addtid_b32 v16 offset:1536\n"
  "v_cvt_pk_bf16_f32 v17, v17, v21\n"
  "v_exp_f32 v18, v18\n"
  "v_exp_f32 v22, v22\n"
  "ds_write_addtid_b32 v17 offset:5632\n"
  "v_cvt_pk_bf16_f32 v18, v18, v22\n"
  "v_exp_f32 v19, v19\n"
  "v_exp_f32 v23, v23\n"
  "ds_write_addtid_b32 v18 offset:9760\n"
  "v_cvt_pk_bf16_f32 v19, v19, v23\n"
  "v_exp_f32 v24, v24\n"
  "v_exp_f32 v28, v28\n"
  "ds_write_addtid_b32 v19 offset:13856\n"
  "v_cvt_pk_bf16_f32 v24, v24, v28\n"
  "v_exp_f32 v25, v25\n"
  "v_exp_f32 v29, v29\n"
  "ds_write_addtid_b32 v24 offset:1792\n"
  "v_cvt_pk_bf16_f32 v25, v25, v29\n"
  "v_exp_f32 v26, v26\n"
  "v_exp_f32 v30, v30\n"
  "ds_write_addtid_b32 v25 offset:5888\n"
  "v_cvt_pk_bf16_f32 v26, v26, v30\n"
  "v_exp_f32 v27, v27\n"
  "v_exp_f32 v31, v31\n"
  "ds_write_addtid_b32 v26 offset:10016\n"
  "v_cvt_pk_bf16_f32 v27, v27, v31\n"
  "ds_write_addtid_b32 v27 offset:14112\n"
  "s_waitcnt lgkmcnt(0)\n"
  "s_barrier\n"
  "s_mov_b32 %0, 0\n"
  "s_branch L_END_%=\n"
  "B_START_%=:\n"
  "v_and_b32 v3, 2, v2\n"
  "v_and_b32 v112, 4, v2\n"
  "v_lshl_add_u32 v112, v3, 3, v112\n"
  "v_and_b32 v3, 1, v2\n"
  "v_lshl_add_u32 v112, v3, 1, v112\n"
  "v_mul_u32_u24 v112, 0x180, v112\n"
  "v_lshl_add_u32 v112, v1, 4, v112\n"
  "v_add_u32 v113, 0x800, v112\n"
  "s_mov_b32 s43, 0x20000\n"
  "s_sub_u32 s62, 53, s75\n"
  "s_bitcmp1_b32 %3, 3\n"
  "s_cselect_b32 s52, s62, s75\n"
  "s_cselect_b32 s54, s75, s62\n"
  "s_cselect_b32 s62, s75, 0\n"
  "s_lshl_b32 s62, s62, 8\n"
  "s_or_b32 s52, s52, s62\n"
  "s_pack_ll_b32_b16 s52, s52, s86\n"
  "s_add_u32 s62, %3, 128\n"
  "s_pack_ll_b32_b16 s53, 53, s62\n"
  "s_bitcmp1_b32 %3, 0\n"
  "s_cselect_b32 s62, -2, 2\n"
  "s_add_u32 s54, s54, s62\n"
  "s_sub_u32 s62, 53, s54\n"
  "s_lshl_b32 s62, s62, 8\n"
  "s_bfe_u32 s63, %3, 0x10003\n"
  "s_cselect_b32 s62, s62, 0\n"
  "s_or_b32 s54, s54, s62\n"
  "s_xor_b32 s63, s63, s86\n"
  "s_add_u32 s63, s63, 384\n"
  "s_pack_ll_b32_b16 s54, s54, s63\n"
  "s_mov_b32 s55, 0\n"
  "s_sub_u32 s40, s80, 2048\n"
  "s_subb_u32 s41, s81, 0\n"
  "s_mov_b32 s42, 100665344\n"
  "s_mov_b64 s[48:49], s[82:83]\n"
  "s_mov_b32 s50, 166723584\n"
  "s_mov_b32 s51, 0x20000\n"
  "s_lshr_b32 s62, s52, 16\n"
  "s_lshl1_add_u32 s62, s62, s65\n"
  "s_mul_i32 s57, s62, 98304\n"
  "s_and_b32 s56, s52, 0xff\n"
  "s_cmp_lg_u32 s53, 0\n"
  "s_cselect_b32 s62, 6, 0\n"
  "s_sub_u32 s56, s56, s62\n"
  "s_mov_b32 s88, 16448\n"
  "s_mov_b32 s89, 16448\n"
  "s_mov_b32 s68, 0xf0000000\n"
  "s_mov_b32 s69, 0\n"
  "s_sub_u32 s70, s67, s68\n"
  "s_mov_b32 s64, s67\n"
  "s_and_b32 s90, s52, 0xff\n"
  "s_sub_u32 s90, s90, 1\n"
  "s_mul_i32 s91, s90, 6144\n"
  "s_add_u32 s91, s91, s64\n"
  "s_lshr_b32 s62, s53, 16\n"
  "s_mul_i32 s62, s62, 53\n"
  "s_bfe_u32 s63, s53, 0x80008\n"
  "s_add_u32 s62, s62, s63\n"
  "s_mul_i32 s64, s62, 6144\n"
  "s_cmp_eq_u32 s53, 0\n"
  "s_cmov_b32 s64, 0xf0000000\n"
  "s_sub_u32 s91, s64, s91\n"
  "s_mov_b32 s96, s91\n"
  "s_and_b32 s92, s53, 0xff\n"
  "s_sub_u32 s92, s92, 1\n"
  "s_mul_i32 s93, s92, 6144\n"
  "s_add_u32 s93, s93, s64\n"
  "s_lshr_b32 s62, s54, 16\n"
  "s_mul_i32 s62, s62, 53\n"
  "s_bfe_u32 s63, s54, 0x80008\n"
  "s_add_u32 s62, s62, s63\n"
  "s_mul_i32 s64, s62, 6144\n"
  "s_cmp_eq_u32 s54, 0\n"
  "s_cmov_b32 s64, 0xf0000000\n"
  "s_sub_u32 s93, s64, s93\n"
  "s_mov_b32 s97, s93\n"
  "s_and_b32 s94, s54, 0xff\n"
  "s_sub_u32 s94, s94, 1\n"
  "s_mul_i32 s95, s94, 6144\n"
  "s_add_u32 s95, s95, s64\n"
  "s_lshr_b32 s62, s55, 16\n"
  "s_mul_i32 s62, s62, 53\n"
  "s_bfe_u32 s63, s55, 0x80008\n"
  "s_add_u32 s62, s62, s63\n"
  "s_mul_i32 s64, s62, 6144\n"
  "s_cmp_eq_u32 s55, 0\n"
  "s_cmov_b32 s64, 0xf0000000\n"
  "s_sub_u32 s95, s64, s95\n"
  "s_add_u32 s66, s66, 6144\n"
  "s_cmp_ge_u32 s66, 102400\n"
  "s_cselect_b32 s64, 36864, 0\n"
  "s_sub_u32 s66, s66, s64\n"
  "s_sub_u32 s90, s90, 1\n"
  "s_cselect_b32 s63, s91, 6144\n"
  "s_cmov_b64 s[90:91], s[92:93]\n"
  "s_cmov_b64 s[92:93], s[94:95]\n"
  "s_add_u32 s67, s67, s63\n"
  "s_add_u32 s66, s66, 6144\n"
  "s_cmp_ge_u32 s66, 102400\n"
  "s_cselect_b32 s64, 36864, 0\n"
  "s_sub_u32 s66, s66, s64\n"
  "s_sub_u32 s90, s90, 1\n"
  "s_cselect_b32 s63, s91, 6144\n"
  "s_cmov_b64 s[90:91], s[92:93]\n"
  "s_cmov_b64 s[92:93], s[94:95]\n"
  "s_add_u32 s67, s67, s63\n"
  "v_lshrrev_b32 v1, 3, v0\n"
  "v_mul_u32_u24 v114, 0x180, v1\n"
  "v_lshrrev_b32 v1, 4, v0\n"
  "v_and_b32 v2, 7, v0\n"
  "v_xor_b32 v2, v2, v1\n"
  "v_lshl_add_u32 v114, v2, 4, v114\n"
  "s_lshl_b32 s62, s65, 8\n"
  "s_add_u32 s62, s62, 2048\n"
  "v_add_u32 v114, s62, v114\n"
  "v_add_u32 v115, 0x800, v114\n"
  "v_lshrrev_b32 v2, 2, v0\n"
  "v_mul_u32_u24 v116, 0x180, v2\n"
  "v_and_b32 v1, 1, v1\n"
  "v_and_b32 v2, 3, v0\n"
  "v_xor_b32 v2, v2, v1\n"
  "v_lshl_add_u32 v116, v2, 4, v116\n"
  "s_lshl_b32 s62, s65, 6\n"
  "s_add_u32 s62, s62, 128\n"
  "v_add_u32 v116, s62, v116\n"
  "s_mul_i32 s85, s65, 86016\n"
  "s_sub_u32 s57, s57, s85\n"
  "s_add_u32 s85, s85, 12672\n"
  "s_mul_i32 s60, s65, 5120\n"
  "s_add_u32 s60, s60, 12288\n"
  "s_mul_i32 s84, s65, 24576\n"
  "s_sub_u32 s58, 6144, s84\n"
  "s_add_u32 s84, s84, 6144\n"
  "s_cmp_eq_u32 s65, 0\n"
  "s_cselect_b64 s[80:81], s[78:79], s[80:81]\n"
  "s_mov_b32 s82, 100663296\n"
  "s_mov_b32 s83, 0x20000\n"
  "s_mov_b64 s[76:77], s[78:79]\n"
  "s_mov_b32 s78, 100663296\n"
  "s_mov_b32 s79, 0x20000\n"
  "v_lshrrev_b32 v1, 2, v0\n"
  "v_lshlrev_b32 v117, 7, v1\n"
  "v_lshlrev_b32 v121, 6, v1\n"
  "v_and_b32 v2, 1, v0\n"
  "v_lshl_add_u32 v117, v2, 3, v117\n"
  "v_lshl_add_u32 v121, v2, 3, v121\n"
  "v_bfe_u32 v2, v0, 1, 1\n"
  "v_lshl_add_u32 v117, v2, 6, v117\n"
  "v_lshl_add_u32 v121, v2, 5, v121\n"
  "v_bfe_u32 v2, v0, 3, 2\n"
  "v_lshl_add_u32 v117, v2, 4, v117\n"
  "v_bfe_u32 v2, v0, 4, 1\n"
  "v_lshl_add_u32 v121, v2, 4, v121\n"
  "v_add_u32 v117, s59, v117\n"
  "v_add_u32 v121, s59, v121\n"
  "v_xor_b32 v118, 16, v117\n"
  "v_xor_b32 v119, 32, v117\n"
  "v_xor_b32 v120, 48, v117\n"
  "v_xor_b32 v122, 16, v121\n"
  "v_and_b32 v1, 1, v0\n"
  "v_mul_u32_u24 v104, 8224, v1\n"
  "v_add_u32 v1, 4, v0\n"
  "v_bfe_u32 v1, v1, 3, 1\n"
  "v_lshl_add_u32 v104, v1, 12, v104\n"
  "v_bfe_u32 v1, v0, 1, 2\n"
  "v_lshl_add_u32 v104, v1, 6, v104\n"
  "v_lshrrev_b32 v1, 4, v0\n"
  "v_lshl_add_u32 v104, v1, 4, v104\n"
  "s_lshl_b32 s62, s65, 8\n"
  "s_sub_u32 s63, 16384, s62\n"
  "s_add_u32 s62, s62, 16128\n"
  "v_add_u32 v105, s63, v104\n"
  "v_add_u32 v104, s62, v104\n"
  "v_lshrrev_b32 v1, 5, v0\n"
  "v_mul_u32_u24 v1, 0x480, v1\n"
  "v_and_b32 v2, 31, v0\n"
  "v_lshl_add_u32 v106, v2, 3, v1\n"
  "s_mul_i32 s62, s65, 4608\n"
  "s_add_u32 s62, s62, 0\n"
  "v_add_u32 v106, s62, v106\n"
  "v_and_b32 v2, 7, v0\n"
  "v_lshl_add_u32 v107, v2, 3, v1\n"
  "v_bfe_u32 v2, v0, 3, 1\n"
  "v_lshl_add_u32 v107, v2, 2, v107\n"
  "v_bfe_u32 v2, v0, 4, 1\n"
  "v_lshl_add_u32 v107, v2, 7, v107\n"
  "s_lshl_b32 s62, s65, 6\n"
  "s_add_u32 s62, s62, 2304\n"
  "v_add_u32 v107, s62, v107\n"
  "v_lshrrev_b32 v1, 4, v0\n"
  "v_mul_u32_u24 v108, 0x120, v1\n"
  "v_and_b32 v2, 3, v0\n"
  "v_lshl_add_u32 v108, v2, 6, v108\n"
  "v_bfe_u32 v2, v0, 2, 2\n"
  "v_lshl_add_u32 v108, v2, 3, v108\n"
  "s_mul_i32 s62, s65, 1152\n"
  "s_add_u32 s62, s62, 8192\n"
  "v_add_u32 v108, s62, v108\n"
  "v_bfe_u32 v3, v0, 3, 1\n"
  "v_lshl_add_u32 v2, v3, 2, v1\n"
  "v_xor_b32 v3, s65, v3\n"
  "v_lshl_add_u32 v2, v2, 1, v3\n"
  "v_mul_u32_u24 v109, 0x180, v2\n"
  "v_and_b32 v2, 7, v0\n"
  "v_lshl_add_u32 v109, v2, 4, v109\n"
  "s_mov_b32 s62, 15104\n"
  "v_lshl_add_u32 v111, v1, 4, s62\n"
  "s_lshl_b32 s62, s65, 6\n"
  "s_add_u32 s62, s62, 6912\n"
  "v_lshl_add_u32 v110, v0, 2, s62\n"
  "v_mov_b32 v3, 0x40000\n"
  "v_cmp_gt_u32 vcc, 16, v0\n"
  "s_nop 1\n"
  "v_cndmask_b32 v110, v3, v110, vcc\n"
  "v_mov_b32 v44, 1.0\n"
  "ds_write_b32 v110, v44 offset:8192\n"
  "v_and_b32 v2, 15, v0\n"
  "v_mov_b32 v124, 0x44444444\n"
  "v_mov_b32 v3, 0xeeeeeeee\n"
  "v_cmp_eq_u32 vcc, 1, v2\n"
  "s_nop 1\n"
  "v_cndmask_b32 v124, v124, v3, vcc\n"
  "v_mov_b32 v3, 0x3f803f80\n"
  "v_cmp_gt_u32 vcc, 2, v2\n"
  "v_mov_b32 v80, 0\n"
  "v_mov_b32 v81, 0\n"
  "v_mov_b32 v82, 0\n"
  "v_mov_b32 v83, 0\n"
  "v_cndmask_b32 v88, 0, v3, vcc\n"
  "v_cndmask_b32 v89, 0, v3, vcc\n"
  "v_cndmask_b32 v90, 0, v3, vcc\n"
  "v_cndmask_b32 v91, 0, v3, vcc\n"
  "s_mov_b32 s99, 0\n"
  "s_waitcnt vmcnt(0)\n"
  "s_barrier\n"
  "v_lshlrev_b32 v125, 7, v0\n"
  "v_mov_b32 v126, 0\n"
  "s_mov_b32 s62, 0x28d22090\n"
  "v_writelane_b32 v126, s62, 2\n"
  "s_mov_b32 s62, 0x39163114\n"
  "v_writelane_b32 v126, s62, 3\n"
  "s_mov_b32 s62, 0x41980000\n"
  "v_writelane_b32 v126, s62, 4\n"
  "s_mov_b32 s62, 0x521c49da\n"
  "v_writelane_b32 v126, s62, 5\n"
  "s_mov_b32 s62, 0x5a1e\n"
  "v_writelane_b32 v126, s62, 6\n"
  "s_mov_b32 s62, 0x6ae262a0\n"
  "v_writelane_b32 v126, s62, 7\n"
  "s_mov_b32 s62, 0x7b267324\n"
  "v_writelane_b32 v126, s62, 8\n"
  "s_mov_b32 s62, 0x8ba80000\n"
  "v_writelane_b32 v126, s62, 9\n"
  "s_mov_b32 s62, 0x9c2c93ea\n"
  "v_writelane_b32 v126, s62, 10\n"
  "s_mov_b32 s62, 0xa4afa46e\n"
  "v_writelane_b32 v126, s62, 11\n"
  "s_mov_b32 s62, 0xb52baced\n"
  "v_writelane_b32 v126, s62, 12\n"
  "s_mov_b32 s62, 0xbd29\n"
  "v_writelane_b32 v126, s62, 13\n"
  "s_mov_b32 s62, 0xcde5c5a7\n"
  "v_writelane_b32 v126, s62, 14\n"
  "s_mov_b32 s62, 0xde21d623\n"
  "v_writelane_b32 v126, s62, 15\n"
  "s_mov_b32 s62, 0xe69f0000\n"
  "v_writelane_b32 v126, s62, 16\n"
  "s_mov_b32 s62, 0xf71beedd\n"
  "v_writelane_b32 v126, s62, 17\n"
  "s_mov_b32 s62, 0xff19\n"
  "v_writelane_b32 v126, s62, 18\n"
  "s_mov_b32 s62, 0x7d50797\n"
  "v_writelane_b32 v126, s62, 19\n"
  "s_mov_b32 s62, 0x110013\n"
  "v_writelane_b32 v126, s62, 20\n"
  "s_mov_b32 s62, 0x28f120b0\n"
  "v_writelane_b32 v126, s62, 29\n"
  "s_mov_b32 s62, 0x39333132\n"
  "v_writelane_b32 v126, s62, 30\n"
  "s_mov_b32 s62, 0x41b40000\n"
  "v_writelane_b32 v126, s62, 31\n"
  "s_mov_b32 s62, 0x523649f5\n"
  "v_writelane_b32 v126, s62, 32\n"
  "s_mov_b32 s62, 0x5a37\n"
  "v_writelane_b32 v126, s62, 33\n"
  "s_mov_b32 s62, 0x6af962b8\n"
  "v_writelane_b32 v126, s62, 34\n"
  "s_mov_b32 s62, 0x7b3b733a\n"
  "v_writelane_b32 v126, s62, 35\n"
  "s_mov_b32 s62, 0x8bbc0000\n"
  "v_writelane_b32 v126, s62, 36\n"
  "s_mov_b32 s62, 0x9c3e93fd\n"
  "v_writelane_b32 v126, s62, 37\n"
  "s_mov_b32 s62, 0xa48fa47f\n"
  "v_writelane_b32 v126, s62, 38\n"
  "s_mov_b32 s62, 0xb50dacce\n"
  "v_writelane_b32 v126, s62, 39\n"
  "s_mov_b32 s62, 0xbd0c\n"
  "v_writelane_b32 v126, s62, 40\n"
  "s_mov_b32 s62, 0xcdcac58b\n"
  "v_writelane_b32 v126, s62, 41\n"
  "s_mov_b32 s62, 0xde08d609\n"
  "v_writelane_b32 v126, s62, 42\n"
  "s_mov_b32 s62, 0xe6870000\n"
  "v_writelane_b32 v126, s62, 43\n"
  "s_mov_b32 s62, 0xf705eec6\n"
  "v_writelane_b32 v126, s62, 44\n"
  "s_mov_b32 s62, 0xff04\n"
  "v_writelane_b32 v126, s62, 45\n"
  "s_mov_b32 s62, 0x7c20783\n"
  "v_writelane_b32 v126, s62, 46\n"
  "s_mov_b32 s62, 0x8000001\n"
  "v_writelane_b32 v126, s62, 47\n"
  "s_sub_u32 s62, 31, s87\n"
  "s_bitcmp1_b32 %3, 3\n"
  "s_cselect_b32 s62, s62, s87\n"
  "s_and_b32 s63, %3, 7\n"
  "s_mul_i32 s63, s63, 24\n"
  "v_mov_b32 v3, 0xffff\n"
  "s_mul_i32 s64, s65, 5\n"
  "v_mov_b32 v6, s63\n"
  "s_and_b32 s63, %3, 7\n"
  "s_xor_b32 s63, s63, 1\n"
  "s_mul_i32 s63, s63, 24\n"
  "s_add_u32 s63, s63, 9216\n"
  "v_mov_b32 v7, s63\n"
  "s_add_u32 s64, s64, 6\n"
  "v_bfe_u32 v2, v126, 0, 6\n"
  "v_mul_u32_u24 v4, 192, v2\n"
  "v_lshrrev_b32 v1, s64, v126\n"
  "v_cmp_gt_u32 vcc, 16, v2\n"
  "s_nop 1\n"
  "v_cndmask_b32 v8, v6, v7, vcc\n"
  "v_bfe_u32 v2, v126, 0, 16\n"
  "v_sub_u32 v1, s62, v1\n"
  "v_and_b32 v1, 31, v1\n"
  "v_cmp_gt_u32 vcc, 24, v1\n"
  "v_add3_u32 v4, v4, v1, v8\n"
  "s_nop 0\n"
  "v_cndmask_b32 v4, v3, v4, vcc\n"
  "v_cmp_ne_u32 vcc, 0, v2\n"
  "s_nop 1\n"
  "v_cndmask_b32 v4, v3, v4, vcc\n"
  "s_add_u32 s64, s64, 16\n"
  "v_bfe_u32 v2, v126, 16, 6\n"
  "v_mul_u32_u24 v5, 192, v2\n"
  "v_lshrrev_b32 v1, s64, v126\n"
  "v_cmp_gt_u32 vcc, 16, v2\n"
  "s_nop 1\n"
  "v_cndmask_b32 v8, v6, v7, vcc\n"
  "v_bfe_u32 v2, v126, 16, 16\n"
  "v_sub_u32 v1, s62, v1\n"
  "v_and_b32 v1, 31, v1\n"
  "v_cmp_gt_u32 vcc, 24, v1\n"
  "v_add3_u32 v5, v5, v1, v8\n"
  "s_nop 0\n"
  "v_cndmask_b32 v5, v3, v5, vcc\n"
  "v_cmp_ne_u32 vcc, 0, v2\n"
  "s_nop 1\n"
  "v_cndmask_b32 v5, v3, v5, vcc\n"
  "v_lshl_or_b32 v126, v5, 16, v4\n"
  "s_mul_i32 s72, s65, -3\n"
  "s_add_u32 s72, s72, 1\n"
  "s_mov_b32 s71, 0x53800000\n"
  "s_barrier\n"
  "s_add_u32 m0, s66, 0\n"
  "s_nop 0\n"
  "buffer_load_dwordx4 v123, s[44:47], s67 offen offset:0 nt lds\n"
  "s_add_u32 m0, s66, 1920\n"
  "s_nop 0\n"
  "buffer_load_dwordx4 v123, s[44:47], s67 offen offset:128 nt lds\n"
  "s_add_u32 m0, s66, 3840\n"
  "s_nop 0\n"
  "buffer_load_dwordx4 v123, s[44:47], s67 offen offset:256 nt lds\n"
  "s_add_u32 s66, s66, 6144\n"
  "s_cmp_ge_u32 s66, 102400\n"
  "s_cselect_b32 s64, 36864, 0\n"
  "s_sub_u32 s66, s66, s64\n"
  "s_sub_u32 s90, s90, 1\n"
  "s_cselect_b32 s63, s91, 6144\n"
  "s_cmov_b64 s[90:91], s[92:93]\n"
  "s_cmov_b64 s[92:93], s[94:95]\n"
  "s_add_u32 s67, s67, s63\n"
  "s_add_u32 m0, s66, 0\n"
  "s_nop 0\n"
  "buffer_load_dwordx4 v123, s[44:47], s67 offen offset:0 nt lds\n"
  "s_add_u32 m0, s66, 1920\n"
  "s_nop 0\n"
  "buffer_load_dwordx4 v123, s[44:47], s67 offen offset:128 nt lds\n"
  "s_add_u32 m0, s66, 3840\n"
  "s_nop 0\n"
  "buffer_load_dwordx4 v123, s[44:47], s67 offen offset:256 nt lds\n"
  "s_add_u32 s66, s66, 6144\n"
  "s_cmp_ge_u32 s66, 102400\n"
  "s_cselect_b32 s64, 36864, 0\n"
  "s_sub_u32 s66, s66, s64\n"
  "s_sub_u32 s90, s90, 1\n"
  "s_cselect_b32 s63, s91, 6144\n"
  "s_cmov_b64 s[90:91], s[92:93]\n"
  "s_cmov_b64 s[92:93], s[94:95]\n"
  "s_add_u32 s67, s67, s63\n"
  "s_add_u32 m0, s66, 0\n"
  "s_nop 0\n"
  "buffer_load_dwordx4 v123, s[44:47], s67 offen offset:0 nt lds\n"
  "s_add_u32 m0, s66, 1920\n"
  "s_nop 0\n"
  "buffer_load_dwordx4 v123, s[44:47], s67 offen offset:128 nt lds\n"
  "s_add_u32 m0, s66, 3840\n"
  "s_nop 0\n"
  "buffer_load_dwordx4 v123, s[44:47], s67 offen offset:256 nt lds\n"
  "s_add_u32 s66, s66, 6144\n"
  "s_cmp_ge_u32 s66, 102400\n"
  "s_cselect_b32 s64, 36864, 0\n"
  "s_sub_u32 s66, s66, s64\n"
  "s_sub_u32 s90, s90, 1\n"
  "s_cselect_b32 s63, s91, 6144\n"
  "s_cmov_b64 s[90:91], s[92:93]\n"
  "s_cmov_b64 s[92:93], s[94:95]\n"
  "s_add_u32 s67, s67, s63\n"
  "s_add_u32 m0, s59, 21504\n"
  "s_nop 0\n"
  "buffer_load_dwordx4 v114, s[40:43], s57 offen offset:0  lds\n"
  "buffer_load_dwordx4 v115, s[40:43], s57 offen offset:1024  lds\n"
  "buffer_load_dwordx4 v116, s[40:43], s57 offen offset:2048  lds\n"
  "s_add_u32 s57, s57, 6144\n"
  "s_add_u32 m0, s59, 24576\n"
  "s_nop 0\n"
  "buffer_load_dwordx4 v114, s[40:43], s57 offen offset:0  lds\n"
  "buffer_load_dwordx4 v115, s[40:43], s57 offen offset:1024  lds\n"
  "buffer_load_dwordx4 v116, s[40:43], s57 offen offset:2048  lds\n"
  "s_add_u32 s57, s57, s58\n"
  "s_add_u32 m0, s59, 27648\n"
  "s_nop 0\n"
  "buffer_load_dwordx4 v114, s[40:43], s57 offen offset:0  lds\n"
  "buffer_load_dwordx4 v115, s[40:43], s57 offen offset:1024  lds\n"
  "buffer_load_dwordx4 v116, s[40:43], s57 offen offset:2048  lds\n"
  "s_add_u32 s57, s57, 6144\n"
  "s_barrier\n"
  "s_add_u32 m0, s59, 0\n"
  "s_nop 0\n"
  "buffer_load_dwordx4 v114, s[40:43], s57 offen offset:0  lds\n"
  "buffer_load_dwordx4 v115, s[40:43], s57 offen offset:1024  lds\n"
  "buffer_load_dwordx4 v116, s[40:43], s57 offen offset:2048  lds\n"
  "s_add_u32 s57, s57, s84\n"
  "s_add_u32 m0, s59, 3072\n"
  "s_nop 0\n"
  "buffer_load_dwordx4 v114, s[40:43], s57 offen offset:0  lds\n"
  "buffer_load_dwordx4 v115, s[40:43], s57 offen offset:1024  lds\n"
  "buffer_load_dwordx4 v116, s[40:43], s57 offen offset:2048  lds\n"
  "s_add_u32 s57, s57, 6144\n"
  "s_add_u32 m0, s59, 6144\n"
  "s_nop 0\n"
  "buffer_load_dwordx4 v114, s[40:43], s57 offen offset:0  lds\n"
  "buffer_load_dwordx4 v115, s[40:43], s57 offen offset:1024  lds\n"
  "buffer_load_dwordx4 v116, s[40:43], s57 offen offset:2048  lds\n"
  "s_add_u32 s57, s57, s58\n"
  "s_add_u32 m0, s59, 9216\n"
  "s_nop 0\n"
  "buffer_load_dwordx4 v114, s[40:43], s57 offen offset:0  lds\n"
  "buffer_load_dwordx4 v115, s[40:43], s57 offen offset:1024  lds\n"
  "buffer_load_dwordx4 v116, s[40:43], s57 offen offset:2048  lds\n"
  "s_add_u32 s57, s57, 6144\n"
  "s_add_u32 m0, s59, 12288\n"
  "s_nop 0\n"
  "buffer_load_dwordx4 v114, s[40:43], s57 offen offset:0  lds\n"
  "buffer_load_dwordx4 v115, s[40:43], s57 offen offset:1024  lds\n"
  "buffer_load_dwordx4 v116, s[40:43], s57 offen offset:2048  lds\n"
  "s_add_u32 s57, s57, s84\n"
  "s_add_u32 m0, s59, 15360\n"
  "s_nop 0\n"
  "buffer_load_dwordx4 v114, s[40:43], s57 offen offset:0  lds\n"
  "buffer_load_dwordx4 v115, s[40:43], s57 offen offset:1024  lds\n"
  "buffer_load_dwordx4 v116, s[40:43], s57 offen offset:2048  lds\n"
  "s_add_u32 s57, s57, 6144\n"
  "s_add_u32 m0, s59, 18432\n"
  "s_nop 0\n"
  "buffer_load_dwordx4 v114, s[40:43], s57 offen offset:0  lds\n"
  "buffer_load_dwordx4 v115, s[40:43], s57 offen offset:1024  lds\n"
  "buffer_load_dwordx4 v116, s[40:43], s57 offen offset:2048  lds\n"
  "s_add_u32 s57, s57, s58\n"
  "s_waitcnt vmcnt(27)\n"
  "ds_read_b64 v[32:33], v117 offset:21504\n"
  "ds_read_b64 v[34:35], v118 offset:21504\n"
  "s_waitcnt lgkmcnt(0)\n"
  "v_max3_f32 v32, |v32|, |v33|, |v34|\n"
  "v_max3_f32 v32, v32, |v35|, 0\n"
  "v_max_f32 v32, 0, v32\n"
  "v_mul_f32 v33, 0x40000000, v32\n"
  "v_mul_f32 v32, 0x2b800000, v32\n"
  "v_max_f32 v82, v82, v32\n"
  "s_nop 0\n"
  "v_readlane_b32 s98, v33, 0\n"
  "v_readlane_b32 s62, v33, 21\n"
  "v_readlane_b32 s63, v33, 42\n"
  "v_readlane_b32 s64, v33, 63\n"
  "s_nop 1\n"
  "s_max_u32 s98, s98, s62\n"
  "s_max_u32 s63, s63, s64\n"
  "s_max_u32 s98, s98, s63\n"
  "s_and_b32 s98, s98, 0x7f800000\n"
  "s_cselect_b32 s62, 0x400000, 0\n"
  "s_or_b32 s98, s98, s62\n"
  "s_nop 0\n"
  "v_mov_b32 v100, s98\n"
  "v_mov_b32 v101, s98\n"
  "v_mov_b32 v102, s98\n"
  "v_mov_b32 v103, s98\n"
  "ds_read_b64_tr_b16 a[0:1], v117 offset:21504\n"
  "ds_read_b64_tr_b16 a[4:5], v118 offset:21504\n"
  "ds_read_b64_tr_b16 a[8:9], v119 offset:21504\n"
  "ds_read_b64_tr_b16 a[12:13], v120 offset:21504\n"
  "ds_read_b64_tr_b16 a[16:17], v121 offset:23552\n"
  "ds_read_b64_tr_b16 a[20:21], v122 offset:23552\n"
  "s_waitcnt vmcnt(24)\n"
  "ds_read_b64_tr_b16 a[2:3], v117 offset:24576\n"
  "ds_read_b64_tr_b16 a[6:7], v118 offset:24576\n"
  "s_add_u32 m0, s59, 21504\n"
  "ds_read_b64_tr_b16 a[10:11], v119 offset:24576\n"
  "ds_read_b64_tr_b16 a[14:15], v120 offset:24576\n"
  "s_waitcnt lgkmcnt(4)\n"
  "buffer_load_dwordx4 v114, s[40:43], s57 offen offset:0  lds\n"
  "buffer_load_dwordx4 v115, s[40:43], s57 offen offset:1024  lds\n"
  "ds_read_b64_tr_b16 a[18:19], v121 offset:26624\n"
  "ds_read_b64_tr_b16 a[22:23], v122 offset:26624\n"
  "s_waitcnt vmcnt(23)\n"
  "ds_read_b64_tr_b16 a[24:25], v117 offset:27648\n"
  "ds_read_b64_tr_b16 a[28:29], v118 offset:27648\n"
  "buffer_load_dwordx4 v116, s[40:43], s57 offen offset:2048  lds\n"
  "s_add_u32 s57, s57, 6144\n"
  "s_add_u32 m0, s59, 24576\n"
  "ds_read_b64_tr_b16 a[32:33], v119 offset:27648\n"
  "ds_read_b64_tr_b16 a[36:37], v120 offset:27648\n"
  "s_waitcnt lgkmcnt(4)\n"
  "buffer_load_dwordx4 v114, s[40:43], s57 offen offset:0  lds\n"
  "buffer_load_dwordx4 v115, s[40:43], s57 offen offset:1024  lds\n"
  "ds_read_b64_tr_b16 a[40:41], v121 offset:29696\n"
  "ds_read_b64_tr_b16 a[44:45], v122 offset:29696\n"
  "s_waitcnt vmcnt(23)\n"
  "ds_read_b64_tr_b16 a[26:27], v117 offset:0\n"
  "ds_read_b64_tr_b16 a[30:31], v118 offset:0\n"
  "buffer_load_dwordx4 v116, s[40:43], s57 offen offset:2048  lds\n"
  "s_add_u32 s57, s57, s84\n"
  "s_add_u32 m0, s59, 27648\n"
  "ds_read_b64_tr_b16 a[34:35], v119 offset:0\n"
  "ds_read_b64_tr_b16 a[38:39], v120 offset:0\n"
  "s_waitcnt lgkmcnt(4)\n"
  "buffer_load_dwordx4 v114, s[40:43], s57 offen offset:0  lds\n"
  "buffer_load_dwordx4 v115, s[40:43], s57 offen offset:1024  lds\n"
  "ds_read_b64_tr_b16 a[42:43], v121 offset:2048\n"
  "ds_read_b64_tr_b16 a[46:47], v122 offset:2048\n"
  "s_waitcnt vmcnt(23)\n"
  "ds_read_b64_tr_b16 a[48:49], v117 offset:3072\n"
  "ds_read_b64_tr_b16 a[52:53], v118 offset:3072\n"
  "buffer_load_dwordx4 v116, s[40:43], s57 offen offset:2048  lds\n"
  "s_add_u32 s57, s57, 6144\n"
  "s_add_u32 m0, s59, 0\n"
  "ds_read_b64_tr_b16 a[56:57], v119 offset:3072\n"
  "ds_read_b64_tr_b16 a[60:61], v120 offset:3072\n"
  "s_waitcnt lgkmcnt(4)\n"
  "buffer_load_dwordx4 v114, s[40:43], s57 offen offset:0  lds\n"
  "buffer_load_dwordx4 v115, s[40:43], s57 offen offset:1024  lds\n"
  "ds_read_b64_tr_b16 a[64:65], v121 offset:5120\n"
  "ds_read_b64_tr_b16 a[68:69], v122 offset:5120\n"
  "s_waitcnt vmcnt(23)\n"
  "ds_read_b64_tr_b16 a[50:51], v117 offset:6144\n"
  "ds_read_b64_tr_b16 a[54:55], v118 offset:6144\n"
  "buffer_load_dwordx4 v116, s[40:43], s57 offen offset:2048  lds\n"
  "s_add_u32 s57, s57, s58\n"
  "s_add_u32 m0, s59, 3072\n"
  "ds_read_b64_tr_b16 a[58:59], v119 offset:6144\n"
  "ds_read_b64_tr_b16 a[62:63], v120 offset:6144\n"
  "s_waitcnt lgkmcnt(4)\n"
  "buffer_load_dwordx4 v114, s[40:43], s57 offen offset:0  lds\n"
  "buffer_load_dwordx4 v115, s[40:43], s57 offen offset:1024  lds\n"
  "ds_read_b64_tr_b16 a[66:67], v121 offset:8192\n"
  "ds_read_b64_tr_b16 a[70:71], v122 offset:8192\n"
  "s_waitcnt vmcnt(23)\n"
  "ds_read_b64_tr_b16 a[72:73], v117 offset:9216\n"
  "ds_read_b64_tr_b16 a[76:77], v118 offset:9216\n"
  "buffer_load_dwordx4 v116, s[40:43], s57 offen offset:2048  lds\n"
  "s_add_u32 s57, s57, 6144\n"
  "s_add_u32 m0, s59, 6144\n"
  "ds_read_b64_tr_b16 a[80:81], v119 offset:9216\n"
  "ds_read_b64_tr_b16 a[84:85], v120 offset:9216\n"
  "s_waitcnt lgkmcnt(4)\n"
  "buffer_load_dwordx4 v114, s[40:43], s57 offen offset:0  lds\n"
  "buffer_load_dwordx4 v115, s[40:43], s57 offen offset:1024  lds\n"
  "ds_read_b64_tr_b16 a[88:89], v121 offset:11264\n"
  "ds_read_b64_tr_b16 a[92:93], v122 offset:11264\n"
  "s_waitcnt vmcnt(23)\n"
  "ds_read_b64_tr_b16 a[74:75], v117 offset:12288\n"
  "ds_read_b64_tr_b16 a[78:79], v118 offset:12288\n"
  "buffer_load_dwordx4 v116, s[40:43], s57 offen offset:2048  lds\n"
  "s_add_u32 s57, s57, s84\n"
  "s_add_u32 m0, s59, 9216\n"
  "ds_read_b64_tr_b16 a[82:83], v119 offset:12288\n"
  "ds_read_b64_tr_b16 a[86:87], v120 offset:12288\n"
  "s_waitcnt lgkmcnt(0)\n"
  "s_barrier\n"
  "buffer_load_dwordx4 v114, s[40:43], s57 offen offset:0  lds\n"
  "buffer_load_dwordx4 v115, s[40:43], s57 offen offset:1024  lds\n"
  "ds_read_b128 v[48:51], v104 offset:0\n"
  "ds_read_b64_tr_b16 a[90:91], v121 offset:14336\n"
  "ds_read_b64_tr_b16 a[94:95], v122 offset:14336\n"
  "s_waitcnt vmcnt(23)\n"
  "ds_read_b64_tr_b16 a[96:97], v117 offset:15360\n"
  "ds_read_b64_tr_b16 a[100:101], v118 offset:15360\n"
  "ds_read_b128 v[84:87], v111\n"
  "ds_read_b128 v[96:99], v111 offset:64\n"
  "s_sub_u32 s56, s56, 1\n"
  "buffer_load_dwordx4 v116, s[40:43], s57 offen offset:2048  lds\n"
  "ds_read_b128 v[52:55], v104 offset:512\n"
  "v_xor_b32 v111, 0x2000, v111\n"
  "s_add_u32 s57, s57, 6144\n"
  "s_add_u32 m0, s59, 12288\n"
  "ds_read_b64_tr_b16 a[104:105], v119 offset:15360\n"
  "ds_read_b64_tr_b16 a[108:109], v120 offset:15360\n"
  "s_waitcnt lgkmcnt(7)\n"
  "buffer_load_dwordx4 v114, s[40:43], s57 offen offset:0  lds\n"
  "buffer_load_dwordx4 v115, s[40:43], s57 offen offset:1024  lds\n"
  "ds_read_b64_tr_b16 a[112:113], v121 offset:17408\n"
  "ds_read_b64_tr_b16 a[116:117], v122 offset:17408\n"
  "s_waitcnt vmcnt(23)\n"
  "ds_read_b64_tr_b16 a[98:99], v117 offset:18432\n"
  "ds_read_b64_tr_b16 a[102:103], v118 offset:18432\n"
  "s_sub_i32 s61, s56, 5\n"
  "s_max_i32 s61, s61, 0\n"
  "buffer_load_dwordx4 v116, s[40:43], s57 offen offset:2048  lds\n"
  "ds_read_b128 v[56:59], v105 offset:0\n"
  "s_add_u32 s57, s57, s58\n"
  "s_add_u32 m0, s59, 15360\n"
  "ds_read_b64_tr_b16 a[106:107], v119 offset:18432\n"
  "ds_read_b64_tr_b16 a[110:111], v120 offset:18432\n"
  "s_waitcnt lgkmcnt(5)\n"
  "buffer_load_dwordx4 v114, s[40:43], s57 offen offset:0  lds\n"
  "buffer_load_dwordx4 v115, s[40:43], s57 offen offset:1024  lds\n"
  "ds_read_b64_tr_b16 a[114:115], v121 offset:20480\n"
  "ds_read_b64_tr_b16 a[118:119], v122 offset:20480\n"
  "s_waitcnt vmcnt(23)\n"
  "ds_read_b64_tr_b16 a[120:121], v117 offset:21504\n"
  "ds_read_b64_tr_b16 a[124:125], v118 offset:21504\n"
  "buffer_load_dwordx4 v116, s[40:43], s57 offen offset:2048  lds\n"
  "ds_read_b128 v[60:63], v105 offset:512\n"
  "v_add_f32 v84, v84, v96\n"
  "s_add_u32 s57, s57, 6144\n"
  "s_add_u32 m0, s59, 18432\n"
  "ds_read_b64_tr_b16 a[128:129], v119 offset:21504\n"
  "ds_read_b64_tr_b16 a[132:133], v120 offset:21504\n"
  "v_add_f32 v85, v85, v97\n"
  "v_add_f32 v86, v86, v98\n"
  "s_waitcnt lgkmcnt(5)\n"
  "buffer_load_dwordx4 v114, s[40:43], s57 offen offset:0  lds\n"
  "buffer_load_dwordx4 v115, s[40:43], s57 offen offset:1024  lds\n"
  "ds_read_b64_tr_b16 a[136:137], v121 offset:23552\n"
  "ds_read_b64_tr_b16 a[140:141], v122 offset:23552\n"
  "s_waitcnt vmcnt(23)\n"
  "ds_read_b64_tr_b16 a[122:123], v117 offset:24576\n"
  "ds_read_b64_tr_b16 a[126:127], v118 offset:24576\n"
  "v_add_f32 v87, v87, v99\n"
  "v_max3_f32 v82, v82, v84, v85\n"
  "buffer_load_dwordx4 v116, s[40:43], s57 offen offset:2048  lds\n"
  "v_rcp_f32 v84, v84\n"
  "s_add_u32 s57, s57, s84\n"
  "s_add_u32 m0, s59, 21504\n"
  "ds_read_b64_tr_b16 a[130:131], v119 offset:24576\n"
  "ds_read_b64_tr_b16 a[134:135], v120 offset:24576\n"
  "v_rcp_f32 v85, v85\n"
  "s_waitcnt lgkmcnt(4)\n"
  "buffer_load_dwordx4 v114, s[40:43], s57 offen offset:0  lds\n"
  "buffer_load_dwordx4 v115, s[40:43], s57 offen offset:1024  lds\n"
  "ds_read_b64_tr_b16 a[138:139], v121 offset:26624\n"
  "ds_read_b64_tr_b16 a[142:143], v122 offset:26624\n"
  "s_waitcnt vmcnt(23)\n"
  "ds_read_b64_tr_b16 a[144:145], v117 offset:27648\n"
  "ds_read_b64_tr_b16 a[148:149], v118 offset:27648\n"
  "v_max3_f32 v82, v82, v86, v87\n"
  "v_max3_f32 v83, v83, v84, v85\n"
  "buffer_load_dwordx4 v116, s[40:43], s57 offen offset:2048  lds\n"
  "v_rcp_f32 v86, v86\n"
  "s_add_u32 s57, s57, 6144\n"
  "s_add_u32 m0, s59, 24576\n"
  "ds_read_b64_tr_b16 a[152:153], v119 offset:27648\n"
  "ds_read_b64_tr_b16 a[156:157], v120 offset:27648\n"
  "v_rcp_f32 v87, v87\n"
  "s_waitcnt lgkmcnt(4)\n"
  "buffer_load_dwordx4 v114, s[40:43], s57 offen offset:0  lds\n"
  "buffer_load_dwordx4 v115, s[40:43], s57 offen offset:1024  lds\n"
  "ds_read_b64_tr_b16 a[160:161], v121 offset:29696\n"
  "ds_read_b64_tr_b16 a[164:165], v122 offset:29696\n"
  "s_waitcnt vmcnt(23)\n"
  "ds_read_b64_tr_b16 a[146:147], v117 offset:0\n"
  "ds_read_b64_tr_b16 a[150:151], v118 offset:0\n"
  "v_max3_f32 v83, v83, v86, v87\n"
  "v_mul_f32 v96, s99, v84\n"
  "buffer_load_dwordx4 v116, s[40:43], s57 offen offset:2048  lds\n"
  "v_mul_f32 v97, s99, v85\n"
  "v_mul_f32 v98, s99, v86\n"
  "s_add_u32 s57, s57, s58\n"
  "s_add_u32 m0, s59, 27648\n"
  "ds_read_b64_tr_b16 a[154:155], v119 offset:0\n"
  "ds_read_b64_tr_b16 a[158:159], v120 offset:0\n"
  "v_mul_f32 v99, s99, v87\n"
  "v_fma_f32 v40, v40, v84, v96\n"
  "v_mfma_f32_16x16x32_bf16 v[0:3], v[48:51], a[0:3], v[100:103]\n"
  "s_waitcnt lgkmcnt(4)\n"
  "buffer_load_dwordx4 v114, s[40:43], s57 offen offset:0  lds\n"
  "buffer_load_dwordx4 v115, s[40:43], s57 offen offset:1024  lds\n"
  "ds_read_b128 v[64:67], v104 offset:1024\n"
  "ds_read_b64_tr_b16 a[162:163], v121 offset:2048\n"
  "ds_read_b64_tr_b16 a[166:167], v122 offset:2048\n"
  "v_mfma_f32_16x16x32_bf16 v[0:3], v[56:59], a[24:27], v[0:3]\n"
  "s_waitcnt vmcnt(23)\n"
  "ds_read_b64_tr_b16 a[168:169], v117 offset:3072\n"
  "ds_read_b64_tr_b16 a[172:173], v118 offset:3072\n"
  "v_fma_f32 v44, v44, v84, v96\n"
  "v_fma_f32 v41, v41, v85, v97\n"
  "v_mfma_f32_16x16x32_bf16 v[4:7], v[56:59], a[28:31], v[100:103]\n"
  "s_xor_b32 s99, 0x80000000, s98\n"
  "buffer_load_dwordx4 v116, s[40:43], s57 offen offset:2048  lds\n"
  "ds_read_b128 v[68:71], v104 offset:1536\n"
  "v_fma_f32 v45, v45, v85, v97\n"
  "v_mfma_f32_16x16x32_bf16 v[4:7], v[48:51], a[4:7], v[4:7]\n"
  "s_add_u32 s57, s57, 6144\n"
  "s_add_u32 m0, s59, 0\n"
  "ds_read_b64_tr_b16 a[176:177], v119 offset:3072\n"
  "ds_read_b64_tr_b16 a[180:181], v120 offset:3072\n"
  "ds_read_b64_tr_b16 v[92:93], v108 offset:0\n"
  "ds_read_b64_tr_b16 v[94:95], v108 offset:32\n"
  "v_mfma_f32_16x16x32_bf16 v[8:11], v[48:51], a[8:11], v[100:103]\n"
  "s_waitcnt lgkmcnt(7)\n"
  "buffer_load_dwordx4 v114, s[40:43], s57 offen offset:0  lds\n"
  "buffer_load_dwordx4 v115, s[40:43], s57 offen offset:1024  lds\n"
  "ds_read_b64_tr_b16 a[184:185], v121 offset:5120\n"
  "ds_read_b64_tr_b16 a[188:189], v122 offset:5120\n"
  "v_mfma_f32_16x16x32_bf16 v[8:11], v[56:59], a[32:35], v[8:11]\n"
  "s_waitcnt vmcnt(23)\n"
  "ds_read_b64_tr_b16 a[170:171], v117 offset:6144\n"
  "ds_read_b64_tr_b16 a[174:175], v118 offset:6144\n"
  "v_fma_f32 v42, v42, v86, v98\n"
  "v_fma_f32 v46, v46, v86, v98\n"
  "v_mfma_f32_16x16x32_bf16 v[12:15], v[56:59], a[36:39], v[100:103]\n"
  "buffer_load_dwordx4 v116, s[40:43], s57 offen offset:2048  lds\n"
  "ds_read_b128 v[72:75], v105 offset:1024\n"
  "v_fma_f32 v43, v43, v87, v99\n"
  "v_mfma_f32_16x16x32_bf16 v[12:15], v[48:51], a[12:15], v[12:15]\n"
  "s_add_u32 s57, s57, s84\n"
  "s_add_u32 m0, s59, 3072\n"
  "ds_read_b64_tr_b16 a[178:179], v119 offset:6144\n"
  "ds_read_b64_tr_b16 a[182:183], v120 offset:6144\n"
  "v_fma_f32 v47, v47, v87, v99\n"
  "v_fma_f32 v24, v24, v84, v96\n"
  "v_mfma_f32_16x16x32_bf16 v[16:19], v[48:51], a[16:19], v[100:103]\n"
  "s_waitcnt lgkmcnt(5)\n"
  "buffer_load_dwordx4 v114, s[40:43], s57 offen offset:0  lds\n"
  "buffer_load_dwordx4 v115, s[40:43], s57 offen offset:1024  lds\n"
  "ds_read_b64_tr_b16 a[186:187], v121 offset:8192\n"
  "ds_read_b64_tr_b16 a[190:191], v122 offset:8192\n"
  "v_mfma_f32_16x16x32_bf16 v[16:19], v[56:59], a[40:43], v[16:19]\n"
  "s_waitcnt vmcnt(23)\n"
  "ds_read_b64_tr_b16 a[192:193], v117 offset:9216\n"
  "ds_read_b64_tr_b16 a[196:197], v118 offset:9216\n"
  "v_fma_f32 v25, v25, v85, v97\n"
  "v_fma_f32 v28, v28, v84, v96\n"
  "v_mfma_f32_16x16x32_bf16 v[20:23], v[56:59], a[44:47], v[100:103]\n"
  "buffer_load_dwordx4 v116, s[40:43], s57 offen offset:2048  lds\n"
  "ds_read_b128 v[76:79], v105 offset:1536\n"
  "v_fma_f32 v29, v29, v85, v97\n"
  "v_mfma_f32_16x16x32_bf16 v[20:23], v[48:51], a[20:23], v[20:23]\n"
  "s_add_u32 s57, s57, 6144\n"
  "s_add_u32 m0, s59, 6144\n"
  "ds_read_b64_tr_b16 a[200:201], v119 offset:9216\n"
  "ds_read_b64_tr_b16 a[204:205], v120 offset:9216\n"
  "v_fma_f32 v32, v32, v84, v96\n"
  "v_fma_f32 v33, v33, v85, v97\n"
  "v_smfmac_f32_16x16x64_bf16 v[80:83], v[88:91], v[48:55], v124\n"
  "s_waitcnt lgkmcnt(5)\n"
  "buffer_load_dwordx4 v114, s[40:43], s57 offen offset:0  lds\n"
  "buffer_load_dwordx4 v115, s[40:43], s57 offen offset:1024  lds\n"
  "ds_read_b64_tr_b16 a[208:209], v121 offset:11264\n"
  "ds_read_b64_tr_b16 a[212:213], v122 offset:11264\n"
  "v_smfmac_f32_16x16x64_bf16 v[80:83], v[88:91], v[64:71], v124\n"
  "buffer_store_dwordx4 v[92:95], v109, s[48:51], s68 offen offset:0 nt\n"
  "s_waitcnt vmcnt(24)\n"
  "ds_read_b64_tr_b16 a[194:195], v117 offset:12288\n"
  "ds_read_b64_tr_b16 a[198:199], v118 offset:12288\n"
  "v_mfma_f32_16x16x32_bf16 v[20:23], v[52:55], a[68:71], v[20:23]\n"
  "buffer_load_dwordx4 v116, s[40:43], s57 offen offset:2048  lds\n"
  "v_fma_f32 v36, v36, v84, v96\n"
  "v_fma_f32 v37, v37, v85, v97\n"
  "v_mfma_f32_16x16x32_bf16 v[20:23], v[60:63], a[92:95], v[20:23]\n"
  "s_add_u32 s57, s57, s58\n"
  "s_add_u32 m0, s59, 9216\n"
  "ds_read_b64_tr_b16 a[202:203], v119 offset:12288\n"
  "ds_read_b64_tr_b16 a[206:207], v120 offset:12288\n"
  "v_fma_f32 v26, v26, v86, v98\n"
  "v_fma_f32 v27, v27, v87, v99\n"
  "v_mfma_f32_16x16x32_bf16 v[16:19], v[60:63], a[88:91], v[16:19]\n"
  "s_waitcnt lgkmcnt(4)\n"
  "buffer_load_dwordx4 v114, s[40:43], s57 offen offset:0  lds\n"
  "buffer_load_dwordx4 v115, s[40:43], s57 offen offset:1024  lds\n"
  "ds_read_b64_tr_b16 a[210:211], v121 offset:14336\n"
  "ds_read_b64_tr_b16 a[214:215], v122 offset:14336\n"
  "v_mfma_f32_16x16x32_bf16 v[16:19], v[52:55], a[64:67], v[16:19]\n"
  "s_waitcnt vmcnt(24)\n"
  "ds_read_b64_tr_b16 a[216:217], v117 offset:15360\n"
  "ds_read_b64_tr_b16 a[220:221], v118 offset:15360\n"
  "v_fma_f32 v30, v30, v86, v98\n"
  "v_fma_f32 v31, v31, v87, v99\n"
  "v_mfma_f32_16x16x32_bf16 v[12:15], v[52:55], a[60:63], v[12:15]\n"
  "buffer_load_dwordx4 v116, s[40:43], s57 offen offset:2048  lds\n"
  "v_fma_f32 v34, v34, v86, v98\n"
  "v_fma_f32 v35, v35, v87, v99\n"
  "v_mfma_f32_16x16x32_bf16 v[12:15], v[60:63], a[84:87], v[12:15]\n"
  "s_add_u32 s57, s57, 6144\n"
  "s_add_u32 m0, s59, 12288\n"
  "ds_read_b64_tr_b16 a[224:225], v119 offset:15360\n"
  "ds_read_b64_tr_b16 a[228:229], v120 offset:15360\n"
  "v_fma_f32 v38, v38, v86, v98\n"
  "v_fma_f32 v39, v39, v87, v99\n"
  "v_mfma_f32_16x16x32_bf16 v[8:11], v[60:63], a[80:83], v[8:11]\n"
  "s_waitcnt lgkmcnt(4)\n"
  "buffer_load_dwordx4 v114, s[40:43], s57 offen offset:0  lds\n"
  "buffer_load_dwordx4 v115, s[40:43], s57 offen offset:1024  lds\n"
  "ds_read_b64_tr_b16 a[232:233], v121 offset:17408\n"
  "ds_read_b64_tr_b16 a[236:237], v122 offset:17408\n"
  "v_mfma_f32_16x16x32_bf16 v[8:11], v[52:55], a[56:59], v[8:11]\n"
  "s_waitcnt vmcnt(24)\n"
  "ds_read_b64_tr_b16 a[218:219], v117 offset:18432\n"
  "ds_read_b64_tr_b16 a[222:223], v118 offset:18432\n"
  "ds_read_b64_tr_b16 v[96:97], v108 offset:2304\n"
  "ds_read_b64_tr_b16 v[98:99], v108 offset:2336\n"
  "v_mfma_f32_16x16x32_bf16 v[4:7], v[52:55], a[52:55], v[4:7]\n"
  "buffer_load_dwordx4 v116, s[40:43], s57 offen offset:2048  lds\n"
  "v_cvt_pk_bf16_f32 v40, v40, v44\n"
  "v_cvt_pk_bf16_f32 v41, v41, v45\n"
  "v_mfma_f32_16x16x32_bf16 v[4:7], v[60:63], a[76:79], v[4:7]\n"
  "s_add_u32 s57, s57, s84\n"
  "s_add_u32 m0, s59, 15360\n"
  "ds_read_b64_tr_b16 a[226:227], v119 offset:18432\n"
  "ds_read_b64_tr_b16 a[230:231], v120 offset:18432\n"
  "v_cvt_pk_bf16_f32 v42, v42, v46\n"
  "v_cvt_pk_bf16_f32 v43, v43, v47\n"
  "v_mfma_f32_16x16x32_bf16 v[0:3], v[60:63], a[72:75], v[0:3]\n"
  "s_waitcnt lgkmcnt(6)\n"
  "buffer_load_dwordx4 v114, s[40:43], s57 offen offset:0  lds\n"
  "buffer_load_dwordx4 v115, s[40:43], s57 offen offset:1024  lds\n"
  "ds_read_b64_tr_b16 a[234:235], v121 offset:20480\n"
  "ds_read_b64_tr_b16 a[238:239], v122 offset:20480\n"
  "v_mfma_f32_16x16x32_bf16 v[0:3], v[52:55], a[48:51], v[0:3]\n"
  "s_waitcnt vmcnt(24)\n"
  "ds_read_b64_tr_b16 a[240:241], v117 offset:21504\n"
  "ds_read_b64_tr_b16 a[244:245], v118 offset:21504\n"
  "v_cvt_pk_bf16_f32 v24, v24, v28\n"
  "v_cvt_pk_bf16_f32 v28, v25, v29\n"
  "v_mfma_f32_16x16x32_bf16 v[0:3], v[64:67], a[96:99], v[0:3]\n"
  "buffer_load_dwordx4 v116, s[40:43], s57 offen offset:2048  lds\n"
  "ds_read_b128 v[48:51], v104 offset:2048\n"
  "v_cvt_pk_bf16_f32 v25, v32, v36\n"
  "v_mfma_f32_16x16x32_bf16 v[0:3], v[72:75], a[120:123], v[0:3]\n"
  "s_add_u32 s57, s57, 6144\n"
  "s_add_u32 m0, s59, 18432\n"
  "ds_read_b64_tr_b16 a[248:249], v119 offset:21504\n"
  "ds_read_b64_tr_b16 a[252:253], v120 offset:21504\n"
  "v_cvt_pk_bf16_f32 v29, v33, v37\n"
  "v_cvt_pk_bf16_f32 v26, v26, v30\n"
  "v_mfma_f32_16x16x32_bf16 v[4:7], v[72:75], a[124:127], v[4:7]\n"
  "s_waitcnt lgkmcnt(5)\n"
  "buffer_load_dwordx4 v114, s[40:43], s57 offen offset:0  lds\n"
  "buffer_load_dwordx4 v115, s[40:43], s57 offen offset:1024  lds\n"
  "ds_read_b128 v[52:55], v104 offset:2560\n"
  "ds_read_b64_tr_b16 v[128:129], v121 offset:23552\n"
  "ds_read_b64_tr_b16 v[132:133], v122 offset:23552\n"
  "v_mfma_f32_16x16x32_bf16 v[4:7], v[64:67], a[100:103], v[4:7]\n"
  "s_waitcnt vmcnt(24)\n"
  "ds_read_b64_tr_b16 a[242:243], v117 offset:24576\n"
  "ds_read_b64_tr_b16 a[246:247], v118 offset:24576\n"
  "v_cvt_pk_bf16_f32 v30, v27, v31\n"
  "v_cvt_pk_bf16_f32 v27, v34, v38\n"
  "v_mfma_f32_16x16x32_bf16 v[8:11], v[64:67], a[104:107], v[8:11]\n"
  "buffer_load_dwordx4 v116, s[40:43], s57 offen offset:2048  lds\n"
  "v_cvt_pk_bf16_f32 v31, v35, v39\n"
  "v_mfma_f32_16x16x32_bf16 v[8:11], v[72:75], a[128:131], v[8:11]\n"
  "s_add_u32 s57, s57, s58\n"
  "s_add_u32 m0, s59, 21504\n"
  "ds_read_b64_tr_b16 a[250:251], v119 offset:24576\n"
  "ds_read_b64_tr_b16 a[254:255], v120 offset:24576\n"
  "ds_read_b64_tr_b16 v[92:93], v108 offset:4608\n"
  "ds_read_b64_tr_b16 v[94:95], v108 offset:4640\n"
  "v_mfma_f32_16x16x32_bf16 v[12:15], v[72:75], a[132:135], v[12:15]\n"
  "s_waitcnt lgkmcnt(6)\n"
  "buffer_load_dwordx4 v114, s[40:43], s57 offen offset:0  lds\n"
  "buffer_load_dwordx4 v115, s[40:43], s57 offen offset:1024  lds\n"
  "ds_read_b128 v[56:59], v105 offset:2048\n"
  "ds_read_b64_tr_b16 v[130:131], v121 offset:26624\n"
  "ds_read_b64_tr_b16 v[134:135], v122 offset:26624\n"
  "v_mfma_f32_16x16x32_bf16 v[12:15], v[64:67], a[108:111], v[12:15]\n"
  "s_waitcnt vmcnt(24)\n"
  "ds_read_b64_tr_b16 v[136:137], v117 offset:27648\n"
  "ds_read_b64_tr_b16 v[140:141], v118 offset:27648\n"
  "ds_write2_b32 v107, v40, v41 offset0:0 offset1:72\n"
  "v_mfma_f32_16x16x32_bf16 v[16:19], v[64:67], a[112:115], v[16:19]\n"
  "buffer_load_dwordx4 v116, s[40:43], s57 offen offset:2048  lds\n"
  "ds_write2_b32 v107, v42, v43 offset0:144 offset1:216\n"
  "v_mfma_f32_16x16x32_bf16 v[16:19], v[72:75], a[136:139], v[16:19]\n"
  "s_add_u32 s57, s57, 6144\n"
  "s_add_u32 m0, s59, 24576\n"
  "ds_read_b64_tr_b16 v[144:145], v119 offset:27648\n"
  "ds_read_b64_tr_b16 v[148:149], v120 offset:27648\n"
  "ds_write_b64 v106, v[24:25] offset:0\n"
  "v_mfma_f32_16x16x32_bf16 v[20:23], v[72:75], a[140:143], v[20:23]\n"
  "s_waitcnt lgkmcnt(7)\n"
  "buffer_load_dwordx4 v114, s[40:43], s57 offen offset:0  lds\n"
  "buffer_load_dwordx4 v115, s[40:43], s57 offen offset:1024  lds\n"
  "ds_read_b128 v[60:63], v105 offset:2560\n"
  "ds_read_b64_tr_b16 v[152:153], v121 offset:29696\n"
  "ds_read_b64_tr_b16 v[156:157], v122 offset:29696\n"
  "v_mfma_f32_16x16x32_bf16 v[20:23], v[64:67], a[116:119], v[20:23]\n"
  "s_waitcnt vmcnt(24)\n"
  "ds_read_b64_tr_b16 v[138:139], v117 offset:0\n"
  "ds_read_b64_tr_b16 v[142:143], v118 offset:0\n"
  "ds_write_b64 v106, v[28:29] offset:288\n"
  "v_mfma_f32_16x16x32_bf16 v[20:23], v[68:71], a[164:167], v[20:23]\n"
  "buffer_load_dwordx4 v116, s[40:43], s57 offen offset:2048  lds\n"
  "ds_write_b64 v106, v[26:27] offset:576\n"
  "v_mfma_f32_16x16x32_bf16 v[20:23], v[76:79], a[188:191], v[20:23]\n"
  "s_add_u32 s57, s57, s84\n"
  "ds_read_b64_tr_b16 v[146:147], v119 offset:0\n"
  "ds_read_b64_tr_b16 v[150:151], v120 offset:0\n"
  "ds_write_b64 v106, v[30:31] offset:864\n"
  "v_mfma_f32_16x16x32_bf16 v[16:19], v[76:79], a[184:187], v[16:19]\n"
  "buffer_store_dwordx4 v[96:99], v109, s[48:51], s68 offen offset:128 nt\n"
  "ds_read_b64_tr_b16 v[154:155], v121 offset:2048\n"
  "ds_read_b64_tr_b16 v[158:159], v122 offset:2048\n"
  "v_xor_b32 v108, 0x2000, v108\n"
  "v_mfma_f32_16x16x32_bf16 v[16:19], v[68:71], a[160:163], v[16:19]\n"
  "s_waitcnt vmcnt(23)\n"
  "ds_read_b64_tr_b16 v[160:161], v117 offset:3072\n"
  "ds_read_b64_tr_b16 v[164:165], v118 offset:3072\n"
  "v_mfma_f32_16x16x32_bf16 v[12:15], v[68:71], a[156:159], v[12:15]\n"
  "v_mfma_f32_16x16x32_bf16 v[12:15], v[76:79], a[180:183], v[12:15]\n"
  "ds_read_b64_tr_b16 v[168:169], v119 offset:3072\n"
  "ds_read_b64_tr_b16 v[172:173], v120 offset:3072\n"
  "v_mfma_f32_16x16x32_bf16 v[8:11], v[76:79], a[176:179], v[8:11]\n"
  "ds_read_b64_tr_b16 v[176:177], v121 offset:5120\n"
  "ds_read_b64_tr_b16 v[180:181], v122 offset:5120\n"
  "v_mfma_f32_16x16x32_bf16 v[8:11], v[68:71], a[152:155], v[8:11]\n"
  "s_waitcnt vmcnt(19)\n"
  "ds_read_b64_tr_b16 v[162:163], v117 offset:6144\n"
  "ds_read_b64_tr_b16 v[166:167], v118 offset:6144\n"
  "v_mfma_f32_16x16x32_bf16 v[4:7], v[68:71], a[148:151], v[4:7]\n"
  "v_mfma_f32_16x16x32_bf16 v[4:7], v[76:79], a[172:175], v[4:7]\n"
  "ds_read_b64_tr_b16 v[170:171], v119 offset:6144\n"
  "ds_read_b64_tr_b16 v[174:175], v120 offset:6144\n"
  "v_mfma_f32_16x16x32_bf16 v[0:3], v[76:79], a[168:171], v[0:3]\n"
  "ds_read_b64_tr_b16 v[178:179], v121 offset:8192\n"
  "ds_read_b64_tr_b16 v[182:183], v122 offset:8192\n"
  "v_mfma_f32_16x16x32_bf16 v[0:3], v[68:71], a[144:147], v[0:3]\n"
  "s_waitcnt lgkmcnt(15)\n"
  "s_waitcnt vmcnt(16)\n"
  "ds_read_b64_tr_b16 v[184:185], v117 offset:9216\n"
  "ds_read_b64_tr_b16 v[188:189], v118 offset:9216\n"
  "v_mfma_f32_16x16x32_bf16 v[0:3], v[48:51], a[192:195], v[0:3]\n"
  "s_add_u32 s72, s72, 1\n"
  "ds_read_b128 v[64:67], v104 offset:3072\n"
  "v_mfma_f32_16x16x32_bf16 v[0:3], v[56:59], a[216:219], v[0:3]\n"
  "s_lshr_b32 s74, s72, 1\n"
  "ds_read_b64_tr_b16 v[192:193], v119 offset:9216\n"
  "ds_read_b64_tr_b16 v[196:197], v120 offset:9216\n"
  "v_readlane_b32 s73, v126, s74\n"
  "v_mfma_f32_16x16x32_bf16 v[4:7], v[56:59], a[220:223], v[4:7]\n"
  "s_lshl_b32 s74, s72, 4\n"
  "buffer_store_dwordx4 v[92:95], v109, s[48:51], s68 offen offset:256 nt\n"
  "ds_read_b128 v[68:71], v104 offset:3584\n"
  "ds_read_b64_tr_b16 v[200:201], v121 offset:11264\n"
  "ds_read_b64_tr_b16 v[204:205], v122 offset:11264\n"
  "v_mfma_f32_16x16x32_bf16 v[4:7], v[48:51], a[196:199], v[4:7]\n"
  "s_waitcnt vmcnt(14)\n"
  "ds_read_b64_tr_b16 v[186:187], v117 offset:12288\n"
  "ds_read_b64_tr_b16 v[190:191], v118 offset:12288\n"
  "v_add_u32 v104, s88, v104\n"
  "v_mfma_f32_16x16x32_bf16 v[8:11], v[48:51], a[200:203], v[8:11]\n"
  "s_lshr_b32 s73, s73, s74\n"
  "v_mfma_f32_16x16x32_bf16 v[8:11], v[56:59], a[224:227], v[8:11]\n"
  "s_and_b32 s73, s73, 0xffff\n"
  "ds_read_b64_tr_b16 v[194:195], v119 offset:12288\n"
  "ds_read_b64_tr_b16 v[198:199], v120 offset:12288\n"
  "v_mfma_f32_16x16x32_bf16 v[12:15], v[56:59], a[228:231], v[12:15]\n"
  "s_lshl_b32 s73, s73, 13\n"
  "ds_read_b128 v[72:75], v105 offset:3072\n"
  "ds_read_b64_tr_b16 v[202:203], v121 offset:14336\n"
  "ds_read_b64_tr_b16 v[206:207], v122 offset:14336\n"
  "v_mfma_f32_16x16x32_bf16 v[12:15], v[48:51], a[204:207], v[12:15]\n"
  "s_waitcnt vmcnt(11)\n"
  "ds_read_b64_tr_b16 v[208:209], v117 offset:15360\n"
  "ds_read_b64_tr_b16 v[212:213], v118 offset:15360\n"
  "v_mfma_f32_16x16x32_bf16 v[16:19], v[48:51], a[208:211], v[16:19]\n"
  "s_add_u32 s68, s68, s69\n"
  "v_mfma_f32_16x16x32_bf16 v[16:19], v[56:59], a[232:235], v[16:19]\n"
  "s_mov_b32 s69, s70\n"
  "ds_read_b64_tr_b16 v[216:217], v119 offset:15360\n"
  "ds_read_b64_tr_b16 v[220:221], v120 offset:15360\n"
  "v_mfma_f32_16x16x32_bf16 v[20:23], v[56:59], a[236:239], v[20:23]\n"
  "s_movk_i32 s70, 6144\n"
  "ds_read_b128 v[76:79], v105 offset:3584\n"
  "ds_read_b64_tr_b16 v[224:225], v121 offset:17408\n"
  "ds_read_b64_tr_b16 v[228:229], v122 offset:17408\n"
  "v_add_u32 v105, s88, v105\n"
  "v_mfma_f32_16x16x32_bf16 v[20:23], v[48:51], a[212:215], v[20:23]\n"
  "s_waitcnt vmcnt(8)\n"
  "ds_read_b64_tr_b16 v[210:211], v117 offset:18432\n"
  "ds_read_b64_tr_b16 v[214:215], v118 offset:18432\n"
  "v_smfmac_f32_16x16x64_bf16 v[80:83], v[88:91], v[48:55], v124\n"
  "s_waitcnt lgkmcnt(15)\n"
  "v_smfmac_f32_16x16x64_bf16 v[80:83], v[88:91], v[64:71], v124\n"
  "ds_read_b64_tr_b16 v[218:219], v119 offset:18432\n"
  "ds_read_b64_tr_b16 v[222:223], v120 offset:18432\n"
  "v_mfma_f32_16x16x32_bf16 v[20:23], v[52:55], v[132:135], v[20:23]\n"
  "s_add_u32 s88, s88, s89\n"
  "ds_read_b64_tr_b16 v[226:227], v121 offset:20480\n"
  "ds_read_b64_tr_b16 v[230:231], v122 offset:20480\n"
  "v_mfma_f32_16x16x32_bf16 v[20:23], v[60:63], v[156:159], v[20:23]\n"
  "s_waitcnt vmcnt(5)\n"
  "ds_read_b64_tr_b16 v[232:233], v117 offset:21504\n"
  "ds_read_b64_tr_b16 v[236:237], v118 offset:21504\n"
  "v_mfma_f32_16x16x32_bf16 v[16:19], v[60:63], v[152:155], v[16:19]\n"
  "s_sub_u32 s88, 0, s88\n"
  "v_add_f32 v44, v80, v81\n"
  "v_mov_b32 v80, 0\n"
  "v_mov_b32 v81, 0\n"
  "v_mfma_f32_16x16x32_bf16 v[16:19], v[52:55], v[128:131], v[16:19]\n"
  "s_xor_b32 s88, s88, s89\n"
  "ds_read_b64_tr_b16 v[240:241], v119 offset:21504\n"
  "ds_read_b64_tr_b16 v[244:245], v120 offset:21504\n"
  "ds_write_b32 v110, v44\n"
  "v_mfma_f32_16x16x32_bf16 v[12:15], v[52:55], a[252:255], v[12:15]\n"
  "s_xor_b32 s89, s88, s89\n"
  "ds_read_b64_tr_b16 v[248:249], v121 offset:23552\n"
  "ds_read_b64_tr_b16 v[252:253], v122 offset:23552\n"
  "v_mfma_f32_16x16x32_bf16 v[12:15], v[60:63], v[148:151], v[12:15]\n"
  "s_xor_b32 s88, s88, s89\n"
  "s_waitcnt vmcnt(2)\n"
  "ds_read_b64_tr_b16 v[234:235], v117 offset:24576\n"
  "ds_read_b64_tr_b16 v[238:239], v118 offset:24576\n"
  "v_mfma_f32_16x16x32_bf16 v[8:11], v[60:63], v[144:147], v[8:11]\n"
  "v_mfma_f32_16x16x32_bf16 v[8:11], v[52:55], a[248:251], v[8:11]\n"
  "ds_read_b64_tr_b16 v[242:243], v119 offset:24576\n"
  "ds_read_b64_tr_b16 v[246:247], v120 offset:24576\n"
  "v_xor_b32 v106, 0x2000, v106\n"
  "v_xor_b32 v107, 0x2000, v107\n"
  "v_mfma_f32_16x16x32_bf16 v[4:7], v[52:55], a[244:247], v[4:7]\n"
  "ds_read_b64_tr_b16 v[250:251], v121 offset:26624\n"
  "ds_read_b64_tr_b16 v[254:255], v122 offset:26624\n"
  "v_xor_b32 v110, 0x2000, v110\n"
  "v_mfma_f32_16x16x32_bf16 v[4:7], v[60:63], v[140:143], v[4:7]\n"
  "v_mfma_f32_16x16x32_bf16 v[0:3], v[60:63], v[136:139], v[0:3]\n"
  "v_mfma_f32_16x16x32_bf16 v[0:3], v[52:55], a[240:243], v[0:3]\n"
  "s_add_u32 m0, s66, 0\n"
  "s_nop 0\n"
  "buffer_load_dwordx4 v123, s[44:47], s67 offen offset:0 nt lds\n"
  "s_add_u32 m0, s66, 1920\n"
  "s_nop 0\n"
  "buffer_load_dwordx4 v123, s[44:47], s67 offen offset:128 nt lds\n"
  "s_add_u32 m0, s66, 3840\n"
  "s_nop 0\n"
  "buffer_load_dwordx4 v123, s[44:47], s67 offen offset:256 nt lds\n"
  "s_add_u32 s66, s66, 6144\n"
  "s_cmp_ge_u32 s66, 102400\n"
  "s_cselect_b32 s64, 36864, 0\n"
  "s_sub_u32 s66, s66, s64\n"
  "s_sub_u32 s90, s90, 1\n"
  "s_cselect_b32 s63, s91, 6144\n"
  "s_cmov_b64 s[90:91], s[92:93]\n"
  "s_cmov_b64 s[92:93], s[94:95]\n"
  "s_add_u32 s67, s67, s63\n"
  "s_add_u32 m0, s66, 0\n"
  "s_nop 0\n"
  "buffer_load_dwordx4 v123, s[44:47], s67 offen offset:0 nt lds\n"
  "s_add_u32 m0, s66, 1920\n"
  "s_nop 0\n"
  "buffer_load_dwordx4 v123, s[44:47], s67 offen offset:128 nt lds\n"
  "s_add_u32 m0, s66, 3840\n"
  "s_nop 0\n"
  "buffer_load_dwordx4 v123, s[44:47], s67 offen offset:256 nt lds\n"
  "s_add_u32 s66, s66, 6144\n"
  "s_cmp_ge_u32 s66, 102400\n"
  "s_cselect_b32 s64, 36864, 0\n"
  "s_sub_u32 s66, s66, s64\n"
  "s_sub_u32 s90, s90, 1\n"
  "s_cselect_b32 s63, s91, 6144\n"
  "s_cmov_b64 s[90:91], s[92:93]\n"
  "s_cmov_b64 s[92:93], s[94:95]\n"
  "s_add_u32 s67, s67, s63\n"
  "s_add_u32 m0, s66, 0\n"
  "s_nop 0\n"
  "buffer_load_dwordx4 v123, s[44:47], s67 offen offset:0 nt lds\n"
  "s_add_u32 m0, s66, 1920\n"
  "s_nop 0\n"
  "buffer_load_dwordx4 v123, s[44:47], s67 offen offset:128 nt lds\n"
  "s_add_u32 m0, s66, 3840\n"
  "s_nop 0\n"
  "buffer_load_dwordx4 v123, s[44:47], s67 offen offset:256 nt lds\n"
  "s_add_u32 s66, s66, 6144\n"
  "s_cmp_ge_u32 s66, 102400\n"
  "s_cselect_b32 s64, 36864, 0\n"
  "s_sub_u32 s66, s66, s64\n"
  "s_sub_u32 s90, s90, 1\n"
  "s_cselect_b32 s63, s91, 6144\n"
  "s_cmov_b64 s[90:91], s[92:93]\n"
  "s_cmov_b64 s[92:93], s[94:95]\n"
  "s_add_u32 s67, s67, s63\n"
  "B_H1_%=:\n"
  "s_waitcnt vmcnt(13)\n"
  "s_branch B_H_%=\n"
  ".p2align 6\n"
  "B_H0_%=:\n"
  "v_mfma_f32_16x16x32_bf16 v[20:23], v[60:63], v[156:159], v[20:23]\n"
  "v_xor_b32 v110, 0x2000, v110\n"
  "v_mfma_f32_16x16x32_bf16 v[20:23], v[52:55], v[132:135], v[20:23]\n"
  "v_mfma_f32_16x16x32_bf16 v[20:23], v[56:59], a[236:239], v[20:23]\n"
  "v_mfma_f32_16x16x32_bf16 v[20:23], v[48:51], a[212:215], v[20:23]\n"
  "B_H_%=:\n"
  "s_waitcnt vmcnt(25) lgkmcnt(0)\n"
  "s_barrier\n"
  "v_mfma_f32_16x16x32_bf16 v[20:23], v[64:67], v[180:183], v[20:23]\n"
  "s_add_u32 m0, s66, 0\n"
  "ds_read_b128 v[48:51], v104 offset:0\n"
  "ds_read_b128 v[84:87], v111\n"
  "v_mfma_f32_16x16x32_bf16 v[20:23], v[72:75], v[204:207], v[20:23]\n"
  "s_sub_u32 s56, s56, 1\n"
  "ds_read_b128 v[96:99], v111 offset:64\n"
  "v_xor_b32 v111, 0x2000, v111\n"
  "v_mfma_f32_16x16x32_bf16 v[20:23], v[68:71], v[228:231], v[20:23]\n"
  "ds_read_b128 v[52:55], v104 offset:512\n"
  "v_mfma_f32_16x16x32_bf16 v[44:47], v[76:79], v[252:255], v[20:23]\n"
  "buffer_load_dwordx4 v123, s[44:47], s67 offen offset:0 nt lds\n"
  "v_mfma_f32_16x16x32_bf16 v[16:19], v[76:79], v[248:251], v[16:19]\n"
  "s_add_u32 m0, s66, 1920\n"
  "v_mfma_f32_16x16x32_bf16 v[16:19], v[68:71], v[224:227], v[16:19]\n"
  "v_mfma_f32_16x16x32_bf16 v[16:19], v[72:75], v[200:203], v[16:19]\n"
  "ds_read_b128 v[56:59], v105 offset:0\n"
  "v_mfma_f32_16x16x32_bf16 v[40:43], v[64:67], v[176:179], v[16:19]\n"
  "buffer_load_dwordx4 v123, s[44:47], s67 offen offset:128 nt lds\n"
  "v_mfma_f32_16x16x32_bf16 v[12:15], v[64:67], v[172:175], v[12:15]\n"
  "s_add_u32 m0, s66, 3840\n"
  "v_mfma_f32_16x16x32_bf16 v[12:15], v[72:75], v[196:199], v[12:15]\n"
  "v_mfma_f32_16x16x32_bf16 v[12:15], v[68:71], v[220:223], v[12:15]\n"
  "s_waitcnt lgkmcnt(2)\n"
  "ds_read_b128 v[60:63], v105 offset:512\n"
  "v_add_f32 v84, v84, v96\n"
  "v_mfma_f32_16x16x32_bf16 v[36:39], v[76:79], v[244:247], v[12:15]\n"
  "buffer_load_dwordx4 v123, s[44:47], s67 offen offset:256 nt lds\n"
  "v_mfma_f32_16x16x32_bf16 v[8:11], v[76:79], v[240:243], v[8:11]\n"
  "s_mov_b32 m0, 7168\n"
  "v_add_f32 v85, v85, v97\n"
  "v_add_f32 v86, v86, v98\n"
  "v_mfma_f32_16x16x32_bf16 v[8:11], v[68:71], v[216:219], v[8:11]\n"
  "v_add_f32 v87, v87, v99\n"
  "v_max3_f32 v82, v82, v84, v85\n"
  "v_mfma_f32_16x16x32_bf16 v[8:11], v[72:75], v[192:195], v[8:11]\n"
  "v_rcp_f32 v84, v84\n"
  "v_mfma_f32_16x16x32_bf16 v[32:35], v[64:67], v[168:171], v[8:11]\n"
  "buffer_load_dword v125, s[80:83], s73 offen  lds\n"
  "v_mfma_f32_16x16x32_bf16 v[4:7], v[64:67], v[164:167], v[4:7]\n"
  "v_rcp_f32 v85, v85\n"
  "v_mfma_f32_16x16x32_bf16 v[4:7], v[72:75], v[188:191], v[4:7]\n"
  "v_max3_f32 v82, v82, v86, v87\n"
  "v_max3_f32 v83, v83, v84, v85\n"
  "v_mfma_f32_16x16x32_bf16 v[4:7], v[68:71], v[212:215], v[4:7]\n"
  "v_rcp_f32 v86, v86\n"
  "v_mfma_f32_16x16x32_bf16 v[28:31], v[76:79], v[236:239], v[4:7]\n"
  "v_rcp_f32 v87, v87\n"
  "v_mfma_f32_16x16x32_bf16 v[0:3], v[76:79], v[232:235], v[0:3]\n"
  "v_max3_f32 v83, v83, v86, v87\n"
  "v_mul_f32 v96, s99, v84\n"
  "v_mfma_f32_16x16x32_bf16 v[0:3], v[68:71], v[208:211], v[0:3]\n"
  "v_mul_f32 v97, s99, v85\n"
  "v_fma_f32 v40, v40, v84, v96\n"
  "v_mfma_f32_16x16x32_bf16 v[0:3], v[72:75], v[184:187], v[0:3]\n"
  "v_fma_f32 v44, v44, v84, v96\n"
  "v_cvt_pk_bf16_f32 v40, v40, v44\n"
  "v_mfma_f32_16x16x32_bf16 v[24:27], v[64:67], v[160:163], v[0:3]\n"
  "s_waitcnt lgkmcnt(0)\n"
  "v_fma_f32 v41, v41, v85, v97\n"
  "v_fma_f32 v45, v45, v85, v97\n"
  "v_mfma_f32_16x16x32_bf16 v[0:3], v[48:51], a[0:3], v[100:103]\n"
  "ds_read_b128 v[64:67], v104 offset:1024\n"
  "v_cvt_pk_bf16_f32 v41, v41, v45\n"
  "v_mfma_f32_16x16x32_bf16 v[0:3], v[56:59], a[24:27], v[0:3]\n"
  "v_mul_f32 v98, s99, v86\n"
  "v_mul_f32 v99, s99, v87\n"
  "v_mfma_f32_16x16x32_bf16 v[0:3], v[52:55], a[48:51], v[0:3]\n"
  "ds_read_b128 v[68:71], v104 offset:1536\n"
  "v_fma_f32 v42, v42, v86, v98\n"
  "v_mfma_f32_16x16x32_bf16 v[0:3], v[60:63], a[72:75], v[0:3]\n"
  "ds_read_b64_tr_b16 v[92:93], v108 offset:0\n"
  "ds_read_b64_tr_b16 v[94:95], v108 offset:32\n"
  "v_mfma_f32_16x16x32_bf16 v[4:7], v[60:63], a[76:79], v[100:103]\n"
  "v_fma_f32 v46, v46, v86, v98\n"
  "v_cvt_pk_bf16_f32 v42, v42, v46\n"
  "v_mfma_f32_16x16x32_bf16 v[4:7], v[52:55], a[52:55], v[4:7]\n"
  "v_fma_f32 v43, v43, v87, v99\n"
  "v_fma_f32 v47, v47, v87, v99\n"
  "v_mfma_f32_16x16x32_bf16 v[4:7], v[56:59], a[28:31], v[4:7]\n"
  "ds_read_b128 v[72:75], v105 offset:1024\n"
  "v_cvt_pk_bf16_f32 v43, v43, v47\n"
  "v_mfma_f32_16x16x32_bf16 v[4:7], v[48:51], a[4:7], v[4:7]\n"
  "v_fma_f32 v24, v24, v84, v96\n"
  "v_fma_f32 v25, v25, v85, v97\n"
  "v_mfma_f32_16x16x32_bf16 v[8:11], v[48:51], a[8:11], v[100:103]\n"
  "v_fma_f32 v28, v28, v84, v96\n"
  "v_fma_f32 v29, v29, v85, v97\n"
  "v_mfma_f32_16x16x32_bf16 v[8:11], v[56:59], a[32:35], v[8:11]\n"
  "v_fma_f32 v32, v32, v84, v96\n"
  "v_fma_f32 v33, v33, v85, v97\n"
  "v_mfma_f32_16x16x32_bf16 v[8:11], v[52:55], a[56:59], v[8:11]\n"
  "ds_read_b128 v[76:79], v105 offset:1536\n"
  "v_fma_f32 v36, v36, v84, v96\n"
  "v_mfma_f32_16x16x32_bf16 v[8:11], v[60:63], a[80:83], v[8:11]\n"
  "v_fma_f32 v37, v37, v85, v97\n"
  "v_fma_f32 v26, v26, v86, v98\n"
  "v_smfmac_f32_16x16x64_bf16 v[80:83], v[88:91], v[48:55], v124\n"
  "s_waitcnt lgkmcnt(4)\n"
  "v_fma_f32 v27, v27, v87, v99\n"
  "v_smfmac_f32_16x16x64_bf16 v[80:83], v[88:91], v[64:71], v124\n"
  "s_waitcnt lgkmcnt(2)\n"
  "buffer_store_dwordx4 v[92:95], v109, s[48:51], s68 offen offset:0 nt\n"
  "ds_write2_b32 v107, v40, v41 offset0:0 offset1:72\n"
  "v_mfma_f32_16x16x32_bf16 v[12:15], v[60:63], a[84:87], v[100:103]\n"
  "v_fma_f32 v30, v30, v86, v98\n"
  "v_fma_f32 v31, v31, v87, v99\n"
  "v_mfma_f32_16x16x32_bf16 v[12:15], v[52:55], a[60:63], v[12:15]\n"
  "v_fma_f32 v34, v34, v86, v98\n"
  "v_fma_f32 v35, v35, v87, v99\n"
  "v_mfma_f32_16x16x32_bf16 v[12:15], v[56:59], a[36:39], v[12:15]\n"
  "v_fma_f32 v38, v38, v86, v98\n"
  "v_fma_f32 v39, v39, v87, v99\n"
  "v_mfma_f32_16x16x32_bf16 v[12:15], v[48:51], a[12:15], v[12:15]\n"
  "ds_write2_b32 v107, v42, v43 offset0:144 offset1:216\n"
  "v_mfma_f32_16x16x32_bf16 v[16:19], v[48:51], a[16:19], v[100:103]\n"
  "v_cvt_pk_bf16_f32 v24, v24, v28\n"
  "v_cvt_pk_bf16_f32 v28, v25, v29\n"
  "v_mfma_f32_16x16x32_bf16 v[16:19], v[56:59], a[40:43], v[16:19]\n"
  "ds_read_b64_tr_b16 v[96:97], v108 offset:2304\n"
  "ds_read_b64_tr_b16 v[98:99], v108 offset:2336\n"
  "v_mfma_f32_16x16x32_bf16 v[16:19], v[52:55], a[64:67], v[16:19]\n"
  "v_cvt_pk_bf16_f32 v25, v32, v36\n"
  "v_cvt_pk_bf16_f32 v29, v33, v37\n"
  "v_mfma_f32_16x16x32_bf16 v[16:19], v[60:63], a[88:91], v[16:19]\n"
  "v_cvt_pk_bf16_f32 v26, v26, v30\n"
  "v_cvt_pk_bf16_f32 v30, v27, v31\n"
  "v_mfma_f32_16x16x32_bf16 v[20:23], v[60:63], a[92:95], v[100:103]\n"
  "v_cvt_pk_bf16_f32 v27, v34, v38\n"
  "v_cvt_pk_bf16_f32 v31, v35, v39\n"
  "v_mfma_f32_16x16x32_bf16 v[20:23], v[52:55], a[68:71], v[20:23]\n"
  "v_mfma_f32_16x16x32_bf16 v[20:23], v[56:59], a[44:47], v[20:23]\n"
  "v_mfma_f32_16x16x32_bf16 v[20:23], v[48:51], a[20:23], v[20:23]\n"
  "s_waitcnt lgkmcnt(4)\n"
  "v_mfma_f32_16x16x32_bf16 v[20:23], v[64:67], a[116:119], v[20:23]\n"
  "s_add_u32 s66, s66, 6144\n"
  "ds_read_b128 v[48:51], v104 offset:2048\n"
  "v_mfma_f32_16x16x32_bf16 v[20:23], v[72:75], a[140:143], v[20:23]\n"
  "s_cmp_ge_u32 s66, 102400\n"
  "s_cselect_b32 s64, 36864, 0\n"
  "v_mfma_f32_16x16x32_bf16 v[20:23], v[68:71], a[164:167], v[20:23]\n"
  "s_sub_u32 s66, s66, s64\n"
  "ds_read_b128 v[52:55], v104 offset:2560\n"
  "v_mfma_f32_16x16x32_bf16 v[20:23], v[76:79], a[188:191], v[20:23]\n"
  "s_sub_u32 s90, s90, 1\n"
  "s_cselect_b32 s63, s91, 6144\n"
  "s_cmov_b64 s[90:91], s[92:93]\n"
  "s_cmov_b64 s[92:93], s[94:95]\n"
  "s_waitcnt lgkmcnt(2)\n"
  "buffer_store_dwordx4 v[96:99], v109, s[48:51], s68 offen offset:128 nt\n"
  "ds_write_b64 v106, v[24:25] offset:0\n"
  "v_mfma_f32_16x16x32_bf16 v[16:19], v[76:79], a[184:187], v[16:19]\n"
  "s_add_u32 s67, s67, s63\n"
  "v_mfma_f32_16x16x32_bf16 v[16:19], v[68:71], a[160:163], v[16:19]\n"
  "ds_read_b64_tr_b16 v[92:93], v108 offset:4608\n"
  "ds_read_b64_tr_b16 v[94:95], v108 offset:4640\n"
  "v_mfma_f32_16x16x32_bf16 v[16:19], v[72:75], a[136:139], v[16:19]\n"
  "ds_read_b128 v[56:59], v105 offset:2048\n"
  "v_xor_b32 v108, 0x2000, v108\n"
  "v_mfma_f32_16x16x32_bf16 v[16:19], v[64:67], a[112:115], v[16:19]\n"
  "v_mfma_f32_16x16x32_bf16 v[12:15], v[64:67], a[108:111], v[12:15]\n"
  "v_mfma_f32_16x16x32_bf16 v[12:15], v[72:75], a[132:135], v[12:15]\n"
  "v_mfma_f32_16x16x32_bf16 v[12:15], v[68:71], a[156:159], v[12:15]\n"
  "ds_read_b128 v[60:63], v105 offset:2560\n"
  "v_mfma_f32_16x16x32_bf16 v[12:15], v[76:79], a[180:183], v[12:15]\n"
  "v_mfma_f32_16x16x32_bf16 v[8:11], v[76:79], a[176:179], v[8:11]\n"
  "v_mfma_f32_16x16x32_bf16 v[8:11], v[68:71], a[152:155], v[8:11]\n"
  "v_mfma_f32_16x16x32_bf16 v[8:11], v[72:75], a[128:131], v[8:11]\n"
  "v_mfma_f32_16x16x32_bf16 v[8:11], v[64:67], a[104:107], v[8:11]\n"
  "s_waitcnt lgkmcnt(2)\n"
  "buffer_store_dwordx4 v[92:95], v109, s[48:51], s68 offen offset:256 nt\n"
  "ds_write_b64 v106, v[28:29] offset:288\n"
  "v_mfma_f32_16x16x32_bf16 v[4:7], v[64:67], a[100:103], v[4:7]\n"
  "s_add_u32 s68, s68, s69\n"
  "v_mfma_f32_16x16x32_bf16 v[4:7], v[72:75], a[124:127], v[4:7]\n"
  "s_mov_b32 s69, s70\n"
  "v_mfma_f32_16x16x32_bf16 v[4:7], v[68:71], a[148:151], v[4:7]\n"
  "s_movk_i32 s70, 6144\n"
  "v_mfma_f32_16x16x32_bf16 v[4:7], v[76:79], a[172:175], v[4:7]\n"
  "v_mfma_f32_16x16x32_bf16 v[0:3], v[76:79], a[168:171], v[0:3]\n"
  "v_mfma_f32_16x16x32_bf16 v[0:3], v[68:71], a[144:147], v[0:3]\n"
  "v_mfma_f32_16x16x32_bf16 v[0:3], v[72:75], a[120:123], v[0:3]\n"
  "v_mfma_f32_16x16x32_bf16 v[0:3], v[64:67], a[96:99], v[0:3]\n"
  "s_waitcnt lgkmcnt(1)\n"
  "v_mfma_f32_16x16x32_bf16 v[0:3], v[48:51], a[192:195], v[0:3]\n"
  "s_add_u32 s72, s72, 1\n"
  "ds_read_b128 v[64:67], v104 offset:3072\n"
  "v_mfma_f32_16x16x32_bf16 v[0:3], v[56:59], a[216:219], v[0:3]\n"
  "s_lshr_b32 s74, s72, 1\n"
  "v_readlane_b32 s73, v126, s74\n"
  "v_mfma_f32_16x16x32_bf16 v[0:3], v[52:55], a[240:243], v[0:3]\n"
  "s_lshl_b32 s74, s72, 4\n"
  "ds_read_b128 v[68:71], v104 offset:3584\n"
  "v_add_u32 v104, s88, v104\n"
  "v_mfma_f32_16x16x32_bf16 v[0:3], v[60:63], v[136:139], v[0:3]\n"
  "ds_write_b64 v106, v[26:27] offset:576\n"
  "v_mfma_f32_16x16x32_bf16 v[4:7], v[60:63], v[140:143], v[4:7]\n"
  "s_lshr_b32 s73, s73, s74\n"
  "v_mfma_f32_16x16x32_bf16 v[4:7], v[52:55], a[244:247], v[4:7]\n"
  "s_and_b32 s73, s73, 0xffff\n"
  "v_mfma_f32_16x16x32_bf16 v[4:7], v[56:59], a[220:223], v[4:7]\n"
  "s_lshl_b32 s73, s73, 13\n"
  "ds_read_b128 v[72:75], v105 offset:3072\n"
  "v_mfma_f32_16x16x32_bf16 v[4:7], v[48:51], a[196:199], v[4:7]\n"
  "ds_write_b64 v106, v[30:31] offset:864\n"
  "v_mfma_f32_16x16x32_bf16 v[8:11], v[48:51], a[200:203], v[8:11]\n"
  "v_mfma_f32_16x16x32_bf16 v[8:11], v[56:59], a[224:227], v[8:11]\n"
  "v_mfma_f32_16x16x32_bf16 v[8:11], v[52:55], a[248:251], v[8:11]\n"
  "ds_read_b128 v[76:79], v105 offset:3584\n"
  "v_add_u32 v105, s88, v105\n"
  "v_mfma_f32_16x16x32_bf16 v[8:11], v[60:63], v[144:147], v[8:11]\n"
  "s_add_u32 s88, s88, s89\n"
  "v_smfmac_f32_16x16x64_bf16 v[80:83], v[88:91], v[48:55], v124\n"
  "s_waitcnt lgkmcnt(4)\n"
  "v_smfmac_f32_16x16x64_bf16 v[80:83], v[88:91], v[64:71], v124\n"
  "s_sub_u32 s88, 0, s88\n"
  "v_mfma_f32_16x16x32_bf16 v[12:15], v[60:63], v[148:151], v[12:15]\n"
  "s_xor_b32 s88, s88, s89\n"
  "v_mfma_f32_16x16x32_bf16 v[12:15], v[52:55], a[252:255], v[12:15]\n"
  "s_xor_b32 s89, s88, s89\n"
  "v_mfma_f32_16x16x32_bf16 v[12:15], v[56:59], a[228:231], v[12:15]\n"
  "s_xor_b32 s88, s88, s89\n"
  "v_add_f32 v44, v80, v81\n"
  "v_mov_b32 v80, 0\n"
  "v_mfma_f32_16x16x32_bf16 v[12:15], v[48:51], a[204:207], v[12:15]\n"
  "ds_write_b32 v110, v44\n"
  "v_mfma_f32_16x16x32_bf16 v[16:19], v[48:51], a[208:211], v[16:19]\n"
  "v_mov_b32 v81, 0\n"
  "v_mfma_f32_16x16x32_bf16 v[16:19], v[56:59], a[232:235], v[16:19]\n"
  "v_mfma_f32_16x16x32_bf16 v[16:19], v[52:55], v[128:131], v[16:19]\n"
  "s_sub_u32 s62, s56, 1\n"
  "s_cmp_lt_u32 s62, s61\n"
  "v_mfma_f32_16x16x32_bf16 v[16:19], v[60:63], v[152:155], v[16:19]\n"
  "v_xor_b32 v106, 0x2000, v106\n"
  "v_xor_b32 v107, 0x2000, v107\n"
  "s_cbranch_scc1 B_H0_%=\n"
  "v_mfma_f32_16x16x32_bf16 v[20:23], v[60:63], v[156:159], v[20:23]\n"
  "v_xor_b32 v110, 0x2000, v110\n"
  "v_mfma_f32_16x16x32_bf16 v[20:23], v[52:55], v[132:135], v[20:23]\n"
  "v_mfma_f32_16x16x32_bf16 v[20:23], v[56:59], a[236:239], v[20:23]\n"
  "v_mfma_f32_16x16x32_bf16 v[20:23], v[48:51], a[212:215], v[20:23]\n"
  "s_cmp_lg_u32 s56, 0\n"
  "s_cbranch_scc1 B_H1_%=\n"
  "s_mov_b32 s52, s53\n"
  "s_mov_b32 s53, s54\n"
  "s_mov_b32 s54, s55\n"
  "s_cmp_eq_u32 s52, 0\n"
  "s_cbranch_scc1 B_LAST_%=\n"
  "s_lshr_b32 s62, s52, 16\n"
  "s_lshl1_add_u32 s62, s62, s65\n"
  "s_mul_i32 s57, s62, 98304\n"
  "s_and_b32 s56, s52, 0xff\n"
  "s_cmp_lg_u32 s53, 0\n"
  "s_cselect_b32 s62, 6, 0\n"
  "s_sub_u32 s56, s56, s62\n"
  "s_waitcnt vmcnt(13) lgkmcnt(0)\n"
  "s_barrier\n"
  "v_mfma_f32_16x16x32_bf16 v[20:23], v[64:67], v[180:183], v[20:23]\n"
  "s_add_u32 m0, s66, 0\n"
  "ds_read_b128 v[48:51], v104 offset:0\n"
  "ds_read_b128 v[84:87], v111\n"
  "v_mfma_f32_16x16x32_bf16 v[20:23], v[72:75], v[204:207], v[20:23]\n"
  "ds_read_b128 v[96:99], v111 offset:64\n"
  "v_xor_b32 v111, 0x2000, v111\n"
  "v_mfma_f32_16x16x32_bf16 v[20:23], v[68:71], v[228:231], v[20:23]\n"
  "ds_read_b128 v[52:55], v104 offset:512\n"
  "v_mfma_f32_16x16x32_bf16 v[44:47], v[76:79], v[252:255], v[20:23]\n"
  "buffer_load_dwordx4 v123, s[44:47], s67 offen offset:0 nt lds\n"
  "v_mfma_f32_16x16x32_bf16 v[16:19], v[76:79], v[248:251], v[16:19]\n"
  "s_add_u32 m0, s66, 1920\n"
  "v_mfma_f32_16x16x32_bf16 v[16:19], v[68:71], v[224:227], v[16:19]\n"
  "v_mfma_f32_16x16x32_bf16 v[16:19], v[72:75], v[200:203], v[16:19]\n"
  "ds_read_b128 v[56:59], v105 offset:0\n"
  "v_mfma_f32_16x16x32_bf16 v[40:43], v[64:67], v[176:179], v[16:19]\n"
  "buffer_load_dwordx4 v123, s[44:47], s67 offen offset:128 nt lds\n"
  "v_mfma_f32_16x16x32_bf16 v[12:15], v[64:67], v[172:175], v[12:15]\n"
  "s_add_u32 m0, s66, 3840\n"
  "v_mfma_f32_16x16x32_bf16 v[12:15], v[72:75], v[196:199], v[12:15]\n"
  "v_mfma_f32_16x16x32_bf16 v[12:15], v[68:71], v[220:223], v[12:15]\n"
  "s_waitcnt lgkmcnt(2)\n"
  "ds_read_b128 v[60:63], v105 offset:512\n"
  "v_add_f32 v84, v84, v96\n"
  "v_mfma_f32_16x16x32_bf16 v[36:39], v[76:79], v[244:247], v[12:15]\n"
  "buffer_load_dwordx4 v123, s[44:47], s67 offen offset:256 nt lds\n"
  "v_mfma_f32_16x16x32_bf16 v[8:11], v[76:79], v[240:243], v[8:11]\n"
  "v_add_f32 v85, v85, v97\n"
  "v_add_f32 v86, v86, v98\n"
  "v_mfma_f32_16x16x32_bf16 v[8:11], v[68:71], v[216:219], v[8:11]\n"
  "v_add_f32 v87, v87, v99\n"
  "v_max3_f32 v82, v82, v84, v85\n"
  "v_mfma_f32_16x16x32_bf16 v[8:11], v[72:75], v[192:195], v[8:11]\n"
  "v_rcp_f32 v84, v84\n"
  "v_mfma_f32_16x16x32_bf16 v[32:35], v[64:67], v[168:171], v[8:11]\n"
  "s_add_u32 s73, s67, s60\n"
  "s_mov_b32 m0, 7168\n"
  "s_nop 0\n"
  "buffer_load_dword v125, s[44:47], s73 offen lds\n"
  "v_mfma_f32_16x16x32_bf16 v[4:7], v[64:67], v[164:167], v[4:7]\n"
  "v_rcp_f32 v85, v85\n"
  "v_mfma_f32_16x16x32_bf16 v[4:7], v[72:75], v[188:191], v[4:7]\n"
  "v_max3_f32 v82, v82, v86, v87\n"
  "v_max3_f32 v83, v83, v84, v85\n"
  "v_mfma_f32_16x16x32_bf16 v[4:7], v[68:71], v[212:215], v[4:7]\n"
  "v_rcp_f32 v86, v86\n"
  "v_mfma_f32_16x16x32_bf16 v[28:31], v[76:79], v[236:239], v[4:7]\n"
  "v_rcp_f32 v87, v87\n"
  "v_mfma_f32_16x16x32_bf16 v[0:3], v[76:79], v[232:235], v[0:3]\n"
  "s_add_u32 m0, s59, 0\n"
  "v_max3_f32 v83, v83, v86, v87\n"
  "v_mul_f32 v96, s99, v84\n"
  "v_mfma_f32_16x16x32_bf16 v[0:3], v[68:71], v[208:211], v[0:3]\n"
  "buffer_load_dwordx4 v112, s[76:79], s57 offen offset:0  lds\n"
  "v_mfma_f32_16x16x32_bf16 v[0:3], v[72:75], v[184:187], v[0:3]\n"
  "v_mul_f32 v97, s99, v85\n"
  "v_fma_f32 v40, v40, v84, v96\n"
  "v_mfma_f32_16x16x32_bf16 v[24:27], v[64:67], v[160:163], v[0:3]\n"
  "s_waitcnt lgkmcnt(0)\n"
  "v_fma_f32 v44, v44, v84, v96\n"
  "v_cvt_pk_bf16_f32 v40, v40, v44\n"
  "v_mfma_f32_16x16x32_bf16 v[0:3], v[48:51], a[0:3], v[100:103]\n"
  "ds_read_b128 v[64:67], v104 offset:1024\n"
  "v_fma_f32 v41, v41, v85, v97\n"
  "v_mfma_f32_16x16x32_bf16 v[0:3], v[56:59], a[24:27], v[0:3]\n"
  "v_fma_f32 v45, v45, v85, v97\n"
  "v_cvt_pk_bf16_f32 v41, v41, v45\n"
  "v_mfma_f32_16x16x32_bf16 v[0:3], v[52:55], a[48:51], v[0:3]\n"
  "ds_read_b128 v[68:71], v104 offset:1536\n"
  "v_mul_f32 v98, s99, v86\n"
  "v_mfma_f32_16x16x32_bf16 v[0:3], v[60:63], a[72:75], v[0:3]\n"
  "ds_read_b64_tr_b16 v[92:93], v108 offset:0\n"
  "ds_read_b64_tr_b16 v[94:95], v108 offset:32\n"
  "v_mfma_f32_16x16x32_bf16 v[4:7], v[60:63], a[76:79], v[100:103]\n"
  "buffer_load_dwordx4 v113, s[76:79], s57 offen offset:1024  lds\n"
  "v_mfma_f32_16x16x32_bf16 v[4:7], v[52:55], a[52:55], v[4:7]\n"
  "v_mul_f32 v99, s99, v87\n"
  "v_fma_f32 v42, v42, v86, v98\n"
  "v_mfma_f32_16x16x32_bf16 v[4:7], v[56:59], a[28:31], v[4:7]\n"
  "ds_read_b128 v[72:75], v105 offset:1024\n"
  "v_fma_f32 v46, v46, v86, v98\n"
  "v_mfma_f32_16x16x32_bf16 v[4:7], v[48:51], a[4:7], v[4:7]\n"
  "v_cvt_pk_bf16_f32 v42, v42, v46\n"
  "v_fma_f32 v43, v43, v87, v99\n"
  "v_mfma_f32_16x16x32_bf16 v[8:11], v[48:51], a[8:11], v[100:103]\n"
  "v_fma_f32 v47, v47, v87, v99\n"
  "v_cvt_pk_bf16_f32 v43, v43, v47\n"
  "v_mfma_f32_16x16x32_bf16 v[8:11], v[56:59], a[32:35], v[8:11]\n"
  "v_fma_f32 v24, v24, v84, v96\n"
  "v_fma_f32 v25, v25, v85, v97\n"
  "v_mfma_f32_16x16x32_bf16 v[8:11], v[52:55], a[56:59], v[8:11]\n"
  "s_add_u32 m0, s59, 1920\n"
  "ds_read_b128 v[76:79], v105 offset:1536\n"
  "v_fma_f32 v28, v28, v84, v96\n"
  "v_mfma_f32_16x16x32_bf16 v[8:11], v[60:63], a[80:83], v[8:11]\n"
  "buffer_load_dwordx4 v112, s[76:79], s57 offen offset:128  lds\n"
  "v_smfmac_f32_16x16x64_bf16 v[80:83], v[88:91], v[48:55], v124\n"
  "s_waitcnt lgkmcnt(4)\n"
  "v_fma_f32 v29, v29, v85, v97\n"
  "v_smfmac_f32_16x16x64_bf16 v[80:83], v[88:91], v[64:71], v124\n"
  "s_waitcnt lgkmcnt(2)\n"
  "buffer_store_dwordx4 v[92:95], v109, s[48:51], s68 offen offset:0 nt\n"
  "ds_write2_b32 v107, v40, v41 offset0:0 offset1:72\n"
  "v_mfma_f32_16x16x32_bf16 v[12:15], v[60:63], a[84:87], v[100:103]\n"
  "v_fma_f32 v32, v32, v84, v96\n"
  "v_fma_f32 v33, v33, v85, v97\n"
  "v_mfma_f32_16x16x32_bf16 v[12:15], v[52:55], a[60:63], v[12:15]\n"
  "v_fma_f32 v36, v36, v84, v96\n"
  "v_fma_f32 v37, v37, v85, v97\n"
  "v_mfma_f32_16x16x32_bf16 v[12:15], v[56:59], a[36:39], v[12:15]\n"
  "v_fma_f32 v26, v26, v86, v98\n"
  "v_fma_f32 v27, v27, v87, v99\n"
  "v_mfma_f32_16x16x32_bf16 v[12:15], v[48:51], a[12:15], v[12:15]\n"
  "ds_write2_b32 v107, v42, v43 offset0:144 offset1:216\n"
  "v_mfma_f32_16x16x32_bf16 v[16:19], v[48:51], a[16:19], v[100:103]\n"
  "buffer_load_dwordx4 v113, s[76:79], s57 offen offset:1152  lds\n"
  "v_mfma_f32_16x16x32_bf16 v[16:19], v[56:59], a[40:43], v[16:19]\n"
  "v_fma_f32 v30, v30, v86, v98\n"
  "v_fma_f32 v31, v31, v87, v99\n"
  "v_mfma_f32_16x16x32_bf16 v[16:19], v[52:55], a[64:67], v[16:19]\n"
  "v_fma_f32 v34, v34, v86, v98\n"
  "v_fma_f32 v35, v35, v87, v99\n"
  "v_mfma_f32_16x16x32_bf16 v[16:19], v[60:63], a[88:91], v[16:19]\n"
  "v_fma_f32 v38, v38, v86, v98\n"
  "v_fma_f32 v39, v39, v87, v99\n"
  "v_mfma_f32_16x16x32_bf16 v[20:23], v[60:63], a[92:95], v[100:103]\n"
  "ds_read_b64_tr_b16 v[96:97], v108 offset:2304\n"
  "ds_read_b64_tr_b16 v[98:99], v108 offset:2336\n"
  "v_mfma_f32_16x16x32_bf16 v[20:23], v[52:55], a[68:71], v[20:23]\n"
  "v_cvt_pk_bf16_f32 v24, v24, v28\n"
  "v_cvt_pk_bf16_f32 v28, v25, v29\n"
  "v_mfma_f32_16x16x32_bf16 v[20:23], v[56:59], a[44:47], v[20:23]\n"
  "s_add_u32 m0, s59, 3840\n"
  "v_cvt_pk_bf16_f32 v25, v32, v36\n"
  "v_cvt_pk_bf16_f32 v29, v33, v37\n"
  "v_mfma_f32_16x16x32_bf16 v[20:23], v[48:51], a[20:23], v[20:23]\n"
  "s_waitcnt lgkmcnt(4)\n"
  "buffer_load_dwordx4 v112, s[76:79], s57 offen offset:256  lds\n"
  "v_mfma_f32_16x16x32_bf16 v[20:23], v[64:67], a[116:119], v[20:23]\n"
  "s_add_u32 s66, s66, 6144\n"
  "ds_read_b128 v[48:51], v104 offset:2048\n"
  "v_cvt_pk_bf16_f32 v26, v26, v30\n"
  "v_mfma_f32_16x16x32_bf16 v[20:23], v[72:75], a[140:143], v[20:23]\n"
  "s_cmp_ge_u32 s66, 102400\n"
  "s_cselect_b32 s64, 36864, 0\n"
  "v_cvt_pk_bf16_f32 v30, v27, v31\n"
  "v_cvt_pk_bf16_f32 v27, v34, v38\n"
  "v_mfma_f32_16x16x32_bf16 v[20:23], v[68:71], a[164:167], v[20:23]\n"
  "s_sub_u32 s66, s66, s64\n"
  "ds_read_b128 v[52:55], v104 offset:2560\n"
  "v_cvt_pk_bf16_f32 v31, v35, v39\n"
  "v_mfma_f32_16x16x32_bf16 v[20:23], v[76:79], a[188:191], v[20:23]\n"
  "s_sub_u32 s90, s90, 1\n"
  "s_cselect_b32 s63, s91, 6144\n"
  "s_cmov_b64 s[90:91], s[92:93]\n"
  "s_cmov_b64 s[92:93], s[94:95]\n"
  "s_waitcnt lgkmcnt(2)\n"
  "buffer_store_dwordx4 v[96:99], v109, s[48:51], s68 offen offset:128 nt\n"
  "ds_write_b64 v106, v[24:25] offset:0\n"
  "v_mfma_f32_16x16x32_bf16 v[16:19], v[76:79], a[184:187], v[16:19]\n"
  "s_add_u32 s67, s67, s63\n"
  "v_mfma_f32_16x16x32_bf16 v[16:19], v[68:71], a[160:163], v[16:19]\n"
  "ds_read_b64_tr_b16 v[92:93], v108 offset:4608\n"
  "ds_read_b64_tr_b16 v[94:95], v108 offset:4640\n"
  "v_mfma_f32_16x16x32_bf16 v[16:19], v[72:75], a[136:139], v[16:19]\n"
  "buffer_load_dwordx4 v113, s[76:79], s57 offen offset:1280  lds\n"
  "ds_read_b128 v[56:59], v105 offset:2048\n"
  "v_mfma_f32_16x16x32_bf16 v[16:19], v[64:67], a[112:115], v[16:19]\n"
  "v_xor_b32 v108, 0x2000, v108\n"
  "v_mfma_f32_16x16x32_bf16 v[12:15], v[64:67], a[108:111], v[12:15]\n"
  "s_add_u32 s57, s57, 384\n"
  "v_mfma_f32_16x16x32_bf16 v[12:15], v[72:75], a[132:135], v[12:15]\n"
  "v_mfma_f32_16x16x32_bf16 v[12:15], v[68:71], a[156:159], v[12:15]\n"
  "ds_read_b128 v[60:63], v105 offset:2560\n"
  "v_mfma_f32_16x16x32_bf16 v[12:15], v[76:79], a[180:183], v[12:15]\n"
  "v_mfma_f32_16x16x32_bf16 v[8:11], v[76:79], a[176:179], v[8:11]\n"
  "s_add_u32 m0, s59, 5888\n"
  "v_mfma_f32_16x16x32_bf16 v[8:11], v[68:71], a[152:155], v[8:11]\n"
  "buffer_load_dwordx4 v112, s[76:79], s57 offen offset:256  lds\n"
  "v_mfma_f32_16x16x32_bf16 v[8:11], v[72:75], a[128:131], v[8:11]\n"
  "v_mfma_f32_16x16x32_bf16 v[8:11], v[64:67], a[104:107], v[8:11]\n"
  "s_waitcnt lgkmcnt(2)\n"
  "buffer_store_dwordx4 v[92:95], v109, s[48:51], s68 offen offset:256 nt\n"
  "ds_write_b64 v106, v[28:29] offset:288\n"
  "v_mfma_f32_16x16x32_bf16 v[4:7], v[64:67], a[100:103], v[4:7]\n"
  "s_add_u32 s68, s68, s69\n"
  "v_mfma_f32_16x16x32_bf16 v[4:7], v[72:75], a[124:127], v[4:7]\n"
  "s_mov_b32 s69, s70\n"
  "v_mfma_f32_16x16x32_bf16 v[4:7], v[68:71], a[148:151], v[4:7]\n"
  "s_movk_i32 s70, 6144\n"
  "v_mfma_f32_16x16x32_bf16 v[4:7], v[76:79], a[172:175], v[4:7]\n"
  "v_mfma_f32_16x16x32_bf16 v[0:3], v[76:79], a[168:171], v[0:3]\n"
  "buffer_load_dwordx4 v113, s[76:79], s57 offen offset:1280  lds\n"
  "v_mfma_f32_16x16x32_bf16 v[0:3], v[68:71], a[144:147], v[0:3]\n"
  "v_mfma_f32_16x16x32_bf16 v[0:3], v[72:75], a[120:123], v[0:3]\n"
  "v_mfma_f32_16x16x32_bf16 v[0:3], v[64:67], a[96:99], v[0:3]\n"
  "s_waitcnt lgkmcnt(1)\n"
  "v_mfma_f32_16x16x32_bf16 v[0:3], v[48:51], a[192:195], v[0:3]\n"
  "ds_read_b128 v[64:67], v104 offset:3072\n"
  "v_mfma_f32_16x16x32_bf16 v[0:3], v[56:59], a[216:219], v[0:3]\n"
  "v_mfma_f32_16x16x32_bf16 v[0:3], v[52:55], a[240:243], v[0:3]\n"
  "s_add_u32 m0, s59, 8064\n"
  "ds_read_b128 v[68:71], v104 offset:3584\n"
  "v_add_u32 v104, s88, v104\n"
  "v_mfma_f32_16x16x32_bf16 v[0:3], v[60:63], v[136:139], v[0:3]\n"
  "ds_write_b64 v106, v[26:27] offset:576\n"
  "v_mfma_f32_16x16x32_bf16 v[4:7], v[60:63], v[140:143], v[4:7]\n"
  "buffer_load_dwordx4 v112, s[76:79], s57 offen offset:128  lds\n"
  "v_mfma_f32_16x16x32_bf16 v[4:7], v[52:55], a[244:247], v[4:7]\n"
  "v_mfma_f32_16x16x32_bf16 v[4:7], v[56:59], a[220:223], v[4:7]\n"
  "ds_read_b128 v[72:75], v105 offset:3072\n"
  "v_mfma_f32_16x16x32_bf16 v[4:7], v[48:51], a[196:199], v[4:7]\n"
  "ds_write_b64 v106, v[30:31] offset:864\n"
  "v_mfma_f32_16x16x32_bf16 v[8:11], v[48:51], a[200:203], v[8:11]\n"
  "v_mfma_f32_16x16x32_bf16 v[8:11], v[56:59], a[224:227], v[8:11]\n"
  "v_mfma_f32_16x16x32_bf16 v[8:11], v[52:55], a[248:251], v[8:11]\n"
  "buffer_load_dwordx4 v113, s[76:79], s57 offen offset:1152  lds\n"
  "ds_read_b128 v[76:79], v105 offset:3584\n"
  "v_mfma_f32_16x16x32_bf16 v[8:11], v[60:63], v[144:147], v[8:11]\n"
  "v_add_u32 v105, s88, v105\n"
  "v_smfmac_f32_16x16x64_bf16 v[80:83], v[88:91], v[48:55], v124\n"
  "s_waitcnt lgkmcnt(4)\n"
  "v_smfmac_f32_16x16x64_bf16 v[80:83], v[88:91], v[64:71], v124\n"
  "s_add_u32 s88, s88, s89\n"
  "v_mfma_f32_16x16x32_bf16 v[12:15], v[60:63], v[148:151], v[12:15]\n"
  "s_sub_u32 s88, 0, s88\n"
  "v_mfma_f32_16x16x32_bf16 v[12:15], v[52:55], a[252:255], v[12:15]\n"
  "s_xor_b32 s88, s88, s89\n"
  "v_mfma_f32_16x16x32_bf16 v[12:15], v[56:59], a[228:231], v[12:15]\n"
  "s_xor_b32 s89, s88, s89\n"
  "v_add_f32 v44, v80, v81\n"
  "v_mov_b32 v80, 0\n"
  "v_mfma_f32_16x16x32_bf16 v[12:15], v[48:51], a[204:207], v[12:15]\n"
  "s_xor_b32 s88, s88, s89\n"
  "ds_write_b32 v110, v44\n"
  "v_mfma_f32_16x16x32_bf16 v[16:19], v[48:51], a[208:211], v[16:19]\n"
  "v_mov_b32 v81, 0\n"
  "v_mfma_f32_16x16x32_bf16 v[16:19], v[56:59], a[232:235], v[16:19]\n"
  "v_mfma_f32_16x16x32_bf16 v[16:19], v[52:55], v[128:131], v[16:19]\n"
  "v_mfma_f32_16x16x32_bf16 v[16:19], v[60:63], v[152:155], v[16:19]\n"
  "v_xor_b32 v106, 0x2000, v106\n"
  "v_xor_b32 v107, 0x2000, v107\n"
  "v_mfma_f32_16x16x32_bf16 v[20:23], v[60:63], v[156:159], v[20:23]\n"
  "v_xor_b32 v110, 0x2000, v110\n"
  "v_mfma_f32_16x16x32_bf16 v[20:23], v[52:55], v[132:135], v[20:23]\n"
  "v_mfma_f32_16x16x32_bf16 v[20:23], v[56:59], a[236:239], v[20:23]\n"
  "v_mfma_f32_16x16x32_bf16 v[20:23], v[48:51], a[212:215], v[20:23]\n"
  "s_waitcnt vmcnt(27) lgkmcnt(0)\n"
  "s_barrier\n"
  "v_mfma_f32_16x16x32_bf16 v[20:23], v[64:67], v[180:183], v[20:23]\n"
  "s_add_u32 m0, s66, 0\n"
  "ds_read_b128 v[48:51], v104 offset:0\n"
  "ds_read_b128 v[84:87], v111\n"
  "v_mfma_f32_16x16x32_bf16 v[20:23], v[72:75], v[204:207], v[20:23]\n"
  "ds_read_b128 v[96:99], v111 offset:64\n"
  "v_xor_b32 v111, 0x2000, v111\n"
  "v_mfma_f32_16x16x32_bf16 v[20:23], v[68:71], v[228:231], v[20:23]\n"
  "ds_read_b128 v[52:55], v104 offset:512\n"
  "v_mfma_f32_16x16x32_bf16 v[44:47], v[76:79], v[252:255], v[20:23]\n"
  "buffer_load_dwordx4 v123, s[44:47], s67 offen offset:0 nt lds\n"
  "v_mfma_f32_16x16x32_bf16 v[16:19], v[76:79], v[248:251], v[16:19]\n"
  "s_add_u32 m0, s66, 1920\n"
  "v_mfma_f32_16x16x32_bf16 v[16:19], v[68:71], v[224:227], v[16:19]\n"
  "v_mfma_f32_16x16x32_bf16 v[16:19], v[72:75], v[200:203], v[16:19]\n"
  "ds_read_b128 v[56:59], v105 offset:0\n"
  "v_mfma_f32_16x16x32_bf16 v[40:43], v[64:67], v[176:179], v[16:19]\n"
  "buffer_load_dwordx4 v123, s[44:47], s67 offen offset:128 nt lds\n"
  "v_mfma_f32_16x16x32_bf16 v[12:15], v[64:67], v[172:175], v[12:15]\n"
  "s_add_u32 m0, s66, 3840\n"
  "v_mfma_f32_16x16x32_bf16 v[12:15], v[72:75], v[196:199], v[12:15]\n"
  "v_mfma_f32_16x16x32_bf16 v[12:15], v[68:71], v[220:223], v[12:15]\n"
  "s_waitcnt lgkmcnt(2)\n"
  "ds_read_b128 v[60:63], v105 offset:512\n"
  "v_add_f32 v84, v84, v96\n"
  "v_mfma_f32_16x16x32_bf16 v[36:39], v[76:79], v[244:247], v[12:15]\n"
  "buffer_load_dwordx4 v123, s[44:47], s67 offen offset:256 nt lds\n"
  "v_mfma_f32_16x16x32_bf16 v[8:11], v[76:79], v[240:243], v[8:11]\n"
  "v_add_f32 v85, v85, v97\n"
  "v_add_f32 v86, v86, v98\n"
  "v_mfma_f32_16x16x32_bf16 v[8:11], v[68:71], v[216:219], v[8:11]\n"
  "v_add_f32 v87, v87, v99\n"
  "v_max3_f32 v82, v82, v84, v85\n"
  "v_mfma_f32_16x16x32_bf16 v[8:11], v[72:75], v[192:195], v[8:11]\n"
  "v_rcp_f32 v84, v84\n"
  "v_mfma_f32_16x16x32_bf16 v[32:35], v[64:67], v[168:171], v[8:11]\n"
  "v_rcp_f32 v85, v85\n"
  "v_mfma_f32_16x16x32_bf16 v[4:7], v[64:67], v[164:167], v[4:7]\n"
  "v_max3_f32 v82, v82, v86, v87\n"
  "v_max3_f32 v83, v83, v84, v85\n"
  "v_mfma_f32_16x16x32_bf16 v[4:7], v[72:75], v[188:191], v[4:7]\n"
  "s_add_u32 s73, s73, 14336\n"
  "s_mov_b32 m0, 7168\n"
  "s_nop 0\n"
  "buffer_load_dword v125, s[44:47], s73 offen lds\n"
  "v_mfma_f32_16x16x32_bf16 v[4:7], v[68:71], v[212:215], v[4:7]\n"
  "v_rcp_f32 v86, v86\n"
  "v_mfma_f32_16x16x32_bf16 v[28:31], v[76:79], v[236:239], v[4:7]\n"
  "v_rcp_f32 v87, v87\n"
  "v_mfma_f32_16x16x32_bf16 v[0:3], v[76:79], v[232:235], v[0:3]\n"
  "s_add_u32 m0, s59, 10240\n"
  "v_max3_f32 v83, v83, v86, v87\n"
  "v_mul_f32 v96, s99, v84\n"
  "v_mfma_f32_16x16x32_bf16 v[0:3], v[68:71], v[208:211], v[0:3]\n"
  "buffer_load_dwordx4 v112, s[76:79], s57 offen offset:0  lds\n"
  "v_mfma_f32_16x16x32_bf16 v[0:3], v[72:75], v[184:187], v[0:3]\n"
  "v_mul_f32 v97, s99, v85\n"
  "v_fma_f32 v40, v40, v84, v96\n"
  "v_mfma_f32_16x16x32_bf16 v[24:27], v[64:67], v[160:163], v[0:3]\n"
  "s_waitcnt lgkmcnt(0)\n"
  "v_fma_f32 v44, v44, v84, v96\n"
  "v_cvt_pk_bf16_f32 v40, v40, v44\n"
  "v_mfma_f32_16x16x32_bf16 v[0:3], v[48:51], a[0:3], v[100:103]\n"
  "ds_read_b128 v[64:67], v104 offset:1024\n"
  "v_fma_f32 v41, v41, v85, v97\n"
  "v_mfma_f32_16x16x32_bf16 v[0:3], v[56:59], a[24:27], v[0:3]\n"
  "v_fma_f32 v45, v45, v85, v97\n"
  "v_cvt_pk_bf16_f32 v41, v41, v45\n"
  "v_mfma_f32_16x16x32_bf16 v[0:3], v[52:55], a[48:51], v[0:3]\n"
  "ds_read_b128 v[68:71], v104 offset:1536\n"
  "v_mul_f32 v98, s99, v86\n"
  "v_mfma_f32_16x16x32_bf16 v[0:3], v[60:63], a[72:75], v[0:3]\n"
  "ds_read_b64_tr_b16 v[92:93], v108 offset:0\n"
  "ds_read_b64_tr_b16 v[94:95], v108 offset:32\n"
  "v_mfma_f32_16x16x32_bf16 v[4:7], v[60:63], a[76:79], v[100:103]\n"
  "buffer_load_dwordx4 v113, s[76:79], s57 offen offset:1024  lds\n"
  "v_mfma_f32_16x16x32_bf16 v[4:7], v[52:55], a[52:55], v[4:7]\n"
  "v_mul_f32 v99, s99, v87\n"
  "v_fma_f32 v42, v42, v86, v98\n"
  "v_mfma_f32_16x16x32_bf16 v[4:7], v[56:59], a[28:31], v[4:7]\n"
  "s_add_u32 s57, s57, 11904\n"
  "ds_read_b128 v[72:75], v105 offset:1024\n"
  "v_fma_f32 v46, v46, v86, v98\n"
  "v_mfma_f32_16x16x32_bf16 v[4:7], v[48:51], a[4:7], v[4:7]\n"
  "v_cvt_pk_bf16_f32 v42, v42, v46\n"
  "v_fma_f32 v43, v43, v87, v99\n"
  "v_mfma_f32_16x16x32_bf16 v[8:11], v[48:51], a[8:11], v[100:103]\n"
  "v_fma_f32 v47, v47, v87, v99\n"
  "v_cvt_pk_bf16_f32 v43, v43, v47\n"
  "v_mfma_f32_16x16x32_bf16 v[8:11], v[56:59], a[32:35], v[8:11]\n"
  "v_fma_f32 v24, v24, v84, v96\n"
  "v_fma_f32 v25, v25, v85, v97\n"
  "v_mfma_f32_16x16x32_bf16 v[8:11], v[52:55], a[56:59], v[8:11]\n"
  "s_add_u32 m0, s59, 12288\n"
  "ds_read_b128 v[76:79], v105 offset:1536\n"
  "v_fma_f32 v28, v28, v84, v96\n"
  "v_mfma_f32_16x16x32_bf16 v[8:11], v[60:63], a[80:83], v[8:11]\n"
  "buffer_load_dwordx4 v112, s[76:79], s57 offen offset:0  lds\n"
  "v_smfmac_f32_16x16x64_bf16 v[80:83], v[88:91], v[48:55], v124\n"
  "s_waitcnt lgkmcnt(4)\n"
  "v_fma_f32 v29, v29, v85, v97\n"
  "v_smfmac_f32_16x16x64_bf16 v[80:83], v[88:91], v[64:71], v124\n"
  "s_waitcnt lgkmcnt(2)\n"
  "buffer_store_dwordx4 v[92:95], v109, s[48:51], s68 offen offset:0 nt\n"
  "ds_write2_b32 v107, v40, v41 offset0:0 offset1:72\n"
  "v_mfma_f32_16x16x32_bf16 v[12:15], v[60:63], a[84:87], v[100:103]\n"
  "v_fma_f32 v32, v32, v84, v96\n"
  "v_fma_f32 v33, v33, v85, v97\n"
  "v_mfma_f32_16x16x32_bf16 v[12:15], v[52:55], a[60:63], v[12:15]\n"
  "v_fma_f32 v36, v36, v84, v96\n"
  "v_fma_f32 v37, v37, v85, v97\n"
  "v_mfma_f32_16x16x32_bf16 v[12:15], v[56:59], a[36:39], v[12:15]\n"
  "v_fma_f32 v26, v26, v86, v98\n"
  "v_fma_f32 v27, v27, v87, v99\n"
  "v_mfma_f32_16x16x32_bf16 v[12:15], v[48:51], a[12:15], v[12:15]\n"
  "ds_write2_b32 v107, v42, v43 offset0:144 offset1:216\n"
  "v_mfma_f32_16x16x32_bf16 v[16:19], v[48:51], a[16:19], v[100:103]\n"
  "buffer_load_dwordx4 v113, s[76:79], s57 offen offset:1024  lds\n"
  "v_mfma_f32_16x16x32_bf16 v[16:19], v[56:59], a[40:43], v[16:19]\n"
  "v_fma_f32 v30, v30, v86, v98\n"
  "v_fma_f32 v31, v31, v87, v99\n"
  "v_mfma_f32_16x16x32_bf16 v[16:19], v[52:55], a[64:67], v[16:19]\n"
  "v_fma_f32 v34, v34, v86, v98\n"
  "v_fma_f32 v35, v35, v87, v99\n"
  "v_mfma_f32_16x16x32_bf16 v[16:19], v[60:63], a[88:91], v[16:19]\n"
  "v_fma_f32 v38, v38, v86, v98\n"
  "v_fma_f32 v39, v39, v87, v99\n"
  "v_mfma_f32_16x16x32_bf16 v[20:23], v[60:63], a[92:95], v[100:103]\n"
  "ds_read_b64_tr_b16 v[96:97], v108 offset:2304\n"
  "ds_read_b64_tr_b16 v[98:99], v108 offset:2336\n"
  "v_mfma_f32_16x16x32_bf16 v[20:23], v[52:55], a[68:71], v[20:23]\n"
  "v_cvt_pk_bf16_f32 v24, v24, v28\n"
  "v_cvt_pk_bf16_f32 v28, v25, v29\n"
  "v_mfma_f32_16x16x32_bf16 v[20:23], v[56:59], a[44:47], v[20:23]\n"
  "s_add_u32 m0, s59, 14208\n"
  "v_cvt_pk_bf16_f32 v25, v32, v36\n"
  "v_cvt_pk_bf16_f32 v29, v33, v37\n"
  "v_mfma_f32_16x16x32_bf16 v[20:23], v[48:51], a[20:23], v[20:23]\n"
  "s_waitcnt lgkmcnt(4)\n"
  "buffer_load_dwordx4 v112, s[76:79], s57 offen offset:128  lds\n"
  "v_mfma_f32_16x16x32_bf16 v[20:23], v[64:67], a[116:119], v[20:23]\n"
  "s_add_u32 s66, s66, 6144\n"
  "ds_read_b128 v[48:51], v104 offset:2048\n"
  "v_cvt_pk_bf16_f32 v26, v26, v30\n"
  "v_mfma_f32_16x16x32_bf16 v[20:23], v[72:75], a[140:143], v[20:23]\n"
  "s_cmp_ge_u32 s66, 102400\n"
  "s_cselect_b32 s64, 36864, 0\n"
  "v_cvt_pk_bf16_f32 v30, v27, v31\n"
  "v_cvt_pk_bf16_f32 v27, v34, v38\n"
  "v_mfma_f32_16x16x32_bf16 v[20:23], v[68:71], a[164:167], v[20:23]\n"
  "s_sub_u32 s66, s66, s64\n"
  "ds_read_b128 v[52:55], v104 offset:2560\n"
  "v_cvt_pk_bf16_f32 v31, v35, v39\n"
  "v_mfma_f32_16x16x32_bf16 v[20:23], v[76:79], a[188:191], v[20:23]\n"
  "s_sub_u32 s90, s90, 1\n"
  "s_cselect_b32 s63, s91, 6144\n"
  "s_cmov_b64 s[90:91], s[92:93]\n"
  "s_cmov_b64 s[92:93], s[94:95]\n"
  "s_waitcnt lgkmcnt(2)\n"
  "buffer_store_dwordx4 v[96:99], v109, s[48:51], s68 offen offset:128 nt\n"
  "ds_write_b64 v106, v[24:25] offset:0\n"
  "v_mfma_f32_16x16x32_bf16 v[16:19], v[76:79], a[184:187], v[16:19]\n"
  "s_add_u32 s67, s67, s63\n"
  "v_mfma_f32_16x16x32_bf16 v[16:19], v[68:71], a[160:163], v[16:19]\n"
  "ds_read_b64_tr_b16 v[92:93], v108 offset:4608\n"
  "ds_read_b64_tr_b16 v[94:95], v108 offset:4640\n"
  "v_mfma_f32_16x16x32_bf16 v[16:19], v[72:75], a[136:139], v[16:19]\n"
  "buffer_load_dwordx4 v113, s[76:79], s57 offen offset:1152  lds\n"
  "ds_read_b128 v[56:59], v105 offset:2048\n"
  "v_mfma_f32_16x16x32_bf16 v[16:19], v[64:67], a[112:115], v[16:19]\n"
  "v_xor_b32 v108, 0x2000, v108\n"
  "v_mfma_f32_16x16x32_bf16 v[12:15], v[64:67], a[108:111], v[12:15]\n"
  "v_mfma_f32_16x16x32_bf16 v[12:15], v[72:75], a[132:135], v[12:15]\n"
  "v_mfma_f32_16x16x32_bf16 v[12:15], v[68:71], a[156:159], v[12:15]\n"
  "ds_read_b128 v[60:63], v105 offset:2560\n"
  "v_mfma_f32_16x16x32_bf16 v[12:15], v[76:79], a[180:183], v[12:15]\n"
  "v_mfma_f32_16x16x32_bf16 v[8:11], v[76:79], a[176:179], v[8:11]\n"
  "s_add_u32 m0, s59, 16128\n"
  "v_mfma_f32_16x16x32_bf16 v[8:11], v[68:71], a[152:155], v[8:11]\n"
  "buffer_load_dwordx4 v112, s[76:79], s57 offen offset:256  lds\n"
  "v_mfma_f32_16x16x32_bf16 v[8:11], v[72:75], a[128:131], v[8:11]\n"
  "v_mfma_f32_16x16x32_bf16 v[8:11], v[64:67], a[104:107], v[8:11]\n"
  "s_waitcnt lgkmcnt(2)\n"
  "buffer_store_dwordx4 v[92:95], v109, s[48:51], s68 offen offset:256 nt\n"
  "ds_write_b64 v106, v[28:29] offset:288\n"
  "v_mfma_f32_16x16x32_bf16 v[4:7], v[64:67], a[100:103], v[4:7]\n"
  "s_add_u32 s68, s68, s69\n"
  "v_mfma_f32_16x16x32_bf16 v[4:7], v[72:75], a[124:127], v[4:7]\n"
  "s_mov_b32 s69, s70\n"
  "v_mfma_f32_16x16x32_bf16 v[4:7], v[68:71], a[148:151], v[4:7]\n"
  "s_movk_i32 s70, 6144\n"
  "v_mfma_f32_16x16x32_bf16 v[4:7], v[76:79], a[172:175], v[4:7]\n"
  "v_mfma_f32_16x16x32_bf16 v[0:3], v[76:79], a[168:171], v[0:3]\n"
  "buffer_load_dwordx4 v113, s[76:79], s57 offen offset:1280  lds\n"
  "v_mfma_f32_16x16x32_bf16 v[0:3], v[68:71], a[144:147], v[0:3]\n"
  "v_mfma_f32_16x16x32_bf16 v[0:3], v[72:75], a[120:123], v[0:3]\n"
  "s_add_u32 s57, s57, 384\n"
  "v_mfma_f32_16x16x32_bf16 v[0:3], v[64:67], a[96:99], v[0:3]\n"
  "s_waitcnt lgkmcnt(1)\n"
  "v_mfma_f32_16x16x32_bf16 v[0:3], v[48:51], a[192:195], v[0:3]\n"
  "ds_read_b128 v[64:67], v104 offset:3072\n"
  "v_mfma_f32_16x16x32_bf16 v[0:3], v[56:59], a[216:219], v[0:3]\n"
  "v_mfma_f32_16x16x32_bf16 v[0:3], v[52:55], a[240:243], v[0:3]\n"
  "s_add_u32 m0, s59, 18176\n"
  "ds_read_b128 v[68:71], v104 offset:3584\n"
  "v_add_u32 v104, s88, v104\n"
  "v_mfma_f32_16x16x32_bf16 v[0:3], v[60:63], v[136:139], v[0:3]\n"
  "ds_write_b64 v106, v[26:27] offset:576\n"
  "v_mfma_f32_16x16x32_bf16 v[4:7], v[60:63], v[140:143], v[4:7]\n"
  "buffer_load_dwordx4 v112, s[76:79], s57 offen offset:256  lds\n"
  "v_mfma_f32_16x16x32_bf16 v[4:7], v[52:55], a[244:247], v[4:7]\n"
  "v_mfma_f32_16x16x32_bf16 v[4:7], v[56:59], a[220:223], v[4:7]\n"
  "ds_read_b128 v[72:75], v105 offset:3072\n"
  "v_mfma_f32_16x16x32_bf16 v[4:7], v[48:51], a[196:199], v[4:7]\n"
  "ds_write_b64 v106, v[30:31] offset:864\n"
  "v_mfma_f32_16x16x32_bf16 v[8:11], v[48:51], a[200:203], v[8:11]\n"
  "v_mfma_f32_16x16x32_bf16 v[8:11], v[56:59], a[224:227], v[8:11]\n"
  "v_mfma_f32_16x16x32_bf16 v[8:11], v[52:55], a[248:251], v[8:11]\n"
  "buffer_load_dwordx4 v113, s[76:79], s57 offen offset:1280  lds\n"
  "ds_read_b128 v[76:79], v105 offset:3584\n"
  "v_mfma_f32_16x16x32_bf16 v[8:11], v[60:63], v[144:147], v[8:11]\n"
  "v_add_u32 v105, s88, v105\n"
  "v_smfmac_f32_16x16x64_bf16 v[80:83], v[88:91], v[48:55], v124\n"
  "s_waitcnt lgkmcnt(4)\n"
  "s_sub_u32 s57, s57, s85\n"
  "v_smfmac_f32_16x16x64_bf16 v[80:83], v[88:91], v[64:71], v124\n"
  "s_add_u32 s88, s88, s89\n"
  "v_mfma_f32_16x16x32_bf16 v[12:15], v[60:63], v[148:151], v[12:15]\n"
  "s_sub_u32 s88, 0, s88\n"
  "v_mfma_f32_16x16x32_bf16 v[12:15], v[52:55], a[252:255], v[12:15]\n"
  "s_xor_b32 s88, s88, s89\n"
  "v_mfma_f32_16x16x32_bf16 v[12:15], v[56:59], a[228:231], v[12:15]\n"
  "s_xor_b32 s89, s88, s89\n"
  "v_add_f32 v44, v80, v81\n"
  "v_mov_b32 v80, 0\n"
  "v_mfma_f32_16x16x32_bf16 v[12:15], v[48:51], a[204:207], v[12:15]\n"
  "s_xor_b32 s88, s88, s89\n"
  "ds_write_b32 v110, v44\n"
  "v_mfma_f32_16x16x32_bf16 v[16:19], v[48:51], a[208:211], v[16:19]\n"
  "v_mov_b32 v81, 0\n"
  "v_mfma_f32_16x16x32_bf16 v[16:19], v[56:59], a[232:235], v[16:19]\n"
  "v_mfma_f32_16x16x32_bf16 v[16:19], v[52:55], v[128:131], v[16:19]\n"
  "v_mfma_f32_16x16x32_bf16 v[16:19], v[60:63], v[152:155], v[16:19]\n"
  "v_xor_b32 v106, 0x2000, v106\n"
  "v_xor_b32 v107, 0x2000, v107\n"
  "v_mfma_f32_16x16x32_bf16 v[20:23], v[60:63], v[156:159], v[20:23]\n"
  "v_xor_b32 v110, 0x2000, v110\n"
  "v_mfma_f32_16x16x32_bf16 v[20:23], v[52:55], v[132:135], v[20:23]\n"
  "v_mfma_f32_16x16x32_bf16 v[20:23], v[56:59], a[236:239], v[20:23]\n"
  "v_mfma_f32_16x16x32_bf16 v[20:23], v[48:51], a[212:215], v[20:23]\n"
  "s_waitcnt vmcnt(17) lgkmcnt(0)\n"
  "s_barrier\n"
  "v_mfma_f32_16x16x32_bf16 v[20:23], v[64:67], v[180:183], v[20:23]\n"
  "s_add_u32 m0, s66, 0\n"
  "ds_read_b128 v[48:51], v104 offset:0\n"
  "ds_read_b128 v[84:87], v111\n"
  "v_mfma_f32_16x16x32_bf16 v[20:23], v[72:75], v[204:207], v[20:23]\n"
  "ds_read_b128 v[96:99], v111 offset:64\n"
  "v_xor_b32 v111, 0x2000, v111\n"
  "v_mfma_f32_16x16x32_bf16 v[20:23], v[68:71], v[228:231], v[20:23]\n"
  "ds_read_b128 v[52:55], v104 offset:512\n"
  "v_mfma_f32_16x16x32_bf16 v[44:47], v[76:79], v[252:255], v[20:23]\n"
  "buffer_load_dwordx4 v123, s[44:47], s67 offen offset:0 nt lds\n"
  "v_mfma_f32_16x16x32_bf16 v[16:19], v[76:79], v[248:251], v[16:19]\n"
  "s_add_u32 m0, s66, 1920\n"
  "v_mfma_f32_16x16x32_bf16 v[16:19], v[68:71], v[224:227], v[16:19]\n"
  "v_mfma_f32_16x16x32_bf16 v[16:19], v[72:75], v[200:203], v[16:19]\n"
  "ds_read_b128 v[56:59], v105 offset:0\n"
  "v_mfma_f32_16x16x32_bf16 v[40:43], v[64:67], v[176:179], v[16:19]\n"
  "buffer_load_dwordx4 v123, s[44:47], s67 offen offset:128 nt lds\n"
  "v_mfma_f32_16x16x32_bf16 v[12:15], v[64:67], v[172:175], v[12:15]\n"
  "s_add_u32 m0, s66, 3840\n"
  "v_mfma_f32_16x16x32_bf16 v[12:15], v[72:75], v[196:199], v[12:15]\n"
  "v_mfma_f32_16x16x32_bf16 v[12:15], v[68:71], v[220:223], v[12:15]\n"
  "s_waitcnt lgkmcnt(2)\n"
  "ds_read_b128 v[60:63], v105 offset:512\n"
  "v_add_f32 v84, v84, v96\n"
  "v_mfma_f32_16x16x32_bf16 v[36:39], v[76:79], v[244:247], v[12:15]\n"
  "buffer_load_dwordx4 v123, s[44:47], s67 offen offset:256 nt lds\n"
  "v_mfma_f32_16x16x32_bf16 v[8:11], v[76:79], v[240:243], v[8:11]\n"
  "v_add_f32 v85, v85, v97\n"
  "v_add_f32 v86, v86, v98\n"
  "v_mfma_f32_16x16x32_bf16 v[8:11], v[68:71], v[216:219], v[8:11]\n"
  "v_add_f32 v87, v87, v99\n"
  "v_max3_f32 v82, v82, v84, v85\n"
  "v_mfma_f32_16x16x32_bf16 v[8:11], v[72:75], v[192:195], v[8:11]\n"
  "s_add_u32 m0, s59, 21504\n"
  "v_rcp_f32 v84, v84\n"
  "v_mfma_f32_16x16x32_bf16 v[32:35], v[64:67], v[168:171], v[8:11]\n"
  "buffer_load_dwordx4 v114, s[40:43], s57 offen offset:0  lds\n"
  "v_mfma_f32_16x16x32_bf16 v[4:7], v[64:67], v[164:167], v[4:7]\n"
  "v_rcp_f32 v85, v85\n"
  "v_mfma_f32_16x16x32_bf16 v[4:7], v[72:75], v[188:191], v[4:7]\n"
  "v_max3_f32 v82, v82, v86, v87\n"
  "v_max3_f32 v83, v83, v84, v85\n"
  "v_mfma_f32_16x16x32_bf16 v[4:7], v[68:71], v[212:215], v[4:7]\n"
  "v_rcp_f32 v86, v86\n"
  "v_mfma_f32_16x16x32_bf16 v[28:31], v[76:79], v[236:239], v[4:7]\n"
  "buffer_load_dwordx4 v115, s[40:43], s57 offen offset:1024  lds\n"
  "v_mfma_f32_16x16x32_bf16 v[0:3], v[76:79], v[232:235], v[0:3]\n"
  "v_rcp_f32 v87, v87\n"
  "v_mfma_f32_16x16x32_bf16 v[0:3], v[68:71], v[208:211], v[0:3]\n"
  "v_max3_f32 v83, v83, v86, v87\n"
  "v_mul_f32 v96, s99, v84\n"
  "v_mfma_f32_16x16x32_bf16 v[0:3], v[72:75], v[184:187], v[0:3]\n"
  "v_mul_f32 v97, s99, v85\n"
  "v_fma_f32 v40, v40, v84, v96\n"
  "v_mfma_f32_16x16x32_bf16 v[24:27], v[64:67], v[160:163], v[0:3]\n"
  "s_waitcnt lgkmcnt(0)\n"
  "buffer_load_dwordx4 v116, s[40:43], s57 offen offset:2048  lds\n"
  "v_mfma_f32_16x16x32_bf16 v[0:3], v[48:51], a[0:3], v[100:103]\n"
  "ds_read_b128 v[64:67], v104 offset:1024\n"
  "v_fma_f32 v44, v44, v84, v96\n"
  "v_mfma_f32_16x16x32_bf16 v[0:3], v[56:59], a[24:27], v[0:3]\n"
  "s_add_u32 s57, s57, 6144\n"
  "v_cvt_pk_bf16_f32 v40, v40, v44\n"
  "v_fma_f32 v41, v41, v85, v97\n"
  "v_mfma_f32_16x16x32_bf16 v[0:3], v[52:55], a[48:51], v[0:3]\n"
  "s_add_u32 m0, s59, 24576\n"
  "ds_read_b128 v[68:71], v104 offset:1536\n"
  "v_fma_f32 v45, v45, v85, v97\n"
  "v_mfma_f32_16x16x32_bf16 v[0:3], v[60:63], a[72:75], v[0:3]\n"
  "buffer_load_dwordx4 v114, s[40:43], s57 offen offset:0  lds\n"
  "v_mfma_f32_16x16x32_bf16 v[4:7], v[60:63], a[76:79], v[100:103]\n"
  "ds_read_b64_tr_b16 v[92:93], v108 offset:0\n"
  "ds_read_b64_tr_b16 v[94:95], v108 offset:32\n"
  "v_mfma_f32_16x16x32_bf16 v[4:7], v[52:55], a[52:55], v[4:7]\n"
  "v_cvt_pk_bf16_f32 v41, v41, v45\n"
  "v_mul_f32 v98, s99, v86\n"
  "v_mfma_f32_16x16x32_bf16 v[4:7], v[56:59], a[28:31], v[4:7]\n"
  "ds_read_b128 v[72:75], v105 offset:1024\n"
  "v_mul_f32 v99, s99, v87\n"
  "v_mfma_f32_16x16x32_bf16 v[4:7], v[48:51], a[4:7], v[4:7]\n"
  "buffer_load_dwordx4 v115, s[40:43], s57 offen offset:1024  lds\n"
  "v_mfma_f32_16x16x32_bf16 v[8:11], v[48:51], a[8:11], v[100:103]\n"
  "v_fma_f32 v42, v42, v86, v98\n"
  "v_fma_f32 v46, v46, v86, v98\n"
  "v_mfma_f32_16x16x32_bf16 v[8:11], v[56:59], a[32:35], v[8:11]\n"
  "v_cvt_pk_bf16_f32 v42, v42, v46\n"
  "v_fma_f32 v43, v43, v87, v99\n"
  "v_mfma_f32_16x16x32_bf16 v[8:11], v[52:55], a[56:59], v[8:11]\n"
  "ds_read_b128 v[76:79], v105 offset:1536\n"
  "v_fma_f32 v47, v47, v87, v99\n"
  "v_mfma_f32_16x16x32_bf16 v[8:11], v[60:63], a[80:83], v[8:11]\n"
  "buffer_load_dwordx4 v116, s[40:43], s57 offen offset:2048  lds\n"
  "v_smfmac_f32_16x16x64_bf16 v[80:83], v[88:91], v[48:55], v124\n"
  "s_waitcnt lgkmcnt(4)\n"
  "v_cvt_pk_bf16_f32 v43, v43, v47\n"
  "v_smfmac_f32_16x16x64_bf16 v[80:83], v[88:91], v[64:71], v124\n"
  "s_add_u32 s57, s57, s58\n"
  "s_waitcnt lgkmcnt(2)\n"
  "buffer_store_dwordx4 v[92:95], v109, s[48:51], s68 offen offset:0 nt\n"
  "ds_write2_b32 v107, v40, v41 offset0:0 offset1:72\n"
  "v_mfma_f32_16x16x32_bf16 v[12:15], v[60:63], a[84:87], v[100:103]\n"
  "s_add_u32 m0, s59, 27648\n"
  "v_fma_f32 v24, v24, v84, v96\n"
  "v_fma_f32 v25, v25, v85, v97\n"
  "v_mfma_f32_16x16x32_bf16 v[12:15], v[52:55], a[60:63], v[12:15]\n"
  "buffer_load_dwordx4 v114, s[40:43], s57 offen offset:0  lds\n"
  "v_mfma_f32_16x16x32_bf16 v[12:15], v[56:59], a[36:39], v[12:15]\n"
  "v_fma_f32 v28, v28, v84, v96\n"
  "v_fma_f32 v29, v29, v85, v97\n"
  "v_mfma_f32_16x16x32_bf16 v[12:15], v[48:51], a[12:15], v[12:15]\n"
  "ds_write2_b32 v107, v42, v43 offset0:144 offset1:216\n"
  "v_mfma_f32_16x16x32_bf16 v[16:19], v[48:51], a[16:19], v[100:103]\n"
  "v_fma_f32 v32, v32, v84, v96\n"
  "v_fma_f32 v33, v33, v85, v97\n"
  "v_mfma_f32_16x16x32_bf16 v[16:19], v[56:59], a[40:43], v[16:19]\n"
  "buffer_load_dwordx4 v115, s[40:43], s57 offen offset:1024  lds\n"
  "v_mfma_f32_16x16x32_bf16 v[16:19], v[52:55], a[64:67], v[16:19]\n"
  "v_fma_f32 v36, v36, v84, v96\n"
  "v_fma_f32 v37, v37, v85, v97\n"
  "v_mfma_f32_16x16x32_bf16 v[16:19], v[60:63], a[88:91], v[16:19]\n"
  "v_fma_f32 v26, v26, v86, v98\n"
  "v_fma_f32 v27, v27, v87, v99\n"
  "v_fma_f32 v30, v30, v86, v98\n"
  "v_fma_f32 v31, v31, v87, v99\n"
  "v_fma_f32 v34, v34, v86, v98\n"
  "v_fma_f32 v35, v35, v87, v99\n"
  "v_fma_f32 v38, v38, v86, v98\n"
  "v_fma_f32 v39, v39, v87, v99\n"
  "v_mfma_f32_16x16x32_bf16 v[20:23], v[60:63], a[92:95], v[100:103]\n"
  "ds_read_b64_tr_b16 v[96:97], v108 offset:2304\n"
  "ds_read_b64_tr_b16 v[98:99], v108 offset:2336\n"
  "v_mfma_f32_16x16x32_bf16 v[20:23], v[52:55], a[68:71], v[20:23]\n"
  "buffer_load_dwordx4 v116, s[40:43], s57 offen offset:2048  lds\n"
  "v_mfma_f32_16x16x32_bf16 v[20:23], v[56:59], a[44:47], v[20:23]\n"
  "v_cvt_pk_bf16_f32 v24, v24, v28\n"
  "v_cvt_pk_bf16_f32 v28, v25, v29\n"
  "v_mfma_f32_16x16x32_bf16 v[20:23], v[48:51], a[20:23], v[20:23]\n"
  "s_waitcnt lgkmcnt(4)\n"
  "s_add_u32 s57, s57, 6144\n"
  "v_cvt_pk_bf16_f32 v25, v32, v36\n"
  "v_cvt_pk_bf16_f32 v29, v33, v37\n"
  "v_mfma_f32_16x16x32_bf16 v[20:23], v[64:67], a[116:119], v[20:23]\n"
  "s_add_u32 s66, s66, 6144\n"
  "ds_read_b128 v[48:51], v104 offset:2048\n"
  "v_cvt_pk_bf16_f32 v26, v26, v30\n"
  "v_mfma_f32_16x16x32_bf16 v[20:23], v[72:75], a[140:143], v[20:23]\n"
  "s_cmp_ge_u32 s66, 102400\n"
  "s_cselect_b32 s64, 36864, 0\n"
  "v_cvt_pk_bf16_f32 v30, v27, v31\n"
  "v_cvt_pk_bf16_f32 v27, v34, v38\n"
  "v_mfma_f32_16x16x32_bf16 v[20:23], v[68:71], a[164:167], v[20:23]\n"
  "s_sub_u32 s66, s66, s64\n"
  "ds_read_b128 v[52:55], v104 offset:2560\n"
  "v_cvt_pk_bf16_f32 v31, v35, v39\n"
  "v_mfma_f32_16x16x32_bf16 v[20:23], v[76:79], a[188:191], v[20:23]\n"
  "s_sub_u32 s90, s90, 1\n"
  "s_cselect_b32 s63, s91, 6144\n"
  "s_cmov_b64 s[90:91], s[92:93]\n"
  "s_cmov_b64 s[92:93], s[94:95]\n"
  "s_waitcnt lgkmcnt(2)\n"
  "buffer_store_dwordx4 v[96:99], v109, s[48:51], s68 offen offset:128 nt\n"
  "ds_write_b64 v106, v[24:25] offset:0\n"
  "v_mfma_f32_16x16x32_bf16 v[16:19], v[76:79], a[184:187], v[16:19]\n"
  "s_add_u32 s67, s67, s63\n"
  "v_mfma_f32_16x16x32_bf16 v[16:19], v[68:71], a[160:163], v[16:19]\n"
  "ds_read_b64_tr_b16 v[92:93], v108 offset:4608\n"
  "ds_read_b64_tr_b16 v[94:95], v108 offset:4640\n"
  "v_mfma_f32_16x16x32_bf16 v[16:19], v[72:75], a[136:139], v[16:19]\n"
  "ds_read_b128 v[56:59], v105 offset:2048\n"
  "v_xor_b32 v108, 0x2000, v108\n"
  "v_mfma_f32_16x16x32_bf16 v[16:19], v[64:67], a[112:115], v[16:19]\n"
  "v_mfma_f32_16x16x32_bf16 v[12:15], v[64:67], a[108:111], v[12:15]\n"
  "v_mfma_f32_16x16x32_bf16 v[12:15], v[72:75], a[132:135], v[12:15]\n"
  "v_mfma_f32_16x16x32_bf16 v[12:15], v[68:71], a[156:159], v[12:15]\n"
  "ds_read_b128 v[60:63], v105 offset:2560\n"
  "v_mfma_f32_16x16x32_bf16 v[12:15], v[76:79], a[180:183], v[12:15]\n"
  "v_mfma_f32_16x16x32_bf16 v[8:11], v[76:79], a[176:179], v[8:11]\n"
  "v_mfma_f32_16x16x32_bf16 v[8:11], v[68:71], a[152:155], v[8:11]\n"
  "v_mfma_f32_16x16x32_bf16 v[8:11], v[72:75], a[128:131], v[8:11]\n"
  "v_mfma_f32_16x16x32_bf16 v[8:11], v[64:67], a[104:107], v[8:11]\n"
  "s_waitcnt lgkmcnt(2)\n"
  "buffer_store_dwordx4 v[92:95], v109, s[48:51], s68 offen offset:256 nt\n"
  "ds_write_b64 v106, v[28:29] offset:288\n"
  "v_mfma_f32_16x16x32_bf16 v[4:7], v[64:67], a[100:103], v[4:7]\n"
  "s_add_u32 s68, s68, s69\n"
  "v_mfma_f32_16x16x32_bf16 v[4:7], v[72:75], a[124:127], v[4:7]\n"
  "s_mov_b32 s69, s70\n"
  "v_mfma_f32_16x16x32_bf16 v[4:7], v[68:71], a[148:151], v[4:7]\n"
  "s_movk_i32 s70, 6144\n"
  "v_mfma_f32_16x16x32_bf16 v[4:7], v[76:79], a[172:175], v[4:7]\n"
  "v_mfma_f32_16x16x32_bf16 v[0:3], v[76:79], a[168:171], v[0:3]\n"
  "v_mfma_f32_16x16x32_bf16 v[0:3], v[68:71], a[144:147], v[0:3]\n"
  "v_mfma_f32_16x16x32_bf16 v[0:3], v[72:75], a[120:123], v[0:3]\n"
  "v_mfma_f32_16x16x32_bf16 v[0:3], v[64:67], a[96:99], v[0:3]\n"
  "s_waitcnt lgkmcnt(1)\n"
  "v_mfma_f32_16x16x32_bf16 v[0:3], v[48:51], a[192:195], v[0:3]\n"
  "ds_read_b128 v[64:67], v104 offset:3072\n"
  "v_mfma_f32_16x16x32_bf16 v[0:3], v[56:59], a[216:219], v[0:3]\n"
  "v_mfma_f32_16x16x32_bf16 v[0:3], v[52:55], a[240:243], v[0:3]\n"
  "ds_read_b128 v[68:71], v104 offset:3584\n"
  "v_add_u32 v104, s88, v104\n"
  "v_mfma_f32_16x16x32_bf16 v[0:3], v[60:63], v[136:139], v[0:3]\n"
  "ds_write_b64 v106, v[26:27] offset:576\n"
  "v_mfma_f32_16x16x32_bf16 v[4:7], v[60:63], v[140:143], v[4:7]\n"
  "v_mfma_f32_16x16x32_bf16 v[4:7], v[52:55], a[244:247], v[4:7]\n"
  "v_mfma_f32_16x16x32_bf16 v[4:7], v[56:59], a[220:223], v[4:7]\n"
  "ds_read_b128 v[72:75], v105 offset:3072\n"
  "v_mfma_f32_16x16x32_bf16 v[4:7], v[48:51], a[196:199], v[4:7]\n"
  "ds_write_b64 v106, v[30:31] offset:864\n"
  "v_mfma_f32_16x16x32_bf16 v[8:11], v[48:51], a[200:203], v[8:11]\n"
  "v_mfma_f32_16x16x32_bf16 v[8:11], v[56:59], a[224:227], v[8:11]\n"
  "v_mfma_f32_16x16x32_bf16 v[8:11], v[52:55], a[248:251], v[8:11]\n"
  "ds_read_b128 v[76:79], v105 offset:3584\n"
  "v_add_u32 v105, s88, v105\n"
  "v_mfma_f32_16x16x32_bf16 v[8:11], v[60:63], v[144:147], v[8:11]\n"
  "s_add_u32 s88, s88, s89\n"
  "v_smfmac_f32_16x16x64_bf16 v[80:83], v[88:91], v[48:55], v124\n"
  "s_waitcnt lgkmcnt(4)\n"
  "v_smfmac_f32_16x16x64_bf16 v[80:83], v[88:91], v[64:71], v124\n"
  "s_sub_u32 s88, 0, s88\n"
  "v_mfma_f32_16x16x32_bf16 v[12:15], v[60:63], v[148:151], v[12:15]\n"
  "s_xor_b32 s88, s88, s89\n"
  "v_mfma_f32_16x16x32_bf16 v[12:15], v[52:55], a[252:255], v[12:15]\n"
  "s_xor_b32 s89, s88, s89\n"
  "v_mfma_f32_16x16x32_bf16 v[12:15], v[56:59], a[228:231], v[12:15]\n"
  "s_xor_b32 s88, s88, s89\n"
  "v_add_f32 v44, v80, v81\n"
  "v_mov_b32 v80, 0\n"
  "v_mfma_f32_16x16x32_bf16 v[12:15], v[48:51], a[204:207], v[12:15]\n"
  "ds_write_b32 v110, v44\n"
  "v_mfma_f32_16x16x32_bf16 v[16:19], v[48:51], a[208:211], v[16:19]\n"
  "v_mov_b32 v81, 0\n"
  "v_mfma_f32_16x16x32_bf16 v[16:19], v[56:59], a[232:235], v[16:19]\n"
  "v_mfma_f32_16x16x32_bf16 v[16:19], v[52:55], v[128:131], v[16:19]\n"
  "v_mfma_f32_16x16x32_bf16 v[16:19], v[60:63], v[152:155], v[16:19]\n"
  "v_xor_b32 v106, 0x2000, v106\n"
  "v_xor_b32 v107, 0x2000, v107\n"
  "v_mfma_f32_16x16x32_bf16 v[20:23], v[60:63], v[156:159], v[20:23]\n"
  "v_xor_b32 v110, 0x2000, v110\n"
  "v_mfma_f32_16x16x32_bf16 v[20:23], v[52:55], v[132:135], v[20:23]\n"
  "v_mfma_f32_16x16x32_bf16 v[20:23], v[56:59], a[236:239], v[20:23]\n"
  "v_mfma_f32_16x16x32_bf16 v[20:23], v[48:51], a[212:215], v[20:23]\n"
  "s_waitcnt vmcnt(15) lgkmcnt(0)\n"
  "s_barrier\n"
  "v_mfma_f32_16x16x32_bf16 v[20:23], v[64:67], v[180:183], v[20:23]\n"
  "s_add_u32 m0, s66, 0\n"
  "ds_read_b128 v[48:51], v104 offset:0\n"
  "ds_read_b128 v[84:87], v111\n"
  "v_mfma_f32_16x16x32_bf16 v[20:23], v[72:75], v[204:207], v[20:23]\n"
  "ds_read_b128 v[96:99], v111 offset:64\n"
  "v_xor_b32 v111, 0x2000, v111\n"
  "v_mfma_f32_16x16x32_bf16 v[20:23], v[68:71], v[228:231], v[20:23]\n"
  "ds_read_b128 v[52:55], v104 offset:512\n"
  "v_mfma_f32_16x16x32_bf16 v[44:47], v[76:79], v[252:255], v[20:23]\n"
  "buffer_load_dwordx4 v123, s[44:47], s67 offen offset:0 nt lds\n"
  "v_mfma_f32_16x16x32_bf16 v[16:19], v[76:79], v[248:251], v[16:19]\n"
  "s_add_u32 m0, s66, 1920\n"
  "v_mfma_f32_16x16x32_bf16 v[16:19], v[68:71], v[224:227], v[16:19]\n"
  "v_mfma_f32_16x16x32_bf16 v[16:19], v[72:75], v[200:203], v[16:19]\n"
  "ds_read_b128 v[56:59], v105 offset:0\n"
  "v_mfma_f32_16x16x32_bf16 v[40:43], v[64:67], v[176:179], v[16:19]\n"
  "buffer_load_dwordx4 v123, s[44:47], s67 offen offset:128 nt lds\n"
  "v_mfma_f32_16x16x32_bf16 v[12:15], v[64:67], v[172:175], v[12:15]\n"
  "s_add_u32 m0, s66, 3840\n"
  "v_mfma_f32_16x16x32_bf16 v[12:15], v[72:75], v[196:199], v[12:15]\n"
  "v_mfma_f32_16x16x32_bf16 v[12:15], v[68:71], v[220:223], v[12:15]\n"
  "s_waitcnt lgkmcnt(2)\n"
  "ds_read_b128 v[60:63], v105 offset:512\n"
  "v_add_f32 v84, v84, v96\n"
  "v_mfma_f32_16x16x32_bf16 v[36:39], v[76:79], v[244:247], v[12:15]\n"
  "buffer_load_dwordx4 v123, s[44:47], s67 offen offset:256 nt lds\n"
  "v_mfma_f32_16x16x32_bf16 v[8:11], v[76:79], v[240:243], v[8:11]\n"
  "v_add_f32 v85, v85, v97\n"
  "v_add_f32 v86, v86, v98\n"
  "v_mfma_f32_16x16x32_bf16 v[8:11], v[68:71], v[216:219], v[8:11]\n"
  "v_add_f32 v87, v87, v99\n"
  "v_max3_f32 v82, v82, v84, v85\n"
  "v_mfma_f32_16x16x32_bf16 v[8:11], v[72:75], v[192:195], v[8:11]\n"
  "v_rcp_f32 v84, v84\n"
  "v_mfma_f32_16x16x32_bf16 v[32:35], v[64:67], v[168:171], v[8:11]\n"
  "v_rcp_f32 v85, v85\n"
  "v_mfma_f32_16x16x32_bf16 v[4:7], v[64:67], v[164:167], v[4:7]\n"
  "v_max3_f32 v82, v82, v86, v87\n"
  "v_max3_f32 v83, v83, v84, v85\n"
  "v_mfma_f32_16x16x32_bf16 v[4:7], v[72:75], v[188:191], v[4:7]\n"
  "v_rcp_f32 v86, v86\n"
  "v_mfma_f32_16x16x32_bf16 v[4:7], v[68:71], v[212:215], v[4:7]\n"
  "v_rcp_f32 v87, v87\n"
  "v_mfma_f32_16x16x32_bf16 v[28:31], v[76:79], v[236:239], v[4:7]\n"
  "v_max3_f32 v83, v83, v86, v87\n"
  "v_mul_f32 v96, s99, v84\n"
  "v_mfma_f32_16x16x32_bf16 v[0:3], v[76:79], v[232:235], v[0:3]\n"
  "v_mul_f32 v97, s99, v85\n"
  "v_fma_f32 v40, v40, v84, v96\n"
  "v_mfma_f32_16x16x32_bf16 v[0:3], v[68:71], v[208:211], v[0:3]\n"
  "v_fma_f32 v44, v44, v84, v96\n"
  "v_cvt_pk_bf16_f32 v40, v40, v44\n"
  "v_mfma_f32_16x16x32_bf16 v[0:3], v[72:75], v[184:187], v[0:3]\n"
  "v_fma_f32 v41, v41, v85, v97\n"
  "v_fma_f32 v45, v45, v85, v97\n"
  "v_mfma_f32_16x16x32_bf16 v[24:27], v[64:67], v[160:163], v[0:3]\n"
  "s_waitcnt lgkmcnt(0)\n"
  "v_cvt_pk_bf16_f32 v41, v41, v45\n"
  "v_mul_f32 v98, s99, v86\n"
  "v_mfma_f32_16x16x32_bf16 v[0:3], v[48:51], a[0:3], v[100:103]\n"
  "ds_read_b128 v[64:67], v104 offset:1024\n"
  "v_mul_f32 v99, s99, v87\n"
  "v_mfma_f32_16x16x32_bf16 v[0:3], v[56:59], a[24:27], v[0:3]\n"
  "v_fma_f32 v42, v42, v86, v98\n"
  "v_fma_f32 v46, v46, v86, v98\n"
  "v_mfma_f32_16x16x32_bf16 v[0:3], v[52:55], a[48:51], v[0:3]\n"
  "ds_read_b128 v[68:71], v104 offset:1536\n"
  "v_cvt_pk_bf16_f32 v42, v42, v46\n"
  "v_mfma_f32_16x16x32_bf16 v[0:3], v[60:63], a[72:75], v[0:3]\n"
  "ds_read_b64_tr_b16 v[92:93], v108 offset:0\n"
  "ds_read_b64_tr_b16 v[94:95], v108 offset:32\n"
  "v_mfma_f32_16x16x32_bf16 v[4:7], v[60:63], a[76:79], v[100:103]\n"
  "v_fma_f32 v43, v43, v87, v99\n"
  "v_fma_f32 v47, v47, v87, v99\n"
  "v_mfma_f32_16x16x32_bf16 v[4:7], v[52:55], a[52:55], v[4:7]\n"
  "v_cvt_pk_bf16_f32 v43, v43, v47\n"
  "v_fma_f32 v24, v24, v84, v96\n"
  "v_mfma_f32_16x16x32_bf16 v[4:7], v[56:59], a[28:31], v[4:7]\n"
  "ds_read_b128 v[72:75], v105 offset:1024\n"
  "v_fma_f32 v25, v25, v85, v97\n"
  "v_mfma_f32_16x16x32_bf16 v[4:7], v[48:51], a[4:7], v[4:7]\n"
  "v_fma_f32 v28, v28, v84, v96\n"
  "v_fma_f32 v29, v29, v85, v97\n"
  "v_mfma_f32_16x16x32_bf16 v[8:11], v[48:51], a[8:11], v[100:103]\n"
  "v_fma_f32 v32, v32, v84, v96\n"
  "v_fma_f32 v33, v33, v85, v97\n"
  "v_mfma_f32_16x16x32_bf16 v[8:11], v[56:59], a[32:35], v[8:11]\n"
  "v_fma_f32 v36, v36, v84, v96\n"
  "v_fma_f32 v37, v37, v85, v97\n"
  "v_mfma_f32_16x16x32_bf16 v[8:11], v[52:55], a[56:59], v[8:11]\n"
  "ds_read_b128 v[76:79], v105 offset:1536\n"
  "v_fma_f32 v26, v26, v86, v98\n"
  "v_mfma_f32_16x16x32_bf16 v[8:11], v[60:63], a[80:83], v[8:11]\n"
  "v_fma_f32 v27, v27, v87, v99\n"
  "v_fma_f32 v30, v30, v86, v98\n"
  "v_smfmac_f32_16x16x64_bf16 v[80:83], v[88:91], v[48:55], v124\n"
  "s_waitcnt lgkmcnt(4)\n"
  "v_fma_f32 v31, v31, v87, v99\n"
  "v_smfmac_f32_16x16x64_bf16 v[80:83], v[88:91], v[64:71], v124\n"
  "s_waitcnt lgkmcnt(2)\n"
  "buffer_store_dwordx4 v[92:95], v109, s[48:51], s68 offen offset:0 nt\n"
  "ds_write2_b32 v107, v40, v41 offset0:0 offset1:72\n"
  "v_mfma_f32_16x16x32_bf16 v[12:15], v[60:63], a[84:87], v[100:103]\n"
  "v_fma_f32 v34, v34, v86, v98\n"
  "v_fma_f32 v35, v35, v87, v99\n"
  "v_mfma_f32_16x16x32_bf16 v[12:15], v[52:55], a[60:63], v[12:15]\n"
  "v_fma_f32 v38, v38, v86, v98\n"
  "v_fma_f32 v39, v39, v87, v99\n"
  "v_mfma_f32_16x16x32_bf16 v[12:15], v[56:59], a[36:39], v[12:15]\n"
  "v_cvt_pk_bf16_f32 v24, v24, v28\n"
  "v_cvt_pk_bf16_f32 v28, v25, v29\n"
  "v_mfma_f32_16x16x32_bf16 v[12:15], v[48:51], a[12:15], v[12:15]\n"
  "ds_write2_b32 v107, v42, v43 offset0:144 offset1:216\n"
  "v_mfma_f32_16x16x32_bf16 v[16:19], v[48:51], a[16:19], v[100:103]\n"
  "v_cvt_pk_bf16_f32 v25, v32, v36\n"
  "v_cvt_pk_bf16_f32 v29, v33, v37\n"
  "v_mfma_f32_16x16x32_bf16 v[16:19], v[56:59], a[40:43], v[16:19]\n"
  "ds_read_b64_tr_b16 v[96:97], v108 offset:2304\n"
  "ds_read_b64_tr_b16 v[98:99], v108 offset:2336\n"
  "v_mfma_f32_16x16x32_bf16 v[16:19], v[52:55], a[64:67], v[16:19]\n"
  "v_cvt_pk_bf16_f32 v26, v26, v30\n"
  "v_cvt_pk_bf16_f32 v30, v27, v31\n"
  "v_mfma_f32_16x16x32_bf16 v[16:19], v[60:63], a[88:91], v[16:19]\n"
  "v_cvt_pk_bf16_f32 v27, v34, v38\n"
  "v_cvt_pk_bf16_f32 v31, v35, v39\n"
  "v_mfma_f32_16x16x32_bf16 v[20:23], v[60:63], a[92:95], v[100:103]\n"
  "v_mfma_f32_16x16x32_bf16 v[20:23], v[52:55], a[68:71], v[20:23]\n"
  "v_mfma_f32_16x16x32_bf16 v[20:23], v[56:59], a[44:47], v[20:23]\n"
  "v_mfma_f32_16x16x32_bf16 v[20:23], v[48:51], a[20:23], v[20:23]\n"
  "s_waitcnt lgkmcnt(4)\n"
  "v_mfma_f32_16x16x32_bf16 v[20:23], v[64:67], a[116:119], v[20:23]\n"
  "s_add_u32 s66, s66, 6144\n"
  "ds_read_b128 v[48:51], v104 offset:2048\n"
  "v_mfma_f32_16x16x32_bf16 v[20:23], v[72:75], a[140:143], v[20:23]\n"
  "s_cmp_ge_u32 s66, 102400\n"
  "s_cselect_b32 s64, 36864, 0\n"
  "v_mfma_f32_16x16x32_bf16 v[20:23], v[68:71], a[164:167], v[20:23]\n"
  "s_sub_u32 s66, s66, s64\n"
  "ds_read_b128 v[52:55], v104 offset:2560\n"
  "v_mfma_f32_16x16x32_bf16 v[20:23], v[76:79], a[188:191], v[20:23]\n"
  "s_sub_u32 s90, s90, 1\n"
  "s_cselect_b32 s63, s91, 6144\n"
  "s_cmov_b64 s[90:91], s[92:93]\n"
  "s_cmov_b64 s[92:93], s[94:95]\n"
  "s_waitcnt lgkmcnt(2)\n"
  "buffer_store_dwordx4 v[96:99], v109, s[48:51], s68 offen offset:128 nt\n"
  "ds_write_b64 v106, v[24:25] offset:0\n"
  "v_mfma_f32_16x16x32_bf16 v[16:19], v[76:79], a[184:187], v[16:19]\n"
  "s_add_u32 s67, s67, s63\n"
  "v_mfma_f32_16x16x32_bf16 v[16:19], v[68:71], a[160:163], v[16:19]\n"
  "ds_read_b64_tr_b16 v[92:93], v108 offset:4608\n"
  "ds_read_b64_tr_b16 v[94:95], v108 offset:4640\n"
  "v_mfma_f32_16x16x32_bf16 v[16:19], v[72:75], a[136:139], v[16:19]\n"
  "ds_read_b128 v[56:59], v105 offset:2048\n"
  "v_xor_b32 v108, 0x2000, v108\n"
  "v_mfma_f32_16x16x32_bf16 v[16:19], v[64:67], a[112:115], v[16:19]\n"
  "v_mfma_f32_16x16x32_bf16 v[12:15], v[64:67], a[108:111], v[12:15]\n"
  "v_mfma_f32_16x16x32_bf16 v[12:15], v[72:75], a[132:135], v[12:15]\n"
  "v_mfma_f32_16x16x32_bf16 v[12:15], v[68:71], a[156:159], v[12:15]\n"
  "ds_read_b128 v[60:63], v105 offset:2560\n"
  "v_mfma_f32_16x16x32_bf16 v[12:15], v[76:79], a[180:183], v[12:15]\n"
  "v_mfma_f32_16x16x32_bf16 v[8:11], v[76:79], a[176:179], v[8:11]\n"
  "v_mfma_f32_16x16x32_bf16 v[8:11], v[68:71], a[152:155], v[8:11]\n"
  "v_mfma_f32_16x16x32_bf16 v[8:11], v[72:75], a[128:131], v[8:11]\n"
  "v_mfma_f32_16x16x32_bf16 v[8:11], v[64:67], a[104:107], v[8:11]\n"
  "s_waitcnt lgkmcnt(2)\n"
  "buffer_store_dwordx4 v[92:95], v109, s[48:51], s68 offen offset:256 nt\n"
  "ds_write_b64 v106, v[28:29] offset:288\n"
  "v_mfma_f32_16x16x32_bf16 v[4:7], v[64:67], a[100:103], v[4:7]\n"
  "s_add_u32 s68, s68, s69\n"
  "v_mfma_f32_16x16x32_bf16 v[4:7], v[72:75], a[124:127], v[4:7]\n"
  "s_mov_b32 s69, s70\n"
  "v_mfma_f32_16x16x32_bf16 v[4:7], v[68:71], a[148:151], v[4:7]\n"
  "s_movk_i32 s70, 6144\n"
  "v_mfma_f32_16x16x32_bf16 v[4:7], v[76:79], a[172:175], v[4:7]\n"
  "v_mfma_f32_16x16x32_bf16 v[0:3], v[76:79], a[168:171], v[0:3]\n"
  "v_mfma_f32_16x16x32_bf16 v[0:3], v[68:71], a[144:147], v[0:3]\n"
  "v_mfma_f32_16x16x32_bf16 v[0:3], v[72:75], a[120:123], v[0:3]\n"
  "v_mfma_f32_16x16x32_bf16 v[0:3], v[64:67], a[96:99], v[0:3]\n"
  "s_waitcnt lgkmcnt(1)\n"
  "v_mfma_f32_16x16x32_bf16 v[0:3], v[48:51], a[192:195], v[0:3]\n"
  "ds_read_b128 v[64:67], v104 offset:3072\n"
  "v_mfma_f32_16x16x32_bf16 v[0:3], v[56:59], a[216:219], v[0:3]\n"
  "v_mfma_f32_16x16x32_bf16 v[0:3], v[52:55], a[240:243], v[0:3]\n"
  "ds_read_b128 v[68:71], v104 offset:3584\n"
  "v_add_u32 v104, s88, v104\n"
  "v_mfma_f32_16x16x32_bf16 v[0:3], v[60:63], v[136:139], v[0:3]\n"
  "ds_write_b64 v106, v[26:27] offset:576\n"
  "v_mfma_f32_16x16x32_bf16 v[4:7], v[60:63], v[140:143], v[4:7]\n"
  "v_mfma_f32_16x16x32_bf16 v[4:7], v[52:55], a[244:247], v[4:7]\n"
  "v_mfma_f32_16x16x32_bf16 v[4:7], v[56:59], a[220:223], v[4:7]\n"
  "ds_read_b128 v[72:75], v105 offset:3072\n"
  "v_mfma_f32_16x16x32_bf16 v[4:7], v[48:51], a[196:199], v[4:7]\n"
  "ds_write_b64 v106, v[30:31] offset:864\n"
  "v_mfma_f32_16x16x32_bf16 v[8:11], v[48:51], a[200:203], v[8:11]\n"
  "v_mfma_f32_16x16x32_bf16 v[8:11], v[56:59], a[224:227], v[8:11]\n"
  "v_mfma_f32_16x16x32_bf16 v[8:11], v[52:55], a[248:251], v[8:11]\n"
  "ds_read_b128 v[76:79], v105 offset:3584\n"
  "v_add_u32 v105, s88, v105\n"
  "v_mfma_f32_16x16x32_bf16 v[8:11], v[60:63], v[144:147], v[8:11]\n"
  "s_add_u32 s88, s88, s89\n"
  "v_smfmac_f32_16x16x64_bf16 v[80:83], v[88:91], v[48:55], v124\n"
  "s_waitcnt lgkmcnt(4)\n"
  "v_smfmac_f32_16x16x64_bf16 v[80:83], v[88:91], v[64:71], v124\n"
  "s_sub_u32 s88, 0, s88\n"
  "v_mfma_f32_16x16x32_bf16 v[12:15], v[60:63], v[148:151], v[12:15]\n"
  "s_xor_b32 s88, s88, s89\n"
  "v_mfma_f32_16x16x32_bf16 v[12:15], v[52:55], a[252:255], v[12:15]\n"
  "s_xor_b32 s89, s88, s89\n"
  "v_mfma_f32_16x16x32_bf16 v[12:15], v[56:59], a[228:231], v[12:15]\n"
  "s_xor_b32 s88, s88, s89\n"
  "v_add_f32 v44, v80, v81\n"
  "v_mov_b32 v80, 0\n"
  "v_mfma_f32_16x16x32_bf16 v[12:15], v[48:51], a[204:207], v[12:15]\n"
  "ds_write_b32 v110, v44\n"
  "v_mfma_f32_16x16x32_bf16 v[16:19], v[48:51], a[208:211], v[16:19]\n"
  "v_mov_b32 v81, 0\n"
  "v_mfma_f32_16x16x32_bf16 v[16:19], v[56:59], a[232:235], v[16:19]\n"
  "v_mfma_f32_16x16x32_bf16 v[16:19], v[52:55], v[128:131], v[16:19]\n"
  "v_mfma_f32_16x16x32_bf16 v[16:19], v[60:63], v[152:155], v[16:19]\n"
  "v_xor_b32 v106, 0x2000, v106\n"
  "v_xor_b32 v107, 0x2000, v107\n"
  "v_mfma_f32_16x16x32_bf16 v[20:23], v[60:63], v[156:159], v[20:23]\n"
  "v_xor_b32 v110, 0x2000, v110\n"
  "v_mfma_f32_16x16x32_bf16 v[20:23], v[52:55], v[132:135], v[20:23]\n"
  "v_mfma_f32_16x16x32_bf16 v[20:23], v[56:59], a[236:239], v[20:23]\n"
  "v_mfma_f32_16x16x32_bf16 v[20:23], v[48:51], a[212:215], v[20:23]\n"
  "s_waitcnt lgkmcnt(0)\n"
  "s_barrier\n"
  "v_mfma_f32_16x16x32_bf16 v[20:23], v[64:67], v[180:183], v[20:23]\n"
  "s_add_u32 m0, s66, 0\n"
  "ds_read_b128 v[48:51], v104 offset:0\n"
  "ds_read_b128 v[84:87], v111\n"
  "v_mfma_f32_16x16x32_bf16 v[20:23], v[72:75], v[204:207], v[20:23]\n"
  "ds_read_b128 v[96:99], v111 offset:64\n"
  "v_xor_b32 v111, 0x2000, v111\n"
  "v_mfma_f32_16x16x32_bf16 v[20:23], v[68:71], v[228:231], v[20:23]\n"
  "ds_read_b128 v[52:55], v104 offset:512\n"
  "v_mfma_f32_16x16x32_bf16 v[44:47], v[76:79], v[252:255], v[20:23]\n"
  "buffer_load_dwordx4 v123, s[44:47], s67 offen offset:0 nt lds\n"
  "v_mfma_f32_16x16x32_bf16 v[16:19], v[76:79], v[248:251], v[16:19]\n"
  "s_add_u32 m0, s66, 1920\n"
  "v_mfma_f32_16x16x32_bf16 v[16:19], v[68:71], v[224:227], v[16:19]\n"
  "v_mfma_f32_16x16x32_bf16 v[16:19], v[72:75], v[200:203], v[16:19]\n"
  "ds_read_b128 v[56:59], v105 offset:0\n"
  "v_mfma_f32_16x16x32_bf16 v[40:43], v[64:67], v[176:179], v[16:19]\n"
  "buffer_load_dwordx4 v123, s[44:47], s67 offen offset:128 nt lds\n"
  "v_mfma_f32_16x16x32_bf16 v[12:15], v[64:67], v[172:175], v[12:15]\n"
  "s_add_u32 m0, s66, 3840\n"
  "v_mfma_f32_16x16x32_bf16 v[12:15], v[72:75], v[196:199], v[12:15]\n"
  "v_mfma_f32_16x16x32_bf16 v[12:15], v[68:71], v[220:223], v[12:15]\n"
  "s_waitcnt lgkmcnt(2)\n"
  "ds_read_b128 v[60:63], v105 offset:512\n"
  "v_add_f32 v84, v84, v96\n"
  "v_mfma_f32_16x16x32_bf16 v[36:39], v[76:79], v[244:247], v[12:15]\n"
  "buffer_load_dwordx4 v123, s[44:47], s67 offen offset:256 nt lds\n"
  "v_mfma_f32_16x16x32_bf16 v[8:11], v[76:79], v[240:243], v[8:11]\n"
  "v_add_f32 v85, v85, v97\n"
  "v_add_f32 v86, v86, v98\n"
  "v_mfma_f32_16x16x32_bf16 v[8:11], v[68:71], v[216:219], v[8:11]\n"
  "v_add_f32 v87, v87, v99\n"
  "v_max3_f32 v82, v82, v84, v85\n"
  "v_mfma_f32_16x16x32_bf16 v[8:11], v[72:75], v[192:195], v[8:11]\n"
  "v_rcp_f32 v84, v84\n"
  "v_mfma_f32_16x16x32_bf16 v[32:35], v[64:67], v[168:171], v[8:11]\n"
  "v_rcp_f32 v85, v85\n"
  "v_mfma_f32_16x16x32_bf16 v[4:7], v[64:67], v[164:167], v[4:7]\n"
  "v_max3_f32 v82, v82, v86, v87\n"
  "v_max3_f32 v83, v83, v84, v85\n"
  "v_mfma_f32_16x16x32_bf16 v[4:7], v[72:75], v[188:191], v[4:7]\n"
  "v_rcp_f32 v86, v86\n"
  "v_mfma_f32_16x16x32_bf16 v[4:7], v[68:71], v[212:215], v[4:7]\n"
  "v_rcp_f32 v87, v87\n"
  "v_mfma_f32_16x16x32_bf16 v[28:31], v[76:79], v[236:239], v[4:7]\n"
  "v_max3_f32 v83, v83, v86, v87\n"
  "v_mul_f32 v96, s99, v84\n"
  "v_mfma_f32_16x16x32_bf16 v[0:3], v[76:79], v[232:235], v[0:3]\n"
  "v_mul_f32 v97, s99, v85\n"
  "v_fma_f32 v40, v40, v84, v96\n"
  "v_mfma_f32_16x16x32_bf16 v[0:3], v[68:71], v[208:211], v[0:3]\n"
  "v_fma_f32 v44, v44, v84, v96\n"
  "v_cvt_pk_bf16_f32 v40, v40, v44\n"
  "v_mfma_f32_16x16x32_bf16 v[0:3], v[72:75], v[184:187], v[0:3]\n"
  "v_fma_f32 v41, v41, v85, v97\n"
  "v_fma_f32 v45, v45, v85, v97\n"
  "v_mfma_f32_16x16x32_bf16 v[24:27], v[64:67], v[160:163], v[0:3]\n"
  "s_waitcnt lgkmcnt(0)\n"
  "v_cvt_pk_bf16_f32 v41, v41, v45\n"
  "v_mul_f32 v98, s99, v86\n"
  "v_mfma_f32_16x16x32_bf16 v[0:3], v[48:51], a[0:3], v[100:103]\n"
  "ds_read_b128 v[64:67], v104 offset:1024\n"
  "v_mul_f32 v99, s99, v87\n"
  "v_mfma_f32_16x16x32_bf16 v[0:3], v[56:59], a[24:27], v[0:3]\n"
  "v_fma_f32 v42, v42, v86, v98\n"
  "v_fma_f32 v46, v46, v86, v98\n"
  "v_mfma_f32_16x16x32_bf16 v[0:3], v[52:55], a[48:51], v[0:3]\n"
  "ds_read_b128 v[68:71], v104 offset:1536\n"
  "v_cvt_pk_bf16_f32 v42, v42, v46\n"
  "v_mfma_f32_16x16x32_bf16 v[0:3], v[60:63], a[72:75], v[0:3]\n"
  "ds_read_b64_tr_b16 v[92:93], v108 offset:0\n"
  "ds_read_b64_tr_b16 v[94:95], v108 offset:32\n"
  "v_mfma_f32_16x16x32_bf16 v[4:7], v[60:63], a[76:79], v[100:103]\n"
  "v_fma_f32 v43, v43, v87, v99\n"
  "v_fma_f32 v47, v47, v87, v99\n"
  "v_mfma_f32_16x16x32_bf16 v[4:7], v[52:55], a[52:55], v[4:7]\n"
  "v_cvt_pk_bf16_f32 v43, v43, v47\n"
  "v_fma_f32 v24, v24, v84, v96\n"
  "v_mfma_f32_16x16x32_bf16 v[4:7], v[56:59], a[28:31], v[4:7]\n"
  "ds_read_b128 v[72:75], v105 offset:1024\n"
  "v_fma_f32 v25, v25, v85, v97\n"
  "v_mfma_f32_16x16x32_bf16 v[4:7], v[48:51], a[4:7], v[4:7]\n"
  "v_fma_f32 v28, v28, v84, v96\n"
  "v_fma_f32 v29, v29, v85, v97\n"
  "v_mfma_f32_16x16x32_bf16 v[8:11], v[48:51], a[8:11], v[100:103]\n"
  "v_fma_f32 v32, v32, v84, v96\n"
  "v_fma_f32 v33, v33, v85, v97\n"
  "v_mfma_f32_16x16x32_bf16 v[8:11], v[56:59], a[32:35], v[8:11]\n"
  "v_fma_f32 v36, v36, v84, v96\n"
  "v_fma_f32 v37, v37, v85, v97\n"
  "v_mfma_f32_16x16x32_bf16 v[8:11], v[52:55], a[56:59], v[8:11]\n"
  "ds_read_b128 v[76:79], v105 offset:1536\n"
  "v_fma_f32 v26, v26, v86, v98\n"
  "v_mfma_f32_16x16x32_bf16 v[8:11], v[60:63], a[80:83], v[8:11]\n"
  "v_fma_f32 v27, v27, v87, v99\n"
  "v_fma_f32 v30, v30, v86, v98\n"
  "v_smfmac_f32_16x16x64_bf16 v[80:83], v[88:91], v[48:55], v124\n"
  "s_waitcnt lgkmcnt(4)\n"
  "v_fma_f32 v31, v31, v87, v99\n"
  "v_smfmac_f32_16x16x64_bf16 v[80:83], v[88:91], v[64:71], v124\n"
  "s_waitcnt lgkmcnt(2)\n"
  "buffer_store_dwordx4 v[92:95], v109, s[48:51], s68 offen offset:0 nt\n"
  "ds_write2_b32 v107, v40, v41 offset0:0 offset1:72\n"
  "v_mfma_f32_16x16x32_bf16 v[12:15], v[60:63], a[84:87], v[100:103]\n"
  "v_fma_f32 v34, v34, v86, v98\n"
  "v_fma_f32 v35, v35, v87, v99\n"
  "v_mfma_f32_16x16x32_bf16 v[12:15], v[52:55], a[60:63], v[12:15]\n"
  "v_fma_f32 v38, v38, v86, v98\n"
  "v_fma_f32 v39, v39, v87, v99\n"
  "v_mfma_f32_16x16x32_bf16 v[12:15], v[56:59], a[36:39], v[12:15]\n"
  "v_cvt_pk_bf16_f32 v24, v24, v28\n"
  "v_cvt_pk_bf16_f32 v28, v25, v29\n"
  "v_mfma_f32_16x16x32_bf16 v[12:15], v[48:51], a[12:15], v[12:15]\n"
  "ds_write2_b32 v107, v42, v43 offset0:144 offset1:216\n"
  "v_mfma_f32_16x16x32_bf16 v[16:19], v[48:51], a[16:19], v[100:103]\n"
  "v_cvt_pk_bf16_f32 v25, v32, v36\n"
  "v_cvt_pk_bf16_f32 v29, v33, v37\n"
  "v_mfma_f32_16x16x32_bf16 v[16:19], v[56:59], a[40:43], v[16:19]\n"
  "ds_read_b64_tr_b16 v[96:97], v108 offset:2304\n"
  "ds_read_b64_tr_b16 v[98:99], v108 offset:2336\n"
  "v_mfma_f32_16x16x32_bf16 v[16:19], v[52:55], a[64:67], v[16:19]\n"
  "v_cvt_pk_bf16_f32 v26, v26, v30\n"
  "v_cvt_pk_bf16_f32 v30, v27, v31\n"
  "v_mfma_f32_16x16x32_bf16 v[16:19], v[60:63], a[88:91], v[16:19]\n"
  "v_cvt_pk_bf16_f32 v27, v34, v38\n"
  "v_cvt_pk_bf16_f32 v31, v35, v39\n"
  "v_mfma_f32_16x16x32_bf16 v[20:23], v[60:63], a[92:95], v[100:103]\n"
  "v_mfma_f32_16x16x32_bf16 v[20:23], v[52:55], a[68:71], v[20:23]\n"
  "v_mfma_f32_16x16x32_bf16 v[20:23], v[56:59], a[44:47], v[20:23]\n"
  "v_mfma_f32_16x16x32_bf16 v[20:23], v[48:51], a[20:23], v[20:23]\n"
  "s_waitcnt lgkmcnt(4)\n"
  "v_mfma_f32_16x16x32_bf16 v[20:23], v[64:67], a[116:119], v[20:23]\n"
  "s_add_u32 s66, s66, 6144\n"
  "ds_read_b128 v[48:51], v104 offset:2048\n"
  "v_mfma_f32_16x16x32_bf16 v[20:23], v[72:75], a[140:143], v[20:23]\n"
  "s_cmp_ge_u32 s66, 102400\n"
  "s_cselect_b32 s64, 36864, 0\n"
  "v_mfma_f32_16x16x32_bf16 v[20:23], v[68:71], a[164:167], v[20:23]\n"
  "s_sub_u32 s66, s66, s64\n"
  "ds_read_b128 v[52:55], v104 offset:2560\n"
  "v_mfma_f32_16x16x32_bf16 v[20:23], v[76:79], a[188:191], v[20:23]\n"
  "s_sub_u32 s90, s90, 1\n"
  "s_cselect_b32 s63, s91, 6144\n"
  "s_cmov_b64 s[90:91], s[92:93]\n"
  "s_cmov_b64 s[92:93], s[94:95]\n"
  "s_waitcnt lgkmcnt(2)\n"
  "buffer_store_dwordx4 v[96:99], v109, s[48:51], s68 offen offset:128 nt\n"
  "ds_write_b64 v106, v[24:25] offset:0\n"
  "v_mfma_f32_16x16x32_bf16 v[16:19], v[76:79], a[184:187], v[16:19]\n"
  "s_add_u32 s67, s67, s63\n"
  "v_mfma_f32_16x16x32_bf16 v[16:19], v[68:71], a[160:163], v[16:19]\n"
  "ds_read_b64_tr_b16 v[92:93], v108 offset:4608\n"
  "ds_read_b64_tr_b16 v[94:95], v108 offset:4640\n"
  "v_mfma_f32_16x16x32_bf16 v[16:19], v[72:75], a[136:139], v[16:19]\n"
  "ds_read_b128 v[56:59], v105 offset:2048\n"
  "v_xor_b32 v108, 0x2000, v108\n"
  "v_mfma_f32_16x16x32_bf16 v[16:19], v[64:67], a[112:115], v[16:19]\n"
  "v_mfma_f32_16x16x32_bf16 v[12:15], v[64:67], a[108:111], v[12:15]\n"
  "v_mfma_f32_16x16x32_bf16 v[12:15], v[72:75], a[132:135], v[12:15]\n"
  "v_mfma_f32_16x16x32_bf16 v[12:15], v[68:71], a[156:159], v[12:15]\n"
  "ds_read_b128 v[60:63], v105 offset:2560\n"
  "v_mfma_f32_16x16x32_bf16 v[12:15], v[76:79], a[180:183], v[12:15]\n"
  "v_mfma_f32_16x16x32_bf16 v[8:11], v[76:79], a[176:179], v[8:11]\n"
  "v_mfma_f32_16x16x32_bf16 v[8:11], v[68:71], a[152:155], v[8:11]\n"
  "v_mfma_f32_16x16x32_bf16 v[8:11], v[72:75], a[128:131], v[8:11]\n"
  "v_mfma_f32_16x16x32_bf16 v[8:11], v[64:67], a[104:107], v[8:11]\n"
  "s_waitcnt lgkmcnt(2)\n"
  "buffer_store_dwordx4 v[92:95], v109, s[48:51], s68 offen offset:256 nt\n"
  "ds_write_b64 v106, v[28:29] offset:288\n"
  "v_mfma_f32_16x16x32_bf16 v[4:7], v[64:67], a[100:103], v[4:7]\n"
  "s_add_u32 s68, s68, s69\n"
  "v_mfma_f32_16x16x32_bf16 v[4:7], v[72:75], a[124:127], v[4:7]\n"
  "s_mov_b32 s69, s70\n"
  "v_mfma_f32_16x16x32_bf16 v[4:7], v[68:71], a[148:151], v[4:7]\n"
  "s_movk_i32 s70, 6144\n"
  "v_mfma_f32_16x16x32_bf16 v[4:7], v[76:79], a[172:175], v[4:7]\n"
  "v_mfma_f32_16x16x32_bf16 v[0:3], v[76:79], a[168:171], v[0:3]\n"
  "v_mfma_f32_16x16x32_bf16 v[0:3], v[68:71], a[144:147], v[0:3]\n"
  "v_mfma_f32_16x16x32_bf16 v[0:3], v[72:75], a[120:123], v[0:3]\n"
  "v_mfma_f32_16x16x32_bf16 v[0:3], v[64:67], a[96:99], v[0:3]\n"
  "s_waitcnt lgkmcnt(1)\n"
  "v_mfma_f32_16x16x32_bf16 v[0:3], v[48:51], a[192:195], v[0:3]\n"
  "ds_read_b128 v[64:67], v104 offset:3072\n"
  "v_mfma_f32_16x16x32_bf16 v[0:3], v[56:59], a[216:219], v[0:3]\n"
  "v_mfma_f32_16x16x32_bf16 v[0:3], v[52:55], a[240:243], v[0:3]\n"
  "ds_read_b128 v[68:71], v104 offset:3584\n"
  "v_add_u32 v104, s88, v104\n"
  "v_mfma_f32_16x16x32_bf16 v[0:3], v[60:63], v[136:139], v[0:3]\n"
  "ds_write_b64 v106, v[26:27] offset:576\n"
  "v_mfma_f32_16x16x32_bf16 v[4:7], v[60:63], v[140:143], v[4:7]\n"
  "v_mfma_f32_16x16x32_bf16 v[4:7], v[52:55], a[244:247], v[4:7]\n"
  "v_mfma_f32_16x16x32_bf16 v[4:7], v[56:59], a[220:223], v[4:7]\n"
  "ds_read_b128 v[72:75], v105 offset:3072\n"
  "v_mfma_f32_16x16x32_bf16 v[4:7], v[48:51], a[196:199], v[4:7]\n"
  "ds_write_b64 v106, v[30:31] offset:864\n"
  "v_mfma_f32_16x16x32_bf16 v[8:11], v[48:51], a[200:203], v[8:11]\n"
  "v_mfma_f32_16x16x32_bf16 v[8:11], v[56:59], a[224:227], v[8:11]\n"
  "v_mfma_f32_16x16x32_bf16 v[8:11], v[52:55], a[248:251], v[8:11]\n"
  "ds_read_b128 v[76:79], v105 offset:3584\n"
  "v_add_u32 v105, s88, v105\n"
  "v_mfma_f32_16x16x32_bf16 v[8:11], v[60:63], v[144:147], v[8:11]\n"
  "s_add_u32 s88, s88, s89\n"
  "v_smfmac_f32_16x16x64_bf16 v[80:83], v[88:91], v[48:55], v124\n"
  "s_waitcnt lgkmcnt(4)\n"
  "v_smfmac_f32_16x16x64_bf16 v[80:83], v[88:91], v[64:71], v124\n"
  "s_sub_u32 s88, 0, s88\n"
  "v_mfma_f32_16x16x32_bf16 v[12:15], v[60:63], v[148:151], v[12:15]\n"
  "s_xor_b32 s88, s88, s89\n"
  "v_mfma_f32_16x16x32_bf16 v[12:15], v[52:55], a[252:255], v[12:15]\n"
  "s_xor_b32 s89, s88, s89\n"
  "v_mfma_f32_16x16x32_bf16 v[12:15], v[56:59], a[228:231], v[12:15]\n"
  "s_xor_b32 s88, s88, s89\n"
  "v_add_f32 v44, v80, v81\n"
  "v_mov_b32 v80, 0\n"
  "v_mfma_f32_16x16x32_bf16 v[12:15], v[48:51], a[204:207], v[12:15]\n"
  "ds_write_b32 v110, v44\n"
  "v_mfma_f32_16x16x32_bf16 v[16:19], v[48:51], a[208:211], v[16:19]\n"
  "v_mov_b32 v81, 0\n"
  "v_mfma_f32_16x16x32_bf16 v[16:19], v[56:59], a[232:235], v[16:19]\n"
  "v_mfma_f32_16x16x32_bf16 v[16:19], v[52:55], v[128:131], v[16:19]\n"
  "v_mfma_f32_16x16x32_bf16 v[16:19], v[60:63], v[152:155], v[16:19]\n"
  "v_xor_b32 v106, 0x2000, v106\n"
  "v_xor_b32 v107, 0x2000, v107\n"
  "v_mfma_f32_16x16x32_bf16 v[20:23], v[60:63], v[156:159], v[20:23]\n"
  "v_xor_b32 v110, 0x2000, v110\n"
  "v_mfma_f32_16x16x32_bf16 v[20:23], v[52:55], v[132:135], v[20:23]\n"
  "v_mfma_f32_16x16x32_bf16 v[20:23], v[56:59], a[236:239], v[20:23]\n"
  "v_mfma_f32_16x16x32_bf16 v[20:23], v[48:51], a[212:215], v[20:23]\n"
  "s_waitcnt lgkmcnt(0)\n"
  "s_barrier\n"
  "v_mfma_f32_16x16x32_bf16 v[20:23], v[64:67], v[180:183], v[20:23]\n"
  "ds_read_b128 v[48:51], v104 offset:0\n"
  "ds_read_b128 v[84:87], v111\n"
  "ds_read_b128 v[96:99], v111 offset:64\n"
  "v_xor_b32 v111, 0x2000, v111\n"
  "v_mfma_f32_16x16x32_bf16 v[20:23], v[72:75], v[204:207], v[20:23]\n"
  "s_add_u32 m0, s59, 0\n"
  "v_mfma_f32_16x16x32_bf16 v[20:23], v[68:71], v[228:231], v[20:23]\n"
  "buffer_load_dwordx4 v114, s[40:43], s57 offen offset:0 nt lds\n"
  "buffer_load_dwordx4 v115, s[40:43], s57 offen offset:1024 nt lds\n"
  "ds_read_b128 v[52:55], v104 offset:512\n"
  "v_mfma_f32_16x16x32_bf16 v[44:47], v[76:79], v[252:255], v[20:23]\n"
  "v_mfma_f32_16x16x32_bf16 v[16:19], v[76:79], v[248:251], v[16:19]\n"
  "buffer_load_dwordx4 v116, s[40:43], s57 offen offset:2048  lds\n"
  "v_mfma_f32_16x16x32_bf16 v[16:19], v[68:71], v[224:227], v[16:19]\n"
  "s_add_u32 s57, s57, s84\n"
  "v_mfma_f32_16x16x32_bf16 v[16:19], v[72:75], v[200:203], v[16:19]\n"
  "ds_read_b128 v[56:59], v105 offset:0\n"
  "v_mfma_f32_16x16x32_bf16 v[40:43], v[64:67], v[176:179], v[16:19]\n"
  "s_add_u32 m0, s66, 0\n"
  "s_nop 0\n"
  "buffer_load_dwordx4 v123, s[44:47], s67 offen offset:0 nt lds\n"
  "s_add_u32 m0, s59, 3072\n"
  "v_mfma_f32_16x16x32_bf16 v[12:15], v[64:67], v[172:175], v[12:15]\n"
  "buffer_load_dwordx4 v114, s[40:43], s57 offen offset:0 nt lds\n"
  "buffer_load_dwordx4 v115, s[40:43], s57 offen offset:1024 nt lds\n"
  "v_mfma_f32_16x16x32_bf16 v[12:15], v[72:75], v[196:199], v[12:15]\n"
  "v_mfma_f32_16x16x32_bf16 v[12:15], v[68:71], v[220:223], v[12:15]\n"
  "s_waitcnt lgkmcnt(2)\n"
  "buffer_load_dwordx4 v116, s[40:43], s57 offen offset:2048  lds\n"
  "ds_read_b128 v[60:63], v105 offset:512\n"
  "v_add_f32 v84, v84, v96\n"
  "v_mfma_f32_16x16x32_bf16 v[36:39], v[76:79], v[244:247], v[12:15]\n"
  "s_add_u32 s57, s57, 6144\n"
  "v_add_f32 v85, v85, v97\n"
  "v_add_f32 v86, v86, v98\n"
  "v_add_f32 v87, v87, v99\n"
  "v_max3_f32 v82, v82, v84, v85\n"
  "v_mfma_f32_16x16x32_bf16 v[8:11], v[76:79], v[240:243], v[8:11]\n"
  "v_rcp_f32 v84, v84\n"
  "v_rcp_f32 v85, v85\n"
  "v_mfma_f32_16x16x32_bf16 v[8:11], v[68:71], v[216:219], v[8:11]\n"
  "s_add_u32 m0, s66, 1920\n"
  "s_nop 0\n"
  "buffer_load_dwordx4 v123, s[44:47], s67 offen offset:128 nt lds\n"
  "s_add_u32 m0, s59, 6144\n"
  "v_max3_f32 v82, v82, v86, v87\n"
  "v_max3_f32 v83, v83, v84, v85\n"
  "v_mfma_f32_16x16x32_bf16 v[8:11], v[72:75], v[192:195], v[8:11]\n"
  "buffer_load_dwordx4 v114, s[40:43], s57 offen offset:0 nt lds\n"
  "buffer_load_dwordx4 v115, s[40:43], s57 offen offset:1024 nt lds\n"
  "v_mfma_f32_16x16x32_bf16 v[32:35], v[64:67], v[168:171], v[8:11]\n"
  "v_rcp_f32 v86, v86\n"
  "v_rcp_f32 v87, v87\n"
  "v_mfma_f32_16x16x32_bf16 v[4:7], v[64:67], v[164:167], v[4:7]\n"
  "buffer_load_dwordx4 v116, s[40:43], s57 offen offset:2048  lds\n"
  "v_max3_f32 v83, v83, v86, v87\n"
  "v_mul_f32 v96, s99, v84\n"
  "v_mfma_f32_16x16x32_bf16 v[4:7], v[72:75], v[188:191], v[4:7]\n"
  "s_add_u32 s57, s57, s58\n"
  "v_mul_f32 v97, s99, v85\n"
  "v_mul_f32 v98, s99, v86\n"
  "v_mul_f32 v99, s99, v87\n"
  "v_fma_f32 v36, v36, v84, v96\n"
  "v_mfma_f32_16x16x32_bf16 v[4:7], v[68:71], v[212:215], v[4:7]\n"
  "v_fma_f32 v32, v32, v84, v96\n"
  "v_fma_f32 v33, v33, v85, v97\n"
  "v_fma_f32 v37, v37, v85, v97\n"
  "v_fma_f32 v34, v34, v86, v98\n"
  "v_mfma_f32_16x16x32_bf16 v[28:31], v[76:79], v[236:239], v[4:7]\n"
  "s_add_u32 m0, s66, 3840\n"
  "s_nop 0\n"
  "buffer_load_dwordx4 v123, s[44:47], s67 offen offset:256 nt lds\n"
  "s_add_u32 m0, s59, 9216\n"
  "v_fma_f32 v35, v35, v87, v99\n"
  "v_fma_f32 v38, v38, v86, v98\n"
  "v_mfma_f32_16x16x32_bf16 v[0:3], v[76:79], v[232:235], v[0:3]\n"
  "s_add_u32 s66, s66, 6144\n"
  "s_cmp_ge_u32 s66, 102400\n"
  "s_cselect_b32 s64, 36864, 0\n"
  "s_sub_u32 s66, s66, s64\n"
  "s_sub_u32 s90, s90, 1\n"
  "s_cselect_b32 s63, s91, 6144\n"
  "s_cmov_b64 s[90:91], s[92:93]\n"
  "s_cmov_b64 s[92:93], s[94:95]\n"
  "s_add_u32 s67, s67, s63\n"
  "buffer_load_dwordx4 v114, s[40:43], s57 offen offset:0 nt lds\n"
  "buffer_load_dwordx4 v115, s[40:43], s57 offen offset:1024 nt lds\n"
  "v_mfma_f32_16x16x32_bf16 v[0:3], v[68:71], v[208:211], v[0:3]\n"
  "v_fma_f32 v39, v39, v87, v99\n"
  "v_fma_f32 v40, v40, v84, v96\n"
  "v_fma_f32 v44, v44, v84, v96\n"
  "v_fma_f32 v41, v41, v85, v97\n"
  "v_mfma_f32_16x16x32_bf16 v[0:3], v[72:75], v[184:187], v[0:3]\n"
  "buffer_load_dwordx4 v116, s[40:43], s57 offen offset:2048  lds\n"
  "v_fma_f32 v28, v28, v84, v96\n"
  "v_fma_f32 v29, v29, v85, v97\n"
  "v_mfma_f32_16x16x32_bf16 v[24:27], v[64:67], v[160:163], v[0:3]\n"
  "s_waitcnt lgkmcnt(0)\n"
  "s_add_u32 s57, s57, 6144\n"
  "v_fma_f32 v30, v30, v86, v98\n"
  "v_fma_f32 v31, v31, v87, v99\n"
  "v_fma_f32 v45, v45, v85, v97\n"
  "v_fma_f32 v42, v42, v86, v98\n"
  "v_mfma_f32_16x16x32_bf16 v[0:3], v[48:51], a[0:3], v[100:103]\n"
  "ds_read_b128 v[64:67], v104 offset:1024\n"
  "v_fma_f32 v46, v46, v86, v98\n"
  "v_fma_f32 v43, v43, v87, v99\n"
  "v_fma_f32 v47, v47, v87, v99\n"
  "v_mfma_f32_16x16x32_bf16 v[0:3], v[56:59], a[24:27], v[0:3]\n"
  "s_add_u32 m0, s59, 12288\n"
  "v_cvt_pk_bf16_f32 v40, v40, v44\n"
  "v_cvt_pk_bf16_f32 v41, v41, v45\n"
  "v_cvt_pk_bf16_f32 v42, v42, v46\n"
  "v_cvt_pk_bf16_f32 v43, v43, v47\n"
  "v_mfma_f32_16x16x32_bf16 v[4:7], v[56:59], a[28:31], v[100:103]\n"
  "buffer_load_dwordx4 v114, s[40:43], s57 offen offset:0 nt lds\n"
  "buffer_load_dwordx4 v115, s[40:43], s57 offen offset:1024 nt lds\n"
  "ds_read_b128 v[68:71], v104 offset:1536\n"
  "v_mfma_f32_16x16x32_bf16 v[4:7], v[48:51], a[4:7], v[4:7]\n"
  "ds_write2_b32 v107, v40, v41 offset0:0 offset1:72\n"
  "v_fma_f32 v24, v24, v84, v96\n"
  "v_mfma_f32_16x16x32_bf16 v[8:11], v[48:51], a[8:11], v[100:103]\n"
  "buffer_load_dwordx4 v116, s[40:43], s57 offen offset:2048  lds\n"
  "v_fma_f32 v25, v25, v85, v97\n"
  "v_cvt_pk_bf16_f32 v24, v24, v28\n"
  "v_mfma_f32_16x16x32_bf16 v[8:11], v[56:59], a[32:35], v[8:11]\n"
  "s_add_u32 s57, s57, s84\n"
  "ds_write2_b32 v107, v42, v43 offset0:144 offset1:216\n"
  "v_cvt_pk_bf16_f32 v28, v25, v29\n"
  "v_mfma_f32_16x16x32_bf16 v[12:15], v[56:59], a[36:39], v[100:103]\n"
  "ds_read_b128 v[72:75], v105 offset:1024\n"
  "v_cvt_pk_bf16_f32 v25, v32, v36\n"
  "v_cvt_pk_bf16_f32 v29, v33, v37\n"
  "v_fma_f32 v26, v26, v86, v98\n"
  "v_mfma_f32_16x16x32_bf16 v[12:15], v[48:51], a[12:15], v[12:15]\n"
  "s_add_u32 m0, s59, 15360\n"
  "ds_write_b64 v106, v[24:25] offset:0\n"
  "v_fma_f32 v27, v27, v87, v99\n"
  "v_mfma_f32_16x16x32_bf16 v[16:19], v[48:51], a[16:19], v[100:103]\n"
  "buffer_load_dwordx4 v114, s[40:43], s57 offen offset:0 nt lds\n"
  "buffer_load_dwordx4 v115, s[40:43], s57 offen offset:1024 nt lds\n"
  "v_mfma_f32_16x16x32_bf16 v[16:19], v[56:59], a[40:43], v[16:19]\n"
  "ds_write_b64 v106, v[28:29] offset:288\n"
  "v_cvt_pk_bf16_f32 v26, v26, v30\n"
  "v_mfma_f32_16x16x32_bf16 v[20:23], v[56:59], a[44:47], v[100:103]\n"
  "buffer_load_dwordx4 v116, s[40:43], s57 offen offset:2048  lds\n"
  "ds_read_b128 v[76:79], v105 offset:1536\n"
  "v_cvt_pk_bf16_f32 v30, v27, v31\n"
  "v_mfma_f32_16x16x32_bf16 v[20:23], v[48:51], a[20:23], v[20:23]\n"
  "s_add_u32 s57, s57, 6144\n"
  "ds_read_b64_tr_b16 v[92:93], v108 offset:0\n"
  "ds_read_b64_tr_b16 v[94:95], v108 offset:32\n"
  "v_cvt_pk_bf16_f32 v27, v34, v38\n"
  "v_cvt_pk_bf16_f32 v31, v35, v39\n"
  "v_smfmac_f32_16x16x64_bf16 v[80:83], v[88:91], v[48:55], v124\n"
  "s_waitcnt lgkmcnt(8)\n"
  "s_waitcnt vmcnt(42)\n"
  "ds_read_b64_tr_b16 a[0:1], v117 offset:21504\n"
  "ds_read_b64_tr_b16 a[4:5], v118 offset:21504\n"
  "v_smfmac_f32_16x16x64_bf16 v[80:83], v[88:91], v[64:71], v124\n"
  "s_add_u32 m0, s59, 18432\n"
  "ds_write_b64 v106, v[26:27] offset:576\n"
  "v_mfma_f32_16x16x32_bf16 v[20:23], v[52:55], a[68:71], v[20:23]\n"
  "buffer_load_dwordx4 v114, s[40:43], s57 offen offset:0 nt lds\n"
  "buffer_load_dwordx4 v115, s[40:43], s57 offen offset:1024 nt lds\n"
  "ds_read_b64_tr_b16 a[8:9], v119 offset:21504\n"
  "ds_read_b64_tr_b16 a[12:13], v120 offset:21504\n"
  "v_mfma_f32_16x16x32_bf16 v[20:23], v[60:63], a[92:95], v[20:23]\n"
  "ds_read_b64_tr_b16 a[16:17], v121 offset:23552\n"
  "ds_read_b64_tr_b16 a[20:21], v122 offset:23552\n"
  "ds_write_b64 v106, v[30:31] offset:864\n"
  "v_mfma_f32_16x16x32_bf16 v[16:19], v[60:63], a[88:91], v[16:19]\n"
  "buffer_load_dwordx4 v116, s[40:43], s57 offen offset:2048  lds\n"
  "s_waitcnt vmcnt(38)\n"
  "ds_read_b64 v[32:33], v117 offset:27648\n"
  "ds_read_b64 v[34:35], v118 offset:27648\n"
  "v_mfma_f32_16x16x32_bf16 v[16:19], v[52:55], a[64:67], v[16:19]\n"
  "s_add_u32 s57, s57, s58\n"
  "ds_read_b64_tr_b16 a[2:3], v117 offset:24576\n"
  "ds_read_b64_tr_b16 a[6:7], v118 offset:24576\n"
  "v_mfma_f32_16x16x32_bf16 v[12:15], v[52:55], a[60:63], v[12:15]\n"
  "s_waitcnt lgkmcnt(12)\n"
  "buffer_store_dwordx4 v[92:95], v109, s[48:51], s68 offen offset:0 nt\n"
  "v_mfma_f32_16x16x32_bf16 v[12:15], v[60:63], a[84:87], v[12:15]\n"
  "ds_read_b64_tr_b16 a[10:11], v119 offset:24576\n"
  "ds_read_b64_tr_b16 a[14:15], v120 offset:24576\n"
  "ds_read_b64_tr_b16 v[96:97], v108 offset:2304\n"
  "ds_read_b64_tr_b16 v[98:99], v108 offset:2336\n"
  "v_mfma_f32_16x16x32_bf16 v[8:11], v[60:63], a[80:83], v[8:11]\n"
  "s_add_u32 m0, s59, 21504\n"
  "ds_read_b64_tr_b16 a[18:19], v121 offset:26624\n"
  "ds_read_b64_tr_b16 a[22:23], v122 offset:26624\n"
  "v_mfma_f32_16x16x32_bf16 v[8:11], v[52:55], a[56:59], v[8:11]\n"
  "s_waitcnt lgkmcnt(11)\n"
  "buffer_load_dwordx4 v114, s[40:43], s57 offen offset:0 nt lds\n"
  "buffer_load_dwordx4 v115, s[40:43], s57 offen offset:1024 nt lds\n"
  "v_mfma_f32_16x16x32_bf16 v[4:7], v[52:55], a[52:55], v[4:7]\n"
  "ds_read_b64_tr_b16 a[24:25], v117 offset:27648\n"
  "ds_read_b64_tr_b16 a[28:29], v118 offset:27648\n"
  "s_waitcnt lgkmcnt(10)\n"
  "v_max3_f32 v32, |v32|, |v33|, |v34|\n"
  "v_max3_f32 v32, v32, |v35|, 0\n"
  "v_mfma_f32_16x16x32_bf16 v[4:7], v[60:63], a[76:79], v[4:7]\n"
  "buffer_load_dwordx4 v116, s[40:43], s57 offen offset:2048  lds\n"
  "v_max_f32 v32, 0, v32\n"
  "v_mul_f32 v33, 0x40000000, v32\n"
  "v_mfma_f32_16x16x32_bf16 v[0:3], v[60:63], a[72:75], v[0:3]\n"
  "s_add_u32 s57, s57, 6144\n"
  "ds_read_b64_tr_b16 a[32:33], v119 offset:27648\n"
  "ds_read_b64_tr_b16 a[36:37], v120 offset:27648\n"
  "v_mul_f32 v32, 0x2b800000, v32\n"
  "v_max_f32 v82, v82, v32\n"
  "v_mfma_f32_16x16x32_bf16 v[0:3], v[52:55], a[48:51], v[0:3]\n"
  "ds_read_b64_tr_b16 a[40:41], v121 offset:29696\n"
  "ds_read_b64_tr_b16 a[44:45], v122 offset:29696\n"
  "v_readlane_b32 s98, v33, 0\n"
  "v_mfma_f32_16x16x32_bf16 v[0:3], v[64:67], a[96:99], v[0:3]\n"
  "s_add_u32 m0, s59, 24576\n"
  "ds_read_b128 v[48:51], v104 offset:2048\n"
  "v_readlane_b32 s62, v33, 21\n"
  "v_mfma_f32_16x16x32_bf16 v[0:3], v[72:75], a[120:123], v[0:3]\n"
  "s_waitcnt lgkmcnt(7)\n"
  "buffer_load_dwordx4 v114, s[40:43], s57 offen offset:0 nt lds\n"
  "buffer_load_dwordx4 v115, s[40:43], s57 offen offset:1024 nt lds\n"
  "s_waitcnt vmcnt(27)\n"
  "ds_read_b64_tr_b16 a[26:27], v117 offset:0\n"
  "ds_read_b64_tr_b16 a[30:31], v118 offset:0\n"
  "v_mfma_f32_16x16x32_bf16 v[4:7], v[72:75], a[124:127], v[4:7]\n"
  "ds_read_b128 v[52:55], v104 offset:2560\n"
  "v_readlane_b32 s63, v33, 42\n"
  "v_mfma_f32_16x16x32_bf16 v[4:7], v[64:67], a[100:103], v[4:7]\n"
  "buffer_load_dwordx4 v116, s[40:43], s57 offen offset:2048  lds\n"
  "ds_read_b64_tr_b16 a[34:35], v119 offset:0\n"
  "ds_read_b64_tr_b16 a[38:39], v120 offset:0\n"
  "v_mfma_f32_16x16x32_bf16 v[8:11], v[64:67], a[104:107], v[8:11]\n"
  "s_add_u32 s57, s57, s84\n"
  "ds_read_b64_tr_b16 a[42:43], v121 offset:2048\n"
  "ds_read_b64_tr_b16 a[46:47], v122 offset:2048\n"
  "v_readlane_b32 s64, v33, 63\n"
  "v_mfma_f32_16x16x32_bf16 v[8:11], v[72:75], a[128:131], v[8:11]\n"
  "buffer_store_dwordx4 v[96:99], v109, s[48:51], s68 offen offset:128 nt\n"
  "ds_read_b64_tr_b16 v[92:93], v108 offset:4608\n"
  "ds_read_b64_tr_b16 v[94:95], v108 offset:4640\n"
  "v_xor_b32 v108, 0x2000, v108\n"
  "v_mfma_f32_16x16x32_bf16 v[12:15], v[72:75], a[132:135], v[12:15]\n"
  "s_add_u32 m0, s59, 27648\n"
  "ds_read_b128 v[56:59], v105 offset:2048\n"
  "s_waitcnt vmcnt(25)\n"
  "ds_read_b64_tr_b16 a[48:49], v117 offset:3072\n"
  "ds_read_b64_tr_b16 a[52:53], v118 offset:3072\n"
  "v_mfma_f32_16x16x32_bf16 v[12:15], v[64:67], a[108:111], v[12:15]\n"
  "s_waitcnt lgkmcnt(13)\n"
  "buffer_load_dwordx4 v114, s[40:43], s57 offen offset:0 nt lds\n"
  "buffer_load_dwordx4 v115, s[40:43], s57 offen offset:1024 nt lds\n"
  "v_mfma_f32_16x16x32_bf16 v[16:19], v[64:67], a[112:115], v[16:19]\n"
  "s_nop 1\n"
  "s_max_u32 s98, s98, s62\n"
  "s_max_u32 s63, s63, s64\n"
  "s_max_u32 s98, s98, s63\n"
  "s_and_b32 s98, s98, 0x7f800000\n"
  "s_cselect_b32 s62, 0x400000, 0\n"
  "s_or_b32 s98, s98, s62\n"
  "s_nop 0\n"
  "ds_read_b64_tr_b16 a[56:57], v119 offset:3072\n"
  "ds_read_b64_tr_b16 a[60:61], v120 offset:3072\n"
  "v_mov_b32 v100, s98\n"
  "v_mov_b32 v101, s98\n"
  "v_mfma_f32_16x16x32_bf16 v[16:19], v[72:75], a[136:139], v[16:19]\n"
  "buffer_load_dwordx4 v116, s[40:43], s57 offen offset:2048  lds\n"
  "ds_read_b64_tr_b16 a[64:65], v121 offset:5120\n"
  "ds_read_b64_tr_b16 a[68:69], v122 offset:5120\n"
  "v_mfma_f32_16x16x32_bf16 v[20:23], v[72:75], a[140:143], v[20:23]\n"
  "s_add_u32 s57, s57, 6144\n"
  "ds_read_b128 v[60:63], v105 offset:2560\n"
  "v_mov_b32 v102, s98\n"
  "v_mov_b32 v103, s98\n"
  "v_mfma_f32_16x16x32_bf16 v[20:23], v[64:67], a[116:119], v[20:23]\n"
  "s_waitcnt vmcnt(24)\n"
  "ds_read_b64_tr_b16 a[50:51], v117 offset:6144\n"
  "ds_read_b64_tr_b16 a[54:55], v118 offset:6144\n"
  "v_mfma_f32_16x16x32_bf16 v[20:23], v[68:71], a[164:167], v[20:23]\n"
  "s_add_u32 m0, s59, 0\n"
  "v_mfma_f32_16x16x32_bf16 v[20:23], v[76:79], a[188:191], v[20:23]\n"
  "s_waitcnt lgkmcnt(12)\n"
  "buffer_load_dwordx4 v114, s[40:43], s57 offen offset:0 nt lds\n"
  "buffer_load_dwordx4 v115, s[40:43], s57 offen offset:1024 nt lds\n"
  "ds_read_b64_tr_b16 a[58:59], v119 offset:6144\n"
  "ds_read_b64_tr_b16 a[62:63], v120 offset:6144\n"
  "v_mfma_f32_16x16x32_bf16 v[16:19], v[76:79], a[184:187], v[16:19]\n"
  "ds_read_b64_tr_b16 a[66:67], v121 offset:8192\n"
  "ds_read_b64_tr_b16 a[70:71], v122 offset:8192\n"
  "v_mfma_f32_16x16x32_bf16 v[16:19], v[68:71], a[160:163], v[16:19]\n"
  "buffer_load_dwordx4 v116, s[40:43], s57 offen offset:2048  lds\n"
  "v_mfma_f32_16x16x32_bf16 v[12:15], v[68:71], a[156:159], v[12:15]\n"
  "s_add_u32 s57, s57, s58\n"
  "s_waitcnt vmcnt(23)\n"
  "ds_read_b64_tr_b16 a[72:73], v117 offset:9216\n"
  "ds_read_b64_tr_b16 a[76:77], v118 offset:9216\n"
  "v_mfma_f32_16x16x32_bf16 v[12:15], v[76:79], a[180:183], v[12:15]\n"
  "s_waitcnt lgkmcnt(15)\n"
  "buffer_store_dwordx4 v[92:95], v109, s[48:51], s68 offen offset:256 nt\n"
  "v_mfma_f32_16x16x32_bf16 v[8:11], v[76:79], a[176:179], v[8:11]\n"
  "s_add_u32 m0, s59, 3072\n"
  "ds_read_b64_tr_b16 a[80:81], v119 offset:9216\n"
  "ds_read_b64_tr_b16 a[84:85], v120 offset:9216\n"
  "v_mfma_f32_16x16x32_bf16 v[8:11], v[68:71], a[152:155], v[8:11]\n"
  "s_waitcnt lgkmcnt(11)\n"
  "buffer_load_dwordx4 v114, s[40:43], s57 offen offset:0 nt lds\n"
  "buffer_load_dwordx4 v115, s[40:43], s57 offen offset:1024 nt lds\n"
  "ds_read_b64_tr_b16 a[88:89], v121 offset:11264\n"
  "ds_read_b64_tr_b16 a[92:93], v122 offset:11264\n"
  "v_mfma_f32_16x16x32_bf16 v[4:7], v[68:71], a[148:151], v[4:7]\n"
  "s_add_u32 s68, s68, s69\n"
  "v_mfma_f32_16x16x32_bf16 v[4:7], v[76:79], a[172:175], v[4:7]\n"
  "buffer_load_dwordx4 v116, s[40:43], s57 offen offset:2048  lds\n"
  "s_waitcnt vmcnt(24)\n"
  "ds_read_b64_tr_b16 a[74:75], v117 offset:12288\n"
  "ds_read_b64_tr_b16 a[78:79], v118 offset:12288\n"
  "v_mfma_f32_16x16x32_bf16 v[0:3], v[76:79], a[168:171], v[0:3]\n"
  "s_add_u32 s57, s57, 6144\n"
  "v_mfma_f32_16x16x32_bf16 v[0:3], v[68:71], a[144:147], v[0:3]\n"
  "s_waitcnt lgkmcnt(14)\n"
  "ds_read_b64_tr_b16 a[82:83], v119 offset:12288\n"
  "ds_read_b64_tr_b16 a[86:87], v120 offset:12288\n"
  "v_mfma_f32_16x16x32_bf16 v[0:3], v[48:51], a[192:195], v[0:3]\n"
  "s_add_u32 m0, s59, 6144\n"
  "ds_read_b128 v[64:67], v104 offset:3072\n"
  "ds_read_b64_tr_b16 a[90:91], v121 offset:14336\n"
  "ds_read_b64_tr_b16 a[94:95], v122 offset:14336\n"
  "v_mfma_f32_16x16x32_bf16 v[0:3], v[56:59], a[216:219], v[0:3]\n"
  "s_waitcnt lgkmcnt(13)\n"
  "buffer_load_dwordx4 v114, s[40:43], s57 offen offset:0 nt lds\n"
  "buffer_load_dwordx4 v115, s[40:43], s57 offen offset:1024 nt lds\n"
  "v_mfma_f32_16x16x32_bf16 v[4:7], v[56:59], a[220:223], v[4:7]\n"
  "ds_read_b128 v[68:71], v104 offset:3584\n"
  "s_waitcnt vmcnt(23)\n"
  "ds_read_b64_tr_b16 a[96:97], v117 offset:15360\n"
  "ds_read_b64_tr_b16 a[100:101], v118 offset:15360\n"
  "v_add_u32 v104, s88, v104\n"
  "v_mfma_f32_16x16x32_bf16 v[4:7], v[48:51], a[196:199], v[4:7]\n"
  "s_mov_b32 s69, s70\n"
  "buffer_load_dwordx4 v116, s[40:43], s57 offen offset:2048  lds\n"
  "v_mfma_f32_16x16x32_bf16 v[8:11], v[48:51], a[200:203], v[8:11]\n"
  "s_add_u32 s57, s57, s84\n"
  "ds_read_b64_tr_b16 a[104:105], v119 offset:15360\n"
  "ds_read_b64_tr_b16 a[108:109], v120 offset:15360\n"
  "v_mfma_f32_16x16x32_bf16 v[8:11], v[56:59], a[224:227], v[8:11]\n"
  "s_mov_b32 s70, s96\n"
  "ds_read_b64_tr_b16 a[112:113], v121 offset:17408\n"
  "ds_read_b64_tr_b16 a[116:117], v122 offset:17408\n"
  "v_mfma_f32_16x16x32_bf16 v[12:15], v[56:59], a[228:231], v[12:15]\n"
  "s_add_u32 m0, s59, 9216\n"
  "ds_read_b128 v[72:75], v105 offset:3072\n"
  "v_mfma_f32_16x16x32_bf16 v[12:15], v[48:51], a[204:207], v[12:15]\n"
  "s_waitcnt lgkmcnt(15)\n"
  "buffer_load_dwordx4 v114, s[40:43], s57 offen offset:0 nt lds\n"
  "buffer_load_dwordx4 v115, s[40:43], s57 offen offset:1024 nt lds\n"
  "s_waitcnt vmcnt(23)\n"
  "ds_read_b64_tr_b16 a[98:99], v117 offset:18432\n"
  "ds_read_b64_tr_b16 a[102:103], v118 offset:18432\n"
  "v_mfma_f32_16x16x32_bf16 v[16:19], v[48:51], a[208:211], v[16:19]\n"
  "s_mov_b32 s96, s97\n"
  "v_mfma_f32_16x16x32_bf16 v[16:19], v[56:59], a[232:235], v[16:19]\n"
  "buffer_load_dwordx4 v116, s[40:43], s57 offen offset:2048  lds\n"
  "ds_read_b64_tr_b16 a[106:107], v119 offset:18432\n"
  "ds_read_b64_tr_b16 a[110:111], v120 offset:18432\n"
  "v_mfma_f32_16x16x32_bf16 v[20:23], v[56:59], a[236:239], v[20:23]\n"
  "s_add_u32 s57, s57, 6144\n"
  "ds_read_b128 v[76:79], v105 offset:3584\n"
  "ds_read_b64_tr_b16 a[114:115], v121 offset:20480\n"
  "ds_read_b64_tr_b16 a[118:119], v122 offset:20480\n"
  "v_add_u32 v105, s88, v105\n"
  "v_mfma_f32_16x16x32_bf16 v[20:23], v[48:51], a[212:215], v[20:23]\n"
  "s_add_u32 s88, s88, s89\n"
  "v_smfmac_f32_16x16x64_bf16 v[80:83], v[88:91], v[48:55], v124\n"
  "s_waitcnt lgkmcnt(14)\n"
  "s_add_u32 m0, s59, 12288\n"
  "s_waitcnt vmcnt(20)\n"
  "ds_read_b64_tr_b16 a[120:121], v117 offset:21504\n"
  "ds_read_b64_tr_b16 a[124:125], v118 offset:21504\n"
  "v_smfmac_f32_16x16x64_bf16 v[80:83], v[88:91], v[64:71], v124\n"
  "buffer_load_dwordx4 v114, s[40:43], s57 offen offset:0 nt lds\n"
  "buffer_load_dwordx4 v115, s[40:43], s57 offen offset:1024 nt lds\n"
  "v_mfma_f32_16x16x32_bf16 v[20:23], v[52:55], v[132:135], v[20:23]\n"
  "s_sub_u32 s88, 0, s88\n"
  "ds_read_b64_tr_b16 a[128:129], v119 offset:21504\n"
  "ds_read_b64_tr_b16 a[132:133], v120 offset:21504\n"
  "v_mfma_f32_16x16x32_bf16 v[20:23], v[60:63], v[156:159], v[20:23]\n"
  "s_xor_b32 s88, s88, s89\n"
  "buffer_load_dwordx4 v116, s[40:43], s57 offen offset:2048  lds\n"
  "ds_read_b64_tr_b16 a[136:137], v121 offset:23552\n"
  "ds_read_b64_tr_b16 a[140:141], v122 offset:23552\n"
  "v_mfma_f32_16x16x32_bf16 v[16:19], v[60:63], v[152:155], v[16:19]\n"
  "s_add_u32 s57, s57, s58\n"
  "v_add_f32 v44, v80, v81\n"
  "v_mov_b32 v80, 0\n"
  "v_mov_b32 v81, 0\n"
  "v_mfma_f32_16x16x32_bf16 v[16:19], v[52:55], v[128:131], v[16:19]\n"
  "s_waitcnt vmcnt(20)\n"
  "ds_read_b64_tr_b16 a[122:123], v117 offset:24576\n"
  "ds_read_b64_tr_b16 a[126:127], v118 offset:24576\n"
  "ds_write_b32 v110, v44\n"
  "v_mfma_f32_16x16x32_bf16 v[12:15], v[52:55], a[252:255], v[12:15]\n"
  "s_add_u32 m0, s59, 15360\n"
  "v_mfma_f32_16x16x32_bf16 v[12:15], v[60:63], v[148:151], v[12:15]\n"
  "s_xor_b32 s89, s88, s89\n"
  "s_xor_b32 s88, s88, s89\n"
  "s_waitcnt lgkmcnt(15)\n"
  "buffer_load_dwordx4 v114, s[40:43], s57 offen offset:0 nt lds\n"
  "buffer_load_dwordx4 v115, s[40:43], s57 offen offset:1024 nt lds\n"
  "ds_read_b64_tr_b16 a[130:131], v119 offset:24576\n"
  "ds_read_b64_tr_b16 a[134:135], v120 offset:24576\n"
  "v_mfma_f32_16x16x32_bf16 v[8:11], v[60:63], v[144:147], v[8:11]\n"
  "ds_read_b64_tr_b16 a[138:139], v121 offset:26624\n"
  "ds_read_b64_tr_b16 a[142:143], v122 offset:26624\n"
  "v_mfma_f32_16x16x32_bf16 v[8:11], v[52:55], a[248:251], v[8:11]\n"
  "buffer_load_dwordx4 v116, s[40:43], s57 offen offset:2048  lds\n"
  "v_xor_b32 v106, 0x2000, v106\n"
  "v_xor_b32 v107, 0x2000, v107\n"
  "v_mfma_f32_16x16x32_bf16 v[4:7], v[52:55], a[244:247], v[4:7]\n"
  "s_add_u32 s57, s57, 6144\n"
  "s_waitcnt vmcnt(19)\n"
  "ds_read_b64_tr_b16 a[144:145], v117 offset:27648\n"
  "ds_read_b64_tr_b16 a[148:149], v118 offset:27648\n"
  "v_xor_b32 v110, 0x2000, v110\n"
  "v_mfma_f32_16x16x32_bf16 v[4:7], v[60:63], v[140:143], v[4:7]\n"
  "v_mfma_f32_16x16x32_bf16 v[0:3], v[60:63], v[136:139], v[0:3]\n"
  "s_add_u32 m0, s59, 18432\n"
  "ds_read_b64_tr_b16 a[152:153], v119 offset:27648\n"
  "ds_read_b64_tr_b16 a[156:157], v120 offset:27648\n"
  "v_mfma_f32_16x16x32_bf16 v[0:3], v[52:55], a[240:243], v[0:3]\n"
  "s_waitcnt lgkmcnt(15)\n"
  "buffer_load_dwordx4 v114, s[40:43], s57 offen offset:0 nt lds\n"
  "buffer_load_dwordx4 v115, s[40:43], s57 offen offset:1024 nt lds\n"
  "ds_read_b64_tr_b16 a[160:161], v121 offset:29696\n"
  "ds_read_b64_tr_b16 a[164:165], v122 offset:29696\n"
  "s_waitcnt lgkmcnt(0)\n"
  "s_barrier\n"
  "v_mfma_f32_16x16x32_bf16 v[0:3], v[64:67], v[160:163], v[0:3]\n"
  "s_add_u32 s72, s72, 7\n"
  "ds_read_b128 v[48:51], v104 offset:0\n"
  "ds_read_b128 v[84:87], v111\n"
  "ds_read_b128 v[96:99], v111 offset:64\n"
  "v_xor_b32 v111, 0x2000, v111\n"
  "v_mfma_f32_16x16x32_bf16 v[0:3], v[72:75], v[184:187], v[0:3]\n"
  "buffer_load_dwordx4 v116, s[40:43], s57 offen offset:2048  lds\n"
  "s_waitcnt vmcnt(19)\n"
  "ds_read_b64_tr_b16 a[146:147], v117 offset:0\n"
  "ds_read_b64_tr_b16 a[150:151], v118 offset:0\n"
  "v_mfma_f32_16x16x32_bf16 v[4:7], v[72:75], v[188:191], v[4:7]\n"
  "s_add_u32 s57, s57, s84\n"
  "ds_read_b128 v[52:55], v104 offset:512\n"
  "v_mfma_f32_16x16x32_bf16 v[4:7], v[64:67], v[164:167], v[4:7]\n"
  "s_sub_u32 s56, s56, 1\n"
  "ds_read_b64_tr_b16 a[154:155], v119 offset:0\n"
  "ds_read_b64_tr_b16 a[158:159], v120 offset:0\n"
  "v_mfma_f32_16x16x32_bf16 v[8:11], v[64:67], v[168:171], v[8:11]\n"
  "s_add_u32 m0, s59, 21504\n"
  "ds_read_b64_tr_b16 a[162:163], v121 offset:2048\n"
  "ds_read_b64_tr_b16 a[166:167], v122 offset:2048\n"
  "v_mfma_f32_16x16x32_bf16 v[8:11], v[72:75], v[192:195], v[8:11]\n"
  "buffer_load_dwordx4 v114, s[40:43], s57 offen offset:0 nt lds\n"
  "buffer_load_dwordx4 v115, s[40:43], s57 offen offset:1024 nt lds\n"
  "v_mfma_f32_16x16x32_bf16 v[12:15], v[72:75], v[196:199], v[12:15]\n"
  "ds_read_b128 v[56:59], v105 offset:0\n"
  "s_waitcnt vmcnt(17)\n"
  "ds_read_b64_tr_b16 a[168:169], v117 offset:3072\n"
  "ds_read_b64_tr_b16 a[172:173], v118 offset:3072\n"
  "v_mfma_f32_16x16x32_bf16 v[12:15], v[64:67], v[172:175], v[12:15]\n"
  "s_mov_b32 s61, 99\n"
  "buffer_load_dwordx4 v116, s[40:43], s57 offen offset:2048  lds\n"
  "v_mfma_f32_16x16x32_bf16 v[16:19], v[64:67], v[176:179], v[16:19]\n"
  "s_add_u32 s57, s57, 6144\n"
  "ds_read_b64_tr_b16 a[176:177], v119 offset:3072\n"
  "ds_read_b64_tr_b16 a[180:181], v120 offset:3072\n"
  "v_mfma_f32_16x16x32_bf16 v[16:19], v[72:75], v[200:203], v[16:19]\n"
  "ds_read_b64_tr_b16 a[184:185], v121 offset:5120\n"
  "ds_read_b64_tr_b16 a[188:189], v122 offset:5120\n"
  "v_mfma_f32_16x16x32_bf16 v[20:23], v[72:75], v[204:207], v[20:23]\n"
  "s_add_u32 m0, s66, 0\n"
  "s_nop 0\n"
  "buffer_load_dwordx4 v123, s[44:47], s67 offen offset:0 nt lds\n"
  "s_add_u32 m0, s59, 24576\n"
  "ds_read_b128 v[60:63], v105 offset:512\n"
  "v_mfma_f32_16x16x32_bf16 v[20:23], v[64:67], v[180:183], v[20:23]\n"
  "buffer_load_dwordx4 v114, s[40:43], s57 offen offset:0 nt lds\n"
  "buffer_load_dwordx4 v115, s[40:43], s57 offen offset:1024 nt lds\n"
  "s_waitcnt vmcnt(18)\n"
  "ds_read_b64_tr_b16 a[170:171], v117 offset:6144\n"
  "ds_read_b64_tr_b16 a[174:175], v118 offset:6144\n"
  "v_mfma_f32_16x16x32_bf16 v[20:23], v[68:71], v[228:231], v[20:23]\n"
  "s_waitcnt lgkmcnt(15)\n"
  "v_add_f32 v84, v84, v96\n"
  "v_add_f32 v85, v85, v97\n"
  "v_add_f32 v86, v86, v98\n"
  "v_add_f32 v87, v87, v99\n"
  "v_mfma_f32_16x16x32_bf16 v[44:47], v[76:79], v[252:255], v[20:23]\n"
  "buffer_load_dwordx4 v116, s[40:43], s57 offen offset:2048  lds\n"
  "ds_read_b64_tr_b16 a[178:179], v119 offset:6144\n"
  "ds_read_b64_tr_b16 a[182:183], v120 offset:6144\n"
  "v_mfma_f32_16x16x32_bf16 v[16:19], v[76:79], v[248:251], v[16:19]\n"
  "s_add_u32 s57, s57, s58\n"
  "ds_read_b64_tr_b16 a[186:187], v121 offset:8192\n"
  "ds_read_b64_tr_b16 a[190:191], v122 offset:8192\n"
  "v_max3_f32 v82, v82, v84, v85\n"
  "v_max3_f32 v82, v82, v86, v87\n"
  "v_mfma_f32_16x16x32_bf16 v[40:43], v[68:71], v[224:227], v[16:19]\n"
  "v_rcp_f32 v84, v84\n"
  "v_rcp_f32 v85, v85\n"
  "v_mfma_f32_16x16x32_bf16 v[12:15], v[68:71], v[220:223], v[12:15]\n"
  "s_add_u32 m0, s66, 1920\n"
  "s_nop 0\n"
  "buffer_load_dwordx4 v123, s[44:47], s67 offen offset:128 nt lds\n"
  "s_add_u32 m0, s59, 27648\n"
  "s_waitcnt vmcnt(17)\n"
  "ds_read_b64_tr_b16 a[192:193], v117 offset:9216\n"
  "ds_read_b64_tr_b16 a[196:197], v118 offset:9216\n"
  "v_mfma_f32_16x16x32_bf16 v[36:39], v[76:79], v[244:247], v[12:15]\n"
  "buffer_load_dwordx4 v114, s[40:43], s57 offen offset:0 nt lds\n"
  "buffer_load_dwordx4 v115, s[40:43], s57 offen offset:1024 nt lds\n"
  "v_mfma_f32_16x16x32_bf16 v[8:11], v[76:79], v[240:243], v[8:11]\n"
  "ds_read_b64_tr_b16 a[200:201], v119 offset:9216\n"
  "ds_read_b64_tr_b16 a[204:205], v120 offset:9216\n"
  "v_rcp_f32 v86, v86\n"
  "v_mfma_f32_16x16x32_bf16 v[32:35], v[68:71], v[216:219], v[8:11]\n"
  "buffer_load_dwordx4 v116, s[40:43], s57 offen offset:2048  lds\n"
  "ds_read_b64_tr_b16 a[208:209], v121 offset:11264\n"
  "ds_read_b64_tr_b16 a[212:213], v122 offset:11264\n"
  "v_mfma_f32_16x16x32_bf16 v[4:7], v[68:71], v[212:215], v[4:7]\n"
  "s_add_u32 s57, s57, 6144\n"
  "v_rcp_f32 v87, v87\n"
  "v_max3_f32 v83, v83, v84, v85\n"
  "v_mul_f32 v96, s99, v84\n"
  "v_mfma_f32_16x16x32_bf16 v[28:31], v[76:79], v[236:239], v[4:7]\n"
  "s_waitcnt vmcnt(17)\n"
  "ds_read_b64_tr_b16 a[194:195], v117 offset:12288\n"
  "ds_read_b64_tr_b16 a[198:199], v118 offset:12288\n"
  "v_max3_f32 v83, v83, v86, v87\n"
  "v_mul_f32 v97, s99, v85\n"
  "v_mfma_f32_16x16x32_bf16 v[0:3], v[76:79], v[232:235], v[0:3]\n"
  "s_add_u32 m0, s66, 3840\n"
  "s_nop 0\n"
  "buffer_load_dwordx4 v123, s[44:47], s67 offen offset:256 nt lds\n"
  "s_add_u32 m0, s59, 0\n"
  "v_mul_f32 v98, s99, v86\n"
  "v_mul_f32 v99, s99, v87\n"
  "v_mfma_f32_16x16x32_bf16 v[24:27], v[68:71], v[208:211], v[0:3]\n"
  "s_waitcnt lgkmcnt(14)\n"
  "s_add_u32 s66, s66, 6144\n"
  "s_cmp_ge_u32 s66, 102400\n"
  "s_cselect_b32 s64, 36864, 0\n"
  "s_sub_u32 s66, s66, s64\n"
  "s_sub_u32 s90, s90, 1\n"
  "s_cselect_b32 s63, s91, 6144\n"
  "s_cmov_b64 s[90:91], s[92:93]\n"
  "s_cmov_b64 s[92:93], s[94:95]\n"
  "s_add_u32 s67, s67, s63\n"
  "buffer_load_dwordx4 v114, s[40:43], s57 offen offset:0 nt lds\n"
  "buffer_load_dwordx4 v115, s[40:43], s57 offen offset:1024 nt lds\n"
  "ds_read_b64_tr_b16 a[202:203], v119 offset:12288\n"
  "ds_read_b64_tr_b16 a[206:207], v120 offset:12288\n"
  "v_mfma_f32_16x16x32_bf16 v[0:3], v[48:51], a[0:3], v[100:103]\n"
  "s_xor_b32 s99, 0x80000000, s98\n"
  "ds_read_b128 v[64:67], v104 offset:1024\n"
  "ds_read_b64_tr_b16 a[210:211], v121 offset:14336\n"
  "ds_read_b64_tr_b16 a[214:215], v122 offset:14336\n"
  "v_fma_f32 v40, v40, v84, v96\n"
  "v_mfma_f32_16x16x32_bf16 v[0:3], v[56:59], a[24:27], v[0:3]\n"
  "buffer_load_dwordx4 v116, s[40:43], s57 offen offset:2048  lds\n"
  "v_fma_f32 v44, v44, v84, v96\n"
  "v_fma_f32 v41, v41, v85, v97\n"
  "v_mfma_f32_16x16x32_bf16 v[4:7], v[56:59], a[28:31], v[100:103]\n"
  "s_add_u32 s57, s57, s84\n"
  "ds_read_b128 v[68:71], v104 offset:1536\n"
  "s_waitcnt vmcnt(18)\n"
  "ds_read_b64_tr_b16 a[216:217], v117 offset:15360\n"
  "ds_read_b64_tr_b16 a[220:221], v118 offset:15360\n"
  "v_fma_f32 v45, v45, v85, v97\n"
  "v_mfma_f32_16x16x32_bf16 v[4:7], v[48:51], a[4:7], v[4:7]\n"
  "ds_read_b64_tr_b16 v[92:93], v108 offset:0\n"
  "ds_read_b64_tr_b16 v[94:95], v108 offset:32\n"
  "v_fma_f32 v42, v42, v86, v98\n"
  "v_fma_f32 v46, v46, v86, v98\n"
  "v_mfma_f32_16x16x32_bf16 v[8:11], v[48:51], a[8:11], v[100:103]\n"
  "s_add_u32 m0, s59, 3072\n"
  "ds_read_b64_tr_b16 a[224:225], v119 offset:15360\n"
  "ds_read_b64_tr_b16 a[228:229], v120 offset:15360\n"
  "v_fma_f32 v43, v43, v87, v99\n"
  "v_fma_f32 v47, v47, v87, v99\n"
  "v_mfma_f32_16x16x32_bf16 v[8:11], v[56:59], a[32:35], v[8:11]\n"
  "buffer_load_dwordx4 v114, s[40:43], s57 offen offset:0 nt lds\n"
  "buffer_load_dwordx4 v115, s[40:43], s57 offen offset:1024 nt lds\n"
  "ds_read_b64_tr_b16 a[232:233], v121 offset:17408\n"
  "ds_read_b64_tr_b16 a[236:237], v122 offset:17408\n"
  "v_mfma_f32_16x16x32_bf16 v[12:15], v[56:59], a[36:39], v[100:103]\n"
  "ds_read_b128 v[72:75], v105 offset:1024\n"
  "v_fma_f32 v24, v24, v84, v96\n"
  "v_fma_f32 v25, v25, v85, v97\n"
  "v_fma_f32 v28, v28, v84, v96\n"
  "v_mfma_f32_16x16x32_bf16 v[12:15], v[48:51], a[12:15], v[12:15]\n"
  "buffer_load_dwordx4 v116, s[40:43], s57 offen offset:2048  lds\n"
  "s_waitcnt vmcnt(18)\n"
  "ds_read_b64_tr_b16 a[218:219], v117 offset:18432\n"
  "ds_read_b64_tr_b16 a[222:223], v118 offset:18432\n"
  "v_mfma_f32_16x16x32_bf16 v[16:19], v[48:51], a[16:19], v[100:103]\n"
  "s_add_u32 s57, s57, 6144\n"
  "v_fma_f32 v29, v29, v85, v97\n"
  "v_fma_f32 v32, v32, v84, v96\n"
  "v_fma_f32 v33, v33, v85, v97\n"
  "v_fma_f32 v36, v36, v84, v96\n"
  "v_mfma_f32_16x16x32_bf16 v[16:19], v[56:59], a[40:43], v[16:19]\n"
  "ds_read_b64_tr_b16 a[226:227], v119 offset:18432\n"
  "ds_read_b64_tr_b16 a[230:231], v120 offset:18432\n"
  "v_fma_f32 v37, v37, v85, v97\n"
  "v_fma_f32 v26, v26, v86, v98\n"
  "v_mfma_f32_16x16x32_bf16 v[20:23], v[56:59], a[44:47], v[100:103]\n"
  "s_add_u32 m0, s59, 6144\n"
  "ds_read_b128 v[76:79], v105 offset:1536\n"
  "ds_read_b64_tr_b16 a[234:235], v121 offset:20480\n"
  "ds_read_b64_tr_b16 a[238:239], v122 offset:20480\n"
  "v_fma_f32 v27, v27, v87, v99\n"
  "v_mfma_f32_16x16x32_bf16 v[20:23], v[48:51], a[20:23], v[20:23]\n"
  "s_waitcnt lgkmcnt(15)\n"
  "buffer_load_dwordx4 v114, s[40:43], s57 offen offset:0 nt lds\n"
  "buffer_load_dwordx4 v115, s[40:43], s57 offen offset:1024 nt lds\n"
  "v_smfmac_f32_16x16x64_bf16 v[80:83], v[88:91], v[48:55], v124\n"
  "s_waitcnt vmcnt(17)\n"
  "ds_read_b64_tr_b16 a[240:241], v117 offset:21504\n"
  "ds_read_b64_tr_b16 a[244:245], v118 offset:21504\n"
  "v_fma_f32 v30, v30, v86, v98\n"
  "v_smfmac_f32_16x16x64_bf16 v[80:83], v[88:91], v[64:71], v124\n"
  "buffer_load_dwordx4 v116, s[40:43], s57 offen offset:2048  lds\n"
  "v_fma_f32 v31, v31, v87, v99\n"
  "v_mfma_f32_16x16x32_bf16 v[20:23], v[52:55], a[68:71], v[20:23]\n"
  "s_add_u32 s57, s57, s58\n"
  "ds_read_b64_tr_b16 a[248:249], v119 offset:21504\n"
  "ds_read_b64_tr_b16 a[252:253], v120 offset:21504\n"
  "v_fma_f32 v34, v34, v86, v98\n"
  "v_fma_f32 v35, v35, v87, v99\n"
  "v_mfma_f32_16x16x32_bf16 v[20:23], v[60:63], a[92:95], v[20:23]\n"
  "s_waitcnt lgkmcnt(15)\n"
  "buffer_store_dwordx4 v[92:95], v109, s[48:51], s68 offen offset:0 nt\n"
  "ds_read_b64_tr_b16 v[128:129], v121 offset:23552\n"
  "ds_read_b64_tr_b16 v[132:133], v122 offset:23552\n"
  "v_fma_f32 v38, v38, v86, v98\n"
  "v_mfma_f32_16x16x32_bf16 v[16:19], v[60:63], a[88:91], v[16:19]\n"
  "s_add_u32 m0, s59, 9216\n"
  "v_fma_f32 v39, v39, v87, v99\n"
  "v_cvt_pk_bf16_f32 v40, v40, v44\n"
  "v_cvt_pk_bf16_f32 v41, v41, v45\n"
  "v_cvt_pk_bf16_f32 v42, v42, v46\n"
  "v_mfma_f32_16x16x32_bf16 v[16:19], v[52:55], a[64:67], v[16:19]\n"
  "buffer_load_dwordx4 v114, s[40:43], s57 offen offset:0 nt lds\n"
  "buffer_load_dwordx4 v115, s[40:43], s57 offen offset:1024 nt lds\n"
  "s_waitcnt vmcnt(17)\n"
  "ds_read_b64_tr_b16 a[242:243], v117 offset:24576\n"
  "ds_read_b64_tr_b16 a[246:247], v118 offset:24576\n"
  "v_mfma_f32_16x16x32_bf16 v[12:15], v[52:55], a[60:63], v[12:15]\n"
  "ds_write2_b32 v107, v40, v41 offset0:0 offset1:72\n"
  "v_cvt_pk_bf16_f32 v43, v43, v47\n"
  "v_mfma_f32_16x16x32_bf16 v[12:15], v[60:63], a[84:87], v[12:15]\n"
  "buffer_load_dwordx4 v116, s[40:43], s57 offen offset:2048  lds\n"
  "ds_read_b64_tr_b16 a[250:251], v119 offset:24576\n"
  "ds_read_b64_tr_b16 a[254:255], v120 offset:24576\n"
  "v_mfma_f32_16x16x32_bf16 v[8:11], v[60:63], a[80:83], v[8:11]\n"
  "s_add_u32 s57, s57, 6144\n"
  "ds_read_b64_tr_b16 v[130:131], v121 offset:26624\n"
  "ds_read_b64_tr_b16 v[134:135], v122 offset:26624\n"
  "ds_read_b64_tr_b16 v[96:97], v108 offset:2304\n"
  "ds_read_b64_tr_b16 v[98:99], v108 offset:2336\n"
  "v_mfma_f32_16x16x32_bf16 v[8:11], v[52:55], a[56:59], v[8:11]\n"
  "ds_write2_b32 v107, v42, v43 offset0:144 offset1:216\n"
  "v_cvt_pk_bf16_f32 v24, v24, v28\n"
  "v_mfma_f32_16x16x32_bf16 v[4:7], v[52:55], a[52:55], v[4:7]\n"
  "s_add_u32 m0, s59, 12288\n"
  "s_waitcnt vmcnt(14)\n"
  "ds_read_b64_tr_b16 v[136:137], v117 offset:27648\n"
  "ds_read_b64_tr_b16 v[140:141], v118 offset:27648\n"
  "v_cvt_pk_bf16_f32 v28, v25, v29\n"
  "v_cvt_pk_bf16_f32 v25, v32, v36\n"
  "v_mfma_f32_16x16x32_bf16 v[4:7], v[60:63], a[76:79], v[4:7]\n"
  "buffer_load_dwordx4 v114, s[40:43], s57 offen offset:0 nt lds\n"
  "buffer_load_dwordx4 v115, s[40:43], s57 offen offset:1024 nt lds\n"
  "v_mfma_f32_16x16x32_bf16 v[0:3], v[60:63], a[72:75], v[0:3]\n"
  "ds_read_b64_tr_b16 v[144:145], v119 offset:27648\n"
  "ds_read_b64_tr_b16 v[148:149], v120 offset:27648\n"
  "v_cvt_pk_bf16_f32 v29, v33, v37\n"
  "v_cvt_pk_bf16_f32 v26, v26, v30\n"
  "v_mfma_f32_16x16x32_bf16 v[0:3], v[52:55], a[48:51], v[0:3]\n"
  "s_waitcnt lgkmcnt(15)\n"
  "buffer_load_dwordx4 v116, s[40:43], s57 offen offset:2048  lds\n"
  "ds_read_b64_tr_b16 v[152:153], v121 offset:29696\n"
  "ds_read_b64_tr_b16 v[156:157], v122 offset:29696\n"
  "v_mfma_f32_16x16x32_bf16 v[0:3], v[64:67], a[96:99], v[0:3]\n"
  "s_add_u32 s57, s57, s84\n"
  "ds_read_b128 v[48:51], v104 offset:2048\n"
  "ds_write_b64 v106, v[24:25] offset:0\n"
  "v_mfma_f32_16x16x32_bf16 v[0:3], v[72:75], a[120:123], v[0:3]\n"
  "s_waitcnt vmcnt(13)\n"
  "ds_read_b64_tr_b16 v[138:139], v117 offset:0\n"
  "ds_read_b64_tr_b16 v[142:143], v118 offset:0\n"
  "v_cvt_pk_bf16_f32 v30, v27, v31\n"
  "v_cvt_pk_bf16_f32 v27, v34, v38\n"
  "v_mfma_f32_16x16x32_bf16 v[4:7], v[72:75], a[124:127], v[4:7]\n"
  "s_add_u32 m0, s59, 15360\n"
  "ds_read_b128 v[52:55], v104 offset:2560\n"
  "ds_write_b64 v106, v[28:29] offset:288\n"
  "v_mfma_f32_16x16x32_bf16 v[4:7], v[64:67], a[100:103], v[4:7]\n"
  "buffer_load_dwordx4 v114, s[40:43], s57 offen offset:0 nt lds\n"
  "buffer_load_dwordx4 v115, s[40:43], s57 offen offset:1024 nt lds\n"
  "ds_read_b64_tr_b16 v[146:147], v119 offset:0\n"
  "ds_read_b64_tr_b16 v[150:151], v120 offset:0\n"
  "v_mfma_f32_16x16x32_bf16 v[8:11], v[64:67], a[104:107], v[8:11]\n"
  "ds_read_b64_tr_b16 v[154:155], v121 offset:2048\n"
  "ds_read_b64_tr_b16 v[158:159], v122 offset:2048\n"
  "v_cvt_pk_bf16_f32 v31, v35, v39\n"
  "v_mfma_f32_16x16x32_bf16 v[8:11], v[72:75], a[128:131], v[8:11]\n"
  "buffer_load_dwordx4 v116, s[40:43], s57 offen offset:2048  lds\n"
  "ds_read_b64_tr_b16 v[92:93], v108 offset:4608\n"
  "ds_read_b64_tr_b16 v[94:95], v108 offset:4640\n"
  "v_mfma_f32_16x16x32_bf16 v[12:15], v[72:75], a[132:135], v[12:15]\n"
  "s_add_u32 s57, s57, 6144\n"
  "ds_read_b128 v[56:59], v105 offset:2048\n"
  "s_waitcnt vmcnt(13)\n"
  "ds_read_b64_tr_b16 v[160:161], v117 offset:3072\n"
  "ds_read_b64_tr_b16 v[164:165], v118 offset:3072\n"
  "v_xor_b32 v108, 0x2000, v108\n"
  "v_mfma_f32_16x16x32_bf16 v[12:15], v[64:67], a[108:111], v[12:15]\n"
  "s_waitcnt lgkmcnt(15)\n"
  "buffer_store_dwordx4 v[96:99], v109, s[48:51], s68 offen offset:128 nt\n"
  "ds_write_b64 v106, v[26:27] offset:576\n"
  "v_mfma_f32_16x16x32_bf16 v[16:19], v[64:67], a[112:115], v[16:19]\n"
  "s_add_u32 m0, s59, 18432\n"
  "ds_read_b64_tr_b16 v[168:169], v119 offset:3072\n"
  "ds_read_b64_tr_b16 v[172:173], v120 offset:3072\n"
  "ds_write_b64 v106, v[30:31] offset:864\n"
  "v_mfma_f32_16x16x32_bf16 v[16:19], v[72:75], a[136:139], v[16:19]\n"
  "buffer_load_dwordx4 v114, s[40:43], s57 offen offset:0 nt lds\n"
  "buffer_load_dwordx4 v115, s[40:43], s57 offen offset:1024 nt lds\n"
  "ds_read_b64_tr_b16 v[176:177], v121 offset:5120\n"
  "ds_read_b64_tr_b16 v[180:181], v122 offset:5120\n"
  "v_mfma_f32_16x16x32_bf16 v[20:23], v[72:75], a[140:143], v[20:23]\n"
  "ds_read_b128 v[60:63], v105 offset:2560\n"
  "v_mfma_f32_16x16x32_bf16 v[20:23], v[64:67], a[116:119], v[20:23]\n"
  "buffer_load_dwordx4 v116, s[40:43], s57 offen offset:2048  lds\n"
  "s_waitcnt vmcnt(14)\n"
  "ds_read_b64_tr_b16 v[162:163], v117 offset:6144\n"
  "ds_read_b64_tr_b16 v[166:167], v118 offset:6144\n"
  "v_mfma_f32_16x16x32_bf16 v[20:23], v[68:71], a[164:167], v[20:23]\n"
  "s_add_u32 s57, s57, s58\n"
  "v_mfma_f32_16x16x32_bf16 v[20:23], v[76:79], a[188:191], v[20:23]\n"
  "ds_read_b64_tr_b16 v[170:171], v119 offset:6144\n"
  "ds_read_b64_tr_b16 v[174:175], v120 offset:6144\n"
  "v_mfma_f32_16x16x32_bf16 v[16:19], v[76:79], a[184:187], v[16:19]\n"
  "s_add_u32 m0, s59, 21504\n"
  "ds_read_b64_tr_b16 v[178:179], v121 offset:8192\n"
  "ds_read_b64_tr_b16 v[182:183], v122 offset:8192\n"
  "v_mfma_f32_16x16x32_bf16 v[16:19], v[68:71], a[160:163], v[16:19]\n"
  "buffer_load_dwordx4 v114, s[40:43], s57 offen offset:0 nt lds\n"
  "buffer_load_dwordx4 v115, s[40:43], s57 offen offset:1024 nt lds\n"
  "v_mfma_f32_16x16x32_bf16 v[12:15], v[68:71], a[156:159], v[12:15]\n"
  "s_waitcnt vmcnt(12)\n"
  "ds_read_b64_tr_b16 v[184:185], v117 offset:9216\n"
  "ds_read_b64_tr_b16 v[188:189], v118 offset:9216\n"
  "v_mfma_f32_16x16x32_bf16 v[12:15], v[76:79], a[180:183], v[12:15]\n"
  "buffer_load_dwordx4 v116, s[40:43], s57 offen offset:2048  lds\n"
  "v_mfma_f32_16x16x32_bf16 v[8:11], v[76:79], a[176:179], v[8:11]\n"
  "s_add_u32 s57, s57, 6144\n"
  "ds_read_b64_tr_b16 v[192:193], v119 offset:9216\n"
  "ds_read_b64_tr_b16 v[196:197], v120 offset:9216\n"
  "v_mfma_f32_16x16x32_bf16 v[8:11], v[68:71], a[152:155], v[8:11]\n"
  "s_add_u32 m0, s59, 24576\n"
  "s_waitcnt lgkmcnt(15)\n"
  "buffer_store_dwordx4 v[92:95], v109, s[48:51], s68 offen offset:256 nt\n"
  "ds_read_b64_tr_b16 v[200:201], v121 offset:11264\n"
  "ds_read_b64_tr_b16 v[204:205], v122 offset:11264\n"
  "v_mfma_f32_16x16x32_bf16 v[4:7], v[68:71], a[148:151], v[4:7]\n"
  "buffer_load_dwordx4 v114, s[40:43], s57 offen offset:0 nt lds\n"
  "buffer_load_dwordx4 v115, s[40:43], s57 offen offset:1024 nt lds\n"
  "v_mfma_f32_16x16x32_bf16 v[4:7], v[76:79], a[172:175], v[4:7]\n"
  "s_waitcnt vmcnt(13)\n"
  "ds_read_b64_tr_b16 v[186:187], v117 offset:12288\n"
  "ds_read_b64_tr_b16 v[190:191], v118 offset:12288\n"
  "v_mfma_f32_16x16x32_bf16 v[0:3], v[76:79], a[168:171], v[0:3]\n"
  "s_add_u32 s68, s68, s69\n"
  "buffer_load_dwordx4 v116, s[40:43], s57 offen offset:2048  lds\n"
  "v_mfma_f32_16x16x32_bf16 v[0:3], v[68:71], a[144:147], v[0:3]\n"
  "s_waitcnt lgkmcnt(14)\n"
  "s_add_u32 s57, s57, s84\n"
  "ds_read_b64_tr_b16 v[194:195], v119 offset:12288\n"
  "ds_read_b64_tr_b16 v[198:199], v120 offset:12288\n"
  "v_mfma_f32_16x16x32_bf16 v[0:3], v[48:51], a[192:195], v[0:3]\n"
  "s_add_u32 s72, s72, 1\n"
  "ds_read_b128 v[64:67], v104 offset:3072\n"
  "ds_read_b64_tr_b16 v[202:203], v121 offset:14336\n"
  "ds_read_b64_tr_b16 v[206:207], v122 offset:14336\n"
  "v_mfma_f32_16x16x32_bf16 v[0:3], v[56:59], a[216:219], v[0:3]\n"
  "s_lshr_b32 s74, s72, 1\n"
  "v_readlane_b32 s73, v126, s74\n"
  "v_mfma_f32_16x16x32_bf16 v[4:7], v[56:59], a[220:223], v[4:7]\n"
  "ds_read_b128 v[68:71], v104 offset:3584\n"
  "s_waitcnt vmcnt(11)\n"
  "ds_read_b64_tr_b16 v[208:209], v117 offset:15360\n"
  "ds_read_b64_tr_b16 v[212:213], v118 offset:15360\n"
  "v_add_u32 v104, s88, v104\n"
  "v_mfma_f32_16x16x32_bf16 v[4:7], v[48:51], a[196:199], v[4:7]\n"
  "s_lshl_b32 s74, s72, 4\n"
  "v_mfma_f32_16x16x32_bf16 v[8:11], v[48:51], a[200:203], v[8:11]\n"
  "s_lshr_b32 s73, s73, s74\n"
  "ds_read_b64_tr_b16 v[216:217], v119 offset:15360\n"
  "ds_read_b64_tr_b16 v[220:221], v120 offset:15360\n"
  "v_mfma_f32_16x16x32_bf16 v[8:11], v[56:59], a[224:227], v[8:11]\n"
  "s_and_b32 s73, s73, 0xffff\n"
  "ds_read_b64_tr_b16 v[224:225], v121 offset:17408\n"
  "ds_read_b64_tr_b16 v[228:229], v122 offset:17408\n"
  "v_mfma_f32_16x16x32_bf16 v[12:15], v[56:59], a[228:231], v[12:15]\n"
  "s_lshl_b32 s73, s73, 13\n"
  "ds_read_b128 v[72:75], v105 offset:3072\n"
  "v_mfma_f32_16x16x32_bf16 v[12:15], v[48:51], a[204:207], v[12:15]\n"
  "s_waitcnt vmcnt(7)\n"
  "ds_read_b64_tr_b16 v[210:211], v117 offset:18432\n"
  "ds_read_b64_tr_b16 v[214:215], v118 offset:18432\n"
  "v_mfma_f32_16x16x32_bf16 v[16:19], v[48:51], a[208:211], v[16:19]\n"
  "s_mov_b32 s69, s70\n"
  "v_mfma_f32_16x16x32_bf16 v[16:19], v[56:59], a[232:235], v[16:19]\n"
  "s_movk_i32 s70, 6144\n"
  "ds_read_b64_tr_b16 v[218:219], v119 offset:18432\n"
  "ds_read_b64_tr_b16 v[222:223], v120 offset:18432\n"
  "v_mfma_f32_16x16x32_bf16 v[20:23], v[56:59], a[236:239], v[20:23]\n"
  "ds_read_b128 v[76:79], v105 offset:3584\n"
  "ds_read_b64_tr_b16 v[226:227], v121 offset:20480\n"
  "ds_read_b64_tr_b16 v[230:231], v122 offset:20480\n"
  "v_add_u32 v105, s88, v105\n"
  "v_mfma_f32_16x16x32_bf16 v[20:23], v[48:51], a[212:215], v[20:23]\n"
  "s_add_u32 s88, s88, s89\n"
  "v_smfmac_f32_16x16x64_bf16 v[80:83], v[88:91], v[48:55], v124\n"
  "s_waitcnt lgkmcnt(14)\n"
  "s_waitcnt vmcnt(4)\n"
  "ds_read_b64_tr_b16 v[232:233], v117 offset:21504\n"
  "ds_read_b64_tr_b16 v[236:237], v118 offset:21504\n"
  "v_smfmac_f32_16x16x64_bf16 v[80:83], v[88:91], v[64:71], v124\n"
  "v_mfma_f32_16x16x32_bf16 v[20:23], v[52:55], v[132:135], v[20:23]\n"
  "s_sub_u32 s88, 0, s88\n"
  "ds_read_b64_tr_b16 v[240:241], v119 offset:21504\n"
  "ds_read_b64_tr_b16 v[244:245], v120 offset:21504\n"
  "v_mfma_f32_16x16x32_bf16 v[20:23], v[60:63], v[156:159], v[20:23]\n"
  "s_xor_b32 s88, s88, s89\n"
  "ds_read_b64_tr_b16 v[248:249], v121 offset:23552\n"
  "ds_read_b64_tr_b16 v[252:253], v122 offset:23552\n"
  "v_mfma_f32_16x16x32_bf16 v[16:19], v[60:63], v[152:155], v[16:19]\n"
  "s_xor_b32 s89, s88, s89\n"
  "v_add_f32 v44, v80, v81\n"
  "v_mov_b32 v80, 0\n"
  "v_mov_b32 v81, 0\n"
  "v_mfma_f32_16x16x32_bf16 v[16:19], v[52:55], v[128:131], v[16:19]\n"
  "s_waitcnt vmcnt(0)\n"
  "ds_read_b64_tr_b16 v[234:235], v117 offset:24576\n"
  "ds_read_b64_tr_b16 v[238:239], v118 offset:24576\n"
  "ds_write_b32 v110, v44\n"
  "v_mfma_f32_16x16x32_bf16 v[12:15], v[52:55], a[252:255], v[12:15]\n"
  "s_xor_b32 s88, s88, s89\n"
  "v_mfma_f32_16x16x32_bf16 v[12:15], v[60:63], v[148:151], v[12:15]\n"
  "ds_read_b64_tr_b16 v[242:243], v119 offset:24576\n"
  "ds_read_b64_tr_b16 v[246:247], v120 offset:24576\n"
  "v_mfma_f32_16x16x32_bf16 v[8:11], v[60:63], v[144:147], v[8:11]\n"
  "ds_read_b64_tr_b16 v[250:251], v121 offset:26624\n"
  "ds_read_b64_tr_b16 v[254:255], v122 offset:26624\n"
  "v_mfma_f32_16x16x32_bf16 v[8:11], v[52:55], a[248:251], v[8:11]\n"
  "v_xor_b32 v106, 0x2000, v106\n"
  "v_xor_b32 v107, 0x2000, v107\n"
  "v_mfma_f32_16x16x32_bf16 v[4:7], v[52:55], a[244:247], v[4:7]\n"
  "v_xor_b32 v110, 0x2000, v110\n"
  "v_mfma_f32_16x16x32_bf16 v[4:7], v[60:63], v[140:143], v[4:7]\n"
  "v_mfma_f32_16x16x32_bf16 v[0:3], v[60:63], v[136:139], v[0:3]\n"
  "v_mfma_f32_16x16x32_bf16 v[0:3], v[52:55], a[240:243], v[0:3]\n"
  "s_branch B_H_%=\n"
  "B_LAST_%=:\n"
  "s_waitcnt lgkmcnt(0)\n"
  "s_barrier\n"
  "v_mfma_f32_16x16x32_bf16 v[20:23], v[64:67], v[180:183], v[20:23]\n"
  "ds_read_b64_tr_b16 v[48:49], v108 offset:0\n"
  "ds_read_b64_tr_b16 v[50:51], v108 offset:32\n"
  "ds_read_b128 v[84:87], v111\n"
  "v_mfma_f32_16x16x32_bf16 v[20:23], v[72:75], v[204:207], v[20:23]\n"
  "ds_read_b64_tr_b16 v[52:53], v108 offset:2304\n"
  "ds_read_b64_tr_b16 v[54:55], v108 offset:2336\n"
  "ds_read_b128 v[96:99], v111 offset:64\n"
  "v_mfma_f32_16x16x32_bf16 v[20:23], v[68:71], v[228:231], v[20:23]\n"
  "v_xor_b32 v111, 0x2000, v111\n"
  "v_mfma_f32_16x16x32_bf16 v[44:47], v[76:79], v[252:255], v[20:23]\n"
  "v_mfma_f32_16x16x32_bf16 v[16:19], v[76:79], v[248:251], v[16:19]\n"
  "v_mfma_f32_16x16x32_bf16 v[16:19], v[68:71], v[224:227], v[16:19]\n"
  "ds_read_b64_tr_b16 v[56:57], v108 offset:4608\n"
  "ds_read_b64_tr_b16 v[58:59], v108 offset:4640\n"
  "v_xor_b32 v108, 0x2000, v108\n"
  "v_mfma_f32_16x16x32_bf16 v[16:19], v[72:75], v[200:203], v[16:19]\n"
  "v_mfma_f32_16x16x32_bf16 v[40:43], v[64:67], v[176:179], v[16:19]\n"
  "s_waitcnt lgkmcnt(2)\n"
  "buffer_store_dwordx4 v[48:51], v109, s[48:51], s68 offen offset:0 nt\n"
  "v_add_f32 v84, v84, v96\n"
  "v_add_f32 v85, v85, v97\n"
  "v_add_f32 v86, v86, v98\n"
  "v_mfma_f32_16x16x32_bf16 v[12:15], v[64:67], v[172:175], v[12:15]\n"
  "v_add_f32 v87, v87, v99\n"
  "v_max3_f32 v82, v82, v84, v85\n"
  "v_max3_f32 v82, v82, v86, v87\n"
  "v_mfma_f32_16x16x32_bf16 v[12:15], v[72:75], v[196:199], v[12:15]\n"
  "v_rcp_f32 v84, v84\n"
  "v_mfma_f32_16x16x32_bf16 v[12:15], v[68:71], v[220:223], v[12:15]\n"
  "v_rcp_f32 v85, v85\n"
  "v_mul_f32 v96, s99, v84\n"
  "v_mfma_f32_16x16x32_bf16 v[36:39], v[76:79], v[244:247], v[12:15]\n"
  "buffer_store_dwordx4 v[52:55], v109, s[48:51], s68 offen offset:128 nt\n"
  "v_rcp_f32 v86, v86\n"
  "v_mul_f32 v97, s99, v85\n"
  "v_mfma_f32_16x16x32_bf16 v[8:11], v[76:79], v[240:243], v[8:11]\n"
  "v_rcp_f32 v87, v87\n"
  "v_mul_f32 v98, s99, v86\n"
  "v_mfma_f32_16x16x32_bf16 v[8:11], v[68:71], v[216:219], v[8:11]\n"
  "v_mul_f32 v99, s99, v87\n"
  "v_fma_f32 v40, v40, v84, v96\n"
  "v_fma_f32 v44, v44, v84, v96\n"
  "v_mfma_f32_16x16x32_bf16 v[8:11], v[72:75], v[192:195], v[8:11]\n"
  "v_cvt_pk_bf16_f32 v40, v40, v44\n"
  "v_fma_f32 v41, v41, v85, v97\n"
  "v_fma_f32 v45, v45, v85, v97\n"
  "v_mfma_f32_16x16x32_bf16 v[32:35], v[64:67], v[168:171], v[8:11]\n"
  "s_waitcnt lgkmcnt(0)\n"
  "buffer_store_dwordx4 v[56:59], v109, s[48:51], s68 offen offset:256 nt\n"
  "v_cvt_pk_bf16_f32 v41, v41, v45\n"
  "v_fma_f32 v42, v42, v86, v98\n"
  "v_fma_f32 v46, v46, v86, v98\n"
  "v_mfma_f32_16x16x32_bf16 v[4:7], v[64:67], v[164:167], v[4:7]\n"
  "s_add_u32 s68, s68, s69\n"
  "ds_write2_b32 v107, v40, v41 offset0:0 offset1:72\n"
  "v_mfma_f32_16x16x32_bf16 v[4:7], v[72:75], v[188:191], v[4:7]\n"
  "s_mov_b32 s69, s70\n"
  "v_cvt_pk_bf16_f32 v42, v42, v46\n"
  "v_fma_f32 v43, v43, v87, v99\n"
  "v_fma_f32 v47, v47, v87, v99\n"
  "v_mfma_f32_16x16x32_bf16 v[4:7], v[68:71], v[212:215], v[4:7]\n"
  "s_movk_i32 s70, 6144\n"
  "v_cvt_pk_bf16_f32 v43, v43, v47\n"
  "v_fma_f32 v32, v32, v84, v96\n"
  "v_fma_f32 v33, v33, v85, v97\n"
  "v_mfma_f32_16x16x32_bf16 v[28:31], v[76:79], v[236:239], v[4:7]\n"
  "ds_write2_b32 v107, v42, v43 offset0:144 offset1:216\n"
  "v_mfma_f32_16x16x32_bf16 v[0:3], v[76:79], v[232:235], v[0:3]\n"
  "v_fma_f32 v36, v36, v84, v96\n"
  "v_fma_f32 v37, v37, v85, v97\n"
  "v_fma_f32 v34, v34, v86, v98\n"
  "v_mfma_f32_16x16x32_bf16 v[0:3], v[68:71], v[208:211], v[0:3]\n"
  "v_fma_f32 v35, v35, v87, v99\n"
  "v_fma_f32 v38, v38, v86, v98\n"
  "v_fma_f32 v39, v39, v87, v99\n"
  "v_mfma_f32_16x16x32_bf16 v[0:3], v[72:75], v[184:187], v[0:3]\n"
  "v_fma_f32 v28, v28, v84, v96\n"
  "v_fma_f32 v29, v29, v85, v97\n"
  "v_fma_f32 v30, v30, v86, v98\n"
  "v_mfma_f32_16x16x32_bf16 v[24:27], v[64:67], v[160:163], v[0:3]\n"
  "v_fma_f32 v31, v31, v87, v99\n"
  "v_max3_f32 v83, v83, v84, v85\n"
  "v_max3_f32 v83, v83, v86, v87\n"
  "s_nop 4\n"
  "v_fma_f32 v24, v24, v84, v96\n"
  "v_fma_f32 v25, v25, v85, v97\n"
  "v_cvt_pk_bf16_f32 v24, v24, v28\n"
  "v_cvt_pk_bf16_f32 v28, v25, v29\n"
  "v_cvt_pk_bf16_f32 v25, v32, v36\n"
  "v_cvt_pk_bf16_f32 v29, v33, v37\n"
  "ds_write_b64 v106, v[24:25] offset:0\n"
  "ds_write_b64 v106, v[28:29] offset:288\n"
  "v_fma_f32 v26, v26, v86, v98\n"
  "v_fma_f32 v27, v27, v87, v99\n"
  "v_cvt_pk_bf16_f32 v26, v26, v30\n"
  "v_cvt_pk_bf16_f32 v30, v27, v31\n"
  "v_cvt_pk_bf16_f32 v27, v34, v38\n"
  "v_cvt_pk_bf16_f32 v31, v35, v39\n"
  "ds_write_b64 v106, v[26:27] offset:576\n"
  "ds_write_b64 v106, v[30:31] offset:864\n"
  "v_mul_f32 v83, 0x51800000, v83\n"
  "v_add_f32 v82, v82, v83\n"
  "ds_write_b32 v104, v82\n"
  "s_waitcnt lgkmcnt(0)\n"
  "s_barrier\n"
  "ds_read_b32 v83, v105\n"
  "ds_read_b64_tr_b16 v[48:49], v108 offset:0\n"
  "ds_read_b64_tr_b16 v[50:51], v108 offset:32\n"
  "ds_read_b64_tr_b16 v[52:53], v108 offset:2304\n"
  "ds_read_b64_tr_b16 v[54:55], v108 offset:2336\n"
  "ds_read_b64_tr_b16 v[56:57], v108 offset:4608\n"
  "ds_read_b64_tr_b16 v[58:59], v108 offset:4640\n"
  "s_waitcnt lgkmcnt(4)\n"
  "buffer_store_dwordx4 v[48:51], v109, s[48:51], s68 offen offset:0 nt\n"
  "s_waitcnt lgkmcnt(2)\n"
  "buffer_store_dwordx4 v[52:55], v109, s[48:51], s68 offen offset:128 nt\n"
  "s_waitcnt lgkmcnt(0)\n"
  "buffer_store_dwordx4 v[56:59], v109, s[48:51], s68 offen offset:256 nt\n"
  "v_add_f32 v82, v82, v83\n"
  "v_cmp_ngt_f32 vcc, s71, v82\n"
  "s_nop 4\n"
  "s_cmp_lg_u64 vcc, 0\n"
  "s_cselect_b32 %0, 1, 0\n"
  "L_END_%=:\n"
  : "=&s"(bad)
  : "s"((unsigned long long)__builtin_amdgcn_kernarg_segment_ptr()), "s"(wid), "s"(wg)
  : "v0", "v1", "v2", "v3", "v4", "v5", "v6", "v7", "v8", "v9", "v10", "v11", "v12", "v13", "v14", "v15", "v16", "v17", "v18", "v19", "v20", "v21", "v22", "v23", "v24", "v25", "v26", "v27", "v28", "v29", "v30", "v31", "v32", "v33", "v34", "v35", "v36", "v37", "v38", "v39", "v40", "v41", "v42", "v43", "v44", "v45", "v46", "v47", "v48", "v49", "v50", "v51", "v52", "v53", "v54", "v55", "v56", "v57", "v58", "v59", "v60", "v61", "v62", "v63", "v64", "v65", "v66", "v67", "v68", "v69", "v70", "v71", "v72", "v73", "v74", "v75", "v76", "v77", "v78", "v79", "v80", "v81", "v82", "v83", "v84", "v85", "v86", "v87", "v88", "v89", "v90", "v91", "v92", "v93", "v94", "v95", "v96", "v97", "v98", "v99", "v100", "v101", "v102", "v103", "v104", "v105", "v106", "v107", "v108", "v109", "v110", "v111", "v112", "v113", "v114", "v115", "v116", "v117", "v118", "v119", "v120", "v121", "v122", "v123", "v124", "v125", "v126", "v127", "v128", "v129", "v130", "v131", "v132", "v133", "v134", "v135", "v136", "v137", "v138", "v139", "v140", "v141", "v142", "v143", "v144", "v145", "v146", "v147", "v148", "v149", "v150", "v151", "v152", "v153", "v154", "v155", "v156", "v157", "v158", "v159", "v160", "v161", "v162", "v163", "v164", "v165", "v166", "v167", "v168", "v169", "v170", "v171", "v172", "v173", "v174", "v175", "v176", "v177", "v178", "v179", "v180", "v181", "v182", "v183", "v184", "v185", "v186", "v187", "v188", "v189", "v190", "v191", "v192", "v193", "v194", "v195", "v196", "v197", "v198", "v199", "v200", "v201", "v202", "v203", "v204", "v205", "v206", "v207", "v208", "v209", "v210", "v211", "v212", "v213", "v214", "v215", "v216", "v217", "v218", "v219", "v220", "v221", "v222", "v223", "v224", "v225", "v226", "v227", "v228", "v229", "v230", "v231", "v232", "v233", "v234", "v235", "v236", "v237", "v238", "v239", "v240", "v241", "v242", "v243", "v244", "v245", "v246", "v247", "v248", "v249", "v250", "v251", "v252", "v253", "v254", "v255", "a0", "a1", "a2", "a3", "a4", "a5", "a6", "a7", "a8", "a9", "a10", "a11", "a12", "a13", "a14", "a15", "a16", "a17", "a18", "a19", "a20", "a21", "a22", "a23", "a24", "a25", "a26", "a27", "a28", "a29", "a30", "a31", "a32", "a33", "a34", "a35", "a36", "a37", "a38", "a39", "a40", "a41", "a42", "a43", "a44", "a45", "a46", "a47", "a48", "a49", "a50", "a51", "a52", "a53", "a54", "a55", "a56", "a57", "a58", "a59", "a60", "a61", "a62", "a63", "a64", "a65", "a66", "a67", "a68", "a69", "a70", "a71", "a72", "a73", "a74", "a75", "a76", "a77", "a78", "a79", "a80", "a81", "a82", "a83", "a84", "a85", "a86", "a87", "a88", "a89", "a90", "a91", "a92", "a93", "a94", "a95", "a96", "a97", "a98", "a99", "a100", "a101", "a102", "a103", "a104", "a105", "a106", "a107", "a108", "a109", "a110", "a111", "a112", "a113", "a114", "a115", "a116", "a117", "a118", "a119", "a120", "a121", "a122", "a123", "a124", "a125", "a126", "a127", "a128", "a129", "a130", "a131", "a132", "a133", "a134", "a135", "a136", "a137", "a138", "a139", "a140", "a141", "a142", "a143", "a144", "a145", "a146", "a147", "a148", "a149", "a150", "a151", "a152", "a153", "a154", "a155", "a156", "a157", "a158", "a159", "a160", "a161", "a162", "a163", "a164", "a165", "a166", "a167", "a168", "a169", "a170", "a171", "a172", "a173", "a174", "a175", "a176", "a177", "a178", "a179", "a180", "a181", "a182", "a183", "a184", "a185", "a186", "a187", "a188", "a189", "a190", "a191", "a192", "a193", "a194", "a195", "a196", "a197", "a198", "a199", "a200", "a201", "a202", "a203", "a204", "a205", "a206", "a207", "a208", "a209", "a210", "a211", "a212", "a213", "a214", "a215", "a216", "a217", "a218", "a219", "a220", "a221", "a222", "a223", "a224", "a225", "a226", "a227", "a228", "a229", "a230", "a231", "a232", "a233", "a234", "a235", "a236", "a237", "a238", "a239", "a240", "a241", "a242", "a243", "a244", "a245", "a246", "a247", "a248", "a249", "a250", "a251", "a252", "a253", "a254", "a255", "s40", "s41", "s42", "s43", "s44", "s45", "s46", "s47", "s48", "s49", "s50", "s51", "s52", "s53", "s54", "s55", "s56", "s57", "s58", "s59", "s60", "s61", "s62", "s63", "s64", "s65", "s66", "s67", "s68", "s69", "s70", "s71", "s72", "s73", "s74", "s75", "s76", "s77", "s78", "s79", "s80", "s81", "s82", "s83", "s84", "s85", "s86", "s87", "s88", "s89", "s90", "s91", "s92", "s93", "s94", "s95", "s96", "s97", "s98", "s99", "vcc", "scc", "memory");
  if (bad) {
    unsigned long long ka = (unsigned long long)__builtin_amdgcn_kernarg_segment_ptr();
    asm volatile("s_waitcnt vmcnt(0)\n\ts_barrier" : "+s"(ka) :: "memory");
    const unsigned short* const* arg = (const unsigned short* const*)ka;
    const int p = wg >> 4, side = (wg >> 3) & 1, x = 8 + (p * 20 >> 4), h0 = 8 * p + (wg & 7), n0 = side ? 53 - x : x;
    const int y = 53 - n0 + (wg & 1 ? -(2) : 2);
#pragma unroll 1
    for (int i = 0; i < 3; ++i) {
      const int head = i == 0 ? h0 : i == 1 ? 128 + wg : 384 + (h0 ^ side), n = i == 0 ? n0 : i == 1 ? 53 : y, t0 = side ? 53 - n : 0;
#pragma unroll 1
      for (int t = wid & 1; t < n; t += 2) exact_tile(arg[0], arg[1], arg[2], (unsigned short*)arg[3], head, 16 * (t0 + t));
    }
  }
}

extern "C" void kernel_launch(void* const* d_in, const int* in_sizes, int n_in,
                              void* d_out, int out_size, void* d_ws, size_t ws_size,
                              hipStream_t stream) {
  (void)hipFuncSetAttribute((const void*)attn_fwd_g, hipFuncAttributeMaxDynamicSharedMemorySize, LDS_BYTES);
  hipLaunchKernelGGL(attn_fwd_g, dim3(256), dim3(256), LDS_BYTES, stream, (const unsigned short*)d_in[0], (const unsigned short*)d_in[1],
                     (const unsigned short*)d_in[2], (unsigned short*)d_out);
}
```
